```python
import torch

def _softmax(x, axis=-1):
    return torch.softmax(x, dim=axis)

def _gelu(x):
    return torch.nn.functional.gelu(x, approximate='tanh')

def _silu(x):
    return torch.nn.functional.silu(x)

def _sigmoid(x):
    return torch.sigmoid(x)

def _layernorm(x, axis=-1, eps=1e-05):
    m = x.mean(dim=axis, keepdim=True)
    v = x.var(dim=axis, keepdim=True, unbiased=False)
    return (x - m) / torch.sqrt(v + eps)

def _scatter_add(tgt, idx, src, axis=0):
    return tgt.clone().index_add_(axis, idx, src)

def _segment_sum(src, seg, n):
    out = torch.zeros((n,) + src.shape[1:], dtype=src.dtype)
    return out.index_add_(0, seg, src)

def _pad(x, pw):
    flat = tuple((v for pair in reversed(pw) for v in pair))
    return torch.nn.functional.pad(x, flat)
kernel_name = 'interleave_layers__memory_aug_recurrent_mem_tr__sliding_local_dilated__prompt'

def setup_inputs():

    _w = lambda a: (lambda t: t.to(torch.bfloat16) if t.is_floating_point() else t)(torch.as_tensor(a))

    def _A_setup():
        B, S, Dm = (1, 2048, 640)
        H, Dh = (32, 192)
        M, seg = (8, 32)
        seg = min(seg, S)
        S = S // seg * seg
        return {'x': _w((lambda *s: torch.randn(*s, dtype=torch.bfloat16) * 0.05)(B, S, Dm)), 'mem0': _w((lambda *s: torch.randn(*s, dtype=torch.bfloat16) * 0.8)(B, M, Dm)), 'Wq': _w((lambda *s: torch.randn(*s, dtype=torch.bfloat16) * 1.6)(Dm, H, Dh) * Dm ** (-0.5)), 'Wk': _w((lambda *s: torch.randn(*s, dtype=torch.bfloat16) * 1.6)(Dm, H, Dh) * Dm ** (-0.5)), 'Wv': _w((lambda *s: torch.randn(*s, dtype=torch.bfloat16) * 0.8)(Dm, H, Dh) * Dm ** (-0.5)), 'Wo': _w((lambda *s: torch.randn(*s, dtype=torch.bfloat16) * 0.8)(H, Dh, Dm) * (H * Dh) ** (-0.5)), 'seg_len': seg}
    _A_in = _A_setup()

    def _B_setup():
        B, S, D = (1, 2048, 640)
        return {'x': _w((lambda *s: torch.randn(*s, dtype=torch.bfloat16) * 0.05)(B, S, D)), 'wqkv': _w((lambda *s: torch.randn(*s, dtype=torch.bfloat16) * 0.05)(D, 3 * D)), 'wo': _w((lambda *s: torch.randn(*s, dtype=torch.bfloat16) * 0.05)(D, D))}
    _B_in = _B_setup()
    return {'A_x': _A_in['x'], 'A_mem0': _A_in['mem0'], 'A_Wq': _A_in['Wq'], 'A_Wk': _A_in['Wk'], 'A_Wv': _A_in['Wv'], 'A_Wo': _A_in['Wo'], 'A_seg_len': _A_in['seg_len'], 'B_x': _B_in['x'], 'B_wqkv': _B_in['wqkv'], 'B_wo': _B_in['wo']}

def reference(A_x, A_mem0, A_Wq, A_Wk, A_Wv, A_Wo, A_seg_len, B_x, B_wqkv, B_wo):

    def _A_ref(x, mem0, Wq, Wk, Wv, Wo, seg_len):
        B, S, Dm = x.shape
        H, Dh = (Wq.shape[1], Wq.shape[2])
        M = mem0.shape[1]
        scale = Dh ** (-0.5)
        n_seg = S // seg_len
        L = M + seg_len
        idx = torch.arange(L)
        mask = (idx[:, None] < M) | (idx[:, None] >= idx[None, :])
        mem = mem0
        outs = []
        for s in range(n_seg):
            xs = x[:, s * seg_len:(s + 1) * seg_len, :].clone()
            h = torch.concatenate([mem, xs], axis=1)
            q = torch.einsum('btd,dhe->bhte', h, Wq)
            k = torch.einsum('btd,dhe->bhte', h, Wk)
            v = torch.einsum('btd,dhe->bhte', h, Wv)
            scores = torch.einsum('bhqe,bhke->bhqk', q, k) * scale
            scores = torch.where(mask[None, None], scores, -1000000000.0)
            p = _softmax(scores, axis=-1)
            o = torch.einsum('bhqk,bhke->bhqe', p, v)
            o = torch.einsum('bhte,hed->btd', o, Wo)
            mem = o[:, :M, :].clone()
            outs.append(o[:, M:, :])
        return torch.concatenate(outs, axis=1)

    def _B_ref(x, wqkv, wo):
        B, S, D = x.shape
        H = 32
        Dh = D // H
        W, dil = (8, 2)
        qkv = (lambda x, *d: x.permute(*d))((x @ wqkv).reshape(B, S, 3, H, Dh), 2, 0, 3, 1, 4)
        q, k, v = (qkv[0], qkv[1], qkv[2])
        scale = Dh ** (-0.5)
        pos = torch.arange(S)
        off = torch.arange(W) * dil
        idx = pos[:, None] - off[None, :]
        mask = idx >= 0
        idx_c = torch.where(mask, idx, 0).reshape(-1)
        kg = k[:, :, idx_c, :].reshape(B, H, S, W, Dh)
        vg = v[:, :, idx_c, :].reshape(B, H, S, W, Dh)
        logits = torch.einsum('bhsd,bhswd->bhsw', q, kg) * scale
        logits = torch.where(mask[None, None, :, :], logits, -1000000000.0)
        attn = _softmax(logits, axis=-1)
        o = torch.einsum('bhsw,bhswd->bhsd', attn, vg)
        return (lambda x, *d: x.permute(*d))(o, 0, 2, 1, 3).reshape(B, S, D) @ wo
    x = A_x
    x = x + _A_ref(_layernorm(x, axis=-1), A_mem0, A_Wq, A_Wk, A_Wv, A_Wo, A_seg_len)
    x = x + _B_ref(_layernorm(x, axis=-1), B_wqkv, B_wo)
    x = x + _A_ref(_layernorm(x, axis=-1), A_mem0, A_Wq, A_Wk, A_Wv, A_Wo, A_seg_len)
    x = x + _B_ref(_layernorm(x, axis=-1), B_wqkv, B_wo)
    return x
```

```cpp
#include <hip/hip_runtime.h>
#include <hip/hip_bf16.h>
#include <stdint.h>

typedef unsigned short bf16_t;
typedef short bf16x8 __attribute__((ext_vector_type(8)));
typedef short bf16x4 __attribute__((ext_vector_type(4)));
typedef float f32x4 __attribute__((ext_vector_type(4)));
typedef unsigned u32x4 __attribute__((ext_vector_type(4)));
typedef unsigned u32x2 __attribute__((ext_vector_type(2)));
typedef unsigned long long u64;
typedef __bf16 bf16v2 __attribute__((ext_vector_type(2)));
typedef float f32v2 __attribute__((ext_vector_type(2)));
#define DEV __device__ __forceinline__
DEV unsigned pk2(float a, float b) { f32v2 f = {a, b}; bf16v2 r = __builtin_convertvector(f, bf16v2); return __builtin_bit_cast(unsigned, r); }
DEV float rb(float a) { return __uint_as_float((pk2(a, 0.f) & 0xffffu) << 16); }
DEV float lo16(unsigned u) { return __uint_as_float(u << 16); }
DEV float hi16(unsigned u) { return __uint_as_float(u & 0xffff0000u); }
constexpr int D_ = 640;
#define RLX_AGENT __ATOMIC_RELAXED, __HIP_MEMORY_SCOPE_AGENT
#define AUX_SC1 16
typedef __amdgpu_buffer_rsrc_t rsrc_t;
DEV rsrc_t mk_rsrc(const void* p, unsigned bytes) { return __builtin_amdgcn_make_buffer_rsrc((void*)p, 0, (int)bytes, 0x00020000); }
DEV void bst16(rsrc_t r, unsigned off, u32x4 v) { __builtin_amdgcn_raw_buffer_store_b128(v, r, off, 0, AUX_SC1); }
DEV void bst8(rsrc_t r, unsigned off, u32x2 v) { __builtin_amdgcn_raw_buffer_store_b64(v, r, off, 0, AUX_SC1); }
DEV void bst4(rsrc_t r, unsigned off, unsigned v) { __builtin_amdgcn_raw_buffer_store_b32(v, r, off, 0, AUX_SC1); }
#define DRAIN() asm volatile("s_waitcnt vmcnt(0)" ::: "memory")
#define DRAIN_BARRIER() asm volatile("s_waitcnt vmcnt(0) lgkmcnt(0)\n\ts_barrier" ::: "memory")
#define RFL(x) ((unsigned)__builtin_amdgcn_readfirstlane((int)(x)))
DEV unsigned lo(const void* p) { return (unsigned)(size_t)p; }
DEV unsigned hi(const void* p) { return (unsigned)((size_t)p >> 32) & 0xffffu; }
DEV unsigned nosent(unsigned x) { return min(x, 0xfffffffeu); }
DEV int sw2(int x) { return ((x & 1) << 1) | (x >> 1); }

DEV unsigned xl(unsigned o, int side) { const unsigned ph = ((o >> 13) << 14) | (o & 8191u); return ph | ((((ph >> 21) ^ side) & 1u) << 13); }
constexpr unsigned Z_TK = 16384, Z_QV = Z_TK + 1024, Z_CT = Z_QV + 1024, Z_QW = Z_CT + 2048, Z_X = 3 * 8192, Z_X1 = 7 * 8192, Z_XN = 9 * 8192, Z_O = 10 * 8192, Z_ST = 11 * 8192, Z_END = 12 * 8192;
constexpr unsigned RG_UT = 1u << 21, RG_UTX = RG_UT + (1u << 20), RG_UM = RG_UTX + 262144, RG_UMX = RG_UM + 262144, RG_PT = RG_UMX + 65536, RG_PM = RG_PT + 65536, RG_MEM = RG_PM + 32768;
constexpr unsigned SLOT = 1u << 22;
constexpr unsigned REC0 = 20u << 20, RC_X1 = 5 * 16384, RC_O = 10 * 16384, RC_X2 = 15 * 16384, ST0 = 1u << 20, QT0 = 52u << 20, DBG0 = 116u << 20, ARENA = 120u << 20;
DEV unsigned rec(int s, int L) { return REC0 + (s << 19) + ((((s >> 2) ^ L) & 1) << 13); }
constexpr unsigned OOB = 0x7f000000u, WBYTES = 640u * 6144u * 2u;
DEV char* arena_base(char* p) { return (char*)(((size_t)p + 0x3fffff) & ~(size_t)0x3fffff); }

struct MegaArgs {
    const bf16_t* x0; const bf16_t* mem0; const bf16_t* Wq; const bf16_t* Wk; const bf16_t* Wv; const bf16_t* Wo; const bf16_t* wqkv; const bf16_t* wo;
    char* ar; bf16_t* out; int layers, pad;
};
__global__ void __launch_bounds__(256) k_zero(char* ar) {
    *(u32x4*)(arena_base(ar) + 2 * Z_TK + (blockIdx.x * 256 + threadIdx.x) * 16) = u32x4{0u, 0u, 0u, 0u};
}
#define ROLE_HEAD const int l = threadIdx.x & 63, l16 = l & 15, lq = l >> 4, w = RFL(threadIdx.x >> 6); char* const ab = arena_base(A.ar); \
    const rsrc_t rr = mk_rsrc(ab, ARENA); const u32x4 rx = {lo(ab), hi(ab), ARENA, 0x20000u}; const u32x4 ones = {~0u, ~0u, ~0u, ~0u}; const u32x2 ones2 = {~0u, ~0u}
DEV void qk_role(const MegaArgs& A, const int L, const int h, const int rep) {
    ROLE_HEAD;
    const int i = 2 * w + (lq & 1), p = l16 < 8 ? l16 >> 2 : l16 & 1, rho = 4 * (i >> 1) + 2 * p + (i & 1), tp = 4 * w + lq;
    const unsigned pm = (l >= 32 || l16 >= 10) ? OOB : xl(RG_PM + (h >> 3) * 8192 +
        (l16 < 8 ? ((h & 7) >> 1) * 1024 + ((2 * (h & 1) + ((l16 >> 1) & 1)) * 16 + rho) * 16 + (l16 & 1) * 8 : 4096 + (((h & 7) >> 1) * 16 + rho) * 16 + (h & 1) * 8), L);
    const unsigned pt = xl(RG_PT + h * 2048 + tp * 16 + (l16 < 8 ? (l16 & 3) * 256 + (l16 >> 2) * 8 : (l16 & 1) * 8 + (l16 < 10 ? 1792 : (l16 < 14 ? 1024 + ((l16 - 10) >> 1) * 256 : 1536))), L);
    u64 cm[4];
    const int k0 = (l16 >= 4 && l16 < 8) ? 4 * (l16 - 4) : ((l16 >= 10 && l16 < 14) ? 4 * (l16 - 10) : -4);
#pragma unroll
    for (int r = 0; r < 4; ++r) cm[r] = __ballot(k0 + r <= tp);
    bst8(rr, pm, ones2); bst8(rr, pt, ones2);
    DRAIN_BARRIER();
    bst4(rr, threadIdx.x < 4 ? xl(Z_QV + l * 256 + h * 4, L) : OOB, 1u);
    const u32x4 s0_ = {RFL(rx[0]), RFL(rx[1]), RFL(rx[2]), RFL(rx[3])};
    const u32x4 s4_ = {RFL(lo(A.Wk + h * 192 + 48 * w)), RFL(hi(A.Wk + h * 192 + 48 * w)), RFL(WBYTES - (h * 192 + 48 * w) * 2), RFL(0x20000u)};
    const u32x4 s8_ = {RFL(lo(A.Wq + h * 192 + 48 * w)), RFL(hi(A.Wq + h * 192 + 48 * w)), RFL(WBYTES - (h * 192 + 48 * w) * 2), RFL(0x20000u)};
    const u32x4 s12_ = {RFL((unsigned)cm[0]), RFL((unsigned)(cm[0] >> 32)), RFL((unsigned)cm[1]), RFL((unsigned)(cm[1] >> 32))};
    const u32x4 s16_ = {RFL((unsigned)cm[2]), RFL((unsigned)(cm[2] >> 32)), RFL((unsigned)cm[3]), RFL((unsigned)(cm[3] >> 32))};
    const u32x4 s20_ = {RFL(w), RFL(L << 13), RFL(xl(RG_MEM + rep * 16384, L) + w * 2048), RFL(w < 2 ? xl(RG_MEM + rep * 16384 + 8192, L) + w * 1024 : OOB)};
    const u32x4 s24_ = {RFL(REC0 + w * 2048), RFL(QT0 + h * 32768 + w * 2048), RFL(h == 0 && w == 0 ? xl(Z_X, L) : OOB), RFL(xl(Z_X, L))};
    const u32x4 s28_ = {RFL(xl(Z_CT + (h & 7) * 256, L)), RFL(xl(Z_XN + rep * 2048, 1)), RFL(xl(Z_QW + h * 128, L)), RFL(DBG0 + ((L * 128 + h) * 4 + w) * 2048)};
    const u32x4 v0_ = {(unsigned)(((0 * 64 + l) / 6) * 12288 + ((0 * 64 + l) % 6) * 16 - 0), (unsigned)(((1 * 64 + l) / 6) * 12288 + ((1 * 64 + l) % 6) * 16 - 1024), (unsigned)(((2 * 64 + l) / 6) * 12288 + ((2 * 64 + l) % 6) * 16 - 2048), (unsigned)((8 * lq + ((l >> 2) & 1) + ((l >> 1) & 4)) * 96 + (l & 3) * 8)};
    const u32x4 v4_ = {(unsigned)(0), (unsigned)(0), (unsigned)(0), (unsigned)(l < 20 ? (l >> 2) * 128 + (l & 3) * 4 : 0)};
    const u32x4 v92_ = {(unsigned)(l * 16 + ((w + 0) & 3) * 11264), (unsigned)(l * 16 + ((w + 1) & 3) * 11264), (unsigned)(l * 16 + ((w + 2) & 3) * 11264), (unsigned)(l * 16 + ((w + 3) & 3) * 11264)};
    const u32x4 v96_ = {(unsigned)(l * 16), (unsigned)(l * 16 + 53248 + w * 20480), (unsigned)((l & 2) ? 0xeeeeu : 0x4444u), (unsigned)(135168 + w * 1808 + l16 * 112 + lq * 4)};
    const u32x4 v100_ = {(unsigned)(135168 + w * 1808 + (l >> 2) * 112 + (l & 3) * 8), (unsigned)(135168 + w * 1808 + l16 * 176 + lq * 16), (unsigned)(135168 + (l16 < 10 ? (2 * w + (lq & 1)) * 176 + l16 * 16 : 1792)), (unsigned)(142400 + w * 4352 + l16 * 272 + lq * 16)};
    const u32x4 v104_ = {(unsigned)(142400 + (4 * w + lq) * 272 + l16 * 16), (unsigned)(pm), (unsigned)(pt), (unsigned)(0xff800000u)};
    asm volatile(
    "s_mov_b32 s32, 0\n"
    "s_mul_i32 s43, s20, 13312\n"
    "s_mul_i32 s44, s20, 6144\n"
    "s_add_u32 s44, s44, 135168\n"
    "s_mul_i32 s45, s20, 20480\n"
    "s_add_u32 s45, s45, 53248\n"
    "v_add_u32 v4, s43, v3\n"
    "v_add_u32 v5, s44, v3\n"
    "v_add_u32 v6, s45, v3\n"
    "s_add_u32 s52, s20, 0\n"
    "s_and_b32 s52, s52, 3\n"
    "s_mul_i32 s52, s52, 1572864\n"
    "s_add_u32 m0, s45, 0\n"
    "s_nop 0\n"
    "buffer_load_dwordx4 v0, s[4:7], s52 offen offset:0 lds\n"
    "buffer_load_dwordx4 v1, s[4:7], s52 offen offset:1024 lds\n"
    "buffer_load_dwordx4 v2, s[4:7], s52 offen offset:2048 lds\n"
    "s_add_u32 s52, s52, 393216\n"
    "s_add_u32 m0, s45, 3072\n"
    "s_nop 0\n"
    "buffer_load_dwordx4 v0, s[4:7], s52 offen offset:0 lds\n"
    "buffer_load_dwordx4 v1, s[4:7], s52 offen offset:1024 lds\n"
    "buffer_load_dwordx4 v2, s[4:7], s52 offen offset:2048 lds\n"
    "s_add_u32 s52, s52, 393216\n"
    "s_add_u32 m0, s45, 6144\n"
    "s_nop 0\n"
    "buffer_load_dwordx4 v0, s[4:7], s52 offen offset:0 lds\n"
    "buffer_load_dwordx4 v1, s[4:7], s52 offen offset:1024 lds\n"
    "buffer_load_dwordx4 v2, s[4:7], s52 offen offset:2048 lds\n"
    "s_add_u32 s52, s52, 393216\n"
    "s_add_u32 m0, s45, 9216\n"
    "s_nop 0\n"
    "buffer_load_dwordx4 v0, s[4:7], s52 offen offset:0 lds\n"
    "buffer_load_dwordx4 v1, s[4:7], s52 offen offset:1024 lds\n"
    "buffer_load_dwordx4 v2, s[4:7], s52 offen offset:2048 lds\n"
    "s_add_u32 s52, s20, 1\n"
    "s_and_b32 s52, s52, 3\n"
    "s_mul_i32 s52, s52, 1572864\n"
    "s_add_u32 m0, s45, 12288\n"
    "s_nop 0\n"
    "buffer_load_dwordx4 v0, s[4:7], s52 offen offset:0 lds\n"
    "buffer_load_dwordx4 v1, s[4:7], s52 offen offset:1024 lds\n"
    "buffer_load_dwordx4 v2, s[4:7], s52 offen offset:2048 lds\n"
    "s_add_u32 s52, s52, 393216\n"
    "s_add_u32 m0, s45, 15360\n"
    "s_nop 0\n"
    "buffer_load_dwordx4 v0, s[4:7], s52 offen offset:0 lds\n"
    "buffer_load_dwordx4 v1, s[4:7], s52 offen offset:1024 lds\n"
    "buffer_load_dwordx4 v2, s[4:7], s52 offen offset:2048 lds\n"
    "s_add_u32 s52, s52, 393216\n"
    "s_add_u32 m0, s43, 0\n"
    "s_nop 0\n"
    "buffer_load_dwordx4 v0, s[4:7], s52 offen offset:0 lds\n"
    "buffer_load_dwordx4 v1, s[4:7], s52 offen offset:1024 lds\n"
    "buffer_load_dwordx4 v2, s[4:7], s52 offen offset:2048 lds\n"
    "s_add_u32 s52, s52, 393216\n"
    "s_add_u32 m0, s43, 3072\n"
    "s_nop 0\n"
    "buffer_load_dwordx4 v0, s[4:7], s52 offen offset:0 lds\n"
    "buffer_load_dwordx4 v1, s[4:7], s52 offen offset:1024 lds\n"
    "buffer_load_dwordx4 v2, s[4:7], s52 offen offset:2048 lds\n"
    "s_add_u32 s52, s20, 2\n"
    "s_and_b32 s52, s52, 3\n"
    "s_mul_i32 s52, s52, 1572864\n"
    "s_add_u32 m0, s43, 6144\n"
    "s_nop 0\n"
    "buffer_load_dwordx4 v0, s[4:7], s52 offen offset:0 lds\n"
    "buffer_load_dwordx4 v1, s[4:7], s52 offen offset:1024 lds\n"
    "buffer_load_dwordx4 v2, s[4:7], s52 offen offset:2048 lds\n"
    "s_add_u32 s52, s52, 393216\n"
    "s_add_u32 m0, s43, 9216\n"
    "s_nop 0\n"
    "buffer_load_dwordx4 v0, s[4:7], s52 offen offset:0 lds\n"
    "buffer_load_dwordx4 v1, s[4:7], s52 offen offset:1024 lds\n"
    "buffer_load_dwordx4 v2, s[4:7], s52 offen offset:2048 lds\n"
    "s_add_u32 s52, s52, 393216\n"
    "s_add_u32 m0, s44, 0\n"
    "s_nop 0\n"
    "buffer_load_dwordx4 v0, s[4:7], s52 offen offset:0 lds\n"
    "buffer_load_dwordx4 v1, s[4:7], s52 offen offset:1024 lds\n"
    "buffer_load_dwordx4 v2, s[4:7], s52 offen offset:2048 lds\n"
    "s_add_u32 s52, s52, 393216\n"
    "s_add_u32 m0, s44, 3072\n"
    "s_nop 0\n"
    "buffer_load_dwordx4 v0, s[4:7], s52 offen offset:0 lds\n"
    "buffer_load_dwordx4 v1, s[4:7], s52 offen offset:1024 lds\n"
    "buffer_load_dwordx4 v2, s[4:7], s52 offen offset:2048 lds\n"
    "s_waitcnt vmcnt(33)\n"
    "ds_read_b64_tr_b16 a[0:1], v6 offset:0\n"
    "ds_read_b64_tr_b16 a[2:3], v6 offset:192\n"
    "ds_read_b64_tr_b16 a[80:81], v6 offset:32\n"
    "ds_read_b64_tr_b16 a[82:83], v6 offset:224\n"
    "ds_read_b64_tr_b16 a[160:161], v6 offset:64\n"
    "ds_read_b64_tr_b16 a[162:163], v6 offset:256\n"
    "s_waitcnt lgkmcnt(0)\n"
    "s_add_u32 s52, s20, 3\n"
    "s_and_b32 s52, s52, 3\n"
    "s_mul_i32 s52, s52, 1572864\n"
    "s_add_u32 m0, s45, 0\n"
    "s_nop 0\n"
    "buffer_load_dwordx4 v0, s[4:7], s52 offen offset:0 lds\n"
    "buffer_load_dwordx4 v1, s[4:7], s52 offen offset:1024 lds\n"
    "buffer_load_dwordx4 v2, s[4:7], s52 offen offset:2048 lds\n"
    "s_waitcnt vmcnt(33)\n"
    "ds_read_b64_tr_b16 a[4:5], v6 offset:3072\n"
    "ds_read_b64_tr_b16 a[6:7], v6 offset:3264\n"
    "ds_read_b64_tr_b16 a[84:85], v6 offset:3104\n"
    "ds_read_b64_tr_b16 a[86:87], v6 offset:3296\n"
    "ds_read_b64_tr_b16 a[164:165], v6 offset:3136\n"
    "ds_read_b64_tr_b16 a[166:167], v6 offset:3328\n"
    "s_waitcnt lgkmcnt(0)\n"
    "s_add_u32 s52, s52, 393216\n"
    "s_add_u32 m0, s45, 3072\n"
    "s_nop 0\n"
    "buffer_load_dwordx4 v0, s[4:7], s52 offen offset:0 lds\n"
    "buffer_load_dwordx4 v1, s[4:7], s52 offen offset:1024 lds\n"
    "buffer_load_dwordx4 v2, s[4:7], s52 offen offset:2048 lds\n"
    "s_waitcnt vmcnt(33)\n"
    "ds_read_b64_tr_b16 a[8:9], v6 offset:6144\n"
    "ds_read_b64_tr_b16 a[10:11], v6 offset:6336\n"
    "ds_read_b64_tr_b16 a[88:89], v6 offset:6176\n"
    "ds_read_b64_tr_b16 a[90:91], v6 offset:6368\n"
    "ds_read_b64_tr_b16 a[168:169], v6 offset:6208\n"
    "ds_read_b64_tr_b16 a[170:171], v6 offset:6400\n"
    "s_waitcnt lgkmcnt(0)\n"
    "s_add_u32 s52, s52, 393216\n"
    "s_add_u32 m0, s45, 6144\n"
    "s_nop 0\n"
    "buffer_load_dwordx4 v0, s[4:7], s52 offen offset:0 lds\n"
    "buffer_load_dwordx4 v1, s[4:7], s52 offen offset:1024 lds\n"
    "buffer_load_dwordx4 v2, s[4:7], s52 offen offset:2048 lds\n"
    "s_waitcnt vmcnt(33)\n"
    "ds_read_b64_tr_b16 a[12:13], v6 offset:9216\n"
    "ds_read_b64_tr_b16 a[14:15], v6 offset:9408\n"
    "ds_read_b64_tr_b16 a[92:93], v6 offset:9248\n"
    "ds_read_b64_tr_b16 a[94:95], v6 offset:9440\n"
    "ds_read_b64_tr_b16 a[172:173], v6 offset:9280\n"
    "ds_read_b64_tr_b16 a[174:175], v6 offset:9472\n"
    "s_waitcnt lgkmcnt(0)\n"
    "s_add_u32 s52, s52, 393216\n"
    "s_add_u32 m0, s45, 9216\n"
    "s_nop 0\n"
    "buffer_load_dwordx4 v0, s[4:7], s52 offen offset:0 lds\n"
    "buffer_load_dwordx4 v1, s[4:7], s52 offen offset:1024 lds\n"
    "buffer_load_dwordx4 v2, s[4:7], s52 offen offset:2048 lds\n"
    "s_waitcnt vmcnt(33)\n"
    "ds_read_b64_tr_b16 a[16:17], v6 offset:12288\n"
    "ds_read_b64_tr_b16 a[18:19], v6 offset:12480\n"
    "ds_read_b64_tr_b16 a[96:97], v6 offset:12320\n"
    "ds_read_b64_tr_b16 a[98:99], v6 offset:12512\n"
    "ds_read_b64_tr_b16 a[176:177], v6 offset:12352\n"
    "ds_read_b64_tr_b16 a[178:179], v6 offset:12544\n"
    "s_waitcnt lgkmcnt(0)\n"
    "s_mov_b32 s52, 6291456\n"
    "s_add_u32 m0, s45, 12288\n"
    "s_nop 0\n"
    "buffer_load_dwordx4 v0, s[4:7], s52 offen offset:0 lds\n"
    "buffer_load_dwordx4 v1, s[4:7], s52 offen offset:1024 lds\n"
    "buffer_load_dwordx4 v2, s[4:7], s52 offen offset:2048 lds\n"
    "s_waitcnt vmcnt(33)\n"
    "ds_read_b64_tr_b16 a[20:21], v6 offset:15360\n"
    "ds_read_b64_tr_b16 a[22:23], v6 offset:15552\n"
    "ds_read_b64_tr_b16 a[100:101], v6 offset:15392\n"
    "ds_read_b64_tr_b16 a[102:103], v6 offset:15584\n"
    "ds_read_b64_tr_b16 a[180:181], v6 offset:15424\n"
    "ds_read_b64_tr_b16 a[182:183], v6 offset:15616\n"
    "s_waitcnt lgkmcnt(0)\n"
    "s_add_u32 s52, s52, 393216\n"
    "s_add_u32 m0, s45, 15360\n"
    "s_nop 0\n"
    "buffer_load_dwordx4 v0, s[4:7], s52 offen offset:0 lds\n"
    "buffer_load_dwordx4 v1, s[4:7], s52 offen offset:1024 lds\n"
    "buffer_load_dwordx4 v2, s[4:7], s52 offen offset:2048 lds\n"
    "s_waitcnt vmcnt(33)\n"
    "ds_read_b64_tr_b16 a[24:25], v4 offset:0\n"
    "ds_read_b64_tr_b16 a[26:27], v4 offset:192\n"
    "ds_read_b64_tr_b16 a[104:105], v4 offset:32\n"
    "ds_read_b64_tr_b16 a[106:107], v4 offset:224\n"
    "ds_read_b64_tr_b16 a[184:185], v4 offset:64\n"
    "ds_read_b64_tr_b16 a[186:187], v4 offset:256\n"
    "s_waitcnt lgkmcnt(0)\n"
    "s_add_u32 s52, s52, 393216\n"
    "s_add_u32 m0, s43, 0\n"
    "s_nop 0\n"
    "buffer_load_dwordx4 v0, s[4:7], s52 offen offset:0 lds\n"
    "buffer_load_dwordx4 v1, s[4:7], s52 offen offset:1024 lds\n"
    "buffer_load_dwordx4 v2, s[4:7], s52 offen offset:2048 lds\n"
    "s_waitcnt vmcnt(33)\n"
    "ds_read_b64_tr_b16 a[28:29], v4 offset:3072\n"
    "ds_read_b64_tr_b16 a[30:31], v4 offset:3264\n"
    "ds_read_b64_tr_b16 a[108:109], v4 offset:3104\n"
    "ds_read_b64_tr_b16 a[110:111], v4 offset:3296\n"
    "ds_read_b64_tr_b16 a[188:189], v4 offset:3136\n"
    "ds_read_b64_tr_b16 a[190:191], v4 offset:3328\n"
    "s_waitcnt lgkmcnt(0)\n"
    "s_add_u32 s52, s52, 393216\n"
    "s_add_u32 m0, s43, 3072\n"
    "s_nop 0\n"
    "buffer_load_dwordx4 v0, s[4:7], s52 offen offset:0 lds\n"
    "buffer_load_dwordx4 v1, s[4:7], s52 offen offset:1024 lds\n"
    "buffer_load_dwordx4 v2, s[4:7], s52 offen offset:2048 lds\n"
    "s_waitcnt vmcnt(33)\n"
    "ds_read_b64_tr_b16 a[32:33], v4 offset:6144\n"
    "ds_read_b64_tr_b16 a[34:35], v4 offset:6336\n"
    "ds_read_b64_tr_b16 a[112:113], v4 offset:6176\n"
    "ds_read_b64_tr_b16 a[114:115], v4 offset:6368\n"
    "ds_read_b64_tr_b16 a[192:193], v4 offset:6208\n"
    "ds_read_b64_tr_b16 a[194:195], v4 offset:6400\n"
    "s_waitcnt lgkmcnt(0)\n"
    "s_add_u32 s52, s20, 0\n"
    "s_and_b32 s52, s52, 3\n"
    "s_mul_i32 s52, s52, 1572864\n"
    "s_add_u32 m0, s43, 6144\n"
    "s_nop 0\n"
    "buffer_load_dwordx4 v0, s[8:11], s52 offen offset:0 lds\n"
    "buffer_load_dwordx4 v1, s[8:11], s52 offen offset:1024 lds\n"
    "buffer_load_dwordx4 v2, s[8:11], s52 offen offset:2048 lds\n"
    "s_waitcnt vmcnt(33)\n"
    "ds_read_b64_tr_b16 a[36:37], v4 offset:9216\n"
    "ds_read_b64_tr_b16 a[38:39], v4 offset:9408\n"
    "ds_read_b64_tr_b16 a[116:117], v4 offset:9248\n"
    "ds_read_b64_tr_b16 a[118:119], v4 offset:9440\n"
    "ds_read_b64_tr_b16 a[196:197], v4 offset:9280\n"
    "ds_read_b64_tr_b16 a[198:199], v4 offset:9472\n"
    "s_waitcnt lgkmcnt(0)\n"
    "s_waitcnt vmcnt(30)\n"
    "ds_read_b64_tr_b16 a[40:41], v5 offset:0\n"
    "ds_read_b64_tr_b16 a[42:43], v5 offset:192\n"
    "ds_read_b64_tr_b16 a[120:121], v5 offset:32\n"
    "ds_read_b64_tr_b16 a[122:123], v5 offset:224\n"
    "ds_read_b64_tr_b16 a[200:201], v5 offset:64\n"
    "ds_read_b64_tr_b16 a[202:203], v5 offset:256\n"
    "s_waitcnt lgkmcnt(0)\n"
    "s_add_u32 s52, s52, 393216\n"
    "s_add_u32 m0, s43, 9216\n"
    "s_nop 0\n"
    "buffer_load_dwordx4 v0, s[8:11], s52 offen offset:0 lds\n"
    "buffer_load_dwordx4 v1, s[8:11], s52 offen offset:1024 lds\n"
    "buffer_load_dwordx4 v2, s[8:11], s52 offen offset:2048 lds\n"
    "s_waitcnt vmcnt(30)\n"
    "ds_read_b64_tr_b16 a[44:45], v5 offset:3072\n"
    "ds_read_b64_tr_b16 a[46:47], v5 offset:3264\n"
    "ds_read_b64_tr_b16 a[124:125], v5 offset:3104\n"
    "ds_read_b64_tr_b16 a[126:127], v5 offset:3296\n"
    "ds_read_b64_tr_b16 a[204:205], v5 offset:3136\n"
    "ds_read_b64_tr_b16 a[206:207], v5 offset:3328\n"
    "s_waitcnt lgkmcnt(0)\n"
    "s_waitcnt vmcnt(27)\n"
    "ds_read_b64_tr_b16 a[48:49], v6 offset:0\n"
    "ds_read_b64_tr_b16 a[50:51], v6 offset:192\n"
    "ds_read_b64_tr_b16 a[128:129], v6 offset:32\n"
    "ds_read_b64_tr_b16 a[130:131], v6 offset:224\n"
    "ds_read_b64_tr_b16 a[208:209], v6 offset:64\n"
    "ds_read_b64_tr_b16 a[210:211], v6 offset:256\n"
    "s_waitcnt lgkmcnt(0)\n"
    "s_add_u32 s52, s52, 393216\n"
    "s_add_u32 m0, s44, 0\n"
    "s_nop 0\n"
    "buffer_load_dwordx4 v0, s[8:11], s52 offen offset:0 lds\n"
    "buffer_load_dwordx4 v1, s[8:11], s52 offen offset:1024 lds\n"
    "buffer_load_dwordx4 v2, s[8:11], s52 offen offset:2048 lds\n"
    "s_waitcnt vmcnt(27)\n"
    "ds_read_b64_tr_b16 a[52:53], v6 offset:3072\n"
    "ds_read_b64_tr_b16 a[54:55], v6 offset:3264\n"
    "ds_read_b64_tr_b16 a[132:133], v6 offset:3104\n"
    "ds_read_b64_tr_b16 a[134:135], v6 offset:3296\n"
    "ds_read_b64_tr_b16 a[212:213], v6 offset:3136\n"
    "ds_read_b64_tr_b16 a[214:215], v6 offset:3328\n"
    "s_waitcnt lgkmcnt(0)\n"
    "s_add_u32 s52, s52, 393216\n"
    "s_add_u32 m0, s45, 3072\n"
    "s_nop 0\n"
    "buffer_load_dwordx4 v0, s[8:11], s52 offen offset:0 lds\n"
    "buffer_load_dwordx4 v1, s[8:11], s52 offen offset:1024 lds\n"
    "buffer_load_dwordx4 v2, s[8:11], s52 offen offset:2048 lds\n"
    "s_waitcnt vmcnt(27)\n"
    "ds_read_b64_tr_b16 a[56:57], v6 offset:6144\n"
    "ds_read_b64_tr_b16 a[58:59], v6 offset:6336\n"
    "ds_read_b64_tr_b16 a[136:137], v6 offset:6176\n"
    "ds_read_b64_tr_b16 a[138:139], v6 offset:6368\n"
    "ds_read_b64_tr_b16 a[216:217], v6 offset:6208\n"
    "ds_read_b64_tr_b16 a[218:219], v6 offset:6400\n"
    "s_waitcnt lgkmcnt(0)\n"
    "s_add_u32 s52, s20, 1\n"
    "s_and_b32 s52, s52, 3\n"
    "s_mul_i32 s52, s52, 1572864\n"
    "s_add_u32 m0, s45, 6144\n"
    "s_nop 0\n"
    "buffer_load_dwordx4 v0, s[8:11], s52 offen offset:0 lds\n"
    "buffer_load_dwordx4 v1, s[8:11], s52 offen offset:1024 lds\n"
    "buffer_load_dwordx4 v2, s[8:11], s52 offen offset:2048 lds\n"
    "s_waitcnt vmcnt(27)\n"
    "ds_read_b64_tr_b16 a[60:61], v6 offset:9216\n"
    "ds_read_b64_tr_b16 a[62:63], v6 offset:9408\n"
    "ds_read_b64_tr_b16 a[140:141], v6 offset:9248\n"
    "ds_read_b64_tr_b16 a[142:143], v6 offset:9440\n"
    "ds_read_b64_tr_b16 a[220:221], v6 offset:9280\n"
    "ds_read_b64_tr_b16 a[222:223], v6 offset:9472\n"
    "s_waitcnt lgkmcnt(0)\n"
    "s_waitcnt vmcnt(24)\n"
    "ds_read_b64_tr_b16 a[64:65], v6 offset:12288\n"
    "ds_read_b64_tr_b16 a[66:67], v6 offset:12480\n"
    "ds_read_b64_tr_b16 a[144:145], v6 offset:12320\n"
    "ds_read_b64_tr_b16 a[146:147], v6 offset:12512\n"
    "ds_read_b64_tr_b16 a[224:225], v6 offset:12352\n"
    "ds_read_b64_tr_b16 a[226:227], v6 offset:12544\n"
    "s_waitcnt lgkmcnt(0)\n"
    "s_add_u32 s52, s52, 393216\n"
    "s_add_u32 m0, s45, 9216\n"
    "s_nop 0\n"
    "buffer_load_dwordx4 v0, s[8:11], s52 offen offset:0 lds\n"
    "buffer_load_dwordx4 v1, s[8:11], s52 offen offset:1024 lds\n"
    "buffer_load_dwordx4 v2, s[8:11], s52 offen offset:2048 lds\n"
    "s_waitcnt vmcnt(24)\n"
    "ds_read_b64_tr_b16 a[68:69], v6 offset:15360\n"
    "ds_read_b64_tr_b16 a[70:71], v6 offset:15552\n"
    "ds_read_b64_tr_b16 a[148:149], v6 offset:15392\n"
    "ds_read_b64_tr_b16 a[150:151], v6 offset:15584\n"
    "ds_read_b64_tr_b16 a[228:229], v6 offset:15424\n"
    "ds_read_b64_tr_b16 a[230:231], v6 offset:15616\n"
    "s_waitcnt lgkmcnt(0)\n"
    "s_add_u32 s52, s52, 393216\n"
    "s_add_u32 m0, s45, 12288\n"
    "s_nop 0\n"
    "buffer_load_dwordx4 v0, s[8:11], s52 offen offset:0 lds\n"
    "buffer_load_dwordx4 v1, s[8:11], s52 offen offset:1024 lds\n"
    "buffer_load_dwordx4 v2, s[8:11], s52 offen offset:2048 lds\n"
    "s_waitcnt vmcnt(24)\n"
    "ds_read_b64_tr_b16 a[72:73], v4 offset:0\n"
    "ds_read_b64_tr_b16 a[74:75], v4 offset:192\n"
    "ds_read_b64_tr_b16 a[152:153], v4 offset:32\n"
    "ds_read_b64_tr_b16 a[154:155], v4 offset:224\n"
    "ds_read_b64_tr_b16 a[232:233], v4 offset:64\n"
    "ds_read_b64_tr_b16 a[234:235], v4 offset:256\n"
    "s_waitcnt lgkmcnt(0)\n"
    "s_waitcnt vmcnt(21)\n"
    "ds_read_b64_tr_b16 a[76:77], v4 offset:3072\n"
    "ds_read_b64_tr_b16 a[78:79], v4 offset:3264\n"
    "ds_read_b64_tr_b16 a[156:157], v4 offset:3104\n"
    "ds_read_b64_tr_b16 a[158:159], v4 offset:3296\n"
    "ds_read_b64_tr_b16 a[236:237], v4 offset:3136\n"
    "ds_read_b64_tr_b16 a[238:239], v4 offset:3328\n"
    "s_waitcnt lgkmcnt(0)\n"
    "s_add_u32 s52, s52, 393216\n"
    "s_add_u32 m0, s45, 15360\n"
    "s_nop 0\n"
    "buffer_load_dwordx4 v0, s[8:11], s52 offen offset:0 lds\n"
    "buffer_load_dwordx4 v1, s[8:11], s52 offen offset:1024 lds\n"
    "buffer_load_dwordx4 v2, s[8:11], s52 offen offset:2048 lds\n"
    "s_waitcnt vmcnt(21)\n"
    "ds_read_b64_tr_b16 a[240:241], v4 offset:6144\n"
    "ds_read_b64_tr_b16 a[242:243], v4 offset:6336\n"
    "ds_read_b64_tr_b16 v[176:177], v4 offset:6176\n"
    "ds_read_b64_tr_b16 v[178:179], v4 offset:6368\n"
    "ds_read_b64_tr_b16 v[8:9], v4 offset:6208\n"
    "ds_read_b64_tr_b16 v[10:11], v4 offset:6400\n"
    "s_waitcnt lgkmcnt(0)\n"
    "ds_write_b128 v97, v[8:11] offset:0\n"
    "s_add_u32 s52, s20, 2\n"
    "s_and_b32 s52, s52, 3\n"
    "s_mul_i32 s52, s52, 1572864\n"
    "s_add_u32 m0, s43, 0\n"
    "s_nop 0\n"
    "buffer_load_dwordx4 v0, s[8:11], s52 offen offset:0 lds\n"
    "buffer_load_dwordx4 v1, s[8:11], s52 offen offset:1024 lds\n"
    "buffer_load_dwordx4 v2, s[8:11], s52 offen offset:2048 lds\n"
    "s_waitcnt vmcnt(21)\n"
    "ds_read_b64_tr_b16 a[244:245], v4 offset:9216\n"
    "ds_read_b64_tr_b16 a[246:247], v4 offset:9408\n"
    "ds_read_b64_tr_b16 v[180:181], v4 offset:9248\n"
    "ds_read_b64_tr_b16 v[182:183], v4 offset:9440\n"
    "ds_read_b64_tr_b16 v[8:9], v4 offset:9280\n"
    "ds_read_b64_tr_b16 v[10:11], v4 offset:9472\n"
    "s_waitcnt lgkmcnt(0)\n"
    "ds_write_b128 v97, v[8:11] offset:1024\n"
    "s_waitcnt vmcnt(18)\n"
    "ds_read_b64_tr_b16 a[248:249], v5 offset:0\n"
    "ds_read_b64_tr_b16 a[250:251], v5 offset:192\n"
    "ds_read_b64_tr_b16 v[184:185], v5 offset:32\n"
    "ds_read_b64_tr_b16 v[186:187], v5 offset:224\n"
    "ds_read_b64_tr_b16 v[8:9], v5 offset:64\n"
    "ds_read_b64_tr_b16 v[10:11], v5 offset:256\n"
    "s_waitcnt lgkmcnt(0)\n"
    "ds_write_b128 v97, v[8:11] offset:2048\n"
    "s_add_u32 s52, s52, 393216\n"
    "s_add_u32 m0, s43, 3072\n"
    "s_nop 0\n"
    "buffer_load_dwordx4 v0, s[8:11], s52 offen offset:0 lds\n"
    "buffer_load_dwordx4 v1, s[8:11], s52 offen offset:1024 lds\n"
    "buffer_load_dwordx4 v2, s[8:11], s52 offen offset:2048 lds\n"
    "s_waitcnt vmcnt(18)\n"
    "ds_read_b64_tr_b16 a[252:253], v6 offset:3072\n"
    "ds_read_b64_tr_b16 a[254:255], v6 offset:3264\n"
    "ds_read_b64_tr_b16 v[188:189], v6 offset:3104\n"
    "ds_read_b64_tr_b16 v[190:191], v6 offset:3296\n"
    "ds_read_b64_tr_b16 v[8:9], v6 offset:3136\n"
    "ds_read_b64_tr_b16 v[10:11], v6 offset:3328\n"
    "s_waitcnt lgkmcnt(0)\n"
    "ds_write_b128 v97, v[8:11] offset:3072\n"
    "s_add_u32 s52, s52, 393216\n"
    "s_add_u32 m0, s43, 6144\n"
    "s_nop 0\n"
    "buffer_load_dwordx4 v0, s[8:11], s52 offen offset:0 lds\n"
    "buffer_load_dwordx4 v1, s[8:11], s52 offen offset:1024 lds\n"
    "buffer_load_dwordx4 v2, s[8:11], s52 offen offset:2048 lds\n"
    "s_waitcnt vmcnt(18)\n"
    "ds_read_b64_tr_b16 v[112:113], v6 offset:6144\n"
    "ds_read_b64_tr_b16 v[114:115], v6 offset:6336\n"
    "ds_read_b64_tr_b16 v[192:193], v6 offset:6176\n"
    "ds_read_b64_tr_b16 v[194:195], v6 offset:6368\n"
    "ds_read_b64_tr_b16 v[8:9], v6 offset:6208\n"
    "ds_read_b64_tr_b16 v[10:11], v6 offset:6400\n"
    "s_waitcnt lgkmcnt(0)\n"
    "ds_write_b128 v97, v[8:11] offset:4096\n"
    "s_add_u32 s52, s52, 393216\n"
    "s_add_u32 m0, s43, 9216\n"
    "s_nop 0\n"
    "buffer_load_dwordx4 v0, s[8:11], s52 offen offset:0 lds\n"
    "buffer_load_dwordx4 v1, s[8:11], s52 offen offset:1024 lds\n"
    "buffer_load_dwordx4 v2, s[8:11], s52 offen offset:2048 lds\n"
    "s_waitcnt vmcnt(18)\n"
    "ds_read_b64_tr_b16 v[116:117], v6 offset:9216\n"
    "ds_read_b64_tr_b16 v[118:119], v6 offset:9408\n"
    "ds_read_b64_tr_b16 v[196:197], v6 offset:9248\n"
    "ds_read_b64_tr_b16 v[198:199], v6 offset:9440\n"
    "ds_read_b64_tr_b16 v[8:9], v6 offset:9280\n"
    "ds_read_b64_tr_b16 v[10:11], v6 offset:9472\n"
    "s_waitcnt lgkmcnt(0)\n"
    "ds_write_b128 v97, v[8:11] offset:5120\n"
    "s_add_u32 s52, s20, 3\n"
    "s_and_b32 s52, s52, 3\n"
    "s_mul_i32 s52, s52, 1572864\n"
    "s_add_u32 m0, s44, 0\n"
    "s_nop 0\n"
    "buffer_load_dwordx4 v0, s[8:11], s52 offen offset:0 lds\n"
    "buffer_load_dwordx4 v1, s[8:11], s52 offen offset:1024 lds\n"
    "buffer_load_dwordx4 v2, s[8:11], s52 offen offset:2048 lds\n"
    "s_waitcnt vmcnt(18)\n"
    "ds_read_b64_tr_b16 v[120:121], v6 offset:12288\n"
    "ds_read_b64_tr_b16 v[122:123], v6 offset:12480\n"
    "ds_read_b64_tr_b16 v[200:201], v6 offset:12320\n"
    "ds_read_b64_tr_b16 v[202:203], v6 offset:12512\n"
    "ds_read_b64_tr_b16 v[8:9], v6 offset:12352\n"
    "ds_read_b64_tr_b16 v[10:11], v6 offset:12544\n"
    "s_waitcnt lgkmcnt(0)\n"
    "ds_write_b128 v97, v[8:11] offset:6144\n"
    "s_add_u32 s52, s52, 393216\n"
    "s_add_u32 m0, s44, 3072\n"
    "s_nop 0\n"
    "buffer_load_dwordx4 v0, s[8:11], s52 offen offset:0 lds\n"
    "buffer_load_dwordx4 v1, s[8:11], s52 offen offset:1024 lds\n"
    "buffer_load_dwordx4 v2, s[8:11], s52 offen offset:2048 lds\n"
    "s_waitcnt vmcnt(18)\n"
    "ds_read_b64_tr_b16 v[124:125], v6 offset:15360\n"
    "ds_read_b64_tr_b16 v[126:127], v6 offset:15552\n"
    "ds_read_b64_tr_b16 v[204:205], v6 offset:15392\n"
    "ds_read_b64_tr_b16 v[206:207], v6 offset:15584\n"
    "ds_read_b64_tr_b16 v[8:9], v6 offset:15424\n"
    "ds_read_b64_tr_b16 v[10:11], v6 offset:15616\n"
    "s_waitcnt lgkmcnt(0)\n"
    "ds_write_b128 v97, v[8:11] offset:7168\n"
    "s_add_u32 s52, s52, 393216\n"
    "s_add_u32 m0, s45, 15360\n"
    "s_nop 0\n"
    "buffer_load_dwordx4 v0, s[8:11], s52 offen offset:0 lds\n"
    "buffer_load_dwordx4 v1, s[8:11], s52 offen offset:1024 lds\n"
    "buffer_load_dwordx4 v2, s[8:11], s52 offen offset:2048 lds\n"
    "s_waitcnt vmcnt(18)\n"
    "ds_read_b64_tr_b16 v[128:129], v4 offset:0\n"
    "ds_read_b64_tr_b16 v[130:131], v4 offset:192\n"
    "ds_read_b64_tr_b16 v[208:209], v4 offset:32\n"
    "ds_read_b64_tr_b16 v[210:211], v4 offset:224\n"
    "ds_read_b64_tr_b16 v[8:9], v4 offset:64\n"
    "ds_read_b64_tr_b16 v[10:11], v4 offset:256\n"
    "s_waitcnt lgkmcnt(0)\n"
    "ds_write_b128 v97, v[8:11] offset:8192\n"
    "s_add_u32 s52, s52, 393216\n"
    "s_add_u32 m0, s43, 0\n"
    "s_nop 0\n"
    "buffer_load_dwordx4 v0, s[8:11], s52 offen offset:0 lds\n"
    "buffer_load_dwordx4 v1, s[8:11], s52 offen offset:1024 lds\n"
    "buffer_load_dwordx4 v2, s[8:11], s52 offen offset:2048 lds\n"
    "s_waitcnt vmcnt(18)\n"
    "ds_read_b64_tr_b16 v[132:133], v4 offset:3072\n"
    "ds_read_b64_tr_b16 v[134:135], v4 offset:3264\n"
    "ds_read_b64_tr_b16 v[212:213], v4 offset:3104\n"
    "ds_read_b64_tr_b16 v[214:215], v4 offset:3296\n"
    "ds_read_b64_tr_b16 v[8:9], v4 offset:3136\n"
    "ds_read_b64_tr_b16 v[10:11], v4 offset:3328\n"
    "s_waitcnt lgkmcnt(0)\n"
    "ds_write_b128 v97, v[8:11] offset:9216\n"
    "s_mov_b32 s52, 6291456\n"
    "s_add_u32 m0, s43, 3072\n"
    "s_nop 0\n"
    "buffer_load_dwordx4 v0, s[8:11], s52 offen offset:0 lds\n"
    "buffer_load_dwordx4 v1, s[8:11], s52 offen offset:1024 lds\n"
    "buffer_load_dwordx4 v2, s[8:11], s52 offen offset:2048 lds\n"
    "s_waitcnt vmcnt(18)\n"
    "ds_read_b64_tr_b16 v[136:137], v4 offset:6144\n"
    "ds_read_b64_tr_b16 v[138:139], v4 offset:6336\n"
    "ds_read_b64_tr_b16 v[216:217], v4 offset:6176\n"
    "ds_read_b64_tr_b16 v[218:219], v4 offset:6368\n"
    "ds_read_b64_tr_b16 v[8:9], v4 offset:6208\n"
    "ds_read_b64_tr_b16 v[10:11], v4 offset:6400\n"
    "s_waitcnt lgkmcnt(0)\n"
    "ds_write_b128 v97, v[8:11] offset:10240\n"
    "s_waitcnt vmcnt(15)\n"
    "ds_read_b64_tr_b16 v[140:141], v4 offset:9216\n"
    "ds_read_b64_tr_b16 v[142:143], v4 offset:9408\n"
    "ds_read_b64_tr_b16 v[220:221], v4 offset:9248\n"
    "ds_read_b64_tr_b16 v[222:223], v4 offset:9440\n"
    "ds_read_b64_tr_b16 v[8:9], v4 offset:9280\n"
    "ds_read_b64_tr_b16 v[10:11], v4 offset:9472\n"
    "s_waitcnt lgkmcnt(0)\n"
    "ds_write_b128 v97, v[8:11] offset:11264\n"
    "s_add_u32 s52, s52, 393216\n"
    "s_add_u32 m0, s43, 6144\n"
    "s_nop 0\n"
    "buffer_load_dwordx4 v0, s[8:11], s52 offen offset:0 lds\n"
    "buffer_load_dwordx4 v1, s[8:11], s52 offen offset:1024 lds\n"
    "buffer_load_dwordx4 v2, s[8:11], s52 offen offset:2048 lds\n"
    "s_waitcnt vmcnt(15)\n"
    "ds_read_b64_tr_b16 v[144:145], v5 offset:0\n"
    "ds_read_b64_tr_b16 v[146:147], v5 offset:192\n"
    "ds_read_b64_tr_b16 v[224:225], v5 offset:32\n"
    "ds_read_b64_tr_b16 v[226:227], v5 offset:224\n"
    "ds_read_b64_tr_b16 v[8:9], v5 offset:64\n"
    "ds_read_b64_tr_b16 v[10:11], v5 offset:256\n"
    "s_waitcnt lgkmcnt(0)\n"
    "ds_write_b128 v97, v[8:11] offset:12288\n"
    "s_add_u32 s52, s52, 393216\n"
    "s_add_u32 m0, s43, 9216\n"
    "s_nop 0\n"
    "buffer_load_dwordx4 v0, s[8:11], s52 offen offset:0 lds\n"
    "buffer_load_dwordx4 v1, s[8:11], s52 offen offset:1024 lds\n"
    "buffer_load_dwordx4 v2, s[8:11], s52 offen offset:2048 lds\n"
    "s_waitcnt vmcnt(15)\n"
    "ds_read_b64_tr_b16 v[148:149], v5 offset:3072\n"
    "ds_read_b64_tr_b16 v[150:151], v5 offset:3264\n"
    "ds_read_b64_tr_b16 v[228:229], v5 offset:3104\n"
    "ds_read_b64_tr_b16 v[230:231], v5 offset:3296\n"
    "ds_read_b64_tr_b16 v[8:9], v5 offset:3136\n"
    "ds_read_b64_tr_b16 v[10:11], v5 offset:3328\n"
    "s_waitcnt lgkmcnt(0)\n"
    "ds_write_b128 v97, v[8:11] offset:13312\n"
    "s_waitcnt vmcnt(12)\n"
    "ds_read_b64_tr_b16 v[152:153], v6 offset:15360\n"
    "ds_read_b64_tr_b16 v[154:155], v6 offset:15552\n"
    "ds_read_b64_tr_b16 v[232:233], v6 offset:15392\n"
    "ds_read_b64_tr_b16 v[234:235], v6 offset:15584\n"
    "ds_read_b64_tr_b16 v[8:9], v6 offset:15424\n"
    "ds_read_b64_tr_b16 v[10:11], v6 offset:15616\n"
    "s_waitcnt lgkmcnt(0)\n"
    "ds_write_b128 v97, v[8:11] offset:14336\n"
    "s_add_u32 s52, s52, 393216\n"
    "s_add_u32 m0, s44, 0\n"
    "s_nop 0\n"
    "buffer_load_dwordx4 v0, s[8:11], s52 offen offset:0 lds\n"
    "buffer_load_dwordx4 v1, s[8:11], s52 offen offset:1024 lds\n"
    "buffer_load_dwordx4 v2, s[8:11], s52 offen offset:2048 lds\n"
    "s_waitcnt vmcnt(12)\n"
    "ds_read_b64_tr_b16 v[156:157], v4 offset:0\n"
    "ds_read_b64_tr_b16 v[158:159], v4 offset:192\n"
    "ds_read_b64_tr_b16 v[236:237], v4 offset:32\n"
    "ds_read_b64_tr_b16 v[238:239], v4 offset:224\n"
    "ds_read_b64_tr_b16 v[8:9], v4 offset:64\n"
    "ds_read_b64_tr_b16 v[10:11], v4 offset:256\n"
    "s_waitcnt lgkmcnt(0)\n"
    "ds_write_b128 v97, v[8:11] offset:15360\n"
    "s_waitcnt vmcnt(9)\n"
    "ds_read_b64_tr_b16 v[160:161], v4 offset:3072\n"
    "ds_read_b64_tr_b16 v[162:163], v4 offset:3264\n"
    "ds_read_b64_tr_b16 v[240:241], v4 offset:3104\n"
    "ds_read_b64_tr_b16 v[242:243], v4 offset:3296\n"
    "ds_read_b64_tr_b16 v[8:9], v4 offset:3136\n"
    "ds_read_b64_tr_b16 v[10:11], v4 offset:3328\n"
    "s_waitcnt lgkmcnt(0)\n"
    "ds_write_b128 v97, v[8:11] offset:16384\n"
    "s_waitcnt vmcnt(6)\n"
    "ds_read_b64_tr_b16 v[164:165], v4 offset:6144\n"
    "ds_read_b64_tr_b16 v[166:167], v4 offset:6336\n"
    "ds_read_b64_tr_b16 v[244:245], v4 offset:6176\n"
    "ds_read_b64_tr_b16 v[246:247], v4 offset:6368\n"
    "ds_read_b64_tr_b16 v[8:9], v4 offset:6208\n"
    "ds_read_b64_tr_b16 v[10:11], v4 offset:6400\n"
    "s_waitcnt lgkmcnt(0)\n"
    "ds_write_b128 v97, v[8:11] offset:17408\n"
    "s_waitcnt vmcnt(3)\n"
    "ds_read_b64_tr_b16 v[168:169], v4 offset:9216\n"
    "ds_read_b64_tr_b16 v[170:171], v4 offset:9408\n"
    "ds_read_b64_tr_b16 v[248:249], v4 offset:9248\n"
    "ds_read_b64_tr_b16 v[250:251], v4 offset:9440\n"
    "ds_read_b64_tr_b16 v[8:9], v4 offset:9280\n"
    "ds_read_b64_tr_b16 v[10:11], v4 offset:9472\n"
    "s_waitcnt lgkmcnt(0)\n"
    "ds_write_b128 v97, v[8:11] offset:18432\n"
    "s_waitcnt vmcnt(0)\n"
    "ds_read_b64_tr_b16 v[172:173], v5 offset:0\n"
    "ds_read_b64_tr_b16 v[174:175], v5 offset:192\n"
    "ds_read_b64_tr_b16 v[252:253], v5 offset:32\n"
    "ds_read_b64_tr_b16 v[254:255], v5 offset:224\n"
    "ds_read_b64_tr_b16 v[8:9], v5 offset:64\n"
    "ds_read_b64_tr_b16 v[10:11], v5 offset:256\n"
    "s_waitcnt lgkmcnt(0)\n"
    "ds_write_b128 v97, v[8:11] offset:19456\n"
    "s_waitcnt lgkmcnt(0)\n"
    "s_barrier\n"
    "s_mov_b32 s38, -2\n"
    "s_mov_b32 s54, 1037384597\n"
    "s_mov_b32 s55, 1037384597\n"
    "s_mov_b32 s56, 16711935\n"
    "s_mov_b32 s57, 16711935\n"
    "s_mov_b32 s58, 0\n"
    "s_mov_b32 s59, 16711935\n"
    "s_mov_b32 s60, 0\n"
    "s_mov_b32 s61, -1\n"
    "s_mov_b32 s62, 67044351\n"
    "s_mov_b32 s63, 67044351\n"
    "v_mov_b32 v8, v107\n"
    "v_mov_b32 v9, v107\n"
    "v_mov_b32 v10, 136960\n"
    "ds_write_b64 v10, v[8:9] offset:0\n"
    "ds_write_b64 v10, v[8:9] offset:8\n"
    "ds_write_b64 v10, v[8:9] offset:1808\n"
    "ds_write_b64 v10, v[8:9] offset:1816\n"
    "ds_write_b64 v10, v[8:9] offset:3616\n"
    "ds_write_b64 v10, v[8:9] offset:3624\n"
    "ds_write_b64 v10, v[8:9] offset:5424\n"
    "ds_write_b64 v10, v[8:9] offset:5432\n"
    "s_lshl_b32 s51, s20, 11\n"
    "v_lshrrev_b32 v4, 2, v96\n"
    "v_and_b32 v6, 12, v4\n"
    "s_lshr_b32 s52, s21, 12\n"
    ".Lqk_po1_%=:\n"
    "buffer_load_dword v8, v4, s[0:3], s28 offen sc1\n"
    "s_waitcnt vmcnt(0)\n"
    "v_cmp_gt_i32 vcc, 1, v8\n"
    "s_nop 0\n"
    "s_cbranch_vccz .Lqk_pd2_%=\n"
    "s_add_u32 s32, s32, 1\n"
    "s_cmp_lt_u32 s32, 0x40000\n"
    "s_cbranch_scc1 .Lqk_sp3_%=\n"
    "v_mov_b32 v8, 20\n"
    "s_mov_b32 s36, 33728\n"
    "buffer_store_dword v8, off, s[0:3], s36 sc1\n"
    "s_waitcnt vmcnt(0)\n"
    "s_branch .Lqk_pd2_%=\n"
    ".Lqk_sp3_%=:\n"
    "s_sleep 4\n"
    "s_branch .Lqk_po1_%=\n"
    ".Lqk_pd2_%=:\n"
    ".Lqk_po4_%=:\n"
    "buffer_load_dword v8, v7, s[0:3], s29 offen sc1\n"
    "s_waitcnt vmcnt(0)\n"
    "v_cmp_gt_i32 vcc, s52, v8\n"
    "s_nop 0\n"
    "s_cbranch_vccz .Lqk_pd5_%=\n"
    "s_add_u32 s32, s32, 1\n"
    "s_cmp_lt_u32 s32, 0x40000\n"
    "s_cbranch_scc1 .Lqk_sp6_%=\n"
    "v_mov_b32 v8, 21\n"
    "s_mov_b32 s36, 33728\n"
    "buffer_store_dword v8, off, s[0:3], s36 sc1\n"
    "s_waitcnt vmcnt(0)\n"
    "s_branch .Lqk_pd5_%=\n"
    ".Lqk_sp6_%=:\n"
    "s_sleep 4\n"
    "s_branch .Lqk_po4_%=\n"
    ".Lqk_pd5_%=:\n"
    "s_add_u32 s45, s24, s21\n"
    "s_add_u32 m0, s51, 0\n"
    "s_nop 0\n"
    "buffer_load_dwordx4 v96, s[0:3], s45 offen sc1 lds\n"
    "buffer_load_dwordx4 v96, s[0:3], s45 offen offset:1024 sc1 lds\n"
    "s_add_u32 m0, s51, 11264\n"
    "s_add_u32 s45, s45, 0x4000\n"
    "buffer_load_dwordx4 v96, s[0:3], s45 offen sc1 lds\n"
    "buffer_load_dwordx4 v96, s[0:3], s45 offen offset:1024 sc1 lds\n"
    "s_add_u32 m0, s51, 22528\n"
    "s_add_u32 s45, s45, 0x4000\n"
    "buffer_load_dwordx4 v96, s[0:3], s45 offen sc1 lds\n"
    "buffer_load_dwordx4 v96, s[0:3], s45 offen offset:1024 sc1 lds\n"
    "s_add_u32 m0, s51, 33792\n"
    "s_add_u32 s45, s45, 0x4000\n"
    "buffer_load_dwordx4 v96, s[0:3], s45 offen sc1 lds\n"
    "buffer_load_dwordx4 v96, s[0:3], s45 offen offset:1024 sc1 lds\n"
    "s_add_u32 m0, s51, 45056\n"
    "s_add_u32 s45, s45, 0x4000\n"
    "buffer_load_dwordx4 v96, s[0:3], s45 offen sc1 lds\n"
    "buffer_load_dwordx4 v96, s[0:3], s45 offen offset:1024 sc1 lds\n"
    ".Lqk_po7_%=:\n"
    "buffer_load_dword v8, v6, s[0:3], s30 offen sc1\n"
    "s_waitcnt vmcnt(0)\n"
    "v_cmp_gt_i32 vcc, 2, v8\n"
    "s_nop 0\n"
    "s_cbranch_vccz .Lqk_pd8_%=\n"
    "s_add_u32 s32, s32, 1\n"
    "s_cmp_lt_u32 s32, 0x40000\n"
    "s_cbranch_scc1 .Lqk_sp9_%=\n"
    "v_mov_b32 v8, 22\n"
    "s_mov_b32 s36, 33728\n"
    "buffer_store_dword v8, off, s[0:3], s36 sc1\n"
    "s_waitcnt vmcnt(0)\n"
    "s_branch .Lqk_pd8_%=\n"
    ".Lqk_sp9_%=:\n"
    "s_sleep 4\n"
    "s_branch .Lqk_po7_%=\n"
    ".Lqk_pd8_%=:\n"
    "s_memrealtime s[34:35]\n"
    "s_waitcnt lgkmcnt(0)\n"
    "s_sub_u32 s49, s34, 912\n"
    "s_mov_b32 s38, -1\n"
    "s_mov_b32 s39, -5\n"
    "s_mov_b32 s42, 2130706432\n"
    "s_mov_b32 s41, 4194304\n"
    "s_mov_b32 s47, s27\n"
    "s_add_u32 s46, s25, s21\n"
    "s_branch .Lqk_bulk_%=\n"
    ".Lqk_take_%=:\n"
    "s_waitcnt vmcnt(2) lgkmcnt(0)\n"
    "ds_write_b128 v92, v[56:59] offset:8192\n"
    "v_smfmac_f32_16x16x64_bf16 v[8:11], v[56:59], v[32:39], v98\n"
    "v_smfmac_f32_16x16x64_bf16 v[12:15], v[56:59], a[160:167], v98\n"
    "s_waitcnt vmcnt(1)\n"
    "ds_write_b128 v92, v[60:63] offset:9216\n"
    "v_max3_u32 v108, v56, v58, v60\n"
    "v_smfmac_f32_16x16x64_bf16 v[0:3], v[56:59], v[176:183], v98\n"
    "s_waitcnt vmcnt(0)\n"
    "ds_write_b128 v92, v[64:67] offset:10240\n"
    "v_max3_u32 v108, v108, v62, v64\n"
    "v_smfmac_f32_16x16x64_bf16 v[4:7], v[56:59], a[80:87], v98\n"
    "v_max_u32 v108, v108, v66\n"
    "v_cmp_eq_u32 vcc, -1, v108\n"
    "v_smfmac_f32_16x16x64_bf16 v[16:19], v[56:59], a[240:247], v98\n"
    "s_cbranch_vccnz .Lqk_retry_%=\n"
    ".Lqk_go_%=:\n"
    "v_smfmac_f32_16x16x64_bf16 v[20:23], v[56:59], a[0:7], v98\n"
    "s_waitcnt lgkmcnt(0)\n"
    "s_barrier\n"
    "ds_read_b128 v[24:27], v93 offset:8192\n"
    "s_memrealtime s[64:65]\n"
    "ds_read_b128 v[28:31], v93 offset:9216\n"
    "v_smfmac_f32_16x16x64_bf16 v[8:11], v[60:63], v[40:47], v98\n"
    "ds_read_b128 v[32:35], v97 offset:6144\n"
    "s_add_u32 m0, s51, 0\n"
    "v_smfmac_f32_16x16x64_bf16 v[12:15], v[60:63], a[168:175], v98\n"
    "ds_read_b128 v[36:39], v97 offset:7168\n"
    "buffer_load_dwordx4 v96, s[0:3], s45 offen sc1 lds\n"
    "v_smfmac_f32_16x16x64_bf16 v[0:3], v[60:63], v[184:191], v98\n"
    "ds_read_b128 v[64:67], v94 offset:8192\n"
    "v_smfmac_f32_16x16x64_bf16 v[4:7], v[60:63], a[88:95], v98\n"
    "ds_read_b128 v[40:43], v97 offset:8192\n"
    "v_smfmac_f32_16x16x64_bf16 v[16:19], v[60:63], a[248:255], v98\n"
    "ds_read_b128 v[44:47], v97 offset:9216\n"
    "v_smfmac_f32_16x16x64_bf16 v[20:23], v[60:63], a[8:15], v98\n"
    "ds_read_b128 v[56:59], v94 offset:9216\n"
    "s_waitcnt lgkmcnt(7)\n"
    "v_smfmac_f32_16x16x64_bf16 v[8:11], v[24:27], v[48:55], v98\n"
    "ds_read_b128 v[48:51], v97 offset:10240\n"
    "buffer_load_dwordx4 v96, s[0:3], s45 offen offset:1024 sc1 lds\n"
    "v_smfmac_f32_16x16x64_bf16 v[12:15], v[24:27], a[176:183], v98\n"
    "ds_read_b128 v[52:55], v97 offset:11264\n"
    "s_add_u32 m0, s51, 11264\n"
    "v_smfmac_f32_16x16x64_bf16 v[0:3], v[24:27], v[192:199], v98\n"
    "s_add_u32 s45, s45, 0x4000\n"
    "v_smfmac_f32_16x16x64_bf16 v[4:7], v[24:27], a[96:103], v98\n"
    "v_smfmac_f32_16x16x64_bf16 v[16:19], v[24:27], v[112:119], v98\n"
    "buffer_load_dwordx4 v96, s[0:3], s45 offen sc1 lds\n"
    "v_smfmac_f32_16x16x64_bf16 v[20:23], v[24:27], a[16:23], v98\n"
    "s_waitcnt lgkmcnt(0)\n"
    "v_smfmac_f32_16x16x64_bf16 v[8:11], v[28:31], v[32:39], v98\n"
    "ds_read_b128 v[32:35], v97 offset:12288\n"
    "s_sub_u32 s36, s64, s49\n"
    "v_smfmac_f32_16x16x64_bf16 v[12:15], v[28:31], a[184:191], v98\n"
    "ds_read_b128 v[36:39], v97 offset:13312\n"
    "s_add_u32 s37, s64, 24\n"
    "v_smfmac_f32_16x16x64_bf16 v[0:3], v[28:31], v[200:207], v98\n"
    "ds_read_b128 v[60:63], v95 offset:8192\n"
    "buffer_load_dwordx4 v96, s[0:3], s45 offen offset:1024 sc1 lds\n"
    "v_smfmac_f32_16x16x64_bf16 v[4:7], v[28:31], a[104:111], v98\n"
    "ds_read_b128 v[24:27], v95 offset:9216\n"
    "s_add_u32 m0, s51, 22528\n"
    "v_smfmac_f32_16x16x64_bf16 v[16:19], v[28:31], v[120:127], v98\n"
    "s_add_u32 s45, s45, 0x4000\n"
    "s_cmp_gt_i32 s36, 0\n"
    "s_cselect_b32 s49, s37, s49\n"
    "v_smfmac_f32_16x16x64_bf16 v[20:23], v[28:31], a[24:31], v98\n"
    "ds_read_b128 v[28:31], v96 offset:10240\n"
    "v_mov_b32 v108, s49\n"
    "v_smfmac_f32_16x16x64_bf16 v[8:11], v[64:67], v[40:47], v98\n"
    "ds_read_b128 v[40:43], v97 offset:14336\n"
    "buffer_load_dwordx4 v96, s[0:3], s45 offen sc1 lds\n"
    "v_smfmac_f32_16x16x64_bf16 v[12:15], v[64:67], a[192:199], v98\n"
    "ds_read_b128 v[44:47], v97 offset:15360\n"
    "v_mov_b32 v109, s39\n"
    "v_smfmac_f32_16x16x64_bf16 v[0:3], v[64:67], v[208:215], v98\n"
    "v_lshlrev_b32 v110, 10, v96\n"
    "v_smfmac_f32_16x16x64_bf16 v[4:7], v[64:67], a[112:119], v98\n"
    "v_smfmac_f32_16x16x64_bf16 v[16:19], v[64:67], v[128:135], v98\n"
    "buffer_load_dwordx4 v96, s[0:3], s45 offen offset:1024 sc1 lds\n"
    "s_add_u32 m0, s51, 33792\n"
    "v_smfmac_f32_16x16x64_bf16 v[20:23], v[64:67], a[32:39], v98\n"
    "ds_read_b128 v[64:67], v96 offset:21504\n"
    "s_add_u32 s45, s45, 0x4000\n"
    "v_smfmac_f32_16x16x64_bf16 v[8:11], v[56:59], v[48:55], v98\n"
    "ds_read_b128 v[48:51], v97 offset:16384\n"
    "v_smfmac_f32_16x16x64_bf16 v[12:15], v[56:59], a[200:207], v98\n"
    "ds_read_b128 v[52:55], v97 offset:17408\n"
    "s_waitcnt lgkmcnt(3)\n"
    "v_smfmac_f32_16x16x64_bf16 v[8:11], v[60:63], v[32:39], v98\n"
    "ds_read_b128 v[32:35], v97 offset:18432\n"
    "buffer_load_dwordx4 v96, s[0:3], s45 offen sc1 lds\n"
    "v_smfmac_f32_16x16x64_bf16 v[12:15], v[60:63], a[208:215], v98\n"
    "ds_read_b128 v[36:39], v97 offset:19456\n"
    "v_smfmac_f32_16x16x64_bf16 v[8:11], v[24:27], v[40:47], v98\n"
    "v_smfmac_f32_16x16x64_bf16 v[12:15], v[24:27], a[216:223], v98\n"
    "v_smfmac_f32_16x16x64_bf16 v[0:3], v[56:59], v[216:223], v98\n"
    "buffer_load_dwordx4 v96, s[0:3], s45 offen offset:1024 sc1 lds\n"
    "s_add_u32 m0, s51, 45056\n"
    "v_smfmac_f32_16x16x64_bf16 v[4:7], v[56:59], a[120:127], v98\n"
    "s_add_u32 s45, s45, 0x4000\n"
    "v_smfmac_f32_16x16x64_bf16 v[0:3], v[60:63], v[224:231], v98\n"
    "v_smfmac_f32_16x16x64_bf16 v[4:7], v[60:63], a[128:135], v98\n"
    "s_waitcnt lgkmcnt(0)\n"
    "v_smfmac_f32_16x16x64_bf16 v[8:11], v[28:31], v[48:55], v98\n"
    "buffer_load_dwordx4 v96, s[0:3], s45 offen sc1 lds\n"
    "v_smfmac_f32_16x16x64_bf16 v[12:15], v[28:31], a[224:231], v98\n"
    "v_smfmac_f32_16x16x64_bf16 v[8:11], v[64:67], v[32:39], v98\n"
    "v_smfmac_f32_16x16x64_bf16 v[12:15], v[64:67], a[232:239], v98\n"
    "v_smfmac_f32_16x16x64_bf16 v[0:3], v[24:27], v[232:239], v98\n"
    "buffer_load_dwordx4 v96, s[0:3], s45 offen offset:1024 sc1 lds\n"
    "v_smfmac_f32_16x16x64_bf16 v[4:7], v[24:27], a[136:143], v98\n"
    "v_smfmac_f32_16x16x64_bf16 v[0:3], v[28:31], v[240:247], v98\n"
    "v_smfmac_f32_16x16x64_bf16 v[4:7], v[28:31], a[144:151], v98\n"
    "v_smfmac_f32_16x16x64_bf16 v[0:3], v[64:67], v[248:255], v98\n"
    "s_mov_b64 exec, 15\n"
    "buffer_store_dwordx2 v[108:109], v110, s[0:3], s48 offen sc1\n"
    "s_mov_b64 exec, -1\n"
    "v_smfmac_f32_16x16x64_bf16 v[4:7], v[64:67], a[152:159], v98\n"
    "v_smfmac_f32_16x16x64_bf16 v[16:19], v[56:59], v[136:143], v98\n"
    "v_add_f32 v8, v8, v10\n"
    "v_add_f32 v9, v9, v11\n"
    "v_smfmac_f32_16x16x64_bf16 v[20:23], v[56:59], a[40:47], v98\n"
    "v_add_f32 v12, v12, v14\n"
    "v_add_f32 v13, v13, v15\n"
    "v_smfmac_f32_16x16x64_bf16 v[16:19], v[60:63], v[144:151], v98\n"
    "v_cvt_pk_bf16_f32 v8, v8, v9\n"
    "v_cvt_pk_bf16_f32 v12, v12, v13\n"
    "v_smfmac_f32_16x16x64_bf16 v[20:23], v[60:63], a[48:55], v98\n"
    "ds_write2_b32 v99, v8, v12 offset0:8 offset1:12\n"
    "ds_read_b64_tr_b16 v[14:15], v100 offset:32\n"
    "v_smfmac_f32_16x16x64_bf16 v[16:19], v[24:27], v[152:159], v98\n"
    "v_add_f32 v0, v0, v2\n"
    "v_add_f32 v1, v1, v3\n"
    "v_smfmac_f32_16x16x64_bf16 v[20:23], v[24:27], a[56:63], v98\n"
    "v_add_f32 v4, v4, v6\n"
    "v_add_f32 v5, v5, v7\n"
    "v_smfmac_f32_16x16x64_bf16 v[16:19], v[28:31], v[160:167], v98\n"
    "v_cvt_pk_bf16_f32 v0, v0, v1\n"
    "v_cvt_pk_bf16_f32 v4, v4, v5\n"
    "v_smfmac_f32_16x16x64_bf16 v[20:23], v[28:31], a[64:71], v98\n"
    "ds_write2_b32 v99, v0, v4 offset0:0 offset1:4\n"
    "ds_read_b64_tr_b16 v[12:13], v100 offset:0\n"
    "v_smfmac_f32_16x16x64_bf16 v[16:19], v[64:67], v[168:175], v98\n"
    "v_smfmac_f32_16x16x64_bf16 v[20:23], v[64:67], a[72:79], v98\n"
    "s_nop 6\n"
    "v_pk_add_f32 v[16:17], v[16:17], v[18:19]\n"
    "v_pk_add_f32 v[20:21], v[20:21], v[22:23]\n"
    "v_cvt_pk_bf16_f32 v16, v16, v17\n"
    "v_cvt_pk_bf16_f32 v20, v20, v21\n"
    "ds_write2_b32 v99, v16, v20 offset0:16 offset1:20\n"
    "ds_read_b64_tr_b16 v[4:5], v100 offset:64\n"
    "s_waitcnt lgkmcnt(2)\n"
    "v_mfma_f32_16x16x32_bf16 v[8:11], v[68:71], v[12:15], 0\n"
    "v_mfma_f32_16x16x32_bf16 v[0:3], v[72:75], v[12:15], 0\n"
    "v_mfma_f32_16x16x32_bf16 v[108:111], v[12:15], v[12:15], 0\n"
    "v_mfma_f32_16x16x32_bf16 v[32:35], v[12:15], v[80:83], 0\n"
    "v_mfma_f32_16x16x32_bf16 v[36:39], v[12:15], v[84:87], 0\n"
    "s_waitcnt lgkmcnt(0)\n"
    "s_nop 0\n"
    "v_mfma_f32_16x16x16_bf16 v[8:11], v[76:77], v[4:5], v[8:11]\n"
    "v_mfma_f32_16x16x16_bf16 v[0:3], v[78:79], v[4:5], v[0:3]\n"
    "v_mfma_f32_16x16x16_bf16 v[108:111], v[4:5], v[4:5], v[108:111]\n"
    "v_mfma_f32_16x16x16_bf16 v[32:35], v[4:5], v[88:89], v[32:35]\n"
    "v_mfma_f32_16x16x16_bf16 v[36:39], v[4:5], v[90:91], v[36:39]\n"
    "s_mov_b64 exec, s[56:57]\n"
    "s_nop 2\n"
    "ds_write_b128 v101, v[8:11]\n"
    "ds_write_b128 v101, v[0:3] offset:64\n"
    "s_mov_b64 exec, s[58:59]\n"
    "ds_write_b128 v101, v[108:111] offset:96\n"
    "s_mov_b64 exec, -1\n"
    "s_waitcnt lgkmcnt(0)\n"
    "s_barrier\n"
    "ds_read_b128 v[40:43], v102 offset:0\n"
    "ds_read_b128 v[44:47], v102 offset:1808\n"
    "ds_read_b128 v[48:51], v102 offset:3616\n"
    "ds_read_b128 v[52:55], v102 offset:5424\n"
    "s_mov_b64 exec, s[60:61]\n"
    "ds_write_b128 v103, v[36:39] offset:96\n"
    "ds_write_b128 v103, v[32:35] offset:192\n"
    "s_mov_b64 exec, -1\n"
    "s_waitcnt lgkmcnt(4)\n"
    "v_pk_add_f32 v[40:41], v[40:41], v[44:45]\n"
    "v_pk_add_f32 v[42:43], v[42:43], v[46:47]\n"
    "s_waitcnt lgkmcnt(2)\n"
    "v_pk_add_f32 v[48:49], v[48:49], v[52:53]\n"
    "v_pk_add_f32 v[50:51], v[50:51], v[54:55]\n"
    "v_pk_add_f32 v[40:41], v[40:41], v[48:49]\n"
    "v_pk_add_f32 v[42:43], v[42:43], v[50:51]\n"
    "v_max3_f32 v16, v40, v41, v42\n"
    "v_max_f32 v16, v16, v43\n"
    "s_nop 1\n"
    "v_max_f32_dpp v16, v16, v16 row_ror:8 row_mask:0xf bank_mask:0xf\n"
    "s_nop 1\n"
    "v_max_f32_dpp v16, v16, v16 row_ror:4 row_mask:0xf bank_mask:0xf\n"
    "s_nop 1\n"
    "v_max_f32_dpp v16, v16, v16 row_ror:2 row_mask:0xf bank_mask:0xf\n"
    "s_nop 1\n"
    "v_max_f32_dpp v16, v16, v16 row_ror:1 row_mask:0xf bank_mask:0xf\n"
    "v_mul_f32 v16, s54, v16\n"
    "v_pk_fma_f32 v[40:41], v[40:41], s[54:55], v[16:17] op_sel_hi:[1,1,0] neg_lo:[0,0,1] neg_hi:[0,0,1]\n"
    "v_pk_fma_f32 v[42:43], v[42:43], s[54:55], v[16:17] op_sel_hi:[1,1,0] neg_lo:[0,0,1] neg_hi:[0,0,1]\n"
    "v_exp_f32 v40, v40\n"
    "v_exp_f32 v41, v41\n"
    "v_exp_f32 v42, v42\n"
    "v_exp_f32 v43, v43\n"
    "s_nop 0\n"
    "v_pk_add_f32 v[16:17], v[40:41], v[42:43]\n"
    "v_add_f32 v16, v16, v17\n"
    "s_nop 1\n"
    "v_add_f32_dpp v16, v16, v16 row_ror:8 row_mask:0xf bank_mask:0xf\n"
    "s_nop 1\n"
    "v_add_f32_dpp v16, v16, v16 row_ror:4 row_mask:0xf bank_mask:0xf\n"
    "s_nop 1\n"
    "v_add_f32_dpp v16, v16, v16 row_ror:2 row_mask:0xf bank_mask:0xf\n"
    "s_nop 1\n"
    "v_add_f32_dpp v16, v16, v16 row_ror:1 row_mask:0xf bank_mask:0xf\n"
    "v_rcp_f32 v16, v16\n"
    "s_nop 0\n"
    "v_pk_mul_f32 v[40:41], v[40:41], v[16:17] op_sel_hi:[1,0]\n"
    "v_pk_mul_f32 v[42:43], v[42:43], v[16:17] op_sel_hi:[1,0]\n"
    "v_cvt_pk_bf16_f32 v40, v40, v41\n"
    "v_cvt_pk_bf16_f32 v41, v42, v43\n"
    "v_min_u32 v40, -2, v40\n"
    "buffer_store_dwordx2 v[40:41], v105, s[0:3], s40 offen sc1\n"
    ".Lqk_bulk_%=:\n"
    "s_waitcnt vmcnt(2) lgkmcnt(0)\n"
    "s_barrier\n"
    "ds_read_b128 v[32:35], v92 offset:0\n"
    "ds_read_b128 v[36:39], v92 offset:1024\n"
    "ds_read_b128 v[56:59], v104 offset:0\n"
    "ds_read_b128 v[60:63], v104 offset:4352\n"
    "ds_read_b128 v[64:67], v104 offset:8704\n"
    "ds_read_b128 v[68:71], v104 offset:13056\n"
    "ds_read_b128 v[40:43], v92 offset:2048\n"
    "ds_read_b128 v[44:47], v92 offset:3072\n"
    "ds_read_b128 v[48:51], v92 offset:4096\n"
    "ds_read_b128 v[52:55], v92 offset:5120\n"
    "s_waitcnt lgkmcnt(8)\n"
    "v_mfma_f32_16x16x32_bf16 v[0:3], a[80:83], v[32:35], 0\n"
    "s_waitcnt lgkmcnt(6)\n"
    "v_add_f32 v56, v56, v60\n"
    "v_mfma_f32_16x16x32_bf16 v[4:7], a[80:83], v[36:39], 0\n"
    "v_add_f32 v57, v57, v61\n"
    "v_add_f32 v58, v58, v62\n"
    "v_mfma_f32_16x16x32_bf16 v[8:11], a[160:163], v[32:35], 0\n"
    "v_add_f32 v59, v59, v63\n"
    "s_waitcnt lgkmcnt(4)\n"
    "v_mfma_f32_16x16x32_bf16 v[12:15], a[160:163], v[36:39], 0\n"
    "v_add_f32 v64, v64, v68\n"
    "v_add_f32 v65, v65, v69\n"
    "v_mfma_f32_16x16x32_bf16 v[16:19], a[0:3], v[32:35], 0\n"
    "v_add_f32 v66, v66, v70\n"
    "v_add_f32 v67, v67, v71\n"
    "v_mfma_f32_16x16x32_bf16 v[20:23], a[0:3], v[36:39], 0\n"
    "v_add_f32 v56, v56, v64\n"
    "v_add_f32 v57, v57, v65\n"
    "v_mfma_f32_16x16x32_bf16 v[24:27], a[240:243], v[32:35], 0\n"
    "ds_read_b128 v[32:35], v92 offset:6144\n"
    "v_add_f32 v58, v58, v66\n"
    "v_mfma_f32_16x16x32_bf16 v[28:31], a[240:243], v[36:39], 0\n"
    "ds_read_b128 v[36:39], v92 offset:7168\n"
    "s_waitcnt lgkmcnt(4)\n"
    "v_mfma_f32_16x16x32_bf16 v[0:3], a[84:87], v[40:43], v[0:3]\n"
    "v_add_f32 v59, v59, v67\n"
    "s_barrier\n"
    "v_mfma_f32_16x16x32_bf16 v[4:7], a[84:87], v[44:47], v[4:7]\n"
    "v_cndmask_b32 v56, v107, v56, s[12:13]\n"
    "v_cndmask_b32 v57, v107, v57, s[14:15]\n"
    "v_mfma_f32_16x16x32_bf16 v[8:11], a[164:167], v[40:43], v[8:11]\n"
    "v_cndmask_b32 v58, v107, v58, s[16:17]\n"
    "v_cndmask_b32 v59, v107, v59, s[18:19]\n"
    "v_mfma_f32_16x16x32_bf16 v[12:15], a[164:167], v[44:47], v[12:15]\n"
    "v_max3_f32 v72, v56, v57, v58\n"
    "v_max_f32 v72, v72, v59\n"
    "v_mfma_f32_16x16x32_bf16 v[16:19], a[4:7], v[40:43], v[16:19]\n"
    "v_cndmask_b32 v74, v107, v72, s[62:63]\n"
    "v_cndmask_b32 v75, v72, v107, s[62:63]\n"
    "v_mfma_f32_16x16x32_bf16 v[20:23], a[4:7], v[44:47], v[20:23]\n"
    "v_max_f32_dpp v74, v74, v74 row_ror:8 row_mask:0xf bank_mask:0xf\n"
    "v_max_f32_dpp v75, v75, v75 row_ror:8 row_mask:0xf bank_mask:0xf\n"
    "v_mfma_f32_16x16x32_bf16 v[24:27], a[244:247], v[40:43], v[24:27]\n"
    "ds_read_b128 v[40:43], v93 offset:0\n"
    "v_max_f32_dpp v74, v74, v74 row_ror:4 row_mask:0xf bank_mask:0xf\n"
    "v_mfma_f32_16x16x32_bf16 v[28:31], a[244:247], v[44:47], v[28:31]\n"
    "ds_read_b128 v[44:47], v93 offset:1024\n"
    "s_waitcnt lgkmcnt(4)\n"
    "v_mfma_f32_16x16x32_bf16 v[0:3], a[88:91], v[48:51], v[0:3]\n"
    "v_max_f32_dpp v75, v75, v75 row_ror:4 row_mask:0xf bank_mask:0xf\n"
    "v_max_f32_dpp v74, v74, v74 row_ror:2 row_mask:0xf bank_mask:0xf\n"
    "v_mfma_f32_16x16x32_bf16 v[4:7], a[88:91], v[52:55], v[4:7]\n"
    "v_max_f32_dpp v75, v75, v75 row_ror:2 row_mask:0xf bank_mask:0xf\n"
    "v_max_f32_dpp v74, v74, v74 row_ror:1 row_mask:0xf bank_mask:0xf\n"
    "v_mfma_f32_16x16x32_bf16 v[8:11], a[168:171], v[48:51], v[8:11]\n"
    "v_max_f32_dpp v75, v75, v75 row_ror:1 row_mask:0xf bank_mask:0xf\n"
    "v_cndmask_b32 v72, v75, v74, s[62:63]\n"
    "v_mfma_f32_16x16x32_bf16 v[12:15], a[168:171], v[52:55], v[12:15]\n"
    "v_mul_f32 v72, s54, v72\n"
    "v_fma_f32 v56, v56, s54, -v72\n"
    "v_mfma_f32_16x16x32_bf16 v[16:19], a[8:11], v[48:51], v[16:19]\n"
    "v_fma_f32 v57, v57, s54, -v72\n"
    "v_fma_f32 v58, v58, s54, -v72\n"
    "v_mfma_f32_16x16x32_bf16 v[20:23], a[8:11], v[52:55], v[20:23]\n"
    "v_fma_f32 v59, v59, s54, -v72\n"
    "v_mfma_f32_16x16x32_bf16 v[24:27], a[248:251], v[48:51], v[24:27]\n"
    "ds_read_b128 v[48:51], v93 offset:2048\n"
    "v_mfma_f32_16x16x32_bf16 v[28:31], a[248:251], v[52:55], v[28:31]\n"
    "ds_read_b128 v[52:55], v93 offset:3072\n"
    "s_waitcnt lgkmcnt(4)\n"
    "v_mfma_f32_16x16x32_bf16 v[0:3], a[92:95], v[32:35], v[0:3]\n"
    "v_exp_f32 v56, v56\n"
    "v_mfma_f32_16x16x32_bf16 v[4:7], a[92:95], v[36:39], v[4:7]\n"
    "v_exp_f32 v57, v57\n"
    "v_mfma_f32_16x16x32_bf16 v[8:11], a[172:175], v[32:35], v[8:11]\n"
    "v_exp_f32 v58, v58\n"
    "v_mfma_f32_16x16x32_bf16 v[12:15], a[172:175], v[36:39], v[12:15]\n"
    "v_exp_f32 v59, v59\n"
    "v_mfma_f32_16x16x32_bf16 v[16:19], a[12:15], v[32:35], v[16:19]\n"
    "v_add_f32 v72, v56, v58\n"
    "v_add_f32 v73, v57, v59\n"
    "v_mfma_f32_16x16x32_bf16 v[20:23], a[12:15], v[36:39], v[20:23]\n"
    "v_add_f32 v72, v72, v73\n"
    "v_cndmask_b32 v74, 0, v72, s[62:63]\n"
    "v_mfma_f32_16x16x32_bf16 v[24:27], a[252:255], v[32:35], v[24:27]\n"
    "ds_read_b128 v[32:35], v93 offset:4096\n"
    "v_cndmask_b32 v75, v72, 0, s[62:63]\n"
    "v_mfma_f32_16x16x32_bf16 v[28:31], a[252:255], v[36:39], v[28:31]\n"
    "ds_read_b128 v[36:39], v93 offset:5120\n"
    "s_waitcnt lgkmcnt(4)\n"
    "v_mfma_f32_16x16x32_bf16 v[0:3], a[96:99], v[40:43], v[0:3]\n"
    "v_add_f32_dpp v74, v74, v74 row_ror:8 row_mask:0xf bank_mask:0xf\n"
    "v_add_f32_dpp v75, v75, v75 row_ror:8 row_mask:0xf bank_mask:0xf\n"
    "v_mfma_f32_16x16x32_bf16 v[4:7], a[96:99], v[44:47], v[4:7]\n"
    "v_add_f32_dpp v74, v74, v74 row_ror:4 row_mask:0xf bank_mask:0xf\n"
    "v_add_f32_dpp v75, v75, v75 row_ror:4 row_mask:0xf bank_mask:0xf\n"
    "v_mfma_f32_16x16x32_bf16 v[8:11], a[176:179], v[40:43], v[8:11]\n"
    "v_add_f32_dpp v74, v74, v74 row_ror:2 row_mask:0xf bank_mask:0xf\n"
    "v_add_f32_dpp v75, v75, v75 row_ror:2 row_mask:0xf bank_mask:0xf\n"
    "v_mfma_f32_16x16x32_bf16 v[12:15], a[176:179], v[44:47], v[12:15]\n"
    "v_add_f32_dpp v74, v74, v74 row_ror:1 row_mask:0xf bank_mask:0xf\n"
    "v_add_f32_dpp v75, v75, v75 row_ror:1 row_mask:0xf bank_mask:0xf\n"
    "v_mfma_f32_16x16x32_bf16 v[16:19], a[16:19], v[40:43], v[16:19]\n"
    "v_cndmask_b32 v72, v75, v74, s[62:63]\n"
    "v_mfma_f32_16x16x32_bf16 v[20:23], a[16:19], v[44:47], v[20:23]\n"
    "v_rcp_f32 v72, v72\n"
    "v_mfma_f32_16x16x32_bf16 v[24:27], v[112:115], v[40:43], v[24:27]\n"
    "ds_read_b128 v[40:43], v93 offset:6144\n"
    "v_mul_f32 v56, v56, v72\n"
    "v_mfma_f32_16x16x32_bf16 v[28:31], v[112:115], v[44:47], v[28:31]\n"
    "ds_read_b128 v[44:47], v93 offset:7168\n"
    "s_waitcnt lgkmcnt(4)\n"
    "v_mfma_f32_16x16x32_bf16 v[0:3], a[100:103], v[48:51], v[0:3]\n"
    "v_mul_f32 v57, v57, v72\n"
    "v_mul_f32 v58, v58, v72\n"
    "v_mfma_f32_16x16x32_bf16 v[4:7], a[100:103], v[52:55], v[4:7]\n"
    "v_mul_f32 v59, v59, v72\n"
    "v_cvt_pk_bf16_f32 v56, v56, v57\n"
    "v_mfma_f32_16x16x32_bf16 v[8:11], a[180:183], v[48:51], v[8:11]\n"
    "v_cvt_pk_bf16_f32 v57, v58, v59\n"
    "v_min_u32 v56, -2, v56\n"
    "v_mfma_f32_16x16x32_bf16 v[12:15], a[180:183], v[52:55], v[12:15]\n"
    "buffer_store_dwordx2 v[56:57], v106, s[0:3], s42 offen sc1\n"
    "v_mfma_f32_16x16x32_bf16 v[16:19], a[20:23], v[48:51], v[16:19]\n"
    "s_cmp_eq_u32 s38, 63\n"
    "s_cbranch_scc1 .Lqk_end_%=\n"
    "v_mfma_f32_16x16x32_bf16 v[20:23], a[20:23], v[52:55], v[20:23]\n"
    "v_mov_b64 v[72:73], -1\n"
    "v_mfma_f32_16x16x32_bf16 v[24:27], v[116:119], v[48:51], v[24:27]\n"
    "ds_read_b128 v[48:51], v94 offset:0\n"
    "v_mfma_f32_16x16x32_bf16 v[28:31], v[116:119], v[52:55], v[28:31]\n"
    "ds_read_b128 v[52:55], v94 offset:1024\n"
    "s_waitcnt lgkmcnt(4)\n"
    "v_mfma_f32_16x16x32_bf16 v[0:3], a[104:107], v[32:35], v[0:3]\n"
    "v_mfma_f32_16x16x32_bf16 v[4:7], a[104:107], v[36:39], v[4:7]\n"
    "v_mfma_f32_16x16x32_bf16 v[8:11], a[184:187], v[32:35], v[8:11]\n"
    "v_mfma_f32_16x16x32_bf16 v[12:15], a[184:187], v[36:39], v[12:15]\n"
    "v_mfma_f32_16x16x32_bf16 v[16:19], a[24:27], v[32:35], v[16:19]\n"
    "buffer_store_dwordx2 v[72:73], v105, s[0:3], s41 offen sc1\n"
    "v_mfma_f32_16x16x32_bf16 v[20:23], a[24:27], v[36:39], v[20:23]\n"
    "v_mfma_f32_16x16x32_bf16 v[24:27], v[120:123], v[32:35], v[24:27]\n"
    "ds_read_b128 v[32:35], v94 offset:2048\n"
    "v_mfma_f32_16x16x32_bf16 v[28:31], v[120:123], v[36:39], v[28:31]\n"
    "ds_read_b128 v[36:39], v94 offset:3072\n"
    "s_waitcnt lgkmcnt(4)\n"
    "v_mfma_f32_16x16x32_bf16 v[0:3], a[108:111], v[40:43], v[0:3]\n"
    "v_mfma_f32_16x16x32_bf16 v[4:7], a[108:111], v[44:47], v[4:7]\n"
    "v_mfma_f32_16x16x32_bf16 v[8:11], a[188:191], v[40:43], v[8:11]\n"
    "v_mfma_f32_16x16x32_bf16 v[12:15], a[188:191], v[44:47], v[12:15]\n"
    "v_mfma_f32_16x16x32_bf16 v[16:19], a[28:31], v[40:43], v[16:19]\n"
    "v_mfma_f32_16x16x32_bf16 v[20:23], a[28:31], v[44:47], v[20:23]\n"
    "buffer_store_dwordx2 v[72:73], v106, s[0:3], s41 offen sc1\n"
    "v_mfma_f32_16x16x32_bf16 v[24:27], v[124:127], v[40:43], v[24:27]\n"
    "ds_read_b128 v[40:43], v94 offset:4096\n"
    "v_mfma_f32_16x16x32_bf16 v[28:31], v[124:127], v[44:47], v[28:31]\n"
    "ds_read_b128 v[44:47], v94 offset:5120\n"
    "s_waitcnt lgkmcnt(4)\n"
    "v_mfma_f32_16x16x32_bf16 v[0:3], a[112:115], v[48:51], v[0:3]\n"
    "v_mfma_f32_16x16x32_bf16 v[4:7], a[112:115], v[52:55], v[4:7]\n"
    "v_mfma_f32_16x16x32_bf16 v[8:11], a[192:195], v[48:51], v[8:11]\n"
    "v_mfma_f32_16x16x32_bf16 v[12:15], a[192:195], v[52:55], v[12:15]\n"
    "v_mfma_f32_16x16x32_bf16 v[16:19], a[32:35], v[48:51], v[16:19]\n"
    "v_mfma_f32_16x16x32_bf16 v[20:23], a[32:35], v[52:55], v[20:23]\n"
    "v_mfma_f32_16x16x32_bf16 v[24:27], v[128:131], v[48:51], v[24:27]\n"
    "ds_read_b128 v[48:51], v94 offset:6144\n"
    "buffer_load_dwordx4 v[80:83], v96, s[0:3], s46 offen sc1\n"
    "v_mfma_f32_16x16x32_bf16 v[28:31], v[128:131], v[52:55], v[28:31]\n"
    "ds_read_b128 v[52:55], v94 offset:7168\n"
    "s_waitcnt lgkmcnt(4)\n"
    "v_mfma_f32_16x16x32_bf16 v[0:3], a[116:119], v[32:35], v[0:3]\n"
    "v_mfma_f32_16x16x32_bf16 v[4:7], a[116:119], v[36:39], v[4:7]\n"
    "v_mfma_f32_16x16x32_bf16 v[8:11], a[196:199], v[32:35], v[8:11]\n"
    "buffer_load_dwordx4 v[84:87], v96, s[0:3], s46 offen offset:1024 sc1\n"
    "v_mfma_f32_16x16x32_bf16 v[12:15], a[196:199], v[36:39], v[12:15]\n"
    "v_mfma_f32_16x16x32_bf16 v[16:19], a[36:39], v[32:35], v[16:19]\n"
    "v_mfma_f32_16x16x32_bf16 v[20:23], a[36:39], v[36:39], v[20:23]\n"
    "v_mfma_f32_16x16x32_bf16 v[24:27], v[132:135], v[32:35], v[24:27]\n"
    "ds_read_b128 v[32:35], v95 offset:0\n"
    "buffer_load_dwordx2 v[108:109], off, s[0:3], s47 sc1\n"
    "v_mfma_f32_16x16x32_bf16 v[28:31], v[132:135], v[36:39], v[28:31]\n"
    "ds_read_b128 v[36:39], v95 offset:1024\n"
    "s_waitcnt lgkmcnt(4)\n"
    "v_mfma_f32_16x16x32_bf16 v[0:3], a[120:123], v[40:43], v[0:3]\n"
    "s_add_u32 s38, s38, 1\n"
    "s_add_u32 s52, s38, 1\n"
    "s_add_u32 s40, s38, 0\n"
    "v_mfma_f32_16x16x32_bf16 v[4:7], a[120:123], v[44:47], v[4:7]\n"
    "s_and_b32 s40, s40, 3\n"
    "s_lshl_b32 s40, s40, 22\n"
    "s_add_u32 s41, s38, 2\n"
    "v_mfma_f32_16x16x32_bf16 v[8:11], a[200:203], v[40:43], v[8:11]\n"
    "s_and_b32 s41, s41, 3\n"
    "s_lshl_b32 s41, s41, 22\n"
    "s_and_b32 s47, s38, 63\n"
    "v_mfma_f32_16x16x32_bf16 v[12:15], a[200:203], v[44:47], v[12:15]\n"
    "s_lshl_b32 s47, s47, 7\n"
    "s_mov_b32 s42, s40\n"
    "s_add_u32 s43, s22, s40\n"
    "v_mfma_f32_16x16x32_bf16 v[16:19], a[40:43], v[40:43], v[16:19]\n"
    "s_add_u32 s44, s23, s40\n"
    "s_add_u32 s48, s26, s47\n"
    "s_add_u32 s47, s27, s47\n"
    "v_mfma_f32_16x16x32_bf16 v[20:23], a[40:43], v[44:47], v[20:23]\n"
    "s_lshl_b32 s45, s52, 19\n"
    "s_lshl_b32 s37, s52, 11\n"
    "s_xor_b32 s37, s37, s21\n"
    "v_mfma_f32_16x16x32_bf16 v[24:27], v[136:139], v[40:43], v[24:27]\n"
    "ds_read_b128 v[40:43], v95 offset:2048\n"
    "s_and_b32 s37, s37, 0x2000\n"
    "v_mfma_f32_16x16x32_bf16 v[28:31], v[136:139], v[44:47], v[28:31]\n"
    "ds_read_b128 v[44:47], v95 offset:3072\n"
    "s_waitcnt lgkmcnt(4)\n"
    "v_mfma_f32_16x16x32_bf16 v[0:3], a[124:127], v[48:51], v[0:3]\n"
    "s_add_u32 s45, s45, s37\n"
    "s_add_u32 s45, s45, s24\n"
    "s_lshl_b32 s46, s52, 20\n"
    "v_mfma_f32_16x16x32_bf16 v[4:7], a[124:127], v[52:55], v[4:7]\n"
    "s_lshl_b32 s37, s52, 12\n"
    "s_xor_b32 s37, s37, s21\n"
    "s_and_b32 s37, s37, 0x2000\n"
    "v_mfma_f32_16x16x32_bf16 v[8:11], a[204:207], v[48:51], v[8:11]\n"
    "s_add_u32 s46, s46, s37\n"
    "s_add_u32 s46, s46, s25\n"
    "v_mfma_f32_16x16x32_bf16 v[12:15], a[204:207], v[52:55], v[12:15]\n"
    "v_mfma_f32_16x16x32_bf16 v[16:19], a[44:47], v[48:51], v[16:19]\n"
    "v_mfma_f32_16x16x32_bf16 v[20:23], a[44:47], v[52:55], v[20:23]\n"
    "v_mfma_f32_16x16x32_bf16 v[24:27], v[140:143], v[48:51], v[24:27]\n"
    "ds_read_b128 v[48:51], v95 offset:4096\n"
    "v_mfma_f32_16x16x32_bf16 v[28:31], v[140:143], v[52:55], v[28:31]\n"
    "ds_read_b128 v[52:55], v95 offset:5120\n"
    "s_waitcnt lgkmcnt(4)\n"
    "v_mfma_f32_16x16x32_bf16 v[0:3], a[128:131], v[32:35], v[0:3]\n"
    "v_mfma_f32_16x16x32_bf16 v[4:7], a[128:131], v[36:39], v[4:7]\n"
    "v_mfma_f32_16x16x32_bf16 v[8:11], a[208:211], v[32:35], v[8:11]\n"
    "v_mfma_f32_16x16x32_bf16 v[12:15], a[208:211], v[36:39], v[12:15]\n"
    "v_mfma_f32_16x16x32_bf16 v[16:19], a[48:51], v[32:35], v[16:19]\n"
    "v_mfma_f32_16x16x32_bf16 v[20:23], a[48:51], v[36:39], v[20:23]\n"
    "v_mfma_f32_16x16x32_bf16 v[24:27], v[144:147], v[32:35], v[24:27]\n"
    "ds_read_b128 v[32:35], v95 offset:6144\n"
    "v_mfma_f32_16x16x32_bf16 v[28:31], v[144:147], v[36:39], v[28:31]\n"
    "ds_read_b128 v[36:39], v95 offset:7168\n"
    "s_waitcnt lgkmcnt(4)\n"
    "v_mfma_f32_16x16x32_bf16 v[0:3], a[132:135], v[40:43], v[0:3]\n"
    "v_mfma_f32_16x16x32_bf16 v[4:7], a[132:135], v[44:47], v[4:7]\n"
    "v_mfma_f32_16x16x32_bf16 v[8:11], a[212:215], v[40:43], v[8:11]\n"
    "v_mfma_f32_16x16x32_bf16 v[12:15], a[212:215], v[44:47], v[12:15]\n"
    "v_mfma_f32_16x16x32_bf16 v[16:19], a[52:55], v[40:43], v[16:19]\n"
    "v_mfma_f32_16x16x32_bf16 v[20:23], a[52:55], v[44:47], v[20:23]\n"
    "v_mfma_f32_16x16x32_bf16 v[24:27], v[148:151], v[40:43], v[24:27]\n"
    "ds_read_b128 v[40:43], v96 offset:45056\n"
    "v_mfma_f32_16x16x32_bf16 v[28:31], v[148:151], v[44:47], v[28:31]\n"
    "ds_read_b128 v[44:47], v96 offset:46080\n"
    "s_waitcnt lgkmcnt(4)\n"
    "v_mfma_f32_16x16x32_bf16 v[0:3], a[136:139], v[48:51], v[0:3]\n"
    "v_mfma_f32_16x16x32_bf16 v[4:7], a[136:139], v[52:55], v[4:7]\n"
    "v_mfma_f32_16x16x32_bf16 v[8:11], a[216:219], v[48:51], v[8:11]\n"
    "v_mfma_f32_16x16x32_bf16 v[12:15], a[216:219], v[52:55], v[12:15]\n"
    "v_mfma_f32_16x16x32_bf16 v[16:19], a[56:59], v[48:51], v[16:19]\n"
    "v_mfma_f32_16x16x32_bf16 v[20:23], a[56:59], v[52:55], v[20:23]\n"
    "v_mfma_f32_16x16x32_bf16 v[24:27], v[152:155], v[48:51], v[24:27]\n"
    "ds_read_b128 v[48:51], v96 offset:47104\n"
    "v_mfma_f32_16x16x32_bf16 v[28:31], v[152:155], v[52:55], v[28:31]\n"
    "ds_read_b128 v[52:55], v96 offset:48128\n"
    "s_waitcnt lgkmcnt(4)\n"
    "v_mfma_f32_16x16x32_bf16 v[0:3], a[140:143], v[32:35], v[0:3]\n"
    "v_mfma_f32_16x16x32_bf16 v[4:7], a[140:143], v[36:39], v[4:7]\n"
    "v_mfma_f32_16x16x32_bf16 v[8:11], a[220:223], v[32:35], v[8:11]\n"
    "s_waitcnt vmcnt(0)\n"
    "v_readfirstlane_b32 s36, v108\n"
    "v_mfma_f32_16x16x32_bf16 v[12:15], a[220:223], v[36:39], v[12:15]\n"
    "v_readfirstlane_b32 s37, v109\n"
    "v_mfma_f32_16x16x32_bf16 v[16:19], a[60:63], v[32:35], v[16:19]\n"
    "s_cmp_eq_u32 s37, s39\n"
    "s_cselect_b32 s49, s36, s49\n"
    "s_add_u32 s49, s49, 304\n"
    "v_mfma_f32_16x16x32_bf16 v[20:23], a[60:63], v[36:39], v[20:23]\n"
    "s_sub_u32 s50, s49, 63\n"
    "s_mov_b32 s39, s52\n"
    "v_mfma_f32_16x16x32_bf16 v[24:27], v[156:159], v[32:35], v[24:27]\n"
    "ds_read_b128 v[32:35], v96 offset:49152\n"
    "v_mfma_f32_16x16x32_bf16 v[28:31], v[156:159], v[36:39], v[28:31]\n"
    "ds_read_b128 v[36:39], v96 offset:50176\n"
    "s_waitcnt lgkmcnt(4)\n"
    "v_mfma_f32_16x16x32_bf16 v[0:3], a[144:147], v[40:43], v[0:3]\n"
    "v_mfma_f32_16x16x32_bf16 v[4:7], a[144:147], v[44:47], v[4:7]\n"
    ".Lqk_rt10_%=:\n"
    "s_memrealtime s[34:35]\n"
    "s_waitcnt lgkmcnt(0)\n"
    "s_sub_u32 s36, s50, s34\n"
    "s_cmp_lt_u32 s36, 600\n"
    "s_cbranch_scc1 .Lqk_rt10_%=\n"
    "buffer_load_dwordx4 v[56:59], v96, s[0:3], s43 offen sc1\n"
    "buffer_load_dwordx4 v[60:63], v96, s[0:3], s43 offen offset:1024 sc1\n"
    "buffer_load_dwordx4 v[64:67], v96, s[0:3], s44 offen sc1\n"
    "v_mfma_f32_16x16x32_bf16 v[8:11], a[224:227], v[40:43], v[8:11]\n"
    "v_mfma_f32_16x16x32_bf16 v[12:15], a[224:227], v[44:47], v[12:15]\n"
    "v_mfma_f32_16x16x32_bf16 v[16:19], a[64:67], v[40:43], v[16:19]\n"
    "v_mfma_f32_16x16x32_bf16 v[20:23], a[64:67], v[44:47], v[20:23]\n"
    "v_mfma_f32_16x16x32_bf16 v[24:27], v[160:163], v[40:43], v[24:27]\n"
    "ds_read_b128 v[40:43], v96 offset:51200\n"
    "v_mfma_f32_16x16x32_bf16 v[28:31], v[160:163], v[44:47], v[28:31]\n"
    "ds_read_b128 v[44:47], v96 offset:52224\n"
    "s_waitcnt lgkmcnt(4)\n"
    "v_mfma_f32_16x16x32_bf16 v[0:3], a[148:151], v[48:51], v[0:3]\n"
    "v_mfma_f32_16x16x32_bf16 v[4:7], a[148:151], v[52:55], v[4:7]\n"
    "v_mfma_f32_16x16x32_bf16 v[8:11], a[228:231], v[48:51], v[8:11]\n"
    "v_mfma_f32_16x16x32_bf16 v[12:15], a[228:231], v[52:55], v[12:15]\n"
    "v_mfma_f32_16x16x32_bf16 v[16:19], a[68:71], v[48:51], v[16:19]\n"
    "v_mfma_f32_16x16x32_bf16 v[20:23], a[68:71], v[52:55], v[20:23]\n"
    "v_mfma_f32_16x16x32_bf16 v[24:27], v[164:167], v[48:51], v[24:27]\n"
    "v_mfma_f32_16x16x32_bf16 v[28:31], v[164:167], v[52:55], v[28:31]\n"
    "s_waitcnt lgkmcnt(2)\n"
    "v_mfma_f32_16x16x32_bf16 v[0:3], a[152:155], v[32:35], v[0:3]\n"
    "v_mfma_f32_16x16x32_bf16 v[4:7], a[152:155], v[36:39], v[4:7]\n"
    "v_mfma_f32_16x16x32_bf16 v[8:11], a[232:235], v[32:35], v[8:11]\n"
    "v_mfma_f32_16x16x32_bf16 v[12:15], a[232:235], v[36:39], v[12:15]\n"
    "v_mfma_f32_16x16x32_bf16 v[16:19], a[72:75], v[32:35], v[16:19]\n"
    "v_mfma_f32_16x16x32_bf16 v[20:23], a[72:75], v[36:39], v[20:23]\n"
    "v_mfma_f32_16x16x32_bf16 v[24:27], v[168:171], v[32:35], v[24:27]\n"
    "v_mfma_f32_16x16x32_bf16 v[28:31], v[168:171], v[36:39], v[28:31]\n"
    "s_waitcnt lgkmcnt(0)\n"
    "v_mfma_f32_16x16x32_bf16 v[0:3], a[156:159], v[40:43], v[0:3]\n"
    "v_mfma_f32_16x16x32_bf16 v[4:7], a[156:159], v[44:47], v[4:7]\n"
    "v_mfma_f32_16x16x32_bf16 v[8:11], a[236:239], v[40:43], v[8:11]\n"
    "v_mfma_f32_16x16x32_bf16 v[12:15], a[236:239], v[44:47], v[12:15]\n"
    "v_mfma_f32_16x16x32_bf16 v[16:19], a[76:79], v[40:43], v[16:19]\n"
    "v_mfma_f32_16x16x32_bf16 v[20:23], a[76:79], v[44:47], v[20:23]\n"
    "v_mfma_f32_16x16x32_bf16 v[24:27], v[172:175], v[40:43], v[24:27]\n"
    "v_mfma_f32_16x16x32_bf16 v[28:31], v[172:175], v[44:47], v[28:31]\n"
    "ds_read_b128 v[32:35], v97 offset:0\n"
    "ds_read_b128 v[36:39], v97 offset:1024\n"
    "ds_read_b128 v[40:43], v97 offset:2048\n"
    "ds_read_b128 v[44:47], v97 offset:3072\n"
    "ds_read_b128 v[48:51], v97 offset:4096\n"
    "ds_read_b128 v[52:55], v97 offset:5120\n"
    "v_cvt_pk_bf16_f32 v68, v0, v1\n"
    "v_cvt_pk_bf16_f32 v69, v2, v3\n"
    "v_cvt_pk_bf16_f32 v70, v8, v9\n"
    "v_cvt_pk_bf16_f32 v71, v10, v11\n"
    "v_cvt_pk_bf16_f32 v72, v4, v5\n"
    "v_cvt_pk_bf16_f32 v73, v6, v7\n"
    "v_cvt_pk_bf16_f32 v74, v12, v13\n"
    "v_cvt_pk_bf16_f32 v75, v14, v15\n"
    "v_mfma_f32_16x16x32_bf16 v[108:111], v[68:71], v[84:87], 0\n"
    "v_cvt_pk_bf16_f32 v88, v24, v25\n"
    "v_cvt_pk_bf16_f32 v89, v26, v27\n"
    "v_cvt_pk_bf16_f32 v90, v28, v29\n"
    "v_cvt_pk_bf16_f32 v91, v30, v31\n"
    "v_mfma_f32_16x16x32_bf16 v[24:27], v[72:75], v[84:87], 0\n"
    "v_cvt_pk_bf16_f32 v76, v16, v17\n"
    "v_cvt_pk_bf16_f32 v77, v18, v19\n"
    "v_cvt_pk_bf16_f32 v78, v20, v21\n"
    "v_cvt_pk_bf16_f32 v79, v22, v23\n"
    "v_mfma_f32_16x16x32_bf16 v[28:31], v[68:71], v[80:83], 0\n"
    "v_mov_b64 v[0:1], 0\n"
    "v_mov_b64 v[2:3], 0\n"
    "v_mfma_f32_16x16x16_bf16 v[108:111], v[76:77], v[90:91], v[108:111]\n"
    "v_mov_b64 v[4:5], 0\n"
    "v_mov_b64 v[6:7], 0\n"
    "v_mfma_f32_16x16x16_bf16 v[24:27], v[78:79], v[90:91], v[24:27]\n"
    "v_mov_b64 v[8:9], 0\n"
    "v_mov_b64 v[10:11], 0\n"
    "v_mfma_f32_16x16x16_bf16 v[28:31], v[76:77], v[88:89], v[28:31]\n"
    "v_mov_b64 v[12:13], 0\n"
    "v_mov_b64 v[14:15], 0\n"
    "v_mov_b64 v[16:17], 0\n"
    "v_mov_b64 v[18:19], 0\n"
    "v_mov_b64 v[20:21], 0\n"
    "v_mov_b64 v[22:23], 0\n"
    "ds_write_b128 v103, v[108:111]\n"
    "ds_write_b128 v103, v[24:27] offset:64\n"
    "ds_write_b128 v103, v[28:31] offset:160\n"
    "s_branch .Lqk_take_%=\n"
    ".Lqk_retry_%=:\n"
    "s_add_u32 s32, s32, 1\n"
    "s_cmp_lt_u32 s32, 0x40000\n"
    "s_cbranch_scc1 .Lqk_sp11_%=\n"
    "v_mov_b32 v108, 1\n"
    "s_mov_b32 s36, 33728\n"
    "buffer_store_dword v108, off, s[0:3], s36 sc1\n"
    "s_waitcnt vmcnt(0)\n"
    "s_branch .Lqk_go_%=\n"
    ".Lqk_sp11_%=:\n"
    "s_nop 7\n"
    "v_mov_b64 v[0:1], 0\n"
    "v_mov_b64 v[2:3], 0\n"
    "v_mov_b64 v[4:5], 0\n"
    "v_mov_b64 v[6:7], 0\n"
    "v_mov_b64 v[8:9], 0\n"
    "v_mov_b64 v[10:11], 0\n"
    "v_mov_b64 v[12:13], 0\n"
    "v_mov_b64 v[14:15], 0\n"
    "v_mov_b64 v[16:17], 0\n"
    "v_mov_b64 v[18:19], 0\n"
    "v_mov_b64 v[20:21], 0\n"
    "v_mov_b64 v[22:23], 0\n"
    "buffer_load_dwordx4 v[56:59], v96, s[0:3], s43 offen sc1\n"
    "buffer_load_dwordx4 v[60:63], v96, s[0:3], s43 offen offset:1024 sc1\n"
    "buffer_load_dwordx4 v[64:67], v96, s[0:3], s44 offen sc1\n"
    "s_waitcnt vmcnt(0)\n"
    "s_branch .Lqk_take_%=\n"
    ".Lqk_end_%=:\n"
    "s_waitcnt vmcnt(0) lgkmcnt(0)\n"
    "s_endpgm\n"
    :: "{s[0:3]}"(s0_), "{s[4:7]}"(s4_), "{s[8:11]}"(s8_), "{s[12:15]}"(s12_), "{s[16:19]}"(s16_), "{s[20:23]}"(s20_), "{s[24:27]}"(s24_), "{s[28:31]}"(s28_), "{v[0:3]}"(v0_), "{v[4:7]}"(v4_), "{v[92:95]}"(v92_), "{v[96:99]}"(v96_), "{v[100:103]}"(v100_), "{v[104:107]}"(v104_) : "memory", "v255", "a255", "s95");
    __builtin_unreachable();
}
DEV void vo_role(const MegaArgs& A, const int L, const int h, const int rep) {
    ROLE_HEAD;
    const unsigned um = xl(RG_UM + ((8 * w + lq) * 4 + (h >> 3)) * 2048 + (h & 7) * 256 + l16 * 16, L);
    const unsigned umx = xl(RG_UMX + ((4 * (2 * w + (lq & 1)) + (l16 >> 2)) * 4 + (h >> 3)) * 512 + (h & 7) * 64 + (l & 3) * 16 + (lq >> 1) * 8, L);
    bst16(rr, um, ones); bst16(rr, um + 65536, ones); bst8(rr, umx, ones2);
    DRAIN_BARRIER();
    bst4(rr, threadIdx.x < 4 ? xl(Z_QV + l * 256 + (32 + h) * 4, L) : OOB, 1u);
    const u32x4 s0_ = {RFL(rx[0]), RFL(rx[1]), RFL(rx[2]), RFL(rx[3])};
    const u32x4 s4_ = {RFL(lo(A.Wo + h * 192 * 640)), RFL(hi(A.Wo + h * 192 * 640)), RFL(192u * 1280u), RFL(0x20000u)};
    const u32x4 s8_ = {RFL(lo(A.Wv + h * 192 + 48 * w)), RFL(hi(A.Wv + h * 192 + 48 * w)), RFL(WBYTES - (h * 192 + 48 * w) * 2), RFL(0x20000u)};
    const u32x4 s12_ = {RFL(w), RFL(L << 13), RFL(L), RFL(xl(RG_MEM + rep * 16384, L) + w * 2048)};
    const u32x4 s16_ = {RFL(w < 2 ? xl(RG_MEM + rep * 16384 + 8192, L) + w * 1024 : OOB), RFL(REC0 + w * 2048), RFL(QT0 + h * 32768 + 16384), RFL(xl(RG_UT + (32 * w + (h >> 3)) * 8192 + (h & 7) * 1024, L))};
    const u32x4 s20_ = {RFL(xl(Z_X + 8192, L)), RFL(xl(Z_CT + (h & 7) * 256, L)), RFL(xl(Z_XN + rep * 2048, 1)), RFL(xl(Z_QW + h * 128, L))};
    const u32x4 s24_ = {RFL(DBG0 + ((L * 128 + 32 + h) * 4 + w) * 2048), RFL(0u), RFL(0u), RFL(0u)};
    const u32x4 v0_ = {(unsigned)(((0 * 64 + l) / 6) * 12288 + ((0 * 64 + l) % 6) * 16 - 0), (unsigned)(((1 * 64 + l) / 6) * 12288 + ((1 * 64 + l) % 6) * 16 - 1024), (unsigned)(((2 * 64 + l) / 6) * 12288 + ((2 * 64 + l) % 6) * 16 - 2048), (unsigned)((8 * lq + ((l >> 2) & 1) + ((l >> 1) & 4)) * 96 + (l & 3) * 8)};
    const u32x4 v4_ = {(unsigned)((4 * 0 + lq) * 1280 + 256 * w + ((l16 ^ (2 * ((4 * 0 + lq) & 7))) * 16) - 0), (unsigned)((4 * 1 + lq) * 1280 + 256 * w + ((l16 ^ (2 * ((4 * 1 + lq) & 7))) * 16) - 1024), (unsigned)((4 * 2 + lq) * 1280 + 256 * w + ((l16 ^ (2 * ((4 * 2 + lq) & 7))) * 16) - 2048), (unsigned)((4 * 3 + lq) * 1280 + 256 * w + ((l16 ^ (2 * ((4 * 3 + lq) & 7))) * 16) - 3072)};
    const u32x4 v8_ = {(unsigned)((l >> 2) * 1280 + 1024 + 64 * w + (l & 3) * 16), (unsigned)((l >> 2) * 256 + ((l >> 2) & 7) * 32 + (l & 3) * 8), (unsigned)(8192 + (l >> 2) * 64 + (l & 3) * 8), (unsigned)(0u)};
    const u32x4 v92_ = {(unsigned)(l * 16 + ((w + 0) & 3) * 13312), (unsigned)(l * 16 + ((w + 1) & 3) * 13312), (unsigned)(l * 16 + ((w + 2) & 3) * 13312), (unsigned)(l * 16 + ((w + 3) & 3) * 13312)};
    const u32x4 v96_ = {(unsigned)(l * 16), (unsigned)(l * 16 + 64512 + w * 20480), (unsigned)((l & 2) ? 0xeeeeu : 0x4444u), (unsigned)(61440 + 512 * w + (l16 >> 2) * 64 + (l & 1) * 32 + sw2(lq) * 8 + (l & 2) * 2)};
    const u32x4 v100_ = {(unsigned)(61440 + 2048 + 256 * w + (l16 >> 2) * 64 + (l & 1) * 32 + sw2(lq) * 8 + (l & 2) * 2), (unsigned)(61440 + 512 * (lq >> 1) + 128 * (lq & 1) + (l & 4) * 8 + (l & 8) * 32 + (l & 3) * 8), (unsigned)(um), (unsigned)(umx)};
    const u32x4 v104_ = {(unsigned)(xl(RG_UTX + ((8 * w + (l16 >> 2)) * 4 + (h >> 3)) * 2048 + (h & 1) * 1024 + ((h & 7) >> 1) * 256 + lq * 64 + (l & 3) * 16, L)), (unsigned)(l < 20 ? (l >> 2) * 128 + (l & 3) * 4 : 0), (unsigned)((l & 3) * 4), (unsigned)(0u)};
    asm volatile(
    "s_mov_b32 s28, 0\n"
    "s_mul_i32 s41, s12, 16128\n"
    "s_mul_i32 s42, s12, 4096\n"
    "s_add_u32 s42, s42, 146432\n"
    "s_mul_i32 s43, s12, 20480\n"
    "s_add_u32 s43, s43, 64512\n"
    "v_add_u32 v16, s41, v3\n"
    "v_add_u32 v17, s42, v3\n"
    "v_add_u32 v18, s43, v3\n"
    "v_add_u32 v24, s41, v9\n"
    "v_add_u32 v32, s41, v10\n"
    "v_xor_b32 v25, 32, v24\n"
    "v_xor_b32 v26, 64, v24\n"
    "v_xor_b32 v27, 96, v24\n"
    "v_xor_b32 v28, 128, v24\n"
    "v_xor_b32 v29, 160, v24\n"
    "v_xor_b32 v30, 192, v24\n"
    "v_xor_b32 v31, 224, v24\n"
    "v_add_u32 v36, s43, v9\n"
    "v_add_u32 v44, s43, v10\n"
    "v_xor_b32 v37, 32, v36\n"
    "v_xor_b32 v38, 64, v36\n"
    "v_xor_b32 v39, 96, v36\n"
    "v_xor_b32 v40, 128, v36\n"
    "v_xor_b32 v41, 160, v36\n"
    "v_xor_b32 v42, 192, v36\n"
    "v_xor_b32 v43, 224, v36\n"
    "s_mov_b32 s49, 0\n"
    "s_add_u32 m0, s43, 0\n"
    "s_nop 0\n"
    "buffer_load_dwordx4 v4, s[4:7], s49 offen offset:0 lds\n"
    "buffer_load_dwordx4 v5, s[4:7], s49 offen offset:1024 lds\n"
    "buffer_load_dwordx4 v6, s[4:7], s49 offen offset:2048 lds\n"
    "buffer_load_dwordx4 v7, s[4:7], s49 offen offset:3072 lds\n"
    "s_add_u32 m0, s43, 8192\n"
    "s_nop 0\n"
    "buffer_load_dwordx4 v8, s[4:7], s49 offen lds\n"
    "s_mov_b32 s49, 20480\n"
    "s_add_u32 m0, s43, 4096\n"
    "s_nop 0\n"
    "buffer_load_dwordx4 v4, s[4:7], s49 offen offset:0 lds\n"
    "buffer_load_dwordx4 v5, s[4:7], s49 offen offset:1024 lds\n"
    "buffer_load_dwordx4 v6, s[4:7], s49 offen offset:2048 lds\n"
    "buffer_load_dwordx4 v7, s[4:7], s49 offen offset:3072 lds\n"
    "s_add_u32 m0, s43, 9216\n"
    "s_nop 0\n"
    "buffer_load_dwordx4 v8, s[4:7], s49 offen lds\n"
    "s_mov_b32 s49, 61440\n"
    "s_add_u32 m0, s43, 10240\n"
    "s_nop 0\n"
    "buffer_load_dwordx4 v4, s[4:7], s49 offen offset:0 lds\n"
    "buffer_load_dwordx4 v5, s[4:7], s49 offen offset:1024 lds\n"
    "buffer_load_dwordx4 v6, s[4:7], s49 offen offset:2048 lds\n"
    "buffer_load_dwordx4 v7, s[4:7], s49 offen offset:3072 lds\n"
    "s_add_u32 m0, s43, 18432\n"
    "s_nop 0\n"
    "buffer_load_dwordx4 v8, s[4:7], s49 offen lds\n"
    "s_mov_b32 s49, 81920\n"
    "s_add_u32 m0, s43, 14336\n"
    "s_nop 0\n"
    "buffer_load_dwordx4 v4, s[4:7], s49 offen offset:0 lds\n"
    "buffer_load_dwordx4 v5, s[4:7], s49 offen offset:1024 lds\n"
    "buffer_load_dwordx4 v6, s[4:7], s49 offen offset:2048 lds\n"
    "buffer_load_dwordx4 v7, s[4:7], s49 offen offset:3072 lds\n"
    "s_add_u32 m0, s43, 19456\n"
    "s_nop 0\n"
    "buffer_load_dwordx4 v8, s[4:7], s49 offen lds\n"
    "s_mov_b32 s49, 122880\n"
    "s_add_u32 m0, s41, 0\n"
    "s_nop 0\n"
    "buffer_load_dwordx4 v4, s[4:7], s49 offen offset:0 lds\n"
    "buffer_load_dwordx4 v5, s[4:7], s49 offen offset:1024 lds\n"
    "buffer_load_dwordx4 v6, s[4:7], s49 offen offset:2048 lds\n"
    "buffer_load_dwordx4 v7, s[4:7], s49 offen offset:3072 lds\n"
    "s_add_u32 m0, s41, 8192\n"
    "s_nop 0\n"
    "buffer_load_dwordx4 v8, s[4:7], s49 offen lds\n"
    "s_mov_b32 s49, 143360\n"
    "s_add_u32 m0, s41, 4096\n"
    "s_nop 0\n"
    "buffer_load_dwordx4 v4, s[4:7], s49 offen offset:0 lds\n"
    "buffer_load_dwordx4 v5, s[4:7], s49 offen offset:1024 lds\n"
    "buffer_load_dwordx4 v6, s[4:7], s49 offen offset:2048 lds\n"
    "buffer_load_dwordx4 v7, s[4:7], s49 offen offset:3072 lds\n"
    "s_add_u32 m0, s41, 9216\n"
    "s_nop 0\n"
    "buffer_load_dwordx4 v8, s[4:7], s49 offen lds\n"
    "s_waitcnt vmcnt(20)\n"
    "ds_read_b64_tr_b16 a[0:1], v36 offset:0\n"
    "ds_read_b64_tr_b16 a[2:3], v36 offset:4096\n"
    "ds_read_b64_tr_b16 a[24:25], v37 offset:0\n"
    "ds_read_b64_tr_b16 a[26:27], v37 offset:4096\n"
    "ds_read_b64_tr_b16 a[48:49], v38 offset:0\n"
    "ds_read_b64_tr_b16 a[50:51], v38 offset:4096\n"
    "ds_read_b64_tr_b16 a[72:73], v39 offset:0\n"
    "ds_read_b64_tr_b16 a[74:75], v39 offset:4096\n"
    "ds_read_b64_tr_b16 a[96:97], v40 offset:0\n"
    "ds_read_b64_tr_b16 a[98:99], v40 offset:4096\n"
    "ds_read_b64_tr_b16 a[120:121], v41 offset:0\n"
    "ds_read_b64_tr_b16 a[122:123], v41 offset:4096\n"
    "ds_read_b64_tr_b16 a[144:145], v42 offset:0\n"
    "ds_read_b64_tr_b16 a[146:147], v42 offset:4096\n"
    "ds_read_b64_tr_b16 a[168:169], v43 offset:0\n"
    "ds_read_b64_tr_b16 a[170:171], v43 offset:4096\n"
    "ds_read_b64_tr_b16 a[192:193], v44 offset:0\n"
    "ds_read_b64_tr_b16 a[194:195], v44 offset:1024\n"
    "ds_read_b64_tr_b16 a[216:217], v44 offset:32\n"
    "ds_read_b64_tr_b16 a[218:219], v44 offset:1056\n"
    "s_waitcnt lgkmcnt(0)\n"
    "s_mov_b32 s49, 184320\n"
    "s_add_u32 m0, s43, 0\n"
    "s_nop 0\n"
    "buffer_load_dwordx4 v4, s[4:7], s49 offen offset:0 lds\n"
    "buffer_load_dwordx4 v5, s[4:7], s49 offen offset:1024 lds\n"
    "buffer_load_dwordx4 v6, s[4:7], s49 offen offset:2048 lds\n"
    "buffer_load_dwordx4 v7, s[4:7], s49 offen offset:3072 lds\n"
    "s_add_u32 m0, s43, 8192\n"
    "s_nop 0\n"
    "buffer_load_dwordx4 v8, s[4:7], s49 offen lds\n"
    "s_mov_b32 s49, 204800\n"
    "s_add_u32 m0, s43, 4096\n"
    "s_nop 0\n"
    "buffer_load_dwordx4 v4, s[4:7], s49 offen offset:0 lds\n"
    "buffer_load_dwordx4 v5, s[4:7], s49 offen offset:1024 lds\n"
    "buffer_load_dwordx4 v6, s[4:7], s49 offen offset:2048 lds\n"
    "buffer_load_dwordx4 v7, s[4:7], s49 offen offset:3072 lds\n"
    "s_add_u32 m0, s43, 9216\n"
    "s_nop 0\n"
    "buffer_load_dwordx4 v8, s[4:7], s49 offen lds\n"
    "s_waitcnt vmcnt(20)\n"
    "ds_read_b64_tr_b16 a[4:5], v36 offset:10240\n"
    "ds_read_b64_tr_b16 a[6:7], v36 offset:14336\n"
    "ds_read_b64_tr_b16 a[28:29], v37 offset:10240\n"
    "ds_read_b64_tr_b16 a[30:31], v37 offset:14336\n"
    "ds_read_b64_tr_b16 a[52:53], v38 offset:10240\n"
    "ds_read_b64_tr_b16 a[54:55], v38 offset:14336\n"
    "ds_read_b64_tr_b16 a[76:77], v39 offset:10240\n"
    "ds_read_b64_tr_b16 a[78:79], v39 offset:14336\n"
    "ds_read_b64_tr_b16 a[100:101], v40 offset:10240\n"
    "ds_read_b64_tr_b16 a[102:103], v40 offset:14336\n"
    "ds_read_b64_tr_b16 a[124:125], v41 offset:10240\n"
    "ds_read_b64_tr_b16 a[126:127], v41 offset:14336\n"
    "ds_read_b64_tr_b16 a[148:149], v42 offset:10240\n"
    "ds_read_b64_tr_b16 a[150:151], v42 offset:14336\n"
    "ds_read_b64_tr_b16 a[172:173], v43 offset:10240\n"
    "ds_read_b64_tr_b16 a[174:175], v43 offset:14336\n"
    "ds_read_b64_tr_b16 a[196:197], v44 offset:10240\n"
    "ds_read_b64_tr_b16 a[198:199], v44 offset:11264\n"
    "ds_read_b64_tr_b16 a[220:221], v44 offset:10272\n"
    "ds_read_b64_tr_b16 a[222:223], v44 offset:11296\n"
    "s_waitcnt lgkmcnt(0)\n"
    "s_mov_b32 s49, 40960\n"
    "s_add_u32 m0, s43, 10240\n"
    "s_nop 0\n"
    "buffer_load_dwordx4 v4, s[4:7], s49 offen offset:0 lds\n"
    "buffer_load_dwordx4 v5, s[4:7], s49 offen offset:1024 lds\n"
    "buffer_load_dwordx4 v6, s[4:7], s49 offen offset:2048 lds\n"
    "buffer_load_dwordx4 v7, s[4:7], s49 offen offset:3072 lds\n"
    "s_add_u32 m0, s43, 18432\n"
    "s_nop 0\n"
    "buffer_load_dwordx4 v8, s[4:7], s49 offen lds\n"
    "s_mov_b32 s49, 102400\n"
    "s_add_u32 m0, s43, 14336\n"
    "s_nop 0\n"
    "buffer_load_dwordx4 v4, s[4:7], s49 offen offset:0 lds\n"
    "buffer_load_dwordx4 v5, s[4:7], s49 offen offset:1024 lds\n"
    "buffer_load_dwordx4 v6, s[4:7], s49 offen offset:2048 lds\n"
    "buffer_load_dwordx4 v7, s[4:7], s49 offen offset:3072 lds\n"
    "s_add_u32 m0, s43, 19456\n"
    "s_nop 0\n"
    "buffer_load_dwordx4 v8, s[4:7], s49 offen lds\n"
    "s_waitcnt vmcnt(20)\n"
    "ds_read_b64_tr_b16 a[8:9], v24 offset:0\n"
    "ds_read_b64_tr_b16 a[10:11], v24 offset:4096\n"
    "ds_read_b64_tr_b16 a[32:33], v25 offset:0\n"
    "ds_read_b64_tr_b16 a[34:35], v25 offset:4096\n"
    "ds_read_b64_tr_b16 a[56:57], v26 offset:0\n"
    "ds_read_b64_tr_b16 a[58:59], v26 offset:4096\n"
    "ds_read_b64_tr_b16 a[80:81], v27 offset:0\n"
    "ds_read_b64_tr_b16 a[82:83], v27 offset:4096\n"
    "ds_read_b64_tr_b16 a[104:105], v28 offset:0\n"
    "ds_read_b64_tr_b16 a[106:107], v28 offset:4096\n"
    "ds_read_b64_tr_b16 a[128:129], v29 offset:0\n"
    "ds_read_b64_tr_b16 a[130:131], v29 offset:4096\n"
    "ds_read_b64_tr_b16 a[152:153], v30 offset:0\n"
    "ds_read_b64_tr_b16 a[154:155], v30 offset:4096\n"
    "ds_read_b64_tr_b16 a[176:177], v31 offset:0\n"
    "ds_read_b64_tr_b16 a[178:179], v31 offset:4096\n"
    "ds_read_b64_tr_b16 a[200:201], v32 offset:0\n"
    "ds_read_b64_tr_b16 a[202:203], v32 offset:1024\n"
    "ds_read_b64_tr_b16 a[224:225], v32 offset:32\n"
    "ds_read_b64_tr_b16 a[226:227], v32 offset:1056\n"
    "s_waitcnt lgkmcnt(0)\n"
    "s_mov_b32 s49, 163840\n"
    "s_add_u32 m0, s41, 0\n"
    "s_nop 0\n"
    "buffer_load_dwordx4 v4, s[4:7], s49 offen offset:0 lds\n"
    "buffer_load_dwordx4 v5, s[4:7], s49 offen offset:1024 lds\n"
    "buffer_load_dwordx4 v6, s[4:7], s49 offen offset:2048 lds\n"
    "buffer_load_dwordx4 v7, s[4:7], s49 offen offset:3072 lds\n"
    "s_add_u32 m0, s41, 8192\n"
    "s_nop 0\n"
    "buffer_load_dwordx4 v8, s[4:7], s49 offen lds\n"
    "s_mov_b32 s49, 225280\n"
    "s_add_u32 m0, s41, 4096\n"
    "s_nop 0\n"
    "buffer_load_dwordx4 v4, s[4:7], s49 offen offset:0 lds\n"
    "buffer_load_dwordx4 v5, s[4:7], s49 offen offset:1024 lds\n"
    "buffer_load_dwordx4 v6, s[4:7], s49 offen offset:2048 lds\n"
    "buffer_load_dwordx4 v7, s[4:7], s49 offen offset:3072 lds\n"
    "s_add_u32 m0, s41, 9216\n"
    "s_nop 0\n"
    "buffer_load_dwordx4 v8, s[4:7], s49 offen lds\n"
    "s_waitcnt vmcnt(20)\n"
    "ds_read_b64_tr_b16 a[12:13], v36 offset:0\n"
    "ds_read_b64_tr_b16 a[14:15], v36 offset:4096\n"
    "ds_read_b64_tr_b16 a[36:37], v37 offset:0\n"
    "ds_read_b64_tr_b16 a[38:39], v37 offset:4096\n"
    "ds_read_b64_tr_b16 a[60:61], v38 offset:0\n"
    "ds_read_b64_tr_b16 a[62:63], v38 offset:4096\n"
    "ds_read_b64_tr_b16 a[84:85], v39 offset:0\n"
    "ds_read_b64_tr_b16 a[86:87], v39 offset:4096\n"
    "ds_read_b64_tr_b16 a[108:109], v40 offset:0\n"
    "ds_read_b64_tr_b16 a[110:111], v40 offset:4096\n"
    "ds_read_b64_tr_b16 a[132:133], v41 offset:0\n"
    "ds_read_b64_tr_b16 a[134:135], v41 offset:4096\n"
    "ds_read_b64_tr_b16 a[156:157], v42 offset:0\n"
    "ds_read_b64_tr_b16 a[158:159], v42 offset:4096\n"
    "ds_read_b64_tr_b16 a[180:181], v43 offset:0\n"
    "ds_read_b64_tr_b16 a[182:183], v43 offset:4096\n"
    "ds_read_b64_tr_b16 a[204:205], v44 offset:0\n"
    "ds_read_b64_tr_b16 a[206:207], v44 offset:1024\n"
    "ds_read_b64_tr_b16 a[228:229], v44 offset:32\n"
    "ds_read_b64_tr_b16 a[230:231], v44 offset:1056\n"
    "s_waitcnt lgkmcnt(0)\n"
    "s_add_u32 s49, s12, 0\n"
    "s_and_b32 s49, s49, 3\n"
    "s_mul_i32 s49, s49, 1572864\n"
    "s_add_u32 m0, s43, 0\n"
    "s_nop 0\n"
    "buffer_load_dwordx4 v0, s[8:11], s49 offen offset:0 lds\n"
    "buffer_load_dwordx4 v1, s[8:11], s49 offen offset:1024 lds\n"
    "buffer_load_dwordx4 v2, s[8:11], s49 offen offset:2048 lds\n"
    "s_add_u32 s49, s49, 393216\n"
    "s_add_u32 m0, s43, 3072\n"
    "s_nop 0\n"
    "buffer_load_dwordx4 v0, s[8:11], s49 offen offset:0 lds\n"
    "buffer_load_dwordx4 v1, s[8:11], s49 offen offset:1024 lds\n"
    "buffer_load_dwordx4 v2, s[8:11], s49 offen offset:2048 lds\n"
    "s_add_u32 s49, s49, 393216\n"
    "s_add_u32 m0, s43, 6144\n"
    "s_nop 0\n"
    "buffer_load_dwordx4 v0, s[8:11], s49 offen offset:0 lds\n"
    "buffer_load_dwordx4 v1, s[8:11], s49 offen offset:1024 lds\n"
    "buffer_load_dwordx4 v2, s[8:11], s49 offen offset:2048 lds\n"
    "s_add_u32 s49, s49, 393216\n"
    "s_add_u32 m0, s41, 12288\n"
    "s_nop 0\n"
    "buffer_load_dwordx4 v0, s[8:11], s49 offen offset:0 lds\n"
    "buffer_load_dwordx4 v1, s[8:11], s49 offen offset:1024 lds\n"
    "buffer_load_dwordx4 v2, s[8:11], s49 offen offset:2048 lds\n"
    "s_add_u32 s49, s12, 1\n"
    "s_and_b32 s49, s49, 3\n"
    "s_mul_i32 s49, s49, 1572864\n"
    "s_add_u32 m0, s42, 0\n"
    "s_nop 0\n"
    "buffer_load_dwordx4 v0, s[8:11], s49 offen offset:0 lds\n"
    "buffer_load_dwordx4 v1, s[8:11], s49 offen offset:1024 lds\n"
    "buffer_load_dwordx4 v2, s[8:11], s49 offen offset:2048 lds\n"
    "s_waitcnt vmcnt(25)\n"
    "ds_read_b64_tr_b16 a[16:17], v36 offset:10240\n"
    "ds_read_b64_tr_b16 a[18:19], v36 offset:14336\n"
    "ds_read_b64_tr_b16 a[40:41], v37 offset:10240\n"
    "ds_read_b64_tr_b16 a[42:43], v37 offset:14336\n"
    "ds_read_b64_tr_b16 a[64:65], v38 offset:10240\n"
    "ds_read_b64_tr_b16 a[66:67], v38 offset:14336\n"
    "ds_read_b64_tr_b16 a[88:89], v39 offset:10240\n"
    "ds_read_b64_tr_b16 a[90:91], v39 offset:14336\n"
    "ds_read_b64_tr_b16 a[112:113], v40 offset:10240\n"
    "ds_read_b64_tr_b16 a[114:115], v40 offset:14336\n"
    "ds_read_b64_tr_b16 a[136:137], v41 offset:10240\n"
    "ds_read_b64_tr_b16 a[138:139], v41 offset:14336\n"
    "ds_read_b64_tr_b16 a[160:161], v42 offset:10240\n"
    "ds_read_b64_tr_b16 a[162:163], v42 offset:14336\n"
    "ds_read_b64_tr_b16 a[184:185], v43 offset:10240\n"
    "ds_read_b64_tr_b16 a[186:187], v43 offset:14336\n"
    "ds_read_b64_tr_b16 a[208:209], v44 offset:10240\n"
    "ds_read_b64_tr_b16 a[210:211], v44 offset:11264\n"
    "ds_read_b64_tr_b16 a[232:233], v44 offset:10272\n"
    "ds_read_b64_tr_b16 a[234:235], v44 offset:11296\n"
    "s_waitcnt lgkmcnt(0)\n"
    "s_add_u32 s49, s49, 393216\n"
    "s_add_u32 m0, s43, 9216\n"
    "s_nop 0\n"
    "buffer_load_dwordx4 v0, s[8:11], s49 offen offset:0 lds\n"
    "buffer_load_dwordx4 v1, s[8:11], s49 offen offset:1024 lds\n"
    "buffer_load_dwordx4 v2, s[8:11], s49 offen offset:2048 lds\n"
    "s_add_u32 s49, s49, 393216\n"
    "s_add_u32 m0, s43, 12288\n"
    "s_nop 0\n"
    "buffer_load_dwordx4 v0, s[8:11], s49 offen offset:0 lds\n"
    "buffer_load_dwordx4 v1, s[8:11], s49 offen offset:1024 lds\n"
    "buffer_load_dwordx4 v2, s[8:11], s49 offen offset:2048 lds\n"
    "s_add_u32 s49, s49, 393216\n"
    "s_add_u32 m0, s43, 15360\n"
    "s_nop 0\n"
    "buffer_load_dwordx4 v0, s[8:11], s49 offen offset:0 lds\n"
    "buffer_load_dwordx4 v1, s[8:11], s49 offen offset:1024 lds\n"
    "buffer_load_dwordx4 v2, s[8:11], s49 offen offset:2048 lds\n"
    "s_waitcnt vmcnt(24)\n"
    "ds_read_b64_tr_b16 a[20:21], v24 offset:0\n"
    "ds_read_b64_tr_b16 a[22:23], v24 offset:4096\n"
    "ds_read_b64_tr_b16 a[44:45], v25 offset:0\n"
    "ds_read_b64_tr_b16 a[46:47], v25 offset:4096\n"
    "ds_read_b64_tr_b16 a[68:69], v26 offset:0\n"
    "ds_read_b64_tr_b16 a[70:71], v26 offset:4096\n"
    "ds_read_b64_tr_b16 a[92:93], v27 offset:0\n"
    "ds_read_b64_tr_b16 a[94:95], v27 offset:4096\n"
    "ds_read_b64_tr_b16 a[116:117], v28 offset:0\n"
    "ds_read_b64_tr_b16 a[118:119], v28 offset:4096\n"
    "ds_read_b64_tr_b16 a[140:141], v29 offset:0\n"
    "ds_read_b64_tr_b16 a[142:143], v29 offset:4096\n"
    "ds_read_b64_tr_b16 a[164:165], v30 offset:0\n"
    "ds_read_b64_tr_b16 a[166:167], v30 offset:4096\n"
    "ds_read_b64_tr_b16 a[188:189], v31 offset:0\n"
    "ds_read_b64_tr_b16 a[190:191], v31 offset:4096\n"
    "ds_read_b64_tr_b16 a[212:213], v32 offset:0\n"
    "ds_read_b64_tr_b16 a[214:215], v32 offset:1024\n"
    "ds_read_b64_tr_b16 a[236:237], v32 offset:32\n"
    "ds_read_b64_tr_b16 a[238:239], v32 offset:1056\n"
    "s_waitcnt lgkmcnt(0)\n"
    "s_add_u32 s49, s12, 2\n"
    "s_and_b32 s49, s49, 3\n"
    "s_mul_i32 s49, s49, 1572864\n"
    "s_add_u32 m0, s41, 0\n"
    "s_nop 0\n"
    "buffer_load_dwordx4 v0, s[8:11], s49 offen offset:0 lds\n"
    "buffer_load_dwordx4 v1, s[8:11], s49 offen offset:1024 lds\n"
    "buffer_load_dwordx4 v2, s[8:11], s49 offen offset:2048 lds\n"
    "s_add_u32 s49, s49, 393216\n"
    "s_add_u32 m0, s41, 3072\n"
    "s_nop 0\n"
    "buffer_load_dwordx4 v0, s[8:11], s49 offen offset:0 lds\n"
    "buffer_load_dwordx4 v1, s[8:11], s49 offen offset:1024 lds\n"
    "buffer_load_dwordx4 v2, s[8:11], s49 offen offset:2048 lds\n"
    "s_add_u32 s49, s49, 393216\n"
    "s_add_u32 m0, s41, 6144\n"
    "s_nop 0\n"
    "buffer_load_dwordx4 v0, s[8:11], s49 offen offset:0 lds\n"
    "buffer_load_dwordx4 v1, s[8:11], s49 offen offset:1024 lds\n"
    "buffer_load_dwordx4 v2, s[8:11], s49 offen offset:2048 lds\n"
    "s_add_u32 s49, s49, 393216\n"
    "s_add_u32 m0, s41, 9216\n"
    "s_nop 0\n"
    "buffer_load_dwordx4 v0, s[8:11], s49 offen offset:0 lds\n"
    "buffer_load_dwordx4 v1, s[8:11], s49 offen offset:1024 lds\n"
    "buffer_load_dwordx4 v2, s[8:11], s49 offen offset:2048 lds\n"
    "s_waitcnt vmcnt(33)\n"
    "ds_read_b64_tr_b16 a[240:241], v18 offset:0\n"
    "ds_read_b64_tr_b16 a[242:243], v18 offset:192\n"
    "ds_read_b64_tr_b16 v[176:177], v18 offset:32\n"
    "ds_read_b64_tr_b16 v[178:179], v18 offset:224\n"
    "ds_read_b64_tr_b16 v[20:21], v18 offset:64\n"
    "ds_read_b64_tr_b16 v[22:23], v18 offset:256\n"
    "s_waitcnt lgkmcnt(0)\n"
    "ds_write_b128 v97, v[20:23] offset:0\n"
    "s_waitcnt vmcnt(30)\n"
    "ds_read_b64_tr_b16 a[244:245], v18 offset:3072\n"
    "ds_read_b64_tr_b16 a[246:247], v18 offset:3264\n"
    "ds_read_b64_tr_b16 v[180:181], v18 offset:3104\n"
    "ds_read_b64_tr_b16 v[182:183], v18 offset:3296\n"
    "ds_read_b64_tr_b16 v[20:21], v18 offset:3136\n"
    "ds_read_b64_tr_b16 v[22:23], v18 offset:3328\n"
    "s_waitcnt lgkmcnt(0)\n"
    "ds_write_b128 v97, v[20:23] offset:1024\n"
    "s_waitcnt vmcnt(27)\n"
    "ds_read_b64_tr_b16 a[248:249], v18 offset:6144\n"
    "ds_read_b64_tr_b16 a[250:251], v18 offset:6336\n"
    "ds_read_b64_tr_b16 v[184:185], v18 offset:6176\n"
    "ds_read_b64_tr_b16 v[186:187], v18 offset:6368\n"
    "ds_read_b64_tr_b16 v[20:21], v18 offset:6208\n"
    "ds_read_b64_tr_b16 v[22:23], v18 offset:6400\n"
    "s_waitcnt lgkmcnt(0)\n"
    "ds_write_b128 v97, v[20:23] offset:2048\n"
    "s_waitcnt vmcnt(24)\n"
    "ds_read_b64_tr_b16 a[252:253], v16 offset:12288\n"
    "ds_read_b64_tr_b16 a[254:255], v16 offset:12480\n"
    "ds_read_b64_tr_b16 v[188:189], v16 offset:12320\n"
    "ds_read_b64_tr_b16 v[190:191], v16 offset:12512\n"
    "ds_read_b64_tr_b16 v[20:21], v16 offset:12352\n"
    "ds_read_b64_tr_b16 v[22:23], v16 offset:12544\n"
    "s_waitcnt lgkmcnt(0)\n"
    "ds_write_b128 v97, v[20:23] offset:3072\n"
    "s_add_u32 s49, s12, 3\n"
    "s_and_b32 s49, s49, 3\n"
    "s_mul_i32 s49, s49, 1572864\n"
    "s_add_u32 m0, s41, 12288\n"
    "s_nop 0\n"
    "buffer_load_dwordx4 v0, s[8:11], s49 offen offset:0 lds\n"
    "buffer_load_dwordx4 v1, s[8:11], s49 offen offset:1024 lds\n"
    "buffer_load_dwordx4 v2, s[8:11], s49 offen offset:2048 lds\n"
    "s_waitcnt vmcnt(24)\n"
    "ds_read_b64_tr_b16 v[112:113], v17 offset:0\n"
    "ds_read_b64_tr_b16 v[114:115], v17 offset:192\n"
    "ds_read_b64_tr_b16 v[192:193], v17 offset:32\n"
    "ds_read_b64_tr_b16 v[194:195], v17 offset:224\n"
    "ds_read_b64_tr_b16 v[20:21], v17 offset:64\n"
    "ds_read_b64_tr_b16 v[22:23], v17 offset:256\n"
    "s_waitcnt lgkmcnt(0)\n"
    "ds_write_b128 v97, v[20:23] offset:4096\n"
    "s_add_u32 s49, s49, 393216\n"
    "s_add_u32 m0, s42, 0\n"
    "s_nop 0\n"
    "buffer_load_dwordx4 v0, s[8:11], s49 offen offset:0 lds\n"
    "buffer_load_dwordx4 v1, s[8:11], s49 offen offset:1024 lds\n"
    "buffer_load_dwordx4 v2, s[8:11], s49 offen offset:2048 lds\n"
    "s_waitcnt vmcnt(24)\n"
    "ds_read_b64_tr_b16 v[116:117], v18 offset:9216\n"
    "ds_read_b64_tr_b16 v[118:119], v18 offset:9408\n"
    "ds_read_b64_tr_b16 v[196:197], v18 offset:9248\n"
    "ds_read_b64_tr_b16 v[198:199], v18 offset:9440\n"
    "ds_read_b64_tr_b16 v[20:21], v18 offset:9280\n"
    "ds_read_b64_tr_b16 v[22:23], v18 offset:9472\n"
    "s_waitcnt lgkmcnt(0)\n"
    "ds_write_b128 v97, v[20:23] offset:5120\n"
    "s_waitcnt vmcnt(21)\n"
    "ds_read_b64_tr_b16 v[120:121], v18 offset:12288\n"
    "ds_read_b64_tr_b16 v[122:123], v18 offset:12480\n"
    "ds_read_b64_tr_b16 v[200:201], v18 offset:12320\n"
    "ds_read_b64_tr_b16 v[202:203], v18 offset:12512\n"
    "ds_read_b64_tr_b16 v[20:21], v18 offset:12352\n"
    "ds_read_b64_tr_b16 v[22:23], v18 offset:12544\n"
    "s_waitcnt lgkmcnt(0)\n"
    "ds_write_b128 v97, v[20:23] offset:6144\n"
    "s_waitcnt vmcnt(18)\n"
    "ds_read_b64_tr_b16 v[124:125], v18 offset:15360\n"
    "ds_read_b64_tr_b16 v[126:127], v18 offset:15552\n"
    "ds_read_b64_tr_b16 v[204:205], v18 offset:15392\n"
    "ds_read_b64_tr_b16 v[206:207], v18 offset:15584\n"
    "ds_read_b64_tr_b16 v[20:21], v18 offset:15424\n"
    "ds_read_b64_tr_b16 v[22:23], v18 offset:15616\n"
    "s_waitcnt lgkmcnt(0)\n"
    "ds_write_b128 v97, v[20:23] offset:7168\n"
    "s_add_u32 s49, s49, 393216\n"
    "s_add_u32 m0, s43, 15360\n"
    "s_nop 0\n"
    "buffer_load_dwordx4 v0, s[8:11], s49 offen offset:0 lds\n"
    "buffer_load_dwordx4 v1, s[8:11], s49 offen offset:1024 lds\n"
    "buffer_load_dwordx4 v2, s[8:11], s49 offen offset:2048 lds\n"
    "s_waitcnt vmcnt(18)\n"
    "ds_read_b64_tr_b16 v[128:129], v16 offset:0\n"
    "ds_read_b64_tr_b16 v[130:131], v16 offset:192\n"
    "ds_read_b64_tr_b16 v[208:209], v16 offset:32\n"
    "ds_read_b64_tr_b16 v[210:211], v16 offset:224\n"
    "ds_read_b64_tr_b16 v[20:21], v16 offset:64\n"
    "ds_read_b64_tr_b16 v[22:23], v16 offset:256\n"
    "s_waitcnt lgkmcnt(0)\n"
    "ds_write_b128 v97, v[20:23] offset:8192\n"
    "s_add_u32 s49, s49, 393216\n"
    "s_add_u32 m0, s41, 0\n"
    "s_nop 0\n"
    "buffer_load_dwordx4 v0, s[8:11], s49 offen offset:0 lds\n"
    "buffer_load_dwordx4 v1, s[8:11], s49 offen offset:1024 lds\n"
    "buffer_load_dwordx4 v2, s[8:11], s49 offen offset:2048 lds\n"
    "s_waitcnt vmcnt(18)\n"
    "ds_read_b64_tr_b16 v[132:133], v16 offset:3072\n"
    "ds_read_b64_tr_b16 v[134:135], v16 offset:3264\n"
    "ds_read_b64_tr_b16 v[212:213], v16 offset:3104\n"
    "ds_read_b64_tr_b16 v[214:215], v16 offset:3296\n"
    "ds_read_b64_tr_b16 v[20:21], v16 offset:3136\n"
    "ds_read_b64_tr_b16 v[22:23], v16 offset:3328\n"
    "s_waitcnt lgkmcnt(0)\n"
    "ds_write_b128 v97, v[20:23] offset:9216\n"
    "s_mov_b32 s49, 6291456\n"
    "s_add_u32 m0, s41, 3072\n"
    "s_nop 0\n"
    "buffer_load_dwordx4 v0, s[8:11], s49 offen offset:0 lds\n"
    "buffer_load_dwordx4 v1, s[8:11], s49 offen offset:1024 lds\n"
    "buffer_load_dwordx4 v2, s[8:11], s49 offen offset:2048 lds\n"
    "s_waitcnt vmcnt(18)\n"
    "ds_read_b64_tr_b16 v[136:137], v16 offset:6144\n"
    "ds_read_b64_tr_b16 v[138:139], v16 offset:6336\n"
    "ds_read_b64_tr_b16 v[216:217], v16 offset:6176\n"
    "ds_read_b64_tr_b16 v[218:219], v16 offset:6368\n"
    "ds_read_b64_tr_b16 v[20:21], v16 offset:6208\n"
    "ds_read_b64_tr_b16 v[22:23], v16 offset:6400\n"
    "s_waitcnt lgkmcnt(0)\n"
    "ds_write_b128 v97, v[20:23] offset:10240\n"
    "s_add_u32 s49, s49, 393216\n"
    "s_add_u32 m0, s41, 6144\n"
    "s_nop 0\n"
    "buffer_load_dwordx4 v0, s[8:11], s49 offen offset:0 lds\n"
    "buffer_load_dwordx4 v1, s[8:11], s49 offen offset:1024 lds\n"
    "buffer_load_dwordx4 v2, s[8:11], s49 offen offset:2048 lds\n"
    "s_waitcnt vmcnt(18)\n"
    "ds_read_b64_tr_b16 v[140:141], v16 offset:9216\n"
    "ds_read_b64_tr_b16 v[142:143], v16 offset:9408\n"
    "ds_read_b64_tr_b16 v[220:221], v16 offset:9248\n"
    "ds_read_b64_tr_b16 v[222:223], v16 offset:9440\n"
    "ds_read_b64_tr_b16 v[20:21], v16 offset:9280\n"
    "ds_read_b64_tr_b16 v[22:23], v16 offset:9472\n"
    "s_waitcnt lgkmcnt(0)\n"
    "ds_write_b128 v97, v[20:23] offset:11264\n"
    "s_add_u32 s49, s49, 393216\n"
    "s_add_u32 m0, s41, 9216\n"
    "s_nop 0\n"
    "buffer_load_dwordx4 v0, s[8:11], s49 offen offset:0 lds\n"
    "buffer_load_dwordx4 v1, s[8:11], s49 offen offset:1024 lds\n"
    "buffer_load_dwordx4 v2, s[8:11], s49 offen offset:2048 lds\n"
    "s_waitcnt vmcnt(18)\n"
    "ds_read_b64_tr_b16 v[144:145], v16 offset:12288\n"
    "ds_read_b64_tr_b16 v[146:147], v16 offset:12480\n"
    "ds_read_b64_tr_b16 v[224:225], v16 offset:12320\n"
    "ds_read_b64_tr_b16 v[226:227], v16 offset:12512\n"
    "ds_read_b64_tr_b16 v[20:21], v16 offset:12352\n"
    "ds_read_b64_tr_b16 v[22:23], v16 offset:12544\n"
    "s_waitcnt lgkmcnt(0)\n"
    "ds_write_b128 v97, v[20:23] offset:12288\n"
    "s_add_u32 s49, s49, 393216\n"
    "s_add_u32 m0, s41, 12288\n"
    "s_nop 0\n"
    "buffer_load_dwordx4 v0, s[8:11], s49 offen offset:0 lds\n"
    "buffer_load_dwordx4 v1, s[8:11], s49 offen offset:1024 lds\n"
    "buffer_load_dwordx4 v2, s[8:11], s49 offen offset:2048 lds\n"
    "s_waitcnt vmcnt(18)\n"
    "ds_read_b64_tr_b16 v[148:149], v17 offset:0\n"
    "ds_read_b64_tr_b16 v[150:151], v17 offset:192\n"
    "ds_read_b64_tr_b16 v[228:229], v17 offset:32\n"
    "ds_read_b64_tr_b16 v[230:231], v17 offset:224\n"
    "ds_read_b64_tr_b16 v[20:21], v17 offset:64\n"
    "ds_read_b64_tr_b16 v[22:23], v17 offset:256\n"
    "s_waitcnt lgkmcnt(0)\n"
    "ds_write_b128 v97, v[20:23] offset:13312\n"
    "s_waitcnt vmcnt(15)\n"
    "ds_read_b64_tr_b16 v[152:153], v18 offset:15360\n"
    "ds_read_b64_tr_b16 v[154:155], v18 offset:15552\n"
    "ds_read_b64_tr_b16 v[232:233], v18 offset:15392\n"
    "ds_read_b64_tr_b16 v[234:235], v18 offset:15584\n"
    "ds_read_b64_tr_b16 v[20:21], v18 offset:15424\n"
    "ds_read_b64_tr_b16 v[22:23], v18 offset:15616\n"
    "s_waitcnt lgkmcnt(0)\n"
    "ds_write_b128 v97, v[20:23] offset:14336\n"
    "s_waitcnt vmcnt(12)\n"
    "ds_read_b64_tr_b16 v[156:157], v16 offset:0\n"
    "ds_read_b64_tr_b16 v[158:159], v16 offset:192\n"
    "ds_read_b64_tr_b16 v[236:237], v16 offset:32\n"
    "ds_read_b64_tr_b16 v[238:239], v16 offset:224\n"
    "ds_read_b64_tr_b16 v[20:21], v16 offset:64\n"
    "ds_read_b64_tr_b16 v[22:23], v16 offset:256\n"
    "s_waitcnt lgkmcnt(0)\n"
    "ds_write_b128 v97, v[20:23] offset:15360\n"
    "s_waitcnt vmcnt(9)\n"
    "ds_read_b64_tr_b16 v[160:161], v16 offset:3072\n"
    "ds_read_b64_tr_b16 v[162:163], v16 offset:3264\n"
    "ds_read_b64_tr_b16 v[240:241], v16 offset:3104\n"
    "ds_read_b64_tr_b16 v[242:243], v16 offset:3296\n"
    "ds_read_b64_tr_b16 v[20:21], v16 offset:3136\n"
    "ds_read_b64_tr_b16 v[22:23], v16 offset:3328\n"
    "s_waitcnt lgkmcnt(0)\n"
    "ds_write_b128 v97, v[20:23] offset:16384\n"
    "s_waitcnt vmcnt(6)\n"
    "ds_read_b64_tr_b16 v[164:165], v16 offset:6144\n"
    "ds_read_b64_tr_b16 v[166:167], v16 offset:6336\n"
    "ds_read_b64_tr_b16 v[244:245], v16 offset:6176\n"
    "ds_read_b64_tr_b16 v[246:247], v16 offset:6368\n"
    "ds_read_b64_tr_b16 v[20:21], v16 offset:6208\n"
    "ds_read_b64_tr_b16 v[22:23], v16 offset:6400\n"
    "s_waitcnt lgkmcnt(0)\n"
    "ds_write_b128 v97, v[20:23] offset:17408\n"
    "s_waitcnt vmcnt(3)\n"
    "ds_read_b64_tr_b16 v[168:169], v16 offset:9216\n"
    "ds_read_b64_tr_b16 v[170:171], v16 offset:9408\n"
    "ds_read_b64_tr_b16 v[248:249], v16 offset:9248\n"
    "ds_read_b64_tr_b16 v[250:251], v16 offset:9440\n"
    "ds_read_b64_tr_b16 v[20:21], v16 offset:9280\n"
    "ds_read_b64_tr_b16 v[22:23], v16 offset:9472\n"
    "s_waitcnt lgkmcnt(0)\n"
    "ds_write_b128 v97, v[20:23] offset:18432\n"
    "s_waitcnt vmcnt(0)\n"
    "ds_read_b64_tr_b16 v[172:173], v16 offset:12288\n"
    "ds_read_b64_tr_b16 v[174:175], v16 offset:12480\n"
    "ds_read_b64_tr_b16 v[252:253], v16 offset:12320\n"
    "ds_read_b64_tr_b16 v[254:255], v16 offset:12512\n"
    "ds_read_b64_tr_b16 v[20:21], v16 offset:12352\n"
    "ds_read_b64_tr_b16 v[22:23], v16 offset:12544\n"
    "s_waitcnt lgkmcnt(0)\n"
    "ds_write_b128 v97, v[20:23] offset:19456\n"
    "s_waitcnt lgkmcnt(0)\n"
    "s_barrier\n"
    "s_mov_b32 s34, -4\n"
    "s_lshl_b32 s48, s12, 11\n"
    "v_lshrrev_b32 v4, 2, v96\n"
    ".Lvo_po1_%=:\n"
    "buffer_load_dword v8, v4, s[0:3], s21 offen sc1\n"
    "s_waitcnt vmcnt(0)\n"
    "v_cmp_gt_i32 vcc, 1, v8\n"
    "s_nop 0\n"
    "s_cbranch_vccz .Lvo_pd2_%=\n"
    "s_add_u32 s28, s28, 1\n"
    "s_cmp_lt_u32 s28, 0x40000\n"
    "s_cbranch_scc1 .Lvo_sp3_%=\n"
    "v_mov_b32 v8, 30\n"
    "s_mov_b32 s32, 33728\n"
    "buffer_store_dword v8, off, s[0:3], s32 sc1\n"
    "s_waitcnt vmcnt(0)\n"
    "s_branch .Lvo_pd2_%=\n"
    ".Lvo_sp3_%=:\n"
    "s_sleep 4\n"
    "s_branch .Lvo_po1_%=\n"
    ".Lvo_pd2_%=:\n"
    ".Lvo_po4_%=:\n"
    "buffer_load_dword v8, v105, s[0:3], s22 offen sc1\n"
    "s_waitcnt vmcnt(0)\n"
    "v_cmp_gt_i32 vcc, s14, v8\n"
    "s_nop 0\n"
    "s_cbranch_vccz .Lvo_pd5_%=\n"
    "s_add_u32 s28, s28, 1\n"
    "s_cmp_lt_u32 s28, 0x40000\n"
    "s_cbranch_scc1 .Lvo_sp6_%=\n"
    "v_mov_b32 v8, 31\n"
    "s_mov_b32 s32, 33728\n"
    "buffer_store_dword v8, off, s[0:3], s32 sc1\n"
    "s_waitcnt vmcnt(0)\n"
    "s_branch .Lvo_pd5_%=\n"
    ".Lvo_sp6_%=:\n"
    "s_sleep 4\n"
    "s_branch .Lvo_po4_%=\n"
    ".Lvo_pd5_%=:\n"
    "s_add_u32 s43, s17, s13\n"
    "s_add_u32 m0, s48, 0\n"
    "s_nop 0\n"
    "buffer_load_dwordx4 v96, s[0:3], s43 offen sc1 lds\n"
    "buffer_load_dwordx4 v96, s[0:3], s43 offen offset:1024 sc1 lds\n"
    "s_add_u32 m0, s48, 13312\n"
    "s_add_u32 s43, s43, 0x4000\n"
    "buffer_load_dwordx4 v96, s[0:3], s43 offen sc1 lds\n"
    "buffer_load_dwordx4 v96, s[0:3], s43 offen offset:1024 sc1 lds\n"
    "s_add_u32 m0, s48, 26624\n"
    "s_add_u32 s43, s43, 0x4000\n"
    "buffer_load_dwordx4 v96, s[0:3], s43 offen sc1 lds\n"
    "buffer_load_dwordx4 v96, s[0:3], s43 offen offset:1024 sc1 lds\n"
    "s_add_u32 m0, s48, 39936\n"
    "s_add_u32 s43, s43, 0x4000\n"
    "buffer_load_dwordx4 v96, s[0:3], s43 offen sc1 lds\n"
    "buffer_load_dwordx4 v96, s[0:3], s43 offen offset:1024 sc1 lds\n"
    "s_add_u32 m0, s48, 53248\n"
    "s_add_u32 s43, s43, 0x4000\n"
    "buffer_load_dwordx4 v96, s[0:3], s43 offen sc1 lds\n"
    "buffer_load_dwordx4 v96, s[0:3], s43 offen offset:1024 sc1 lds\n"
    "s_memrealtime s[30:31]\n"
    "s_waitcnt vmcnt(0) lgkmcnt(0)\n"
    "s_sub_u32 s46, s30, 1520\n"
    "s_mov_b32 s34, -3\n"
    "s_mov_b32 s35, -5\n"
    "s_mov_b32 s45, s20\n"
    "s_mov_b32 s38, 2130706432\n"
    "s_mov_b32 s39, 2130706432\n"
    "s_branch .Lvo_vph_%=\n"
    ".Lvo_take_%=:\n"
    "s_waitcnt vmcnt(4) lgkmcnt(0)\n"
    "ds_write_b128 v92, v[72:75] offset:8192\n"
    "v_smfmac_f32_16x16x64_bf16 v[84:87], v[72:75], v[0:7], v98\n"
    "s_waitcnt vmcnt(3)\n"
    "ds_write_b128 v92, v[76:79] offset:9216\n"
    "v_max3_u32 v108, v72, v74, v76\n"
    "s_waitcnt vmcnt(2)\n"
    "ds_write_b128 v92, v[80:83] offset:10240\n"
    "v_max3_u32 v108, v108, v78, v80\n"
    "v_smfmac_f32_16x16x64_bf16 v[64:67], v[72:75], a[240:247], v98\n"
    "v_max_u32 v108, v108, v82\n"
    "v_cmp_eq_u32 vcc, -1, v108\n"
    "s_nop 0\n"
    "s_cbranch_vccnz .Lvo_retry_%=\n"
    ".Lvo_go_%=:\n"
    "s_waitcnt lgkmcnt(0)\n"
    "s_barrier\n"
    "ds_read_b128 v[88:91], v93 offset:8192\n"
    "ds_read_b128 v[80:83], v93 offset:9216\n"
    "v_smfmac_f32_16x16x64_bf16 v[68:71], v[72:75], v[176:183], v98\n"
    "ds_read_b128 v[72:75], v94 offset:8192\n"
    "v_smfmac_f32_16x16x64_bf16 v[84:87], v[76:79], v[8:15], v98\n"
    "s_waitcnt vmcnt(0)\n"
    "v_cmp_gt_i32 vcc, s50, v110\n"
    "v_smfmac_f32_16x16x64_bf16 v[64:67], v[76:79], a[248:255], v98\n"
    "s_cbranch_vccnz .Lvo_wq_%=\n"
    ".Lvo_wqb_%=:\n"
    "buffer_load_dwordx4 v[32:35], v96, s[0:3], s44 offen offset:0 sc1\n"
    "v_smfmac_f32_16x16x64_bf16 v[68:71], v[76:79], v[184:191], v98\n"
    "ds_read_b128 v[76:79], v94 offset:9216\n"
    "s_waitcnt lgkmcnt(3)\n"
    "v_smfmac_f32_16x16x64_bf16 v[84:87], v[88:91], v[16:23], v98\n"
    "ds_read_b128 v[0:3], v97 offset:12288\n"
    "v_smfmac_f32_16x16x64_bf16 v[64:67], v[88:91], v[112:119], v98\n"
    "ds_read_b128 v[4:7], v97 offset:13312\n"
    "v_smfmac_f32_16x16x64_bf16 v[68:71], v[88:91], v[192:199], v98\n"
    "ds_read_b128 v[88:91], v95 offset:8192\n"
    "s_waitcnt lgkmcnt(5)\n"
    "v_smfmac_f32_16x16x64_bf16 v[84:87], v[80:83], v[24:31], v98\n"
    "ds_read_b128 v[8:11], v97 offset:14336\n"
    "buffer_load_dwordx4 v[36:39], v96, s[0:3], s44 offen offset:1024 sc1\n"
    "v_smfmac_f32_16x16x64_bf16 v[64:67], v[80:83], v[120:127], v98\n"
    "ds_read_b128 v[12:15], v97 offset:15360\n"
    "v_smfmac_f32_16x16x64_bf16 v[68:71], v[80:83], v[200:207], v98\n"
    "ds_read_b128 v[80:83], v95 offset:9216\n"
    "s_waitcnt lgkmcnt(7)\n"
    "v_smfmac_f32_16x16x64_bf16 v[84:87], v[72:75], v[48:55], v98\n"
    "ds_read_b128 v[16:19], v97 offset:16384\n"
    "v_smfmac_f32_16x16x64_bf16 v[64:67], v[72:75], v[128:135], v98\n"
    "ds_read_b128 v[20:23], v97 offset:17408\n"
    "buffer_load_dwordx4 v[40:43], v96, s[0:3], s44 offen offset:2048 sc1\n"
    "v_smfmac_f32_16x16x64_bf16 v[68:71], v[72:75], v[208:215], v98\n"
    "ds_read_b128 v[72:75], v96 offset:10240\n"
    "s_waitcnt lgkmcnt(9)\n"
    "v_smfmac_f32_16x16x64_bf16 v[84:87], v[76:79], v[56:63], v98\n"
    "ds_read_b128 v[24:27], v97 offset:18432\n"
    "v_smfmac_f32_16x16x64_bf16 v[64:67], v[76:79], v[136:143], v98\n"
    "ds_read_b128 v[28:31], v97 offset:19456\n"
    "v_mov_b64 v[48:49], 0\n"
    "v_smfmac_f32_16x16x64_bf16 v[68:71], v[76:79], v[216:223], v98\n"
    "ds_read_b128 v[76:79], v96 offset:23552\n"
    "s_waitcnt lgkmcnt(9)\n"
    "v_smfmac_f32_16x16x64_bf16 v[84:87], v[88:91], v[0:7], v98\n"
    "buffer_load_dwordx4 v[44:47], v96, s[0:3], s44 offen offset:3072 sc1\n"
    "v_cmp_gt_i32 vcc, s51, v111\n"
    "v_smfmac_f32_16x16x64_bf16 v[64:67], v[88:91], v[144:151], v98\n"
    "s_cbranch_vccnz .Lvo_wx_%=\n"
    ".Lvo_wxb_%=:\n"
    "s_add_u32 m0, s48, 0\n"
    "v_smfmac_f32_16x16x64_bf16 v[68:71], v[88:91], v[224:231], v98\n"
    "s_waitcnt lgkmcnt(3)\n"
    "v_mov_b64 v[50:51], 0\n"
    "v_smfmac_f32_16x16x64_bf16 v[84:87], v[80:83], v[8:15], v98\n"
    "v_mov_b64 v[52:53], 0\n"
    "v_mov_b64 v[54:55], 0\n"
    "v_smfmac_f32_16x16x64_bf16 v[84:87], v[72:75], v[16:23], v98\n"
    "s_waitcnt lgkmcnt(0)\n"
    "buffer_load_dwordx4 v96, s[0:3], s43 offen sc1 lds\n"
    "v_smfmac_f32_16x16x64_bf16 v[84:87], v[76:79], v[24:31], v98\n"
    "v_mov_b64 v[56:57], 0\n"
    "v_mov_b64 v[58:59], 0\n"
    "v_smfmac_f32_16x16x64_bf16 v[64:67], v[80:83], v[152:159], v98\n"
    "v_mov_b64 v[60:61], 0\n"
    "v_mov_b64 v[62:63], 0\n"
    "v_smfmac_f32_16x16x64_bf16 v[64:67], v[72:75], v[160:167], v98\n"
    "v_mov_b64 v[24:25], 0\n"
    "v_mov_b64 v[26:27], 0\n"
    "v_smfmac_f32_16x16x64_bf16 v[64:67], v[76:79], v[168:175], v98\n"
    "buffer_load_dwordx4 v96, s[0:3], s43 offen offset:1024 sc1 lds\n"
    "v_mov_b64 v[28:29], 0\n"
    "v_smfmac_f32_16x16x64_bf16 v[68:71], v[80:83], v[232:239], v98\n"
    "v_mov_b64 v[30:31], 0\n"
    "v_smfmac_f32_16x16x64_bf16 v[68:71], v[72:75], v[240:247], v98\n"
    "v_smfmac_f32_16x16x64_bf16 v[68:71], v[76:79], v[248:255], v98\n"
    "v_pk_add_f32 v[84:85], v[84:85], v[86:87]\n"
    "v_cvt_pk_bf16_f32 v84, v84, v85\n"
    "ds_write_b32 v100, v84 offset:0\n"
    "v_pk_add_f32 v[64:65], v[64:65], v[66:67]\n"
    "v_cvt_pk_bf16_f32 v64, v64, v65\n"
    "ds_write_b32 v99, v64 offset:0\n"
    "s_nop 1\n"
    "v_pk_add_f32 v[68:69], v[68:69], v[70:71]\n"
    "v_cvt_pk_bf16_f32 v68, v68, v69\n"
    "ds_write_b32 v99, v68 offset:256\n"
    "s_waitcnt lgkmcnt(0)\n"
    "s_barrier\n"
    "ds_read_b64_tr_b16 v[72:73], v101 offset:0\n"
    "ds_read_b64_tr_b16 v[74:75], v101 offset:64\n"
    "ds_read_b64_tr_b16 v[76:77], v101 offset:1024\n"
    "ds_read_b64_tr_b16 v[78:79], v101 offset:1088\n"
    "ds_read_b64_tr_b16 v[80:81], v101 offset:2048\n"
    "ds_read_b64_tr_b16 v[82:83], v101 offset:2112\n"
    "v_mov_b64 v[64:65], 0\n"
    "v_mov_b64 v[66:67], 0\n"
    "v_mov_b64 v[68:69], 0\n"
    "v_mov_b64 v[70:71], 0\n"
    "v_mov_b64 v[84:85], 0\n"
    "v_mov_b64 v[86:87], 0\n"
    "v_mov_b64 v[88:89], 0\n"
    "v_mov_b64 v[90:91], 0\n"
    "s_waitcnt lgkmcnt(4)\n"
    "v_smfmac_f32_16x16x64_bf16 v[24:27], v[72:75], a[192:199], v98\n"
    "s_add_u32 m0, s48, 13312\n"
    "s_add_u32 s43, s43, 0x4000\n"
    "v_smfmac_f32_16x16x64_bf16 v[28:31], v[72:75], a[216:223], v98\n"
    "s_waitcnt lgkmcnt(2)\n"
    "buffer_load_dwordx4 v96, s[0:3], s43 offen sc1 lds\n"
    "v_smfmac_f32_16x16x64_bf16 v[24:27], v[76:79], a[200:207], v98\n"
    "ds_read_b128 v[0:3], v96 offset:11264\n"
    "v_smfmac_f32_16x16x64_bf16 v[28:31], v[76:79], a[224:231], v98\n"
    "s_waitcnt lgkmcnt(1)\n"
    "ds_read_b128 v[4:7], v96 offset:12288\n"
    "v_smfmac_f32_16x16x64_bf16 v[24:27], v[80:83], a[208:215], v98\n"
    "ds_read_b128 v[8:11], v96 offset:24576\n"
    "v_smfmac_f32_16x16x64_bf16 v[28:31], v[80:83], a[232:239], v98\n"
    "buffer_load_dwordx4 v96, s[0:3], s43 offen offset:1024 sc1 lds\n"
    "s_add_u32 m0, s48, 26624\n"
    "v_smfmac_f32_16x16x64_bf16 v[48:51], v[72:75], a[0:7], v98\n"
    "s_add_u32 s43, s43, 0x4000\n"
    "ds_read_b128 v[12:15], v96 offset:25600\n"
    "v_smfmac_f32_16x16x64_bf16 v[48:51], v[76:79], a[8:15], v98\n"
    "ds_read_b128 v[16:19], v96 offset:37888\n"
    "v_smfmac_f32_16x16x64_bf16 v[48:51], v[80:83], a[16:23], v98\n"
    "ds_read_b128 v[20:23], v96 offset:38912\n"
    "v_smfmac_f32_16x16x64_bf16 v[52:55], v[72:75], a[24:31], v98\n"
    "buffer_load_dwordx4 v96, s[0:3], s43 offen sc1 lds\n"
    "v_smfmac_f32_16x16x64_bf16 v[52:55], v[76:79], a[32:39], v98\n"
    "v_smfmac_f32_16x16x64_bf16 v[52:55], v[80:83], a[40:47], v98\n"
    "v_smfmac_f32_16x16x64_bf16 v[56:59], v[72:75], a[48:55], v98\n"
    "v_add_f32 v24, v24, v26\n"
    "v_add_f32 v25, v25, v27\n"
    "v_smfmac_f32_16x16x64_bf16 v[56:59], v[76:79], a[56:63], v98\n"
    "v_cvt_pk_bf16_f32 v108, v24, v25\n"
    "v_add_f32 v28, v28, v30\n"
    "v_smfmac_f32_16x16x64_bf16 v[56:59], v[80:83], a[64:71], v98\n"
    "v_add_f32 v29, v29, v31\n"
    "v_cvt_pk_bf16_f32 v109, v28, v29\n"
    "v_smfmac_f32_16x16x64_bf16 v[60:63], v[72:75], a[72:79], v98\n"
    "v_smfmac_f32_16x16x64_bf16 v[60:63], v[76:79], a[80:87], v98\n"
    "v_permlane16_swap_b32 v108, v109\n"
    "v_smfmac_f32_16x16x64_bf16 v[60:63], v[80:83], a[88:95], v98\n"
    "v_min_u32 v108, -2, v108\n"
    "buffer_store_dwordx2 v[108:109], v103, s[0:3], s36 offen sc1\n"
    "v_smfmac_f32_16x16x64_bf16 v[64:67], v[72:75], a[96:103], v98\n"
    "v_add_f32 v48, v48, v50\n"
    "v_add_f32 v49, v49, v51\n"
    "v_smfmac_f32_16x16x64_bf16 v[64:67], v[76:79], a[104:111], v98\n"
    "v_cvt_pk_bf16_f32 v48, v48, v49\n"
    "v_add_f32 v52, v52, v54\n"
    "v_smfmac_f32_16x16x64_bf16 v[64:67], v[80:83], a[112:119], v98\n"
    "v_add_f32 v53, v53, v55\n"
    "v_cvt_pk_bf16_f32 v49, v52, v53\n"
    "v_smfmac_f32_16x16x64_bf16 v[68:71], v[72:75], a[120:127], v98\n"
    "v_add_f32 v56, v56, v58\n"
    "v_add_f32 v57, v57, v59\n"
    "v_smfmac_f32_16x16x64_bf16 v[68:71], v[76:79], a[128:135], v98\n"
    "v_cvt_pk_bf16_f32 v50, v56, v57\n"
    "ds_read_b128 v[24:27], v96 offset:51200\n"
    "v_smfmac_f32_16x16x64_bf16 v[68:71], v[80:83], a[136:143], v98\n"
    "ds_read_b128 v[28:31], v96 offset:52224\n"
    "v_smfmac_f32_16x16x64_bf16 v[84:87], v[72:75], a[144:151], v98\n"
    "v_permlane32_swap_b32 v48, v50\n"
    "v_smfmac_f32_16x16x64_bf16 v[84:87], v[76:79], a[152:159], v98\n"
    "v_add_f32 v60, v60, v62\n"
    "v_add_f32 v61, v61, v63\n"
    "v_smfmac_f32_16x16x64_bf16 v[84:87], v[80:83], a[160:167], v98\n"
    "v_cvt_pk_bf16_f32 v51, v60, v61\n"
    "buffer_load_dwordx4 v96, s[0:3], s43 offen offset:1024 sc1 lds\n"
    "v_smfmac_f32_16x16x64_bf16 v[88:91], v[72:75], a[168:175], v98\n"
    "v_smfmac_f32_16x16x64_bf16 v[88:91], v[76:79], a[176:183], v98\n"
    "v_permlane32_swap_b32 v49, v51\n"
    "v_smfmac_f32_16x16x64_bf16 v[88:91], v[80:83], a[184:191], v98\n"
    ".Lvo_uph_%=:\n"
    "s_waitcnt lgkmcnt(0)\n"
    "v_mfma_f32_16x16x32_bf16 v[56:59], v[0:3], a[0:3], 0\n"
    "v_permlane16_swap_b32 v48, v49\n"
    "v_mfma_f32_16x16x32_bf16 v[60:63], v[4:7], a[0:3], 0\n"
    "v_permlane16_swap_b32 v50, v51\n"
    "v_mfma_f32_16x16x32_bf16 v[56:59], v[8:11], a[4:7], v[56:59]\n"
    "v_min_u32 v48, -2, v48\n"
    "v_min_u32 v50, -2, v50\n"
    "v_mfma_f32_16x16x32_bf16 v[60:63], v[12:15], a[4:7], v[60:63]\n"
    "buffer_store_dwordx4 v[48:51], v102, s[0:3], s36 offen sc1\n"
    "v_add_f32 v64, v64, v66\n"
    "v_mfma_f32_16x16x32_bf16 v[56:59], v[16:19], a[8:11], v[56:59]\n"
    "v_add_f32 v65, v65, v67\n"
    "v_cvt_pk_bf16_f32 v64, v64, v65\n"
    "v_mfma_f32_16x16x32_bf16 v[60:63], v[20:23], a[8:11], v[60:63]\n"
    "v_add_f32 v68, v68, v70\n"
    "v_add_f32 v69, v69, v71\n"
    "v_mfma_f32_16x16x32_bf16 v[56:59], v[24:27], a[12:15], v[56:59]\n"
    "v_cvt_pk_bf16_f32 v65, v68, v69\n"
    "v_add_f32 v84, v84, v86\n"
    "s_waitcnt vmcnt(8)\n"
    "v_swap_b32 v34, v36\n"
    "v_swap_b32 v35, v37\n"
    "v_swap_b32 v42, v44\n"
    "v_swap_b32 v43, v45\n"
    "v_mfma_f32_16x16x32_bf16 v[60:63], v[28:31], a[12:15], v[60:63]\n"
    "v_add_f32 v85, v85, v87\n"
    "v_cvt_pk_bf16_f32 v66, v84, v85\n"
    "v_mfma_f32_16x16x32_bf16 v[56:59], v[32:35], a[16:19], v[56:59]\n"
    "s_add_u32 m0, s48, 39936\n"
    "s_add_u32 s43, s43, 0x4000\n"
    "v_mfma_f32_16x16x32_bf16 v[60:63], v[36:39], a[16:19], v[60:63]\n"
    "v_permlane32_swap_b32 v64, v66\n"
    "v_mfma_f32_16x16x32_bf16 v[56:59], v[40:43], a[20:23], v[56:59]\n"
    "v_add_f32 v88, v88, v90\n"
    "v_add_f32 v89, v89, v91\n"
    "v_mfma_f32_16x16x32_bf16 v[60:63], v[44:47], a[20:23], v[60:63]\n"
    "v_cvt_pk_bf16_f32 v67, v88, v89\n"
    "buffer_load_dwordx4 v96, s[0:3], s43 offen sc1 lds\n"
    "v_mfma_f32_16x16x32_bf16 v[48:51], v[0:3], a[24:27], 0\n"
    "v_mfma_f32_16x16x32_bf16 v[52:55], v[4:7], a[24:27], 0\n"
    "v_permlane32_swap_b32 v65, v67\n"
    "v_mfma_f32_16x16x32_bf16 v[48:51], v[8:11], a[28:31], v[48:51]\n"
    "v_cvt_pk_bf16_f32 v72, v56, v57\n"
    "v_cvt_pk_bf16_f32 v74, v58, v59\n"
    "v_mfma_f32_16x16x32_bf16 v[52:55], v[12:15], a[28:31], v[52:55]\n"
    "v_permlane16_swap_b32 v64, v65\n"
    "v_mfma_f32_16x16x32_bf16 v[48:51], v[16:19], a[32:35], v[48:51]\n"
    "v_permlane16_swap_b32 v66, v67\n"
    "v_mfma_f32_16x16x32_bf16 v[52:55], v[20:23], a[32:35], v[52:55]\n"
    "v_min_u32 v64, -2, v64\n"
    "v_min_u32 v66, -2, v66\n"
    "v_mfma_f32_16x16x32_bf16 v[48:51], v[24:27], a[36:39], v[48:51]\n"
    "buffer_store_dwordx4 v[64:67], v102, s[0:3], s37 offen sc1\n"
    "v_cvt_pk_bf16_f32 v73, v60, v61\n"
    "v_mfma_f32_16x16x32_bf16 v[52:55], v[28:31], a[36:39], v[52:55]\n"
    "s_cmp_eq_u32 s34, 63\n"
    "s_cbranch_scc1 .Lvo_end_%=\n"
    "v_mfma_f32_16x16x32_bf16 v[48:51], v[32:35], a[40:43], v[48:51]\n"
    "v_cvt_pk_bf16_f32 v75, v62, v63\n"
    "v_mfma_f32_16x16x32_bf16 v[52:55], v[36:39], a[40:43], v[52:55]\n"
    "v_mfma_f32_16x16x32_bf16 v[48:51], v[40:43], a[44:47], v[48:51]\n"
    "v_mfma_f32_16x16x32_bf16 v[52:55], v[44:47], a[44:47], v[52:55]\n"
    "v_mfma_f32_16x16x32_bf16 v[56:59], v[0:3], a[48:51], 0\n"
    "v_mfma_f32_16x16x32_bf16 v[60:63], v[4:7], a[48:51], 0\n"
    "v_mfma_f32_16x16x32_bf16 v[56:59], v[8:11], a[52:55], v[56:59]\n"
    "buffer_load_dwordx4 v96, s[0:3], s43 offen offset:1024 sc1 lds\n"
    "s_add_u32 m0, s48, 53248\n"
    "v_mfma_f32_16x16x32_bf16 v[60:63], v[12:15], a[52:55], v[60:63]\n"
    "s_add_u32 s43, s43, 0x4000\n"
    "v_mfma_f32_16x16x32_bf16 v[56:59], v[16:19], a[56:59], v[56:59]\n"
    "v_mfma_f32_16x16x32_bf16 v[60:63], v[20:23], a[56:59], v[60:63]\n"
    "v_mfma_f32_16x16x32_bf16 v[56:59], v[24:27], a[60:63], v[56:59]\n"
    "v_cvt_pk_bf16_f32 v76, v48, v49\n"
    "v_cvt_pk_bf16_f32 v78, v50, v51\n"
    "v_mfma_f32_16x16x32_bf16 v[60:63], v[28:31], a[60:63], v[60:63]\n"
    "v_cvt_pk_bf16_f32 v77, v52, v53\n"
    "v_cvt_pk_bf16_f32 v79, v54, v55\n"
    "v_mfma_f32_16x16x32_bf16 v[56:59], v[32:35], a[64:67], v[56:59]\n"
    "buffer_load_dwordx4 v96, s[0:3], s43 offen sc1 lds\n"
    "v_mov_b64 v[88:89], -1\n"
    "v_mfma_f32_16x16x32_bf16 v[60:63], v[36:39], a[64:67], v[60:63]\n"
    "v_mov_b64 v[90:91], -1\n"
    "v_mfma_f32_16x16x32_bf16 v[56:59], v[40:43], a[68:71], v[56:59]\n"
    "v_mfma_f32_16x16x32_bf16 v[60:63], v[44:47], a[68:71], v[60:63]\n"
    "v_mfma_f32_16x16x32_bf16 v[48:51], v[0:3], a[72:75], 0\n"
    "buffer_load_dwordx4 v96, s[0:3], s43 offen offset:1024 sc1 lds\n"
    "v_mfma_f32_16x16x32_bf16 v[52:55], v[4:7], a[72:75], 0\n"
    "v_mfma_f32_16x16x32_bf16 v[48:51], v[8:11], a[76:79], v[48:51]\n"
    "v_mfma_f32_16x16x32_bf16 v[52:55], v[12:15], a[76:79], v[52:55]\n"
    "v_mfma_f32_16x16x32_bf16 v[48:51], v[16:19], a[80:83], v[48:51]\n"
    "buffer_store_dwordx4 v[72:75], v96, s[0:3], s40 offen sc1\n"
    "s_add_u32 s40, s40, 0x10000\n"
    "v_mfma_f32_16x16x32_bf16 v[52:55], v[20:23], a[80:83], v[52:55]\n"
    "v_mfma_f32_16x16x32_bf16 v[48:51], v[24:27], a[84:87], v[48:51]\n"
    "v_cvt_pk_bf16_f32 v80, v56, v57\n"
    "v_cvt_pk_bf16_f32 v82, v58, v59\n"
    "v_mfma_f32_16x16x32_bf16 v[52:55], v[28:31], a[84:87], v[52:55]\n"
    "v_cvt_pk_bf16_f32 v81, v60, v61\n"
    "v_cvt_pk_bf16_f32 v83, v62, v63\n"
    "v_mfma_f32_16x16x32_bf16 v[48:51], v[32:35], a[88:91], v[48:51]\n"
    "v_mfma_f32_16x16x32_bf16 v[52:55], v[36:39], a[88:91], v[52:55]\n"
    "v_mfma_f32_16x16x32_bf16 v[48:51], v[40:43], a[92:95], v[48:51]\n"
    "v_mfma_f32_16x16x32_bf16 v[52:55], v[44:47], a[92:95], v[52:55]\n"
    "v_mfma_f32_16x16x32_bf16 v[56:59], v[0:3], a[96:99], 0\n"
    "buffer_store_dwordx4 v[76:79], v96, s[0:3], s40 offen sc1\n"
    "s_add_u32 s40, s40, 0x10000\n"
    "v_mfma_f32_16x16x32_bf16 v[60:63], v[4:7], a[96:99], 0\n"
    "v_mfma_f32_16x16x32_bf16 v[56:59], v[8:11], a[100:103], v[56:59]\n"
    "v_mfma_f32_16x16x32_bf16 v[60:63], v[12:15], a[100:103], v[60:63]\n"
    "v_mfma_f32_16x16x32_bf16 v[56:59], v[16:19], a[104:107], v[56:59]\n"
    "v_mfma_f32_16x16x32_bf16 v[60:63], v[20:23], a[104:107], v[60:63]\n"
    "v_mfma_f32_16x16x32_bf16 v[56:59], v[24:27], a[108:111], v[56:59]\n"
    "v_cvt_pk_bf16_f32 v72, v48, v49\n"
    "v_cvt_pk_bf16_f32 v74, v50, v51\n"
    "v_mfma_f32_16x16x32_bf16 v[60:63], v[28:31], a[108:111], v[60:63]\n"
    "v_cvt_pk_bf16_f32 v73, v52, v53\n"
    "v_cvt_pk_bf16_f32 v75, v54, v55\n"
    "v_mfma_f32_16x16x32_bf16 v[56:59], v[32:35], a[112:115], v[56:59]\n"
    "buffer_store_dwordx4 v[80:83], v96, s[0:3], s40 offen sc1\n"
    "s_add_u32 s40, s40, 0x10000\n"
    "v_mfma_f32_16x16x32_bf16 v[60:63], v[36:39], a[112:115], v[60:63]\n"
    "v_mfma_f32_16x16x32_bf16 v[56:59], v[40:43], a[116:119], v[56:59]\n"
    "v_mfma_f32_16x16x32_bf16 v[60:63], v[44:47], a[116:119], v[60:63]\n"
    "v_mfma_f32_16x16x32_bf16 v[48:51], v[0:3], a[120:123], 0\n"
    "v_mfma_f32_16x16x32_bf16 v[52:55], v[4:7], a[120:123], 0\n"
    "v_mfma_f32_16x16x32_bf16 v[48:51], v[8:11], a[124:127], v[48:51]\n"
    "v_mfma_f32_16x16x32_bf16 v[52:55], v[12:15], a[124:127], v[52:55]\n"
    "v_mfma_f32_16x16x32_bf16 v[48:51], v[16:19], a[128:131], v[48:51]\n"
    "buffer_store_dwordx4 v[72:75], v96, s[0:3], s40 offen sc1\n"
    "s_add_u32 s40, s40, 0x10000\n"
    "v_mfma_f32_16x16x32_bf16 v[52:55], v[20:23], a[128:131], v[52:55]\n"
    "v_mfma_f32_16x16x32_bf16 v[48:51], v[24:27], a[132:135], v[48:51]\n"
    "v_cvt_pk_bf16_f32 v76, v56, v57\n"
    "v_cvt_pk_bf16_f32 v78, v58, v59\n"
    "v_mfma_f32_16x16x32_bf16 v[52:55], v[28:31], a[132:135], v[52:55]\n"
    "v_cvt_pk_bf16_f32 v77, v60, v61\n"
    "v_cvt_pk_bf16_f32 v79, v62, v63\n"
    "v_mfma_f32_16x16x32_bf16 v[48:51], v[32:35], a[136:139], v[48:51]\n"
    "v_mfma_f32_16x16x32_bf16 v[52:55], v[36:39], a[136:139], v[52:55]\n"
    "v_mfma_f32_16x16x32_bf16 v[48:51], v[40:43], a[140:143], v[48:51]\n"
    "v_mfma_f32_16x16x32_bf16 v[52:55], v[44:47], a[140:143], v[52:55]\n"
    "v_mfma_f32_16x16x32_bf16 v[56:59], v[0:3], a[144:147], 0\n"
    "buffer_store_dwordx4 v[76:79], v96, s[0:3], s40 offen sc1\n"
    "s_add_u32 s40, s40, 0x10000\n"
    "v_mfma_f32_16x16x32_bf16 v[60:63], v[4:7], a[144:147], 0\n"
    "v_mfma_f32_16x16x32_bf16 v[56:59], v[8:11], a[148:151], v[56:59]\n"
    "v_mfma_f32_16x16x32_bf16 v[60:63], v[12:15], a[148:151], v[60:63]\n"
    "v_mfma_f32_16x16x32_bf16 v[56:59], v[16:19], a[152:155], v[56:59]\n"
    "v_mfma_f32_16x16x32_bf16 v[60:63], v[20:23], a[152:155], v[60:63]\n"
    "v_mfma_f32_16x16x32_bf16 v[56:59], v[24:27], a[156:159], v[56:59]\n"
    "v_cvt_pk_bf16_f32 v80, v48, v49\n"
    "v_cvt_pk_bf16_f32 v82, v50, v51\n"
    "v_mfma_f32_16x16x32_bf16 v[60:63], v[28:31], a[156:159], v[60:63]\n"
    "v_cvt_pk_bf16_f32 v81, v52, v53\n"
    "v_cvt_pk_bf16_f32 v83, v54, v55\n"
    "v_mfma_f32_16x16x32_bf16 v[56:59], v[32:35], a[160:163], v[56:59]\n"
    "buffer_store_dwordx4 v[80:83], v96, s[0:3], s40 offen sc1\n"
    "s_add_u32 s40, s40, 0x10000\n"
    "v_mfma_f32_16x16x32_bf16 v[60:63], v[36:39], a[160:163], v[60:63]\n"
    "v_mfma_f32_16x16x32_bf16 v[56:59], v[40:43], a[164:167], v[56:59]\n"
    "v_mfma_f32_16x16x32_bf16 v[60:63], v[44:47], a[164:167], v[60:63]\n"
    "v_mfma_f32_16x16x32_bf16 v[48:51], v[0:3], a[168:171], 0\n"
    "v_mfma_f32_16x16x32_bf16 v[52:55], v[4:7], a[168:171], 0\n"
    "v_mfma_f32_16x16x32_bf16 v[48:51], v[8:11], a[172:175], v[48:51]\n"
    "v_mfma_f32_16x16x32_bf16 v[52:55], v[12:15], a[172:175], v[52:55]\n"
    "v_mfma_f32_16x16x32_bf16 v[48:51], v[16:19], a[176:179], v[48:51]\n"
    "buffer_store_dwordx4 v[88:91], v102, s[0:3], s38 offen sc1\n"
    "v_mfma_f32_16x16x32_bf16 v[52:55], v[20:23], a[176:179], v[52:55]\n"
    "v_mfma_f32_16x16x32_bf16 v[48:51], v[24:27], a[180:183], v[48:51]\n"
    "v_cvt_pk_bf16_f32 v72, v56, v57\n"
    "v_cvt_pk_bf16_f32 v74, v58, v59\n"
    "v_mfma_f32_16x16x32_bf16 v[52:55], v[28:31], a[180:183], v[52:55]\n"
    "v_cvt_pk_bf16_f32 v73, v60, v61\n"
    "v_cvt_pk_bf16_f32 v75, v62, v63\n"
    "v_mfma_f32_16x16x32_bf16 v[48:51], v[32:35], a[184:187], v[48:51]\n"
    "v_mfma_f32_16x16x32_bf16 v[52:55], v[36:39], a[184:187], v[52:55]\n"
    "v_mfma_f32_16x16x32_bf16 v[48:51], v[40:43], a[188:191], v[48:51]\n"
    "v_mfma_f32_16x16x32_bf16 v[52:55], v[44:47], a[188:191], v[52:55]\n"
    "v_mfma_f32_16x16x32_bf16 v[56:59], v[0:3], a[192:195], 0\n"
    "buffer_store_dwordx4 v[72:75], v96, s[0:3], s40 offen sc1\n"
    "s_add_u32 s40, s40, 0x10000\n"
    "v_mfma_f32_16x16x32_bf16 v[60:63], v[4:7], a[192:195], 0\n"
    "v_mfma_f32_16x16x32_bf16 v[56:59], v[8:11], a[196:199], v[56:59]\n"
    "v_mfma_f32_16x16x32_bf16 v[60:63], v[12:15], a[196:199], v[60:63]\n"
    "v_mfma_f32_16x16x32_bf16 v[56:59], v[16:19], a[200:203], v[56:59]\n"
    "v_mfma_f32_16x16x32_bf16 v[60:63], v[20:23], a[200:203], v[60:63]\n"
    "v_mfma_f32_16x16x32_bf16 v[56:59], v[24:27], a[204:207], v[56:59]\n"
    "v_cvt_pk_bf16_f32 v76, v48, v49\n"
    "v_cvt_pk_bf16_f32 v78, v50, v51\n"
    "v_mfma_f32_16x16x32_bf16 v[60:63], v[28:31], a[204:207], v[60:63]\n"
    "v_cvt_pk_bf16_f32 v77, v52, v53\n"
    "v_cvt_pk_bf16_f32 v79, v54, v55\n"
    "v_mfma_f32_16x16x32_bf16 v[56:59], v[32:35], a[208:211], v[56:59]\n"
    "buffer_store_dwordx4 v[76:79], v96, s[0:3], s40 offen sc1\n"
    "s_add_u32 s40, s40, 0x10000\n"
    "v_mfma_f32_16x16x32_bf16 v[60:63], v[36:39], a[208:211], v[60:63]\n"
    "v_mfma_f32_16x16x32_bf16 v[56:59], v[40:43], a[212:215], v[56:59]\n"
    "v_mfma_f32_16x16x32_bf16 v[60:63], v[44:47], a[212:215], v[60:63]\n"
    "v_mfma_f32_16x16x32_bf16 v[48:51], v[0:3], a[216:219], 0\n"
    "v_mfma_f32_16x16x32_bf16 v[52:55], v[4:7], a[216:219], 0\n"
    "v_mfma_f32_16x16x32_bf16 v[48:51], v[8:11], a[220:223], v[48:51]\n"
    "v_mfma_f32_16x16x32_bf16 v[52:55], v[12:15], a[220:223], v[52:55]\n"
    "v_mfma_f32_16x16x32_bf16 v[48:51], v[16:19], a[224:227], v[48:51]\n"
    "buffer_store_dwordx4 v[88:91], v102, s[0:3], s39 offen sc1\n"
    "v_mfma_f32_16x16x32_bf16 v[52:55], v[20:23], a[224:227], v[52:55]\n"
    "v_mfma_f32_16x16x32_bf16 v[48:51], v[24:27], a[228:231], v[48:51]\n"
    "v_cvt_pk_bf16_f32 v80, v56, v57\n"
    "v_cvt_pk_bf16_f32 v82, v58, v59\n"
    "v_mfma_f32_16x16x32_bf16 v[52:55], v[28:31], a[228:231], v[52:55]\n"
    "v_cvt_pk_bf16_f32 v81, v60, v61\n"
    "v_cvt_pk_bf16_f32 v83, v62, v63\n"
    "v_mfma_f32_16x16x32_bf16 v[48:51], v[32:35], a[232:235], v[48:51]\n"
    "v_mfma_f32_16x16x32_bf16 v[52:55], v[36:39], a[232:235], v[52:55]\n"
    "v_mfma_f32_16x16x32_bf16 v[48:51], v[40:43], a[236:239], v[48:51]\n"
    "v_mfma_f32_16x16x32_bf16 v[52:55], v[44:47], a[236:239], v[52:55]\n"
    "s_nop 6\n"
    "v_cvt_pk_bf16_f32 v72, v48, v49\n"
    "v_cvt_pk_bf16_f32 v74, v50, v51\n"
    "v_cvt_pk_bf16_f32 v73, v52, v53\n"
    "v_cvt_pk_bf16_f32 v75, v54, v55\n"
    "buffer_store_dwordx4 v[80:83], v104, s[0:3], s38 offen sc1\n"
    ".Lvo_vph_%=:\n"
    "s_waitcnt vmcnt(11) lgkmcnt(0)\n"
    "s_barrier\n"
    "ds_read_b128 v[24:27], v92 offset:0\n"
    "ds_read_b128 v[28:31], v92 offset:1024\n"
    "ds_read_b128 v[32:35], v92 offset:2048\n"
    "ds_read_b128 v[36:39], v92 offset:3072\n"
    "ds_read_b128 v[40:43], v92 offset:4096\n"
    "ds_read_b128 v[44:47], v92 offset:5120\n"
    "s_waitcnt lgkmcnt(4)\n"
    "v_mfma_f32_16x16x32_bf16 v[48:51], a[240:243], v[24:27], 0\n"
    "buffer_store_dwordx4 v[72:75], v104, s[0:3], s39 offen sc1\n"
    "v_mfma_f32_16x16x32_bf16 v[52:55], a[240:243], v[28:31], 0\n"
    "s_add_u32 s34, s34, 1\n"
    "s_add_u32 s36, s34, 0\n"
    "s_and_b32 s36, s36, 3\n"
    "v_mfma_f32_16x16x32_bf16 v[56:59], v[176:179], v[24:27], 0\n"
    "ds_read_b128 v[24:27], v92 offset:6144\n"
    "s_lshl_b32 s36, s36, 22\n"
    "v_mfma_f32_16x16x32_bf16 v[60:63], v[176:179], v[28:31], 0\n"
    "ds_read_b128 v[28:31], v92 offset:7168\n"
    "s_waitcnt lgkmcnt(4)\n"
    "v_mfma_f32_16x16x32_bf16 v[48:51], a[244:247], v[32:35], v[48:51]\n"
    "s_add_u32 s41, s15, s36\n"
    "s_add_u32 s42, s16, s36\n"
    "s_add_u32 s37, s36, 0x10000\n"
    "v_mfma_f32_16x16x32_bf16 v[52:55], a[244:247], v[36:39], v[52:55]\n"
    "s_add_u32 s50, s34, 3\n"
    "s_cmp_gt_i32 s50, 64\n"
    "s_cselect_b32 s50, 0, s50\n"
    "v_mfma_f32_16x16x32_bf16 v[56:59], v[180:183], v[32:35], v[56:59]\n"
    "ds_read_b128 v[32:35], v93 offset:0\n"
    "s_add_u32 s51, s34, 4\n"
    "v_mfma_f32_16x16x32_bf16 v[60:63], v[180:183], v[36:39], v[60:63]\n"
    "ds_read_b128 v[36:39], v93 offset:1024\n"
    "s_waitcnt lgkmcnt(4)\n"
    "v_mfma_f32_16x16x32_bf16 v[48:51], a[248:251], v[40:43], v[48:51]\n"
    "buffer_load_dwordx2 v[108:109], off, s[0:3], s45 sc1\n"
    "v_mfma_f32_16x16x32_bf16 v[52:55], a[248:251], v[44:47], v[52:55]\n"
    "s_cmp_gt_i32 s51, 64\n"
    "s_cselect_b32 s51, 0, s51\n"
    "s_mul_i32 s51, s51, s14\n"
    "v_mfma_f32_16x16x32_bf16 v[56:59], v[184:187], v[40:43], v[56:59]\n"
    "ds_read_b128 v[40:43], v93 offset:2048\n"
    "v_mov_b64 v[64:65], 0\n"
    "v_mfma_f32_16x16x32_bf16 v[60:63], v[184:187], v[44:47], v[60:63]\n"
    "ds_read_b128 v[44:47], v93 offset:3072\n"
    "s_waitcnt lgkmcnt(4)\n"
    "v_mfma_f32_16x16x32_bf16 v[48:51], a[252:255], v[24:27], v[48:51]\n"
    "buffer_store_dwordx2 v[88:89], v103, s[0:3], s38 offen sc1\n"
    "v_mov_b64 v[66:67], 0\n"
    "v_mfma_f32_16x16x32_bf16 v[52:55], a[252:255], v[28:31], v[52:55]\n"
    "v_mov_b64 v[68:69], 0\n"
    "v_mov_b64 v[70:71], 0\n"
    "v_mfma_f32_16x16x32_bf16 v[56:59], v[188:191], v[24:27], v[56:59]\n"
    "ds_read_b128 v[24:27], v93 offset:4096\n"
    "v_mov_b64 v[84:85], 0\n"
    "v_mfma_f32_16x16x32_bf16 v[60:63], v[188:191], v[28:31], v[60:63]\n"
    "ds_read_b128 v[28:31], v93 offset:5120\n"
    "s_waitcnt lgkmcnt(4)\n"
    "v_mfma_f32_16x16x32_bf16 v[48:51], v[112:115], v[32:35], v[48:51]\n"
    "v_mov_b64 v[86:87], 0\n"
    "v_mfma_f32_16x16x32_bf16 v[52:55], v[112:115], v[36:39], v[52:55]\n"
    "v_mfma_f32_16x16x32_bf16 v[56:59], v[192:195], v[32:35], v[56:59]\n"
    "ds_read_b128 v[32:35], v93 offset:6144\n"
    "v_mfma_f32_16x16x32_bf16 v[60:63], v[192:195], v[36:39], v[60:63]\n"
    "ds_read_b128 v[36:39], v93 offset:7168\n"
    "s_waitcnt lgkmcnt(4)\n"
    "v_mfma_f32_16x16x32_bf16 v[48:51], v[116:119], v[40:43], v[48:51]\n"
    "v_mfma_f32_16x16x32_bf16 v[52:55], v[116:119], v[44:47], v[52:55]\n"
    "v_mfma_f32_16x16x32_bf16 v[56:59], v[196:199], v[40:43], v[56:59]\n"
    "ds_read_b128 v[40:43], v94 offset:0\n"
    "v_mfma_f32_16x16x32_bf16 v[60:63], v[196:199], v[44:47], v[60:63]\n"
    "ds_read_b128 v[44:47], v94 offset:1024\n"
    "s_waitcnt lgkmcnt(4)\n"
    "v_mfma_f32_16x16x32_bf16 v[48:51], v[120:123], v[24:27], v[48:51]\n"
    "v_mfma_f32_16x16x32_bf16 v[52:55], v[120:123], v[28:31], v[52:55]\n"
    "v_mfma_f32_16x16x32_bf16 v[56:59], v[200:203], v[24:27], v[56:59]\n"
    "ds_read_b128 v[24:27], v94 offset:2048\n"
    "v_mfma_f32_16x16x32_bf16 v[60:63], v[200:203], v[28:31], v[60:63]\n"
    "ds_read_b128 v[28:31], v94 offset:3072\n"
    "s_waitcnt lgkmcnt(4)\n"
    "v_mfma_f32_16x16x32_bf16 v[48:51], v[124:127], v[32:35], v[48:51]\n"
    "v_mfma_f32_16x16x32_bf16 v[52:55], v[124:127], v[36:39], v[52:55]\n"
    "v_mfma_f32_16x16x32_bf16 v[56:59], v[204:207], v[32:35], v[56:59]\n"
    "ds_read_b128 v[32:35], v94 offset:4096\n"
    "v_mfma_f32_16x16x32_bf16 v[60:63], v[204:207], v[36:39], v[60:63]\n"
    "ds_read_b128 v[36:39], v94 offset:5120\n"
    "s_waitcnt lgkmcnt(4)\n"
    "v_mfma_f32_16x16x32_bf16 v[48:51], v[128:131], v[40:43], v[48:51]\n"
    "v_mfma_f32_16x16x32_bf16 v[52:55], v[128:131], v[44:47], v[52:55]\n"
    "v_mfma_f32_16x16x32_bf16 v[56:59], v[208:211], v[40:43], v[56:59]\n"
    "ds_read_b128 v[40:43], v94 offset:6144\n"
    "v_mfma_f32_16x16x32_bf16 v[60:63], v[208:211], v[44:47], v[60:63]\n"
    "ds_read_b128 v[44:47], v94 offset:7168\n"
    "s_waitcnt lgkmcnt(4)\n"
    "v_mfma_f32_16x16x32_bf16 v[48:51], v[132:135], v[24:27], v[48:51]\n"
    "s_waitcnt vmcnt(1)\n"
    "v_readfirstlane_b32 s32, v108\n"
    "v_mfma_f32_16x16x32_bf16 v[52:55], v[132:135], v[28:31], v[52:55]\n"
    "v_readfirstlane_b32 s33, v109\n"
    "v_mfma_f32_16x16x32_bf16 v[56:59], v[212:215], v[24:27], v[56:59]\n"
    "ds_read_b128 v[24:27], v95 offset:0\n"
    "v_mfma_f32_16x16x32_bf16 v[60:63], v[212:215], v[28:31], v[60:63]\n"
    "ds_read_b128 v[28:31], v95 offset:1024\n"
    "s_waitcnt lgkmcnt(4)\n"
    "v_mfma_f32_16x16x32_bf16 v[48:51], v[136:139], v[32:35], v[48:51]\n"
    "s_cmp_eq_u32 s33, s35\n"
    "s_cselect_b32 s46, s32, s46\n"
    "s_add_u32 s46, s46, 304\n"
    "v_mfma_f32_16x16x32_bf16 v[52:55], v[136:139], v[36:39], v[52:55]\n"
    "s_sub_u32 s47, s46, 63\n"
    "v_mfma_f32_16x16x32_bf16 v[56:59], v[216:219], v[32:35], v[56:59]\n"
    "ds_read_b128 v[32:35], v95 offset:2048\n"
    "v_mfma_f32_16x16x32_bf16 v[60:63], v[216:219], v[36:39], v[60:63]\n"
    "ds_read_b128 v[36:39], v95 offset:3072\n"
    "s_waitcnt lgkmcnt(4)\n"
    ".Lvo_rt7_%=:\n"
    "s_memrealtime s[30:31]\n"
    "s_waitcnt lgkmcnt(0)\n"
    "s_sub_u32 s32, s47, s30\n"
    "s_cmp_lt_u32 s32, 600\n"
    "s_cbranch_scc1 .Lvo_rt7_%=\n"
    "buffer_load_dwordx4 v[72:75], v96, s[0:3], s41 offen sc1\n"
    "buffer_load_dwordx4 v[76:79], v96, s[0:3], s41 offen offset:1024 sc1\n"
    "buffer_load_dwordx4 v[80:83], v96, s[0:3], s42 offen sc1\n"
    "v_mfma_f32_16x16x32_bf16 v[48:51], v[140:143], v[40:43], v[48:51]\n"
    "s_add_u32 s49, s34, 3\n"
    "s_lshl_b32 s43, s49, 19\n"
    "s_lshl_b32 s33, s49, 11\n"
    "v_mfma_f32_16x16x32_bf16 v[52:55], v[140:143], v[44:47], v[52:55]\n"
    "s_xor_b32 s33, s33, s13\n"
    "s_and_b32 s33, s33, 0x2000\n"
    "s_add_u32 s43, s43, s33\n"
    "v_mfma_f32_16x16x32_bf16 v[56:59], v[220:223], v[40:43], v[56:59]\n"
    "ds_read_b128 v[40:43], v95 offset:4096\n"
    "s_add_u32 s43, s43, s17\n"
    "v_mfma_f32_16x16x32_bf16 v[60:63], v[220:223], v[44:47], v[60:63]\n"
    "ds_read_b128 v[44:47], v95 offset:5120\n"
    "s_waitcnt lgkmcnt(4)\n"
    "v_mfma_f32_16x16x32_bf16 v[48:51], v[144:147], v[24:27], v[48:51]\n"
    "s_add_u32 s49, s34, 2\n"
    "s_lshl_b32 s44, s49, 20\n"
    "s_lshl_b32 s33, s49, 12\n"
    "v_mfma_f32_16x16x32_bf16 v[52:55], v[144:147], v[28:31], v[52:55]\n"
    "s_xor_b32 s33, s33, s13\n"
    "s_and_b32 s33, s33, 0x2000\n"
    "s_add_u32 s44, s44, s33\n"
    "v_mfma_f32_16x16x32_bf16 v[56:59], v[224:227], v[24:27], v[56:59]\n"
    "ds_read_b128 v[24:27], v95 offset:6144\n"
    "s_add_u32 s44, s44, s18\n"
    "v_mfma_f32_16x16x32_bf16 v[60:63], v[224:227], v[28:31], v[60:63]\n"
    "ds_read_b128 v[28:31], v95 offset:7168\n"
    "s_waitcnt lgkmcnt(4)\n"
    "v_mfma_f32_16x16x32_bf16 v[48:51], v[148:151], v[32:35], v[48:51]\n"
    "s_add_u32 s38, s34, 2\n"
    "s_and_b32 s38, s38, 3\n"
    "s_lshl_b32 s38, s38, 22\n"
    "v_mfma_f32_16x16x32_bf16 v[52:55], v[148:151], v[36:39], v[52:55]\n"
    "s_add_u32 s39, s38, 0x10000\n"
    "s_add_u32 s40, s19, s38\n"
    "s_and_b32 s45, s34, 63\n"
    "v_mfma_f32_16x16x32_bf16 v[56:59], v[228:231], v[32:35], v[56:59]\n"
    "ds_read_b128 v[32:35], v96 offset:53248\n"
    "s_lshl_b32 s45, s45, 7\n"
    "v_mfma_f32_16x16x32_bf16 v[60:63], v[228:231], v[36:39], v[60:63]\n"
    "ds_read_b128 v[36:39], v96 offset:54272\n"
    "s_waitcnt lgkmcnt(4)\n"
    "v_mfma_f32_16x16x32_bf16 v[48:51], v[152:155], v[40:43], v[48:51]\n"
    "s_add_u32 s45, s20, s45\n"
    "s_add_u32 s35, s34, 1\n"
    "v_mfma_f32_16x16x32_bf16 v[52:55], v[152:155], v[44:47], v[52:55]\n"
    "s_cmp_lt_i32 s34, 0\n"
    "s_cselect_b32 s35, -5, s35\n"
    "v_mfma_f32_16x16x32_bf16 v[56:59], v[232:235], v[40:43], v[56:59]\n"
    "ds_read_b128 v[40:43], v96 offset:55296\n"
    "buffer_load_dword v110, v106, s[0:3], s23 offen sc1\n"
    "v_mfma_f32_16x16x32_bf16 v[60:63], v[232:235], v[44:47], v[60:63]\n"
    "ds_read_b128 v[44:47], v96 offset:56320\n"
    "s_waitcnt lgkmcnt(4)\n"
    "v_mfma_f32_16x16x32_bf16 v[48:51], v[156:159], v[24:27], v[48:51]\n"
    "ds_read_b128 v[0:3], v97 offset:0\n"
    "v_mfma_f32_16x16x32_bf16 v[52:55], v[156:159], v[28:31], v[52:55]\n"
    "ds_read_b128 v[4:7], v97 offset:1024\n"
    "v_mfma_f32_16x16x32_bf16 v[56:59], v[236:239], v[24:27], v[56:59]\n"
    "ds_read_b128 v[24:27], v96 offset:57344\n"
    "v_mfma_f32_16x16x32_bf16 v[60:63], v[236:239], v[28:31], v[60:63]\n"
    "ds_read_b128 v[28:31], v96 offset:58368\n"
    "s_waitcnt lgkmcnt(6)\n"
    "v_mfma_f32_16x16x32_bf16 v[48:51], v[160:163], v[32:35], v[48:51]\n"
    "buffer_load_dword v111, v105, s[0:3], s22 offen sc1\n"
    "ds_read_b128 v[8:11], v97 offset:2048\n"
    "v_mfma_f32_16x16x32_bf16 v[52:55], v[160:163], v[36:39], v[52:55]\n"
    "ds_read_b128 v[12:15], v97 offset:3072\n"
    "v_mfma_f32_16x16x32_bf16 v[56:59], v[240:243], v[32:35], v[56:59]\n"
    "ds_read_b128 v[32:35], v96 offset:59392\n"
    "v_mfma_f32_16x16x32_bf16 v[60:63], v[240:243], v[36:39], v[60:63]\n"
    "ds_read_b128 v[36:39], v96 offset:60416\n"
    "s_waitcnt lgkmcnt(8)\n"
    "v_mfma_f32_16x16x32_bf16 v[48:51], v[164:167], v[40:43], v[48:51]\n"
    "ds_read_b128 v[16:19], v97 offset:4096\n"
    "v_mfma_f32_16x16x32_bf16 v[52:55], v[164:167], v[44:47], v[52:55]\n"
    "ds_read_b128 v[20:23], v97 offset:5120\n"
    "v_mfma_f32_16x16x32_bf16 v[56:59], v[244:247], v[40:43], v[56:59]\n"
    "v_mfma_f32_16x16x32_bf16 v[60:63], v[244:247], v[44:47], v[60:63]\n"
    "s_waitcnt lgkmcnt(6)\n"
    "v_mfma_f32_16x16x32_bf16 v[48:51], v[168:171], v[24:27], v[48:51]\n"
    "v_mfma_f32_16x16x32_bf16 v[52:55], v[168:171], v[28:31], v[52:55]\n"
    "v_mfma_f32_16x16x32_bf16 v[56:59], v[248:251], v[24:27], v[56:59]\n"
    "v_mfma_f32_16x16x32_bf16 v[60:63], v[248:251], v[28:31], v[60:63]\n"
    "s_waitcnt lgkmcnt(2)\n"
    "v_mfma_f32_16x16x32_bf16 v[48:51], v[172:175], v[32:35], v[48:51]\n"
    "v_mfma_f32_16x16x32_bf16 v[52:55], v[172:175], v[36:39], v[52:55]\n"
    "v_mfma_f32_16x16x32_bf16 v[56:59], v[252:255], v[32:35], v[56:59]\n"
    "v_mfma_f32_16x16x32_bf16 v[60:63], v[252:255], v[36:39], v[60:63]\n"
    "ds_read_b128 v[24:27], v97 offset:6144\n"
    "ds_read_b128 v[28:31], v97 offset:7168\n"
    "s_nop 2\n"
    "v_cvt_pk_bf16_f32 v32, v48, v49\n"
    "v_cvt_pk_bf16_f32 v33, v50, v51\n"
    "v_cvt_pk_bf16_f32 v34, v56, v57\n"
    "v_cvt_pk_bf16_f32 v35, v58, v59\n"
    "ds_write_b128 v92, v[32:35] offset:11264\n"
    "v_cvt_pk_bf16_f32 v36, v52, v53\n"
    "v_cvt_pk_bf16_f32 v37, v54, v55\n"
    "v_cvt_pk_bf16_f32 v38, v60, v61\n"
    "v_cvt_pk_bf16_f32 v39, v62, v63\n"
    "ds_write_b128 v92, v[36:39] offset:12288\n"
    "ds_read_b128 v[48:51], v97 offset:8192\n"
    "ds_read_b128 v[52:55], v97 offset:9216\n"
    "ds_read_b128 v[56:59], v97 offset:10240\n"
    "ds_read_b128 v[60:63], v97 offset:11264\n"
    "s_cmp_lt_i32 s34, 0\n"
    "s_cbranch_scc0 .Lvo_take_%=\n"
    "s_waitcnt vmcnt(0) lgkmcnt(0)\n"
    "s_barrier\n"
    ".Lvo_po8_%=:\n"
    "buffer_load_dword v110, v106, s[0:3], s23 offen sc1\n"
    "s_waitcnt vmcnt(0)\n"
    "v_cmp_gt_i32 vcc, s50, v110\n"
    "s_nop 0\n"
    "s_cbranch_vccz .Lvo_pd9_%=\n"
    "s_add_u32 s28, s28, 1\n"
    "s_cmp_lt_u32 s28, 0x40000\n"
    "s_cbranch_scc1 .Lvo_sp10_%=\n"
    "v_mov_b32 v110, 32\n"
    "s_mov_b32 s32, 33728\n"
    "buffer_store_dword v110, off, s[0:3], s32 sc1\n"
    "s_waitcnt vmcnt(0)\n"
    "s_branch .Lvo_pd9_%=\n"
    ".Lvo_sp10_%=:\n"
    "s_sleep 4\n"
    "s_branch .Lvo_po8_%=\n"
    ".Lvo_pd9_%=:\n"
    ".Lvo_po11_%=:\n"
    "buffer_load_dword v111, v105, s[0:3], s22 offen sc1\n"
    "s_waitcnt vmcnt(0)\n"
    "v_cmp_gt_i32 vcc, s51, v111\n"
    "s_nop 0\n"
    "s_cbranch_vccz .Lvo_pd12_%=\n"
    "s_add_u32 s28, s28, 1\n"
    "s_cmp_lt_u32 s28, 0x40000\n"
    "s_cbranch_scc1 .Lvo_sp13_%=\n"
    "v_mov_b32 v111, 35\n"
    "s_mov_b32 s32, 33728\n"
    "buffer_store_dword v111, off, s[0:3], s32 sc1\n"
    "s_waitcnt vmcnt(0)\n"
    "s_branch .Lvo_pd12_%=\n"
    ".Lvo_sp13_%=:\n"
    "s_sleep 4\n"
    "s_branch .Lvo_po11_%=\n"
    ".Lvo_pd12_%=:\n"
    "buffer_load_dwordx4 v[32:35], v96, s[0:3], s44 offen offset:0 sc1\n"
    "buffer_load_dwordx4 v[36:39], v96, s[0:3], s44 offen offset:1024 sc1\n"
    "buffer_load_dwordx4 v[40:43], v96, s[0:3], s44 offen offset:2048 sc1\n"
    "buffer_load_dwordx4 v[44:47], v96, s[0:3], s44 offen offset:3072 sc1\n"
    "s_add_u32 m0, s48, 0\n"
    "s_nop 0\n"
    "buffer_load_dwordx4 v96, s[0:3], s43 offen sc1 lds\n"
    "buffer_load_dwordx4 v96, s[0:3], s43 offen offset:1024 sc1 lds\n"
    "s_add_u32 m0, s48, 13312\n"
    "s_add_u32 s43, s43, 0x4000\n"
    "buffer_load_dwordx4 v96, s[0:3], s43 offen sc1 lds\n"
    "buffer_load_dwordx4 v96, s[0:3], s43 offen offset:1024 sc1 lds\n"
    "s_add_u32 m0, s48, 26624\n"
    "s_add_u32 s43, s43, 0x4000\n"
    "buffer_load_dwordx4 v96, s[0:3], s43 offen sc1 lds\n"
    "buffer_load_dwordx4 v96, s[0:3], s43 offen offset:1024 sc1 lds\n"
    "ds_read_b128 v[0:3], v96 offset:11264\n"
    "ds_read_b128 v[4:7], v96 offset:12288\n"
    "ds_read_b128 v[8:11], v96 offset:24576\n"
    "ds_read_b128 v[12:15], v96 offset:25600\n"
    "ds_read_b128 v[16:19], v96 offset:37888\n"
    "ds_read_b128 v[20:23], v96 offset:38912\n"
    "ds_read_b128 v[24:27], v96 offset:51200\n"
    "ds_read_b128 v[28:31], v96 offset:52224\n"
    "s_waitcnt vmcnt(0)\n"
    "s_mov_b32 s37, 2130706432\n"
    "s_mov_b32 s36, 2130706432\n"
    "s_branch .Lvo_uph_%=\n"
    ".Lvo_retry_%=:\n"
    "s_add_u32 s28, s28, 1\n"
    "s_cmp_lt_u32 s28, 0x40000\n"
    "s_cbranch_scc1 .Lvo_sp14_%=\n"
    "v_mov_b32 v108, 2\n"
    "s_mov_b32 s32, 33728\n"
    "buffer_store_dword v108, off, s[0:3], s32 sc1\n"
    "s_waitcnt vmcnt(0)\n"
    "s_branch .Lvo_go_%=\n"
    ".Lvo_sp14_%=:\n"
    "s_nop 7\n"
    "v_mov_b64 v[64:65], 0\n"
    "v_mov_b64 v[66:67], 0\n"
    "v_mov_b64 v[68:69], 0\n"
    "v_mov_b64 v[70:71], 0\n"
    "v_mov_b64 v[84:85], 0\n"
    "v_mov_b64 v[86:87], 0\n"
    "buffer_load_dwordx4 v[72:75], v96, s[0:3], s41 offen sc1\n"
    "buffer_load_dwordx4 v[76:79], v96, s[0:3], s41 offen offset:1024 sc1\n"
    "buffer_load_dwordx4 v[80:83], v96, s[0:3], s42 offen sc1\n"
    "s_waitcnt vmcnt(0)\n"
    "s_branch .Lvo_take_%=\n"
    ".Lvo_wq_%=:\n"
    ".Lvo_po15_%=:\n"
    "buffer_load_dword v110, v106, s[0:3], s23 offen sc1\n"
    "s_waitcnt vmcnt(0)\n"
    "v_cmp_gt_i32 vcc, s50, v110\n"
    "s_nop 0\n"
    "s_cbranch_vccz .Lvo_pd16_%=\n"
    "s_add_u32 s28, s28, 1\n"
    "s_cmp_lt_u32 s28, 0x40000\n"
    "s_cbranch_scc1 .Lvo_sp17_%=\n"
    "v_mov_b32 v110, 33\n"
    "s_mov_b32 s32, 33728\n"
    "buffer_store_dword v110, off, s[0:3], s32 sc1\n"
    "s_waitcnt vmcnt(0)\n"
    "s_branch .Lvo_pd16_%=\n"
    ".Lvo_sp17_%=:\n"
    "s_sleep 4\n"
    "s_branch .Lvo_po15_%=\n"
    ".Lvo_pd16_%=:\n"
    "s_branch .Lvo_wqb_%=\n"
    ".Lvo_wx_%=:\n"
    ".Lvo_po18_%=:\n"
    "buffer_load_dword v111, v105, s[0:3], s22 offen sc1\n"
    "s_waitcnt vmcnt(0)\n"
    "v_cmp_gt_i32 vcc, s51, v111\n"
    "s_nop 0\n"
    "s_cbranch_vccz .Lvo_pd19_%=\n"
    "s_add_u32 s28, s28, 1\n"
    "s_cmp_lt_u32 s28, 0x40000\n"
    "s_cbranch_scc1 .Lvo_sp20_%=\n"
    "v_mov_b32 v111, 34\n"
    "s_mov_b32 s32, 33728\n"
    "buffer_store_dword v111, off, s[0:3], s32 sc1\n"
    "s_waitcnt vmcnt(0)\n"
    "s_branch .Lvo_pd19_%=\n"
    ".Lvo_sp20_%=:\n"
    "s_sleep 4\n"
    "s_branch .Lvo_po18_%=\n"
    ".Lvo_pd19_%=:\n"
    "s_branch .Lvo_wxb_%=\n"
    ".Lvo_end_%=:\n"
    "s_waitcnt vmcnt(0) lgkmcnt(0)\n"
    "s_endpgm\n"
    :: "{s[0:3]}"(s0_), "{s[4:7]}"(s4_), "{s[8:11]}"(s8_), "{s[12:15]}"(s12_), "{s[16:19]}"(s16_), "{s[20:23]}"(s20_), "{s[24:27]}"(s24_), "{v[0:3]}"(v0_), "{v[4:7]}"(v4_), "{v[8:11]}"(v8_), "{v[92:95]}"(v92_), "{v[96:99]}"(v96_), "{v[100:103]}"(v100_), "{v[104:107]}"(v104_) : "memory", "v255", "a255", "s95");
    __builtin_unreachable();
}
DEV void ct_role(const MegaArgs& A, const int L, const int u) {
    ROLE_HEAD;
    {
        const int sg = 2 * u + L;
        u32x4 xa[8], xb[8];
#pragma unroll
        for (int r = 0; r < 8; ++r) {
            const bf16_t* xr = A.x0 + (size_t)(32 * sg + 8 * w + r) * D_;
            xa[r] = *(const u32x4*)(xr + 8 * l);
            xb[r] = *(const u32x4*)(xr + 512 + 8 * l16);
        }
        const float mk = l < 16 ? 1.f : 0.f;
#pragma unroll
        for (int r = 0; r < 8; ++r) {
            float v[16], s = 0.f, s2 = 0.f, q = 0.f, q2 = 0.f;
#pragma unroll
            for (int e = 0; e < 4; ++e) { v[2 * e] = lo16(xa[r][e]); v[2 * e + 1] = hi16(xa[r][e]); v[8 + 2 * e] = lo16(xb[r][e]); v[9 + 2 * e] = hi16(xb[r][e]); }
#pragma unroll
            for (int e = 0; e < 8; ++e) { s += v[e]; s2 += v[8 + e]; }
            s += l < 16 ? s2 : 0.f;
            for (int o = 32; o > 0; o >>= 1) s += __shfl_xor(s, o);
            const float mean = s * (1.0f / D_);
#pragma unroll
            for (int e = 0; e < 8; ++e) { const float d = v[e] - mean, d2 = v[8 + e] - mean; q += d * d; q2 += d2 * d2; }
            q += l < 16 ? q2 : 0.f;
            for (int o = 32; o > 0; o >>= 1) q += __shfl_xor(q, o);
            const float mb = rb(mean), rd = __builtin_amdgcn_rcpf(rb(sqrtf(rb(rb(q * (1.0f / D_)) + 1e-5f))));
            u32x4 oa, ob;
#pragma unroll
            for (int e = 0; e < 4; ++e) {
                oa[sw2(e)] = pk2(rb(v[2 * e] - mb) * rd, rb(v[2 * e + 1] - mb) * rd);
                ob[sw2(e)] = pk2(rb(v[8 + 2 * e] - mb) * rd, rb(v[9 + 2 * e] - mb) * rd);
            }
            const int row = 8 * w + r;
            const unsigned o = rec(sg, 0) + (row >> 4) * 1024 + (row & 15) * 16 + (l16 >> 2) * 2048 + (l & 3) * 256;
            bst16(rr, o + lq * 16384, oa);
            bst16(rr, l < 16 ? o + 65536 : OOB, ob);
        }
    }
    const int r5 = l / 5, q5 = l - 5 * r5, c0 = q5 < 4 ? 16 * u + 4 * q5 : 512 + 4 * u, tk = 8 * w + r5;
    const unsigned mpos = l < 40 ? xl(RG_MEM + w * 16384 + (c0 >> 6) * 1024 + (((c0 >> 4) & 3) * 16 + 4 * (r5 >> 1) + ((c0 >> 1) & 2) + (r5 & 1)) * 16 + (c0 & 8), L) : OOB;
    const unsigned xpos = l < 40 ? (c0 >> 7) * 16384 + ((c0 >> 5) & 3) * 2048 + (tk >> 4) * 1024 + (((c0 >> 3) & 3) * 16 + (tk & 15)) * 16 + (c0 & 4) * 2 : OOB;
    const unsigned rpos = (L || l >= 40) ? xpos + RC_X2 : tk * 1280 + c0 * 2;
    const u32x4 rs = L ? rx : u32x4{lo(A.x0), hi(A.x0), 2048u * 1280u, 0x20000u};
    const u32x2 m0 = *(const u32x2*)(A.mem0 + (r5 & 7) * D_ + c0);
    bst8(rr, mpos, u32x2{nosent(m0[0]), m0[1]}); bst8(rr, mpos + SLOT, ones2); bst8(rr, mpos + 2 * SLOT, ones2);
    DRAIN_BARRIER();
    bst4(rr, threadIdx.x < 16 ? xl(Z_CT + (l & 7) * 256 + (32 * L + u) * 4, l >> 3) : OOB, 1u);
    if (!((A.layers >> L) & 1)) return;
    const u32x4 s0_ = {RFL(rx[0]), RFL(rx[1]), RFL(rx[2]), RFL(rx[3])};
    const u32x4 s4_ = {RFL(rs[0]), RFL(rs[1]), RFL(rs[2]), RFL(rs[3])};
    const u32x4 s8_ = {RFL(L << 13), RFL(REC0), RFL(xl(RG_UT + (u * 4 + w) * 8192, L)), RFL(xl(RG_UTX + (u * 4 + w) * 2048, L))};
    const u32x4 s12_ = {RFL(xl(RG_UM + (u * 4 + w) * 2048, L)), RFL(xl(RG_UMX + (u * 4 + w) * 512, L)), RFL(xl(RG_PT + w * 16384, L)), RFL(xl(RG_PM + w * 8192, L))};
    const u32x4 s16_ = {RFL(xl(Z_X + 16384 + (u & 1) * 8192, L)), RFL(xl(Z_QV + (u & 3) * 256, L)), RFL(DBG0 + ((L * 128 + 64 + u) * 4 + w) * 2048), RFL(0u)};
    const u32x4 v192_ = {(unsigned)(l * 16), (unsigned)(l * 16 + 4096), (unsigned)((l & 2) ? 0xeeeeu : 0x4444u), (unsigned)(0x4444u)};
    const u32x4 v196_ = {(unsigned)((l16 >> 2) * 256 + lq * 64 + (l & 3) * 16), (unsigned)(lq * 64 + (l & 3) * 16), (unsigned)((lq >> 1) * 2048 + 1024 + (lq & 1) * 256 + l16 * 16), (unsigned)(lq * 2048 + 1536 + l16 * 16)};
    const u32x4 v200_ = {(unsigned)(0 + w * 640 + lq * 160 + l16 * 4), (unsigned)(l16 < 4 ? 0 + w * 640 + lq * 160 + 64 + l16 * 4 : 12800), (unsigned)(0 + (l < 40 ? l : 0) * 16), (unsigned)(mpos)};
    const u32x4 v204_ = {(unsigned)(2560 + w * 2560 + lq * 320 + l16 * 4), (unsigned)(l16 < 4 ? 2560 + w * 2560 + lq * 320 + 64 + l16 * 4 : 12800), (unsigned)(3840 + w * 2560 + lq * 320 + l16 * 4), (unsigned)(l16 < 4 ? 3840 + w * 2560 + lq * 320 + 64 + l16 * 4 : 12800)};
    const u32x4 v208_ = {(unsigned)(2560 + w * 640 + (l < 40 ? l : 0) * 16), (unsigned)(xpos + RC_X1), (unsigned)(rpos), (unsigned)(l < 4 ? xl(Z_X1 + l * 4096 + u * 128 + w * 4, L) : OOB)};
    asm volatile(
    "s_mov_b32 s20, 0\n"
    "v_lshrrev_b32 v4, 2, v192\n"
    ".Lct_po1_%=:\n"
    "buffer_load_dword v8, v4, s[0:3], s17 offen sc1\n"
    "s_waitcnt vmcnt(0)\n"
    "v_cmp_gt_i32 vcc, 1, v8\n"
    "s_nop 0\n"
    "s_cbranch_vccz .Lct_pd2_%=\n"
    "s_add_u32 s20, s20, 1\n"
    "s_cmp_lt_u32 s20, 0x40000\n"
    "s_cbranch_scc1 .Lct_sp3_%=\n"
    "v_mov_b32 v8, 40\n"
    "s_mov_b32 s24, 33728\n"
    "buffer_store_dword v8, off, s[0:3], s24 sc1\n"
    "s_waitcnt vmcnt(0)\n"
    "s_branch .Lct_pd2_%=\n"
    ".Lct_sp3_%=:\n"
    "s_sleep 4\n"
    "s_branch .Lct_po1_%=\n"
    ".Lct_pd2_%=:\n"
    "s_memrealtime s[22:23]\n"
    "s_waitcnt lgkmcnt(0)\n"
    "s_sub_u32 s41, s22, 912\n"
    "s_mov_b32 s26, -1\n"
    "s_add_u32 s26, s26, 1\n"
    "s_add_u32 s27, s26, 1\n"
    "s_add_u32 s28, s26, 0\n"
    "s_and_b32 s28, s28, 3\n"
    "s_lshl_b32 s28, s28, 22\n"
    "s_add_u32 s29, s26, 1\n"
    "s_and_b32 s29, s29, 3\n"
    "s_lshl_b32 s29, s29, 22\n"
    "s_add_u32 s30, s26, 3\n"
    "s_and_b32 s30, s30, 3\n"
    "s_lshl_b32 s30, s30, 22\n"
    "s_and_b32 s40, s26, 63\n"
    "s_lshl_b32 s40, s40, 7\n"
    "s_add_u32 s31, s12, s28\n"
    "s_add_u32 s32, s13, s28\n"
    "s_add_u32 s33, s15, s28\n"
    "s_add_u32 s34, s14, s28\n"
    "s_add_u32 s38, s10, s29\n"
    "s_add_u32 s39, s11, s29\n"
    "s_add_u32 s40, s16, s40\n"
    "s_add_u32 s35, s34, 4096\n"
    "s_add_u32 s36, s34, 16384\n"
    "s_add_u32 s37, s34, 20480\n"
    "s_add_u32 s43, s41, 356\n"
    "s_lshl_b32 s45, s26, 19\n"
    "s_lshl_b32 s25, s26, 11\n"
    "s_xor_b32 s25, s25, s8\n"
    "s_and_b32 s25, s25, 0x2000\n"
    "s_add_u32 s45, s45, s25\n"
    "s_add_u32 s45, s45, s9\n"
    "s_mul_i32 s46, s26, 40960\n"
    "s_cmp_eq_u32 s8, 0\n"
    "s_cselect_b32 s46, s46, s45\n"
    "v_mov_b64 v[76:77], 0\n"
    "v_mov_b64 v[78:79], 0\n"
    "v_mov_b64 v[80:81], 0\n"
    "v_mov_b64 v[82:83], 0\n"
    ".Lct_p0_%=:\n"
    "buffer_load_dwordx4 v[40:43], v192, s[0:3], s31 offen offset:0 sc1\n"
    "buffer_load_dwordx4 v[44:47], v192, s[0:3], s31 offen offset:1024 sc1\n"
    "buffer_load_dwordx4 v[48:51], v197, s[0:3], s32 offen offset:0 sc1\n"
    "buffer_load_dwordx4 v[52:55], v197, s[0:3], s32 offen offset:256 sc1\n"
    "s_waitcnt vmcnt(0)\n"
    "v_max3_u32 v184, v40, v42, v40\n"
    "v_max3_u32 v184, v44, v46, v184\n"
    "v_max3_u32 v184, v48, v50, v184\n"
    "v_max3_u32 v184, v52, v54, v184\n"
    "v_cmp_eq_u32 vcc, -1, v184\n"
    "s_nop 0\n"
    "s_cbranch_vccz .Lct_p1_%=\n"
    "s_add_u32 s20, s20, 1\n"
    "s_cmp_lt_u32 s20, 0x40000\n"
    "s_cbranch_scc1 .Lct_sp4_%=\n"
    "v_mov_b32 v184, 41\n"
    "s_mov_b32 s24, 33728\n"
    "buffer_store_dword v184, off, s[0:3], s24 sc1\n"
    "s_waitcnt vmcnt(0)\n"
    "s_branch .Lct_p1_%=\n"
    ".Lct_sp4_%=:\n"
    "s_sleep 8\n"
    "s_branch .Lct_p0_%=\n"
    ".Lct_p1_%=:\n"
    "s_mov_b32 s38, s10\n"
    "s_mov_b32 s39, s11\n"
    "buffer_load_dwordx4 a[0:3], v192, s[0:3], s38 offen offset:0 sc1\n"
    "buffer_load_dwordx4 a[4:7], v192, s[0:3], s38 offen offset:1024 sc1\n"
    "buffer_load_dwordx4 a[8:11], v192, s[0:3], s38 offen offset:2048 sc1\n"
    "buffer_load_dwordx4 a[12:15], v192, s[0:3], s38 offen offset:3072 sc1\n"
    "buffer_load_dwordx4 a[16:19], v193, s[0:3], s38 offen offset:0 sc1\n"
    "buffer_load_dwordx4 a[20:23], v193, s[0:3], s38 offen offset:1024 sc1\n"
    "buffer_load_dwordx4 a[24:27], v193, s[0:3], s38 offen offset:2048 sc1\n"
    "buffer_load_dwordx4 a[28:31], v193, s[0:3], s38 offen offset:3072 sc1\n"
    "buffer_load_dwordx4 v[0:3], v196, s[0:3], s39 offen offset:0 sc1\n"
    "buffer_load_dwordx4 v[4:7], v196, s[0:3], s39 offen offset:1024 sc1\n"
    "s_add_u32 s38, s10, s29\n"
    "s_add_u32 s39, s11, s29\n"
    "v_mov_b32 v189, 2130706432\n"
    "s_waitcnt vmcnt(0)\n"
    ".Lct_top_%=:\n"
    ".Lct_rt5_%=:\n"
    "s_memrealtime s[22:23]\n"
    "s_waitcnt lgkmcnt(0)\n"
    "s_sub_u32 s24, s43, s22\n"
    "s_cmp_lt_u32 s24, 600\n"
    "s_cbranch_scc1 .Lct_rt5_%=\n"
    ".Lct_x0_%=:\n"
    "buffer_load_dwordx2 v[190:191], off, s[0:3], s40 sc1\n"
    "s_waitcnt vmcnt(2)\n"
    "v_mov_b64 v[8:9], v[0:1]\n"
    "v_mov_b64 v[10:11], v[2:3]\n"
    "v_mov_b64 v[12:13], v[4:5]\n"
    "v_mov_b64 v[14:15], v[6:7]\n"
    "v_mov_b32_dpp v16, v0 row_shl:4 row_mask:0xf bank_mask:0xf bound_ctrl:1\n"
    "v_mov_b32_dpp v17, v1 row_shl:4 row_mask:0xf bank_mask:0xf bound_ctrl:1\n"
    "v_mov_b32_dpp v18, v2 row_shl:4 row_mask:0xf bank_mask:0xf bound_ctrl:1\n"
    "v_mov_b32_dpp v19, v3 row_shl:4 row_mask:0xf bank_mask:0xf bound_ctrl:1\n"
    "v_mov_b32_dpp v20, v4 row_shl:4 row_mask:0xf bank_mask:0xf bound_ctrl:1\n"
    "v_mov_b32_dpp v21, v5 row_shl:4 row_mask:0xf bank_mask:0xf bound_ctrl:1\n"
    "v_mov_b32_dpp v22, v6 row_shl:4 row_mask:0xf bank_mask:0xf bound_ctrl:1\n"
    "v_mov_b32_dpp v23, v7 row_shl:4 row_mask:0xf bank_mask:0xf bound_ctrl:1\n"
    "v_mov_b32_dpp v24, v0 row_shl:8 row_mask:0xf bank_mask:0xf bound_ctrl:1\n"
    "v_mov_b32_dpp v25, v1 row_shl:8 row_mask:0xf bank_mask:0xf bound_ctrl:1\n"
    "v_mov_b32_dpp v26, v2 row_shl:8 row_mask:0xf bank_mask:0xf bound_ctrl:1\n"
    "v_mov_b32_dpp v27, v3 row_shl:8 row_mask:0xf bank_mask:0xf bound_ctrl:1\n"
    "v_mov_b32_dpp v28, v4 row_shl:8 row_mask:0xf bank_mask:0xf bound_ctrl:1\n"
    "v_mov_b32_dpp v29, v5 row_shl:8 row_mask:0xf bank_mask:0xf bound_ctrl:1\n"
    "v_mov_b32_dpp v30, v6 row_shl:8 row_mask:0xf bank_mask:0xf bound_ctrl:1\n"
    "v_mov_b32_dpp v31, v7 row_shl:8 row_mask:0xf bank_mask:0xf bound_ctrl:1\n"
    "v_mov_b32_dpp v32, v0 row_shl:12 row_mask:0xf bank_mask:0xf bound_ctrl:1\n"
    "v_mov_b32_dpp v33, v1 row_shl:12 row_mask:0xf bank_mask:0xf bound_ctrl:1\n"
    "v_mov_b32_dpp v34, v2 row_shl:12 row_mask:0xf bank_mask:0xf bound_ctrl:1\n"
    "v_mov_b32_dpp v35, v3 row_shl:12 row_mask:0xf bank_mask:0xf bound_ctrl:1\n"
    "v_mov_b32_dpp v36, v4 row_shl:12 row_mask:0xf bank_mask:0xf bound_ctrl:1\n"
    "v_mov_b32_dpp v37, v5 row_shl:12 row_mask:0xf bank_mask:0xf bound_ctrl:1\n"
    "v_mov_b32_dpp v38, v6 row_shl:12 row_mask:0xf bank_mask:0xf bound_ctrl:1\n"
    "v_mov_b32_dpp v39, v7 row_shl:12 row_mask:0xf bank_mask:0xf bound_ctrl:1\n"
    "s_waitcnt vmcnt(0)\n"
    "buffer_store_dword v188, v189, s[0:3], 0 offen sc1\n"
    "v_readfirstlane_b32 s25, v191\n"
    "v_readfirstlane_b32 s41, v190\n"
    "s_cmp_eq_u32 s25, s27\n"
    "s_cbranch_scc0 .Lct_xr0_%=\n"
    ".Lct_xg0_%=:\n"
    "s_add_u32 s43, s41, 99\n"
    "s_add_u32 s44, s41, 115\n"
    ".Lct_rt6_%=:\n"
    "s_memrealtime s[22:23]\n"
    "s_waitcnt lgkmcnt(0)\n"
    "s_sub_u32 s24, s43, s22\n"
    "s_cmp_lt_u32 s24, 600\n"
    "s_cbranch_scc1 .Lct_rt6_%=\n"
    "buffer_load_dwordx4 v[40:43], v192, s[0:3], s31 offen offset:0 sc1\n"
    "buffer_load_dwordx4 v[44:47], v192, s[0:3], s31 offen offset:1024 sc1\n"
    "buffer_load_dwordx4 v[48:51], v197, s[0:3], s32 offen offset:0 sc1\n"
    "buffer_load_dwordx4 v[52:55], v197, s[0:3], s32 offen offset:256 sc1\n"
    ".Lct_rt7_%=:\n"
    "s_memrealtime s[22:23]\n"
    "s_waitcnt lgkmcnt(0)\n"
    "s_sub_u32 s24, s44, s22\n"
    "s_cmp_lt_u32 s24, 600\n"
    "s_cbranch_scc1 .Lct_rt7_%=\n"
    "buffer_load_dwordx4 v[72:75], v193, s[0:3], s33 offen sc1\n"
    "buffer_load_dwordx4 v[56:59], v192, s[0:3], s33 offen offset:0 sc1\n"
    "buffer_load_dwordx4 v[60:63], v192, s[0:3], s33 offen offset:1024 sc1\n"
    "buffer_load_dwordx4 v[64:67], v192, s[0:3], s33 offen offset:2048 sc1\n"
    "buffer_load_dwordx4 v[68:71], v192, s[0:3], s33 offen offset:3072 sc1\n"
    ".Lct_take0_%=:\n"
    "s_waitcnt vmcnt(5)\n"
    "v_max3_u32 v184, v40, v42, v40\n"
    "v_max3_u32 v184, v44, v46, v184\n"
    "v_max3_u32 v184, v48, v50, v184\n"
    "v_max3_u32 v184, v52, v54, v184\n"
    "s_waitcnt vmcnt(4)\n"
    "v_smfmac_f32_16x16x64_bf16 v[76:79], v[72:75], v[40:47], v194\n"
    "v_max3_u32 v184, v72, v74, v184\n"
    "v_smfmac_f32_16x16x64_bf16 v[80:83], v[72:75], v[48:55], v194\n"
    "s_waitcnt vmcnt(3)\n"
    "v_smfmac_f32_16x16x64_bf16 v[80:83], v[56:59], v[8:15], v194\n"
    "v_max3_u32 v184, v56, v58, v184\n"
    "v_smfmac_f32_16x16x64_bf16 v[76:79], v[56:59], a[0:7], v194\n"
    "s_waitcnt vmcnt(2)\n"
    "v_smfmac_f32_16x16x64_bf16 v[80:83], v[60:63], v[16:23], v194\n"
    "v_max3_u32 v184, v60, v62, v184\n"
    "v_smfmac_f32_16x16x64_bf16 v[76:79], v[60:63], a[8:15], v194\n"
    "s_waitcnt vmcnt(1)\n"
    "v_smfmac_f32_16x16x64_bf16 v[80:83], v[64:67], v[24:31], v194\n"
    "v_max3_u32 v184, v64, v66, v184\n"
    "v_smfmac_f32_16x16x64_bf16 v[76:79], v[64:67], a[16:23], v194\n"
    "s_waitcnt vmcnt(0)\n"
    "v_smfmac_f32_16x16x64_bf16 v[80:83], v[68:71], v[32:39], v194\n"
    "v_max3_u32 v184, v68, v70, v184\n"
    "v_smfmac_f32_16x16x64_bf16 v[76:79], v[68:71], a[24:31], v194\n"
    "v_cmp_eq_u32 vcc, -1, v184\n"
    "s_nop 0\n"
    "s_cbranch_vccnz .Lct_retry0_%=\n"
    ".Lct_go0_%=:\n"
    "s_nop 2\n"
    "v_pk_add_f32 v[80:81], v[80:81], v[82:83]\n"
    "ds_write2_b32 v201, v80, v81 offset1:20\n"
    "v_pk_add_f32 v[76:77], v[76:77], v[78:79]\n"
    "ds_write2_b32 v200, v76, v77 offset1:20\n"
    "s_waitcnt lgkmcnt(0)\n"
    "s_barrier\n"
    "ds_read_b128 v[164:167], v202 offset:0\n"
    "ds_read_b128 v[168:171], v202 offset:640\n"
    "ds_read_b128 v[172:175], v202 offset:1280\n"
    "ds_read_b128 v[176:179], v202 offset:1920\n"
    "s_waitcnt lgkmcnt(2)\n"
    "v_pk_add_f32 v[164:165], v[164:165], v[168:169]\n"
    "v_pk_add_f32 v[166:167], v[166:167], v[170:171]\n"
    "s_waitcnt lgkmcnt(0)\n"
    "v_pk_add_f32 v[172:173], v[172:173], v[176:177]\n"
    "v_pk_add_f32 v[174:175], v[174:175], v[178:179]\n"
    "v_pk_add_f32 v[164:165], v[164:165], v[172:173]\n"
    "v_pk_add_f32 v[166:167], v[166:167], v[174:175]\n"
    "v_cvt_pk_bf16_f32 v164, v164, v165\n"
    "v_cvt_pk_bf16_f32 v165, v166, v167\n"
    "v_min_u32 v164, -2, v164\n"
    "buffer_store_dwordx2 v[164:165], v203, s[0:3], s29 offen sc1\n"
    "s_sleep 2\n"
    "buffer_load_dwordx4 v[132:135], v199, s[0:3], s34 offen offset:0 sc1\n"
    "buffer_load_dwordx4 v[136:139], v199, s[0:3], s36 offen offset:0 sc1\n"
    "buffer_load_dwordx4 v[140:143], v199, s[0:3], s34 offen offset:256 sc1\n"
    "buffer_load_dwordx4 v[144:147], v199, s[0:3], s36 offen offset:256 sc1\n"
    "buffer_load_dwordx4 v[116:119], v198, s[0:3], s34 offen sc1\n"
    "buffer_load_dwordx4 v[84:87], v192, s[0:3], s34 offen offset:0 sc1\n"
    "buffer_load_dwordx4 v[88:91], v192, s[0:3], s34 offen offset:2048 sc1\n"
    "buffer_load_dwordx4 v[120:123], v198, s[0:3], s35 offen sc1\n"
    "buffer_load_dwordx4 v[92:95], v192, s[0:3], s35 offen offset:0 sc1\n"
    "buffer_load_dwordx4 v[96:99], v192, s[0:3], s35 offen offset:2048 sc1\n"
    "buffer_load_dwordx4 v[124:127], v198, s[0:3], s36 offen sc1\n"
    "buffer_load_dwordx4 v[100:103], v192, s[0:3], s36 offen offset:0 sc1\n"
    "buffer_load_dwordx4 v[104:107], v192, s[0:3], s36 offen offset:2048 sc1\n"
    "buffer_load_dwordx4 v[128:131], v198, s[0:3], s37 offen sc1\n"
    "buffer_load_dwordx4 v[108:111], v192, s[0:3], s37 offen offset:0 sc1\n"
    "buffer_load_dwordx4 v[112:115], v192, s[0:3], s37 offen offset:2048 sc1\n"
    "buffer_load_dwordx2 v[186:187], v210, s[4:7], s46 offen sc1\n"
    "v_mov_b64 v[166:167], -1\n"
    "buffer_store_dwordx2 v[166:167], v203, s[0:3], s30 offen sc1\n"
    "s_waitcnt vmcnt(17)\n"
    "v_max3_u32 v184, v132, v134, v132\n"
    "v_swap_b32 v133, v134\n"
    "s_nop 1\n"
    "v_mfma_f32_16x16x32_bf16 v[148:151], v[132:135], v[40:43], 0\n"
    "v_mfma_f32_16x16x32_bf16 v[152:155], v[132:135], v[48:51], 0\n"
    "buffer_load_dwordx4 a[32:35], v192, s[0:3], s38 offen offset:0 sc1\n"
    "s_waitcnt vmcnt(17)\n"
    "v_max3_u32 v184, v136, v138, v184\n"
    "v_swap_b32 v137, v138\n"
    "s_nop 1\n"
    "v_mfma_f32_16x16x32_bf16 v[148:151], v[136:139], v[44:47], v[148:151]\n"
    "v_mfma_f32_16x16x32_bf16 v[152:155], v[136:139], v[52:55], v[152:155]\n"
    "buffer_load_dwordx4 a[36:39], v192, s[0:3], s38 offen offset:1024 sc1\n"
    "s_waitcnt vmcnt(17)\n"
    "v_max3_u32 v184, v140, v142, v184\n"
    "v_swap_b32 v141, v142\n"
    "s_nop 1\n"
    "v_mfma_f32_16x16x32_bf16 v[156:159], v[140:143], v[40:43], 0\n"
    "v_mfma_f32_16x16x32_bf16 v[160:163], v[140:143], v[48:51], 0\n"
    "buffer_load_dwordx4 a[40:43], v192, s[0:3], s38 offen offset:2048 sc1\n"
    "s_waitcnt vmcnt(17)\n"
    "v_max3_u32 v184, v144, v146, v184\n"
    "v_swap_b32 v145, v146\n"
    "s_nop 1\n"
    "v_mfma_f32_16x16x32_bf16 v[156:159], v[144:147], v[44:47], v[156:159]\n"
    "v_mfma_f32_16x16x32_bf16 v[160:163], v[144:147], v[52:55], v[160:163]\n"
    "buffer_load_dwordx4 a[44:47], v192, s[0:3], s38 offen offset:3072 sc1\n"
    "s_waitcnt vmcnt(17)\n"
    "v_max3_u32 v184, v116, v118, v184\n"
    "v_smfmac_f32_16x16x64_bf16 v[148:151], v[116:119], a[0:7], v195\n"
    "v_smfmac_f32_16x16x64_bf16 v[152:155], v[116:119], v[8:15], v195\n"
    "buffer_load_dwordx4 a[48:51], v193, s[0:3], s38 offen offset:0 sc1\n"
    "s_waitcnt vmcnt(17)\n"
    "v_max3_u32 v184, v84, v86, v184\n"
    "v_swap_b32 v85, v86\n"
    "s_nop 1\n"
    "v_mfma_f32_16x16x32_bf16 v[156:159], v[84:87], a[0:3], v[156:159]\n"
    "v_mfma_f32_16x16x32_bf16 v[160:163], v[84:87], v[8:11], v[160:163]\n"
    "buffer_load_dwordx4 a[52:55], v193, s[0:3], s38 offen offset:1024 sc1\n"
    "s_waitcnt vmcnt(17)\n"
    "v_max3_u32 v184, v88, v90, v184\n"
    "v_swap_b32 v89, v90\n"
    "s_nop 1\n"
    "v_mfma_f32_16x16x32_bf16 v[156:159], v[88:91], a[4:7], v[156:159]\n"
    "v_mfma_f32_16x16x32_bf16 v[160:163], v[88:91], v[12:15], v[160:163]\n"
    "buffer_load_dwordx4 a[56:59], v193, s[0:3], s38 offen offset:2048 sc1\n"
    "s_waitcnt vmcnt(17)\n"
    "v_max3_u32 v184, v120, v122, v184\n"
    "v_smfmac_f32_16x16x64_bf16 v[148:151], v[120:123], a[8:15], v195\n"
    "v_smfmac_f32_16x16x64_bf16 v[152:155], v[120:123], v[16:23], v195\n"
    "buffer_load_dwordx4 a[60:63], v193, s[0:3], s38 offen offset:3072 sc1\n"
    "s_waitcnt vmcnt(17)\n"
    "v_max3_u32 v184, v92, v94, v184\n"
    "v_swap_b32 v93, v94\n"
    "s_nop 1\n"
    "v_mfma_f32_16x16x32_bf16 v[156:159], v[92:95], a[8:11], v[156:159]\n"
    "v_mfma_f32_16x16x32_bf16 v[160:163], v[92:95], v[16:19], v[160:163]\n"
    "buffer_load_dwordx4 v[0:3], v196, s[0:3], s39 offen offset:0 sc1\n"
    "s_waitcnt vmcnt(17)\n"
    "v_max3_u32 v184, v96, v98, v184\n"
    "v_swap_b32 v97, v98\n"
    "s_nop 1\n"
    "v_mfma_f32_16x16x32_bf16 v[156:159], v[96:99], a[12:15], v[156:159]\n"
    "v_mfma_f32_16x16x32_bf16 v[160:163], v[96:99], v[20:23], v[160:163]\n"
    "buffer_load_dwordx4 v[4:7], v196, s[0:3], s39 offen offset:1024 sc1\n"
    "s_waitcnt vmcnt(17)\n"
    "v_max3_u32 v184, v124, v126, v184\n"
    "v_smfmac_f32_16x16x64_bf16 v[148:151], v[124:127], a[16:23], v195\n"
    "v_smfmac_f32_16x16x64_bf16 v[152:155], v[124:127], v[24:31], v195\n"
    "s_waitcnt vmcnt(16)\n"
    "v_max3_u32 v184, v100, v102, v184\n"
    "v_swap_b32 v101, v102\n"
    "s_nop 1\n"
    "v_mfma_f32_16x16x32_bf16 v[156:159], v[100:103], a[16:19], v[156:159]\n"
    "v_mfma_f32_16x16x32_bf16 v[160:163], v[100:103], v[24:27], v[160:163]\n"
    "s_waitcnt vmcnt(15)\n"
    "v_max3_u32 v184, v104, v106, v184\n"
    "v_swap_b32 v105, v106\n"
    "s_nop 1\n"
    "v_mfma_f32_16x16x32_bf16 v[156:159], v[104:107], a[20:23], v[156:159]\n"
    "v_mfma_f32_16x16x32_bf16 v[160:163], v[104:107], v[28:31], v[160:163]\n"
    "s_waitcnt vmcnt(14)\n"
    "v_max3_u32 v184, v128, v130, v184\n"
    "v_smfmac_f32_16x16x64_bf16 v[148:151], v[128:131], a[24:31], v195\n"
    "v_smfmac_f32_16x16x64_bf16 v[152:155], v[128:131], v[32:39], v195\n"
    "s_waitcnt vmcnt(13)\n"
    "v_max3_u32 v184, v108, v110, v184\n"
    "v_swap_b32 v109, v110\n"
    "s_nop 1\n"
    "v_mfma_f32_16x16x32_bf16 v[156:159], v[108:111], a[24:27], v[156:159]\n"
    "v_mfma_f32_16x16x32_bf16 v[160:163], v[108:111], v[32:35], v[160:163]\n"
    "s_waitcnt vmcnt(12)\n"
    "v_max3_u32 v184, v112, v114, v184\n"
    "v_swap_b32 v113, v114\n"
    "s_nop 1\n"
    "v_mfma_f32_16x16x32_bf16 v[156:159], v[112:115], a[28:31], v[156:159]\n"
    "v_mfma_f32_16x16x32_bf16 v[160:163], v[112:115], v[36:39], v[160:163]\n"
    "v_cmp_eq_u32 vcc, -1, v184\n"
    "s_nop 0\n"
    "s_cbranch_vccnz .Lct_tr0_%=\n"
    ".Lct_tg0_%=:\n"
    "ds_write2_b32 v204, v148, v149 offset0:0 offset1:20\n"
    "ds_write2_b32 v204, v150, v151 offset0:40 offset1:60\n"
    "ds_write2_b32 v205, v152, v153 offset0:0 offset1:20\n"
    "ds_write2_b32 v205, v154, v155 offset0:40 offset1:60\n"
    "ds_write2_b32 v206, v156, v157 offset0:0 offset1:20\n"
    "ds_write2_b32 v206, v158, v159 offset0:40 offset1:60\n"
    "ds_write2_b32 v207, v160, v161 offset0:0 offset1:20\n"
    "ds_write2_b32 v207, v162, v163 offset0:40 offset1:60\n"
    "v_mov_b32 v188, s27\n"
    "v_mov_b32 v189, v211\n"
    "s_waitcnt vmcnt(11)\n"
    "v_lshlrev_b32 v148, 16, v186\n"
    "v_and_b32 v149, 0xffff0000, v186\n"
    "v_lshlrev_b32 v150, 16, v187\n"
    "v_and_b32 v151, 0xffff0000, v187\n"
    "s_waitcnt lgkmcnt(0)\n"
    "s_barrier\n"
    "ds_read_b128 v[164:167], v208 offset:0\n"
    "ds_read_b128 v[168:171], v208 offset:2560\n"
    "ds_read_b128 v[172:175], v208 offset:5120\n"
    "ds_read_b128 v[176:179], v208 offset:7680\n"
    "s_waitcnt lgkmcnt(2)\n"
    "v_pk_add_f32 v[164:165], v[164:165], v[168:169]\n"
    "v_pk_add_f32 v[166:167], v[166:167], v[170:171]\n"
    "s_waitcnt lgkmcnt(0)\n"
    "v_pk_add_f32 v[172:173], v[172:173], v[176:177]\n"
    "v_pk_add_f32 v[174:175], v[174:175], v[178:179]\n"
    "v_pk_add_f32 v[164:165], v[164:165], v[172:173]\n"
    "v_pk_add_f32 v[166:167], v[166:167], v[174:175]\n"
    "v_cvt_pk_bf16_f32 v164, v164, v165\n"
    "v_cvt_pk_bf16_f32 v165, v166, v167\n"
    "v_lshlrev_b32 v168, 16, v164\n"
    "v_and_b32 v169, 0xffff0000, v164\n"
    "v_lshlrev_b32 v170, 16, v165\n"
    "v_and_b32 v171, 0xffff0000, v165\n"
    "v_pk_add_f32 v[168:169], v[168:169], v[148:149]\n"
    "v_pk_add_f32 v[170:171], v[170:171], v[150:151]\n"
    "v_cvt_pk_bf16_f32 v164, v168, v169\n"
    "v_cvt_pk_bf16_f32 v165, v170, v171\n"
    "buffer_store_dwordx2 v[164:165], v209, s[0:3], s45 offen sc1\n"
    "s_add_u32 s26, s26, 1\n"
    "s_add_u32 s27, s26, 1\n"
    "s_add_u32 s28, s26, 0\n"
    "s_and_b32 s28, s28, 3\n"
    "s_lshl_b32 s28, s28, 22\n"
    "s_add_u32 s29, s26, 1\n"
    "s_and_b32 s29, s29, 3\n"
    "s_lshl_b32 s29, s29, 22\n"
    "s_add_u32 s30, s26, 3\n"
    "s_and_b32 s30, s30, 3\n"
    "s_lshl_b32 s30, s30, 22\n"
    "s_and_b32 s40, s26, 63\n"
    "s_lshl_b32 s40, s40, 7\n"
    "s_add_u32 s31, s12, s28\n"
    "s_add_u32 s32, s13, s28\n"
    "s_add_u32 s33, s15, s28\n"
    "s_add_u32 s34, s14, s28\n"
    "s_add_u32 s38, s10, s29\n"
    "s_add_u32 s39, s11, s29\n"
    "s_add_u32 s40, s16, s40\n"
    "s_add_u32 s35, s34, 4096\n"
    "s_add_u32 s36, s34, 16384\n"
    "s_add_u32 s37, s34, 20480\n"
    "s_add_u32 s43, s41, 356\n"
    "s_lshl_b32 s45, s26, 19\n"
    "s_lshl_b32 s25, s26, 11\n"
    "s_xor_b32 s25, s25, s8\n"
    "s_and_b32 s25, s25, 0x2000\n"
    "s_add_u32 s45, s45, s25\n"
    "s_add_u32 s45, s45, s9\n"
    "s_mul_i32 s46, s26, 40960\n"
    "s_cmp_eq_u32 s8, 0\n"
    "s_cselect_b32 s46, s46, s45\n"
    "v_mov_b64 v[76:77], 0\n"
    "v_mov_b64 v[78:79], 0\n"
    "v_mov_b64 v[80:81], 0\n"
    "v_mov_b64 v[82:83], 0\n"
    ".Lct_rt11_%=:\n"
    "s_memrealtime s[22:23]\n"
    "s_waitcnt lgkmcnt(0)\n"
    "s_sub_u32 s24, s43, s22\n"
    "s_cmp_lt_u32 s24, 600\n"
    "s_cbranch_scc1 .Lct_rt11_%=\n"
    ".Lct_x1_%=:\n"
    "buffer_load_dwordx2 v[190:191], off, s[0:3], s40 sc1\n"
    "s_waitcnt vmcnt(2)\n"
    "v_mov_b64 v[8:9], v[0:1]\n"
    "v_mov_b64 v[10:11], v[2:3]\n"
    "v_mov_b64 v[12:13], v[4:5]\n"
    "v_mov_b64 v[14:15], v[6:7]\n"
    "v_mov_b32_dpp v16, v0 row_shl:4 row_mask:0xf bank_mask:0xf bound_ctrl:1\n"
    "v_mov_b32_dpp v17, v1 row_shl:4 row_mask:0xf bank_mask:0xf bound_ctrl:1\n"
    "v_mov_b32_dpp v18, v2 row_shl:4 row_mask:0xf bank_mask:0xf bound_ctrl:1\n"
    "v_mov_b32_dpp v19, v3 row_shl:4 row_mask:0xf bank_mask:0xf bound_ctrl:1\n"
    "v_mov_b32_dpp v20, v4 row_shl:4 row_mask:0xf bank_mask:0xf bound_ctrl:1\n"
    "v_mov_b32_dpp v21, v5 row_shl:4 row_mask:0xf bank_mask:0xf bound_ctrl:1\n"
    "v_mov_b32_dpp v22, v6 row_shl:4 row_mask:0xf bank_mask:0xf bound_ctrl:1\n"
    "v_mov_b32_dpp v23, v7 row_shl:4 row_mask:0xf bank_mask:0xf bound_ctrl:1\n"
    "v_mov_b32_dpp v24, v0 row_shl:8 row_mask:0xf bank_mask:0xf bound_ctrl:1\n"
    "v_mov_b32_dpp v25, v1 row_shl:8 row_mask:0xf bank_mask:0xf bound_ctrl:1\n"
    "v_mov_b32_dpp v26, v2 row_shl:8 row_mask:0xf bank_mask:0xf bound_ctrl:1\n"
    "v_mov_b32_dpp v27, v3 row_shl:8 row_mask:0xf bank_mask:0xf bound_ctrl:1\n"
    "v_mov_b32_dpp v28, v4 row_shl:8 row_mask:0xf bank_mask:0xf bound_ctrl:1\n"
    "v_mov_b32_dpp v29, v5 row_shl:8 row_mask:0xf bank_mask:0xf bound_ctrl:1\n"
    "v_mov_b32_dpp v30, v6 row_shl:8 row_mask:0xf bank_mask:0xf bound_ctrl:1\n"
    "v_mov_b32_dpp v31, v7 row_shl:8 row_mask:0xf bank_mask:0xf bound_ctrl:1\n"
    "v_mov_b32_dpp v32, v0 row_shl:12 row_mask:0xf bank_mask:0xf bound_ctrl:1\n"
    "v_mov_b32_dpp v33, v1 row_shl:12 row_mask:0xf bank_mask:0xf bound_ctrl:1\n"
    "v_mov_b32_dpp v34, v2 row_shl:12 row_mask:0xf bank_mask:0xf bound_ctrl:1\n"
    "v_mov_b32_dpp v35, v3 row_shl:12 row_mask:0xf bank_mask:0xf bound_ctrl:1\n"
    "v_mov_b32_dpp v36, v4 row_shl:12 row_mask:0xf bank_mask:0xf bound_ctrl:1\n"
    "v_mov_b32_dpp v37, v5 row_shl:12 row_mask:0xf bank_mask:0xf bound_ctrl:1\n"
    "v_mov_b32_dpp v38, v6 row_shl:12 row_mask:0xf bank_mask:0xf bound_ctrl:1\n"
    "v_mov_b32_dpp v39, v7 row_shl:12 row_mask:0xf bank_mask:0xf bound_ctrl:1\n"
    "s_waitcnt vmcnt(0)\n"
    "buffer_store_dword v188, v189, s[0:3], 0 offen sc1\n"
    "v_readfirstlane_b32 s25, v191\n"
    "v_readfirstlane_b32 s41, v190\n"
    "s_cmp_eq_u32 s25, s27\n"
    "s_cbranch_scc0 .Lct_xr1_%=\n"
    ".Lct_xg1_%=:\n"
    "s_add_u32 s43, s41, 99\n"
    "s_add_u32 s44, s41, 115\n"
    ".Lct_rt12_%=:\n"
    "s_memrealtime s[22:23]\n"
    "s_waitcnt lgkmcnt(0)\n"
    "s_sub_u32 s24, s43, s22\n"
    "s_cmp_lt_u32 s24, 600\n"
    "s_cbranch_scc1 .Lct_rt12_%=\n"
    "buffer_load_dwordx4 v[40:43], v192, s[0:3], s31 offen offset:0 sc1\n"
    "buffer_load_dwordx4 v[44:47], v192, s[0:3], s31 offen offset:1024 sc1\n"
    "buffer_load_dwordx4 v[48:51], v197, s[0:3], s32 offen offset:0 sc1\n"
    "buffer_load_dwordx4 v[52:55], v197, s[0:3], s32 offen offset:256 sc1\n"
    ".Lct_rt13_%=:\n"
    "s_memrealtime s[22:23]\n"
    "s_waitcnt lgkmcnt(0)\n"
    "s_sub_u32 s24, s44, s22\n"
    "s_cmp_lt_u32 s24, 600\n"
    "s_cbranch_scc1 .Lct_rt13_%=\n"
    "buffer_load_dwordx4 v[72:75], v193, s[0:3], s33 offen sc1\n"
    "buffer_load_dwordx4 v[56:59], v192, s[0:3], s33 offen offset:0 sc1\n"
    "buffer_load_dwordx4 v[60:63], v192, s[0:3], s33 offen offset:1024 sc1\n"
    "buffer_load_dwordx4 v[64:67], v192, s[0:3], s33 offen offset:2048 sc1\n"
    "buffer_load_dwordx4 v[68:71], v192, s[0:3], s33 offen offset:3072 sc1\n"
    ".Lct_take1_%=:\n"
    "s_waitcnt vmcnt(5)\n"
    "v_max3_u32 v184, v40, v42, v40\n"
    "v_max3_u32 v184, v44, v46, v184\n"
    "v_max3_u32 v184, v48, v50, v184\n"
    "v_max3_u32 v184, v52, v54, v184\n"
    "s_waitcnt vmcnt(4)\n"
    "v_smfmac_f32_16x16x64_bf16 v[76:79], v[72:75], v[40:47], v194\n"
    "v_max3_u32 v184, v72, v74, v184\n"
    "v_smfmac_f32_16x16x64_bf16 v[80:83], v[72:75], v[48:55], v194\n"
    "s_waitcnt vmcnt(3)\n"
    "v_smfmac_f32_16x16x64_bf16 v[80:83], v[56:59], v[8:15], v194\n"
    "v_max3_u32 v184, v56, v58, v184\n"
    "v_smfmac_f32_16x16x64_bf16 v[76:79], v[56:59], a[32:39], v194\n"
    "s_waitcnt vmcnt(2)\n"
    "v_smfmac_f32_16x16x64_bf16 v[80:83], v[60:63], v[16:23], v194\n"
    "v_max3_u32 v184, v60, v62, v184\n"
    "v_smfmac_f32_16x16x64_bf16 v[76:79], v[60:63], a[40:47], v194\n"
    "s_waitcnt vmcnt(1)\n"
    "v_smfmac_f32_16x16x64_bf16 v[80:83], v[64:67], v[24:31], v194\n"
    "v_max3_u32 v184, v64, v66, v184\n"
    "v_smfmac_f32_16x16x64_bf16 v[76:79], v[64:67], a[48:55], v194\n"
    "s_waitcnt vmcnt(0)\n"
    "v_smfmac_f32_16x16x64_bf16 v[80:83], v[68:71], v[32:39], v194\n"
    "v_max3_u32 v184, v68, v70, v184\n"
    "v_smfmac_f32_16x16x64_bf16 v[76:79], v[68:71], a[56:63], v194\n"
    "v_cmp_eq_u32 vcc, -1, v184\n"
    "s_nop 0\n"
    "s_cbranch_vccnz .Lct_retry1_%=\n"
    ".Lct_go1_%=:\n"
    "s_nop 2\n"
    "v_pk_add_f32 v[80:81], v[80:81], v[82:83]\n"
    "ds_write2_b32 v201, v80, v81 offset1:20\n"
    "v_pk_add_f32 v[76:77], v[76:77], v[78:79]\n"
    "ds_write2_b32 v200, v76, v77 offset1:20\n"
    "s_waitcnt lgkmcnt(0)\n"
    "s_barrier\n"
    "ds_read_b128 v[164:167], v202 offset:0\n"
    "ds_read_b128 v[168:171], v202 offset:640\n"
    "ds_read_b128 v[172:175], v202 offset:1280\n"
    "ds_read_b128 v[176:179], v202 offset:1920\n"
    "s_waitcnt lgkmcnt(2)\n"
    "v_pk_add_f32 v[164:165], v[164:165], v[168:169]\n"
    "v_pk_add_f32 v[166:167], v[166:167], v[170:171]\n"
    "s_waitcnt lgkmcnt(0)\n"
    "v_pk_add_f32 v[172:173], v[172:173], v[176:177]\n"
    "v_pk_add_f32 v[174:175], v[174:175], v[178:179]\n"
    "v_pk_add_f32 v[164:165], v[164:165], v[172:173]\n"
    "v_pk_add_f32 v[166:167], v[166:167], v[174:175]\n"
    "v_cvt_pk_bf16_f32 v164, v164, v165\n"
    "v_cvt_pk_bf16_f32 v165, v166, v167\n"
    "v_min_u32 v164, -2, v164\n"
    "buffer_store_dwordx2 v[164:165], v203, s[0:3], s29 offen sc1\n"
    "s_sleep 2\n"
    "buffer_load_dwordx4 v[132:135], v199, s[0:3], s34 offen offset:0 sc1\n"
    "buffer_load_dwordx4 v[136:139], v199, s[0:3], s36 offen offset:0 sc1\n"
    "buffer_load_dwordx4 v[140:143], v199, s[0:3], s34 offen offset:256 sc1\n"
    "buffer_load_dwordx4 v[144:147], v199, s[0:3], s36 offen offset:256 sc1\n"
    "buffer_load_dwordx4 v[116:119], v198, s[0:3], s34 offen sc1\n"
    "buffer_load_dwordx4 v[84:87], v192, s[0:3], s34 offen offset:0 sc1\n"
    "buffer_load_dwordx4 v[88:91], v192, s[0:3], s34 offen offset:2048 sc1\n"
    "buffer_load_dwordx4 v[120:123], v198, s[0:3], s35 offen sc1\n"
    "buffer_load_dwordx4 v[92:95], v192, s[0:3], s35 offen offset:0 sc1\n"
    "buffer_load_dwordx4 v[96:99], v192, s[0:3], s35 offen offset:2048 sc1\n"
    "buffer_load_dwordx4 v[124:127], v198, s[0:3], s36 offen sc1\n"
    "buffer_load_dwordx4 v[100:103], v192, s[0:3], s36 offen offset:0 sc1\n"
    "buffer_load_dwordx4 v[104:107], v192, s[0:3], s36 offen offset:2048 sc1\n"
    "buffer_load_dwordx4 v[128:131], v198, s[0:3], s37 offen sc1\n"
    "buffer_load_dwordx4 v[108:111], v192, s[0:3], s37 offen offset:0 sc1\n"
    "buffer_load_dwordx4 v[112:115], v192, s[0:3], s37 offen offset:2048 sc1\n"
    "buffer_load_dwordx2 v[186:187], v210, s[4:7], s46 offen sc1\n"
    "v_mov_b64 v[166:167], -1\n"
    "buffer_store_dwordx2 v[166:167], v203, s[0:3], s30 offen sc1\n"
    "s_waitcnt vmcnt(17)\n"
    "v_max3_u32 v184, v132, v134, v132\n"
    "v_swap_b32 v133, v134\n"
    "s_nop 1\n"
    "v_mfma_f32_16x16x32_bf16 v[148:151], v[132:135], v[40:43], 0\n"
    "v_mfma_f32_16x16x32_bf16 v[152:155], v[132:135], v[48:51], 0\n"
    "buffer_load_dwordx4 a[0:3], v192, s[0:3], s38 offen offset:0 sc1\n"
    "s_waitcnt vmcnt(17)\n"
    "v_max3_u32 v184, v136, v138, v184\n"
    "v_swap_b32 v137, v138\n"
    "s_nop 1\n"
    "v_mfma_f32_16x16x32_bf16 v[148:151], v[136:139], v[44:47], v[148:151]\n"
    "v_mfma_f32_16x16x32_bf16 v[152:155], v[136:139], v[52:55], v[152:155]\n"
    "buffer_load_dwordx4 a[4:7], v192, s[0:3], s38 offen offset:1024 sc1\n"
    "s_waitcnt vmcnt(17)\n"
    "v_max3_u32 v184, v140, v142, v184\n"
    "v_swap_b32 v141, v142\n"
    "s_nop 1\n"
    "v_mfma_f32_16x16x32_bf16 v[156:159], v[140:143], v[40:43], 0\n"
    "v_mfma_f32_16x16x32_bf16 v[160:163], v[140:143], v[48:51], 0\n"
    "buffer_load_dwordx4 a[8:11], v192, s[0:3], s38 offen offset:2048 sc1\n"
    "s_waitcnt vmcnt(17)\n"
    "v_max3_u32 v184, v144, v146, v184\n"
    "v_swap_b32 v145, v146\n"
    "s_nop 1\n"
    "v_mfma_f32_16x16x32_bf16 v[156:159], v[144:147], v[44:47], v[156:159]\n"
    "v_mfma_f32_16x16x32_bf16 v[160:163], v[144:147], v[52:55], v[160:163]\n"
    "buffer_load_dwordx4 a[12:15], v192, s[0:3], s38 offen offset:3072 sc1\n"
    "s_waitcnt vmcnt(17)\n"
    "v_max3_u32 v184, v116, v118, v184\n"
    "v_smfmac_f32_16x16x64_bf16 v[148:151], v[116:119], a[32:39], v195\n"
    "v_smfmac_f32_16x16x64_bf16 v[152:155], v[116:119], v[8:15], v195\n"
    "buffer_load_dwordx4 a[16:19], v193, s[0:3], s38 offen offset:0 sc1\n"
    "s_waitcnt vmcnt(17)\n"
    "v_max3_u32 v184, v84, v86, v184\n"
    "v_swap_b32 v85, v86\n"
    "s_nop 1\n"
    "v_mfma_f32_16x16x32_bf16 v[156:159], v[84:87], a[32:35], v[156:159]\n"
    "v_mfma_f32_16x16x32_bf16 v[160:163], v[84:87], v[8:11], v[160:163]\n"
    "buffer_load_dwordx4 a[20:23], v193, s[0:3], s38 offen offset:1024 sc1\n"
    "s_waitcnt vmcnt(17)\n"
    "v_max3_u32 v184, v88, v90, v184\n"
    "v_swap_b32 v89, v90\n"
    "s_nop 1\n"
    "v_mfma_f32_16x16x32_bf16 v[156:159], v[88:91], a[36:39], v[156:159]\n"
    "v_mfma_f32_16x16x32_bf16 v[160:163], v[88:91], v[12:15], v[160:163]\n"
    "buffer_load_dwordx4 a[24:27], v193, s[0:3], s38 offen offset:2048 sc1\n"
    "s_waitcnt vmcnt(17)\n"
    "v_max3_u32 v184, v120, v122, v184\n"
    "v_smfmac_f32_16x16x64_bf16 v[148:151], v[120:123], a[40:47], v195\n"
    "v_smfmac_f32_16x16x64_bf16 v[152:155], v[120:123], v[16:23], v195\n"
    "buffer_load_dwordx4 a[28:31], v193, s[0:3], s38 offen offset:3072 sc1\n"
    "s_waitcnt vmcnt(17)\n"
    "v_max3_u32 v184, v92, v94, v184\n"
    "v_swap_b32 v93, v94\n"
    "s_nop 1\n"
    "v_mfma_f32_16x16x32_bf16 v[156:159], v[92:95], a[40:43], v[156:159]\n"
    "v_mfma_f32_16x16x32_bf16 v[160:163], v[92:95], v[16:19], v[160:163]\n"
    "buffer_load_dwordx4 v[0:3], v196, s[0:3], s39 offen offset:0 sc1\n"
    "s_waitcnt vmcnt(17)\n"
    "v_max3_u32 v184, v96, v98, v184\n"
    "v_swap_b32 v97, v98\n"
    "s_nop 1\n"
    "v_mfma_f32_16x16x32_bf16 v[156:159], v[96:99], a[44:47], v[156:159]\n"
    "v_mfma_f32_16x16x32_bf16 v[160:163], v[96:99], v[20:23], v[160:163]\n"
    "buffer_load_dwordx4 v[4:7], v196, s[0:3], s39 offen offset:1024 sc1\n"
    "s_waitcnt vmcnt(17)\n"
    "v_max3_u32 v184, v124, v126, v184\n"
    "v_smfmac_f32_16x16x64_bf16 v[148:151], v[124:127], a[48:55], v195\n"
    "v_smfmac_f32_16x16x64_bf16 v[152:155], v[124:127], v[24:31], v195\n"
    "s_waitcnt vmcnt(16)\n"
    "v_max3_u32 v184, v100, v102, v184\n"
    "v_swap_b32 v101, v102\n"
    "s_nop 1\n"
    "v_mfma_f32_16x16x32_bf16 v[156:159], v[100:103], a[48:51], v[156:159]\n"
    "v_mfma_f32_16x16x32_bf16 v[160:163], v[100:103], v[24:27], v[160:163]\n"
    "s_waitcnt vmcnt(15)\n"
    "v_max3_u32 v184, v104, v106, v184\n"
    "v_swap_b32 v105, v106\n"
    "s_nop 1\n"
    "v_mfma_f32_16x16x32_bf16 v[156:159], v[104:107], a[52:55], v[156:159]\n"
    "v_mfma_f32_16x16x32_bf16 v[160:163], v[104:107], v[28:31], v[160:163]\n"
    "s_waitcnt vmcnt(14)\n"
    "v_max3_u32 v184, v128, v130, v184\n"
    "v_smfmac_f32_16x16x64_bf16 v[148:151], v[128:131], a[56:63], v195\n"
    "v_smfmac_f32_16x16x64_bf16 v[152:155], v[128:131], v[32:39], v195\n"
    "s_waitcnt vmcnt(13)\n"
    "v_max3_u32 v184, v108, v110, v184\n"
    "v_swap_b32 v109, v110\n"
    "s_nop 1\n"
    "v_mfma_f32_16x16x32_bf16 v[156:159], v[108:111], a[56:59], v[156:159]\n"
    "v_mfma_f32_16x16x32_bf16 v[160:163], v[108:111], v[32:35], v[160:163]\n"
    "s_waitcnt vmcnt(12)\n"
    "v_max3_u32 v184, v112, v114, v184\n"
    "v_swap_b32 v113, v114\n"
    "s_nop 1\n"
    "v_mfma_f32_16x16x32_bf16 v[156:159], v[112:115], a[60:63], v[156:159]\n"
    "v_mfma_f32_16x16x32_bf16 v[160:163], v[112:115], v[36:39], v[160:163]\n"
    "v_cmp_eq_u32 vcc, -1, v184\n"
    "s_nop 0\n"
    "s_cbranch_vccnz .Lct_tr1_%=\n"
    ".Lct_tg1_%=:\n"
    "ds_write2_b32 v204, v148, v149 offset0:0 offset1:20\n"
    "ds_write2_b32 v204, v150, v151 offset0:40 offset1:60\n"
    "ds_write2_b32 v205, v152, v153 offset0:0 offset1:20\n"
    "ds_write2_b32 v205, v154, v155 offset0:40 offset1:60\n"
    "ds_write2_b32 v206, v156, v157 offset0:0 offset1:20\n"
    "ds_write2_b32 v206, v158, v159 offset0:40 offset1:60\n"
    "ds_write2_b32 v207, v160, v161 offset0:0 offset1:20\n"
    "ds_write2_b32 v207, v162, v163 offset0:40 offset1:60\n"
    "v_mov_b32 v188, s27\n"
    "v_mov_b32 v189, v211\n"
    "s_waitcnt vmcnt(11)\n"
    "v_lshlrev_b32 v148, 16, v186\n"
    "v_and_b32 v149, 0xffff0000, v186\n"
    "v_lshlrev_b32 v150, 16, v187\n"
    "v_and_b32 v151, 0xffff0000, v187\n"
    "s_waitcnt lgkmcnt(0)\n"
    "s_barrier\n"
    "ds_read_b128 v[164:167], v208 offset:0\n"
    "ds_read_b128 v[168:171], v208 offset:2560\n"
    "ds_read_b128 v[172:175], v208 offset:5120\n"
    "ds_read_b128 v[176:179], v208 offset:7680\n"
    "s_waitcnt lgkmcnt(2)\n"
    "v_pk_add_f32 v[164:165], v[164:165], v[168:169]\n"
    "v_pk_add_f32 v[166:167], v[166:167], v[170:171]\n"
    "s_waitcnt lgkmcnt(0)\n"
    "v_pk_add_f32 v[172:173], v[172:173], v[176:177]\n"
    "v_pk_add_f32 v[174:175], v[174:175], v[178:179]\n"
    "v_pk_add_f32 v[164:165], v[164:165], v[172:173]\n"
    "v_pk_add_f32 v[166:167], v[166:167], v[174:175]\n"
    "v_cvt_pk_bf16_f32 v164, v164, v165\n"
    "v_cvt_pk_bf16_f32 v165, v166, v167\n"
    "v_lshlrev_b32 v168, 16, v164\n"
    "v_and_b32 v169, 0xffff0000, v164\n"
    "v_lshlrev_b32 v170, 16, v165\n"
    "v_and_b32 v171, 0xffff0000, v165\n"
    "v_pk_add_f32 v[168:169], v[168:169], v[148:149]\n"
    "v_pk_add_f32 v[170:171], v[170:171], v[150:151]\n"
    "v_cvt_pk_bf16_f32 v164, v168, v169\n"
    "v_cvt_pk_bf16_f32 v165, v170, v171\n"
    "buffer_store_dwordx2 v[164:165], v209, s[0:3], s45 offen sc1\n"
    "s_add_u32 s26, s26, 1\n"
    "s_add_u32 s27, s26, 1\n"
    "s_add_u32 s28, s26, 0\n"
    "s_and_b32 s28, s28, 3\n"
    "s_lshl_b32 s28, s28, 22\n"
    "s_add_u32 s29, s26, 1\n"
    "s_and_b32 s29, s29, 3\n"
    "s_lshl_b32 s29, s29, 22\n"
    "s_add_u32 s30, s26, 3\n"
    "s_and_b32 s30, s30, 3\n"
    "s_lshl_b32 s30, s30, 22\n"
    "s_and_b32 s40, s26, 63\n"
    "s_lshl_b32 s40, s40, 7\n"
    "s_add_u32 s31, s12, s28\n"
    "s_add_u32 s32, s13, s28\n"
    "s_add_u32 s33, s15, s28\n"
    "s_add_u32 s34, s14, s28\n"
    "s_add_u32 s38, s10, s29\n"
    "s_add_u32 s39, s11, s29\n"
    "s_add_u32 s40, s16, s40\n"
    "s_add_u32 s35, s34, 4096\n"
    "s_add_u32 s36, s34, 16384\n"
    "s_add_u32 s37, s34, 20480\n"
    "s_add_u32 s43, s41, 356\n"
    "s_lshl_b32 s45, s26, 19\n"
    "s_lshl_b32 s25, s26, 11\n"
    "s_xor_b32 s25, s25, s8\n"
    "s_and_b32 s25, s25, 0x2000\n"
    "s_add_u32 s45, s45, s25\n"
    "s_add_u32 s45, s45, s9\n"
    "s_mul_i32 s46, s26, 40960\n"
    "s_cmp_eq_u32 s8, 0\n"
    "s_cselect_b32 s46, s46, s45\n"
    "v_mov_b64 v[76:77], 0\n"
    "v_mov_b64 v[78:79], 0\n"
    "v_mov_b64 v[80:81], 0\n"
    "v_mov_b64 v[82:83], 0\n"
    "s_cmp_lt_u32 s26, 64\n"
    "s_cbranch_scc1 .Lct_top_%=\n"
    "s_waitcnt vmcnt(0)\n"
    "buffer_store_dword v188, v189, s[0:3], 0 offen sc1\n"
    "s_waitcnt vmcnt(0) lgkmcnt(0)\n"
    "s_endpgm\n"
    ".Lct_xr0_%=:\n"
    "s_add_u32 s20, s20, 1\n"
    "s_cmp_lt_u32 s20, 0x40000\n"
    "s_cbranch_scc1 .Lct_sp8_%=\n"
    "v_mov_b32 v184, 42\n"
    "s_mov_b32 s24, 33728\n"
    "buffer_store_dword v184, off, s[0:3], s24 sc1\n"
    "s_waitcnt vmcnt(0)\n"
    "s_branch .Lct_xg0_%=\n"
    ".Lct_sp8_%=:\n"
    "s_sleep 4\n"
    "s_branch .Lct_x0_%=\n"
    ".Lct_retry0_%=:\n"
    "s_add_u32 s20, s20, 1\n"
    "s_cmp_lt_u32 s20, 0x40000\n"
    "s_cbranch_scc1 .Lct_sp9_%=\n"
    "v_mov_b32 v184, 3\n"
    "s_mov_b32 s24, 33728\n"
    "buffer_store_dword v184, off, s[0:3], s24 sc1\n"
    "s_waitcnt vmcnt(0)\n"
    "s_branch .Lct_go0_%=\n"
    ".Lct_sp9_%=:\n"
    "s_waitcnt vmcnt(0)\n"
    "v_mov_b64 v[76:77], 0\n"
    "v_mov_b64 v[78:79], 0\n"
    "v_mov_b64 v[80:81], 0\n"
    "v_mov_b64 v[82:83], 0\n"
    "buffer_load_dwordx4 v[40:43], v192, s[0:3], s31 offen offset:0 sc1\n"
    "buffer_load_dwordx4 v[44:47], v192, s[0:3], s31 offen offset:1024 sc1\n"
    "buffer_load_dwordx4 v[48:51], v197, s[0:3], s32 offen offset:0 sc1\n"
    "buffer_load_dwordx4 v[52:55], v197, s[0:3], s32 offen offset:256 sc1\n"
    "buffer_load_dwordx4 v[72:75], v193, s[0:3], s33 offen sc1\n"
    "buffer_load_dwordx4 v[56:59], v192, s[0:3], s33 offen offset:0 sc1\n"
    "buffer_load_dwordx4 v[60:63], v192, s[0:3], s33 offen offset:1024 sc1\n"
    "buffer_load_dwordx4 v[64:67], v192, s[0:3], s33 offen offset:2048 sc1\n"
    "buffer_load_dwordx4 v[68:71], v192, s[0:3], s33 offen offset:3072 sc1\n"
    "s_waitcnt vmcnt(0)\n"
    "s_branch .Lct_take0_%=\n"
    ".Lct_tr0_%=:\n"
    "s_add_u32 s20, s20, 1\n"
    "s_cmp_lt_u32 s20, 0x40000\n"
    "s_cbranch_scc1 .Lct_sp10_%=\n"
    "v_mov_b32 v184, 4\n"
    "s_mov_b32 s24, 33728\n"
    "buffer_store_dword v184, off, s[0:3], s24 sc1\n"
    "s_waitcnt vmcnt(0)\n"
    "s_branch .Lct_tg0_%=\n"
    ".Lct_sp10_%=:\n"
    "buffer_load_dwordx4 v[132:135], v199, s[0:3], s34 offen offset:0 sc1\n"
    "buffer_load_dwordx4 v[136:139], v199, s[0:3], s36 offen offset:0 sc1\n"
    "buffer_load_dwordx4 v[140:143], v199, s[0:3], s34 offen offset:256 sc1\n"
    "buffer_load_dwordx4 v[144:147], v199, s[0:3], s36 offen offset:256 sc1\n"
    "buffer_load_dwordx4 v[116:119], v198, s[0:3], s34 offen sc1\n"
    "buffer_load_dwordx4 v[84:87], v192, s[0:3], s34 offen offset:0 sc1\n"
    "buffer_load_dwordx4 v[88:91], v192, s[0:3], s34 offen offset:2048 sc1\n"
    "buffer_load_dwordx4 v[120:123], v198, s[0:3], s35 offen sc1\n"
    "buffer_load_dwordx4 v[92:95], v192, s[0:3], s35 offen offset:0 sc1\n"
    "buffer_load_dwordx4 v[96:99], v192, s[0:3], s35 offen offset:2048 sc1\n"
    "buffer_load_dwordx4 v[124:127], v198, s[0:3], s36 offen sc1\n"
    "buffer_load_dwordx4 v[100:103], v192, s[0:3], s36 offen offset:0 sc1\n"
    "buffer_load_dwordx4 v[104:107], v192, s[0:3], s36 offen offset:2048 sc1\n"
    "buffer_load_dwordx4 v[128:131], v198, s[0:3], s37 offen sc1\n"
    "buffer_load_dwordx4 v[108:111], v192, s[0:3], s37 offen offset:0 sc1\n"
    "buffer_load_dwordx4 v[112:115], v192, s[0:3], s37 offen offset:2048 sc1\n"
    "s_waitcnt vmcnt(0)\n"
    "v_max3_u32 v184, v132, v134, v132\n"
    "v_swap_b32 v133, v134\n"
    "s_nop 1\n"
    "v_mfma_f32_16x16x32_bf16 v[148:151], v[132:135], v[40:43], 0\n"
    "v_mfma_f32_16x16x32_bf16 v[152:155], v[132:135], v[48:51], 0\n"
    "v_max3_u32 v184, v136, v138, v184\n"
    "v_swap_b32 v137, v138\n"
    "s_nop 1\n"
    "v_mfma_f32_16x16x32_bf16 v[148:151], v[136:139], v[44:47], v[148:151]\n"
    "v_mfma_f32_16x16x32_bf16 v[152:155], v[136:139], v[52:55], v[152:155]\n"
    "v_max3_u32 v184, v140, v142, v184\n"
    "v_swap_b32 v141, v142\n"
    "s_nop 1\n"
    "v_mfma_f32_16x16x32_bf16 v[156:159], v[140:143], v[40:43], 0\n"
    "v_mfma_f32_16x16x32_bf16 v[160:163], v[140:143], v[48:51], 0\n"
    "v_max3_u32 v184, v144, v146, v184\n"
    "v_swap_b32 v145, v146\n"
    "s_nop 1\n"
    "v_mfma_f32_16x16x32_bf16 v[156:159], v[144:147], v[44:47], v[156:159]\n"
    "v_mfma_f32_16x16x32_bf16 v[160:163], v[144:147], v[52:55], v[160:163]\n"
    "v_max3_u32 v184, v116, v118, v184\n"
    "v_smfmac_f32_16x16x64_bf16 v[148:151], v[116:119], a[0:7], v195\n"
    "v_smfmac_f32_16x16x64_bf16 v[152:155], v[116:119], v[8:15], v195\n"
    "v_max3_u32 v184, v84, v86, v184\n"
    "v_swap_b32 v85, v86\n"
    "s_nop 1\n"
    "v_mfma_f32_16x16x32_bf16 v[156:159], v[84:87], a[0:3], v[156:159]\n"
    "v_mfma_f32_16x16x32_bf16 v[160:163], v[84:87], v[8:11], v[160:163]\n"
    "v_max3_u32 v184, v88, v90, v184\n"
    "v_swap_b32 v89, v90\n"
    "s_nop 1\n"
    "v_mfma_f32_16x16x32_bf16 v[156:159], v[88:91], a[4:7], v[156:159]\n"
    "v_mfma_f32_16x16x32_bf16 v[160:163], v[88:91], v[12:15], v[160:163]\n"
    "v_max3_u32 v184, v120, v122, v184\n"
    "v_smfmac_f32_16x16x64_bf16 v[148:151], v[120:123], a[8:15], v195\n"
    "v_smfmac_f32_16x16x64_bf16 v[152:155], v[120:123], v[16:23], v195\n"
    "v_max3_u32 v184, v92, v94, v184\n"
    "v_swap_b32 v93, v94\n"
    "s_nop 1\n"
    "v_mfma_f32_16x16x32_bf16 v[156:159], v[92:95], a[8:11], v[156:159]\n"
    "v_mfma_f32_16x16x32_bf16 v[160:163], v[92:95], v[16:19], v[160:163]\n"
    "v_max3_u32 v184, v96, v98, v184\n"
    "v_swap_b32 v97, v98\n"
    "s_nop 1\n"
    "v_mfma_f32_16x16x32_bf16 v[156:159], v[96:99], a[12:15], v[156:159]\n"
    "v_mfma_f32_16x16x32_bf16 v[160:163], v[96:99], v[20:23], v[160:163]\n"
    "v_max3_u32 v184, v124, v126, v184\n"
    "v_smfmac_f32_16x16x64_bf16 v[148:151], v[124:127], a[16:23], v195\n"
    "v_smfmac_f32_16x16x64_bf16 v[152:155], v[124:127], v[24:31], v195\n"
    "v_max3_u32 v184, v100, v102, v184\n"
    "v_swap_b32 v101, v102\n"
    "s_nop 1\n"
    "v_mfma_f32_16x16x32_bf16 v[156:159], v[100:103], a[16:19], v[156:159]\n"
    "v_mfma_f32_16x16x32_bf16 v[160:163], v[100:103], v[24:27], v[160:163]\n"
    "v_max3_u32 v184, v104, v106, v184\n"
    "v_swap_b32 v105, v106\n"
    "s_nop 1\n"
    "v_mfma_f32_16x16x32_bf16 v[156:159], v[104:107], a[20:23], v[156:159]\n"
    "v_mfma_f32_16x16x32_bf16 v[160:163], v[104:107], v[28:31], v[160:163]\n"
    "v_max3_u32 v184, v128, v130, v184\n"
    "v_smfmac_f32_16x16x64_bf16 v[148:151], v[128:131], a[24:31], v195\n"
    "v_smfmac_f32_16x16x64_bf16 v[152:155], v[128:131], v[32:39], v195\n"
    "v_max3_u32 v184, v108, v110, v184\n"
    "v_swap_b32 v109, v110\n"
    "s_nop 1\n"
    "v_mfma_f32_16x16x32_bf16 v[156:159], v[108:111], a[24:27], v[156:159]\n"
    "v_mfma_f32_16x16x32_bf16 v[160:163], v[108:111], v[32:35], v[160:163]\n"
    "v_max3_u32 v184, v112, v114, v184\n"
    "v_swap_b32 v113, v114\n"
    "s_nop 1\n"
    "v_mfma_f32_16x16x32_bf16 v[156:159], v[112:115], a[28:31], v[156:159]\n"
    "v_mfma_f32_16x16x32_bf16 v[160:163], v[112:115], v[36:39], v[160:163]\n"
    "v_cmp_eq_u32 vcc, -1, v184\n"
    "s_nop 0\n"
    "s_cbranch_vccnz .Lct_tr0_%=\n"
    "s_nop 7\n"
    "s_branch .Lct_tg0_%=\n"
    ".Lct_xr1_%=:\n"
    "s_add_u32 s20, s20, 1\n"
    "s_cmp_lt_u32 s20, 0x40000\n"
    "s_cbranch_scc1 .Lct_sp14_%=\n"
    "v_mov_b32 v184, 42\n"
    "s_mov_b32 s24, 33728\n"
    "buffer_store_dword v184, off, s[0:3], s24 sc1\n"
    "s_waitcnt vmcnt(0)\n"
    "s_branch .Lct_xg1_%=\n"
    ".Lct_sp14_%=:\n"
    "s_sleep 4\n"
    "s_branch .Lct_x1_%=\n"
    ".Lct_retry1_%=:\n"
    "s_add_u32 s20, s20, 1\n"
    "s_cmp_lt_u32 s20, 0x40000\n"
    "s_cbranch_scc1 .Lct_sp15_%=\n"
    "v_mov_b32 v184, 3\n"
    "s_mov_b32 s24, 33728\n"
    "buffer_store_dword v184, off, s[0:3], s24 sc1\n"
    "s_waitcnt vmcnt(0)\n"
    "s_branch .Lct_go1_%=\n"
    ".Lct_sp15_%=:\n"
    "s_waitcnt vmcnt(0)\n"
    "v_mov_b64 v[76:77], 0\n"
    "v_mov_b64 v[78:79], 0\n"
    "v_mov_b64 v[80:81], 0\n"
    "v_mov_b64 v[82:83], 0\n"
    "buffer_load_dwordx4 v[40:43], v192, s[0:3], s31 offen offset:0 sc1\n"
    "buffer_load_dwordx4 v[44:47], v192, s[0:3], s31 offen offset:1024 sc1\n"
    "buffer_load_dwordx4 v[48:51], v197, s[0:3], s32 offen offset:0 sc1\n"
    "buffer_load_dwordx4 v[52:55], v197, s[0:3], s32 offen offset:256 sc1\n"
    "buffer_load_dwordx4 v[72:75], v193, s[0:3], s33 offen sc1\n"
    "buffer_load_dwordx4 v[56:59], v192, s[0:3], s33 offen offset:0 sc1\n"
    "buffer_load_dwordx4 v[60:63], v192, s[0:3], s33 offen offset:1024 sc1\n"
    "buffer_load_dwordx4 v[64:67], v192, s[0:3], s33 offen offset:2048 sc1\n"
    "buffer_load_dwordx4 v[68:71], v192, s[0:3], s33 offen offset:3072 sc1\n"
    "s_waitcnt vmcnt(0)\n"
    "s_branch .Lct_take1_%=\n"
    ".Lct_tr1_%=:\n"
    "s_add_u32 s20, s20, 1\n"
    "s_cmp_lt_u32 s20, 0x40000\n"
    "s_cbranch_scc1 .Lct_sp16_%=\n"
    "v_mov_b32 v184, 4\n"
    "s_mov_b32 s24, 33728\n"
    "buffer_store_dword v184, off, s[0:3], s24 sc1\n"
    "s_waitcnt vmcnt(0)\n"
    "s_branch .Lct_tg1_%=\n"
    ".Lct_sp16_%=:\n"
    "buffer_load_dwordx4 v[132:135], v199, s[0:3], s34 offen offset:0 sc1\n"
    "buffer_load_dwordx4 v[136:139], v199, s[0:3], s36 offen offset:0 sc1\n"
    "buffer_load_dwordx4 v[140:143], v199, s[0:3], s34 offen offset:256 sc1\n"
    "buffer_load_dwordx4 v[144:147], v199, s[0:3], s36 offen offset:256 sc1\n"
    "buffer_load_dwordx4 v[116:119], v198, s[0:3], s34 offen sc1\n"
    "buffer_load_dwordx4 v[84:87], v192, s[0:3], s34 offen offset:0 sc1\n"
    "buffer_load_dwordx4 v[88:91], v192, s[0:3], s34 offen offset:2048 sc1\n"
    "buffer_load_dwordx4 v[120:123], v198, s[0:3], s35 offen sc1\n"
    "buffer_load_dwordx4 v[92:95], v192, s[0:3], s35 offen offset:0 sc1\n"
    "buffer_load_dwordx4 v[96:99], v192, s[0:3], s35 offen offset:2048 sc1\n"
    "buffer_load_dwordx4 v[124:127], v198, s[0:3], s36 offen sc1\n"
    "buffer_load_dwordx4 v[100:103], v192, s[0:3], s36 offen offset:0 sc1\n"
    "buffer_load_dwordx4 v[104:107], v192, s[0:3], s36 offen offset:2048 sc1\n"
    "buffer_load_dwordx4 v[128:131], v198, s[0:3], s37 offen sc1\n"
    "buffer_load_dwordx4 v[108:111], v192, s[0:3], s37 offen offset:0 sc1\n"
    "buffer_load_dwordx4 v[112:115], v192, s[0:3], s37 offen offset:2048 sc1\n"
    "s_waitcnt vmcnt(0)\n"
    "v_max3_u32 v184, v132, v134, v132\n"
    "v_swap_b32 v133, v134\n"
    "s_nop 1\n"
    "v_mfma_f32_16x16x32_bf16 v[148:151], v[132:135], v[40:43], 0\n"
    "v_mfma_f32_16x16x32_bf16 v[152:155], v[132:135], v[48:51], 0\n"
    "v_max3_u32 v184, v136, v138, v184\n"
    "v_swap_b32 v137, v138\n"
    "s_nop 1\n"
    "v_mfma_f32_16x16x32_bf16 v[148:151], v[136:139], v[44:47], v[148:151]\n"
    "v_mfma_f32_16x16x32_bf16 v[152:155], v[136:139], v[52:55], v[152:155]\n"
    "v_max3_u32 v184, v140, v142, v184\n"
    "v_swap_b32 v141, v142\n"
    "s_nop 1\n"
    "v_mfma_f32_16x16x32_bf16 v[156:159], v[140:143], v[40:43], 0\n"
    "v_mfma_f32_16x16x32_bf16 v[160:163], v[140:143], v[48:51], 0\n"
    "v_max3_u32 v184, v144, v146, v184\n"
    "v_swap_b32 v145, v146\n"
    "s_nop 1\n"
    "v_mfma_f32_16x16x32_bf16 v[156:159], v[144:147], v[44:47], v[156:159]\n"
    "v_mfma_f32_16x16x32_bf16 v[160:163], v[144:147], v[52:55], v[160:163]\n"
    "v_max3_u32 v184, v116, v118, v184\n"
    "v_smfmac_f32_16x16x64_bf16 v[148:151], v[116:119], a[32:39], v195\n"
    "v_smfmac_f32_16x16x64_bf16 v[152:155], v[116:119], v[8:15], v195\n"
    "v_max3_u32 v184, v84, v86, v184\n"
    "v_swap_b32 v85, v86\n"
    "s_nop 1\n"
    "v_mfma_f32_16x16x32_bf16 v[156:159], v[84:87], a[32:35], v[156:159]\n"
    "v_mfma_f32_16x16x32_bf16 v[160:163], v[84:87], v[8:11], v[160:163]\n"
    "v_max3_u32 v184, v88, v90, v184\n"
    "v_swap_b32 v89, v90\n"
    "s_nop 1\n"
    "v_mfma_f32_16x16x32_bf16 v[156:159], v[88:91], a[36:39], v[156:159]\n"
    "v_mfma_f32_16x16x32_bf16 v[160:163], v[88:91], v[12:15], v[160:163]\n"
    "v_max3_u32 v184, v120, v122, v184\n"
    "v_smfmac_f32_16x16x64_bf16 v[148:151], v[120:123], a[40:47], v195\n"
    "v_smfmac_f32_16x16x64_bf16 v[152:155], v[120:123], v[16:23], v195\n"
    "v_max3_u32 v184, v92, v94, v184\n"
    "v_swap_b32 v93, v94\n"
    "s_nop 1\n"
    "v_mfma_f32_16x16x32_bf16 v[156:159], v[92:95], a[40:43], v[156:159]\n"
    "v_mfma_f32_16x16x32_bf16 v[160:163], v[92:95], v[16:19], v[160:163]\n"
    "v_max3_u32 v184, v96, v98, v184\n"
    "v_swap_b32 v97, v98\n"
    "s_nop 1\n"
    "v_mfma_f32_16x16x32_bf16 v[156:159], v[96:99], a[44:47], v[156:159]\n"
    "v_mfma_f32_16x16x32_bf16 v[160:163], v[96:99], v[20:23], v[160:163]\n"
    "v_max3_u32 v184, v124, v126, v184\n"
    "v_smfmac_f32_16x16x64_bf16 v[148:151], v[124:127], a[48:55], v195\n"
    "v_smfmac_f32_16x16x64_bf16 v[152:155], v[124:127], v[24:31], v195\n"
    "v_max3_u32 v184, v100, v102, v184\n"
    "v_swap_b32 v101, v102\n"
    "s_nop 1\n"
    "v_mfma_f32_16x16x32_bf16 v[156:159], v[100:103], a[48:51], v[156:159]\n"
    "v_mfma_f32_16x16x32_bf16 v[160:163], v[100:103], v[24:27], v[160:163]\n"
    "v_max3_u32 v184, v104, v106, v184\n"
    "v_swap_b32 v105, v106\n"
    "s_nop 1\n"
    "v_mfma_f32_16x16x32_bf16 v[156:159], v[104:107], a[52:55], v[156:159]\n"
    "v_mfma_f32_16x16x32_bf16 v[160:163], v[104:107], v[28:31], v[160:163]\n"
    "v_max3_u32 v184, v128, v130, v184\n"
    "v_smfmac_f32_16x16x64_bf16 v[148:151], v[128:131], a[56:63], v195\n"
    "v_smfmac_f32_16x16x64_bf16 v[152:155], v[128:131], v[32:39], v195\n"
    "v_max3_u32 v184, v108, v110, v184\n"
    "v_swap_b32 v109, v110\n"
    "s_nop 1\n"
    "v_mfma_f32_16x16x32_bf16 v[156:159], v[108:111], a[56:59], v[156:159]\n"
    "v_mfma_f32_16x16x32_bf16 v[160:163], v[108:111], v[32:35], v[160:163]\n"
    "v_max3_u32 v184, v112, v114, v184\n"
    "v_swap_b32 v113, v114\n"
    "s_nop 1\n"
    "v_mfma_f32_16x16x32_bf16 v[156:159], v[112:115], a[60:63], v[156:159]\n"
    "v_mfma_f32_16x16x32_bf16 v[160:163], v[112:115], v[36:39], v[160:163]\n"
    "v_cmp_eq_u32 vcc, -1, v184\n"
    "s_nop 0\n"
    "s_cbranch_vccnz .Lct_tr1_%=\n"
    "s_nop 7\n"
    "s_branch .Lct_tg1_%=\n"
    :: "{s[0:3]}"(s0_), "{s[4:7]}"(s4_), "{s[8:11]}"(s8_), "{s[12:15]}"(s12_), "{s[16:19]}"(s16_), "{v[192:195]}"(v192_), "{v[196:199]}"(v196_), "{v[200:203]}"(v200_), "{v[204:207]}"(v204_), "{v[208:211]}"(v208_) : "memory", "v255", "a255", "s95");
    __builtin_unreachable();
}
DEV void qw_role(const MegaArgs& A, const int L, const int h, const int rep) {
    ROLE_HEAD;
    const u32x4 s0_ = {RFL(rx[0]), RFL(rx[1]), RFL(rx[2]), RFL(rx[3])};
    const u32x4 s4_ = {RFL(lo(A.Wq + h * 192 + 48 * w + 16)), RFL(hi(A.Wq + h * 192 + 48 * w + 16)), RFL(WBYTES - (h * 192 + 48 * w + 16) * 2), RFL(0x20000u)};
    const u32x4 s8_ = {RFL(lo(A.Wv + h * 192 + 48 * w + 32)), RFL(hi(A.Wv + h * 192 + 48 * w + 32)), RFL(WBYTES - (h * 192 + 48 * w + 32) * 2), RFL(0x20000u)};
    const u32x4 s12_ = {RFL(w), RFL(REC0 + w * 2048), RFL(QT0 + h * 32768), RFL((A.layers & 2) ? 64 : 0)};
    const u32x4 s16_ = {RFL(xl(Z_CT + (h & 7) * 256, L)), RFL(xl(Z_XN + rep * 2048, 1)), RFL(DBG0 + ((L * 128 + 112 + (h & 15)) * 4 + w) * 2048), RFL(0u)};
    const u32x4 v0_ = {(unsigned)((l >> 2) * 12288 + (l & 3) * 16), (unsigned)((16 + (l >> 2)) * 12288 + (l & 3) * 16 - 1024), (unsigned)((l >> 1) * 12288 + (l & 1) * 16), (unsigned)(w * 40960 + (8 * lq + ((l >> 2) & 1) + ((l >> 1) & 4)) * 64 + (l & 3) * 8)};
    const u32x4 v4_ = {(unsigned)(w * 40960 + 2048 + (8 * lq + ((l >> 2) & 1) + ((l >> 1) & 4)) * 32 + (l & 3) * 8), (unsigned)(0u), (unsigned)(0u), (unsigned)(0u)};
    const u32x4 v64_ = {(unsigned)(l * 16), (unsigned)(l * 16 + 40960), (unsigned)(l * 16 + w * 2048), (unsigned)(l * 16 + w * 1024 + 16384)};
    const u32x4 v68_ = {(unsigned)(l < 20 ? (l >> 2) * 128 + (l & 3) * 4 : 0), (unsigned)(l == 0 ? xl(Z_QW + h * 128 + w * 4, 0) : OOB), (unsigned)(0u), (unsigned)(0u)};
    asm volatile(
    "s_mov_b32 s20, 0\n"
    "s_mul_i32 s32, s12, 40960\n"
    "s_mov_b32 s38, -393216\n"
    "s_add_u32 s38, s38, 393216\n"
    "s_add_u32 m0, s32, 0\n"
    "s_nop 0\n"
    "buffer_load_dwordx4 v0, s[4:7], s38 offen offset:0 lds\n"
    "buffer_load_dwordx4 v1, s[4:7], s38 offen offset:1024 lds\n"
    "s_add_u32 m0, s32, 2048\n"
    "s_nop 0\n"
    "buffer_load_dwordx4 v2, s[8:11], s38 offen lds\n"
    "s_add_u32 s38, s38, 393216\n"
    "s_add_u32 m0, s32, 3072\n"
    "s_nop 0\n"
    "buffer_load_dwordx4 v0, s[4:7], s38 offen offset:0 lds\n"
    "buffer_load_dwordx4 v1, s[4:7], s38 offen offset:1024 lds\n"
    "s_add_u32 m0, s32, 5120\n"
    "s_nop 0\n"
    "buffer_load_dwordx4 v2, s[8:11], s38 offen lds\n"
    "s_add_u32 s38, s38, 393216\n"
    "s_add_u32 m0, s32, 6144\n"
    "s_nop 0\n"
    "buffer_load_dwordx4 v0, s[4:7], s38 offen offset:0 lds\n"
    "buffer_load_dwordx4 v1, s[4:7], s38 offen offset:1024 lds\n"
    "s_add_u32 m0, s32, 8192\n"
    "s_nop 0\n"
    "buffer_load_dwordx4 v2, s[8:11], s38 offen lds\n"
    "s_add_u32 s38, s38, 393216\n"
    "s_add_u32 m0, s32, 9216\n"
    "s_nop 0\n"
    "buffer_load_dwordx4 v0, s[4:7], s38 offen offset:0 lds\n"
    "buffer_load_dwordx4 v1, s[4:7], s38 offen offset:1024 lds\n"
    "s_add_u32 m0, s32, 11264\n"
    "s_nop 0\n"
    "buffer_load_dwordx4 v2, s[8:11], s38 offen lds\n"
    "s_add_u32 s38, s38, 393216\n"
    "s_add_u32 m0, s32, 12288\n"
    "s_nop 0\n"
    "buffer_load_dwordx4 v0, s[4:7], s38 offen offset:0 lds\n"
    "buffer_load_dwordx4 v1, s[4:7], s38 offen offset:1024 lds\n"
    "s_add_u32 m0, s32, 14336\n"
    "s_nop 0\n"
    "buffer_load_dwordx4 v2, s[8:11], s38 offen lds\n"
    "s_add_u32 s38, s38, 393216\n"
    "s_add_u32 m0, s32, 15360\n"
    "s_nop 0\n"
    "buffer_load_dwordx4 v0, s[4:7], s38 offen offset:0 lds\n"
    "buffer_load_dwordx4 v1, s[4:7], s38 offen offset:1024 lds\n"
    "s_add_u32 m0, s32, 17408\n"
    "s_nop 0\n"
    "buffer_load_dwordx4 v2, s[8:11], s38 offen lds\n"
    "s_add_u32 s38, s38, 393216\n"
    "s_add_u32 m0, s32, 18432\n"
    "s_nop 0\n"
    "buffer_load_dwordx4 v0, s[4:7], s38 offen offset:0 lds\n"
    "buffer_load_dwordx4 v1, s[4:7], s38 offen offset:1024 lds\n"
    "s_add_u32 m0, s32, 20480\n"
    "s_nop 0\n"
    "buffer_load_dwordx4 v2, s[8:11], s38 offen lds\n"
    "s_add_u32 s38, s38, 393216\n"
    "s_add_u32 m0, s32, 21504\n"
    "s_nop 0\n"
    "buffer_load_dwordx4 v0, s[4:7], s38 offen offset:0 lds\n"
    "buffer_load_dwordx4 v1, s[4:7], s38 offen offset:1024 lds\n"
    "s_add_u32 m0, s32, 23552\n"
    "s_nop 0\n"
    "buffer_load_dwordx4 v2, s[8:11], s38 offen lds\n"
    "s_add_u32 s38, s38, 393216\n"
    "s_add_u32 m0, s32, 24576\n"
    "s_nop 0\n"
    "buffer_load_dwordx4 v0, s[4:7], s38 offen offset:0 lds\n"
    "buffer_load_dwordx4 v1, s[4:7], s38 offen offset:1024 lds\n"
    "s_add_u32 m0, s32, 26624\n"
    "s_nop 0\n"
    "buffer_load_dwordx4 v2, s[8:11], s38 offen lds\n"
    "s_add_u32 s38, s38, 393216\n"
    "s_add_u32 m0, s32, 27648\n"
    "s_nop 0\n"
    "buffer_load_dwordx4 v0, s[4:7], s38 offen offset:0 lds\n"
    "buffer_load_dwordx4 v1, s[4:7], s38 offen offset:1024 lds\n"
    "s_add_u32 m0, s32, 29696\n"
    "s_nop 0\n"
    "buffer_load_dwordx4 v2, s[8:11], s38 offen lds\n"
    "s_add_u32 s38, s38, 393216\n"
    "s_add_u32 m0, s32, 30720\n"
    "s_nop 0\n"
    "buffer_load_dwordx4 v0, s[4:7], s38 offen offset:0 lds\n"
    "buffer_load_dwordx4 v1, s[4:7], s38 offen offset:1024 lds\n"
    "s_add_u32 m0, s32, 32768\n"
    "s_nop 0\n"
    "buffer_load_dwordx4 v2, s[8:11], s38 offen lds\n"
    "s_add_u32 s38, s38, 393216\n"
    "s_add_u32 m0, s32, 33792\n"
    "s_nop 0\n"
    "buffer_load_dwordx4 v0, s[4:7], s38 offen offset:0 lds\n"
    "buffer_load_dwordx4 v1, s[4:7], s38 offen offset:1024 lds\n"
    "s_add_u32 m0, s32, 35840\n"
    "s_nop 0\n"
    "buffer_load_dwordx4 v2, s[8:11], s38 offen lds\n"
    "s_add_u32 s38, s38, 393216\n"
    "s_add_u32 m0, s32, 36864\n"
    "s_nop 0\n"
    "buffer_load_dwordx4 v0, s[4:7], s38 offen offset:0 lds\n"
    "buffer_load_dwordx4 v1, s[4:7], s38 offen offset:1024 lds\n"
    "s_add_u32 m0, s32, 38912\n"
    "s_nop 0\n"
    "buffer_load_dwordx4 v2, s[8:11], s38 offen lds\n"
    "s_waitcnt vmcnt(36)\n"
    "ds_read_b64_tr_b16 a[0:1], v3 offset:0\n"
    "ds_read_b64_tr_b16 a[2:3], v3 offset:128\n"
    "ds_read_b64_tr_b16 a[80:81], v3 offset:32\n"
    "ds_read_b64_tr_b16 a[82:83], v3 offset:160\n"
    "ds_read_b64_tr_b16 a[160:161], v4 offset:0\n"
    "ds_read_b64_tr_b16 a[162:163], v4 offset:64\n"
    "s_waitcnt lgkmcnt(0)\n"
    "s_add_u32 s38, s38, 393216\n"
    "s_add_u32 m0, s32, 0\n"
    "s_nop 0\n"
    "buffer_load_dwordx4 v0, s[4:7], s38 offen offset:0 lds\n"
    "buffer_load_dwordx4 v1, s[4:7], s38 offen offset:1024 lds\n"
    "s_add_u32 m0, s32, 2048\n"
    "s_nop 0\n"
    "buffer_load_dwordx4 v2, s[8:11], s38 offen lds\n"
    "s_waitcnt vmcnt(36)\n"
    "ds_read_b64_tr_b16 a[4:5], v3 offset:3072\n"
    "ds_read_b64_tr_b16 a[6:7], v3 offset:3200\n"
    "ds_read_b64_tr_b16 a[84:85], v3 offset:3104\n"
    "ds_read_b64_tr_b16 a[86:87], v3 offset:3232\n"
    "ds_read_b64_tr_b16 a[164:165], v4 offset:3072\n"
    "ds_read_b64_tr_b16 a[166:167], v4 offset:3136\n"
    "s_waitcnt lgkmcnt(0)\n"
    "s_add_u32 s38, s38, 393216\n"
    "s_add_u32 m0, s32, 3072\n"
    "s_nop 0\n"
    "buffer_load_dwordx4 v0, s[4:7], s38 offen offset:0 lds\n"
    "buffer_load_dwordx4 v1, s[4:7], s38 offen offset:1024 lds\n"
    "s_add_u32 m0, s32, 5120\n"
    "s_nop 0\n"
    "buffer_load_dwordx4 v2, s[8:11], s38 offen lds\n"
    "s_waitcnt vmcnt(36)\n"
    "ds_read_b64_tr_b16 a[8:9], v3 offset:6144\n"
    "ds_read_b64_tr_b16 a[10:11], v3 offset:6272\n"
    "ds_read_b64_tr_b16 a[88:89], v3 offset:6176\n"
    "ds_read_b64_tr_b16 a[90:91], v3 offset:6304\n"
    "ds_read_b64_tr_b16 a[168:169], v4 offset:6144\n"
    "ds_read_b64_tr_b16 a[170:171], v4 offset:6208\n"
    "s_waitcnt lgkmcnt(0)\n"
    "s_add_u32 s38, s38, 393216\n"
    "s_add_u32 m0, s32, 6144\n"
    "s_nop 0\n"
    "buffer_load_dwordx4 v0, s[4:7], s38 offen offset:0 lds\n"
    "buffer_load_dwordx4 v1, s[4:7], s38 offen offset:1024 lds\n"
    "s_add_u32 m0, s32, 8192\n"
    "s_nop 0\n"
    "buffer_load_dwordx4 v2, s[8:11], s38 offen lds\n"
    "s_waitcnt vmcnt(36)\n"
    "ds_read_b64_tr_b16 a[12:13], v3 offset:9216\n"
    "ds_read_b64_tr_b16 a[14:15], v3 offset:9344\n"
    "ds_read_b64_tr_b16 a[92:93], v3 offset:9248\n"
    "ds_read_b64_tr_b16 a[94:95], v3 offset:9376\n"
    "ds_read_b64_tr_b16 a[172:173], v4 offset:9216\n"
    "ds_read_b64_tr_b16 a[174:175], v4 offset:9280\n"
    "s_waitcnt lgkmcnt(0)\n"
    "s_add_u32 s38, s38, 393216\n"
    "s_add_u32 m0, s32, 9216\n"
    "s_nop 0\n"
    "buffer_load_dwordx4 v0, s[4:7], s38 offen offset:0 lds\n"
    "buffer_load_dwordx4 v1, s[4:7], s38 offen offset:1024 lds\n"
    "s_add_u32 m0, s32, 11264\n"
    "s_nop 0\n"
    "buffer_load_dwordx4 v2, s[8:11], s38 offen lds\n"
    "s_waitcnt vmcnt(36)\n"
    "ds_read_b64_tr_b16 a[16:17], v3 offset:12288\n"
    "ds_read_b64_tr_b16 a[18:19], v3 offset:12416\n"
    "ds_read_b64_tr_b16 a[96:97], v3 offset:12320\n"
    "ds_read_b64_tr_b16 a[98:99], v3 offset:12448\n"
    "ds_read_b64_tr_b16 a[176:177], v4 offset:12288\n"
    "ds_read_b64_tr_b16 a[178:179], v4 offset:12352\n"
    "s_waitcnt lgkmcnt(0)\n"
    "s_add_u32 s38, s38, 393216\n"
    "s_add_u32 m0, s32, 12288\n"
    "s_nop 0\n"
    "buffer_load_dwordx4 v0, s[4:7], s38 offen offset:0 lds\n"
    "buffer_load_dwordx4 v1, s[4:7], s38 offen offset:1024 lds\n"
    "s_add_u32 m0, s32, 14336\n"
    "s_nop 0\n"
    "buffer_load_dwordx4 v2, s[8:11], s38 offen lds\n"
    "s_waitcnt vmcnt(36)\n"
    "ds_read_b64_tr_b16 a[20:21], v3 offset:15360\n"
    "ds_read_b64_tr_b16 a[22:23], v3 offset:15488\n"
    "ds_read_b64_tr_b16 a[100:101], v3 offset:15392\n"
    "ds_read_b64_tr_b16 a[102:103], v3 offset:15520\n"
    "ds_read_b64_tr_b16 a[180:181], v4 offset:15360\n"
    "ds_read_b64_tr_b16 a[182:183], v4 offset:15424\n"
    "s_waitcnt lgkmcnt(0)\n"
    "s_add_u32 s38, s38, 393216\n"
    "s_add_u32 m0, s32, 15360\n"
    "s_nop 0\n"
    "buffer_load_dwordx4 v0, s[4:7], s38 offen offset:0 lds\n"
    "buffer_load_dwordx4 v1, s[4:7], s38 offen offset:1024 lds\n"
    "s_add_u32 m0, s32, 17408\n"
    "s_nop 0\n"
    "buffer_load_dwordx4 v2, s[8:11], s38 offen lds\n"
    "s_waitcnt vmcnt(36)\n"
    "ds_read_b64_tr_b16 a[24:25], v3 offset:18432\n"
    "ds_read_b64_tr_b16 a[26:27], v3 offset:18560\n"
    "ds_read_b64_tr_b16 a[104:105], v3 offset:18464\n"
    "ds_read_b64_tr_b16 a[106:107], v3 offset:18592\n"
    "ds_read_b64_tr_b16 a[184:185], v4 offset:18432\n"
    "ds_read_b64_tr_b16 a[186:187], v4 offset:18496\n"
    "s_waitcnt lgkmcnt(0)\n"
    "s_add_u32 s38, s38, 393216\n"
    "s_add_u32 m0, s32, 18432\n"
    "s_nop 0\n"
    "buffer_load_dwordx4 v0, s[4:7], s38 offen offset:0 lds\n"
    "buffer_load_dwordx4 v1, s[4:7], s38 offen offset:1024 lds\n"
    "s_add_u32 m0, s32, 20480\n"
    "s_nop 0\n"
    "buffer_load_dwordx4 v2, s[8:11], s38 offen lds\n"
    "s_waitcnt vmcnt(36)\n"
    "ds_read_b64_tr_b16 a[28:29], v3 offset:21504\n"
    "ds_read_b64_tr_b16 a[30:31], v3 offset:21632\n"
    "ds_read_b64_tr_b16 a[108:109], v3 offset:21536\n"
    "ds_read_b64_tr_b16 a[110:111], v3 offset:21664\n"
    "ds_read_b64_tr_b16 a[188:189], v4 offset:21504\n"
    "ds_read_b64_tr_b16 a[190:191], v4 offset:21568\n"
    "s_waitcnt lgkmcnt(0)\n"
    "s_waitcnt vmcnt(33)\n"
    "ds_read_b64_tr_b16 a[32:33], v3 offset:24576\n"
    "ds_read_b64_tr_b16 a[34:35], v3 offset:24704\n"
    "ds_read_b64_tr_b16 a[112:113], v3 offset:24608\n"
    "ds_read_b64_tr_b16 a[114:115], v3 offset:24736\n"
    "ds_read_b64_tr_b16 a[192:193], v4 offset:24576\n"
    "ds_read_b64_tr_b16 a[194:195], v4 offset:24640\n"
    "s_waitcnt lgkmcnt(0)\n"
    "s_waitcnt vmcnt(30)\n"
    "ds_read_b64_tr_b16 a[36:37], v3 offset:27648\n"
    "ds_read_b64_tr_b16 a[38:39], v3 offset:27776\n"
    "ds_read_b64_tr_b16 a[116:117], v3 offset:27680\n"
    "ds_read_b64_tr_b16 a[118:119], v3 offset:27808\n"
    "ds_read_b64_tr_b16 a[196:197], v4 offset:27648\n"
    "ds_read_b64_tr_b16 a[198:199], v4 offset:27712\n"
    "s_waitcnt lgkmcnt(0)\n"
    "s_waitcnt vmcnt(27)\n"
    "ds_read_b64_tr_b16 a[40:41], v3 offset:30720\n"
    "ds_read_b64_tr_b16 a[42:43], v3 offset:30848\n"
    "ds_read_b64_tr_b16 a[120:121], v3 offset:30752\n"
    "ds_read_b64_tr_b16 a[122:123], v3 offset:30880\n"
    "ds_read_b64_tr_b16 a[200:201], v4 offset:30720\n"
    "ds_read_b64_tr_b16 a[202:203], v4 offset:30784\n"
    "s_waitcnt lgkmcnt(0)\n"
    "s_waitcnt vmcnt(24)\n"
    "ds_read_b64_tr_b16 a[44:45], v3 offset:33792\n"
    "ds_read_b64_tr_b16 a[46:47], v3 offset:33920\n"
    "ds_read_b64_tr_b16 a[124:125], v3 offset:33824\n"
    "ds_read_b64_tr_b16 a[126:127], v3 offset:33952\n"
    "ds_read_b64_tr_b16 a[204:205], v4 offset:33792\n"
    "ds_read_b64_tr_b16 a[206:207], v4 offset:33856\n"
    "s_waitcnt lgkmcnt(0)\n"
    "s_waitcnt vmcnt(21)\n"
    "ds_read_b64_tr_b16 a[48:49], v3 offset:36864\n"
    "ds_read_b64_tr_b16 a[50:51], v3 offset:36992\n"
    "ds_read_b64_tr_b16 a[128:129], v3 offset:36896\n"
    "ds_read_b64_tr_b16 a[130:131], v3 offset:37024\n"
    "ds_read_b64_tr_b16 a[208:209], v4 offset:36864\n"
    "ds_read_b64_tr_b16 a[210:211], v4 offset:36928\n"
    "s_waitcnt lgkmcnt(0)\n"
    "s_waitcnt vmcnt(18)\n"
    "ds_read_b64_tr_b16 a[52:53], v3 offset:0\n"
    "ds_read_b64_tr_b16 a[54:55], v3 offset:128\n"
    "ds_read_b64_tr_b16 a[132:133], v3 offset:32\n"
    "ds_read_b64_tr_b16 a[134:135], v3 offset:160\n"
    "ds_read_b64_tr_b16 a[212:213], v4 offset:0\n"
    "ds_read_b64_tr_b16 a[214:215], v4 offset:64\n"
    "s_waitcnt lgkmcnt(0)\n"
    "s_waitcnt vmcnt(15)\n"
    "ds_read_b64_tr_b16 a[56:57], v3 offset:3072\n"
    "ds_read_b64_tr_b16 a[58:59], v3 offset:3200\n"
    "ds_read_b64_tr_b16 a[136:137], v3 offset:3104\n"
    "ds_read_b64_tr_b16 a[138:139], v3 offset:3232\n"
    "ds_read_b64_tr_b16 a[216:217], v4 offset:3072\n"
    "ds_read_b64_tr_b16 a[218:219], v4 offset:3136\n"
    "s_waitcnt lgkmcnt(0)\n"
    "s_waitcnt vmcnt(12)\n"
    "ds_read_b64_tr_b16 a[60:61], v3 offset:6144\n"
    "ds_read_b64_tr_b16 a[62:63], v3 offset:6272\n"
    "ds_read_b64_tr_b16 a[140:141], v3 offset:6176\n"
    "ds_read_b64_tr_b16 a[142:143], v3 offset:6304\n"
    "ds_read_b64_tr_b16 a[220:221], v4 offset:6144\n"
    "ds_read_b64_tr_b16 a[222:223], v4 offset:6208\n"
    "s_waitcnt lgkmcnt(0)\n"
    "s_waitcnt vmcnt(9)\n"
    "ds_read_b64_tr_b16 a[64:65], v3 offset:9216\n"
    "ds_read_b64_tr_b16 a[66:67], v3 offset:9344\n"
    "ds_read_b64_tr_b16 a[144:145], v3 offset:9248\n"
    "ds_read_b64_tr_b16 a[146:147], v3 offset:9376\n"
    "ds_read_b64_tr_b16 a[224:225], v4 offset:9216\n"
    "ds_read_b64_tr_b16 a[226:227], v4 offset:9280\n"
    "s_waitcnt lgkmcnt(0)\n"
    "s_waitcnt vmcnt(6)\n"
    "ds_read_b64_tr_b16 a[68:69], v3 offset:12288\n"
    "ds_read_b64_tr_b16 a[70:71], v3 offset:12416\n"
    "ds_read_b64_tr_b16 a[148:149], v3 offset:12320\n"
    "ds_read_b64_tr_b16 a[150:151], v3 offset:12448\n"
    "ds_read_b64_tr_b16 a[228:229], v4 offset:12288\n"
    "ds_read_b64_tr_b16 a[230:231], v4 offset:12352\n"
    "s_waitcnt lgkmcnt(0)\n"
    "s_waitcnt vmcnt(3)\n"
    "ds_read_b64_tr_b16 a[72:73], v3 offset:15360\n"
    "ds_read_b64_tr_b16 a[74:75], v3 offset:15488\n"
    "ds_read_b64_tr_b16 a[152:153], v3 offset:15392\n"
    "ds_read_b64_tr_b16 a[154:155], v3 offset:15520\n"
    "ds_read_b64_tr_b16 a[232:233], v4 offset:15360\n"
    "ds_read_b64_tr_b16 a[234:235], v4 offset:15424\n"
    "s_waitcnt lgkmcnt(0)\n"
    "s_waitcnt vmcnt(0)\n"
    "ds_read_b64_tr_b16 a[76:77], v3 offset:18432\n"
    "ds_read_b64_tr_b16 a[78:79], v3 offset:18560\n"
    "ds_read_b64_tr_b16 a[156:157], v3 offset:18464\n"
    "ds_read_b64_tr_b16 a[158:159], v3 offset:18592\n"
    "ds_read_b64_tr_b16 a[236:237], v4 offset:18432\n"
    "ds_read_b64_tr_b16 a[238:239], v4 offset:18496\n"
    "s_waitcnt lgkmcnt(0)\n"
    "s_waitcnt lgkmcnt(0)\n"
    "s_barrier\n"
    "s_mov_b32 s26, -2\n"
    "v_lshrrev_b32 v4, 2, v64\n"
    ".Lqw_po1_%=:\n"
    "buffer_load_dword v8, v4, s[0:3], s16 offen sc1\n"
    "s_waitcnt vmcnt(0)\n"
    "v_cmp_gt_i32 vcc, 1, v8\n"
    "s_nop 0\n"
    "s_cbranch_vccz .Lqw_pd2_%=\n"
    "s_add_u32 s20, s20, 1\n"
    "s_cmp_lt_u32 s20, 0x40000\n"
    "s_cbranch_scc1 .Lqw_sp3_%=\n"
    "v_mov_b32 v8, 50\n"
    "s_mov_b32 s24, 33728\n"
    "buffer_store_dword v8, off, s[0:3], s24 sc1\n"
    "s_waitcnt vmcnt(0)\n"
    "s_branch .Lqw_pd2_%=\n"
    ".Lqw_sp3_%=:\n"
    "s_sleep 4\n"
    "s_branch .Lqw_po1_%=\n"
    ".Lqw_pd2_%=:\n"
    "s_mov_b32 s26, -1\n"
    "s_mov_b32 s35, 0\n"
    "s_lshl_b32 s37, s12, 11\n"
    "s_mov_b32 s43, 2130706432\n"
    "s_mov_b32 s44, 0\n"
    "s_mov_b32 s41, 2130706432\n"
    "s_mov_b32 s42, 0\n"
    ".Lqw_top_%=:\n"
    "s_add_u32 s26, s26, 1\n"
    "s_sub_u32 s27, s26, 10\n"
    "s_ashr_i32 s28, s27, 1\n"
    "s_and_b32 s29, s27, 1\n"
    "s_add_u32 s30, s28, 10\n"
    "s_cmp_lt_i32 s27, 0\n"
    "s_cselect_b32 s29, 0, s29\n"
    "s_cselect_b32 s30, s26, s30\n"
    "s_cmp_eq_u32 s29, 1\n"
    "s_cselect_b32 s30, s28, s30\n"
    "s_cselect_b32 s27, s15, 64\n"
    "s_lshl_b32 s31, s29, 13\n"
    "s_add_u32 s40, s30, 1\n"
    "s_lshl_b32 s32, s30, 19\n"
    "s_lshl_b32 s25, s30, 11\n"
    "s_xor_b32 s25, s25, s31\n"
    "s_and_b32 s25, s25, 0x2000\n"
    "s_add_u32 s32, s32, s25\n"
    "s_add_u32 s32, s32, s13\n"
    "s_lshl_b32 s33, s30, 20\n"
    "s_lshl_b32 s25, s30, 12\n"
    "s_xor_b32 s25, s25, s31\n"
    "s_and_b32 s25, s25, 0x2000\n"
    "s_add_u32 s33, s33, s25\n"
    "s_add_u32 s33, s33, s14\n"
    "s_cmp_lt_u32 s30, s27\n"
    "s_cselect_b32 s36, 1, 0\n"
    "s_cselect_b32 s32, s32, 2130706432\n"
    "s_cselect_b32 s39, s31, 2130706432\n"
    "s_and_b32 s27, s36, s29\n"
    "s_cbranch_scc1 .Lqw_wx_%=\n"
    ".Lqw_wxb_%=:\n"
    "s_cmp_eq_u32 s35, 0\n"
    "s_cbranch_scc1 .Lqw_skip_%=\n"
    "ds_read_b128 v[24:27], v65 offset:0\n"
    "ds_read_b128 v[28:31], v65 offset:1024\n"
    "ds_read_b128 v[32:35], v65 offset:2048\n"
    "ds_read_b128 v[36:39], v65 offset:3072\n"
    "ds_read_b128 v[40:43], v65 offset:4096\n"
    "ds_read_b128 v[44:47], v65 offset:5120\n"
    "s_waitcnt lgkmcnt(4)\n"
    "v_mfma_f32_16x16x32_bf16 v[0:3], a[0:3], v[24:27], 0\n"
    "s_add_u32 m0, s37, 0\n"
    "v_mfma_f32_16x16x32_bf16 v[4:7], a[0:3], v[28:31], 0\n"
    "v_mfma_f32_16x16x32_bf16 v[8:11], a[80:83], v[24:27], 0\n"
    "v_mfma_f32_16x16x32_bf16 v[12:15], a[80:83], v[28:31], 0\n"
    "v_mfma_f32_16x16x32_bf16 v[16:19], a[160:163], v[24:27], 0\n"
    "ds_read_b128 v[24:27], v65 offset:6144\n"
    "v_mfma_f32_16x16x32_bf16 v[20:23], a[160:163], v[28:31], 0\n"
    "ds_read_b128 v[28:31], v65 offset:7168\n"
    "s_waitcnt lgkmcnt(4)\n"
    "v_mfma_f32_16x16x32_bf16 v[0:3], a[4:7], v[32:35], v[0:3]\n"
    "v_mfma_f32_16x16x32_bf16 v[4:7], a[4:7], v[36:39], v[4:7]\n"
    "v_mfma_f32_16x16x32_bf16 v[8:11], a[84:87], v[32:35], v[8:11]\n"
    "v_mfma_f32_16x16x32_bf16 v[12:15], a[84:87], v[36:39], v[12:15]\n"
    "v_mfma_f32_16x16x32_bf16 v[16:19], a[164:167], v[32:35], v[16:19]\n"
    "ds_read_b128 v[32:35], v65 offset:8192\n"
    "v_mfma_f32_16x16x32_bf16 v[20:23], a[164:167], v[36:39], v[20:23]\n"
    "ds_read_b128 v[36:39], v65 offset:9216\n"
    "s_waitcnt lgkmcnt(4)\n"
    "v_mfma_f32_16x16x32_bf16 v[0:3], a[8:11], v[40:43], v[0:3]\n"
    "buffer_load_dwordx4 v64, s[0:3], s32 offen sc1 lds\n"
    "v_mfma_f32_16x16x32_bf16 v[4:7], a[8:11], v[44:47], v[4:7]\n"
    "v_mfma_f32_16x16x32_bf16 v[8:11], a[88:91], v[40:43], v[8:11]\n"
    "v_mfma_f32_16x16x32_bf16 v[12:15], a[88:91], v[44:47], v[12:15]\n"
    "v_mfma_f32_16x16x32_bf16 v[16:19], a[168:171], v[40:43], v[16:19]\n"
    "ds_read_b128 v[40:43], v65 offset:10240\n"
    "buffer_load_dwordx4 v64, s[0:3], s32 offen offset:1024 sc1 lds\n"
    "v_mfma_f32_16x16x32_bf16 v[20:23], a[168:171], v[44:47], v[20:23]\n"
    "ds_read_b128 v[44:47], v65 offset:11264\n"
    "s_waitcnt lgkmcnt(4)\n"
    "v_mfma_f32_16x16x32_bf16 v[0:3], a[12:15], v[24:27], v[0:3]\n"
    "s_add_u32 m0, s37, 8192\n"
    "s_add_u32 s32, s32, 0x4000\n"
    "v_mfma_f32_16x16x32_bf16 v[4:7], a[12:15], v[28:31], v[4:7]\n"
    "v_mfma_f32_16x16x32_bf16 v[8:11], a[92:95], v[24:27], v[8:11]\n"
    "buffer_load_dwordx4 v64, s[0:3], s32 offen sc1 lds\n"
    "v_mfma_f32_16x16x32_bf16 v[12:15], a[92:95], v[28:31], v[12:15]\n"
    "v_mfma_f32_16x16x32_bf16 v[16:19], a[172:175], v[24:27], v[16:19]\n"
    "ds_read_b128 v[24:27], v65 offset:12288\n"
    "v_mfma_f32_16x16x32_bf16 v[20:23], a[172:175], v[28:31], v[20:23]\n"
    "ds_read_b128 v[28:31], v65 offset:13312\n"
    "s_waitcnt lgkmcnt(4)\n"
    "v_mfma_f32_16x16x32_bf16 v[0:3], a[16:19], v[32:35], v[0:3]\n"
    "buffer_load_dwordx4 v64, s[0:3], s32 offen offset:1024 sc1 lds\n"
    "s_add_u32 m0, s37, 16384\n"
    "v_mfma_f32_16x16x32_bf16 v[4:7], a[16:19], v[36:39], v[4:7]\n"
    "s_add_u32 s32, s32, 0x4000\n"
    "v_mfma_f32_16x16x32_bf16 v[8:11], a[96:99], v[32:35], v[8:11]\n"
    "v_mfma_f32_16x16x32_bf16 v[12:15], a[96:99], v[36:39], v[12:15]\n"
    "v_mfma_f32_16x16x32_bf16 v[16:19], a[176:179], v[32:35], v[16:19]\n"
    "ds_read_b128 v[32:35], v65 offset:14336\n"
    "buffer_load_dwordx4 v64, s[0:3], s32 offen sc1 lds\n"
    "v_mfma_f32_16x16x32_bf16 v[20:23], a[176:179], v[36:39], v[20:23]\n"
    "ds_read_b128 v[36:39], v65 offset:15360\n"
    "s_waitcnt lgkmcnt(4)\n"
    "v_mfma_f32_16x16x32_bf16 v[0:3], a[20:23], v[40:43], v[0:3]\n"
    "v_mfma_f32_16x16x32_bf16 v[4:7], a[20:23], v[44:47], v[4:7]\n"
    "v_mfma_f32_16x16x32_bf16 v[8:11], a[100:103], v[40:43], v[8:11]\n"
    "buffer_load_dwordx4 v64, s[0:3], s32 offen offset:1024 sc1 lds\n"
    "s_add_u32 m0, s37, 24576\n"
    "v_mfma_f32_16x16x32_bf16 v[12:15], a[100:103], v[44:47], v[12:15]\n"
    "s_add_u32 s32, s32, 0x4000\n"
    "v_mfma_f32_16x16x32_bf16 v[16:19], a[180:183], v[40:43], v[16:19]\n"
    "ds_read_b128 v[40:43], v65 offset:16384\n"
    "v_mfma_f32_16x16x32_bf16 v[20:23], a[180:183], v[44:47], v[20:23]\n"
    "ds_read_b128 v[44:47], v65 offset:17408\n"
    "s_waitcnt lgkmcnt(4)\n"
    "v_mfma_f32_16x16x32_bf16 v[0:3], a[24:27], v[24:27], v[0:3]\n"
    "s_waitcnt vmcnt(6)\n"
    "v_mov_b32 v61, s44\n"
    "v_mfma_f32_16x16x32_bf16 v[4:7], a[24:27], v[28:31], v[4:7]\n"
    "buffer_store_dword v61, v69, s[0:3], s43 offen sc1\n"
    "v_mfma_f32_16x16x32_bf16 v[8:11], a[104:107], v[24:27], v[8:11]\n"
    "v_mfma_f32_16x16x32_bf16 v[12:15], a[104:107], v[28:31], v[12:15]\n"
    "v_mfma_f32_16x16x32_bf16 v[16:19], a[184:187], v[24:27], v[16:19]\n"
    "ds_read_b128 v[24:27], v65 offset:18432\n"
    "v_mfma_f32_16x16x32_bf16 v[20:23], a[184:187], v[28:31], v[20:23]\n"
    "ds_read_b128 v[28:31], v65 offset:19456\n"
    "s_waitcnt lgkmcnt(4)\n"
    "v_mfma_f32_16x16x32_bf16 v[0:3], a[28:31], v[32:35], v[0:3]\n"
    "v_mfma_f32_16x16x32_bf16 v[4:7], a[28:31], v[36:39], v[4:7]\n"
    "v_mfma_f32_16x16x32_bf16 v[8:11], a[108:111], v[32:35], v[8:11]\n"
    "v_mfma_f32_16x16x32_bf16 v[12:15], a[108:111], v[36:39], v[12:15]\n"
    "v_mfma_f32_16x16x32_bf16 v[16:19], a[188:191], v[32:35], v[16:19]\n"
    "ds_read_b128 v[32:35], v65 offset:20480\n"
    "buffer_load_dwordx4 v64, s[0:3], s32 offen sc1 lds\n"
    "v_mfma_f32_16x16x32_bf16 v[20:23], a[188:191], v[36:39], v[20:23]\n"
    "ds_read_b128 v[36:39], v65 offset:21504\n"
    "s_waitcnt lgkmcnt(4)\n"
    "v_mfma_f32_16x16x32_bf16 v[0:3], a[32:35], v[40:43], v[0:3]\n"
    "v_mfma_f32_16x16x32_bf16 v[4:7], a[32:35], v[44:47], v[4:7]\n"
    "v_mfma_f32_16x16x32_bf16 v[8:11], a[112:115], v[40:43], v[8:11]\n"
    "buffer_load_dwordx4 v64, s[0:3], s32 offen offset:1024 sc1 lds\n"
    "s_add_u32 m0, s37, 32768\n"
    "v_mfma_f32_16x16x32_bf16 v[12:15], a[112:115], v[44:47], v[12:15]\n"
    "s_add_u32 s32, s32, 0x4000\n"
    "v_mfma_f32_16x16x32_bf16 v[16:19], a[192:195], v[40:43], v[16:19]\n"
    "ds_read_b128 v[40:43], v65 offset:22528\n"
    "v_mfma_f32_16x16x32_bf16 v[20:23], a[192:195], v[44:47], v[20:23]\n"
    "ds_read_b128 v[44:47], v65 offset:23552\n"
    "s_waitcnt lgkmcnt(4)\n"
    "v_mfma_f32_16x16x32_bf16 v[0:3], a[36:39], v[24:27], v[0:3]\n"
    "buffer_load_dwordx4 v64, s[0:3], s32 offen sc1 lds\n"
    "v_mfma_f32_16x16x32_bf16 v[4:7], a[36:39], v[28:31], v[4:7]\n"
    "v_mfma_f32_16x16x32_bf16 v[8:11], a[116:119], v[24:27], v[8:11]\n"
    "v_mfma_f32_16x16x32_bf16 v[12:15], a[116:119], v[28:31], v[12:15]\n"
    "v_mfma_f32_16x16x32_bf16 v[16:19], a[196:199], v[24:27], v[16:19]\n"
    "ds_read_b128 v[24:27], v65 offset:24576\n"
    "buffer_load_dwordx4 v64, s[0:3], s32 offen offset:1024 sc1 lds\n"
    "v_mfma_f32_16x16x32_bf16 v[20:23], a[196:199], v[28:31], v[20:23]\n"
    "ds_read_b128 v[28:31], v65 offset:25600\n"
    "s_waitcnt lgkmcnt(4)\n"
    "v_mfma_f32_16x16x32_bf16 v[0:3], a[40:43], v[32:35], v[0:3]\n"
    "v_mfma_f32_16x16x32_bf16 v[4:7], a[40:43], v[36:39], v[4:7]\n"
    "v_mfma_f32_16x16x32_bf16 v[8:11], a[120:123], v[32:35], v[8:11]\n"
    "v_mfma_f32_16x16x32_bf16 v[12:15], a[120:123], v[36:39], v[12:15]\n"
    "v_mfma_f32_16x16x32_bf16 v[16:19], a[200:203], v[32:35], v[16:19]\n"
    "ds_read_b128 v[32:35], v65 offset:26624\n"
    "v_mfma_f32_16x16x32_bf16 v[20:23], a[200:203], v[36:39], v[20:23]\n"
    "ds_read_b128 v[36:39], v65 offset:27648\n"
    "s_waitcnt lgkmcnt(4)\n"
    "v_mfma_f32_16x16x32_bf16 v[0:3], a[44:47], v[40:43], v[0:3]\n"
    "v_mfma_f32_16x16x32_bf16 v[4:7], a[44:47], v[44:47], v[4:7]\n"
    "v_mfma_f32_16x16x32_bf16 v[8:11], a[124:127], v[40:43], v[8:11]\n"
    "v_mfma_f32_16x16x32_bf16 v[12:15], a[124:127], v[44:47], v[12:15]\n"
    "v_mfma_f32_16x16x32_bf16 v[16:19], a[204:207], v[40:43], v[16:19]\n"
    "ds_read_b128 v[40:43], v65 offset:28672\n"
    "v_mfma_f32_16x16x32_bf16 v[20:23], a[204:207], v[44:47], v[20:23]\n"
    "ds_read_b128 v[44:47], v65 offset:29696\n"
    "s_waitcnt lgkmcnt(4)\n"
    "v_mfma_f32_16x16x32_bf16 v[0:3], a[48:51], v[24:27], v[0:3]\n"
    "v_mfma_f32_16x16x32_bf16 v[4:7], a[48:51], v[28:31], v[4:7]\n"
    "v_mfma_f32_16x16x32_bf16 v[8:11], a[128:131], v[24:27], v[8:11]\n"
    "v_mfma_f32_16x16x32_bf16 v[12:15], a[128:131], v[28:31], v[12:15]\n"
    "v_mfma_f32_16x16x32_bf16 v[16:19], a[208:211], v[24:27], v[16:19]\n"
    "ds_read_b128 v[24:27], v65 offset:30720\n"
    "v_mfma_f32_16x16x32_bf16 v[20:23], a[208:211], v[28:31], v[20:23]\n"
    "ds_read_b128 v[28:31], v65 offset:31744\n"
    "s_waitcnt lgkmcnt(4)\n"
    "v_mfma_f32_16x16x32_bf16 v[0:3], a[52:55], v[32:35], v[0:3]\n"
    "v_mfma_f32_16x16x32_bf16 v[4:7], a[52:55], v[36:39], v[4:7]\n"
    "v_mfma_f32_16x16x32_bf16 v[8:11], a[132:135], v[32:35], v[8:11]\n"
    "v_mfma_f32_16x16x32_bf16 v[12:15], a[132:135], v[36:39], v[12:15]\n"
    "v_mfma_f32_16x16x32_bf16 v[16:19], a[212:215], v[32:35], v[16:19]\n"
    "ds_read_b128 v[32:35], v65 offset:32768\n"
    "v_mfma_f32_16x16x32_bf16 v[20:23], a[212:215], v[36:39], v[20:23]\n"
    "ds_read_b128 v[36:39], v65 offset:33792\n"
    "s_waitcnt lgkmcnt(4)\n"
    "v_mfma_f32_16x16x32_bf16 v[0:3], a[56:59], v[40:43], v[0:3]\n"
    "v_mfma_f32_16x16x32_bf16 v[4:7], a[56:59], v[44:47], v[4:7]\n"
    "v_mfma_f32_16x16x32_bf16 v[8:11], a[136:139], v[40:43], v[8:11]\n"
    "v_mfma_f32_16x16x32_bf16 v[12:15], a[136:139], v[44:47], v[12:15]\n"
    "v_mfma_f32_16x16x32_bf16 v[16:19], a[216:219], v[40:43], v[16:19]\n"
    "ds_read_b128 v[40:43], v65 offset:34816\n"
    "v_mfma_f32_16x16x32_bf16 v[20:23], a[216:219], v[44:47], v[20:23]\n"
    "ds_read_b128 v[44:47], v65 offset:35840\n"
    "s_waitcnt lgkmcnt(4)\n"
    "v_mfma_f32_16x16x32_bf16 v[0:3], a[60:63], v[24:27], v[0:3]\n"
    "v_mfma_f32_16x16x32_bf16 v[4:7], a[60:63], v[28:31], v[4:7]\n"
    "v_mfma_f32_16x16x32_bf16 v[8:11], a[140:143], v[24:27], v[8:11]\n"
    "v_mfma_f32_16x16x32_bf16 v[12:15], a[140:143], v[28:31], v[12:15]\n"
    "v_mfma_f32_16x16x32_bf16 v[16:19], a[220:223], v[24:27], v[16:19]\n"
    "ds_read_b128 v[24:27], v65 offset:36864\n"
    "v_mfma_f32_16x16x32_bf16 v[20:23], a[220:223], v[28:31], v[20:23]\n"
    "ds_read_b128 v[28:31], v65 offset:37888\n"
    "s_waitcnt lgkmcnt(4)\n"
    "v_mfma_f32_16x16x32_bf16 v[0:3], a[64:67], v[32:35], v[0:3]\n"
    "v_mfma_f32_16x16x32_bf16 v[4:7], a[64:67], v[36:39], v[4:7]\n"
    "v_mfma_f32_16x16x32_bf16 v[8:11], a[144:147], v[32:35], v[8:11]\n"
    "v_mfma_f32_16x16x32_bf16 v[12:15], a[144:147], v[36:39], v[12:15]\n"
    "v_mfma_f32_16x16x32_bf16 v[16:19], a[224:227], v[32:35], v[16:19]\n"
    "ds_read_b128 v[32:35], v65 offset:38912\n"
    "v_mfma_f32_16x16x32_bf16 v[20:23], a[224:227], v[36:39], v[20:23]\n"
    "ds_read_b128 v[36:39], v65 offset:39936\n"
    "s_waitcnt lgkmcnt(4)\n"
    "v_mfma_f32_16x16x32_bf16 v[0:3], a[68:71], v[40:43], v[0:3]\n"
    "v_mfma_f32_16x16x32_bf16 v[4:7], a[68:71], v[44:47], v[4:7]\n"
    "v_mfma_f32_16x16x32_bf16 v[8:11], a[148:151], v[40:43], v[8:11]\n"
    "v_mfma_f32_16x16x32_bf16 v[12:15], a[148:151], v[44:47], v[12:15]\n"
    "v_mfma_f32_16x16x32_bf16 v[16:19], a[228:231], v[40:43], v[16:19]\n"
    "v_mfma_f32_16x16x32_bf16 v[20:23], a[228:231], v[44:47], v[20:23]\n"
    "s_waitcnt lgkmcnt(2)\n"
    "v_mfma_f32_16x16x32_bf16 v[0:3], a[72:75], v[24:27], v[0:3]\n"
    "v_mfma_f32_16x16x32_bf16 v[4:7], a[72:75], v[28:31], v[4:7]\n"
    "v_mfma_f32_16x16x32_bf16 v[8:11], a[152:155], v[24:27], v[8:11]\n"
    "v_mfma_f32_16x16x32_bf16 v[12:15], a[152:155], v[28:31], v[12:15]\n"
    "v_mfma_f32_16x16x32_bf16 v[16:19], a[232:235], v[24:27], v[16:19]\n"
    "v_mfma_f32_16x16x32_bf16 v[20:23], a[232:235], v[28:31], v[20:23]\n"
    "s_waitcnt lgkmcnt(0)\n"
    "v_mfma_f32_16x16x32_bf16 v[0:3], a[76:79], v[32:35], v[0:3]\n"
    "v_mfma_f32_16x16x32_bf16 v[4:7], a[76:79], v[36:39], v[4:7]\n"
    "v_mfma_f32_16x16x32_bf16 v[8:11], a[156:159], v[32:35], v[8:11]\n"
    "v_mfma_f32_16x16x32_bf16 v[12:15], a[156:159], v[36:39], v[12:15]\n"
    "v_mfma_f32_16x16x32_bf16 v[16:19], a[236:239], v[32:35], v[16:19]\n"
    "v_mfma_f32_16x16x32_bf16 v[20:23], a[236:239], v[36:39], v[20:23]\n"
    "s_nop 2\n"
    "v_cvt_pk_bf16_f32 v48, v0, v1\n"
    "v_cvt_pk_bf16_f32 v49, v2, v3\n"
    "v_cvt_pk_bf16_f32 v50, v8, v9\n"
    "v_cvt_pk_bf16_f32 v51, v10, v11\n"
    "buffer_store_dwordx4 v[48:51], v66, s[0:3], s34 offen offset:0 sc1\n"
    "v_cvt_pk_bf16_f32 v52, v4, v5\n"
    "v_cvt_pk_bf16_f32 v53, v6, v7\n"
    "v_cvt_pk_bf16_f32 v54, v12, v13\n"
    "v_cvt_pk_bf16_f32 v55, v14, v15\n"
    "buffer_store_dwordx4 v[52:55], v66, s[0:3], s34 offen offset:1024 sc1\n"
    "v_cvt_pk_bf16_f32 v56, v16, v17\n"
    "v_cvt_pk_bf16_f32 v57, v18, v19\n"
    "v_cvt_pk_bf16_f32 v58, v20, v21\n"
    "v_cvt_pk_bf16_f32 v59, v22, v23\n"
    "buffer_store_dwordx4 v[56:59], v67, s[0:3], s34 offen sc1\n"
    ".Lqw_join_%=:\n"
    "s_mov_b32 s43, s41\n"
    "s_mov_b32 s44, s42\n"
    "s_mov_b32 s41, s39\n"
    "s_mov_b32 s42, s40\n"
    "s_mov_b32 s34, s33\n"
    "s_mov_b32 s35, s36\n"
    "s_xor_b32 s37, s37, 40960\n"
    "v_xor_b32 v65, 40960, v65\n"
    "s_waitcnt vmcnt(3) lgkmcnt(0)\n"
    "s_barrier\n"
    "s_cmp_lt_u32 s26, 138\n"
    "s_cbranch_scc1 .Lqw_top_%=\n"
    "s_waitcnt vmcnt(0)\n"
    "v_mov_b32 v61, s44\n"
    "buffer_store_dword v61, v69, s[0:3], s43 offen sc1\n"
    "s_waitcnt vmcnt(0) lgkmcnt(0)\n"
    "s_endpgm\n"
    ".Lqw_wx_%=:\n"
    ".Lqw_po4_%=:\n"
    "buffer_load_dword v60, v68, s[0:3], s17 offen sc1\n"
    "s_waitcnt vmcnt(0)\n"
    "v_mov_b32 v61, s44\n"
    "buffer_store_dword v61, v69, s[0:3], s43 offen sc1\n"
    "s_mov_b32 s43, 2130706432\n"
    "v_cmp_gt_i32 vcc, s40, v60\n"
    "s_nop 0\n"
    "s_cbranch_vccz .Lqw_pd5_%=\n"
    "s_add_u32 s20, s20, 1\n"
    "s_cmp_lt_u32 s20, 0x40000\n"
    "s_cbranch_scc1 .Lqw_sp6_%=\n"
    "v_mov_b32 v60, 51\n"
    "s_mov_b32 s24, 33728\n"
    "buffer_store_dword v60, off, s[0:3], s24 sc1\n"
    "s_waitcnt vmcnt(0)\n"
    "s_branch .Lqw_pd5_%=\n"
    ".Lqw_sp6_%=:\n"
    "s_sleep 4\n"
    "s_branch .Lqw_po4_%=\n"
    ".Lqw_pd5_%=:\n"
    "s_branch .Lqw_wxb_%=\n"
    ".Lqw_skip_%=:\n"
    "s_add_u32 m0, s37, 0\n"
    "s_nop 0\n"
    "buffer_load_dwordx4 v64, s[0:3], s32 offen sc1 lds\n"
    "buffer_load_dwordx4 v64, s[0:3], s32 offen offset:1024 sc1 lds\n"
    "s_add_u32 m0, s37, 8192\n"
    "s_add_u32 s32, s32, 0x4000\n"
    "buffer_load_dwordx4 v64, s[0:3], s32 offen sc1 lds\n"
    "buffer_load_dwordx4 v64, s[0:3], s32 offen offset:1024 sc1 lds\n"
    "s_add_u32 m0, s37, 16384\n"
    "s_add_u32 s32, s32, 0x4000\n"
    "buffer_load_dwordx4 v64, s[0:3], s32 offen sc1 lds\n"
    "buffer_load_dwordx4 v64, s[0:3], s32 offen offset:1024 sc1 lds\n"
    "s_add_u32 m0, s37, 24576\n"
    "s_add_u32 s32, s32, 0x4000\n"
    "buffer_load_dwordx4 v64, s[0:3], s32 offen sc1 lds\n"
    "buffer_load_dwordx4 v64, s[0:3], s32 offen offset:1024 sc1 lds\n"
    "s_add_u32 m0, s37, 32768\n"
    "s_add_u32 s32, s32, 0x4000\n"
    "buffer_load_dwordx4 v64, s[0:3], s32 offen sc1 lds\n"
    "buffer_load_dwordx4 v64, s[0:3], s32 offen offset:1024 sc1 lds\n"
    "s_waitcnt vmcnt(0)\n"
    "v_mov_b32 v61, s44\n"
    "buffer_store_dword v61, v69, s[0:3], s43 offen sc1\n"
    "s_branch .Lqw_join_%=\n"
    :: "{s[0:3]}"(s0_), "{s[4:7]}"(s4_), "{s[8:11]}"(s8_), "{s[12:15]}"(s12_), "{s[16:19]}"(s16_), "{v[0:3]}"(v0_), "{v[4:7]}"(v4_), "{v[64:67]}"(v64_), "{v[68:71]}"(v68_) : "memory", "v255", "a255", "s95");
    __builtin_unreachable();
}
DEV void b1_role(const MegaArgs& A, const int L, const int i, const int rep) {
    ROLE_HEAD;
    const int qd = 4 * w + lq, sw = (l16 & 6) << 3, hw = w < 3 ? w : 2, hd = 3 * i + w;
    const bool live = w < 3 && hd < 32;
    const unsigned om = live ? RC_O - RC_X1 - w * 2048 + (hd >> 3) * 16384 + ((hd >> 1) & 3) * 2048 + (lq & 1) * 1024 + (2 * (hd & 1) + (lq >> 1)) * 256 + l16 * 16 : OOB;
    const unsigned ox = live && l < 16 ? RC_O - RC_X1 - w * 2048 + 65536 + hd * 256 + l16 * 16 : OOB;
    u64 wm[4];
#pragma unroll
    for (int e = 0; e < 4; ++e) { const int d = l16 + 16 - 16 * (e >> 1) - 4 * lq - 2 * (e & 1) - (l16 & 1); wm[e] = __ballot(d >= 0 && d <= 14); }
    const u32x4 s0_ = {RFL(rx[0]), RFL(rx[1]), RFL(rx[2]), RFL(rx[3])};
    const u32x4 s4_ = {RFL(lo(A.wqkv + 60 * i + 16 * w)), RFL(hi(A.wqkv + 60 * i + 16 * w)), RFL((640 * 1920 - 60 * i - 16 * w) * 2), RFL(0x20000u)};
    const u32x4 s8_ = {RFL((unsigned)wm[0]), RFL((unsigned)(wm[0] >> 32)), RFL((unsigned)wm[1]), RFL((unsigned)(wm[1] >> 32))};
    const u32x4 s12_ = {RFL((unsigned)wm[2]), RFL((unsigned)(wm[2] >> 32)), RFL((unsigned)wm[3]), RFL((unsigned)(wm[3] >> 32))};
    const u32x4 s16_ = {RFL(w == 3 ? 0xf000f000u : 0u), RFL(w == 3 ? 0xf000f000u : 0u), RFL(w), RFL(L << 13)};
    const u32x4 s20_ = {RFL(REC0 + RC_X1 + w * 2048), RFL(xl(Z_X1 + rep * 4096, L)), RFL(DBG0 + ((L * 128 + 96 + i) * 4 + w) * 2048), RFL(0u)};
    const u32x4 v0_ = {(unsigned)((l >> 1) * 3840 + (l & 1) * 16 + 0), (unsigned)((l >> 1) * 3840 + (l & 1) * 16 + 256), (unsigned)((l >> 1) * 3840 + (l & 1) * 16 + 512), (unsigned)(w * 40960 + (8 * lq + (l16 >> 2)) * 32 + (l & 3) * 8)};
    const u32x4 v224_ = {(unsigned)(l * 16 + ((w + 0) & 3) * 2048), (unsigned)(l * 16 + ((w + 1) & 3) * 2048), (unsigned)(l * 16 + ((w + 2) & 3) * 2048), (unsigned)(l * 16 + ((w + 3) & 3) * 2048)};
    const u32x4 v228_ = {(unsigned)(l * 16), (unsigned)((l & 31) * 128), (unsigned)(l < 4 ? xl(Z_O + l * 2048 + i * 128 + w * 4, L) : OOB), (unsigned)(qd == 15 ? 86016 : 40960 + (qd / 5) * 2048 + l16 * 64 + (((qd % 5) * 8) ^ sw))};
    const u32x4 v232_ = {(unsigned)(qd == 15 ? 86016 : 49152 + (qd / 5) * 4096 + l16 * 64 + (((qd % 5) * 8) ^ sw)), (unsigned)(qd == 15 ? 86016 : 61440 + (qd / 5) * 4096 + l16 * 64 + (((qd % 5) * 8) ^ sw)), (unsigned)(40960 + hw * 2048 + l16 * 64 + ((lq * 16) ^ sw)), (unsigned)(49152 + hw * 4096 + l16 * 64 + ((lq * 16) ^ sw))};
    const u32x4 v236_ = {(unsigned)(51200 + hw * 4096 + l16 * 64 + ((lq * 16) ^ sw)), (unsigned)(61440 + hw * 4096 + (l >> 2) * 64 + ((0 + (l & 3) * 8) ^ (((l >> 2) & 6) << 3))), (unsigned)(63488 + hw * 4096 + (l >> 2) * 64 + ((0 + (l & 3) * 8) ^ (((l >> 2) & 6) << 3))), (unsigned)(61440 + hw * 4096 + (l >> 2) * 64 + ((32 + (l & 3) * 8) ^ (((l >> 2) & 6) << 3)))};
    const u32x4 v240_ = {(unsigned)(63488 + hw * 4096 + (l >> 2) * 64 + ((32 + (l & 3) * 8) ^ (((l >> 2) & 6) << 3))), (unsigned)(73728 + w * 2048 + l * 16), (unsigned)(73728 + (l >> 5) * 1024 + ((l16 >> 2) * 16 + l16) * 16 + (l & 3) * 4), (unsigned)(81920 + w * 512 + l * 4)};
    const u32x4 v244_ = {(unsigned)(81920 + 3 * 512 + (l >> 5) * 256 + (48 + l16) * 4), (unsigned)(om), (unsigned)(ox), (unsigned)((l & 1) ? 0x01000c0cu : 0x0c0c0100u)};
    const u32x4 v248_ = {(unsigned)((l & 1) ? 0x03020c0cu : 0x0c0c0302u), (unsigned)(0xff800000u), (unsigned)(0u), (unsigned)(0u)};
    asm volatile(
    "s_mov_b32 s24, 0\n"
    "s_mul_i32 s32, s18, 40960\n"
    "s_add_u32 s35, s18, 0\n"
    "s_and_b32 s35, s35, 3\n"
    "s_add_u32 s35, s35, 0\n"
    "s_mul_i32 s35, s35, 122880\n"
    "s_add_u32 m0, s32, 0\n"
    "s_nop 0\n"
    "buffer_load_dwordx4 v0, s[4:7], s35 offen offset:0 lds\n"
    "buffer_load_dwordx4 v1, s[4:7], s35 offen offset:1024 lds\n"
    "buffer_load_dwordx4 v2, s[4:7], s35 offen offset:2048 lds\n"
    "s_add_u32 s35, s18, 1\n"
    "s_and_b32 s35, s35, 3\n"
    "s_add_u32 s35, s35, 0\n"
    "s_mul_i32 s35, s35, 122880\n"
    "s_add_u32 m0, s32, 3072\n"
    "s_nop 0\n"
    "buffer_load_dwordx4 v0, s[4:7], s35 offen offset:0 lds\n"
    "buffer_load_dwordx4 v1, s[4:7], s35 offen offset:1024 lds\n"
    "buffer_load_dwordx4 v2, s[4:7], s35 offen offset:2048 lds\n"
    "s_add_u32 s35, s18, 2\n"
    "s_and_b32 s35, s35, 3\n"
    "s_add_u32 s35, s35, 0\n"
    "s_mul_i32 s35, s35, 122880\n"
    "s_add_u32 m0, s32, 6144\n"
    "s_nop 0\n"
    "buffer_load_dwordx4 v0, s[4:7], s35 offen offset:0 lds\n"
    "buffer_load_dwordx4 v1, s[4:7], s35 offen offset:1024 lds\n"
    "buffer_load_dwordx4 v2, s[4:7], s35 offen offset:2048 lds\n"
    "s_add_u32 s35, s18, 3\n"
    "s_and_b32 s35, s35, 3\n"
    "s_add_u32 s35, s35, 0\n"
    "s_mul_i32 s35, s35, 122880\n"
    "s_add_u32 m0, s32, 9216\n"
    "s_nop 0\n"
    "buffer_load_dwordx4 v0, s[4:7], s35 offen offset:0 lds\n"
    "buffer_load_dwordx4 v1, s[4:7], s35 offen offset:1024 lds\n"
    "buffer_load_dwordx4 v2, s[4:7], s35 offen offset:2048 lds\n"
    "s_add_u32 s35, s18, 0\n"
    "s_and_b32 s35, s35, 3\n"
    "s_add_u32 s35, s35, 4\n"
    "s_mul_i32 s35, s35, 122880\n"
    "s_add_u32 m0, s32, 12288\n"
    "s_nop 0\n"
    "buffer_load_dwordx4 v0, s[4:7], s35 offen offset:0 lds\n"
    "buffer_load_dwordx4 v1, s[4:7], s35 offen offset:1024 lds\n"
    "buffer_load_dwordx4 v2, s[4:7], s35 offen offset:2048 lds\n"
    "s_add_u32 s35, s18, 1\n"
    "s_and_b32 s35, s35, 3\n"
    "s_add_u32 s35, s35, 4\n"
    "s_mul_i32 s35, s35, 122880\n"
    "s_add_u32 m0, s32, 15360\n"
    "s_nop 0\n"
    "buffer_load_dwordx4 v0, s[4:7], s35 offen offset:0 lds\n"
    "buffer_load_dwordx4 v1, s[4:7], s35 offen offset:1024 lds\n"
    "buffer_load_dwordx4 v2, s[4:7], s35 offen offset:2048 lds\n"
    "s_add_u32 s35, s18, 2\n"
    "s_and_b32 s35, s35, 3\n"
    "s_add_u32 s35, s35, 4\n"
    "s_mul_i32 s35, s35, 122880\n"
    "s_add_u32 m0, s32, 18432\n"
    "s_nop 0\n"
    "buffer_load_dwordx4 v0, s[4:7], s35 offen offset:0 lds\n"
    "buffer_load_dwordx4 v1, s[4:7], s35 offen offset:1024 lds\n"
    "buffer_load_dwordx4 v2, s[4:7], s35 offen offset:2048 lds\n"
    "s_add_u32 s35, s18, 3\n"
    "s_and_b32 s35, s35, 3\n"
    "s_add_u32 s35, s35, 4\n"
    "s_mul_i32 s35, s35, 122880\n"
    "s_add_u32 m0, s32, 21504\n"
    "s_nop 0\n"
    "buffer_load_dwordx4 v0, s[4:7], s35 offen offset:0 lds\n"
    "buffer_load_dwordx4 v1, s[4:7], s35 offen offset:1024 lds\n"
    "buffer_load_dwordx4 v2, s[4:7], s35 offen offset:2048 lds\n"
    "s_add_u32 s35, s18, 0\n"
    "s_and_b32 s35, s35, 3\n"
    "s_add_u32 s35, s35, 8\n"
    "s_mul_i32 s35, s35, 122880\n"
    "s_add_u32 m0, s32, 24576\n"
    "s_nop 0\n"
    "buffer_load_dwordx4 v0, s[4:7], s35 offen offset:0 lds\n"
    "buffer_load_dwordx4 v1, s[4:7], s35 offen offset:1024 lds\n"
    "buffer_load_dwordx4 v2, s[4:7], s35 offen offset:2048 lds\n"
    "s_add_u32 s35, s18, 1\n"
    "s_and_b32 s35, s35, 3\n"
    "s_add_u32 s35, s35, 8\n"
    "s_mul_i32 s35, s35, 122880\n"
    "s_add_u32 m0, s32, 27648\n"
    "s_nop 0\n"
    "buffer_load_dwordx4 v0, s[4:7], s35 offen offset:0 lds\n"
    "buffer_load_dwordx4 v1, s[4:7], s35 offen offset:1024 lds\n"
    "buffer_load_dwordx4 v2, s[4:7], s35 offen offset:2048 lds\n"
    "s_add_u32 s35, s18, 2\n"
    "s_and_b32 s35, s35, 3\n"
    "s_add_u32 s35, s35, 8\n"
    "s_mul_i32 s35, s35, 122880\n"
    "s_add_u32 m0, s32, 30720\n"
    "s_nop 0\n"
    "buffer_load_dwordx4 v0, s[4:7], s35 offen offset:0 lds\n"
    "buffer_load_dwordx4 v1, s[4:7], s35 offen offset:1024 lds\n"
    "buffer_load_dwordx4 v2, s[4:7], s35 offen offset:2048 lds\n"
    "s_add_u32 s35, s18, 3\n"
    "s_and_b32 s35, s35, 3\n"
    "s_add_u32 s35, s35, 8\n"
    "s_mul_i32 s35, s35, 122880\n"
    "s_add_u32 m0, s32, 33792\n"
    "s_nop 0\n"
    "buffer_load_dwordx4 v0, s[4:7], s35 offen offset:0 lds\n"
    "buffer_load_dwordx4 v1, s[4:7], s35 offen offset:1024 lds\n"
    "buffer_load_dwordx4 v2, s[4:7], s35 offen offset:2048 lds\n"
    "s_add_u32 s35, s18, 0\n"
    "s_and_b32 s35, s35, 3\n"
    "s_add_u32 s35, s35, 12\n"
    "s_mul_i32 s35, s35, 122880\n"
    "s_add_u32 m0, s32, 36864\n"
    "s_nop 0\n"
    "buffer_load_dwordx4 v0, s[4:7], s35 offen offset:0 lds\n"
    "buffer_load_dwordx4 v1, s[4:7], s35 offen offset:1024 lds\n"
    "buffer_load_dwordx4 v2, s[4:7], s35 offen offset:2048 lds\n"
    "s_waitcnt vmcnt(36)\n"
    "ds_read_b64_tr_b16 a[0:1], v3 offset:0\n"
    "ds_read_b64_tr_b16 a[2:3], v3 offset:128\n"
    "ds_read_b64_tr_b16 a[80:81], v3 offset:1024\n"
    "ds_read_b64_tr_b16 a[82:83], v3 offset:1152\n"
    "ds_read_b64_tr_b16 a[160:161], v3 offset:2048\n"
    "ds_read_b64_tr_b16 a[162:163], v3 offset:2176\n"
    "s_waitcnt lgkmcnt(0)\n"
    "s_add_u32 s35, s18, 1\n"
    "s_and_b32 s35, s35, 3\n"
    "s_add_u32 s35, s35, 12\n"
    "s_mul_i32 s35, s35, 122880\n"
    "s_add_u32 m0, s32, 0\n"
    "s_nop 0\n"
    "buffer_load_dwordx4 v0, s[4:7], s35 offen offset:0 lds\n"
    "buffer_load_dwordx4 v1, s[4:7], s35 offen offset:1024 lds\n"
    "buffer_load_dwordx4 v2, s[4:7], s35 offen offset:2048 lds\n"
    "s_waitcnt vmcnt(36)\n"
    "ds_read_b64_tr_b16 a[4:5], v3 offset:3072\n"
    "ds_read_b64_tr_b16 a[6:7], v3 offset:3200\n"
    "ds_read_b64_tr_b16 a[84:85], v3 offset:4096\n"
    "ds_read_b64_tr_b16 a[86:87], v3 offset:4224\n"
    "ds_read_b64_tr_b16 a[164:165], v3 offset:5120\n"
    "ds_read_b64_tr_b16 a[166:167], v3 offset:5248\n"
    "s_waitcnt lgkmcnt(0)\n"
    "s_add_u32 s35, s18, 2\n"
    "s_and_b32 s35, s35, 3\n"
    "s_add_u32 s35, s35, 12\n"
    "s_mul_i32 s35, s35, 122880\n"
    "s_add_u32 m0, s32, 3072\n"
    "s_nop 0\n"
    "buffer_load_dwordx4 v0, s[4:7], s35 offen offset:0 lds\n"
    "buffer_load_dwordx4 v1, s[4:7], s35 offen offset:1024 lds\n"
    "buffer_load_dwordx4 v2, s[4:7], s35 offen offset:2048 lds\n"
    "s_waitcnt vmcnt(36)\n"
    "ds_read_b64_tr_b16 a[8:9], v3 offset:6144\n"
    "ds_read_b64_tr_b16 a[10:11], v3 offset:6272\n"
    "ds_read_b64_tr_b16 a[88:89], v3 offset:7168\n"
    "ds_read_b64_tr_b16 a[90:91], v3 offset:7296\n"
    "ds_read_b64_tr_b16 a[168:169], v3 offset:8192\n"
    "ds_read_b64_tr_b16 a[170:171], v3 offset:8320\n"
    "s_waitcnt lgkmcnt(0)\n"
    "s_add_u32 s35, s18, 3\n"
    "s_and_b32 s35, s35, 3\n"
    "s_add_u32 s35, s35, 12\n"
    "s_mul_i32 s35, s35, 122880\n"
    "s_add_u32 m0, s32, 6144\n"
    "s_nop 0\n"
    "buffer_load_dwordx4 v0, s[4:7], s35 offen offset:0 lds\n"
    "buffer_load_dwordx4 v1, s[4:7], s35 offen offset:1024 lds\n"
    "buffer_load_dwordx4 v2, s[4:7], s35 offen offset:2048 lds\n"
    "s_waitcnt vmcnt(36)\n"
    "ds_read_b64_tr_b16 a[12:13], v3 offset:9216\n"
    "ds_read_b64_tr_b16 a[14:15], v3 offset:9344\n"
    "ds_read_b64_tr_b16 a[92:93], v3 offset:10240\n"
    "ds_read_b64_tr_b16 a[94:95], v3 offset:10368\n"
    "ds_read_b64_tr_b16 a[172:173], v3 offset:11264\n"
    "ds_read_b64_tr_b16 a[174:175], v3 offset:11392\n"
    "s_waitcnt lgkmcnt(0)\n"
    "s_add_u32 s35, s18, 0\n"
    "s_and_b32 s35, s35, 3\n"
    "s_add_u32 s35, s35, 16\n"
    "s_mul_i32 s35, s35, 122880\n"
    "s_add_u32 m0, s32, 9216\n"
    "s_nop 0\n"
    "buffer_load_dwordx4 v0, s[4:7], s35 offen offset:0 lds\n"
    "buffer_load_dwordx4 v1, s[4:7], s35 offen offset:1024 lds\n"
    "buffer_load_dwordx4 v2, s[4:7], s35 offen offset:2048 lds\n"
    "s_waitcnt vmcnt(36)\n"
    "ds_read_b64_tr_b16 a[16:17], v3 offset:12288\n"
    "ds_read_b64_tr_b16 a[18:19], v3 offset:12416\n"
    "ds_read_b64_tr_b16 a[96:97], v3 offset:13312\n"
    "ds_read_b64_tr_b16 a[98:99], v3 offset:13440\n"
    "ds_read_b64_tr_b16 a[176:177], v3 offset:14336\n"
    "ds_read_b64_tr_b16 a[178:179], v3 offset:14464\n"
    "s_waitcnt lgkmcnt(0)\n"
    "s_add_u32 s35, s18, 1\n"
    "s_and_b32 s35, s35, 3\n"
    "s_add_u32 s35, s35, 16\n"
    "s_mul_i32 s35, s35, 122880\n"
    "s_add_u32 m0, s32, 12288\n"
    "s_nop 0\n"
    "buffer_load_dwordx4 v0, s[4:7], s35 offen offset:0 lds\n"
    "buffer_load_dwordx4 v1, s[4:7], s35 offen offset:1024 lds\n"
    "buffer_load_dwordx4 v2, s[4:7], s35 offen offset:2048 lds\n"
    "s_waitcnt vmcnt(36)\n"
    "ds_read_b64_tr_b16 a[20:21], v3 offset:15360\n"
    "ds_read_b64_tr_b16 a[22:23], v3 offset:15488\n"
    "ds_read_b64_tr_b16 a[100:101], v3 offset:16384\n"
    "ds_read_b64_tr_b16 a[102:103], v3 offset:16512\n"
    "ds_read_b64_tr_b16 a[180:181], v3 offset:17408\n"
    "ds_read_b64_tr_b16 a[182:183], v3 offset:17536\n"
    "s_waitcnt lgkmcnt(0)\n"
    "s_add_u32 s35, s18, 2\n"
    "s_and_b32 s35, s35, 3\n"
    "s_add_u32 s35, s35, 16\n"
    "s_mul_i32 s35, s35, 122880\n"
    "s_add_u32 m0, s32, 15360\n"
    "s_nop 0\n"
    "buffer_load_dwordx4 v0, s[4:7], s35 offen offset:0 lds\n"
    "buffer_load_dwordx4 v1, s[4:7], s35 offen offset:1024 lds\n"
    "buffer_load_dwordx4 v2, s[4:7], s35 offen offset:2048 lds\n"
    "s_waitcnt vmcnt(36)\n"
    "ds_read_b64_tr_b16 a[24:25], v3 offset:18432\n"
    "ds_read_b64_tr_b16 a[26:27], v3 offset:18560\n"
    "ds_read_b64_tr_b16 a[104:105], v3 offset:19456\n"
    "ds_read_b64_tr_b16 a[106:107], v3 offset:19584\n"
    "ds_read_b64_tr_b16 a[184:185], v3 offset:20480\n"
    "ds_read_b64_tr_b16 a[186:187], v3 offset:20608\n"
    "s_waitcnt lgkmcnt(0)\n"
    "s_add_u32 s35, s18, 3\n"
    "s_and_b32 s35, s35, 3\n"
    "s_add_u32 s35, s35, 16\n"
    "s_mul_i32 s35, s35, 122880\n"
    "s_add_u32 m0, s32, 18432\n"
    "s_nop 0\n"
    "buffer_load_dwordx4 v0, s[4:7], s35 offen offset:0 lds\n"
    "buffer_load_dwordx4 v1, s[4:7], s35 offen offset:1024 lds\n"
    "buffer_load_dwordx4 v2, s[4:7], s35 offen offset:2048 lds\n"
    "s_waitcnt vmcnt(36)\n"
    "ds_read_b64_tr_b16 a[28:29], v3 offset:21504\n"
    "ds_read_b64_tr_b16 a[30:31], v3 offset:21632\n"
    "ds_read_b64_tr_b16 a[108:109], v3 offset:22528\n"
    "ds_read_b64_tr_b16 a[110:111], v3 offset:22656\n"
    "ds_read_b64_tr_b16 a[188:189], v3 offset:23552\n"
    "ds_read_b64_tr_b16 a[190:191], v3 offset:23680\n"
    "s_waitcnt lgkmcnt(0)\n"
    "s_waitcnt vmcnt(33)\n"
    "ds_read_b64_tr_b16 a[32:33], v3 offset:24576\n"
    "ds_read_b64_tr_b16 a[34:35], v3 offset:24704\n"
    "ds_read_b64_tr_b16 a[112:113], v3 offset:25600\n"
    "ds_read_b64_tr_b16 a[114:115], v3 offset:25728\n"
    "ds_read_b64_tr_b16 a[192:193], v3 offset:26624\n"
    "ds_read_b64_tr_b16 a[194:195], v3 offset:26752\n"
    "s_waitcnt lgkmcnt(0)\n"
    "s_waitcnt vmcnt(30)\n"
    "ds_read_b64_tr_b16 a[36:37], v3 offset:27648\n"
    "ds_read_b64_tr_b16 a[38:39], v3 offset:27776\n"
    "ds_read_b64_tr_b16 a[116:117], v3 offset:28672\n"
    "ds_read_b64_tr_b16 a[118:119], v3 offset:28800\n"
    "ds_read_b64_tr_b16 a[196:197], v3 offset:29696\n"
    "ds_read_b64_tr_b16 a[198:199], v3 offset:29824\n"
    "s_waitcnt lgkmcnt(0)\n"
    "s_waitcnt vmcnt(27)\n"
    "ds_read_b64_tr_b16 a[40:41], v3 offset:30720\n"
    "ds_read_b64_tr_b16 a[42:43], v3 offset:30848\n"
    "ds_read_b64_tr_b16 a[120:121], v3 offset:31744\n"
    "ds_read_b64_tr_b16 a[122:123], v3 offset:31872\n"
    "ds_read_b64_tr_b16 a[200:201], v3 offset:32768\n"
    "ds_read_b64_tr_b16 a[202:203], v3 offset:32896\n"
    "s_waitcnt lgkmcnt(0)\n"
    "s_waitcnt vmcnt(24)\n"
    "ds_read_b64_tr_b16 a[44:45], v3 offset:33792\n"
    "ds_read_b64_tr_b16 a[46:47], v3 offset:33920\n"
    "ds_read_b64_tr_b16 a[124:125], v3 offset:34816\n"
    "ds_read_b64_tr_b16 a[126:127], v3 offset:34944\n"
    "ds_read_b64_tr_b16 a[204:205], v3 offset:35840\n"
    "ds_read_b64_tr_b16 a[206:207], v3 offset:35968\n"
    "s_waitcnt lgkmcnt(0)\n"
    "s_waitcnt vmcnt(21)\n"
    "ds_read_b64_tr_b16 a[48:49], v3 offset:36864\n"
    "ds_read_b64_tr_b16 a[50:51], v3 offset:36992\n"
    "ds_read_b64_tr_b16 a[128:129], v3 offset:37888\n"
    "ds_read_b64_tr_b16 a[130:131], v3 offset:38016\n"
    "ds_read_b64_tr_b16 a[208:209], v3 offset:38912\n"
    "ds_read_b64_tr_b16 a[210:211], v3 offset:39040\n"
    "s_waitcnt lgkmcnt(0)\n"
    "s_waitcnt vmcnt(18)\n"
    "ds_read_b64_tr_b16 a[52:53], v3 offset:0\n"
    "ds_read_b64_tr_b16 a[54:55], v3 offset:128\n"
    "ds_read_b64_tr_b16 a[132:133], v3 offset:1024\n"
    "ds_read_b64_tr_b16 a[134:135], v3 offset:1152\n"
    "ds_read_b64_tr_b16 a[212:213], v3 offset:2048\n"
    "ds_read_b64_tr_b16 a[214:215], v3 offset:2176\n"
    "s_waitcnt lgkmcnt(0)\n"
    "s_waitcnt vmcnt(15)\n"
    "ds_read_b64_tr_b16 a[56:57], v3 offset:3072\n"
    "ds_read_b64_tr_b16 a[58:59], v3 offset:3200\n"
    "ds_read_b64_tr_b16 a[136:137], v3 offset:4096\n"
    "ds_read_b64_tr_b16 a[138:139], v3 offset:4224\n"
    "ds_read_b64_tr_b16 a[216:217], v3 offset:5120\n"
    "ds_read_b64_tr_b16 a[218:219], v3 offset:5248\n"
    "s_waitcnt lgkmcnt(0)\n"
    "s_waitcnt vmcnt(12)\n"
    "ds_read_b64_tr_b16 a[60:61], v3 offset:6144\n"
    "ds_read_b64_tr_b16 a[62:63], v3 offset:6272\n"
    "ds_read_b64_tr_b16 a[140:141], v3 offset:7168\n"
    "ds_read_b64_tr_b16 a[142:143], v3 offset:7296\n"
    "ds_read_b64_tr_b16 a[220:221], v3 offset:8192\n"
    "ds_read_b64_tr_b16 a[222:223], v3 offset:8320\n"
    "s_waitcnt lgkmcnt(0)\n"
    "s_waitcnt vmcnt(9)\n"
    "ds_read_b64_tr_b16 a[64:65], v3 offset:9216\n"
    "ds_read_b64_tr_b16 a[66:67], v3 offset:9344\n"
    "ds_read_b64_tr_b16 a[144:145], v3 offset:10240\n"
    "ds_read_b64_tr_b16 a[146:147], v3 offset:10368\n"
    "ds_read_b64_tr_b16 a[224:225], v3 offset:11264\n"
    "ds_read_b64_tr_b16 a[226:227], v3 offset:11392\n"
    "s_waitcnt lgkmcnt(0)\n"
    "s_waitcnt vmcnt(6)\n"
    "ds_read_b64_tr_b16 a[68:69], v3 offset:12288\n"
    "ds_read_b64_tr_b16 a[70:71], v3 offset:12416\n"
    "ds_read_b64_tr_b16 a[148:149], v3 offset:13312\n"
    "ds_read_b64_tr_b16 a[150:151], v3 offset:13440\n"
    "ds_read_b64_tr_b16 a[228:229], v3 offset:14336\n"
    "ds_read_b64_tr_b16 a[230:231], v3 offset:14464\n"
    "s_waitcnt lgkmcnt(0)\n"
    "s_waitcnt vmcnt(3)\n"
    "ds_read_b64_tr_b16 a[72:73], v3 offset:15360\n"
    "ds_read_b64_tr_b16 a[74:75], v3 offset:15488\n"
    "ds_read_b64_tr_b16 a[152:153], v3 offset:16384\n"
    "ds_read_b64_tr_b16 a[154:155], v3 offset:16512\n"
    "ds_read_b64_tr_b16 a[232:233], v3 offset:17408\n"
    "ds_read_b64_tr_b16 a[234:235], v3 offset:17536\n"
    "s_waitcnt lgkmcnt(0)\n"
    "s_waitcnt vmcnt(0)\n"
    "ds_read_b64_tr_b16 a[76:77], v3 offset:18432\n"
    "ds_read_b64_tr_b16 a[78:79], v3 offset:18560\n"
    "ds_read_b64_tr_b16 a[156:157], v3 offset:19456\n"
    "ds_read_b64_tr_b16 a[158:159], v3 offset:19584\n"
    "ds_read_b64_tr_b16 a[236:237], v3 offset:20480\n"
    "ds_read_b64_tr_b16 a[238:239], v3 offset:20608\n"
    "s_waitcnt lgkmcnt(0)\n"
    "s_waitcnt lgkmcnt(0)\n"
    "s_barrier\n"
    "v_mov_b32 v32, 0x3f803f80\n"
    "v_mov_b32 v36, 0\n"
    "v_mov_b32 v33, 0x3f803f80\n"
    "v_mov_b32 v37, 0\n"
    "v_mov_b32 v34, 0x3f803f80\n"
    "v_mov_b32 v38, 0\n"
    "v_mov_b32 v35, 0x3f803f80\n"
    "v_mov_b32 v39, 0\n"
    "s_nop 0\n"
    "v_mfma_f32_16x16x32_bf16 v[96:99], a[0:3], v[32:35], 0\n"
    "v_mfma_f32_16x16x32_bf16 v[100:103], a[80:83], v[32:35], 0\n"
    "v_mfma_f32_16x16x32_bf16 v[104:107], a[160:163], v[32:35], 0\n"
    "v_mfma_f32_16x16x32_bf16 v[96:99], a[4:7], v[32:35], v[96:99]\n"
    "v_mfma_f32_16x16x32_bf16 v[100:103], a[84:87], v[32:35], v[100:103]\n"
    "v_mfma_f32_16x16x32_bf16 v[104:107], a[164:167], v[32:35], v[104:107]\n"
    "v_mfma_f32_16x16x32_bf16 v[96:99], a[8:11], v[32:35], v[96:99]\n"
    "v_mfma_f32_16x16x32_bf16 v[100:103], a[88:91], v[32:35], v[100:103]\n"
    "v_mfma_f32_16x16x32_bf16 v[104:107], a[168:171], v[32:35], v[104:107]\n"
    "v_mfma_f32_16x16x32_bf16 v[96:99], a[12:15], v[32:35], v[96:99]\n"
    "v_mfma_f32_16x16x32_bf16 v[100:103], a[92:95], v[32:35], v[100:103]\n"
    "v_mfma_f32_16x16x32_bf16 v[104:107], a[172:175], v[32:35], v[104:107]\n"
    "v_mfma_f32_16x16x32_bf16 v[96:99], a[16:19], v[32:35], v[96:99]\n"
    "v_mfma_f32_16x16x32_bf16 v[100:103], a[96:99], v[32:35], v[100:103]\n"
    "v_mfma_f32_16x16x32_bf16 v[104:107], a[176:179], v[32:35], v[104:107]\n"
    "v_mfma_f32_16x16x32_bf16 v[96:99], a[20:23], v[32:35], v[96:99]\n"
    "v_mfma_f32_16x16x32_bf16 v[100:103], a[100:103], v[32:35], v[100:103]\n"
    "v_mfma_f32_16x16x32_bf16 v[104:107], a[180:183], v[32:35], v[104:107]\n"
    "v_mfma_f32_16x16x32_bf16 v[96:99], a[24:27], v[32:35], v[96:99]\n"
    "v_mfma_f32_16x16x32_bf16 v[100:103], a[104:107], v[32:35], v[100:103]\n"
    "v_mfma_f32_16x16x32_bf16 v[104:107], a[184:187], v[32:35], v[104:107]\n"
    "v_mfma_f32_16x16x32_bf16 v[96:99], a[28:31], v[32:35], v[96:99]\n"
    "v_mfma_f32_16x16x32_bf16 v[100:103], a[108:111], v[32:35], v[100:103]\n"
    "v_mfma_f32_16x16x32_bf16 v[104:107], a[188:191], v[32:35], v[104:107]\n"
    "v_mfma_f32_16x16x32_bf16 v[96:99], a[32:35], v[32:35], v[96:99]\n"
    "v_mfma_f32_16x16x32_bf16 v[100:103], a[112:115], v[32:35], v[100:103]\n"
    "v_mfma_f32_16x16x32_bf16 v[104:107], a[192:195], v[32:35], v[104:107]\n"
    "v_mfma_f32_16x16x32_bf16 v[96:99], a[36:39], v[32:35], v[96:99]\n"
    "v_mfma_f32_16x16x32_bf16 v[100:103], a[116:119], v[32:35], v[100:103]\n"
    "v_mfma_f32_16x16x32_bf16 v[104:107], a[196:199], v[32:35], v[104:107]\n"
    "v_mfma_f32_16x16x32_bf16 v[96:99], a[40:43], v[32:35], v[96:99]\n"
    "v_mfma_f32_16x16x32_bf16 v[100:103], a[120:123], v[32:35], v[100:103]\n"
    "v_mfma_f32_16x16x32_bf16 v[104:107], a[200:203], v[32:35], v[104:107]\n"
    "v_mfma_f32_16x16x32_bf16 v[96:99], a[44:47], v[32:35], v[96:99]\n"
    "v_mfma_f32_16x16x32_bf16 v[100:103], a[124:127], v[32:35], v[100:103]\n"
    "v_mfma_f32_16x16x32_bf16 v[104:107], a[204:207], v[32:35], v[104:107]\n"
    "v_mfma_f32_16x16x32_bf16 v[96:99], a[48:51], v[32:35], v[96:99]\n"
    "v_mfma_f32_16x16x32_bf16 v[100:103], a[128:131], v[32:35], v[100:103]\n"
    "v_mfma_f32_16x16x32_bf16 v[104:107], a[208:211], v[32:35], v[104:107]\n"
    "v_mfma_f32_16x16x32_bf16 v[96:99], a[52:55], v[32:35], v[96:99]\n"
    "v_mfma_f32_16x16x32_bf16 v[100:103], a[132:135], v[32:35], v[100:103]\n"
    "v_mfma_f32_16x16x32_bf16 v[104:107], a[212:215], v[32:35], v[104:107]\n"
    "v_mfma_f32_16x16x32_bf16 v[96:99], a[56:59], v[32:35], v[96:99]\n"
    "v_mfma_f32_16x16x32_bf16 v[100:103], a[136:139], v[32:35], v[100:103]\n"
    "v_mfma_f32_16x16x32_bf16 v[104:107], a[216:219], v[32:35], v[104:107]\n"
    "v_mfma_f32_16x16x32_bf16 v[96:99], a[60:63], v[32:35], v[96:99]\n"
    "v_mfma_f32_16x16x32_bf16 v[100:103], a[140:143], v[32:35], v[100:103]\n"
    "v_mfma_f32_16x16x32_bf16 v[104:107], a[220:223], v[32:35], v[104:107]\n"
    "v_mfma_f32_16x16x32_bf16 v[96:99], a[64:67], v[32:35], v[96:99]\n"
    "v_mfma_f32_16x16x32_bf16 v[100:103], a[144:147], v[32:35], v[100:103]\n"
    "v_mfma_f32_16x16x32_bf16 v[104:107], a[224:227], v[32:35], v[104:107]\n"
    "v_mfma_f32_16x16x32_bf16 v[96:99], a[68:71], v[32:35], v[96:99]\n"
    "v_mfma_f32_16x16x32_bf16 v[100:103], a[148:151], v[32:35], v[100:103]\n"
    "v_mfma_f32_16x16x32_bf16 v[104:107], a[228:231], v[32:35], v[104:107]\n"
    "v_mfma_f32_16x16x32_bf16 v[96:99], a[72:75], v[32:35], v[96:99]\n"
    "v_mfma_f32_16x16x32_bf16 v[100:103], a[152:155], v[32:35], v[100:103]\n"
    "v_mfma_f32_16x16x32_bf16 v[104:107], a[232:235], v[32:35], v[104:107]\n"
    "v_mfma_f32_16x16x32_bf16 v[96:99], a[76:79], v[32:35], v[96:99]\n"
    "v_mfma_f32_16x16x32_bf16 v[100:103], a[156:159], v[32:35], v[100:103]\n"
    "v_mfma_f32_16x16x32_bf16 v[104:107], a[236:239], v[32:35], v[104:107]\n"
    "s_lshl_b32 s35, s18, 13\n"
    "v_add_u32 v8, s35, v228\n"
    "ds_write_b128 v8, v[36:39] offset:40960\n"
    "ds_write_b128 v8, v[36:39] offset:41984\n"
    "ds_write_b128 v8, v[36:39] offset:43008\n"
    "ds_write_b128 v8, v[36:39] offset:44032\n"
    "ds_write_b128 v8, v[36:39] offset:45056\n"
    "ds_write_b128 v8, v[36:39] offset:46080\n"
    "ds_write_b128 v8, v[36:39] offset:47104\n"
    "ds_write_b128 v8, v[36:39] offset:48128\n"
    "s_nop 7\n"
    "s_nop 7\n"
    "s_mov_b64 exec, s[16:17]\n"
    "v_accvgpr_write_b32 a0, v32\n"
    "v_accvgpr_write_b32 a1, v32\n"
    "v_accvgpr_write_b32 a2, v32\n"
    "v_accvgpr_write_b32 a3, v32\n"
    "v_accvgpr_write_b32 a4, v32\n"
    "v_accvgpr_write_b32 a5, v32\n"
    "v_accvgpr_write_b32 a6, v32\n"
    "v_accvgpr_write_b32 a7, v32\n"
    "v_accvgpr_write_b32 a8, v32\n"
    "v_accvgpr_write_b32 a9, v32\n"
    "v_accvgpr_write_b32 a10, v32\n"
    "v_accvgpr_write_b32 a11, v32\n"
    "v_accvgpr_write_b32 a12, v32\n"
    "v_accvgpr_write_b32 a13, v32\n"
    "v_accvgpr_write_b32 a14, v32\n"
    "v_accvgpr_write_b32 a15, v32\n"
    "v_accvgpr_write_b32 a16, v32\n"
    "v_accvgpr_write_b32 a17, v32\n"
    "v_accvgpr_write_b32 a18, v32\n"
    "v_accvgpr_write_b32 a19, v32\n"
    "v_accvgpr_write_b32 a20, v32\n"
    "v_accvgpr_write_b32 a21, v32\n"
    "v_accvgpr_write_b32 a22, v32\n"
    "v_accvgpr_write_b32 a23, v32\n"
    "v_accvgpr_write_b32 a24, v32\n"
    "v_accvgpr_write_b32 a25, v32\n"
    "v_accvgpr_write_b32 a26, v32\n"
    "v_accvgpr_write_b32 a27, v32\n"
    "v_accvgpr_write_b32 a28, v32\n"
    "v_accvgpr_write_b32 a29, v32\n"
    "v_accvgpr_write_b32 a30, v32\n"
    "v_accvgpr_write_b32 a31, v32\n"
    "v_accvgpr_write_b32 a32, v32\n"
    "v_accvgpr_write_b32 a33, v32\n"
    "v_accvgpr_write_b32 a34, v32\n"
    "v_accvgpr_write_b32 a35, v32\n"
    "v_accvgpr_write_b32 a36, v32\n"
    "v_accvgpr_write_b32 a37, v32\n"
    "v_accvgpr_write_b32 a38, v32\n"
    "v_accvgpr_write_b32 a39, v32\n"
    "v_accvgpr_write_b32 a40, v32\n"
    "v_accvgpr_write_b32 a41, v32\n"
    "v_accvgpr_write_b32 a42, v32\n"
    "v_accvgpr_write_b32 a43, v32\n"
    "v_accvgpr_write_b32 a44, v32\n"
    "v_accvgpr_write_b32 a45, v32\n"
    "v_accvgpr_write_b32 a46, v32\n"
    "v_accvgpr_write_b32 a47, v32\n"
    "v_accvgpr_write_b32 a48, v32\n"
    "v_accvgpr_write_b32 a49, v32\n"
    "v_accvgpr_write_b32 a50, v32\n"
    "v_accvgpr_write_b32 a51, v32\n"
    "v_accvgpr_write_b32 a52, v32\n"
    "v_accvgpr_write_b32 a53, v32\n"
    "v_accvgpr_write_b32 a54, v32\n"
    "v_accvgpr_write_b32 a55, v32\n"
    "v_accvgpr_write_b32 a56, v32\n"
    "v_accvgpr_write_b32 a57, v32\n"
    "v_accvgpr_write_b32 a58, v32\n"
    "v_accvgpr_write_b32 a59, v32\n"
    "v_accvgpr_write_b32 a60, v32\n"
    "v_accvgpr_write_b32 a61, v32\n"
    "v_accvgpr_write_b32 a62, v32\n"
    "v_accvgpr_write_b32 a63, v32\n"
    "v_accvgpr_write_b32 a64, v32\n"
    "v_accvgpr_write_b32 a65, v32\n"
    "v_accvgpr_write_b32 a66, v32\n"
    "v_accvgpr_write_b32 a67, v32\n"
    "v_accvgpr_write_b32 a68, v32\n"
    "v_accvgpr_write_b32 a69, v32\n"
    "v_accvgpr_write_b32 a70, v32\n"
    "v_accvgpr_write_b32 a71, v32\n"
    "v_accvgpr_write_b32 a72, v32\n"
    "v_accvgpr_write_b32 a73, v32\n"
    "v_accvgpr_write_b32 a74, v32\n"
    "v_accvgpr_write_b32 a75, v32\n"
    "v_accvgpr_write_b32 a76, v32\n"
    "v_accvgpr_write_b32 a77, v32\n"
    "v_accvgpr_write_b32 a78, v32\n"
    "v_accvgpr_write_b32 a79, v32\n"
    "s_mov_b64 exec, -1\n"
    "s_mov_b32 s30, -2\n"
    "s_mov_b32 s30, 0\n"
    "s_mov_b32 s31, 1\n"
    "s_lshl_b32 s34, s18, 11\n"
    "s_add_u32 s32, s20, s19\n"
    "s_mov_b32 s37, 0x3ea52b5c\n"
    "s_mov_b64 s[38:39], 0\n"
    "s_mov_b64 s[40:41], 0\n"
    "s_mov_b32 s36, 0\n"
    "s_mov_b32 s42, 0xaaaaaaaa\n"
    "s_mov_b32 s43, 0xaaaaaaaa\n"
    "s_waitcnt lgkmcnt(0)\n"
    ".Lb1_top_%=:\n"
    "s_cmp_lg_u32 s18, 0\n"
    "s_cbranch_scc1 .Lb1_pd2_%=\n"
    ".Lb1_rt3_%=:\n"
    "s_memrealtime s[26:27]\n"
    "s_waitcnt lgkmcnt(0)\n"
    "s_sub_u32 s28, s36, s26\n"
    "s_cmp_lt_u32 s28, 600\n"
    "s_cbranch_scc1 .Lb1_rt3_%=\n"
    ".Lb1_po1_%=:\n"
    "buffer_load_dwordx4 v[108:111], v229, s[0:3], s21 offen sc1\n"
    "s_memrealtime s[26:27]\n"
    "s_waitcnt vmcnt(0) lgkmcnt(0)\n"
    "s_add_u32 s36, s26, 274\n"
    "v_min3_i32 v108, v108, v109, v110\n"
    "v_min_i32 v108, v108, v111\n"
    "v_cmp_gt_i32 vcc, s31, v108\n"
    "s_nop 0\n"
    "s_cbranch_vccz .Lb1_pd2_%=\n"
    "s_add_u32 s24, s24, 1\n"
    "s_cmp_lt_u32 s24, 0x40000\n"
    "s_cbranch_scc1 .Lb1_sp4_%=\n"
    "v_mov_b32 v108, 60\n"
    "s_mov_b32 s28, 33728\n"
    "buffer_store_dword v108, off, s[0:3], s28 sc1\n"
    "s_waitcnt vmcnt(0)\n"
    "s_branch .Lb1_pd2_%=\n"
    ".Lb1_sp4_%=:\n"
    "s_sleep 2\n"
    "s_branch .Lb1_po1_%=\n"
    ".Lb1_pd2_%=:\n"
    "s_barrier\n"
    "s_mov_b32 s33, s32\n"
    "s_add_u32 m0, s34, 0\n"
    "s_nop 0\n"
    "buffer_load_dwordx4 v228, s[0:3], s32 offen sc1 lds\n"
    "buffer_load_dwordx4 v228, s[0:3], s32 offen offset:1024 sc1 lds\n"
    "s_add_u32 m0, s34, 8192\n"
    "s_add_u32 s32, s32, 0x4000\n"
    "buffer_load_dwordx4 v228, s[0:3], s32 offen sc1 lds\n"
    "buffer_load_dwordx4 v228, s[0:3], s32 offen offset:1024 sc1 lds\n"
    "s_add_u32 m0, s34, 16384\n"
    "s_add_u32 s32, s32, 0x4000\n"
    "buffer_load_dwordx4 v228, s[0:3], s32 offen sc1 lds\n"
    "buffer_load_dwordx4 v228, s[0:3], s32 offen offset:1024 sc1 lds\n"
    "s_add_u32 m0, s34, 24576\n"
    "s_add_u32 s32, s32, 0x4000\n"
    "buffer_load_dwordx4 v228, s[0:3], s32 offen sc1 lds\n"
    "buffer_load_dwordx4 v228, s[0:3], s32 offen offset:1024 sc1 lds\n"
    "s_add_u32 m0, s34, 32768\n"
    "s_add_u32 s32, s32, 0x4000\n"
    "buffer_load_dwordx4 v228, s[0:3], s32 offen sc1 lds\n"
    "buffer_load_dwordx4 v228, s[0:3], s32 offen offset:1024 sc1 lds\n"
    "s_waitcnt vmcnt(8)\n"
    "s_barrier\n"
    "ds_read_b128 v[32:35], v224 offset:0\n"
    "ds_read_b128 v[36:39], v224 offset:1024\n"
    "ds_read_b128 v[40:43], v225 offset:0\n"
    "ds_read_b128 v[44:47], v225 offset:1024\n"
    "s_waitcnt lgkmcnt(2)\n"
    "v_mfma_f32_16x16x32_bf16 v[24:27], v[32:35], v[32:35], 0\n"
    "ds_read_b128 v[48:51], v226 offset:0\n"
    "v_mfma_f32_16x16x32_bf16 v[28:31], v[36:39], v[36:39], 0\n"
    "ds_read_b128 v[52:55], v226 offset:1024\n"
    "v_mfma_f32_16x16x32_bf16 v[0:3], a[0:3], v[32:35], 0\n"
    "v_mfma_f32_16x16x32_bf16 v[4:7], a[0:3], v[36:39], 0\n"
    "v_mfma_f32_16x16x32_bf16 v[8:11], a[80:83], v[32:35], 0\n"
    "s_add_u32 s35, s30, 1\n"
    "s_lshl_b32 s32, s35, 19\n"
    "s_lshl_b32 s29, s35, 11\n"
    "v_mfma_f32_16x16x32_bf16 v[12:15], a[80:83], v[36:39], 0\n"
    "s_xor_b32 s29, s29, s19\n"
    "s_and_b32 s29, s29, 0x2000\n"
    "s_add_u32 s32, s32, s29\n"
    "v_mfma_f32_16x16x32_bf16 v[16:19], a[160:163], v[32:35], 0\n"
    "s_add_u32 s32, s32, s20\n"
    "v_xor_b32 v232, 0x800, v232\n"
    "v_mfma_f32_16x16x32_bf16 v[20:23], a[160:163], v[36:39], 0\n"
    "s_waitcnt lgkmcnt(2)\n"
    "v_xor_b32 v233, 0x800, v233\n"
    "v_mfma_f32_16x16x32_bf16 v[0:3], a[4:7], v[40:43], v[0:3]\n"
    "ds_read_b128 v[56:59], v227 offset:0\n"
    "v_xor_b32 v235, 0x800, v235\n"
    "v_mfma_f32_16x16x32_bf16 v[4:7], a[4:7], v[44:47], v[4:7]\n"
    "ds_read_b128 v[60:63], v227 offset:1024\n"
    "v_xor_b32 v236, 0x800, v236\n"
    "v_mfma_f32_16x16x32_bf16 v[8:11], a[84:87], v[40:43], v[8:11]\n"
    "v_xor_b32 v237, 0x800, v237\n"
    "v_xor_b32 v238, 0x800, v238\n"
    "v_mfma_f32_16x16x32_bf16 v[12:15], a[84:87], v[44:47], v[12:15]\n"
    "v_xor_b32 v239, 0x800, v239\n"
    "v_xor_b32 v240, 0x800, v240\n"
    "v_mfma_f32_16x16x32_bf16 v[16:19], a[164:167], v[40:43], v[16:19]\n"
    "v_mov_b32 v116, s31\n"
    "v_mfma_f32_16x16x32_bf16 v[20:23], a[164:167], v[44:47], v[20:23]\n"
    "s_waitcnt lgkmcnt(2)\n"
    "v_mfma_f32_16x16x32_bf16 v[0:3], a[8:11], v[48:51], v[0:3]\n"
    "s_waitcnt vmcnt(6)\n"
    "s_barrier\n"
    "v_mfma_f32_16x16x32_bf16 v[4:7], a[8:11], v[52:55], v[4:7]\n"
    "ds_read_b128 v[64:67], v224 offset:8192\n"
    "v_mfma_f32_16x16x32_bf16 v[8:11], a[88:91], v[48:51], v[8:11]\n"
    "ds_read_b128 v[68:71], v224 offset:9216\n"
    "v_mfma_f32_16x16x32_bf16 v[12:15], a[88:91], v[52:55], v[12:15]\n"
    "v_mfma_f32_16x16x32_bf16 v[16:19], a[168:171], v[48:51], v[16:19]\n"
    "v_mfma_f32_16x16x32_bf16 v[20:23], a[168:171], v[52:55], v[20:23]\n"
    "s_waitcnt lgkmcnt(2)\n"
    "v_mfma_f32_16x16x32_bf16 v[0:3], a[12:15], v[56:59], v[0:3]\n"
    "ds_read_b128 v[72:75], v225 offset:8192\n"
    "v_mfma_f32_16x16x32_bf16 v[4:7], a[12:15], v[60:63], v[4:7]\n"
    "ds_read_b128 v[76:79], v225 offset:9216\n"
    "v_mfma_f32_16x16x32_bf16 v[8:11], a[92:95], v[56:59], v[8:11]\n"
    "v_mfma_f32_16x16x32_bf16 v[12:15], a[92:95], v[60:63], v[12:15]\n"
    "v_mfma_f32_16x16x32_bf16 v[16:19], a[172:175], v[56:59], v[16:19]\n"
    "v_mfma_f32_16x16x32_bf16 v[20:23], a[172:175], v[60:63], v[20:23]\n"
    "s_waitcnt lgkmcnt(2)\n"
    "v_mfma_f32_16x16x32_bf16 v[24:27], v[64:67], v[64:67], v[24:27]\n"
    "ds_read_b128 v[80:83], v226 offset:8192\n"
    "v_mfma_f32_16x16x32_bf16 v[28:31], v[68:71], v[68:71], v[28:31]\n"
    "ds_read_b128 v[84:87], v226 offset:9216\n"
    "v_mfma_f32_16x16x32_bf16 v[0:3], a[16:19], v[64:67], v[0:3]\n"
    "v_mfma_f32_16x16x32_bf16 v[4:7], a[16:19], v[68:71], v[4:7]\n"
    "v_mfma_f32_16x16x32_bf16 v[8:11], a[96:99], v[64:67], v[8:11]\n"
    "v_mfma_f32_16x16x32_bf16 v[12:15], a[96:99], v[68:71], v[12:15]\n"
    "v_mfma_f32_16x16x32_bf16 v[16:19], a[176:179], v[64:67], v[16:19]\n"
    "v_mfma_f32_16x16x32_bf16 v[20:23], a[176:179], v[68:71], v[20:23]\n"
    "s_waitcnt lgkmcnt(2)\n"
    "v_mfma_f32_16x16x32_bf16 v[0:3], a[20:23], v[72:75], v[0:3]\n"
    "ds_read_b128 v[88:91], v227 offset:8192\n"
    "v_mfma_f32_16x16x32_bf16 v[4:7], a[20:23], v[76:79], v[4:7]\n"
    "ds_read_b128 v[92:95], v227 offset:9216\n"
    "v_mfma_f32_16x16x32_bf16 v[8:11], a[100:103], v[72:75], v[8:11]\n"
    "v_mfma_f32_16x16x32_bf16 v[12:15], a[100:103], v[76:79], v[12:15]\n"
    "v_mfma_f32_16x16x32_bf16 v[16:19], a[180:183], v[72:75], v[16:19]\n"
    "v_mfma_f32_16x16x32_bf16 v[20:23], a[180:183], v[76:79], v[20:23]\n"
    "s_waitcnt lgkmcnt(2)\n"
    "v_mfma_f32_16x16x32_bf16 v[0:3], a[24:27], v[80:83], v[0:3]\n"
    "s_waitcnt vmcnt(4)\n"
    "s_barrier\n"
    "v_mfma_f32_16x16x32_bf16 v[4:7], a[24:27], v[84:87], v[4:7]\n"
    "ds_read_b128 v[32:35], v224 offset:16384\n"
    "v_mfma_f32_16x16x32_bf16 v[8:11], a[104:107], v[80:83], v[8:11]\n"
    "ds_read_b128 v[36:39], v224 offset:17408\n"
    "v_mfma_f32_16x16x32_bf16 v[12:15], a[104:107], v[84:87], v[12:15]\n"
    "v_mfma_f32_16x16x32_bf16 v[16:19], a[184:187], v[80:83], v[16:19]\n"
    "v_mfma_f32_16x16x32_bf16 v[20:23], a[184:187], v[84:87], v[20:23]\n"
    "s_waitcnt lgkmcnt(2)\n"
    "v_mfma_f32_16x16x32_bf16 v[0:3], a[28:31], v[88:91], v[0:3]\n"
    "ds_read_b128 v[40:43], v225 offset:16384\n"
    "v_mfma_f32_16x16x32_bf16 v[4:7], a[28:31], v[92:95], v[4:7]\n"
    "ds_read_b128 v[44:47], v225 offset:17408\n"
    "v_mfma_f32_16x16x32_bf16 v[8:11], a[108:111], v[88:91], v[8:11]\n"
    "v_mfma_f32_16x16x32_bf16 v[12:15], a[108:111], v[92:95], v[12:15]\n"
    "v_mfma_f32_16x16x32_bf16 v[16:19], a[188:191], v[88:91], v[16:19]\n"
    "v_mfma_f32_16x16x32_bf16 v[20:23], a[188:191], v[92:95], v[20:23]\n"
    "s_waitcnt lgkmcnt(2)\n"
    "v_mfma_f32_16x16x32_bf16 v[24:27], v[32:35], v[32:35], v[24:27]\n"
    "ds_read_b128 v[48:51], v226 offset:16384\n"
    "v_mfma_f32_16x16x32_bf16 v[28:31], v[36:39], v[36:39], v[28:31]\n"
    "ds_read_b128 v[52:55], v226 offset:17408\n"
    "v_mfma_f32_16x16x32_bf16 v[0:3], a[32:35], v[32:35], v[0:3]\n"
    "v_mfma_f32_16x16x32_bf16 v[4:7], a[32:35], v[36:39], v[4:7]\n"
    "v_mfma_f32_16x16x32_bf16 v[8:11], a[112:115], v[32:35], v[8:11]\n"
    "v_mfma_f32_16x16x32_bf16 v[12:15], a[112:115], v[36:39], v[12:15]\n"
    "v_mfma_f32_16x16x32_bf16 v[16:19], a[192:195], v[32:35], v[16:19]\n"
    "v_mfma_f32_16x16x32_bf16 v[20:23], a[192:195], v[36:39], v[20:23]\n"
    "s_waitcnt lgkmcnt(2)\n"
    "v_mfma_f32_16x16x32_bf16 v[0:3], a[36:39], v[40:43], v[0:3]\n"
    "ds_read_b128 v[56:59], v227 offset:16384\n"
    "v_mfma_f32_16x16x32_bf16 v[4:7], a[36:39], v[44:47], v[4:7]\n"
    "ds_read_b128 v[60:63], v227 offset:17408\n"
    "v_mfma_f32_16x16x32_bf16 v[8:11], a[116:119], v[40:43], v[8:11]\n"
    "v_mfma_f32_16x16x32_bf16 v[12:15], a[116:119], v[44:47], v[12:15]\n"
    "v_mfma_f32_16x16x32_bf16 v[16:19], a[196:199], v[40:43], v[16:19]\n"
    "v_mfma_f32_16x16x32_bf16 v[20:23], a[196:199], v[44:47], v[20:23]\n"
    "s_waitcnt lgkmcnt(2)\n"
    "v_mfma_f32_16x16x32_bf16 v[0:3], a[40:43], v[48:51], v[0:3]\n"
    "s_waitcnt vmcnt(2)\n"
    "s_barrier\n"
    "v_mfma_f32_16x16x32_bf16 v[4:7], a[40:43], v[52:55], v[4:7]\n"
    "ds_read_b128 v[64:67], v224 offset:24576\n"
    "v_mfma_f32_16x16x32_bf16 v[8:11], a[120:123], v[48:51], v[8:11]\n"
    "ds_read_b128 v[68:71], v224 offset:25600\n"
    "v_mfma_f32_16x16x32_bf16 v[12:15], a[120:123], v[52:55], v[12:15]\n"
    "ds_read_b128 v[72:75], v225 offset:24576\n"
    "v_mfma_f32_16x16x32_bf16 v[16:19], a[200:203], v[48:51], v[16:19]\n"
    "ds_read_b128 v[76:79], v225 offset:25600\n"
    "v_mfma_f32_16x16x32_bf16 v[20:23], a[200:203], v[52:55], v[20:23]\n"
    "s_waitcnt lgkmcnt(4)\n"
    "ds_read_b128 v[80:83], v226 offset:24576\n"
    "v_mfma_f32_16x16x32_bf16 v[0:3], a[44:47], v[56:59], v[0:3]\n"
    "ds_read_b128 v[84:87], v226 offset:25600\n"
    "v_mfma_f32_16x16x32_bf16 v[4:7], a[44:47], v[60:63], v[4:7]\n"
    "ds_read_b128 v[88:91], v227 offset:24576\n"
    "v_mfma_f32_16x16x32_bf16 v[8:11], a[124:127], v[56:59], v[8:11]\n"
    "ds_read_b128 v[92:95], v227 offset:25600\n"
    "s_waitcnt vmcnt(0)\n"
    "v_mfma_f32_16x16x32_bf16 v[12:15], a[124:127], v[60:63], v[12:15]\n"
    "s_barrier\n"
    "ds_read_b128 v[32:35], v224 offset:32768\n"
    "v_mfma_f32_16x16x32_bf16 v[16:19], a[204:207], v[56:59], v[16:19]\n"
    "ds_read_b128 v[36:39], v224 offset:33792\n"
    "v_mfma_f32_16x16x32_bf16 v[20:23], a[204:207], v[60:63], v[20:23]\n"
    "s_waitcnt lgkmcnt(8)\n"
    "ds_read_b128 v[40:43], v225 offset:32768\n"
    "v_mfma_f32_16x16x32_bf16 v[24:27], v[64:67], v[64:67], v[24:27]\n"
    "ds_read_b128 v[44:47], v225 offset:33792\n"
    "v_mfma_f32_16x16x32_bf16 v[28:31], v[68:71], v[68:71], v[28:31]\n"
    "ds_read_b128 v[48:51], v226 offset:32768\n"
    "v_mfma_f32_16x16x32_bf16 v[0:3], a[48:51], v[64:67], v[0:3]\n"
    "ds_read_b128 v[52:55], v226 offset:33792\n"
    "v_mfma_f32_16x16x32_bf16 v[4:7], a[48:51], v[68:71], v[4:7]\n"
    "s_waitcnt lgkmcnt(10)\n"
    "ds_read_b128 v[56:59], v227 offset:32768\n"
    "v_mfma_f32_16x16x32_bf16 v[0:3], a[52:55], v[72:75], v[0:3]\n"
    "ds_read_b128 v[60:63], v227 offset:33792\n"
    "v_mfma_f32_16x16x32_bf16 v[4:7], a[52:55], v[76:79], v[4:7]\n"
    "s_waitcnt lgkmcnt(10)\n"
    "v_mfma_f32_16x16x32_bf16 v[0:3], a[56:59], v[80:83], v[0:3]\n"
    "v_mfma_f32_16x16x32_bf16 v[4:7], a[56:59], v[84:87], v[4:7]\n"
    "s_waitcnt lgkmcnt(8)\n"
    "v_mfma_f32_16x16x32_bf16 v[0:3], a[60:63], v[88:91], v[0:3]\n"
    "v_mfma_f32_16x16x32_bf16 v[4:7], a[60:63], v[92:95], v[4:7]\n"
    "s_waitcnt lgkmcnt(6)\n"
    "v_mfma_f32_16x16x32_bf16 v[24:27], v[32:35], v[32:35], v[24:27]\n"
    "v_mfma_f32_16x16x32_bf16 v[28:31], v[36:39], v[36:39], v[28:31]\n"
    "v_mfma_f32_16x16x32_bf16 v[0:3], a[64:67], v[32:35], v[0:3]\n"
    "v_mfma_f32_16x16x32_bf16 v[4:7], a[64:67], v[36:39], v[4:7]\n"
    "s_waitcnt lgkmcnt(4)\n"
    "v_mfma_f32_16x16x32_bf16 v[0:3], a[68:71], v[40:43], v[0:3]\n"
    "v_mfma_f32_16x16x32_bf16 v[4:7], a[68:71], v[44:47], v[4:7]\n"
    "s_waitcnt lgkmcnt(2)\n"
    "v_mfma_f32_16x16x32_bf16 v[0:3], a[72:75], v[48:51], v[0:3]\n"
    "v_mfma_f32_16x16x32_bf16 v[4:7], a[72:75], v[52:55], v[4:7]\n"
    "s_waitcnt lgkmcnt(0)\n"
    "v_mfma_f32_16x16x32_bf16 v[0:3], a[76:79], v[56:59], v[0:3]\n"
    "v_mfma_f32_16x16x32_bf16 v[4:7], a[76:79], v[60:63], v[4:7]\n"
    "ds_write_b128 v241, v[24:27]\n"
    "ds_write_b128 v241, v[28:31] offset:1024\n"
    "v_mfma_f32_16x16x32_bf16 v[8:11], a[128:131], v[64:67], v[8:11]\n"
    "v_mfma_f32_16x16x32_bf16 v[12:15], a[128:131], v[68:71], v[12:15]\n"
    "v_mfma_f32_16x16x32_bf16 v[8:11], a[132:135], v[72:75], v[8:11]\n"
    "v_mfma_f32_16x16x32_bf16 v[12:15], a[132:135], v[76:79], v[12:15]\n"
    "v_mfma_f32_16x16x32_bf16 v[8:11], a[136:139], v[80:83], v[8:11]\n"
    "v_mfma_f32_16x16x32_bf16 v[12:15], a[136:139], v[84:87], v[12:15]\n"
    "ds_write2st64_b32 v243, v0, v4 offset1:1\n"
    "s_waitcnt lgkmcnt(0)\n"
    "v_mfma_f32_16x16x32_bf16 v[8:11], a[140:143], v[88:91], v[8:11]\n"
    "s_barrier\n"
    "ds_read2st64_b32 v[24:25], v242 offset1:8\n"
    "v_mfma_f32_16x16x32_bf16 v[12:15], a[140:143], v[92:95], v[12:15]\n"
    "ds_read2st64_b32 v[26:27], v242 offset0:16 offset1:24\n"
    "ds_read_b32 v108, v244\n"
    "v_mfma_f32_16x16x32_bf16 v[8:11], a[144:147], v[32:35], v[8:11]\n"
    "v_mfma_f32_16x16x32_bf16 v[12:15], a[144:147], v[36:39], v[12:15]\n"
    "v_mfma_f32_16x16x32_bf16 v[8:11], a[148:151], v[40:43], v[8:11]\n"
    "v_mfma_f32_16x16x32_bf16 v[12:15], a[148:151], v[44:47], v[12:15]\n"
    "v_mfma_f32_16x16x32_bf16 v[8:11], a[152:155], v[48:51], v[8:11]\n"
    "s_waitcnt lgkmcnt(0)\n"
    "v_add_f32 v24, v24, v26\n"
    "v_mfma_f32_16x16x32_bf16 v[12:15], a[152:155], v[52:55], v[12:15]\n"
    "v_add_f32 v25, v25, v27\n"
    "v_add_f32 v112, v24, v25\n"
    "v_mfma_f32_16x16x32_bf16 v[8:11], a[156:159], v[56:59], v[8:11]\n"
    "v_mul_f32 v108, 0x3acccccd, v108\n"
    "v_mul_f32 v112, 0x3acccccd, v112\n"
    "v_mfma_f32_16x16x32_bf16 v[12:15], a[156:159], v[60:63], v[12:15]\n"
    "v_fma_f32 v112, -v108, v108, v112\n"
    "v_max_f32 v112, 0, v112\n"
    "v_mfma_f32_16x16x32_bf16 v[16:19], a[208:211], v[64:67], v[16:19]\n"
    "v_cvt_pk_bf16_f32 v108, v108, v112\n"
    "v_and_b32 v112, 0xffff0000, v108\n"
    "v_mfma_f32_16x16x32_bf16 v[20:23], a[208:211], v[68:71], v[20:23]\n"
    "v_lshlrev_b32 v108, 16, v108\n"
    "v_add_f32 v112, 0x3727c5ac, v112\n"
    "v_mfma_f32_16x16x32_bf16 v[16:19], a[212:215], v[72:75], v[16:19]\n"
    "v_mov_b32 v110, v108\n"
    "v_cvt_pk_bf16_f32 v112, v112, v112\n"
    "v_mfma_f32_16x16x32_bf16 v[20:23], a[212:215], v[76:79], v[20:23]\n"
    "v_lshlrev_b32 v112, 16, v112\n"
    "v_mfma_f32_16x16x32_bf16 v[16:19], a[216:219], v[80:83], v[16:19]\n"
    "v_sqrt_f32 v112, v112\n"
    "v_mfma_f32_16x16x32_bf16 v[20:23], a[216:219], v[84:87], v[20:23]\n"
    "v_permlane32_swap_b32 v108, v110\n"
    "v_mfma_f32_16x16x32_bf16 v[16:19], a[220:223], v[88:91], v[16:19]\n"
    "v_cvt_pk_bf16_f32 v112, v112, v112\n"
    "v_lshlrev_b32 v112, 16, v112\n"
    "v_mfma_f32_16x16x32_bf16 v[20:23], a[220:223], v[92:95], v[20:23]\n"
    "v_rcp_f32 v112, v112\n"
    "v_mfma_f32_16x16x32_bf16 v[16:19], a[224:227], v[32:35], v[16:19]\n"
    "v_mov_b32 v114, v112\n"
    "v_mfma_f32_16x16x32_bf16 v[20:23], a[224:227], v[36:39], v[20:23]\n"
    "v_mfma_f32_16x16x32_bf16 v[16:19], a[228:231], v[40:43], v[16:19]\n"
    "v_permlane32_swap_b32 v112, v114\n"
    "v_mfma_f32_16x16x32_bf16 v[20:23], a[228:231], v[44:47], v[20:23]\n"
    "v_fma_f32 v0, v96, -v108, v0\n"
    "v_fma_f32 v1, v97, -v108, v1\n"
    "v_mfma_f32_16x16x32_bf16 v[16:19], a[232:235], v[48:51], v[16:19]\n"
    "v_fma_f32 v2, v98, -v108, v2\n"
    "v_fma_f32 v3, v99, -v108, v3\n"
    "v_mfma_f32_16x16x32_bf16 v[20:23], a[232:235], v[52:55], v[20:23]\n"
    "v_fma_f32 v4, v96, -v110, v4\n"
    "v_fma_f32 v5, v97, -v110, v5\n"
    "v_mfma_f32_16x16x32_bf16 v[16:19], a[236:239], v[56:59], v[16:19]\n"
    "v_fma_f32 v6, v98, -v110, v6\n"
    "v_fma_f32 v7, v99, -v110, v7\n"
    "v_mfma_f32_16x16x32_bf16 v[20:23], a[236:239], v[60:63], v[20:23]\n"
    "v_mul_f32 v0, v0, v112\n"
    "v_mul_f32 v1, v1, v112\n"
    "v_mul_f32 v2, v2, v112\n"
    "v_mul_f32 v3, v3, v112\n"
    "v_mul_f32 v4, v4, v114\n"
    "v_mul_f32 v5, v5, v114\n"
    "v_mul_f32 v6, v6, v114\n"
    "v_mul_f32 v7, v7, v114\n"
    "v_cvt_pk_bf16_f32 v0, v0, v1\n"
    "v_cvt_pk_bf16_f32 v1, v2, v3\n"
    "ds_write_b64 v231, v[0:1] offset:0\n"
    "v_cvt_pk_bf16_f32 v4, v4, v5\n"
    "v_cvt_pk_bf16_f32 v5, v6, v7\n"
    "ds_write_b64 v231, v[4:5] offset:1024\n"
    "v_pk_fma_f32 v[8:9], v[100:101], v[108:109], v[8:9] op_sel_hi:[1,0,1] neg_lo:[0,1,0] neg_hi:[0,1,0]\n"
    "v_pk_fma_f32 v[10:11], v[102:103], v[108:109], v[10:11] op_sel_hi:[1,0,1] neg_lo:[0,1,0] neg_hi:[0,1,0]\n"
    "v_pk_fma_f32 v[12:13], v[100:101], v[110:111], v[12:13] op_sel_hi:[1,0,1] neg_lo:[0,1,0] neg_hi:[0,1,0]\n"
    "v_pk_fma_f32 v[14:15], v[102:103], v[110:111], v[14:15] op_sel_hi:[1,0,1] neg_lo:[0,1,0] neg_hi:[0,1,0]\n"
    "v_pk_mul_f32 v[8:9], v[8:9], v[112:113] op_sel_hi:[1,0]\n"
    "v_pk_mul_f32 v[10:11], v[10:11], v[112:113] op_sel_hi:[1,0]\n"
    "v_pk_mul_f32 v[12:13], v[12:13], v[114:115] op_sel_hi:[1,0]\n"
    "v_pk_mul_f32 v[14:15], v[14:15], v[114:115] op_sel_hi:[1,0]\n"
    "v_cvt_pk_bf16_f32 v8, v8, v9\n"
    "v_cvt_pk_bf16_f32 v9, v10, v11\n"
    "ds_write_b64 v232, v[8:9] offset:0\n"
    "v_cvt_pk_bf16_f32 v12, v12, v13\n"
    "v_cvt_pk_bf16_f32 v13, v14, v15\n"
    "ds_write_b64 v232, v[12:13] offset:1024\n"
    "v_pk_fma_f32 v[16:17], v[104:105], v[108:109], v[16:17] op_sel_hi:[1,0,1] neg_lo:[0,1,0] neg_hi:[0,1,0]\n"
    "v_pk_fma_f32 v[18:19], v[106:107], v[108:109], v[18:19] op_sel_hi:[1,0,1] neg_lo:[0,1,0] neg_hi:[0,1,0]\n"
    "v_pk_fma_f32 v[20:21], v[104:105], v[110:111], v[20:21] op_sel_hi:[1,0,1] neg_lo:[0,1,0] neg_hi:[0,1,0]\n"
    "v_pk_fma_f32 v[22:23], v[106:107], v[110:111], v[22:23] op_sel_hi:[1,0,1] neg_lo:[0,1,0] neg_hi:[0,1,0]\n"
    "v_pk_mul_f32 v[16:17], v[16:17], v[112:113] op_sel_hi:[1,0]\n"
    "v_pk_mul_f32 v[18:19], v[18:19], v[112:113] op_sel_hi:[1,0]\n"
    "v_pk_mul_f32 v[20:21], v[20:21], v[114:115] op_sel_hi:[1,0]\n"
    "v_pk_mul_f32 v[22:23], v[22:23], v[114:115] op_sel_hi:[1,0]\n"
    "v_cvt_pk_bf16_f32 v16, v16, v17\n"
    "v_cvt_pk_bf16_f32 v17, v18, v19\n"
    "ds_write_b64 v233, v[16:17] offset:0\n"
    "v_cvt_pk_bf16_f32 v20, v20, v21\n"
    "v_cvt_pk_bf16_f32 v21, v22, v23\n"
    "ds_write_b64 v233, v[20:21] offset:1024\n"
    "s_waitcnt lgkmcnt(0)\n"
    "s_barrier\n"
    "ds_read_b128 v[32:35], v236 offset:1024\n"
    "ds_read_b128 v[44:47], v234 offset:0\n"
    "ds_read_b128 v[36:39], v235 offset:0\n"
    "ds_read_b128 v[48:51], v234 offset:1024\n"
    "ds_read_b128 v[40:43], v235 offset:1024\n"
    "ds_read_b64_tr_b16 v[52:53], v238 offset:1024\n"
    "ds_read_b64_tr_b16 v[54:55], v237 offset:0\n"
    "ds_read_b64_tr_b16 v[56:57], v237 offset:1024\n"
    "ds_read_b64_tr_b16 v[58:59], v240 offset:1024\n"
    "ds_read_b64_tr_b16 v[60:61], v239 offset:0\n"
    "ds_read_b64_tr_b16 v[62:63], v239 offset:1024\n"
    "s_waitcnt lgkmcnt(9)\n"
    "v_mfma_f32_16x16x32_bf16 v[64:67], v[32:35], v[44:47], 0\n"
    "s_waitcnt lgkmcnt(8)\n"
    "v_mfma_f32_16x16x32_bf16 v[68:71], v[36:39], v[44:47], 0\n"
    "s_waitcnt lgkmcnt(7)\n"
    "v_mfma_f32_16x16x32_bf16 v[72:75], v[36:39], v[48:51], 0\n"
    "s_waitcnt lgkmcnt(6)\n"
    "v_mfma_f32_16x16x32_bf16 v[76:79], v[40:43], v[48:51], 0\n"
    "s_nop 1\n"
    "v_cndmask_b32 v64, v64, v65, s[42:43]\n"
    "v_cndmask_b32 v65, v66, v67, s[42:43]\n"
    "v_cndmask_b32 v66, v68, v69, s[42:43]\n"
    "v_cndmask_b32 v67, v70, v71, s[42:43]\n"
    "v_cndmask_b32 v68, v72, v73, s[42:43]\n"
    "v_cndmask_b32 v69, v74, v75, s[42:43]\n"
    "v_cndmask_b32 v70, v76, v77, s[42:43]\n"
    "v_cndmask_b32 v71, v78, v79, s[42:43]\n"
    "v_cndmask_b32 v64, v249, v64, s[38:39]\n"
    "v_cndmask_b32 v65, v249, v65, s[40:41]\n"
    "v_cndmask_b32 v66, v249, v66, s[12:13]\n"
    "v_cndmask_b32 v67, v249, v67, s[14:15]\n"
    "v_cndmask_b32 v68, v249, v68, s[8:9]\n"
    "v_cndmask_b32 v69, v249, v69, s[10:11]\n"
    "v_cndmask_b32 v70, v249, v70, s[12:13]\n"
    "v_cndmask_b32 v71, v249, v71, s[14:15]\n"
    "v_max3_f32 v72, v64, v65, v66\n"
    "v_max3_f32 v74, v68, v69, v70\n"
    "v_max_f32 v72, v72, v67\n"
    "v_max_f32 v74, v74, v71\n"
    "s_nop 1\n"
    "v_permlane16_swap_b32 v72, v74\n"
    "v_max_f32 v72, v72, v74\n"
    "v_mov_b32 v74, v72\n"
    "s_nop 1\n"
    "v_permlane32_swap_b32 v72, v74\n"
    "v_max_f32 v72, v72, v74\n"
    "v_mul_f32 v72, s37, v72\n"
    "v_mov_b32 v74, v72\n"
    "s_nop 1\n"
    "v_permlane16_swap_b32 v72, v74\n"
    "v_fma_f32 v64, v64, s37, -v72\n"
    "v_fma_f32 v65, v65, s37, -v72\n"
    "v_fma_f32 v66, v66, s37, -v72\n"
    "v_fma_f32 v67, v67, s37, -v72\n"
    "v_fma_f32 v68, v68, s37, -v74\n"
    "v_fma_f32 v69, v69, s37, -v74\n"
    "v_fma_f32 v70, v70, s37, -v74\n"
    "v_fma_f32 v71, v71, s37, -v74\n"
    "v_exp_f32 v64, v64\n"
    "v_exp_f32 v65, v65\n"
    "v_exp_f32 v66, v66\n"
    "v_exp_f32 v67, v67\n"
    "v_exp_f32 v68, v68\n"
    "v_exp_f32 v69, v69\n"
    "v_exp_f32 v70, v70\n"
    "v_exp_f32 v71, v71\n"
    "v_pk_add_f32 v[72:73], v[64:65], v[66:67]\n"
    "v_pk_add_f32 v[74:75], v[68:69], v[70:71]\n"
    "v_add_f32 v72, v72, v73\n"
    "v_add_f32 v74, v74, v75\n"
    "s_nop 1\n"
    "v_permlane16_swap_b32 v72, v74\n"
    "v_add_f32 v72, v72, v74\n"
    "v_mov_b32 v74, v72\n"
    "s_nop 1\n"
    "v_permlane32_swap_b32 v72, v74\n"
    "v_add_f32 v72, v72, v74\n"
    "v_rcp_f32 v72, v72\n"
    "s_nop 0\n"
    "v_mov_b32 v74, v72\n"
    "s_nop 1\n"
    "v_permlane16_swap_b32 v72, v74\n"
    "v_pk_mul_f32 v[64:65], v[64:65], v[72:73] op_sel_hi:[1,0]\n"
    "v_pk_mul_f32 v[66:67], v[66:67], v[72:73] op_sel_hi:[1,0]\n"
    "v_pk_mul_f32 v[68:69], v[68:69], v[74:75] op_sel_hi:[1,0]\n"
    "v_pk_mul_f32 v[70:71], v[70:71], v[74:75] op_sel_hi:[1,0]\n"
    "v_cvt_pk_bf16_f32 v64, v64, v65\n"
    "v_cvt_pk_bf16_f32 v66, v66, v67\n"
    "v_cvt_pk_bf16_f32 v68, v68, v69\n"
    "v_cvt_pk_bf16_f32 v70, v70, v71\n"
    "v_perm_b32 v80, v64, v64, v247\n"
    "v_perm_b32 v81, v64, v64, v248\n"
    "v_perm_b32 v82, v66, v66, v247\n"
    "v_perm_b32 v83, v66, v66, v248\n"
    "v_perm_b32 v84, v68, v68, v247\n"
    "v_perm_b32 v85, v68, v68, v248\n"
    "v_perm_b32 v86, v70, v70, v247\n"
    "v_perm_b32 v87, v70, v70, v248\n"
    "s_waitcnt lgkmcnt(0)\n"
    "v_mfma_f32_16x16x32_bf16 v[0:3], v[52:55], v[80:83], 0\n"
    "v_mfma_f32_16x16x32_bf16 v[4:7], v[54:57], v[84:87], 0\n"
    "v_mfma_f32_16x16x32_bf16 v[8:11], v[58:61], v[80:83], 0\n"
    "v_mfma_f32_16x16x32_bf16 v[12:15], v[60:63], v[84:87], 0\n"
    "s_mov_b64 s[38:39], s[8:9]\n"
    "s_mov_b64 s[40:41], s[10:11]\n"
    "s_nop 2\n"
    "v_cvt_pk_bf16_f32 v16, v0, v1\n"
    "v_cvt_pk_bf16_f32 v17, v2, v3\n"
    "v_cvt_pk_bf16_f32 v18, v4, v5\n"
    "v_cvt_pk_bf16_f32 v19, v6, v7\n"
    "s_nop 0\n"
    "v_permlane16_swap_b32 v16, v18\n"
    "v_permlane16_swap_b32 v17, v19\n"
    "buffer_store_dwordx4 v[16:19], v245, s[0:3], s33 offen sc1\n"
    "v_cvt_pk_bf16_f32 v20, v8, v9\n"
    "v_cvt_pk_bf16_f32 v21, v10, v11\n"
    "v_cvt_pk_bf16_f32 v22, v12, v13\n"
    "v_cvt_pk_bf16_f32 v23, v14, v15\n"
    "buffer_store_dwordx4 v[20:23], v246, s[0:3], s33 offen sc1\n"
    "s_waitcnt vmcnt(0)\n"
    "buffer_store_dword v116, v230, s[0:3], 0 offen sc1\n"
    "s_mov_b32 s30, s31\n"
    "s_add_u32 s31, s31, 1\n"
    "s_cmp_lt_u32 s30, 64\n"
    "s_cbranch_scc1 .Lb1_top_%=\n"
    "s_waitcnt vmcnt(0) lgkmcnt(0)\n"
    "s_endpgm\n"
    :: "{s[0:3]}"(s0_), "{s[4:7]}"(s4_), "{s[8:11]}"(s8_), "{s[12:15]}"(s12_), "{s[16:19]}"(s16_), "{s[20:23]}"(s20_), "{v[0:3]}"(v0_), "{v[224:227]}"(v224_), "{v[228:231]}"(v228_), "{v[232:235]}"(v232_), "{v[236:239]}"(v236_), "{v[240:243]}"(v240_), "{v[244:247]}"(v244_), "{v[248:251]}"(v248_) : "memory", "v255", "a255", "s95");
    __builtin_unreachable();
}
DEV void b2_role(const MegaArgs& A, const int L, const int c, const int rep) {
    ROLE_HEAD;
    const int b = 4 * c + w;
    const u32x4 ro = L ? u32x4{lo(A.out), hi(A.out), 2048u * 1280u, 0x20000u} : rx;
    if (L == 0) {
#pragma unroll
        for (int k = 0; k < 16; ++k) bst16(rr, ST0 + b * 32768 + (k >> 3) * 16384 + (k & 7) * 1024 + l * 16, ones);
        DRAIN_BARRIER();
        bst4(rr, threadIdx.x == 0 ? xl(Z_ST + c * 128, 0) : OOB, 1u);
    }
    const u32x4 s0_ = {RFL(rx[0]), RFL(rx[1]), RFL(rx[2]), RFL(rx[3])};
    const u32x4 s4_ = {RFL(lo(A.wo + 32 * b)), RFL(hi(A.wo + 32 * b)), RFL((640 * 640 - 32 * b) * 2), RFL(0x20000u)};
    const u32x4 s8_ = {RFL(ro[0]), RFL(ro[1]), RFL(ro[2]), RFL(ro[3])};
    const u32x4 s12_ = {RFL(w), RFL(L << 13), RFL(REC0 + RC_O + w * 2048), RFL(xl(Z_O + rep * 2048, L))};
    const u32x4 s16_ = {RFL(xl(Z_ST, 0)), RFL(ST0), RFL(DBG0 + ((L * 128 + 107 + c) * 4 + w) * 2048), RFL(0u)};
    const u32x4 v0_ = {(unsigned)((0 + (l >> 2)) * 1280 + (l & 3) * 16 - 0), (unsigned)((16 + (l >> 2)) * 1280 + (l & 3) * 16 - 1024), (unsigned)((32 + (l >> 2)) * 1280 + (l & 3) * 16 - 2048), (unsigned)((48 + (l >> 2)) * 1280 + (l & 3) * 16 - 3072)};
    const u32x4 v4_ = {(unsigned)(w * 40960 + (8 * (lq & 1) + 20 * (lq >> 1) + (l16 >> 2)) * 64 + (l & 3) * 16), (unsigned)(w * 40960 + (20 * lq + 16 + (l16 >> 2)) * 64 + (l & 3) * 16), (unsigned)((l < 5 ? l : 0) * 128), (unsigned)(0u)};
    const u32x4 v224_ = {(unsigned)(l * 16), (unsigned)((l < 11 ? l : 0) * 128), (unsigned)(l < 4 ? xl(Z_XN + l * 2048 + c * 128 + w * 4, 1) : OOB), (unsigned)(c * 16384 + l * 16)};
    const u32x4 v228_ = {(unsigned)(L ? (l16 + 0) * 1280 + b * 64 + lq * 16 : RC_X2 + c * 16384 + 0 + l * 16), (unsigned)(L ? (l16 + 16) * 1280 + b * 64 + lq * 16 : RC_X2 + c * 16384 + 1024 + l * 16), (unsigned)(b * 32768 + l16 * 16 + lq * 4), (unsigned)((0 + lq) * 32768 + l16 * 16)};
    const u32x4 v232_ = {(unsigned)((4 + lq) * 32768 + l16 * 16), (unsigned)((8 + lq) * 32768 + l16 * 16), (unsigned)((12 + lq) * 32768 + l16 * 16), (unsigned)((16 + lq) * 32768 + l16 * 16)};
    asm volatile(
    "s_mov_b32 s20, 0\n"
    "s_mul_i32 s28, s12, 40960\n"
    "s_mov_b32 s34, 0\n"
    "s_add_u32 m0, s28, 0\n"
    "s_nop 0\n"
    "buffer_load_dwordx4 v0, s[4:7], s34 offen offset:0 lds\n"
    "buffer_load_dwordx4 v1, s[4:7], s34 offen offset:1024 lds\n"
    "buffer_load_dwordx4 v2, s[4:7], s34 offen offset:2048 lds\n"
    "buffer_load_dwordx4 v3, s[4:7], s34 offen offset:3072 lds\n"
    "s_mov_b32 s34, 81920\n"
    "s_add_u32 m0, s28, 4096\n"
    "s_nop 0\n"
    "buffer_load_dwordx4 v0, s[4:7], s34 offen offset:0 lds\n"
    "buffer_load_dwordx4 v1, s[4:7], s34 offen offset:1024 lds\n"
    "buffer_load_dwordx4 v2, s[4:7], s34 offen offset:2048 lds\n"
    "buffer_load_dwordx4 v3, s[4:7], s34 offen offset:3072 lds\n"
    "s_mov_b32 s34, 163840\n"
    "s_add_u32 m0, s28, 8192\n"
    "s_nop 0\n"
    "buffer_load_dwordx4 v0, s[4:7], s34 offen offset:0 lds\n"
    "buffer_load_dwordx4 v1, s[4:7], s34 offen offset:1024 lds\n"
    "buffer_load_dwordx4 v2, s[4:7], s34 offen offset:2048 lds\n"
    "buffer_load_dwordx4 v3, s[4:7], s34 offen offset:3072 lds\n"
    "s_mov_b32 s34, 245760\n"
    "s_add_u32 m0, s28, 12288\n"
    "s_nop 0\n"
    "buffer_load_dwordx4 v0, s[4:7], s34 offen offset:0 lds\n"
    "buffer_load_dwordx4 v1, s[4:7], s34 offen offset:1024 lds\n"
    "buffer_load_dwordx4 v2, s[4:7], s34 offen offset:2048 lds\n"
    "buffer_load_dwordx4 v3, s[4:7], s34 offen offset:3072 lds\n"
    "s_mov_b32 s34, 327680\n"
    "s_add_u32 m0, s28, 16384\n"
    "s_nop 0\n"
    "buffer_load_dwordx4 v0, s[4:7], s34 offen offset:0 lds\n"
    "buffer_load_dwordx4 v1, s[4:7], s34 offen offset:1024 lds\n"
    "buffer_load_dwordx4 v2, s[4:7], s34 offen offset:2048 lds\n"
    "buffer_load_dwordx4 v3, s[4:7], s34 offen offset:3072 lds\n"
    "s_mov_b32 s34, 409600\n"
    "s_add_u32 m0, s28, 20480\n"
    "s_nop 0\n"
    "buffer_load_dwordx4 v0, s[4:7], s34 offen offset:0 lds\n"
    "buffer_load_dwordx4 v1, s[4:7], s34 offen offset:1024 lds\n"
    "buffer_load_dwordx4 v2, s[4:7], s34 offen offset:2048 lds\n"
    "buffer_load_dwordx4 v3, s[4:7], s34 offen offset:3072 lds\n"
    "s_mov_b32 s34, 491520\n"
    "s_add_u32 m0, s28, 24576\n"
    "s_nop 0\n"
    "buffer_load_dwordx4 v0, s[4:7], s34 offen offset:0 lds\n"
    "buffer_load_dwordx4 v1, s[4:7], s34 offen offset:1024 lds\n"
    "buffer_load_dwordx4 v2, s[4:7], s34 offen offset:2048 lds\n"
    "buffer_load_dwordx4 v3, s[4:7], s34 offen offset:3072 lds\n"
    "s_mov_b32 s34, 573440\n"
    "s_add_u32 m0, s28, 28672\n"
    "s_nop 0\n"
    "buffer_load_dwordx4 v0, s[4:7], s34 offen offset:0 lds\n"
    "buffer_load_dwordx4 v1, s[4:7], s34 offen offset:1024 lds\n"
    "buffer_load_dwordx4 v2, s[4:7], s34 offen offset:2048 lds\n"
    "buffer_load_dwordx4 v3, s[4:7], s34 offen offset:3072 lds\n"
    "s_mov_b32 s34, 655360\n"
    "s_add_u32 m0, s28, 32768\n"
    "s_nop 0\n"
    "buffer_load_dwordx4 v0, s[4:7], s34 offen offset:0 lds\n"
    "buffer_load_dwordx4 v1, s[4:7], s34 offen offset:1024 lds\n"
    "buffer_load_dwordx4 v2, s[4:7], s34 offen offset:2048 lds\n"
    "buffer_load_dwordx4 v3, s[4:7], s34 offen offset:3072 lds\n"
    "s_mov_b32 s34, 737280\n"
    "s_add_u32 m0, s28, 36864\n"
    "s_nop 0\n"
    "buffer_load_dwordx4 v0, s[4:7], s34 offen offset:0 lds\n"
    "buffer_load_dwordx4 v1, s[4:7], s34 offen offset:1024 lds\n"
    "buffer_load_dwordx4 v2, s[4:7], s34 offen offset:2048 lds\n"
    "buffer_load_dwordx4 v3, s[4:7], s34 offen offset:3072 lds\n"
    "s_waitcnt vmcnt(0)\n"
    "ds_read_b64_tr_b16 a[0:1], v4 offset:0\n"
    "ds_read_b64_tr_b16 a[2:3], v4 offset:256\n"
    "ds_read_b64_tr_b16 a[80:81], v4 offset:8\n"
    "ds_read_b64_tr_b16 a[82:83], v4 offset:264\n"
    "s_waitcnt lgkmcnt(0)\n"
    "ds_read_b64_tr_b16 a[4:5], v4 offset:2560\n"
    "ds_read_b64_tr_b16 a[6:7], v4 offset:2816\n"
    "ds_read_b64_tr_b16 a[84:85], v4 offset:2568\n"
    "ds_read_b64_tr_b16 a[86:87], v4 offset:2824\n"
    "s_waitcnt lgkmcnt(0)\n"
    "ds_read_b64_tr_b16 a[8:9], v4 offset:5120\n"
    "ds_read_b64_tr_b16 a[10:11], v4 offset:5376\n"
    "ds_read_b64_tr_b16 a[88:89], v4 offset:5128\n"
    "ds_read_b64_tr_b16 a[90:91], v4 offset:5384\n"
    "s_waitcnt lgkmcnt(0)\n"
    "ds_read_b64_tr_b16 a[12:13], v4 offset:7680\n"
    "ds_read_b64_tr_b16 a[14:15], v4 offset:7936\n"
    "ds_read_b64_tr_b16 a[92:93], v4 offset:7688\n"
    "ds_read_b64_tr_b16 a[94:95], v4 offset:7944\n"
    "s_waitcnt lgkmcnt(0)\n"
    "ds_read_b64_tr_b16 a[16:17], v4 offset:10240\n"
    "ds_read_b64_tr_b16 a[18:19], v4 offset:10496\n"
    "ds_read_b64_tr_b16 a[96:97], v4 offset:10248\n"
    "ds_read_b64_tr_b16 a[98:99], v4 offset:10504\n"
    "s_waitcnt lgkmcnt(0)\n"
    "ds_read_b64_tr_b16 a[20:21], v4 offset:12800\n"
    "ds_read_b64_tr_b16 a[22:23], v4 offset:13056\n"
    "ds_read_b64_tr_b16 a[100:101], v4 offset:12808\n"
    "ds_read_b64_tr_b16 a[102:103], v4 offset:13064\n"
    "s_waitcnt lgkmcnt(0)\n"
    "ds_read_b64_tr_b16 a[24:25], v4 offset:15360\n"
    "ds_read_b64_tr_b16 a[26:27], v4 offset:15616\n"
    "ds_read_b64_tr_b16 a[104:105], v4 offset:15368\n"
    "ds_read_b64_tr_b16 a[106:107], v4 offset:15624\n"
    "s_waitcnt lgkmcnt(0)\n"
    "ds_read_b64_tr_b16 a[28:29], v4 offset:17920\n"
    "ds_read_b64_tr_b16 a[30:31], v4 offset:18176\n"
    "ds_read_b64_tr_b16 a[108:109], v4 offset:17928\n"
    "ds_read_b64_tr_b16 a[110:111], v4 offset:18184\n"
    "s_waitcnt lgkmcnt(0)\n"
    "ds_read_b64_tr_b16 a[32:33], v4 offset:20480\n"
    "ds_read_b64_tr_b16 a[34:35], v4 offset:20736\n"
    "ds_read_b64_tr_b16 a[112:113], v4 offset:20488\n"
    "ds_read_b64_tr_b16 a[114:115], v4 offset:20744\n"
    "s_waitcnt lgkmcnt(0)\n"
    "ds_read_b64_tr_b16 a[36:37], v4 offset:23040\n"
    "ds_read_b64_tr_b16 a[38:39], v4 offset:23296\n"
    "ds_read_b64_tr_b16 a[116:117], v4 offset:23048\n"
    "ds_read_b64_tr_b16 a[118:119], v4 offset:23304\n"
    "s_waitcnt lgkmcnt(0)\n"
    "ds_read_b64_tr_b16 a[40:41], v4 offset:25600\n"
    "ds_read_b64_tr_b16 a[42:43], v4 offset:25856\n"
    "ds_read_b64_tr_b16 a[120:121], v4 offset:25608\n"
    "ds_read_b64_tr_b16 a[122:123], v4 offset:25864\n"
    "s_waitcnt lgkmcnt(0)\n"
    "ds_read_b64_tr_b16 a[44:45], v4 offset:28160\n"
    "ds_read_b64_tr_b16 a[46:47], v4 offset:28416\n"
    "ds_read_b64_tr_b16 a[124:125], v4 offset:28168\n"
    "ds_read_b64_tr_b16 a[126:127], v4 offset:28424\n"
    "s_waitcnt lgkmcnt(0)\n"
    "ds_read_b64_tr_b16 a[48:49], v4 offset:30720\n"
    "ds_read_b64_tr_b16 a[50:51], v4 offset:30976\n"
    "ds_read_b64_tr_b16 a[128:129], v4 offset:30728\n"
    "ds_read_b64_tr_b16 a[130:131], v4 offset:30984\n"
    "s_waitcnt lgkmcnt(0)\n"
    "ds_read_b64_tr_b16 a[52:53], v4 offset:33280\n"
    "ds_read_b64_tr_b16 a[54:55], v4 offset:33536\n"
    "ds_read_b64_tr_b16 a[132:133], v4 offset:33288\n"
    "ds_read_b64_tr_b16 a[134:135], v4 offset:33544\n"
    "s_waitcnt lgkmcnt(0)\n"
    "ds_read_b64_tr_b16 a[56:57], v4 offset:35840\n"
    "ds_read_b64_tr_b16 a[58:59], v4 offset:36096\n"
    "ds_read_b64_tr_b16 a[136:137], v4 offset:35848\n"
    "ds_read_b64_tr_b16 a[138:139], v4 offset:36104\n"
    "s_waitcnt lgkmcnt(0)\n"
    "ds_read_b64_tr_b16 a[60:61], v4 offset:38400\n"
    "ds_read_b64_tr_b16 a[62:63], v4 offset:38656\n"
    "ds_read_b64_tr_b16 a[140:141], v4 offset:38408\n"
    "ds_read_b64_tr_b16 a[142:143], v4 offset:38664\n"
    "s_waitcnt lgkmcnt(0)\n"
    "ds_read_b64_tr_b16 a[64:65], v5 offset:0\n"
    "ds_read_b64_tr_b16 a[66:67], v5 offset:5120\n"
    "ds_read_b64_tr_b16 a[144:145], v5 offset:8\n"
    "ds_read_b64_tr_b16 a[146:147], v5 offset:5128\n"
    "s_waitcnt lgkmcnt(0)\n"
    "ds_read_b64_tr_b16 a[68:69], v5 offset:10240\n"
    "ds_read_b64_tr_b16 a[70:71], v5 offset:15360\n"
    "ds_read_b64_tr_b16 a[148:149], v5 offset:10248\n"
    "ds_read_b64_tr_b16 a[150:151], v5 offset:15368\n"
    "s_waitcnt lgkmcnt(0)\n"
    "ds_read_b64_tr_b16 a[72:73], v5 offset:20480\n"
    "ds_read_b64_tr_b16 a[74:75], v5 offset:25600\n"
    "ds_read_b64_tr_b16 a[152:153], v5 offset:20488\n"
    "ds_read_b64_tr_b16 a[154:155], v5 offset:25608\n"
    "s_waitcnt lgkmcnt(0)\n"
    "ds_read_b64_tr_b16 a[76:77], v5 offset:30720\n"
    "ds_read_b64_tr_b16 a[78:79], v5 offset:35840\n"
    "ds_read_b64_tr_b16 a[156:157], v5 offset:30728\n"
    "ds_read_b64_tr_b16 a[158:159], v5 offset:35848\n"
    "s_waitcnt lgkmcnt(0)\n"
    "s_mov_b32 s26, -2\n"
    ".Lb2_po1_%=:\n"
    "buffer_load_dword v8, v6, s[0:3], s16 offen sc1\n"
    "s_waitcnt vmcnt(0)\n"
    "v_cmp_gt_i32 vcc, 1, v8\n"
    "s_nop 0\n"
    "s_cbranch_vccz .Lb2_pd2_%=\n"
    "s_add_u32 s20, s20, 1\n"
    "s_cmp_lt_u32 s20, 0x40000\n"
    "s_cbranch_scc1 .Lb2_sp3_%=\n"
    "v_mov_b32 v8, 70\n"
    "s_mov_b32 s24, 33728\n"
    "buffer_store_dword v8, off, s[0:3], s24 sc1\n"
    "s_waitcnt vmcnt(0)\n"
    "s_branch .Lb2_pd2_%=\n"
    ".Lb2_sp3_%=:\n"
    "s_sleep 4\n"
    "s_branch .Lb2_po1_%=\n"
    ".Lb2_pd2_%=:\n"
    "s_mov_b32 s26, 0\n"
    "s_lshl_b32 s33, s12, 11\n"
    "s_mov_b32 s35, 0\n"
    ".Lb2_top_%=:\n"
    "s_lshl_b32 s28, s26, 19\n"
    "s_lshl_b32 s25, s26, 11\n"
    "s_xor_b32 s25, s25, s13\n"
    "s_and_b32 s25, s25, 0x2000\n"
    "s_add_u32 s28, s28, s25\n"
    "s_add_u32 s28, s28, s14\n"
    "s_add_u32 s27, s26, 1\n"
    "s_sub_u32 s29, s28, 81920\n"
    "s_sub_u32 s31, s28, 163840\n"
    "s_xor_b32 s31, s31, 0x2000\n"
    "s_mul_i32 s30, s26, 40960\n"
    "s_cmp_eq_u32 s13, 0\n"
    "s_cselect_b32 s30, s31, s30\n"
    "s_and_b32 s32, s26, 32\n"
    "s_add_u32 s32, s32, s26\n"
    "s_lshl_b32 s32, s32, 8\n"
    "s_add_u32 s32, s32, s17\n"
    "s_cmp_lg_u32 s12, 0\n"
    "s_cbranch_scc1 .Lb2_pd5_%=\n"
    ".Lb2_rt6_%=:\n"
    "s_memrealtime s[22:23]\n"
    "s_waitcnt lgkmcnt(0)\n"
    "s_sub_u32 s24, s35, s22\n"
    "s_cmp_lt_u32 s24, 600\n"
    "s_cbranch_scc1 .Lb2_rt6_%=\n"
    ".Lb2_po4_%=:\n"
    "buffer_load_dwordx4 v[84:87], v225, s[0:3], s15 offen sc1\n"
    "s_memrealtime s[22:23]\n"
    "s_waitcnt vmcnt(0) lgkmcnt(0)\n"
    "s_add_u32 s35, s22, 274\n"
    "v_min3_i32 v84, v84, v85, v86\n"
    "v_min_i32 v84, v84, v87\n"
    "v_cmp_gt_i32 vcc, s27, v84\n"
    "s_nop 0\n"
    "s_cbranch_vccz .Lb2_pd5_%=\n"
    "s_add_u32 s20, s20, 1\n"
    "s_cmp_lt_u32 s20, 0x40000\n"
    "s_cbranch_scc1 .Lb2_sp7_%=\n"
    "v_mov_b32 v84, 71\n"
    "s_mov_b32 s24, 33728\n"
    "buffer_store_dword v84, off, s[0:3], s24 sc1\n"
    "s_waitcnt vmcnt(0)\n"
    "s_branch .Lb2_pd5_%=\n"
    ".Lb2_sp7_%=:\n"
    "s_sleep 2\n"
    "s_branch .Lb2_po4_%=\n"
    ".Lb2_pd5_%=:\n"
    "s_barrier\n"
    "s_add_u32 m0, s33, 0\n"
    "s_nop 0\n"
    "buffer_load_dwordx4 v224, s[0:3], s28 offen sc1 lds\n"
    "buffer_load_dwordx4 v224, s[0:3], s28 offen offset:1024 sc1 lds\n"
    "s_add_u32 m0, s33, 8192\n"
    "s_add_u32 s28, s28, 0x4000\n"
    "buffer_load_dwordx4 v224, s[0:3], s28 offen sc1 lds\n"
    "buffer_load_dwordx4 v224, s[0:3], s28 offen offset:1024 sc1 lds\n"
    "s_add_u32 m0, s33, 16384\n"
    "s_add_u32 s28, s28, 0x4000\n"
    "buffer_load_dwordx4 v224, s[0:3], s28 offen sc1 lds\n"
    "buffer_load_dwordx4 v224, s[0:3], s28 offen offset:1024 sc1 lds\n"
    "s_add_u32 m0, s33, 24576\n"
    "s_add_u32 s28, s28, 0x4000\n"
    "buffer_load_dwordx4 v224, s[0:3], s28 offen sc1 lds\n"
    "buffer_load_dwordx4 v224, s[0:3], s28 offen offset:1024 sc1 lds\n"
    "s_add_u32 m0, s33, 32768\n"
    "s_add_u32 s28, s28, 0x4000\n"
    "buffer_load_dwordx4 v224, s[0:3], s28 offen sc1 lds\n"
    "buffer_load_dwordx4 v224, s[0:3], s28 offen offset:1024 sc1 lds\n"
    "buffer_load_dwordx4 v[48:51], v227, s[0:3], s29 offen offset:0 sc1\n"
    "buffer_load_dwordx4 v[52:55], v227, s[0:3], s29 offen offset:1024 sc1\n"
    "s_waitcnt vmcnt(10)\n"
    "s_barrier\n"
    "ds_read_b128 v[16:19], v224 offset:0\n"
    "ds_read_b128 v[20:23], v224 offset:1024\n"
    "ds_read_b128 v[24:27], v224 offset:2048\n"
    "ds_read_b128 v[28:31], v224 offset:3072\n"
    "ds_read_b128 v[32:35], v224 offset:4096\n"
    "ds_read_b128 v[36:39], v224 offset:5120\n"
    "s_waitcnt lgkmcnt(4)\n"
    "v_mfma_f32_16x16x32_bf16 v[0:3], a[0:3], v[16:19], 0\n"
    "ds_read_b128 v[40:43], v224 offset:6144\n"
    "v_mfma_f32_16x16x32_bf16 v[8:11], a[0:3], v[20:23], 0\n"
    "ds_read_b128 v[44:47], v224 offset:7168\n"
    "v_mfma_f32_16x16x32_bf16 v[4:7], a[80:83], v[16:19], 0\n"
    "v_mfma_f32_16x16x32_bf16 v[12:15], a[80:83], v[20:23], 0\n"
    "s_waitcnt lgkmcnt(4)\n"
    "v_mfma_f32_16x16x32_bf16 v[0:3], a[4:7], v[24:27], v[0:3]\n"
    "s_waitcnt vmcnt(8)\n"
    "s_barrier\n"
    "v_mfma_f32_16x16x32_bf16 v[8:11], a[4:7], v[28:31], v[8:11]\n"
    "ds_read_b128 v[16:19], v224 offset:8192\n"
    "v_mfma_f32_16x16x32_bf16 v[4:7], a[84:87], v[24:27], v[4:7]\n"
    "ds_read_b128 v[20:23], v224 offset:9216\n"
    "v_mfma_f32_16x16x32_bf16 v[12:15], a[84:87], v[28:31], v[12:15]\n"
    "s_waitcnt lgkmcnt(4)\n"
    "v_mfma_f32_16x16x32_bf16 v[0:3], a[8:11], v[32:35], v[0:3]\n"
    "ds_read_b128 v[24:27], v224 offset:10240\n"
    "v_mfma_f32_16x16x32_bf16 v[8:11], a[8:11], v[36:39], v[8:11]\n"
    "ds_read_b128 v[28:31], v224 offset:11264\n"
    "v_mfma_f32_16x16x32_bf16 v[4:7], a[88:91], v[32:35], v[4:7]\n"
    "v_mfma_f32_16x16x32_bf16 v[12:15], a[88:91], v[36:39], v[12:15]\n"
    "s_waitcnt lgkmcnt(4)\n"
    "v_mfma_f32_16x16x32_bf16 v[0:3], a[12:15], v[40:43], v[0:3]\n"
    "ds_read_b128 v[32:35], v224 offset:12288\n"
    "v_mfma_f32_16x16x32_bf16 v[8:11], a[12:15], v[44:47], v[8:11]\n"
    "ds_read_b128 v[36:39], v224 offset:13312\n"
    "v_mfma_f32_16x16x32_bf16 v[4:7], a[92:95], v[40:43], v[4:7]\n"
    "v_mfma_f32_16x16x32_bf16 v[12:15], a[92:95], v[44:47], v[12:15]\n"
    "s_waitcnt lgkmcnt(4)\n"
    "v_mfma_f32_16x16x32_bf16 v[0:3], a[16:19], v[16:19], v[0:3]\n"
    "ds_read_b128 v[40:43], v224 offset:14336\n"
    "v_mfma_f32_16x16x32_bf16 v[8:11], a[16:19], v[20:23], v[8:11]\n"
    "ds_read_b128 v[44:47], v224 offset:15360\n"
    "v_mfma_f32_16x16x32_bf16 v[4:7], a[96:99], v[16:19], v[4:7]\n"
    "v_mfma_f32_16x16x32_bf16 v[12:15], a[96:99], v[20:23], v[12:15]\n"
    "s_waitcnt lgkmcnt(4)\n"
    "v_mfma_f32_16x16x32_bf16 v[0:3], a[20:23], v[24:27], v[0:3]\n"
    "s_waitcnt vmcnt(6)\n"
    "s_barrier\n"
    "v_mfma_f32_16x16x32_bf16 v[8:11], a[20:23], v[28:31], v[8:11]\n"
    "ds_read_b128 v[16:19], v224 offset:16384\n"
    "v_mfma_f32_16x16x32_bf16 v[4:7], a[100:103], v[24:27], v[4:7]\n"
    "ds_read_b128 v[20:23], v224 offset:17408\n"
    "v_mfma_f32_16x16x32_bf16 v[12:15], a[100:103], v[28:31], v[12:15]\n"
    "s_waitcnt lgkmcnt(4)\n"
    "v_mfma_f32_16x16x32_bf16 v[0:3], a[24:27], v[32:35], v[0:3]\n"
    "ds_read_b128 v[24:27], v224 offset:18432\n"
    "v_mfma_f32_16x16x32_bf16 v[8:11], a[24:27], v[36:39], v[8:11]\n"
    "ds_read_b128 v[28:31], v224 offset:19456\n"
    "v_mfma_f32_16x16x32_bf16 v[4:7], a[104:107], v[32:35], v[4:7]\n"
    "v_mfma_f32_16x16x32_bf16 v[12:15], a[104:107], v[36:39], v[12:15]\n"
    "s_waitcnt lgkmcnt(4)\n"
    "v_mfma_f32_16x16x32_bf16 v[0:3], a[28:31], v[40:43], v[0:3]\n"
    "ds_read_b128 v[32:35], v224 offset:20480\n"
    "v_mfma_f32_16x16x32_bf16 v[8:11], a[28:31], v[44:47], v[8:11]\n"
    "ds_read_b128 v[36:39], v224 offset:21504\n"
    "v_mfma_f32_16x16x32_bf16 v[4:7], a[108:111], v[40:43], v[4:7]\n"
    "v_mfma_f32_16x16x32_bf16 v[12:15], a[108:111], v[44:47], v[12:15]\n"
    "s_waitcnt lgkmcnt(4)\n"
    "v_mfma_f32_16x16x32_bf16 v[0:3], a[32:35], v[16:19], v[0:3]\n"
    "ds_read_b128 v[40:43], v224 offset:22528\n"
    "v_mfma_f32_16x16x32_bf16 v[8:11], a[32:35], v[20:23], v[8:11]\n"
    "ds_read_b128 v[44:47], v224 offset:23552\n"
    "v_mfma_f32_16x16x32_bf16 v[4:7], a[112:115], v[16:19], v[4:7]\n"
    "v_mfma_f32_16x16x32_bf16 v[12:15], a[112:115], v[20:23], v[12:15]\n"
    "s_waitcnt lgkmcnt(4)\n"
    "v_mfma_f32_16x16x32_bf16 v[0:3], a[36:39], v[24:27], v[0:3]\n"
    "s_waitcnt vmcnt(4)\n"
    "s_barrier\n"
    "v_mfma_f32_16x16x32_bf16 v[8:11], a[36:39], v[28:31], v[8:11]\n"
    "ds_read_b128 v[16:19], v224 offset:24576\n"
    "v_mfma_f32_16x16x32_bf16 v[4:7], a[116:119], v[24:27], v[4:7]\n"
    "ds_read_b128 v[20:23], v224 offset:25600\n"
    "v_mfma_f32_16x16x32_bf16 v[12:15], a[116:119], v[28:31], v[12:15]\n"
    "s_waitcnt lgkmcnt(4)\n"
    "v_mfma_f32_16x16x32_bf16 v[0:3], a[40:43], v[32:35], v[0:3]\n"
    "ds_read_b128 v[24:27], v224 offset:26624\n"
    "v_mfma_f32_16x16x32_bf16 v[8:11], a[40:43], v[36:39], v[8:11]\n"
    "ds_read_b128 v[28:31], v224 offset:27648\n"
    "v_mfma_f32_16x16x32_bf16 v[4:7], a[120:123], v[32:35], v[4:7]\n"
    "v_mfma_f32_16x16x32_bf16 v[12:15], a[120:123], v[36:39], v[12:15]\n"
    "s_waitcnt lgkmcnt(4)\n"
    "v_mfma_f32_16x16x32_bf16 v[0:3], a[44:47], v[40:43], v[0:3]\n"
    "ds_read_b128 v[32:35], v224 offset:28672\n"
    "v_mfma_f32_16x16x32_bf16 v[8:11], a[44:47], v[44:47], v[8:11]\n"
    "ds_read_b128 v[36:39], v224 offset:29696\n"
    "v_mfma_f32_16x16x32_bf16 v[4:7], a[124:127], v[40:43], v[4:7]\n"
    "v_mfma_f32_16x16x32_bf16 v[12:15], a[124:127], v[44:47], v[12:15]\n"
    "s_waitcnt lgkmcnt(4)\n"
    "v_mfma_f32_16x16x32_bf16 v[0:3], a[48:51], v[16:19], v[0:3]\n"
    "ds_read_b128 v[40:43], v224 offset:30720\n"
    "v_mfma_f32_16x16x32_bf16 v[8:11], a[48:51], v[20:23], v[8:11]\n"
    "ds_read_b128 v[44:47], v224 offset:31744\n"
    "v_mfma_f32_16x16x32_bf16 v[4:7], a[128:131], v[16:19], v[4:7]\n"
    "v_mfma_f32_16x16x32_bf16 v[12:15], a[128:131], v[20:23], v[12:15]\n"
    "s_waitcnt lgkmcnt(4)\n"
    "v_mfma_f32_16x16x32_bf16 v[0:3], a[52:55], v[24:27], v[0:3]\n"
    "s_waitcnt vmcnt(2)\n"
    "s_barrier\n"
    "v_mfma_f32_16x16x32_bf16 v[8:11], a[52:55], v[28:31], v[8:11]\n"
    "ds_read_b128 v[16:19], v224 offset:32768\n"
    "v_mfma_f32_16x16x32_bf16 v[4:7], a[132:135], v[24:27], v[4:7]\n"
    "ds_read_b128 v[20:23], v224 offset:33792\n"
    "v_mfma_f32_16x16x32_bf16 v[12:15], a[132:135], v[28:31], v[12:15]\n"
    "s_waitcnt lgkmcnt(4)\n"
    "v_mfma_f32_16x16x32_bf16 v[0:3], a[56:59], v[32:35], v[0:3]\n"
    "ds_read_b128 v[24:27], v224 offset:34816\n"
    "v_mfma_f32_16x16x32_bf16 v[8:11], a[56:59], v[36:39], v[8:11]\n"
    "ds_read_b128 v[28:31], v224 offset:35840\n"
    "v_mfma_f32_16x16x32_bf16 v[4:7], a[136:139], v[32:35], v[4:7]\n"
    "v_mfma_f32_16x16x32_bf16 v[12:15], a[136:139], v[36:39], v[12:15]\n"
    "s_waitcnt lgkmcnt(4)\n"
    "v_mfma_f32_16x16x32_bf16 v[0:3], a[60:63], v[40:43], v[0:3]\n"
    "s_waitcnt lgkmcnt(2)\n"
    "ds_read_b128 v[32:35], v224 offset:36864\n"
    "v_mfma_f32_16x16x32_bf16 v[8:11], a[60:63], v[44:47], v[8:11]\n"
    "v_swap_b32 v18, v20\n"
    "v_mfma_f32_16x16x32_bf16 v[4:7], a[140:143], v[40:43], v[4:7]\n"
    "v_swap_b32 v19, v21\n"
    "v_mfma_f32_16x16x32_bf16 v[12:15], a[140:143], v[44:47], v[12:15]\n"
    "ds_read_b128 v[36:39], v224 offset:37888\n"
    "v_mfma_f32_16x16x32_bf16 v[0:3], a[64:67], v[16:19], v[0:3]\n"
    "s_waitcnt lgkmcnt(2)\n"
    "ds_read_b128 v[40:43], v224 offset:38912\n"
    "v_mfma_f32_16x16x32_bf16 v[8:11], a[64:67], v[20:23], v[8:11]\n"
    "v_swap_b32 v26, v28\n"
    "v_mfma_f32_16x16x32_bf16 v[4:7], a[144:147], v[16:19], v[4:7]\n"
    "v_swap_b32 v27, v29\n"
    "v_mfma_f32_16x16x32_bf16 v[12:15], a[144:147], v[20:23], v[12:15]\n"
    "ds_read_b128 v[44:47], v224 offset:39936\n"
    "v_mfma_f32_16x16x32_bf16 v[0:3], a[68:71], v[24:27], v[0:3]\n"
    "s_waitcnt lgkmcnt(2)\n"
    "v_mfma_f32_16x16x32_bf16 v[8:11], a[68:71], v[28:31], v[8:11]\n"
    "v_swap_b32 v34, v36\n"
    "v_mfma_f32_16x16x32_bf16 v[4:7], a[148:151], v[24:27], v[4:7]\n"
    "v_swap_b32 v35, v37\n"
    "v_mfma_f32_16x16x32_bf16 v[12:15], a[148:151], v[28:31], v[12:15]\n"
    "s_nop 0\n"
    "v_mfma_f32_16x16x32_bf16 v[0:3], a[72:75], v[32:35], v[0:3]\n"
    "s_waitcnt lgkmcnt(0)\n"
    "v_mfma_f32_16x16x32_bf16 v[8:11], a[72:75], v[36:39], v[8:11]\n"
    "v_swap_b32 v42, v44\n"
    "v_mfma_f32_16x16x32_bf16 v[4:7], a[152:155], v[32:35], v[4:7]\n"
    "v_swap_b32 v43, v45\n"
    "v_mfma_f32_16x16x32_bf16 v[12:15], a[152:155], v[36:39], v[12:15]\n"
    "s_nop 0\n"
    "v_mfma_f32_16x16x32_bf16 v[0:3], a[76:79], v[40:43], v[0:3]\n"
    "v_mfma_f32_16x16x32_bf16 v[8:11], a[76:79], v[44:47], v[8:11]\n"
    "v_mfma_f32_16x16x32_bf16 v[4:7], a[156:159], v[40:43], v[4:7]\n"
    "v_mfma_f32_16x16x32_bf16 v[12:15], a[156:159], v[44:47], v[12:15]\n"
    "s_waitcnt vmcnt(0)\n"
    "v_lshlrev_b32 v16, 16, v48\n"
    "v_and_b32 v17, 0xffff0000, v48\n"
    "v_lshlrev_b32 v18, 16, v49\n"
    "v_and_b32 v19, 0xffff0000, v49\n"
    "v_lshlrev_b32 v20, 16, v50\n"
    "v_and_b32 v21, 0xffff0000, v50\n"
    "v_lshlrev_b32 v22, 16, v51\n"
    "v_and_b32 v23, 0xffff0000, v51\n"
    "v_lshlrev_b32 v24, 16, v52\n"
    "v_and_b32 v25, 0xffff0000, v52\n"
    "v_lshlrev_b32 v26, 16, v53\n"
    "v_and_b32 v27, 0xffff0000, v53\n"
    "v_lshlrev_b32 v28, 16, v54\n"
    "v_and_b32 v29, 0xffff0000, v54\n"
    "v_lshlrev_b32 v30, 16, v55\n"
    "v_and_b32 v31, 0xffff0000, v55\n"
    "v_cvt_pk_bf16_f32 v56, v0, v1\n"
    "v_cvt_pk_bf16_f32 v57, v2, v3\n"
    "v_cvt_pk_bf16_f32 v58, v4, v5\n"
    "v_cvt_pk_bf16_f32 v59, v6, v7\n"
    "v_lshlrev_b32 v0, 16, v56\n"
    "v_and_b32 v1, 0xffff0000, v56\n"
    "v_lshlrev_b32 v2, 16, v57\n"
    "v_and_b32 v3, 0xffff0000, v57\n"
    "v_lshlrev_b32 v4, 16, v58\n"
    "v_and_b32 v5, 0xffff0000, v58\n"
    "v_lshlrev_b32 v6, 16, v59\n"
    "v_and_b32 v7, 0xffff0000, v59\n"
    "v_pk_add_f32 v[0:1], v[0:1], v[16:17]\n"
    "v_pk_add_f32 v[2:3], v[2:3], v[18:19]\n"
    "v_pk_add_f32 v[4:5], v[4:5], v[20:21]\n"
    "v_pk_add_f32 v[6:7], v[6:7], v[22:23]\n"
    "v_cvt_pk_bf16_f32 v56, v0, v1\n"
    "v_cvt_pk_bf16_f32 v57, v2, v3\n"
    "v_cvt_pk_bf16_f32 v58, v4, v5\n"
    "v_cvt_pk_bf16_f32 v59, v6, v7\n"
    "v_cvt_pk_bf16_f32 v60, v8, v9\n"
    "v_cvt_pk_bf16_f32 v61, v10, v11\n"
    "v_cvt_pk_bf16_f32 v62, v12, v13\n"
    "v_cvt_pk_bf16_f32 v63, v14, v15\n"
    "v_lshlrev_b32 v8, 16, v60\n"
    "v_and_b32 v9, 0xffff0000, v60\n"
    "v_lshlrev_b32 v10, 16, v61\n"
    "v_and_b32 v11, 0xffff0000, v61\n"
    "v_lshlrev_b32 v12, 16, v62\n"
    "v_and_b32 v13, 0xffff0000, v62\n"
    "v_lshlrev_b32 v14, 16, v63\n"
    "v_and_b32 v15, 0xffff0000, v63\n"
    "v_pk_add_f32 v[8:9], v[8:9], v[24:25]\n"
    "v_pk_add_f32 v[10:11], v[10:11], v[26:27]\n"
    "v_pk_add_f32 v[12:13], v[12:13], v[28:29]\n"
    "v_pk_add_f32 v[14:15], v[14:15], v[30:31]\n"
    "v_cvt_pk_bf16_f32 v60, v8, v9\n"
    "v_cvt_pk_bf16_f32 v61, v10, v11\n"
    "v_cvt_pk_bf16_f32 v62, v12, v13\n"
    "v_cvt_pk_bf16_f32 v63, v14, v15\n"
    "s_cmp_lg_u32 s13, 0\n"
    "s_cbranch_scc1 .Lb2_out_%=\n"
    "v_lshlrev_b32 v0, 16, v56\n"
    "v_and_b32 v1, 0xffff0000, v56\n"
    "v_lshlrev_b32 v2, 16, v57\n"
    "v_and_b32 v3, 0xffff0000, v57\n"
    "v_lshlrev_b32 v4, 16, v58\n"
    "v_and_b32 v5, 0xffff0000, v58\n"
    "v_lshlrev_b32 v6, 16, v59\n"
    "v_and_b32 v7, 0xffff0000, v59\n"
    "v_lshlrev_b32 v8, 16, v60\n"
    "v_and_b32 v9, 0xffff0000, v60\n"
    "v_lshlrev_b32 v10, 16, v61\n"
    "v_and_b32 v11, 0xffff0000, v61\n"
    "v_lshlrev_b32 v12, 16, v62\n"
    "v_and_b32 v13, 0xffff0000, v62\n"
    "v_lshlrev_b32 v14, 16, v63\n"
    "v_and_b32 v15, 0xffff0000, v63\n"
    "v_pk_add_f32 v[64:65], v[0:1], v[2:3]\n"
    "v_pk_mul_f32 v[66:67], v[0:1], v[0:1]\n"
    "v_pk_add_f32 v[16:17], v[4:5], v[6:7]\n"
    "v_pk_fma_f32 v[66:67], v[2:3], v[2:3], v[66:67]\n"
    "v_pk_add_f32 v[64:65], v[64:65], v[16:17]\n"
    "v_pk_fma_f32 v[66:67], v[4:5], v[4:5], v[66:67]\n"
    "v_add_f32 v64, v64, v65\n"
    "v_pk_fma_f32 v[66:67], v[6:7], v[6:7], v[66:67]\n"
    "v_add_f32 v66, v66, v67\n"
    "v_pk_add_f32 v[68:69], v[8:9], v[10:11]\n"
    "v_pk_mul_f32 v[70:71], v[8:9], v[8:9]\n"
    "v_pk_add_f32 v[16:17], v[12:13], v[14:15]\n"
    "v_pk_fma_f32 v[70:71], v[10:11], v[10:11], v[70:71]\n"
    "v_pk_add_f32 v[68:69], v[68:69], v[16:17]\n"
    "v_pk_fma_f32 v[70:71], v[12:13], v[12:13], v[70:71]\n"
    "v_add_f32 v68, v68, v69\n"
    "v_pk_fma_f32 v[70:71], v[14:15], v[14:15], v[70:71]\n"
    "v_add_f32 v70, v70, v71\n"
    "v_permlane16_swap_b32 v64, v66\n"
    "s_nop 0\n"
    "v_permlane16_swap_b32 v68, v70\n"
    "v_add_f32 v64, v64, v66\n"
    "v_add_f32 v68, v68, v70\n"
    "s_nop 1\n"
    "v_permlane32_swap_b32 v64, v68\n"
    "v_add_f32 v64, v64, v68\n"
    "v_min_u32 v64, -2, v64\n"
    "buffer_store_dword v64, v230, s[0:3], s32 offen sc1\n"
    "buffer_store_dwordx4 v[56:59], v228, s[8:11], s30 offen sc1\n"
    "buffer_store_dwordx4 v[60:63], v229, s[8:11], s30 offen sc1\n"
    "s_sleep 6\n"
    ".Lb2_sl_%=:\n"
    "buffer_load_dwordx4 v[64:67], v231, s[0:3], s32 offen sc1\n"
    "buffer_load_dwordx4 v[68:71], v232, s[0:3], s32 offen sc1\n"
    "buffer_load_dwordx4 v[72:75], v233, s[0:3], s32 offen sc1\n"
    "buffer_load_dwordx4 v[76:79], v234, s[0:3], s32 offen sc1\n"
    "buffer_load_dwordx4 v[80:83], v235, s[0:3], s32 offen sc1\n"
    "s_waitcnt vmcnt(4)\n"
    "v_max3_u32 v84, v64, v65, v64\n"
    "v_max3_u32 v84, v66, v67, v84\n"
    "s_waitcnt vmcnt(3)\n"
    "v_max3_u32 v84, v68, v69, v84\n"
    "v_max3_u32 v84, v70, v71, v84\n"
    "v_pk_add_f32 v[64:65], v[64:65], v[68:69]\n"
    "v_pk_add_f32 v[66:67], v[66:67], v[70:71]\n"
    "s_waitcnt vmcnt(2)\n"
    "v_max3_u32 v84, v72, v73, v84\n"
    "v_max3_u32 v84, v74, v75, v84\n"
    "v_pk_add_f32 v[64:65], v[64:65], v[72:73]\n"
    "v_pk_add_f32 v[66:67], v[66:67], v[74:75]\n"
    "s_waitcnt vmcnt(1)\n"
    "v_max3_u32 v84, v76, v77, v84\n"
    "v_max3_u32 v84, v78, v79, v84\n"
    "v_pk_add_f32 v[64:65], v[64:65], v[76:77]\n"
    "v_pk_add_f32 v[66:67], v[66:67], v[78:79]\n"
    "s_waitcnt vmcnt(0)\n"
    "v_max3_u32 v84, v80, v81, v84\n"
    "v_max3_u32 v84, v82, v83, v84\n"
    "v_pk_add_f32 v[64:65], v[64:65], v[80:81]\n"
    "v_pk_add_f32 v[66:67], v[66:67], v[82:83]\n"
    "v_cmp_eq_u32 vcc, -1, v84\n"
    "s_nop 0\n"
    "s_cbranch_vccnz .Lb2_sr_%=\n"
    ".Lb2_sg_%=:\n"
    "v_permlane16_swap_b32 v64, v65\n"
    "v_permlane16_swap_b32 v66, v67\n"
    "v_add_f32 v64, v64, v65\n"
    "v_add_f32 v66, v66, v67\n"
    "s_nop 1\n"
    "v_permlane32_swap_b32 v64, v66\n"
    "v_add_f32 v64, v64, v66\n"
    "v_mov_b32 v66, v64\n"
    "s_nop 1\n"
    "v_permlane16_swap_b32 v64, v66\n"
    "v_mul_f32 v64, 0x3acccccd, v64\n"
    "v_mul_f32 v66, 0x3acccccd, v66\n"
    "v_fma_f32 v66, -v64, v64, v66\n"
    "v_max_f32 v66, 0, v66\n"
    "v_cvt_pk_bf16_f32 v64, v64, v66\n"
    "v_and_b32 v66, 0xffff0000, v64\n"
    "v_lshlrev_b32 v64, 16, v64\n"
    "v_add_f32 v66, 0x3727c5ac, v66\n"
    "v_mov_b32 v68, v64\n"
    "v_cvt_pk_bf16_f32 v66, v66, v66\n"
    "v_lshlrev_b32 v66, 16, v66\n"
    "v_sqrt_f32 v66, v66\n"
    "v_permlane32_swap_b32 v64, v68\n"
    "v_cvt_pk_bf16_f32 v66, v66, v66\n"
    "v_lshlrev_b32 v66, 16, v66\n"
    "v_rcp_f32 v66, v66\n"
    "s_nop 0\n"
    "v_mov_b32 v70, v66\n"
    "s_nop 1\n"
    "v_permlane32_swap_b32 v66, v70\n"
    "v_pk_add_f32 v[0:1], v[0:1], v[64:65] op_sel_hi:[1,0] neg_lo:[0,1] neg_hi:[0,1]\n"
    "v_pk_add_f32 v[2:3], v[2:3], v[64:65] op_sel_hi:[1,0] neg_lo:[0,1] neg_hi:[0,1]\n"
    "v_pk_add_f32 v[4:5], v[4:5], v[64:65] op_sel_hi:[1,0] neg_lo:[0,1] neg_hi:[0,1]\n"
    "v_pk_add_f32 v[6:7], v[6:7], v[64:65] op_sel_hi:[1,0] neg_lo:[0,1] neg_hi:[0,1]\n"
    "v_cvt_pk_bf16_f32 v16, v0, v1\n"
    "v_cvt_pk_bf16_f32 v17, v2, v3\n"
    "v_cvt_pk_bf16_f32 v18, v4, v5\n"
    "v_cvt_pk_bf16_f32 v19, v6, v7\n"
    "v_lshlrev_b32 v0, 16, v16\n"
    "v_and_b32 v1, 0xffff0000, v16\n"
    "v_lshlrev_b32 v2, 16, v17\n"
    "v_and_b32 v3, 0xffff0000, v17\n"
    "v_lshlrev_b32 v4, 16, v18\n"
    "v_and_b32 v5, 0xffff0000, v18\n"
    "v_lshlrev_b32 v6, 16, v19\n"
    "v_and_b32 v7, 0xffff0000, v19\n"
    "v_pk_mul_f32 v[0:1], v[0:1], v[66:67] op_sel_hi:[1,0]\n"
    "v_pk_mul_f32 v[2:3], v[2:3], v[66:67] op_sel_hi:[1,0]\n"
    "v_pk_mul_f32 v[4:5], v[4:5], v[66:67] op_sel_hi:[1,0]\n"
    "v_pk_mul_f32 v[6:7], v[6:7], v[66:67] op_sel_hi:[1,0]\n"
    "v_cvt_pk_bf16_f32 v56, v0, v1\n"
    "v_cvt_pk_bf16_f32 v58, v2, v3\n"
    "v_cvt_pk_bf16_f32 v57, v4, v5\n"
    "v_cvt_pk_bf16_f32 v59, v6, v7\n"
    "buffer_store_dwordx4 v[56:59], v227, s[0:3], s31 offen offset:0 sc1\n"
    "v_pk_add_f32 v[8:9], v[8:9], v[68:69] op_sel_hi:[1,0] neg_lo:[0,1] neg_hi:[0,1]\n"
    "v_pk_add_f32 v[10:11], v[10:11], v[68:69] op_sel_hi:[1,0] neg_lo:[0,1] neg_hi:[0,1]\n"
    "v_pk_add_f32 v[12:13], v[12:13], v[68:69] op_sel_hi:[1,0] neg_lo:[0,1] neg_hi:[0,1]\n"
    "v_pk_add_f32 v[14:15], v[14:15], v[68:69] op_sel_hi:[1,0] neg_lo:[0,1] neg_hi:[0,1]\n"
    "v_cvt_pk_bf16_f32 v16, v8, v9\n"
    "v_cvt_pk_bf16_f32 v17, v10, v11\n"
    "v_cvt_pk_bf16_f32 v18, v12, v13\n"
    "v_cvt_pk_bf16_f32 v19, v14, v15\n"
    "v_lshlrev_b32 v8, 16, v16\n"
    "v_and_b32 v9, 0xffff0000, v16\n"
    "v_lshlrev_b32 v10, 16, v17\n"
    "v_and_b32 v11, 0xffff0000, v17\n"
    "v_lshlrev_b32 v12, 16, v18\n"
    "v_and_b32 v13, 0xffff0000, v18\n"
    "v_lshlrev_b32 v14, 16, v19\n"
    "v_and_b32 v15, 0xffff0000, v19\n"
    "v_pk_mul_f32 v[8:9], v[8:9], v[70:71] op_sel_hi:[1,0]\n"
    "v_pk_mul_f32 v[10:11], v[10:11], v[70:71] op_sel_hi:[1,0]\n"
    "v_pk_mul_f32 v[12:13], v[12:13], v[70:71] op_sel_hi:[1,0]\n"
    "v_pk_mul_f32 v[14:15], v[14:15], v[70:71] op_sel_hi:[1,0]\n"
    "v_cvt_pk_bf16_f32 v60, v8, v9\n"
    "v_cvt_pk_bf16_f32 v62, v10, v11\n"
    "v_cvt_pk_bf16_f32 v61, v12, v13\n"
    "v_cvt_pk_bf16_f32 v63, v14, v15\n"
    "buffer_store_dwordx4 v[60:63], v227, s[0:3], s31 offen offset:1024 sc1\n"
    "v_mov_b32 v84, s27\n"
    "s_waitcnt vmcnt(0)\n"
    "buffer_store_dword v84, v226, s[0:3], 0 offen sc1\n"
    ".Lb2_nx_%=:\n"
    "s_mov_b32 s26, s27\n"
    "s_cmp_lt_u32 s26, 64\n"
    "s_cbranch_scc1 .Lb2_top_%=\n"
    "s_waitcnt vmcnt(0) lgkmcnt(0)\n"
    "s_endpgm\n"
    ".Lb2_out_%=:\n"
    "buffer_store_dwordx4 v[56:59], v228, s[8:11], s30 offen\n"
    "buffer_store_dwordx4 v[60:63], v229, s[8:11], s30 offen\n"
    "s_branch .Lb2_nx_%=\n"
    ".Lb2_sr_%=:\n"
    "s_add_u32 s20, s20, 1\n"
    "s_cmp_lt_u32 s20, 0x40000\n"
    "s_cbranch_scc1 .Lb2_sp8_%=\n"
    "v_mov_b32 v84, 72\n"
    "s_mov_b32 s24, 33728\n"
    "buffer_store_dword v84, off, s[0:3], s24 sc1\n"
    "s_waitcnt vmcnt(0)\n"
    "s_branch .Lb2_sg_%=\n"
    ".Lb2_sp8_%=:\n"
    "s_sleep 2\n"
    "s_branch .Lb2_sl_%=\n"
    :: "{s[0:3]}"(s0_), "{s[4:7]}"(s4_), "{s[8:11]}"(s8_), "{s[12:15]}"(s12_), "{s[16:19]}"(s16_), "{v[0:3]}"(v0_), "{v[4:7]}"(v4_), "{v[224:227]}"(v224_), "{v[228:231]}"(v228_), "{v[232:235]}"(v232_) : "memory", "v255", "a255", "s95");
    __builtin_unreachable();
}
constexpr int MEGA_LDS = 163840;
__global__ void __launch_bounds__(256, 1) mega_kernel(MegaArgs A) {
    extern __shared__ __attribute__((aligned(16))) char smem[];
    int* sRole = (int*)smem;
    const int x4 = (int)(__builtin_amdgcn_s_getreg((3 << 11) | 20) & 3u);
    if (threadIdx.x < 64) {
        const rsrc_t rf = mk_rsrc(arena_base(A.ar), ARENA);
        unsigned tm[2] = {~0u, ~0u};
#pragma unroll
        for (int k = 0; k < 4; ++k) {
            const unsigned long long t0 = __builtin_amdgcn_s_memtime();
            if (threadIdx.x == 0) bst4(rf, (blockIdx.x >> 7) * 16384 + (blockIdx.x & 127) * 64 + (k & 1) * 8192, 0u);
            DRAIN();
            const unsigned d = (unsigned)(__builtin_amdgcn_s_memtime() - t0);
            tm[k & 1] = d < tm[k & 1] ? d : tm[k & 1];
        }
        if (threadIdx.x == 0) {
            unsigned* cnt = (unsigned*)(arena_base(A.ar) + xl(Z_TK, 0));
            int role = -1;
            auto take = [&](int pool) {
                if (role >= 0) return;
                const int k = (int)__hip_atomic_fetch_add(cnt + 16 * pool, 1u, RLX_AGENT), x = pool & 3;
                if (k < 32) role = (pool >> 2) * 128 + (k < 24 ? (k >> 3) * 32 + 8 * x + (k & 7) : 96 + ((k - 24) >> 2) * 16 + 4 * x + (k & 3));
            };
            take((tm[1] < tm[0] ? 4 : 0) + x4);
            for (int p = 0; p < 8; ++p) take(p);
            *sRole = role;
        }
    }
    __syncthreads();
    const int role = RFL(*sRole), L = role >> 7, r = role & 127;
    __syncthreads();
    const bool on = (A.layers >> L) & 1;
    if (r < 32) { if (on) qk_role(A, L, r, x4); }
    else if (r < 64) { if (on) vo_role(A, L, r - 32, x4); }
    else if (r < 96) ct_role(A, L, r - 64);
    else if (r < 112) { if (on) { if (r < 107) b1_role(A, L, r - 96, x4); else b2_role(A, L, r - 107, x4); } }
    else qw_role(A, L, 16 * L + r - 112, x4);
}

#define LAYERS 3
extern "C" void kernel_launch(void* const* d_in, const int* in_sizes, int n_in, void* d_out, int out_size, void* d_ws, size_t ws_size, hipStream_t stream) {
    MegaArgs A;
    A.x0 = (const bf16_t*)d_in[0]; A.mem0 = (const bf16_t*)d_in[1]; A.Wq = (const bf16_t*)d_in[2]; A.Wk = (const bf16_t*)d_in[3]; A.Wv = (const bf16_t*)d_in[4];
    A.Wo = (const bf16_t*)d_in[5]; A.wqkv = (const bf16_t*)d_in[8]; A.wo = (const bf16_t*)d_in[9];
    A.ar = (char*)d_ws + 266240;
    A.out = (bf16_t*)d_out; A.layers = LAYERS; A.pad = 0;
    (void)hipFuncSetAttribute((const void*)mega_kernel, hipFuncAttributeMaxDynamicSharedMemorySize, MEGA_LDS);
    k_zero<<<2 * (Z_END - Z_TK) / 4096, 256, 0, stream>>>(A.ar);
    mega_kernel<<<256, 256, MEGA_LDS, stream>>>(A);
}
```

```cpp
#include <hip/hip_runtime.h>
#include <hip/hip_bf16.h>
#include <stdint.h>

typedef unsigned short bf16_t;
typedef short bf16x8 __attribute__((ext_vector_type(8)));
typedef short bf16x4 __attribute__((ext_vector_type(4)));
typedef float f32x4 __attribute__((ext_vector_type(4)));
typedef unsigned u32x4 __attribute__((ext_vector_type(4)));
typedef unsigned u32x2 __attribute__((ext_vector_type(2)));
typedef unsigned long long u64;
typedef __bf16 bf16v2 __attribute__((ext_vector_type(2)));
typedef float f32v2 __attribute__((ext_vector_type(2)));
#define DEV __device__ __forceinline__
DEV unsigned pk2(float a, float b) { f32v2 f = {a, b}; bf16v2 r = __builtin_convertvector(f, bf16v2); return __builtin_bit_cast(unsigned, r); }
DEV float rb(float a) { return __uint_as_float((pk2(a, 0.f) & 0xffffu) << 16); }
DEV float lo16(unsigned u) { return __uint_as_float(u << 16); }
DEV float hi16(unsigned u) { return __uint_as_float(u & 0xffff0000u); }
constexpr int D_ = 640;
#define RLX_AGENT __ATOMIC_RELAXED, __HIP_MEMORY_SCOPE_AGENT
#define AUX_SC1 16
typedef __amdgpu_buffer_rsrc_t rsrc_t;
DEV rsrc_t mk_rsrc(const void* p, unsigned bytes) { return __builtin_amdgcn_make_buffer_rsrc((void*)p, 0, (int)bytes, 0x00020000); }
DEV void bst16(rsrc_t r, unsigned off, u32x4 v) { __builtin_amdgcn_raw_buffer_store_b128(v, r, off, 0, AUX_SC1); }
DEV void bst8(rsrc_t r, unsigned off, u32x2 v) { __builtin_amdgcn_raw_buffer_store_b64(v, r, off, 0, AUX_SC1); }
DEV void bst4(rsrc_t r, unsigned off, unsigned v) { __builtin_amdgcn_raw_buffer_store_b32(v, r, off, 0, AUX_SC1); }
#define DRAIN() asm volatile("s_waitcnt vmcnt(0)" ::: "memory")
#define DRAIN_BARRIER() asm volatile("s_waitcnt vmcnt(0) lgkmcnt(0)\n\ts_barrier" ::: "memory")
#define RFL(x) ((unsigned)__builtin_amdgcn_readfirstlane((int)(x)))
DEV unsigned lo(const void* p) { return (unsigned)(size_t)p; }
DEV unsigned hi(const void* p) { return (unsigned)((size_t)p >> 32) & 0xffffu; }
DEV unsigned nosent(unsigned x) { return min(x, 0xfffffffeu); }
DEV int sw2(int x) { return ((x & 1) << 1) | (x >> 1); }

DEV unsigned xl(unsigned o, int side) { const unsigned ph = ((o >> 13) << 14) | (o & 8191u); return ph | ((((ph >> 21) ^ side) & 1u) << 13); }
constexpr unsigned Z_TK = 16384, Z_QV = Z_TK + 1024, Z_CT = Z_QV + 1024, Z_QW = Z_CT + 2048, Z_X = 3 * 8192, Z_X1 = 7 * 8192, Z_XN = 9 * 8192, Z_O = 10 * 8192, Z_ST = 11 * 8192, Z_END = 12 * 8192;
constexpr unsigned RG_UT = 1u << 21, RG_UTX = RG_UT + (1u << 20), RG_UM = RG_UTX + 262144, RG_UMX = RG_UM + 262144, RG_PT = RG_UMX + 65536, RG_PM = RG_PT + 65536, RG_MEM = RG_PM + 32768;
constexpr unsigned SLOT = 1u << 22;
constexpr unsigned REC0 = 20u << 20, RC_X1 = 5 * 16384, RC_O = 10 * 16384, RC_X2 = 15 * 16384, ST0 = 1u << 20, QT0 = 52u << 20, DBG0 = 116u << 20, ARENA = 120u << 20;
DEV unsigned rec(int s, int L) { return REC0 + (s << 19) + ((((s >> 2) ^ L) & 1) << 13); }
constexpr unsigned OOB = 0x7f000000u, WBYTES = 640u * 6144u * 2u;
DEV char* arena_base(char* p) { return (char*)(((size_t)p + 0x3fffff) & ~(size_t)0x3fffff); }

struct MegaArgs {
    const bf16_t* x0; const bf16_t* mem0; const bf16_t* Wq; const bf16_t* Wk; const bf16_t* Wv; const bf16_t* Wo; const bf16_t* wqkv; const bf16_t* wo;
    char* ar; bf16_t* out; int layers, pad;
};
__global__ void __launch_bounds__(256) k_zero(char* ar) {
    *(u32x4*)(arena_base(ar) + 2 * Z_TK + (blockIdx.x * 256 + threadIdx.x) * 16) = u32x4{0u, 0u, 0u, 0u};
}
#define ROLE_HEAD const int l = threadIdx.x & 63, l16 = l & 15, lq = l >> 4, w = RFL(threadIdx.x >> 6); char* const ab = arena_base(A.ar); \
    const rsrc_t rr = mk_rsrc(ab, ARENA); const u32x4 rx = {lo(ab), hi(ab), ARENA, 0x20000u}; const u32x4 ones = {~0u, ~0u, ~0u, ~0u}; const u32x2 ones2 = {~0u, ~0u}
DEV void qk_role(const MegaArgs& A, const int L, const int h, const int rep) {
    ROLE_HEAD;
    const int i = 2 * w + (lq & 1), p = l16 < 8 ? l16 >> 2 : l16 & 1, rho = 4 * (i >> 1) + 2 * p + (i & 1), tp = 4 * w + lq;
    const unsigned pm = (l >= 32 || l16 >= 10) ? OOB : xl(RG_PM + (h >> 3) * 8192 +
        (l16 < 8 ? ((h & 7) >> 1) * 1024 + ((2 * (h & 1) + ((l16 >> 1) & 1)) * 16 + rho) * 16 + (l16 & 1) * 8 : 4096 + (((h & 7) >> 1) * 16 + rho) * 16 + (h & 1) * 8), L);
    const unsigned pt = xl(RG_PT + h * 2048 + tp * 16 + (l16 < 8 ? (l16 & 3) * 256 + (l16 >> 2) * 8 : (l16 & 1) * 8 + (l16 < 10 ? 1792 : (l16 < 14 ? 1024 + ((l16 - 10) >> 1) * 256 : 1536))), L);
    u64 cm[4];
    const int k0 = (l16 >= 4 && l16 < 8) ? 4 * (l16 - 4) : ((l16 >= 10 && l16 < 14) ? 4 * (l16 - 10) : -4);
#pragma unroll
    for (int r = 0; r < 4; ++r) cm[r] = __ballot(k0 + r <= tp);
    bst8(rr, pm, ones2); bst8(rr, pt, ones2);
    DRAIN_BARRIER();
    bst4(rr, threadIdx.x < 4 ? xl(Z_QV + l * 256 + h * 4, L) : OOB, 1u);
    const u32x4 s0_ = {RFL(rx[0]), RFL(rx[1]), RFL(rx[2]), RFL(rx[3])};
    const u32x4 s4_ = {RFL(lo(A.Wk + h * 192)), RFL(hi(A.Wk + h * 192)), RFL(WBYTES - h * 384), RFL(0x20000u)};
    const u32x4 s8_ = {RFL(lo(A.Wq + h * 192)), RFL(hi(A.Wq + h * 192)), RFL(WBYTES - h * 384), RFL(0x20000u)};
    const u32x4 s12_ = {RFL((unsigned)cm[0]), RFL((unsigned)(cm[0] >> 32)), RFL((unsigned)cm[1]), RFL((unsigned)(cm[1] >> 32))};
    const u32x4 s16_ = {RFL((unsigned)cm[2]), RFL((unsigned)(cm[2] >> 32)), RFL((unsigned)cm[3]), RFL((unsigned)(cm[3] >> 32))};
    const u32x4 s20_ = {RFL(w), RFL(L << 13), RFL(xl(RG_MEM + rep * 16384, L) + w * 2048), RFL(w < 2 ? xl(RG_MEM + rep * 16384 + 8192, L) + w * 1024 : OOB)};
    const u32x4 s24_ = {RFL(REC0 + w * 2048), RFL(QT0 + h * 32768 + w * 2048), RFL(h == 0 && w == 0 ? xl(Z_X, L) : OOB), RFL(xl(Z_X, L))};
    const u32x4 s28_ = {RFL(xl(Z_CT + (h & 7) * 256, L)), RFL(xl(Z_XN + rep * 2048, 1)), RFL(xl(Z_QW + h * 128, L)), RFL(DBG0 + ((L * 128 + h) * 4 + w) * 2048)};
    const u32x4 v0_ = {(unsigned)(((3 * w + 0) * 64 + l) / 24 * 12288 + ((((3 * w + 0) * 64 + l) % 24) ^ ((((3 * w + 0) * 64 + l) / 48) & 6)) * 16 - 0), (unsigned)(((3 * w + 1) * 64 + l) / 24 * 12288 + ((((3 * w + 1) * 64 + l) % 24) ^ ((((3 * w + 1) * 64 + l) / 48) & 6)) * 16 - 1024), (unsigned)(((3 * w + 2) * 64 + l) / 24 * 12288 + ((((3 * w + 2) * 64 + l) % 24) ^ ((((3 * w + 2) * 64 + l) / 48) & 6)) * 16 - 2048), (unsigned)((8 * lq + ((l >> 2) & 1) + ((l >> 1) & 4)) * 384 + (w ^ (((8 * lq + ((l >> 2) & 1) + ((l >> 1) & 4)) >> 2) & 3)) * 32 + (l & 3) * 8 + ((w + 0) & 3) * 12288)};
    const u32x4 v4_ = {(unsigned)((8 * lq + ((l >> 2) & 1) + ((l >> 1) & 4)) * 384 + (w ^ (((8 * lq + ((l >> 2) & 1) + ((l >> 1) & 4)) >> 2) & 3)) * 32 + (l & 3) * 8 + ((w + 1) & 3) * 12288), (unsigned)((8 * lq + ((l >> 2) & 1) + ((l >> 1) & 4)) * 384 + (w ^ (((8 * lq + ((l >> 2) & 1) + ((l >> 1) & 4)) >> 2) & 3)) * 32 + (l & 3) * 8 + ((w + 2) & 3) * 12288), (unsigned)((8 * lq + ((l >> 2) & 1) + ((l >> 1) & 4)) * 384 + (w ^ (((8 * lq + ((l >> 2) & 1) + ((l >> 1) & 4)) >> 2) & 3)) * 32 + (l & 3) * 8 + ((w + 3) & 3) * 12288), (unsigned)((8 * lq + ((l >> 2) & 1) + ((l >> 1) & 4)) * 384 + (w ^ (((8 * lq + ((l >> 2) & 1) + ((l >> 1) & 4)) >> 2) & 3)) * 32 + (l & 3) * 8)};
    const u32x4 v92_ = {(unsigned)(l * 16 + ((w + 0) & 3) * 11264), (unsigned)(l * 16 + ((w + 1) & 3) * 11264), (unsigned)(l * 16 + ((w + 2) & 3) * 11264), (unsigned)(l * 16 + ((w + 3) & 3) * 11264)};
    const u32x4 v96_ = {(unsigned)(l * 16), (unsigned)(l * 16 + 53248 + w * 20480), (unsigned)((l & 2) ? 0xeeeeu : 0x4444u), (unsigned)(135168 + w * 1808 + l16 * 112 + lq * 4)};
    const u32x4 v100_ = {(unsigned)(135168 + w * 1808 + (l >> 2) * 112 + (l & 3) * 8), (unsigned)(135168 + w * 1808 + l16 * 176 + lq * 16), (unsigned)(135168 + (l16 < 10 ? (2 * w + (lq & 1)) * 176 + l16 * 16 : 1792)), (unsigned)(142400 + w * 4352 + l16 * 272 + lq * 16)};
    const u32x4 v104_ = {(unsigned)(142400 + (4 * w + lq) * 272 + l16 * 16), (unsigned)(pm), (unsigned)(pt), (unsigned)(0xff800000u)};
    asm volatile(
    "s_mov_b32 s32, 0\n"
    "s_cmp_eq_u32 s21, 0\n"
    "s_cbranch_scc1 .Lqk_ho1_%=\n"
    "s_sleep 35\n"
    "s_sleep 35\n"
    "s_sleep 35\n"
    "s_sleep 35\n"
    "s_sleep 35\n"
    "s_sleep 35\n"
    "s_sleep 35\n"
    "s_sleep 35\n"
    "s_sleep 35\n"
    "s_sleep 35\n"
    "s_sleep 35\n"
    "s_sleep 35\n"
    "s_sleep 35\n"
    "s_sleep 35\n"
    ".Lqk_ho1_%=:\n"
    "s_mul_i32 s43, s20, 3072\n"
    "s_mov_b32 s52, 0\n"
    "s_add_u32 m0, s43, 0\n"
    "s_nop 0\n"
    "buffer_load_dwordx4 v0, s[4:7], s52 offen offset:0 nt lds\n"
    "buffer_load_dwordx4 v1, s[4:7], s52 offen offset:1024 nt lds\n"
    "buffer_load_dwordx4 v2, s[4:7], s52 offen offset:2048 nt lds\n"
    "s_mov_b32 s52, 1572864\n"
    "s_add_u32 m0, s43, 12288\n"
    "s_nop 0\n"
    "buffer_load_dwordx4 v0, s[4:7], s52 offen offset:0 nt lds\n"
    "buffer_load_dwordx4 v1, s[4:7], s52 offen offset:1024 nt lds\n"
    "buffer_load_dwordx4 v2, s[4:7], s52 offen offset:2048 nt lds\n"
    "s_mov_b32 s52, 3145728\n"
    "s_add_u32 m0, s43, 24576\n"
    "s_nop 0\n"
    "buffer_load_dwordx4 v0, s[4:7], s52 offen offset:0 nt lds\n"
    "buffer_load_dwordx4 v1, s[4:7], s52 offen offset:1024 nt lds\n"
    "buffer_load_dwordx4 v2, s[4:7], s52 offen offset:2048 nt lds\n"
    "s_mov_b32 s52, 4718592\n"
    "s_add_u32 m0, s43, 36864\n"
    "s_nop 0\n"
    "buffer_load_dwordx4 v0, s[4:7], s52 offen offset:0 nt lds\n"
    "buffer_load_dwordx4 v1, s[4:7], s52 offen offset:1024 nt lds\n"
    "buffer_load_dwordx4 v2, s[4:7], s52 offen offset:2048 nt lds\n"
    "s_mov_b32 s52, 393216\n"
    "s_add_u32 m0, s43, 49152\n"
    "s_nop 0\n"
    "buffer_load_dwordx4 v0, s[4:7], s52 offen offset:0 nt lds\n"
    "buffer_load_dwordx4 v1, s[4:7], s52 offen offset:1024 nt lds\n"
    "buffer_load_dwordx4 v2, s[4:7], s52 offen offset:2048 nt lds\n"
    "s_mov_b32 s52, 1966080\n"
    "s_add_u32 m0, s43, 61440\n"
    "s_nop 0\n"
    "buffer_load_dwordx4 v0, s[4:7], s52 offen offset:0 nt lds\n"
    "buffer_load_dwordx4 v1, s[4:7], s52 offen offset:1024 nt lds\n"
    "buffer_load_dwordx4 v2, s[4:7], s52 offen offset:2048 nt lds\n"
    "s_mov_b32 s52, 3538944\n"
    "s_add_u32 m0, s43, 73728\n"
    "s_nop 0\n"
    "buffer_load_dwordx4 v0, s[4:7], s52 offen offset:0 nt lds\n"
    "buffer_load_dwordx4 v1, s[4:7], s52 offen offset:1024 nt lds\n"
    "buffer_load_dwordx4 v2, s[4:7], s52 offen offset:2048 nt lds\n"
    "s_mov_b32 s52, 5111808\n"
    "s_add_u32 m0, s43, 86016\n"
    "s_nop 0\n"
    "buffer_load_dwordx4 v0, s[4:7], s52 offen offset:0 nt lds\n"
    "buffer_load_dwordx4 v1, s[4:7], s52 offen offset:1024 nt lds\n"
    "buffer_load_dwordx4 v2, s[4:7], s52 offen offset:2048 nt lds\n"
    "s_mov_b32 s52, 786432\n"
    "s_add_u32 m0, s43, 98304\n"
    "s_nop 0\n"
    "buffer_load_dwordx4 v0, s[4:7], s52 offen offset:0 nt lds\n"
    "buffer_load_dwordx4 v1, s[4:7], s52 offen offset:1024 nt lds\n"
    "buffer_load_dwordx4 v2, s[4:7], s52 offen offset:2048 nt lds\n"
    "s_mov_b32 s52, 2359296\n"
    "s_add_u32 m0, s43, 110592\n"
    "s_nop 0\n"
    "buffer_load_dwordx4 v0, s[4:7], s52 offen offset:0 nt lds\n"
    "buffer_load_dwordx4 v1, s[4:7], s52 offen offset:1024 nt lds\n"
    "buffer_load_dwordx4 v2, s[4:7], s52 offen offset:2048 nt lds\n"
    "s_mov_b32 s52, 3932160\n"
    "s_add_u32 m0, s43, 122880\n"
    "s_nop 0\n"
    "buffer_load_dwordx4 v0, s[4:7], s52 offen offset:0 nt lds\n"
    "buffer_load_dwordx4 v1, s[4:7], s52 offen offset:1024 nt lds\n"
    "buffer_load_dwordx4 v2, s[4:7], s52 offen offset:2048 nt lds\n"
    "s_mov_b32 s52, 5505024\n"
    "s_add_u32 m0, s43, 135168\n"
    "s_nop 0\n"
    "buffer_load_dwordx4 v0, s[4:7], s52 offen offset:0 nt lds\n"
    "buffer_load_dwordx4 v1, s[4:7], s52 offen offset:1024 nt lds\n"
    "buffer_load_dwordx4 v2, s[4:7], s52 offen offset:2048 nt lds\n"
    "s_waitcnt vmcnt(24)\n"
    "s_barrier\n"
    "v_add_u32 v8, 0, v3\n"
    "ds_read_b64_tr_b16 a[0:1], v8 offset:0\n"
    "ds_read_b64_tr_b16 a[2:3], v8 offset:768\n"
    "ds_read_b64_tr_b16 a[80:81], v8 offset:128\n"
    "ds_read_b64_tr_b16 a[82:83], v8 offset:896\n"
    "ds_read_b64_tr_b16 a[160:161], v8 offset:256\n"
    "ds_read_b64_tr_b16 a[162:163], v8 offset:1024\n"
    "v_add_u32 v9, 0, v4\n"
    "ds_read_b64_tr_b16 a[16:17], v9 offset:0\n"
    "ds_read_b64_tr_b16 a[18:19], v9 offset:768\n"
    "ds_read_b64_tr_b16 a[96:97], v9 offset:128\n"
    "ds_read_b64_tr_b16 a[98:99], v9 offset:896\n"
    "ds_read_b64_tr_b16 a[176:177], v9 offset:256\n"
    "ds_read_b64_tr_b16 a[178:179], v9 offset:1024\n"
    "v_add_u32 v10, 0, v5\n"
    "ds_read_b64_tr_b16 a[32:33], v10 offset:0\n"
    "ds_read_b64_tr_b16 a[34:35], v10 offset:768\n"
    "ds_read_b64_tr_b16 a[112:113], v10 offset:128\n"
    "ds_read_b64_tr_b16 a[114:115], v10 offset:896\n"
    "ds_read_b64_tr_b16 a[192:193], v10 offset:256\n"
    "ds_read_b64_tr_b16 a[194:195], v10 offset:1024\n"
    "v_add_u32 v11, 0, v6\n"
    "ds_read_b64_tr_b16 a[48:49], v11 offset:0\n"
    "ds_read_b64_tr_b16 a[50:51], v11 offset:768\n"
    "ds_read_b64_tr_b16 a[128:129], v11 offset:128\n"
    "ds_read_b64_tr_b16 a[130:131], v11 offset:896\n"
    "ds_read_b64_tr_b16 a[208:209], v11 offset:256\n"
    "ds_read_b64_tr_b16 a[210:211], v11 offset:1024\n"
    "s_waitcnt lgkmcnt(0)\n"
    "s_barrier\n"
    "s_mov_b32 s52, 1179648\n"
    "s_add_u32 m0, s43, 0\n"
    "s_nop 0\n"
    "buffer_load_dwordx4 v0, s[4:7], s52 offen offset:0 nt lds\n"
    "buffer_load_dwordx4 v1, s[4:7], s52 offen offset:1024 nt lds\n"
    "buffer_load_dwordx4 v2, s[4:7], s52 offen offset:2048 nt lds\n"
    "s_mov_b32 s52, 2752512\n"
    "s_add_u32 m0, s43, 12288\n"
    "s_nop 0\n"
    "buffer_load_dwordx4 v0, s[4:7], s52 offen offset:0 nt lds\n"
    "buffer_load_dwordx4 v1, s[4:7], s52 offen offset:1024 nt lds\n"
    "buffer_load_dwordx4 v2, s[4:7], s52 offen offset:2048 nt lds\n"
    "s_mov_b32 s52, 4325376\n"
    "s_add_u32 m0, s43, 24576\n"
    "s_nop 0\n"
    "buffer_load_dwordx4 v0, s[4:7], s52 offen offset:0 nt lds\n"
    "buffer_load_dwordx4 v1, s[4:7], s52 offen offset:1024 nt lds\n"
    "buffer_load_dwordx4 v2, s[4:7], s52 offen offset:2048 nt lds\n"
    "s_mov_b32 s52, 5898240\n"
    "s_add_u32 m0, s43, 36864\n"
    "s_nop 0\n"
    "buffer_load_dwordx4 v0, s[4:7], s52 offen offset:0 nt lds\n"
    "buffer_load_dwordx4 v1, s[4:7], s52 offen offset:1024 nt lds\n"
    "buffer_load_dwordx4 v2, s[4:7], s52 offen offset:2048 nt lds\n"
    "s_waitcnt vmcnt(24)\n"
    "s_barrier\n"
    "v_add_u32 v8, 49152, v3\n"
    "ds_read_b64_tr_b16 a[4:5], v8 offset:0\n"
    "ds_read_b64_tr_b16 a[6:7], v8 offset:768\n"
    "ds_read_b64_tr_b16 a[84:85], v8 offset:128\n"
    "ds_read_b64_tr_b16 a[86:87], v8 offset:896\n"
    "ds_read_b64_tr_b16 a[164:165], v8 offset:256\n"
    "ds_read_b64_tr_b16 a[166:167], v8 offset:1024\n"
    "v_add_u32 v9, 49152, v4\n"
    "ds_read_b64_tr_b16 a[20:21], v9 offset:0\n"
    "ds_read_b64_tr_b16 a[22:23], v9 offset:768\n"
    "ds_read_b64_tr_b16 a[100:101], v9 offset:128\n"
    "ds_read_b64_tr_b16 a[102:103], v9 offset:896\n"
    "ds_read_b64_tr_b16 a[180:181], v9 offset:256\n"
    "ds_read_b64_tr_b16 a[182:183], v9 offset:1024\n"
    "v_add_u32 v10, 49152, v5\n"
    "ds_read_b64_tr_b16 a[36:37], v10 offset:0\n"
    "ds_read_b64_tr_b16 a[38:39], v10 offset:768\n"
    "ds_read_b64_tr_b16 a[116:117], v10 offset:128\n"
    "ds_read_b64_tr_b16 a[118:119], v10 offset:896\n"
    "ds_read_b64_tr_b16 a[196:197], v10 offset:256\n"
    "ds_read_b64_tr_b16 a[198:199], v10 offset:1024\n"
    "v_add_u32 v11, 49152, v6\n"
    "ds_read_b64_tr_b16 a[52:53], v11 offset:0\n"
    "ds_read_b64_tr_b16 a[54:55], v11 offset:768\n"
    "ds_read_b64_tr_b16 a[132:133], v11 offset:128\n"
    "ds_read_b64_tr_b16 a[134:135], v11 offset:896\n"
    "ds_read_b64_tr_b16 a[212:213], v11 offset:256\n"
    "ds_read_b64_tr_b16 a[214:215], v11 offset:1024\n"
    "s_waitcnt lgkmcnt(0)\n"
    "s_barrier\n"
    "s_mov_b32 s52, 6291456\n"
    "s_add_u32 m0, s43, 49152\n"
    "s_nop 0\n"
    "buffer_load_dwordx4 v0, s[4:7], s52 offen offset:0 nt lds\n"
    "buffer_load_dwordx4 v1, s[4:7], s52 offen offset:1024 nt lds\n"
    "buffer_load_dwordx4 v2, s[4:7], s52 offen offset:2048 nt lds\n"
    "s_mov_b32 s52, 6684672\n"
    "s_add_u32 m0, s43, 61440\n"
    "s_nop 0\n"
    "buffer_load_dwordx4 v0, s[4:7], s52 offen offset:0 nt lds\n"
    "buffer_load_dwordx4 v1, s[4:7], s52 offen offset:1024 nt lds\n"
    "buffer_load_dwordx4 v2, s[4:7], s52 offen offset:2048 nt lds\n"
    "s_mov_b32 s52, 7077888\n"
    "s_add_u32 m0, s43, 73728\n"
    "s_nop 0\n"
    "buffer_load_dwordx4 v0, s[4:7], s52 offen offset:0 nt lds\n"
    "buffer_load_dwordx4 v1, s[4:7], s52 offen offset:1024 nt lds\n"
    "buffer_load_dwordx4 v2, s[4:7], s52 offen offset:2048 nt lds\n"
    "s_mov_b32 s52, 7471104\n"
    "s_add_u32 m0, s43, 86016\n"
    "s_nop 0\n"
    "buffer_load_dwordx4 v0, s[4:7], s52 offen offset:0 nt lds\n"
    "buffer_load_dwordx4 v1, s[4:7], s52 offen offset:1024 nt lds\n"
    "buffer_load_dwordx4 v2, s[4:7], s52 offen offset:2048 nt lds\n"
    "s_waitcnt vmcnt(24)\n"
    "s_barrier\n"
    "v_add_u32 v8, 98304, v3\n"
    "ds_read_b64_tr_b16 a[8:9], v8 offset:0\n"
    "ds_read_b64_tr_b16 a[10:11], v8 offset:768\n"
    "ds_read_b64_tr_b16 a[88:89], v8 offset:128\n"
    "ds_read_b64_tr_b16 a[90:91], v8 offset:896\n"
    "ds_read_b64_tr_b16 a[168:169], v8 offset:256\n"
    "ds_read_b64_tr_b16 a[170:171], v8 offset:1024\n"
    "v_add_u32 v9, 98304, v4\n"
    "ds_read_b64_tr_b16 a[24:25], v9 offset:0\n"
    "ds_read_b64_tr_b16 a[26:27], v9 offset:768\n"
    "ds_read_b64_tr_b16 a[104:105], v9 offset:128\n"
    "ds_read_b64_tr_b16 a[106:107], v9 offset:896\n"
    "ds_read_b64_tr_b16 a[184:185], v9 offset:256\n"
    "ds_read_b64_tr_b16 a[186:187], v9 offset:1024\n"
    "v_add_u32 v10, 98304, v5\n"
    "ds_read_b64_tr_b16 a[40:41], v10 offset:0\n"
    "ds_read_b64_tr_b16 a[42:43], v10 offset:768\n"
    "ds_read_b64_tr_b16 a[120:121], v10 offset:128\n"
    "ds_read_b64_tr_b16 a[122:123], v10 offset:896\n"
    "ds_read_b64_tr_b16 a[200:201], v10 offset:256\n"
    "ds_read_b64_tr_b16 a[202:203], v10 offset:1024\n"
    "v_add_u32 v11, 98304, v6\n"
    "ds_read_b64_tr_b16 a[56:57], v11 offset:0\n"
    "ds_read_b64_tr_b16 a[58:59], v11 offset:768\n"
    "ds_read_b64_tr_b16 a[136:137], v11 offset:128\n"
    "ds_read_b64_tr_b16 a[138:139], v11 offset:896\n"
    "ds_read_b64_tr_b16 a[216:217], v11 offset:256\n"
    "ds_read_b64_tr_b16 a[218:219], v11 offset:1024\n"
    "s_waitcnt lgkmcnt(0)\n"
    "s_barrier\n"
    "s_mov_b32 s52, 0\n"
    "s_add_u32 m0, s43, 98304\n"
    "s_nop 0\n"
    "buffer_load_dwordx4 v0, s[8:11], s52 offen offset:0 nt lds\n"
    "buffer_load_dwordx4 v1, s[8:11], s52 offen offset:1024 nt lds\n"
    "buffer_load_dwordx4 v2, s[8:11], s52 offen offset:2048 nt lds\n"
    "s_mov_b32 s52, 1572864\n"
    "s_add_u32 m0, s43, 110592\n"
    "s_nop 0\n"
    "buffer_load_dwordx4 v0, s[8:11], s52 offen offset:0 nt lds\n"
    "buffer_load_dwordx4 v1, s[8:11], s52 offen offset:1024 nt lds\n"
    "buffer_load_dwordx4 v2, s[8:11], s52 offen offset:2048 nt lds\n"
    "s_mov_b32 s52, 3145728\n"
    "s_add_u32 m0, s43, 122880\n"
    "s_nop 0\n"
    "buffer_load_dwordx4 v0, s[8:11], s52 offen offset:0 nt lds\n"
    "buffer_load_dwordx4 v1, s[8:11], s52 offen offset:1024 nt lds\n"
    "buffer_load_dwordx4 v2, s[8:11], s52 offen offset:2048 nt lds\n"
    "s_mov_b32 s52, 4718592\n"
    "s_add_u32 m0, s43, 135168\n"
    "s_nop 0\n"
    "buffer_load_dwordx4 v0, s[8:11], s52 offen offset:0 nt lds\n"
    "buffer_load_dwordx4 v1, s[8:11], s52 offen offset:1024 nt lds\n"
    "buffer_load_dwordx4 v2, s[8:11], s52 offen offset:2048 nt lds\n"
    "s_waitcnt vmcnt(24)\n"
    "s_barrier\n"
    "v_add_u32 v8, 0, v3\n"
    "ds_read_b64_tr_b16 a[12:13], v8 offset:0\n"
    "ds_read_b64_tr_b16 a[14:15], v8 offset:768\n"
    "ds_read_b64_tr_b16 a[92:93], v8 offset:128\n"
    "ds_read_b64_tr_b16 a[94:95], v8 offset:896\n"
    "ds_read_b64_tr_b16 a[172:173], v8 offset:256\n"
    "ds_read_b64_tr_b16 a[174:175], v8 offset:1024\n"
    "v_add_u32 v9, 0, v4\n"
    "ds_read_b64_tr_b16 a[28:29], v9 offset:0\n"
    "ds_read_b64_tr_b16 a[30:31], v9 offset:768\n"
    "ds_read_b64_tr_b16 a[108:109], v9 offset:128\n"
    "ds_read_b64_tr_b16 a[110:111], v9 offset:896\n"
    "ds_read_b64_tr_b16 a[188:189], v9 offset:256\n"
    "ds_read_b64_tr_b16 a[190:191], v9 offset:1024\n"
    "v_add_u32 v10, 0, v5\n"
    "ds_read_b64_tr_b16 a[44:45], v10 offset:0\n"
    "ds_read_b64_tr_b16 a[46:47], v10 offset:768\n"
    "ds_read_b64_tr_b16 a[124:125], v10 offset:128\n"
    "ds_read_b64_tr_b16 a[126:127], v10 offset:896\n"
    "ds_read_b64_tr_b16 a[204:205], v10 offset:256\n"
    "ds_read_b64_tr_b16 a[206:207], v10 offset:1024\n"
    "v_add_u32 v11, 0, v6\n"
    "ds_read_b64_tr_b16 a[60:61], v11 offset:0\n"
    "ds_read_b64_tr_b16 a[62:63], v11 offset:768\n"
    "ds_read_b64_tr_b16 a[140:141], v11 offset:128\n"
    "ds_read_b64_tr_b16 a[142:143], v11 offset:896\n"
    "ds_read_b64_tr_b16 a[220:221], v11 offset:256\n"
    "ds_read_b64_tr_b16 a[222:223], v11 offset:1024\n"
    "s_waitcnt lgkmcnt(0)\n"
    "s_barrier\n"
    "s_mov_b32 s52, 393216\n"
    "s_add_u32 m0, s43, 0\n"
    "s_nop 0\n"
    "buffer_load_dwordx4 v0, s[8:11], s52 offen offset:0 nt lds\n"
    "buffer_load_dwordx4 v1, s[8:11], s52 offen offset:1024 nt lds\n"
    "buffer_load_dwordx4 v2, s[8:11], s52 offen offset:2048 nt lds\n"
    "s_mov_b32 s52, 1966080\n"
    "s_add_u32 m0, s43, 12288\n"
    "s_nop 0\n"
    "buffer_load_dwordx4 v0, s[8:11], s52 offen offset:0 nt lds\n"
    "buffer_load_dwordx4 v1, s[8:11], s52 offen offset:1024 nt lds\n"
    "buffer_load_dwordx4 v2, s[8:11], s52 offen offset:2048 nt lds\n"
    "s_mov_b32 s52, 3538944\n"
    "s_add_u32 m0, s43, 24576\n"
    "s_nop 0\n"
    "buffer_load_dwordx4 v0, s[8:11], s52 offen offset:0 nt lds\n"
    "buffer_load_dwordx4 v1, s[8:11], s52 offen offset:1024 nt lds\n"
    "buffer_load_dwordx4 v2, s[8:11], s52 offen offset:2048 nt lds\n"
    "s_mov_b32 s52, 5111808\n"
    "s_add_u32 m0, s43, 36864\n"
    "s_nop 0\n"
    "buffer_load_dwordx4 v0, s[8:11], s52 offen offset:0 nt lds\n"
    "buffer_load_dwordx4 v1, s[8:11], s52 offen offset:1024 nt lds\n"
    "buffer_load_dwordx4 v2, s[8:11], s52 offen offset:2048 nt lds\n"
    "s_waitcnt vmcnt(24)\n"
    "s_barrier\n"
    "v_add_u32 v8, 49152, v7\n"
    "ds_read_b64_tr_b16 a[64:65], v8 offset:0\n"
    "ds_read_b64_tr_b16 a[66:67], v8 offset:768\n"
    "ds_read_b64_tr_b16 a[144:145], v8 offset:128\n"
    "ds_read_b64_tr_b16 a[146:147], v8 offset:896\n"
    "ds_read_b64_tr_b16 a[224:225], v8 offset:256\n"
    "ds_read_b64_tr_b16 a[226:227], v8 offset:1024\n"
    "v_add_u32 v9, 61440, v7\n"
    "ds_read_b64_tr_b16 a[68:69], v9 offset:0\n"
    "ds_read_b64_tr_b16 a[70:71], v9 offset:768\n"
    "ds_read_b64_tr_b16 a[148:149], v9 offset:128\n"
    "ds_read_b64_tr_b16 a[150:151], v9 offset:896\n"
    "ds_read_b64_tr_b16 a[228:229], v9 offset:256\n"
    "ds_read_b64_tr_b16 a[230:231], v9 offset:1024\n"
    "v_add_u32 v10, 73728, v7\n"
    "ds_read_b64_tr_b16 a[72:73], v10 offset:0\n"
    "ds_read_b64_tr_b16 a[74:75], v10 offset:768\n"
    "ds_read_b64_tr_b16 a[152:153], v10 offset:128\n"
    "ds_read_b64_tr_b16 a[154:155], v10 offset:896\n"
    "ds_read_b64_tr_b16 a[232:233], v10 offset:256\n"
    "ds_read_b64_tr_b16 a[234:235], v10 offset:1024\n"
    "v_add_u32 v11, 86016, v7\n"
    "ds_read_b64_tr_b16 a[76:77], v11 offset:0\n"
    "ds_read_b64_tr_b16 a[78:79], v11 offset:768\n"
    "ds_read_b64_tr_b16 a[156:157], v11 offset:128\n"
    "ds_read_b64_tr_b16 a[158:159], v11 offset:896\n"
    "ds_read_b64_tr_b16 a[236:237], v11 offset:256\n"
    "ds_read_b64_tr_b16 a[238:239], v11 offset:1024\n"
    "s_waitcnt lgkmcnt(0)\n"
    "s_barrier\n"
    "s_mov_b32 s52, 786432\n"
    "s_add_u32 m0, s43, 49152\n"
    "s_nop 0\n"
    "buffer_load_dwordx4 v0, s[8:11], s52 offen offset:0 nt lds\n"
    "buffer_load_dwordx4 v1, s[8:11], s52 offen offset:1024 nt lds\n"
    "buffer_load_dwordx4 v2, s[8:11], s52 offen offset:2048 nt lds\n"
    "s_mov_b32 s52, 2359296\n"
    "s_add_u32 m0, s43, 61440\n"
    "s_nop 0\n"
    "buffer_load_dwordx4 v0, s[8:11], s52 offen offset:0 nt lds\n"
    "buffer_load_dwordx4 v1, s[8:11], s52 offen offset:1024 nt lds\n"
    "buffer_load_dwordx4 v2, s[8:11], s52 offen offset:2048 nt lds\n"
    "s_mov_b32 s52, 3932160\n"
    "s_add_u32 m0, s43, 73728\n"
    "s_nop 0\n"
    "buffer_load_dwordx4 v0, s[8:11], s52 offen offset:0 nt lds\n"
    "buffer_load_dwordx4 v1, s[8:11], s52 offen offset:1024 nt lds\n"
    "buffer_load_dwordx4 v2, s[8:11], s52 offen offset:2048 nt lds\n"
    "s_mov_b32 s52, 5505024\n"
    "s_add_u32 m0, s43, 86016\n"
    "s_nop 0\n"
    "buffer_load_dwordx4 v0, s[8:11], s52 offen offset:0 nt lds\n"
    "buffer_load_dwordx4 v1, s[8:11], s52 offen offset:1024 nt lds\n"
    "buffer_load_dwordx4 v2, s[8:11], s52 offen offset:2048 nt lds\n"
    "s_waitcnt vmcnt(24)\n"
    "s_barrier\n"
    "v_add_u32 v8, 98304, v3\n"
    "ds_read_b64_tr_b16 a[240:241], v8 offset:0\n"
    "ds_read_b64_tr_b16 a[242:243], v8 offset:768\n"
    "ds_read_b64_tr_b16 v[176:177], v8 offset:128\n"
    "ds_read_b64_tr_b16 v[178:179], v8 offset:896\n"
    "ds_read_b64_tr_b16 v[12:13], v8 offset:256\n"
    "ds_read_b64_tr_b16 v[14:15], v8 offset:1024\n"
    "v_add_u32 v9, 98304, v4\n"
    "ds_read_b64_tr_b16 v[112:113], v9 offset:0\n"
    "ds_read_b64_tr_b16 v[114:115], v9 offset:768\n"
    "ds_read_b64_tr_b16 v[192:193], v9 offset:128\n"
    "ds_read_b64_tr_b16 v[194:195], v9 offset:896\n"
    "ds_read_b64_tr_b16 v[28:29], v9 offset:256\n"
    "ds_read_b64_tr_b16 v[30:31], v9 offset:1024\n"
    "v_add_u32 v10, 98304, v5\n"
    "ds_read_b64_tr_b16 v[128:129], v10 offset:0\n"
    "ds_read_b64_tr_b16 v[130:131], v10 offset:768\n"
    "ds_read_b64_tr_b16 v[208:209], v10 offset:128\n"
    "ds_read_b64_tr_b16 v[210:211], v10 offset:896\n"
    "ds_read_b64_tr_b16 v[44:45], v10 offset:256\n"
    "ds_read_b64_tr_b16 v[46:47], v10 offset:1024\n"
    "v_add_u32 v11, 98304, v6\n"
    "ds_read_b64_tr_b16 v[144:145], v11 offset:0\n"
    "ds_read_b64_tr_b16 v[146:147], v11 offset:768\n"
    "ds_read_b64_tr_b16 v[224:225], v11 offset:128\n"
    "ds_read_b64_tr_b16 v[226:227], v11 offset:896\n"
    "ds_read_b64_tr_b16 v[60:61], v11 offset:256\n"
    "ds_read_b64_tr_b16 v[62:63], v11 offset:1024\n"
    "s_waitcnt lgkmcnt(0)\n"
    "s_barrier\n"
    "s_mov_b32 s52, 1179648\n"
    "s_add_u32 m0, s43, 98304\n"
    "s_nop 0\n"
    "buffer_load_dwordx4 v0, s[8:11], s52 offen offset:0 nt lds\n"
    "buffer_load_dwordx4 v1, s[8:11], s52 offen offset:1024 nt lds\n"
    "buffer_load_dwordx4 v2, s[8:11], s52 offen offset:2048 nt lds\n"
    "s_mov_b32 s52, 2752512\n"
    "s_add_u32 m0, s43, 110592\n"
    "s_nop 0\n"
    "buffer_load_dwordx4 v0, s[8:11], s52 offen offset:0 nt lds\n"
    "buffer_load_dwordx4 v1, s[8:11], s52 offen offset:1024 nt lds\n"
    "buffer_load_dwordx4 v2, s[8:11], s52 offen offset:2048 nt lds\n"
    "s_mov_b32 s52, 4325376\n"
    "s_add_u32 m0, s43, 122880\n"
    "s_nop 0\n"
    "buffer_load_dwordx4 v0, s[8:11], s52 offen offset:0 nt lds\n"
    "buffer_load_dwordx4 v1, s[8:11], s52 offen offset:1024 nt lds\n"
    "buffer_load_dwordx4 v2, s[8:11], s52 offen offset:2048 nt lds\n"
    "s_mov_b32 s52, 5898240\n"
    "s_add_u32 m0, s43, 135168\n"
    "s_nop 0\n"
    "buffer_load_dwordx4 v0, s[8:11], s52 offen offset:0 nt lds\n"
    "buffer_load_dwordx4 v1, s[8:11], s52 offen offset:1024 nt lds\n"
    "buffer_load_dwordx4 v2, s[8:11], s52 offen offset:2048 nt lds\n"
    "s_waitcnt vmcnt(24)\n"
    "s_barrier\n"
    "v_add_u32 v8, 0, v3\n"
    "ds_read_b64_tr_b16 a[244:245], v8 offset:0\n"
    "ds_read_b64_tr_b16 a[246:247], v8 offset:768\n"
    "ds_read_b64_tr_b16 v[180:181], v8 offset:128\n"
    "ds_read_b64_tr_b16 v[182:183], v8 offset:896\n"
    "ds_read_b64_tr_b16 v[16:17], v8 offset:256\n"
    "ds_read_b64_tr_b16 v[18:19], v8 offset:1024\n"
    "v_add_u32 v9, 0, v4\n"
    "ds_read_b64_tr_b16 v[116:117], v9 offset:0\n"
    "ds_read_b64_tr_b16 v[118:119], v9 offset:768\n"
    "ds_read_b64_tr_b16 v[196:197], v9 offset:128\n"
    "ds_read_b64_tr_b16 v[198:199], v9 offset:896\n"
    "ds_read_b64_tr_b16 v[32:33], v9 offset:256\n"
    "ds_read_b64_tr_b16 v[34:35], v9 offset:1024\n"
    "v_add_u32 v10, 0, v5\n"
    "ds_read_b64_tr_b16 v[132:133], v10 offset:0\n"
    "ds_read_b64_tr_b16 v[134:135], v10 offset:768\n"
    "ds_read_b64_tr_b16 v[212:213], v10 offset:128\n"
    "ds_read_b64_tr_b16 v[214:215], v10 offset:896\n"
    "ds_read_b64_tr_b16 v[48:49], v10 offset:256\n"
    "ds_read_b64_tr_b16 v[50:51], v10 offset:1024\n"
    "v_add_u32 v11, 0, v6\n"
    "ds_read_b64_tr_b16 v[148:149], v11 offset:0\n"
    "ds_read_b64_tr_b16 v[150:151], v11 offset:768\n"
    "ds_read_b64_tr_b16 v[228:229], v11 offset:128\n"
    "ds_read_b64_tr_b16 v[230:231], v11 offset:896\n"
    "ds_read_b64_tr_b16 v[64:65], v11 offset:256\n"
    "ds_read_b64_tr_b16 v[66:67], v11 offset:1024\n"
    "s_waitcnt lgkmcnt(0)\n"
    "s_barrier\n"
    "s_mov_b32 s52, 6291456\n"
    "s_add_u32 m0, s43, 0\n"
    "s_nop 0\n"
    "buffer_load_dwordx4 v0, s[8:11], s52 offen offset:0 nt lds\n"
    "buffer_load_dwordx4 v1, s[8:11], s52 offen offset:1024 nt lds\n"
    "buffer_load_dwordx4 v2, s[8:11], s52 offen offset:2048 nt lds\n"
    "s_mov_b32 s52, 6684672\n"
    "s_add_u32 m0, s43, 12288\n"
    "s_nop 0\n"
    "buffer_load_dwordx4 v0, s[8:11], s52 offen offset:0 nt lds\n"
    "buffer_load_dwordx4 v1, s[8:11], s52 offen offset:1024 nt lds\n"
    "buffer_load_dwordx4 v2, s[8:11], s52 offen offset:2048 nt lds\n"
    "s_mov_b32 s52, 7077888\n"
    "s_add_u32 m0, s43, 24576\n"
    "s_nop 0\n"
    "buffer_load_dwordx4 v0, s[8:11], s52 offen offset:0 nt lds\n"
    "buffer_load_dwordx4 v1, s[8:11], s52 offen offset:1024 nt lds\n"
    "buffer_load_dwordx4 v2, s[8:11], s52 offen offset:2048 nt lds\n"
    "s_mov_b32 s52, 7471104\n"
    "s_add_u32 m0, s43, 36864\n"
    "s_nop 0\n"
    "buffer_load_dwordx4 v0, s[8:11], s52 offen offset:0 nt lds\n"
    "buffer_load_dwordx4 v1, s[8:11], s52 offen offset:1024 nt lds\n"
    "buffer_load_dwordx4 v2, s[8:11], s52 offen offset:2048 nt lds\n"
    "s_waitcnt vmcnt(24)\n"
    "s_barrier\n"
    "v_add_u32 v8, 49152, v3\n"
    "ds_read_b64_tr_b16 a[248:249], v8 offset:0\n"
    "ds_read_b64_tr_b16 a[250:251], v8 offset:768\n"
    "ds_read_b64_tr_b16 v[184:185], v8 offset:128\n"
    "ds_read_b64_tr_b16 v[186:187], v8 offset:896\n"
    "ds_read_b64_tr_b16 v[20:21], v8 offset:256\n"
    "ds_read_b64_tr_b16 v[22:23], v8 offset:1024\n"
    "v_add_u32 v9, 49152, v4\n"
    "ds_read_b64_tr_b16 v[120:121], v9 offset:0\n"
    "ds_read_b64_tr_b16 v[122:123], v9 offset:768\n"
    "ds_read_b64_tr_b16 v[200:201], v9 offset:128\n"
    "ds_read_b64_tr_b16 v[202:203], v9 offset:896\n"
    "ds_read_b64_tr_b16 v[36:37], v9 offset:256\n"
    "ds_read_b64_tr_b16 v[38:39], v9 offset:1024\n"
    "v_add_u32 v10, 49152, v5\n"
    "ds_read_b64_tr_b16 v[136:137], v10 offset:0\n"
    "ds_read_b64_tr_b16 v[138:139], v10 offset:768\n"
    "ds_read_b64_tr_b16 v[216:217], v10 offset:128\n"
    "ds_read_b64_tr_b16 v[218:219], v10 offset:896\n"
    "ds_read_b64_tr_b16 v[52:53], v10 offset:256\n"
    "ds_read_b64_tr_b16 v[54:55], v10 offset:1024\n"
    "v_add_u32 v11, 49152, v6\n"
    "ds_read_b64_tr_b16 v[152:153], v11 offset:0\n"
    "ds_read_b64_tr_b16 v[154:155], v11 offset:768\n"
    "ds_read_b64_tr_b16 v[232:233], v11 offset:128\n"
    "ds_read_b64_tr_b16 v[234:235], v11 offset:896\n"
    "ds_read_b64_tr_b16 v[68:69], v11 offset:256\n"
    "ds_read_b64_tr_b16 v[70:71], v11 offset:1024\n"
    "s_waitcnt lgkmcnt(0)\n"
    "s_barrier\n"
    "s_waitcnt vmcnt(12)\n"
    "s_barrier\n"
    "v_add_u32 v8, 98304, v3\n"
    "ds_read_b64_tr_b16 a[252:253], v8 offset:0\n"
    "ds_read_b64_tr_b16 a[254:255], v8 offset:768\n"
    "ds_read_b64_tr_b16 v[188:189], v8 offset:128\n"
    "ds_read_b64_tr_b16 v[190:191], v8 offset:896\n"
    "ds_read_b64_tr_b16 v[24:25], v8 offset:256\n"
    "ds_read_b64_tr_b16 v[26:27], v8 offset:1024\n"
    "v_add_u32 v9, 98304, v4\n"
    "ds_read_b64_tr_b16 v[124:125], v9 offset:0\n"
    "ds_read_b64_tr_b16 v[126:127], v9 offset:768\n"
    "ds_read_b64_tr_b16 v[204:205], v9 offset:128\n"
    "ds_read_b64_tr_b16 v[206:207], v9 offset:896\n"
    "ds_read_b64_tr_b16 v[40:41], v9 offset:256\n"
    "ds_read_b64_tr_b16 v[42:43], v9 offset:1024\n"
    "v_add_u32 v10, 98304, v5\n"
    "ds_read_b64_tr_b16 v[140:141], v10 offset:0\n"
    "ds_read_b64_tr_b16 v[142:143], v10 offset:768\n"
    "ds_read_b64_tr_b16 v[220:221], v10 offset:128\n"
    "ds_read_b64_tr_b16 v[222:223], v10 offset:896\n"
    "ds_read_b64_tr_b16 v[56:57], v10 offset:256\n"
    "ds_read_b64_tr_b16 v[58:59], v10 offset:1024\n"
    "v_add_u32 v11, 98304, v6\n"
    "ds_read_b64_tr_b16 v[156:157], v11 offset:0\n"
    "ds_read_b64_tr_b16 v[158:159], v11 offset:768\n"
    "ds_read_b64_tr_b16 v[236:237], v11 offset:128\n"
    "ds_read_b64_tr_b16 v[238:239], v11 offset:896\n"
    "ds_read_b64_tr_b16 v[72:73], v11 offset:256\n"
    "ds_read_b64_tr_b16 v[74:75], v11 offset:1024\n"
    "s_waitcnt lgkmcnt(0)\n"
    "s_barrier\n"
    "s_waitcnt vmcnt(0)\n"
    "s_barrier\n"
    "v_add_u32 v8, 0, v7\n"
    "ds_read_b64_tr_b16 v[160:161], v8 offset:0\n"
    "ds_read_b64_tr_b16 v[162:163], v8 offset:768\n"
    "ds_read_b64_tr_b16 v[240:241], v8 offset:128\n"
    "ds_read_b64_tr_b16 v[242:243], v8 offset:896\n"
    "ds_read_b64_tr_b16 v[76:77], v8 offset:256\n"
    "ds_read_b64_tr_b16 v[78:79], v8 offset:1024\n"
    "v_add_u32 v9, 12288, v7\n"
    "ds_read_b64_tr_b16 v[164:165], v9 offset:0\n"
    "ds_read_b64_tr_b16 v[166:167], v9 offset:768\n"
    "ds_read_b64_tr_b16 v[244:245], v9 offset:128\n"
    "ds_read_b64_tr_b16 v[246:247], v9 offset:896\n"
    "ds_read_b64_tr_b16 v[80:81], v9 offset:256\n"
    "ds_read_b64_tr_b16 v[82:83], v9 offset:1024\n"
    "v_add_u32 v10, 24576, v7\n"
    "ds_read_b64_tr_b16 v[168:169], v10 offset:0\n"
    "ds_read_b64_tr_b16 v[170:171], v10 offset:768\n"
    "ds_read_b64_tr_b16 v[248:249], v10 offset:128\n"
    "ds_read_b64_tr_b16 v[250:251], v10 offset:896\n"
    "ds_read_b64_tr_b16 v[84:85], v10 offset:256\n"
    "ds_read_b64_tr_b16 v[86:87], v10 offset:1024\n"
    "v_add_u32 v11, 36864, v7\n"
    "ds_read_b64_tr_b16 v[172:173], v11 offset:0\n"
    "ds_read_b64_tr_b16 v[174:175], v11 offset:768\n"
    "ds_read_b64_tr_b16 v[252:253], v11 offset:128\n"
    "ds_read_b64_tr_b16 v[254:255], v11 offset:896\n"
    "ds_read_b64_tr_b16 v[88:89], v11 offset:256\n"
    "ds_read_b64_tr_b16 v[90:91], v11 offset:1024\n"
    "s_waitcnt lgkmcnt(0)\n"
    "s_barrier\n"
    "ds_write_b128 v97, v[12:15] offset:0\n"
    "ds_write_b128 v97, v[16:19] offset:1024\n"
    "ds_write_b128 v97, v[20:23] offset:2048\n"
    "ds_write_b128 v97, v[24:27] offset:3072\n"
    "ds_write_b128 v97, v[28:31] offset:4096\n"
    "ds_write_b128 v97, v[32:35] offset:5120\n"
    "ds_write_b128 v97, v[36:39] offset:6144\n"
    "ds_write_b128 v97, v[40:43] offset:7168\n"
    "ds_write_b128 v97, v[44:47] offset:8192\n"
    "ds_write_b128 v97, v[48:51] offset:9216\n"
    "ds_write_b128 v97, v[52:55] offset:10240\n"
    "ds_write_b128 v97, v[56:59] offset:11264\n"
    "ds_write_b128 v97, v[60:63] offset:12288\n"
    "ds_write_b128 v97, v[64:67] offset:13312\n"
    "ds_write_b128 v97, v[68:71] offset:14336\n"
    "ds_write_b128 v97, v[72:75] offset:15360\n"
    "ds_write_b128 v97, v[76:79] offset:16384\n"
    "ds_write_b128 v97, v[80:83] offset:17408\n"
    "ds_write_b128 v97, v[84:87] offset:18432\n"
    "ds_write_b128 v97, v[88:91] offset:19456\n"
    "s_mov_b32 s38, -2\n"
    "s_mov_b32 s54, 1037384597\n"
    "s_mov_b32 s55, 1037384597\n"
    "s_mov_b32 s56, 16711935\n"
    "s_mov_b32 s57, 16711935\n"
    "s_mov_b32 s58, 0\n"
    "s_mov_b32 s59, 16711935\n"
    "s_mov_b32 s60, 0\n"
    "s_mov_b32 s61, -1\n"
    "s_mov_b32 s62, 67044351\n"
    "s_mov_b32 s63, 67044351\n"
    "v_mov_b32 v8, v107\n"
    "v_mov_b32 v9, v107\n"
    "v_mov_b32 v10, 136960\n"
    "ds_write_b64 v10, v[8:9] offset:0\n"
    "ds_write_b64 v10, v[8:9] offset:8\n"
    "ds_write_b64 v10, v[8:9] offset:1808\n"
    "ds_write_b64 v10, v[8:9] offset:1816\n"
    "ds_write_b64 v10, v[8:9] offset:3616\n"
    "ds_write_b64 v10, v[8:9] offset:3624\n"
    "ds_write_b64 v10, v[8:9] offset:5424\n"
    "ds_write_b64 v10, v[8:9] offset:5432\n"
    "s_lshl_b32 s51, s20, 11\n"
    "v_lshrrev_b32 v4, 2, v96\n"
    "v_and_b32 v6, 12, v4\n"
    "v_lshlrev_b32 v5, 1, v96\n"
    "v_and_b32 v5, 0x380, v5\n"
    "v_min_u32 v5, 0x200, v5\n"
    "v_add_u32 v5, v5, v6\n"
    "s_lshr_b32 s52, s21, 12\n"
    ".Lqk_po2_%=:\n"
    "buffer_load_dword v8, v4, s[0:3], s28 offen sc1\n"
    "s_waitcnt vmcnt(0)\n"
    "v_cmp_gt_i32 vcc, 1, v8\n"
    "s_nop 0\n"
    "s_cbranch_vccz .Lqk_pd3_%=\n"
    "s_add_u32 s32, s32, 1\n"
    "s_cmp_lt_u32 s32, 0x40000\n"
    "s_cbranch_scc1 .Lqk_sp4_%=\n"
    "v_mov_b32 v8, 20\n"
    "s_mov_b32 s36, 33728\n"
    "buffer_store_dword v8, off, s[0:3], s36 sc1\n"
    "s_waitcnt vmcnt(0)\n"
    "s_branch .Lqk_pd3_%=\n"
    ".Lqk_sp4_%=:\n"
    "s_sleep 4\n"
    "s_branch .Lqk_po2_%=\n"
    ".Lqk_pd3_%=:\n"
    ".Lqk_po5_%=:\n"
    "buffer_load_dword v8, v5, s[0:3], s29 offen sc1\n"
    "s_waitcnt vmcnt(0)\n"
    "v_cmp_gt_i32 vcc, s52, v8\n"
    "s_nop 0\n"
    "s_cbranch_vccz .Lqk_pd6_%=\n"
    "s_add_u32 s32, s32, 1\n"
    "s_cmp_lt_u32 s32, 0x40000\n"
    "s_cbranch_scc1 .Lqk_sp7_%=\n"
    "v_mov_b32 v8, 21\n"
    "s_mov_b32 s36, 33728\n"
    "buffer_store_dword v8, off, s[0:3], s36 sc1\n"
    "s_waitcnt vmcnt(0)\n"
    "s_branch .Lqk_pd6_%=\n"
    ".Lqk_sp7_%=:\n"
    "s_sleep 4\n"
    "s_branch .Lqk_po5_%=\n"
    ".Lqk_pd6_%=:\n"
    "s_add_u32 s45, s24, s21\n"
    "s_add_u32 m0, s51, 0\n"
    "s_nop 0\n"
    "buffer_load_dwordx4 v96, s[0:3], s45 offen sc1 lds\n"
    "buffer_load_dwordx4 v96, s[0:3], s45 offen offset:1024 sc1 lds\n"
    "s_add_u32 m0, s51, 11264\n"
    "s_add_u32 s45, s45, 0x4000\n"
    "buffer_load_dwordx4 v96, s[0:3], s45 offen sc1 lds\n"
    "buffer_load_dwordx4 v96, s[0:3], s45 offen offset:1024 sc1 lds\n"
    "s_add_u32 m0, s51, 22528\n"
    "s_add_u32 s45, s45, 0x4000\n"
    "buffer_load_dwordx4 v96, s[0:3], s45 offen sc1 lds\n"
    "buffer_load_dwordx4 v96, s[0:3], s45 offen offset:1024 sc1 lds\n"
    "s_add_u32 m0, s51, 33792\n"
    "s_add_u32 s45, s45, 0x4000\n"
    "buffer_load_dwordx4 v96, s[0:3], s45 offen sc1 lds\n"
    "buffer_load_dwordx4 v96, s[0:3], s45 offen offset:1024 sc1 lds\n"
    "s_add_u32 m0, s51, 45056\n"
    "s_add_u32 s45, s45, 0x4000\n"
    "buffer_load_dwordx4 v96, s[0:3], s45 offen sc1 lds\n"
    "buffer_load_dwordx4 v96, s[0:3], s45 offen offset:1024 sc1 lds\n"
    ".Lqk_po8_%=:\n"
    "buffer_load_dword v8, v6, s[0:3], s30 offen sc1\n"
    "s_waitcnt vmcnt(0)\n"
    "v_cmp_gt_i32 vcc, 2, v8\n"
    "s_nop 0\n"
    "s_cbranch_vccz .Lqk_pd9_%=\n"
    "s_add_u32 s32, s32, 1\n"
    "s_cmp_lt_u32 s32, 0x40000\n"
    "s_cbranch_scc1 .Lqk_sp10_%=\n"
    "v_mov_b32 v8, 22\n"
    "s_mov_b32 s36, 33728\n"
    "buffer_store_dword v8, off, s[0:3], s36 sc1\n"
    "s_waitcnt vmcnt(0)\n"
    "s_branch .Lqk_pd9_%=\n"
    ".Lqk_sp10_%=:\n"
    "s_sleep 4\n"
    "s_branch .Lqk_po8_%=\n"
    ".Lqk_pd9_%=:\n"
    "s_memrealtime s[34:35]\n"
    "s_waitcnt lgkmcnt(0)\n"
    "s_sub_u32 s49, s34, 912\n"
    "s_mov_b32 s38, -1\n"
    "s_mov_b32 s39, -5\n"
    "s_mov_b32 s42, 2130706432\n"
    "s_mov_b32 s41, 4194304\n"
    "s_mov_b32 s47, s27\n"
    "s_add_u32 s46, s25, s21\n"
    "s_branch .Lqk_bulk_%=\n"
    ".Lqk_take_%=:\n"
    "s_waitcnt vmcnt(2) lgkmcnt(0)\n"
    "ds_write_b128 v92, v[56:59] offset:8192\n"
    "v_smfmac_f32_16x16x64_bf16 v[8:11], v[56:59], v[32:39], v98\n"
    "v_smfmac_f32_16x16x64_bf16 v[12:15], v[56:59], a[160:167], v98\n"
    "s_waitcnt vmcnt(1)\n"
    "ds_write_b128 v92, v[60:63] offset:9216\n"
    "v_max3_u32 v108, v56, v58, v60\n"
    "v_smfmac_f32_16x16x64_bf16 v[0:3], v[56:59], v[176:183], v98\n"
    "s_waitcnt vmcnt(0)\n"
    "ds_write_b128 v92, v[64:67] offset:10240\n"
    "v_max3_u32 v108, v108, v62, v64\n"
    "v_smfmac_f32_16x16x64_bf16 v[4:7], v[56:59], a[80:87], v98\n"
    "v_max_u32 v108, v108, v66\n"
    "v_cmp_eq_u32 vcc, -1, v108\n"
    "v_smfmac_f32_16x16x64_bf16 v[16:19], v[56:59], a[240:247], v98\n"
    "s_cbranch_vccnz .Lqk_retry_%=\n"
    ".Lqk_go_%=:\n"
    "v_smfmac_f32_16x16x64_bf16 v[20:23], v[56:59], a[0:7], v98\n"
    "s_waitcnt lgkmcnt(0)\n"
    "s_barrier\n"
    "ds_read_b128 v[24:27], v93 offset:8192\n"
    "s_memrealtime s[64:65]\n"
    "ds_read_b128 v[28:31], v93 offset:9216\n"
    "v_smfmac_f32_16x16x64_bf16 v[8:11], v[60:63], v[40:47], v98\n"
    "ds_read_b128 v[32:35], v97 offset:6144\n"
    "s_add_u32 m0, s51, 0\n"
    "v_smfmac_f32_16x16x64_bf16 v[12:15], v[60:63], a[168:175], v98\n"
    "ds_read_b128 v[36:39], v97 offset:7168\n"
    "buffer_load_dwordx4 v96, s[0:3], s45 offen sc1 lds\n"
    "v_smfmac_f32_16x16x64_bf16 v[0:3], v[60:63], v[184:191], v98\n"
    "ds_read_b128 v[64:67], v94 offset:8192\n"
    "v_smfmac_f32_16x16x64_bf16 v[4:7], v[60:63], a[88:95], v98\n"
    "ds_read_b128 v[40:43], v97 offset:8192\n"
    "v_smfmac_f32_16x16x64_bf16 v[16:19], v[60:63], a[248:255], v98\n"
    "ds_read_b128 v[44:47], v97 offset:9216\n"
    "v_smfmac_f32_16x16x64_bf16 v[20:23], v[60:63], a[8:15], v98\n"
    "ds_read_b128 v[56:59], v94 offset:9216\n"
    "s_waitcnt lgkmcnt(7)\n"
    "v_smfmac_f32_16x16x64_bf16 v[8:11], v[24:27], v[48:55], v98\n"
    "ds_read_b128 v[48:51], v97 offset:10240\n"
    "buffer_load_dwordx4 v96, s[0:3], s45 offen offset:1024 sc1 lds\n"
    "v_smfmac_f32_16x16x64_bf16 v[12:15], v[24:27], a[176:183], v98\n"
    "ds_read_b128 v[52:55], v97 offset:11264\n"
    "s_add_u32 m0, s51, 11264\n"
    "v_smfmac_f32_16x16x64_bf16 v[0:3], v[24:27], v[192:199], v98\n"
    "s_add_u32 s45, s45, 0x4000\n"
    "v_smfmac_f32_16x16x64_bf16 v[4:7], v[24:27], a[96:103], v98\n"
    "v_smfmac_f32_16x16x64_bf16 v[16:19], v[24:27], v[112:119], v98\n"
    "buffer_load_dwordx4 v96, s[0:3], s45 offen sc1 lds\n"
    "v_smfmac_f32_16x16x64_bf16 v[20:23], v[24:27], a[16:23], v98\n"
    "s_waitcnt lgkmcnt(0)\n"
    "v_smfmac_f32_16x16x64_bf16 v[8:11], v[28:31], v[32:39], v98\n"
    "ds_read_b128 v[32:35], v97 offset:12288\n"
    "s_sub_u32 s36, s64, s49\n"
    "v_smfmac_f32_16x16x64_bf16 v[12:15], v[28:31], a[184:191], v98\n"
    "ds_read_b128 v[36:39], v97 offset:13312\n"
    "s_add_u32 s37, s64, 24\n"
    "v_smfmac_f32_16x16x64_bf16 v[0:3], v[28:31], v[200:207], v98\n"
    "ds_read_b128 v[60:63], v95 offset:8192\n"
    "buffer_load_dwordx4 v96, s[0:3], s45 offen offset:1024 sc1 lds\n"
    "v_smfmac_f32_16x16x64_bf16 v[4:7], v[28:31], a[104:111], v98\n"
    "ds_read_b128 v[24:27], v95 offset:9216\n"
    "s_add_u32 m0, s51, 22528\n"
    "v_smfmac_f32_16x16x64_bf16 v[16:19], v[28:31], v[120:127], v98\n"
    "s_add_u32 s45, s45, 0x4000\n"
    "s_cmp_gt_i32 s36, 0\n"
    "s_cselect_b32 s49, s37, s49\n"
    "v_smfmac_f32_16x16x64_bf16 v[20:23], v[28:31], a[24:31], v98\n"
    "ds_read_b128 v[28:31], v96 offset:10240\n"
    "v_mov_b32 v108, s49\n"
    "v_smfmac_f32_16x16x64_bf16 v[8:11], v[64:67], v[40:47], v98\n"
    "ds_read_b128 v[40:43], v97 offset:14336\n"
    "buffer_load_dwordx4 v96, s[0:3], s45 offen sc1 lds\n"
    "v_smfmac_f32_16x16x64_bf16 v[12:15], v[64:67], a[192:199], v98\n"
    "ds_read_b128 v[44:47], v97 offset:15360\n"
    "v_mov_b32 v109, s39\n"
    "v_smfmac_f32_16x16x64_bf16 v[0:3], v[64:67], v[208:215], v98\n"
    "v_lshlrev_b32 v110, 10, v96\n"
    "v_smfmac_f32_16x16x64_bf16 v[4:7], v[64:67], a[112:119], v98\n"
    "v_smfmac_f32_16x16x64_bf16 v[16:19], v[64:67], v[128:135], v98\n"
    "buffer_load_dwordx4 v96, s[0:3], s45 offen offset:1024 sc1 lds\n"
    "s_add_u32 m0, s51, 33792\n"
    "v_smfmac_f32_16x16x64_bf16 v[20:23], v[64:67], a[32:39], v98\n"
    "ds_read_b128 v[64:67], v96 offset:21504\n"
    "s_add_u32 s45, s45, 0x4000\n"
    "v_smfmac_f32_16x16x64_bf16 v[8:11], v[56:59], v[48:55], v98\n"
    "ds_read_b128 v[48:51], v97 offset:16384\n"
    "v_smfmac_f32_16x16x64_bf16 v[12:15], v[56:59], a[200:207], v98\n"
    "ds_read_b128 v[52:55], v97 offset:17408\n"
    "s_waitcnt lgkmcnt(3)\n"
    "v_smfmac_f32_16x16x64_bf16 v[8:11], v[60:63], v[32:39], v98\n"
    "ds_read_b128 v[32:35], v97 offset:18432\n"
    "buffer_load_dwordx4 v96, s[0:3], s45 offen sc1 lds\n"
    "v_smfmac_f32_16x16x64_bf16 v[12:15], v[60:63], a[208:215], v98\n"
    "ds_read_b128 v[36:39], v97 offset:19456\n"
    "v_smfmac_f32_16x16x64_bf16 v[8:11], v[24:27], v[40:47], v98\n"
    "v_smfmac_f32_16x16x64_bf16 v[12:15], v[24:27], a[216:223], v98\n"
    "v_smfmac_f32_16x16x64_bf16 v[0:3], v[56:59], v[216:223], v98\n"
    "buffer_load_dwordx4 v96, s[0:3], s45 offen offset:1024 sc1 lds\n"
    "s_add_u32 m0, s51, 45056\n"
    "v_smfmac_f32_16x16x64_bf16 v[4:7], v[56:59], a[120:127], v98\n"
    "s_add_u32 s45, s45, 0x4000\n"
    "v_smfmac_f32_16x16x64_bf16 v[0:3], v[60:63], v[224:231], v98\n"
    "v_smfmac_f32_16x16x64_bf16 v[4:7], v[60:63], a[128:135], v98\n"
    "s_waitcnt lgkmcnt(0)\n"
    "v_smfmac_f32_16x16x64_bf16 v[8:11], v[28:31], v[48:55], v98\n"
    "buffer_load_dwordx4 v96, s[0:3], s45 offen sc1 lds\n"
    "v_smfmac_f32_16x16x64_bf16 v[12:15], v[28:31], a[224:231], v98\n"
    "v_smfmac_f32_16x16x64_bf16 v[8:11], v[64:67], v[32:39], v98\n"
    "v_smfmac_f32_16x16x64_bf16 v[12:15], v[64:67], a[232:239], v98\n"
    "v_smfmac_f32_16x16x64_bf16 v[0:3], v[24:27], v[232:239], v98\n"
    "buffer_load_dwordx4 v96, s[0:3], s45 offen offset:1024 sc1 lds\n"
    "v_smfmac_f32_16x16x64_bf16 v[4:7], v[24:27], a[136:143], v98\n"
    "v_smfmac_f32_16x16x64_bf16 v[0:3], v[28:31], v[240:247], v98\n"
    "v_smfmac_f32_16x16x64_bf16 v[4:7], v[28:31], a[144:151], v98\n"
    "v_smfmac_f32_16x16x64_bf16 v[0:3], v[64:67], v[248:255], v98\n"
    "s_mov_b64 exec, 15\n"
    "buffer_store_dwordx2 v[108:109], v110, s[0:3], s48 offen sc1\n"
    "s_mov_b64 exec, -1\n"
    "v_smfmac_f32_16x16x64_bf16 v[4:7], v[64:67], a[152:159], v98\n"
    "v_smfmac_f32_16x16x64_bf16 v[16:19], v[56:59], v[136:143], v98\n"
    "v_add_f32 v8, v8, v10\n"
    "v_add_f32 v9, v9, v11\n"
    "v_smfmac_f32_16x16x64_bf16 v[20:23], v[56:59], a[40:47], v98\n"
    "v_add_f32 v12, v12, v14\n"
    "v_add_f32 v13, v13, v15\n"
    "v_smfmac_f32_16x16x64_bf16 v[16:19], v[60:63], v[144:151], v98\n"
    "v_cvt_pk_bf16_f32 v8, v8, v9\n"
    "v_cvt_pk_bf16_f32 v12, v12, v13\n"
    "v_smfmac_f32_16x16x64_bf16 v[20:23], v[60:63], a[48:55], v98\n"
    "ds_write2_b32 v99, v8, v12 offset0:8 offset1:12\n"
    "ds_read_b64_tr_b16 v[14:15], v100 offset:32\n"
    "v_smfmac_f32_16x16x64_bf16 v[16:19], v[24:27], v[152:159], v98\n"
    "v_add_f32 v0, v0, v2\n"
    "v_add_f32 v1, v1, v3\n"
    "v_smfmac_f32_16x16x64_bf16 v[20:23], v[24:27], a[56:63], v98\n"
    "v_add_f32 v4, v4, v6\n"
    "v_add_f32 v5, v5, v7\n"
    "v_smfmac_f32_16x16x64_bf16 v[16:19], v[28:31], v[160:167], v98\n"
    "v_cvt_pk_bf16_f32 v0, v0, v1\n"
    "v_cvt_pk_bf16_f32 v4, v4, v5\n"
    "v_smfmac_f32_16x16x64_bf16 v[20:23], v[28:31], a[64:71], v98\n"
    "ds_write2_b32 v99, v0, v4 offset0:0 offset1:4\n"
    "ds_read_b64_tr_b16 v[12:13], v100 offset:0\n"
    "v_smfmac_f32_16x16x64_bf16 v[16:19], v[64:67], v[168:175], v98\n"
    "v_smfmac_f32_16x16x64_bf16 v[20:23], v[64:67], a[72:79], v98\n"
    "s_nop 6\n"
    "v_pk_add_f32 v[16:17], v[16:17], v[18:19]\n"
    "v_pk_add_f32 v[20:21], v[20:21], v[22:23]\n"
    "v_cvt_pk_bf16_f32 v16, v16, v17\n"
    "v_cvt_pk_bf16_f32 v20, v20, v21\n"
    "ds_write2_b32 v99, v16, v20 offset0:16 offset1:20\n"
    "ds_read_b64_tr_b16 v[4:5], v100 offset:64\n"
    "s_waitcnt lgkmcnt(2)\n"
    "v_mfma_f32_16x16x32_bf16 v[8:11], v[68:71], v[12:15], 0\n"
    "v_mfma_f32_16x16x32_bf16 v[0:3], v[72:75], v[12:15], 0\n"
    "v_mfma_f32_16x16x32_bf16 v[108:111], v[12:15], v[12:15], 0\n"
    "v_mfma_f32_16x16x32_bf16 v[32:35], v[12:15], v[80:83], 0\n"
    "v_mfma_f32_16x16x32_bf16 v[36:39], v[12:15], v[84:87], 0\n"
    "s_waitcnt lgkmcnt(0)\n"
    "s_nop 0\n"
    "v_mfma_f32_16x16x16_bf16 v[8:11], v[76:77], v[4:5], v[8:11]\n"
    "v_mfma_f32_16x16x16_bf16 v[0:3], v[78:79], v[4:5], v[0:3]\n"
    "v_mfma_f32_16x16x16_bf16 v[108:111], v[4:5], v[4:5], v[108:111]\n"
    "v_mfma_f32_16x16x16_bf16 v[32:35], v[4:5], v[88:89], v[32:35]\n"
    "v_mfma_f32_16x16x16_bf16 v[36:39], v[4:5], v[90:91], v[36:39]\n"
    "s_mov_b64 exec, s[56:57]\n"
    "s_nop 2\n"
    "ds_write_b128 v101, v[8:11]\n"
    "ds_write_b128 v101, v[0:3] offset:64\n"
    "s_mov_b64 exec, s[58:59]\n"
    "ds_write_b128 v101, v[108:111] offset:96\n"
    "s_mov_b64 exec, -1\n"
    "s_waitcnt lgkmcnt(0)\n"
    "s_barrier\n"
    "ds_read_b128 v[40:43], v102 offset:0\n"
    "ds_read_b128 v[44:47], v102 offset:1808\n"
    "ds_read_b128 v[48:51], v102 offset:3616\n"
    "ds_read_b128 v[52:55], v102 offset:5424\n"
    "s_mov_b64 exec, s[60:61]\n"
    "ds_write_b128 v103, v[36:39] offset:96\n"
    "ds_write_b128 v103, v[32:35] offset:192\n"
    "s_mov_b64 exec, -1\n"
    "s_waitcnt lgkmcnt(4)\n"
    "v_pk_add_f32 v[40:41], v[40:41], v[44:45]\n"
    "v_pk_add_f32 v[42:43], v[42:43], v[46:47]\n"
    "s_waitcnt lgkmcnt(2)\n"
    "v_pk_add_f32 v[48:49], v[48:49], v[52:53]\n"
    "v_pk_add_f32 v[50:51], v[50:51], v[54:55]\n"
    "v_pk_add_f32 v[40:41], v[40:41], v[48:49]\n"
    "v_pk_add_f32 v[42:43], v[42:43], v[50:51]\n"
    "v_max3_f32 v16, v40, v41, v42\n"
    "v_max_f32 v16, v16, v43\n"
    "s_nop 1\n"
    "v_max_f32_dpp v16, v16, v16 row_ror:8 row_mask:0xf bank_mask:0xf\n"
    "s_nop 1\n"
    "v_max_f32_dpp v16, v16, v16 row_ror:4 row_mask:0xf bank_mask:0xf\n"
    "s_nop 1\n"
    "v_max_f32_dpp v16, v16, v16 row_ror:2 row_mask:0xf bank_mask:0xf\n"
    "s_nop 1\n"
    "v_max_f32_dpp v16, v16, v16 row_ror:1 row_mask:0xf bank_mask:0xf\n"
    "v_mul_f32 v16, s54, v16\n"
    "v_pk_fma_f32 v[40:41], v[40:41], s[54:55], v[16:17] op_sel_hi:[1,1,0] neg_lo:[0,0,1] neg_hi:[0,0,1]\n"
    "v_pk_fma_f32 v[42:43], v[42:43], s[54:55], v[16:17] op_sel_hi:[1,1,0] neg_lo:[0,0,1] neg_hi:[0,0,1]\n"
    "v_exp_f32 v40, v40\n"
    "v_exp_f32 v41, v41\n"
    "v_exp_f32 v42, v42\n"
    "v_exp_f32 v43, v43\n"
    "s_nop 0\n"
    "v_pk_add_f32 v[16:17], v[40:41], v[42:43]\n"
    "v_add_f32 v16, v16, v17\n"
    "s_nop 1\n"
    "v_add_f32_dpp v16, v16, v16 row_ror:8 row_mask:0xf bank_mask:0xf\n"
    "s_nop 1\n"
    "v_add_f32_dpp v16, v16, v16 row_ror:4 row_mask:0xf bank_mask:0xf\n"
    "s_nop 1\n"
    "v_add_f32_dpp v16, v16, v16 row_ror:2 row_mask:0xf bank_mask:0xf\n"
    "s_nop 1\n"
    "v_add_f32_dpp v16, v16, v16 row_ror:1 row_mask:0xf bank_mask:0xf\n"
    "v_rcp_f32 v16, v16\n"
    "s_nop 0\n"
    "v_pk_mul_f32 v[40:41], v[40:41], v[16:17] op_sel_hi:[1,0]\n"
    "v_pk_mul_f32 v[42:43], v[42:43], v[16:17] op_sel_hi:[1,0]\n"
    "v_cvt_pk_bf16_f32 v40, v40, v41\n"
    "v_cvt_pk_bf16_f32 v41, v42, v43\n"
    "v_min_u32 v40, -2, v40\n"
    "buffer_store_dwordx2 v[40:41], v105, s[0:3], s40 offen sc1\n"
    ".Lqk_bulk_%=:\n"
    "s_waitcnt vmcnt(2) lgkmcnt(0)\n"
    "s_barrier\n"
    "ds_read_b128 v[32:35], v92 offset:0\n"
    "ds_read_b128 v[36:39], v92 offset:1024\n"
    "ds_read_b128 v[56:59], v104 offset:0\n"
    "ds_read_b128 v[60:63], v104 offset:4352\n"
    "ds_read_b128 v[64:67], v104 offset:8704\n"
    "ds_read_b128 v[68:71], v104 offset:13056\n"
    "ds_read_b128 v[40:43], v92 offset:2048\n"
    "ds_read_b128 v[44:47], v92 offset:3072\n"
    "ds_read_b128 v[48:51], v92 offset:4096\n"
    "ds_read_b128 v[52:55], v92 offset:5120\n"
    "s_waitcnt lgkmcnt(8)\n"
    "v_mfma_f32_16x16x32_bf16 v[0:3], a[80:83], v[32:35], 0\n"
    "s_waitcnt lgkmcnt(6)\n"
    "v_add_f32 v56, v56, v60\n"
    "v_mfma_f32_16x16x32_bf16 v[4:7], a[80:83], v[36:39], 0\n"
    "v_add_f32 v57, v57, v61\n"
    "v_add_f32 v58, v58, v62\n"
    "v_mfma_f32_16x16x32_bf16 v[8:11], a[160:163], v[32:35], 0\n"
    "v_add_f32 v59, v59, v63\n"
    "s_waitcnt lgkmcnt(4)\n"
    "v_mfma_f32_16x16x32_bf16 v[12:15], a[160:163], v[36:39], 0\n"
    "v_add_f32 v64, v64, v68\n"
    "v_add_f32 v65, v65, v69\n"
    "v_mfma_f32_16x16x32_bf16 v[16:19], a[0:3], v[32:35], 0\n"
    "v_add_f32 v66, v66, v70\n"
    "v_add_f32 v67, v67, v71\n"
    "v_mfma_f32_16x16x32_bf16 v[20:23], a[0:3], v[36:39], 0\n"
    "v_add_f32 v56, v56, v64\n"
    "v_add_f32 v57, v57, v65\n"
    "v_mfma_f32_16x16x32_bf16 v[24:27], a[240:243], v[32:35], 0\n"
    "ds_read_b128 v[32:35], v92 offset:6144\n"
    "v_add_f32 v58, v58, v66\n"
    "v_mfma_f32_16x16x32_bf16 v[28:31], a[240:243], v[36:39], 0\n"
    "ds_read_b128 v[36:39], v92 offset:7168\n"
    "s_waitcnt lgkmcnt(4)\n"
    "v_mfma_f32_16x16x32_bf16 v[0:3], a[84:87], v[40:43], v[0:3]\n"
    "v_add_f32 v59, v59, v67\n"
    "s_barrier\n"
    "v_mfma_f32_16x16x32_bf16 v[4:7], a[84:87], v[44:47], v[4:7]\n"
    "v_cndmask_b32 v56, v107, v56, s[12:13]\n"
    "v_cndmask_b32 v57, v107, v57, s[14:15]\n"
    "v_mfma_f32_16x16x32_bf16 v[8:11], a[164:167], v[40:43], v[8:11]\n"
    "v_cndmask_b32 v58, v107, v58, s[16:17]\n"
    "v_cndmask_b32 v59, v107, v59, s[18:19]\n"
    "v_mfma_f32_16x16x32_bf16 v[12:15], a[164:167], v[44:47], v[12:15]\n"
    "v_max3_f32 v72, v56, v57, v58\n"
    "v_max_f32 v72, v72, v59\n"
    "v_mfma_f32_16x16x32_bf16 v[16:19], a[4:7], v[40:43], v[16:19]\n"
    "v_cndmask_b32 v74, v107, v72, s[62:63]\n"
    "v_cndmask_b32 v75, v72, v107, s[62:63]\n"
    "v_mfma_f32_16x16x32_bf16 v[20:23], a[4:7], v[44:47], v[20:23]\n"
    "v_max_f32_dpp v74, v74, v74 row_ror:8 row_mask:0xf bank_mask:0xf\n"
    "v_max_f32_dpp v75, v75, v75 row_ror:8 row_mask:0xf bank_mask:0xf\n"
    "v_mfma_f32_16x16x32_bf16 v[24:27], a[244:247], v[40:43], v[24:27]\n"
    "ds_read_b128 v[40:43], v93 offset:0\n"
    "v_max_f32_dpp v74, v74, v74 row_ror:4 row_mask:0xf bank_mask:0xf\n"
    "v_mfma_f32_16x16x32_bf16 v[28:31], a[244:247], v[44:47], v[28:31]\n"
    "ds_read_b128 v[44:47], v93 offset:1024\n"
    "s_waitcnt lgkmcnt(4)\n"
    "v_mfma_f32_16x16x32_bf16 v[0:3], a[88:91], v[48:51], v[0:3]\n"
    "v_max_f32_dpp v75, v75, v75 row_ror:4 row_mask:0xf bank_mask:0xf\n"
    "v_max_f32_dpp v74, v74, v74 row_ror:2 row_mask:0xf bank_mask:0xf\n"
    "v_mfma_f32_16x16x32_bf16 v[4:7], a[88:91], v[52:55], v[4:7]\n"
    "v_max_f32_dpp v75, v75, v75 row_ror:2 row_mask:0xf bank_mask:0xf\n"
    "v_max_f32_dpp v74, v74, v74 row_ror:1 row_mask:0xf bank_mask:0xf\n"
    "v_mfma_f32_16x16x32_bf16 v[8:11], a[168:171], v[48:51], v[8:11]\n"
    "v_max_f32_dpp v75, v75, v75 row_ror:1 row_mask:0xf bank_mask:0xf\n"
    "v_cndmask_b32 v72, v75, v74, s[62:63]\n"
    "v_mfma_f32_16x16x32_bf16 v[12:15], a[168:171], v[52:55], v[12:15]\n"
    "v_mul_f32 v72, s54, v72\n"
    "v_fma_f32 v56, v56, s54, -v72\n"
    "v_mfma_f32_16x16x32_bf16 v[16:19], a[8:11], v[48:51], v[16:19]\n"
    "v_fma_f32 v57, v57, s54, -v72\n"
    "v_fma_f32 v58, v58, s54, -v72\n"
    "v_mfma_f32_16x16x32_bf16 v[20:23], a[8:11], v[52:55], v[20:23]\n"
    "v_fma_f32 v59, v59, s54, -v72\n"
    "v_mfma_f32_16x16x32_bf16 v[24:27], a[248:251], v[48:51], v[24:27]\n"
    "ds_read_b128 v[48:51], v93 offset:2048\n"
    "v_mfma_f32_16x16x32_bf16 v[28:31], a[248:251], v[52:55], v[28:31]\n"
    "ds_read_b128 v[52:55], v93 offset:3072\n"
    "s_waitcnt lgkmcnt(4)\n"
    "v_mfma_f32_16x16x32_bf16 v[0:3], a[92:95], v[32:35], v[0:3]\n"
    "v_exp_f32 v56, v56\n"
    "v_mfma_f32_16x16x32_bf16 v[4:7], a[92:95], v[36:39], v[4:7]\n"
    "v_exp_f32 v57, v57\n"
    "v_mfma_f32_16x16x32_bf16 v[8:11], a[172:175], v[32:35], v[8:11]\n"
    "v_exp_f32 v58, v58\n"
    "v_mfma_f32_16x16x32_bf16 v[12:15], a[172:175], v[36:39], v[12:15]\n"
    "v_exp_f32 v59, v59\n"
    "v_mfma_f32_16x16x32_bf16 v[16:19], a[12:15], v[32:35], v[16:19]\n"
    "v_add_f32 v72, v56, v58\n"
    "v_add_f32 v73, v57, v59\n"
    "v_mfma_f32_16x16x32_bf16 v[20:23], a[12:15], v[36:39], v[20:23]\n"
    "v_add_f32 v72, v72, v73\n"
    "v_cndmask_b32 v74, 0, v72, s[62:63]\n"
    "v_mfma_f32_16x16x32_bf16 v[24:27], a[252:255], v[32:35], v[24:27]\n"
    "ds_read_b128 v[32:35], v93 offset:4096\n"
    "v_cndmask_b32 v75, v72, 0, s[62:63]\n"
    "v_mfma_f32_16x16x32_bf16 v[28:31], a[252:255], v[36:39], v[28:31]\n"
    "ds_read_b128 v[36:39], v93 offset:5120\n"
    "s_waitcnt lgkmcnt(4)\n"
    "v_mfma_f32_16x16x32_bf16 v[0:3], a[96:99], v[40:43], v[0:3]\n"
    "v_add_f32_dpp v74, v74, v74 row_ror:8 row_mask:0xf bank_mask:0xf\n"
    "v_add_f32_dpp v75, v75, v75 row_ror:8 row_mask:0xf bank_mask:0xf\n"
    "v_mfma_f32_16x16x32_bf16 v[4:7], a[96:99], v[44:47], v[4:7]\n"
    "v_add_f32_dpp v74, v74, v74 row_ror:4 row_mask:0xf bank_mask:0xf\n"
    "v_add_f32_dpp v75, v75, v75 row_ror:4 row_mask:0xf bank_mask:0xf\n"
    "v_mfma_f32_16x16x32_bf16 v[8:11], a[176:179], v[40:43], v[8:11]\n"
    "v_add_f32_dpp v74, v74, v74 row_ror:2 row_mask:0xf bank_mask:0xf\n"
    "v_add_f32_dpp v75, v75, v75 row_ror:2 row_mask:0xf bank_mask:0xf\n"
    "v_mfma_f32_16x16x32_bf16 v[12:15], a[176:179], v[44:47], v[12:15]\n"
    "v_add_f32_dpp v74, v74, v74 row_ror:1 row_mask:0xf bank_mask:0xf\n"
    "v_add_f32_dpp v75, v75, v75 row_ror:1 row_mask:0xf bank_mask:0xf\n"
    "v_mfma_f32_16x16x32_bf16 v[16:19], a[16:19], v[40:43], v[16:19]\n"
    "v_cndmask_b32 v72, v75, v74, s[62:63]\n"
    "v_mfma_f32_16x16x32_bf16 v[20:23], a[16:19], v[44:47], v[20:23]\n"
    "v_rcp_f32 v72, v72\n"
    "v_mfma_f32_16x16x32_bf16 v[24:27], v[112:115], v[40:43], v[24:27]\n"
    "ds_read_b128 v[40:43], v93 offset:6144\n"
    "v_mul_f32 v56, v56, v72\n"
    "v_mfma_f32_16x16x32_bf16 v[28:31], v[112:115], v[44:47], v[28:31]\n"
    "ds_read_b128 v[44:47], v93 offset:7168\n"
    "s_waitcnt lgkmcnt(4)\n"
    "v_mfma_f32_16x16x32_bf16 v[0:3], a[100:103], v[48:51], v[0:3]\n"
    "v_mul_f32 v57, v57, v72\n"
    "v_mul_f32 v58, v58, v72\n"
    "v_mfma_f32_16x16x32_bf16 v[4:7], a[100:103], v[52:55], v[4:7]\n"
    "v_mul_f32 v59, v59, v72\n"
    "v_cvt_pk_bf16_f32 v56, v56, v57\n"
    "v_mfma_f32_16x16x32_bf16 v[8:11], a[180:183], v[48:51], v[8:11]\n"
    "v_cvt_pk_bf16_f32 v57, v58, v59\n"
    "v_min_u32 v56, -2, v56\n"
    "v_mfma_f32_16x16x32_bf16 v[12:15], a[180:183], v[52:55], v[12:15]\n"
    "buffer_store_dwordx2 v[56:57], v106, s[0:3], s42 offen sc1\n"
    "v_mfma_f32_16x16x32_bf16 v[16:19], a[20:23], v[48:51], v[16:19]\n"
    "s_cmp_eq_u32 s38, 63\n"
    "s_cbranch_scc1 .Lqk_end_%=\n"
    "v_mfma_f32_16x16x32_bf16 v[20:23], a[20:23], v[52:55], v[20:23]\n"
    "v_mov_b64 v[72:73], -1\n"
    "v_mfma_f32_16x16x32_bf16 v[24:27], v[116:119], v[48:51], v[24:27]\n"
    "ds_read_b128 v[48:51], v94 offset:0\n"
    "v_mfma_f32_16x16x32_bf16 v[28:31], v[116:119], v[52:55], v[28:31]\n"
    "ds_read_b128 v[52:55], v94 offset:1024\n"
    "s_waitcnt lgkmcnt(4)\n"
    "v_mfma_f32_16x16x32_bf16 v[0:3], a[104:107], v[32:35], v[0:3]\n"
    "v_mfma_f32_16x16x32_bf16 v[4:7], a[104:107], v[36:39], v[4:7]\n"
    "v_mfma_f32_16x16x32_bf16 v[8:11], a[184:187], v[32:35], v[8:11]\n"
    "v_mfma_f32_16x16x32_bf16 v[12:15], a[184:187], v[36:39], v[12:15]\n"
    "v_mfma_f32_16x16x32_bf16 v[16:19], a[24:27], v[32:35], v[16:19]\n"
    "buffer_store_dwordx2 v[72:73], v105, s[0:3], s41 offen sc1\n"
    "v_mfma_f32_16x16x32_bf16 v[20:23], a[24:27], v[36:39], v[20:23]\n"
    "v_mfma_f32_16x16x32_bf16 v[24:27], v[120:123], v[32:35], v[24:27]\n"
    "ds_read_b128 v[32:35], v94 offset:2048\n"
    "v_mfma_f32_16x16x32_bf16 v[28:31], v[120:123], v[36:39], v[28:31]\n"
    "ds_read_b128 v[36:39], v94 offset:3072\n"
    "s_waitcnt lgkmcnt(4)\n"
    "v_mfma_f32_16x16x32_bf16 v[0:3], a[108:111], v[40:43], v[0:3]\n"
    "v_mfma_f32_16x16x32_bf16 v[4:7], a[108:111], v[44:47], v[4:7]\n"
    "v_mfma_f32_16x16x32_bf16 v[8:11], a[188:191], v[40:43], v[8:11]\n"
    "v_mfma_f32_16x16x32_bf16 v[12:15], a[188:191], v[44:47], v[12:15]\n"
    "v_mfma_f32_16x16x32_bf16 v[16:19], a[28:31], v[40:43], v[16:19]\n"
    "v_mfma_f32_16x16x32_bf16 v[20:23], a[28:31], v[44:47], v[20:23]\n"
    "buffer_store_dwordx2 v[72:73], v106, s[0:3], s41 offen sc1\n"
    "v_mfma_f32_16x16x32_bf16 v[24:27], v[124:127], v[40:43], v[24:27]\n"
    "ds_read_b128 v[40:43], v94 offset:4096\n"
    "v_mfma_f32_16x16x32_bf16 v[28:31], v[124:127], v[44:47], v[28:31]\n"
    "ds_read_b128 v[44:47], v94 offset:5120\n"
    "s_waitcnt lgkmcnt(4)\n"
    "v_mfma_f32_16x16x32_bf16 v[0:3], a[112:115], v[48:51], v[0:3]\n"
    "v_mfma_f32_16x16x32_bf16 v[4:7], a[112:115], v[52:55], v[4:7]\n"
    "v_mfma_f32_16x16x32_bf16 v[8:11], a[192:195], v[48:51], v[8:11]\n"
    "v_mfma_f32_16x16x32_bf16 v[12:15], a[192:195], v[52:55], v[12:15]\n"
    "v_mfma_f32_16x16x32_bf16 v[16:19], a[32:35], v[48:51], v[16:19]\n"
    "v_mfma_f32_16x16x32_bf16 v[20:23], a[32:35], v[52:55], v[20:23]\n"
    "v_mfma_f32_16x16x32_bf16 v[24:27], v[128:131], v[48:51], v[24:27]\n"
    "ds_read_b128 v[48:51], v94 offset:6144\n"
    "buffer_load_dwordx4 v[80:83], v96, s[0:3], s46 offen sc1\n"
    "v_mfma_f32_16x16x32_bf16 v[28:31], v[128:131], v[52:55], v[28:31]\n"
    "ds_read_b128 v[52:55], v94 offset:7168\n"
    "s_waitcnt lgkmcnt(4)\n"
    "v_mfma_f32_16x16x32_bf16 v[0:3], a[116:119], v[32:35], v[0:3]\n"
    "v_mfma_f32_16x16x32_bf16 v[4:7], a[116:119], v[36:39], v[4:7]\n"
    "v_mfma_f32_16x16x32_bf16 v[8:11], a[196:199], v[32:35], v[8:11]\n"
    "buffer_load_dwordx4 v[84:87], v96, s[0:3], s46 offen offset:1024 sc1\n"
    "v_mfma_f32_16x16x32_bf16 v[12:15], a[196:199], v[36:39], v[12:15]\n"
    "v_mfma_f32_16x16x32_bf16 v[16:19], a[36:39], v[32:35], v[16:19]\n"
    "v_mfma_f32_16x16x32_bf16 v[20:23], a[36:39], v[36:39], v[20:23]\n"
    "v_mfma_f32_16x16x32_bf16 v[24:27], v[132:135], v[32:35], v[24:27]\n"
    "ds_read_b128 v[32:35], v95 offset:0\n"
    "buffer_load_dwordx2 v[108:109], off, s[0:3], s47 sc1\n"
    "v_mfma_f32_16x16x32_bf16 v[28:31], v[132:135], v[36:39], v[28:31]\n"
    "ds_read_b128 v[36:39], v95 offset:1024\n"
    "s_waitcnt lgkmcnt(4)\n"
    "v_mfma_f32_16x16x32_bf16 v[0:3], a[120:123], v[40:43], v[0:3]\n"
    "s_add_u32 s38, s38, 1\n"
    "s_add_u32 s52, s38, 1\n"
    "s_add_u32 s40, s38, 0\n"
    "v_mfma_f32_16x16x32_bf16 v[4:7], a[120:123], v[44:47], v[4:7]\n"
    "s_and_b32 s40, s40, 3\n"
    "s_lshl_b32 s40, s40, 22\n"
    "s_add_u32 s41, s38, 2\n"
    "v_mfma_f32_16x16x32_bf16 v[8:11], a[200:203], v[40:43], v[8:11]\n"
    "s_and_b32 s41, s41, 3\n"
    "s_lshl_b32 s41, s41, 22\n"
    "s_and_b32 s47, s38, 63\n"
    "v_mfma_f32_16x16x32_bf16 v[12:15], a[200:203], v[44:47], v[12:15]\n"
    "s_lshl_b32 s47, s47, 7\n"
    "s_mov_b32 s42, s40\n"
    "s_add_u32 s43, s22, s40\n"
    "v_mfma_f32_16x16x32_bf16 v[16:19], a[40:43], v[40:43], v[16:19]\n"
    "s_add_u32 s44, s23, s40\n"
    "s_add_u32 s48, s26, s47\n"
    "s_add_u32 s47, s27, s47\n"
    "v_mfma_f32_16x16x32_bf16 v[20:23], a[40:43], v[44:47], v[20:23]\n"
    "s_lshl_b32 s45, s52, 19\n"
    "s_lshl_b32 s37, s52, 11\n"
    "s_xor_b32 s37, s37, s21\n"
    "v_mfma_f32_16x16x32_bf16 v[24:27], v[136:139], v[40:43], v[24:27]\n"
    "ds_read_b128 v[40:43], v95 offset:2048\n"
    "s_and_b32 s37, s37, 0x2000\n"
    "v_mfma_f32_16x16x32_bf16 v[28:31], v[136:139], v[44:47], v[28:31]\n"
    "ds_read_b128 v[44:47], v95 offset:3072\n"
    "s_waitcnt lgkmcnt(4)\n"
    "v_mfma_f32_16x16x32_bf16 v[0:3], a[124:127], v[48:51], v[0:3]\n"
    "s_add_u32 s45, s45, s37\n"
    "s_add_u32 s45, s45, s24\n"
    "s_lshl_b32 s46, s52, 20\n"
    "v_mfma_f32_16x16x32_bf16 v[4:7], a[124:127], v[52:55], v[4:7]\n"
    "s_lshl_b32 s37, s52, 12\n"
    "s_xor_b32 s37, s37, s21\n"
    "s_and_b32 s37, s37, 0x2000\n"
    "v_mfma_f32_16x16x32_bf16 v[8:11], a[204:207], v[48:51], v[8:11]\n"
    "s_add_u32 s46, s46, s37\n"
    "s_add_u32 s46, s46, s25\n"
    "v_mfma_f32_16x16x32_bf16 v[12:15], a[204:207], v[52:55], v[12:15]\n"
    "v_mfma_f32_16x16x32_bf16 v[16:19], a[44:47], v[48:51], v[16:19]\n"
    "v_mfma_f32_16x16x32_bf16 v[20:23], a[44:47], v[52:55], v[20:23]\n"
    "v_mfma_f32_16x16x32_bf16 v[24:27], v[140:143], v[48:51], v[24:27]\n"
    "ds_read_b128 v[48:51], v95 offset:4096\n"
    "v_mfma_f32_16x16x32_bf16 v[28:31], v[140:143], v[52:55], v[28:31]\n"
    "ds_read_b128 v[52:55], v95 offset:5120\n"
    "s_waitcnt lgkmcnt(4)\n"
    "v_mfma_f32_16x16x32_bf16 v[0:3], a[128:131], v[32:35], v[0:3]\n"
    "v_mfma_f32_16x16x32_bf16 v[4:7], a[128:131], v[36:39], v[4:7]\n"
    "v_mfma_f32_16x16x32_bf16 v[8:11], a[208:211], v[32:35], v[8:11]\n"
    "v_mfma_f32_16x16x32_bf16 v[12:15], a[208:211], v[36:39], v[12:15]\n"
    "v_mfma_f32_16x16x32_bf16 v[16:19], a[48:51], v[32:35], v[16:19]\n"
    "v_mfma_f32_16x16x32_bf16 v[20:23], a[48:51], v[36:39], v[20:23]\n"
    "v_mfma_f32_16x16x32_bf16 v[24:27], v[144:147], v[32:35], v[24:27]\n"
    "ds_read_b128 v[32:35], v95 offset:6144\n"
    "v_mfma_f32_16x16x32_bf16 v[28:31], v[144:147], v[36:39], v[28:31]\n"
    "ds_read_b128 v[36:39], v95 offset:7168\n"
    "s_waitcnt lgkmcnt(4)\n"
    "v_mfma_f32_16x16x32_bf16 v[0:3], a[132:135], v[40:43], v[0:3]\n"
    "v_mfma_f32_16x16x32_bf16 v[4:7], a[132:135], v[44:47], v[4:7]\n"
    "v_mfma_f32_16x16x32_bf16 v[8:11], a[212:215], v[40:43], v[8:11]\n"
    "v_mfma_f32_16x16x32_bf16 v[12:15], a[212:215], v[44:47], v[12:15]\n"
    "v_mfma_f32_16x16x32_bf16 v[16:19], a[52:55], v[40:43], v[16:19]\n"
    "v_mfma_f32_16x16x32_bf16 v[20:23], a[52:55], v[44:47], v[20:23]\n"
    "v_mfma_f32_16x16x32_bf16 v[24:27], v[148:151], v[40:43], v[24:27]\n"
    "ds_read_b128 v[40:43], v96 offset:45056\n"
    "v_mfma_f32_16x16x32_bf16 v[28:31], v[148:151], v[44:47], v[28:31]\n"
    "ds_read_b128 v[44:47], v96 offset:46080\n"
    "s_waitcnt lgkmcnt(4)\n"
    "v_mfma_f32_16x16x32_bf16 v[0:3], a[136:139], v[48:51], v[0:3]\n"
    "v_mfma_f32_16x16x32_bf16 v[4:7], a[136:139], v[52:55], v[4:7]\n"
    "v_mfma_f32_16x16x32_bf16 v[8:11], a[216:219], v[48:51], v[8:11]\n"
    "v_mfma_f32_16x16x32_bf16 v[12:15], a[216:219], v[52:55], v[12:15]\n"
    "v_mfma_f32_16x16x32_bf16 v[16:19], a[56:59], v[48:51], v[16:19]\n"
    "v_mfma_f32_16x16x32_bf16 v[20:23], a[56:59], v[52:55], v[20:23]\n"
    "v_mfma_f32_16x16x32_bf16 v[24:27], v[152:155], v[48:51], v[24:27]\n"
    "ds_read_b128 v[48:51], v96 offset:47104\n"
    "v_mfma_f32_16x16x32_bf16 v[28:31], v[152:155], v[52:55], v[28:31]\n"
    "ds_read_b128 v[52:55], v96 offset:48128\n"
    "s_waitcnt lgkmcnt(4)\n"
    "v_mfma_f32_16x16x32_bf16 v[0:3], a[140:143], v[32:35], v[0:3]\n"
    "v_mfma_f32_16x16x32_bf16 v[4:7], a[140:143], v[36:39], v[4:7]\n"
    "v_mfma_f32_16x16x32_bf16 v[8:11], a[220:223], v[32:35], v[8:11]\n"
    "s_waitcnt vmcnt(0)\n"
    "v_readfirstlane_b32 s36, v108\n"
    "v_mfma_f32_16x16x32_bf16 v[12:15], a[220:223], v[36:39], v[12:15]\n"
    "v_readfirstlane_b32 s37, v109\n"
    "v_mfma_f32_16x16x32_bf16 v[16:19], a[60:63], v[32:35], v[16:19]\n"
    "s_cmp_eq_u32 s37, s39\n"
    "s_cselect_b32 s49, s36, s49\n"
    "s_add_u32 s49, s49, 304\n"
    "v_mfma_f32_16x16x32_bf16 v[20:23], a[60:63], v[36:39], v[20:23]\n"
    "s_sub_u32 s50, s49, 63\n"
    "s_mov_b32 s39, s52\n"
    "v_mfma_f32_16x16x32_bf16 v[24:27], v[156:159], v[32:35], v[24:27]\n"
    "ds_read_b128 v[32:35], v96 offset:49152\n"
    "v_mfma_f32_16x16x32_bf16 v[28:31], v[156:159], v[36:39], v[28:31]\n"
    "ds_read_b128 v[36:39], v96 offset:50176\n"
    "s_waitcnt lgkmcnt(4)\n"
    "v_mfma_f32_16x16x32_bf16 v[0:3], a[144:147], v[40:43], v[0:3]\n"
    "v_mfma_f32_16x16x32_bf16 v[4:7], a[144:147], v[44:47], v[4:7]\n"
    ".Lqk_rt11_%=:\n"
    "s_memrealtime s[34:35]\n"
    "s_waitcnt lgkmcnt(0)\n"
    "s_sub_u32 s36, s50, s34\n"
    "s_cmp_lt_u32 s36, 600\n"
    "s_cbranch_scc1 .Lqk_rt11_%=\n"
    "buffer_load_dwordx4 v[56:59], v96, s[0:3], s43 offen sc1\n"
    "buffer_load_dwordx4 v[60:63], v96, s[0:3], s43 offen offset:1024 sc1\n"
    "buffer_load_dwordx4 v[64:67], v96, s[0:3], s44 offen sc1\n"
    "v_mfma_f32_16x16x32_bf16 v[8:11], a[224:227], v[40:43], v[8:11]\n"
    "v_mfma_f32_16x16x32_bf16 v[12:15], a[224:227], v[44:47], v[12:15]\n"
    "v_mfma_f32_16x16x32_bf16 v[16:19], a[64:67], v[40:43], v[16:19]\n"
    "v_mfma_f32_16x16x32_bf16 v[20:23], a[64:67], v[44:47], v[20:23]\n"
    "v_mfma_f32_16x16x32_bf16 v[24:27], v[160:163], v[40:43], v[24:27]\n"
    "ds_read_b128 v[40:43], v96 offset:51200\n"
    "v_mfma_f32_16x16x32_bf16 v[28:31], v[160:163], v[44:47], v[28:31]\n"
    "ds_read_b128 v[44:47], v96 offset:52224\n"
    "s_waitcnt lgkmcnt(4)\n"
    "v_mfma_f32_16x16x32_bf16 v[0:3], a[148:151], v[48:51], v[0:3]\n"
    "v_mfma_f32_16x16x32_bf16 v[4:7], a[148:151], v[52:55], v[4:7]\n"
    "v_mfma_f32_16x16x32_bf16 v[8:11], a[228:231], v[48:51], v[8:11]\n"
    "v_mfma_f32_16x16x32_bf16 v[12:15], a[228:231], v[52:55], v[12:15]\n"
    "v_mfma_f32_16x16x32_bf16 v[16:19], a[68:71], v[48:51], v[16:19]\n"
    "v_mfma_f32_16x16x32_bf16 v[20:23], a[68:71], v[52:55], v[20:23]\n"
    "v_mfma_f32_16x16x32_bf16 v[24:27], v[164:167], v[48:51], v[24:27]\n"
    "v_mfma_f32_16x16x32_bf16 v[28:31], v[164:167], v[52:55], v[28:31]\n"
    "s_waitcnt lgkmcnt(2)\n"
    "v_mfma_f32_16x16x32_bf16 v[0:3], a[152:155], v[32:35], v[0:3]\n"
    "v_mfma_f32_16x16x32_bf16 v[4:7], a[152:155], v[36:39], v[4:7]\n"
    "v_mfma_f32_16x16x32_bf16 v[8:11], a[232:235], v[32:35], v[8:11]\n"
    "v_mfma_f32_16x16x32_bf16 v[12:15], a[232:235], v[36:39], v[12:15]\n"
    "v_mfma_f32_16x16x32_bf16 v[16:19], a[72:75], v[32:35], v[16:19]\n"
    "v_mfma_f32_16x16x32_bf16 v[20:23], a[72:75], v[36:39], v[20:23]\n"
    "v_mfma_f32_16x16x32_bf16 v[24:27], v[168:171], v[32:35], v[24:27]\n"
    "v_mfma_f32_16x16x32_bf16 v[28:31], v[168:171], v[36:39], v[28:31]\n"
    "s_waitcnt lgkmcnt(0)\n"
    "v_mfma_f32_16x16x32_bf16 v[0:3], a[156:159], v[40:43], v[0:3]\n"
    "v_mfma_f32_16x16x32_bf16 v[4:7], a[156:159], v[44:47], v[4:7]\n"
    "v_mfma_f32_16x16x32_bf16 v[8:11], a[236:239], v[40:43], v[8:11]\n"
    "v_mfma_f32_16x16x32_bf16 v[12:15], a[236:239], v[44:47], v[12:15]\n"
    "v_mfma_f32_16x16x32_bf16 v[16:19], a[76:79], v[40:43], v[16:19]\n"
    "v_mfma_f32_16x16x32_bf16 v[20:23], a[76:79], v[44:47], v[20:23]\n"
    "v_mfma_f32_16x16x32_bf16 v[24:27], v[172:175], v[40:43], v[24:27]\n"
    "v_mfma_f32_16x16x32_bf16 v[28:31], v[172:175], v[44:47], v[28:31]\n"
    "ds_read_b128 v[32:35], v97 offset:0\n"
    "ds_read_b128 v[36:39], v97 offset:1024\n"
    "ds_read_b128 v[40:43], v97 offset:2048\n"
    "ds_read_b128 v[44:47], v97 offset:3072\n"
    "ds_read_b128 v[48:51], v97 offset:4096\n"
    "ds_read_b128 v[52:55], v97 offset:5120\n"
    "v_cvt_pk_bf16_f32 v68, v0, v1\n"
    "v_cvt_pk_bf16_f32 v69, v2, v3\n"
    "v_cvt_pk_bf16_f32 v70, v8, v9\n"
    "v_cvt_pk_bf16_f32 v71, v10, v11\n"
    "v_cvt_pk_bf16_f32 v72, v4, v5\n"
    "v_cvt_pk_bf16_f32 v73, v6, v7\n"
    "v_cvt_pk_bf16_f32 v74, v12, v13\n"
    "v_cvt_pk_bf16_f32 v75, v14, v15\n"
    "v_mfma_f32_16x16x32_bf16 v[108:111], v[68:71], v[84:87], 0\n"
    "v_cvt_pk_bf16_f32 v88, v24, v25\n"
    "v_cvt_pk_bf16_f32 v89, v26, v27\n"
    "v_cvt_pk_bf16_f32 v90, v28, v29\n"
    "v_cvt_pk_bf16_f32 v91, v30, v31\n"
    "v_mfma_f32_16x16x32_bf16 v[24:27], v[72:75], v[84:87], 0\n"
    "v_cvt_pk_bf16_f32 v76, v16, v17\n"
    "v_cvt_pk_bf16_f32 v77, v18, v19\n"
    "v_cvt_pk_bf16_f32 v78, v20, v21\n"
    "v_cvt_pk_bf16_f32 v79, v22, v23\n"
    "v_mfma_f32_16x16x32_bf16 v[28:31], v[68:71], v[80:83], 0\n"
    "v_mov_b64 v[0:1], 0\n"
    "v_mov_b64 v[2:3], 0\n"
    "v_mfma_f32_16x16x16_bf16 v[108:111], v[76:77], v[90:91], v[108:111]\n"
    "v_mov_b64 v[4:5], 0\n"
    "v_mov_b64 v[6:7], 0\n"
    "v_mfma_f32_16x16x16_bf16 v[24:27], v[78:79], v[90:91], v[24:27]\n"
    "v_mov_b64 v[8:9], 0\n"
    "v_mov_b64 v[10:11], 0\n"
    "v_mfma_f32_16x16x16_bf16 v[28:31], v[76:77], v[88:89], v[28:31]\n"
    "v_mov_b64 v[12:13], 0\n"
    "v_mov_b64 v[14:15], 0\n"
    "v_mov_b64 v[16:17], 0\n"
    "v_mov_b64 v[18:19], 0\n"
    "v_mov_b64 v[20:21], 0\n"
    "v_mov_b64 v[22:23], 0\n"
    "ds_write_b128 v103, v[108:111]\n"
    "ds_write_b128 v103, v[24:27] offset:64\n"
    "ds_write_b128 v103, v[28:31] offset:160\n"
    "s_branch .Lqk_take_%=\n"
    ".Lqk_retry_%=:\n"
    "s_add_u32 s32, s32, 1\n"
    "s_cmp_lt_u32 s32, 0x40000\n"
    "s_cbranch_scc1 .Lqk_sp12_%=\n"
    "v_mov_b32 v108, 1\n"
    "s_mov_b32 s36, 33728\n"
    "buffer_store_dword v108, off, s[0:3], s36 sc1\n"
    "s_waitcnt vmcnt(0)\n"
    "s_branch .Lqk_go_%=\n"
    ".Lqk_sp12_%=:\n"
    "s_nop 7\n"
    "v_mov_b64 v[0:1], 0\n"
    "v_mov_b64 v[2:3], 0\n"
    "v_mov_b64 v[4:5], 0\n"
    "v_mov_b64 v[6:7], 0\n"
    "v_mov_b64 v[8:9], 0\n"
    "v_mov_b64 v[10:11], 0\n"
    "v_mov_b64 v[12:13], 0\n"
    "v_mov_b64 v[14:15], 0\n"
    "v_mov_b64 v[16:17], 0\n"
    "v_mov_b64 v[18:19], 0\n"
    "v_mov_b64 v[20:21], 0\n"
    "v_mov_b64 v[22:23], 0\n"
    "buffer_load_dwordx4 v[56:59], v96, s[0:3], s43 offen sc1\n"
    "buffer_load_dwordx4 v[60:63], v96, s[0:3], s43 offen offset:1024 sc1\n"
    "buffer_load_dwordx4 v[64:67], v96, s[0:3], s44 offen sc1\n"
    "s_waitcnt vmcnt(0)\n"
    "s_branch .Lqk_take_%=\n"
    ".Lqk_end_%=:\n"
    "s_waitcnt vmcnt(0) lgkmcnt(0)\n"
    "s_endpgm\n"
    :: "{s[0:3]}"(s0_), "{s[4:7]}"(s4_), "{s[8:11]}"(s8_), "{s[12:15]}"(s12_), "{s[16:19]}"(s16_), "{s[20:23]}"(s20_), "{s[24:27]}"(s24_), "{s[28:31]}"(s28_), "{v[0:3]}"(v0_), "{v[4:7]}"(v4_), "{v[92:95]}"(v92_), "{v[96:99]}"(v96_), "{v[100:103]}"(v100_), "{v[104:107]}"(v104_) : "memory", "v255", "a255", "s95");
    __builtin_unreachable();
}
DEV void vo_role(const MegaArgs& A, const int L, const int h, const int rep) {
    ROLE_HEAD;
    const unsigned um = xl(RG_UM + ((8 * w + lq) * 4 + (h >> 3)) * 2048 + (h & 7) * 256 + l16 * 16, L);
    const unsigned umx = xl(RG_UMX + ((4 * (2 * w + (lq & 1)) + (l16 >> 2)) * 4 + (h >> 3)) * 512 + (h & 7) * 64 + (l & 3) * 16 + (lq >> 1) * 8, L);
    bst16(rr, um, ones); bst16(rr, um + 65536, ones); bst8(rr, umx, ones2);
    DRAIN_BARRIER();
    bst4(rr, threadIdx.x < 4 ? xl(Z_QV + l * 256 + (32 + h) * 4, L) : OOB, 1u);
    const u32x4 s0_ = {RFL(rx[0]), RFL(rx[1]), RFL(rx[2]), RFL(rx[3])};
    const u32x4 s4_ = {RFL(lo(A.Wo + h * 192 * 640)), RFL(hi(A.Wo + h * 192 * 640)), RFL(192u * 1280u), RFL(0x20000u)};
    const u32x4 s8_ = {RFL(lo(A.Wv + h * 192)), RFL(hi(A.Wv + h * 192)), RFL(WBYTES - h * 384), RFL(0x20000u)};
    const u32x4 s12_ = {RFL(w), RFL(L << 13), RFL(L), RFL(xl(RG_MEM + rep * 16384, L) + w * 2048)};
    const u32x4 s16_ = {RFL(w < 2 ? xl(RG_MEM + rep * 16384 + 8192, L) + w * 1024 : OOB), RFL(REC0 + w * 2048), RFL(QT0 + h * 32768 + 16384), RFL(xl(RG_UT + (32 * w + (h >> 3)) * 8192 + (h & 7) * 1024, L))};
    const u32x4 s20_ = {RFL(xl(Z_X + 8192, L)), RFL(xl(Z_CT + (h & 7) * 256, L)), RFL(xl(Z_XN + rep * 2048, 1)), RFL(xl(Z_QW + h * 128, L))};
    const u32x4 s24_ = {RFL(DBG0 + ((L * 128 + 32 + h) * 4 + w) * 2048), RFL(0u), RFL(0u), RFL(0u)};
    const u32x4 v0_ = {(unsigned)(((3 * w + 0) * 64 + l) / 24 * 12288 + ((((3 * w + 0) * 64 + l) % 24) ^ ((((3 * w + 0) * 64 + l) / 48) & 6)) * 16 - 0), (unsigned)(((3 * w + 1) * 64 + l) / 24 * 12288 + ((((3 * w + 1) * 64 + l) % 24) ^ ((((3 * w + 1) * 64 + l) / 48) & 6)) * 16 - 1024), (unsigned)(((3 * w + 2) * 64 + l) / 24 * 12288 + ((((3 * w + 2) * 64 + l) % 24) ^ ((((3 * w + 2) * 64 + l) / 48) & 6)) * 16 - 2048), (unsigned)((8 * lq + ((l >> 2) & 1) + ((l >> 1) & 4)) * 384 + (w ^ (((8 * lq + ((l >> 2) & 1) + ((l >> 1) & 4)) >> 2) & 3)) * 32 + (l & 3) * 8 + ((w + 0) & 3) * 12288)};
    const u32x4 v4_ = {(unsigned)((8 * lq + ((l >> 2) & 1) + ((l >> 1) & 4)) * 384 + (w ^ (((8 * lq + ((l >> 2) & 1) + ((l >> 1) & 4)) >> 2) & 3)) * 32 + (l & 3) * 8 + ((w + 1) & 3) * 12288), (unsigned)((8 * lq + ((l >> 2) & 1) + ((l >> 1) & 4)) * 384 + (w ^ (((8 * lq + ((l >> 2) & 1) + ((l >> 1) & 4)) >> 2) & 3)) * 32 + (l & 3) * 8 + ((w + 2) & 3) * 12288), (unsigned)((8 * lq + ((l >> 2) & 1) + ((l >> 1) & 4)) * 384 + (w ^ (((8 * lq + ((l >> 2) & 1) + ((l >> 1) & 4)) >> 2) & 3)) * 32 + (l & 3) * 8 + ((w + 3) & 3) * 12288), (unsigned)((8 * lq + ((l >> 2) & 1) + ((l >> 1) & 4)) * 384 + (w ^ (((8 * lq + ((l >> 2) & 1) + ((l >> 1) & 4)) >> 2) & 3)) * 32 + (l & 3) * 8)};
    const u32x4 v8_ = {(unsigned)(0), (unsigned)(0), (unsigned)(0), (unsigned)(0)};
    const u32x4 v12_ = {(unsigned)((4 * 0 + lq) * 1280 + 256 * w + ((l16 ^ (2 * ((4 * 0 + lq) & 7))) * 16) - 0), (unsigned)((4 * 1 + lq) * 1280 + 256 * w + ((l16 ^ (2 * ((4 * 1 + lq) & 7))) * 16) - 1024), (unsigned)((4 * 2 + lq) * 1280 + 256 * w + ((l16 ^ (2 * ((4 * 2 + lq) & 7))) * 16) - 2048), (unsigned)((4 * 3 + lq) * 1280 + 256 * w + ((l16 ^ (2 * ((4 * 3 + lq) & 7))) * 16) - 3072)};
    const u32x4 v16_ = {(unsigned)((4 * w + lq) * 1280 + 1024 + ((l16 ^ (2 * ((4 * w + lq) & 7))) * 16)), (unsigned)(w * 8192 + (l >> 2) * 256 + ((l >> 2) & 7) * 32 + (l & 3) * 8), (unsigned)(32768 + (l >> 2) * 256 + (((l >> 2) & 7) ^ (2 * w)) * 32 + (l & 3) * 8), (unsigned)(0u)};
    const u32x4 v92_ = {(unsigned)(l * 16 + ((w + 0) & 3) * 13312), (unsigned)(l * 16 + ((w + 1) & 3) * 13312), (unsigned)(l * 16 + ((w + 2) & 3) * 13312), (unsigned)(l * 16 + ((w + 3) & 3) * 13312)};
    const u32x4 v96_ = {(unsigned)(l * 16), (unsigned)(l * 16 + 64512 + w * 20480), (unsigned)((l & 2) ? 0xeeeeu : 0x4444u), (unsigned)(61440 + 512 * w + (l16 >> 2) * 64 + (l & 1) * 32 + sw2(lq) * 8 + (l & 2) * 2)};
    const u32x4 v100_ = {(unsigned)(61440 + 2048 + 256 * w + (l16 >> 2) * 64 + (l & 1) * 32 + sw2(lq) * 8 + (l & 2) * 2), (unsigned)(61440 + 512 * (lq >> 1) + 128 * (lq & 1) + (l & 4) * 8 + (l & 8) * 32 + (l & 3) * 8), (unsigned)(um), (unsigned)(umx)};
    const u32x4 v104_ = {(unsigned)(xl(RG_UTX + ((8 * w + (l16 >> 2)) * 4 + (h >> 3)) * 2048 + (h & 1) * 1024 + ((h & 7) >> 1) * 256 + lq * 64 + (l & 3) * 16, L)), (unsigned)(l < 20 ? (l >> 2) * 128 + (l & 3) * 4 : 0), (unsigned)((l & 3) * 4), (unsigned)(0u)};
    asm volatile(
    "s_mov_b32 s28, 0\n"
    "s_cmp_eq_u32 s13, 0\n"
    "s_cbranch_scc1 .Lvo_ho1_%=\n"
    "s_sleep 35\n"
    "s_sleep 35\n"
    "s_sleep 35\n"
    "s_sleep 35\n"
    "s_sleep 35\n"
    "s_sleep 35\n"
    "s_sleep 35\n"
    "s_sleep 35\n"
    "s_sleep 35\n"
    "s_sleep 35\n"
    "s_sleep 35\n"
    "s_sleep 35\n"
    "s_sleep 35\n"
    "s_sleep 35\n"
    ".Lvo_ho1_%=:\n"
    "s_mul_i32 s41, s12, 3072\n"
    "s_lshl_b32 s42, s12, 13\n"
    "s_lshl_b32 s43, s12, 10\n"
    "s_mov_b32 s49, 0\n"
    "s_add_u32 m0, s42, 0\n"
    "s_nop 0\n"
    "buffer_load_dwordx4 v12, s[4:7], s49 offen offset:0 nt lds\n"
    "buffer_load_dwordx4 v13, s[4:7], s49 offen offset:1024 nt lds\n"
    "buffer_load_dwordx4 v14, s[4:7], s49 offen offset:2048 nt lds\n"
    "buffer_load_dwordx4 v15, s[4:7], s49 offen offset:3072 nt lds\n"
    "s_add_u32 m0, s43, 32768\n"
    "s_nop 0\n"
    "buffer_load_dwordx4 v16, s[4:7], s49 offen nt lds\n"
    "s_mov_b32 s49, 81920\n"
    "s_add_u32 m0, s42, 4096\n"
    "s_nop 0\n"
    "buffer_load_dwordx4 v12, s[4:7], s49 offen offset:0 nt lds\n"
    "buffer_load_dwordx4 v13, s[4:7], s49 offen offset:1024 nt lds\n"
    "buffer_load_dwordx4 v14, s[4:7], s49 offen offset:2048 nt lds\n"
    "buffer_load_dwordx4 v15, s[4:7], s49 offen offset:3072 nt lds\n"
    "s_add_u32 m0, s43, 36864\n"
    "s_nop 0\n"
    "buffer_load_dwordx4 v16, s[4:7], s49 offen nt lds\n"
    "s_mov_b32 s49, 20480\n"
    "s_add_u32 m0, s42, 49152\n"
    "s_nop 0\n"
    "buffer_load_dwordx4 v12, s[4:7], s49 offen offset:0 nt lds\n"
    "buffer_load_dwordx4 v13, s[4:7], s49 offen offset:1024 nt lds\n"
    "buffer_load_dwordx4 v14, s[4:7], s49 offen offset:2048 nt lds\n"
    "buffer_load_dwordx4 v15, s[4:7], s49 offen offset:3072 nt lds\n"
    "s_add_u32 m0, s43, 81920\n"
    "s_nop 0\n"
    "buffer_load_dwordx4 v16, s[4:7], s49 offen nt lds\n"
    "s_mov_b32 s49, 102400\n"
    "s_add_u32 m0, s42, 53248\n"
    "s_nop 0\n"
    "buffer_load_dwordx4 v12, s[4:7], s49 offen offset:0 nt lds\n"
    "buffer_load_dwordx4 v13, s[4:7], s49 offen offset:1024 nt lds\n"
    "buffer_load_dwordx4 v14, s[4:7], s49 offen offset:2048 nt lds\n"
    "buffer_load_dwordx4 v15, s[4:7], s49 offen offset:3072 nt lds\n"
    "s_add_u32 m0, s43, 86016\n"
    "s_nop 0\n"
    "buffer_load_dwordx4 v16, s[4:7], s49 offen nt lds\n"
    "s_mov_b32 s49, 40960\n"
    "s_add_u32 m0, s42, 98304\n"
    "s_nop 0\n"
    "buffer_load_dwordx4 v12, s[4:7], s49 offen offset:0 nt lds\n"
    "buffer_load_dwordx4 v13, s[4:7], s49 offen offset:1024 nt lds\n"
    "buffer_load_dwordx4 v14, s[4:7], s49 offen offset:2048 nt lds\n"
    "buffer_load_dwordx4 v15, s[4:7], s49 offen offset:3072 nt lds\n"
    "s_add_u32 m0, s43, 131072\n"
    "s_nop 0\n"
    "buffer_load_dwordx4 v16, s[4:7], s49 offen nt lds\n"
    "s_mov_b32 s49, 122880\n"
    "s_add_u32 m0, s42, 102400\n"
    "s_nop 0\n"
    "buffer_load_dwordx4 v12, s[4:7], s49 offen offset:0 nt lds\n"
    "buffer_load_dwordx4 v13, s[4:7], s49 offen offset:1024 nt lds\n"
    "buffer_load_dwordx4 v14, s[4:7], s49 offen offset:2048 nt lds\n"
    "buffer_load_dwordx4 v15, s[4:7], s49 offen offset:3072 nt lds\n"
    "s_add_u32 m0, s43, 135168\n"
    "s_nop 0\n"
    "buffer_load_dwordx4 v16, s[4:7], s49 offen nt lds\n"
    "s_waitcnt vmcnt(20)\n"
    "s_barrier\n"
    "v_add_u32 v24, 0, v17\n"
    "v_add_u32 v32, 0, v18\n"
    "v_xor_b32 v33, 32, v32\n"
    "v_xor_b32 v25, 32, v24\n"
    "v_xor_b32 v26, 64, v24\n"
    "v_xor_b32 v27, 96, v24\n"
    "v_xor_b32 v28, 128, v24\n"
    "v_xor_b32 v29, 160, v24\n"
    "v_xor_b32 v30, 192, v24\n"
    "v_xor_b32 v31, 224, v24\n"
    "ds_read_b64_tr_b16 a[0:1], v24 offset:0\n"
    "ds_read_b64_tr_b16 a[2:3], v24 offset:4096\n"
    "ds_read_b64_tr_b16 a[24:25], v25 offset:0\n"
    "ds_read_b64_tr_b16 a[26:27], v25 offset:4096\n"
    "ds_read_b64_tr_b16 a[48:49], v26 offset:0\n"
    "ds_read_b64_tr_b16 a[50:51], v26 offset:4096\n"
    "ds_read_b64_tr_b16 a[72:73], v27 offset:0\n"
    "ds_read_b64_tr_b16 a[74:75], v27 offset:4096\n"
    "ds_read_b64_tr_b16 a[96:97], v28 offset:0\n"
    "ds_read_b64_tr_b16 a[98:99], v28 offset:4096\n"
    "ds_read_b64_tr_b16 a[120:121], v29 offset:0\n"
    "ds_read_b64_tr_b16 a[122:123], v29 offset:4096\n"
    "ds_read_b64_tr_b16 a[144:145], v30 offset:0\n"
    "ds_read_b64_tr_b16 a[146:147], v30 offset:4096\n"
    "ds_read_b64_tr_b16 a[168:169], v31 offset:0\n"
    "ds_read_b64_tr_b16 a[170:171], v31 offset:4096\n"
    "ds_read_b64_tr_b16 a[192:193], v32 offset:0\n"
    "ds_read_b64_tr_b16 a[194:195], v32 offset:4096\n"
    "ds_read_b64_tr_b16 a[216:217], v33 offset:0\n"
    "ds_read_b64_tr_b16 a[218:219], v33 offset:4096\n"
    "s_waitcnt lgkmcnt(0)\n"
    "s_barrier\n"
    "s_mov_b32 s49, 61440\n"
    "s_add_u32 m0, s42, 0\n"
    "s_nop 0\n"
    "buffer_load_dwordx4 v12, s[4:7], s49 offen offset:0 nt lds\n"
    "buffer_load_dwordx4 v13, s[4:7], s49 offen offset:1024 nt lds\n"
    "buffer_load_dwordx4 v14, s[4:7], s49 offen offset:2048 nt lds\n"
    "buffer_load_dwordx4 v15, s[4:7], s49 offen offset:3072 nt lds\n"
    "s_add_u32 m0, s43, 32768\n"
    "s_nop 0\n"
    "buffer_load_dwordx4 v16, s[4:7], s49 offen nt lds\n"
    "s_mov_b32 s49, 143360\n"
    "s_add_u32 m0, s42, 4096\n"
    "s_nop 0\n"
    "buffer_load_dwordx4 v12, s[4:7], s49 offen offset:0 nt lds\n"
    "buffer_load_dwordx4 v13, s[4:7], s49 offen offset:1024 nt lds\n"
    "buffer_load_dwordx4 v14, s[4:7], s49 offen offset:2048 nt lds\n"
    "buffer_load_dwordx4 v15, s[4:7], s49 offen offset:3072 nt lds\n"
    "s_add_u32 m0, s43, 36864\n"
    "s_nop 0\n"
    "buffer_load_dwordx4 v16, s[4:7], s49 offen nt lds\n"
    "s_waitcnt vmcnt(20)\n"
    "s_barrier\n"
    "v_add_u32 v24, 49152, v17\n"
    "v_add_u32 v32, 49152, v18\n"
    "v_xor_b32 v33, 32, v32\n"
    "v_xor_b32 v25, 32, v24\n"
    "v_xor_b32 v26, 64, v24\n"
    "v_xor_b32 v27, 96, v24\n"
    "v_xor_b32 v28, 128, v24\n"
    "v_xor_b32 v29, 160, v24\n"
    "v_xor_b32 v30, 192, v24\n"
    "v_xor_b32 v31, 224, v24\n"
    "ds_read_b64_tr_b16 a[4:5], v24 offset:0\n"
    "ds_read_b64_tr_b16 a[6:7], v24 offset:4096\n"
    "ds_read_b64_tr_b16 a[28:29], v25 offset:0\n"
    "ds_read_b64_tr_b16 a[30:31], v25 offset:4096\n"
    "ds_read_b64_tr_b16 a[52:53], v26 offset:0\n"
    "ds_read_b64_tr_b16 a[54:55], v26 offset:4096\n"
    "ds_read_b64_tr_b16 a[76:77], v27 offset:0\n"
    "ds_read_b64_tr_b16 a[78:79], v27 offset:4096\n"
    "ds_read_b64_tr_b16 a[100:101], v28 offset:0\n"
    "ds_read_b64_tr_b16 a[102:103], v28 offset:4096\n"
    "ds_read_b64_tr_b16 a[124:125], v29 offset:0\n"
    "ds_read_b64_tr_b16 a[126:127], v29 offset:4096\n"
    "ds_read_b64_tr_b16 a[148:149], v30 offset:0\n"
    "ds_read_b64_tr_b16 a[150:151], v30 offset:4096\n"
    "ds_read_b64_tr_b16 a[172:173], v31 offset:0\n"
    "ds_read_b64_tr_b16 a[174:175], v31 offset:4096\n"
    "ds_read_b64_tr_b16 a[196:197], v32 offset:0\n"
    "ds_read_b64_tr_b16 a[198:199], v32 offset:4096\n"
    "ds_read_b64_tr_b16 a[220:221], v33 offset:0\n"
    "ds_read_b64_tr_b16 a[222:223], v33 offset:4096\n"
    "s_waitcnt lgkmcnt(0)\n"
    "s_barrier\n"
    "s_mov_b32 s49, 163840\n"
    "s_add_u32 m0, s42, 49152\n"
    "s_nop 0\n"
    "buffer_load_dwordx4 v12, s[4:7], s49 offen offset:0 nt lds\n"
    "buffer_load_dwordx4 v13, s[4:7], s49 offen offset:1024 nt lds\n"
    "buffer_load_dwordx4 v14, s[4:7], s49 offen offset:2048 nt lds\n"
    "buffer_load_dwordx4 v15, s[4:7], s49 offen offset:3072 nt lds\n"
    "s_add_u32 m0, s43, 81920\n"
    "s_nop 0\n"
    "buffer_load_dwordx4 v16, s[4:7], s49 offen nt lds\n"
    "s_mov_b32 s49, 184320\n"
    "s_add_u32 m0, s42, 53248\n"
    "s_nop 0\n"
    "buffer_load_dwordx4 v12, s[4:7], s49 offen offset:0 nt lds\n"
    "buffer_load_dwordx4 v13, s[4:7], s49 offen offset:1024 nt lds\n"
    "buffer_load_dwordx4 v14, s[4:7], s49 offen offset:2048 nt lds\n"
    "buffer_load_dwordx4 v15, s[4:7], s49 offen offset:3072 nt lds\n"
    "s_add_u32 m0, s43, 86016\n"
    "s_nop 0\n"
    "buffer_load_dwordx4 v16, s[4:7], s49 offen nt lds\n"
    "s_waitcnt vmcnt(20)\n"
    "s_barrier\n"
    "v_add_u32 v24, 98304, v17\n"
    "v_add_u32 v32, 98304, v18\n"
    "v_xor_b32 v33, 32, v32\n"
    "v_xor_b32 v25, 32, v24\n"
    "v_xor_b32 v26, 64, v24\n"
    "v_xor_b32 v27, 96, v24\n"
    "v_xor_b32 v28, 128, v24\n"
    "v_xor_b32 v29, 160, v24\n"
    "v_xor_b32 v30, 192, v24\n"
    "v_xor_b32 v31, 224, v24\n"
    "ds_read_b64_tr_b16 a[8:9], v24 offset:0\n"
    "ds_read_b64_tr_b16 a[10:11], v24 offset:4096\n"
    "ds_read_b64_tr_b16 a[32:33], v25 offset:0\n"
    "ds_read_b64_tr_b16 a[34:35], v25 offset:4096\n"
    "ds_read_b64_tr_b16 a[56:57], v26 offset:0\n"
    "ds_read_b64_tr_b16 a[58:59], v26 offset:4096\n"
    "ds_read_b64_tr_b16 a[80:81], v27 offset:0\n"
    "ds_read_b64_tr_b16 a[82:83], v27 offset:4096\n"
    "ds_read_b64_tr_b16 a[104:105], v28 offset:0\n"
    "ds_read_b64_tr_b16 a[106:107], v28 offset:4096\n"
    "ds_read_b64_tr_b16 a[128:129], v29 offset:0\n"
    "ds_read_b64_tr_b16 a[130:131], v29 offset:4096\n"
    "ds_read_b64_tr_b16 a[152:153], v30 offset:0\n"
    "ds_read_b64_tr_b16 a[154:155], v30 offset:4096\n"
    "ds_read_b64_tr_b16 a[176:177], v31 offset:0\n"
    "ds_read_b64_tr_b16 a[178:179], v31 offset:4096\n"
    "ds_read_b64_tr_b16 a[200:201], v32 offset:0\n"
    "ds_read_b64_tr_b16 a[202:203], v32 offset:4096\n"
    "ds_read_b64_tr_b16 a[224:225], v33 offset:0\n"
    "ds_read_b64_tr_b16 a[226:227], v33 offset:4096\n"
    "s_waitcnt lgkmcnt(0)\n"
    "s_barrier\n"
    "s_mov_b32 s49, 204800\n"
    "s_add_u32 m0, s42, 98304\n"
    "s_nop 0\n"
    "buffer_load_dwordx4 v12, s[4:7], s49 offen offset:0 nt lds\n"
    "buffer_load_dwordx4 v13, s[4:7], s49 offen offset:1024 nt lds\n"
    "buffer_load_dwordx4 v14, s[4:7], s49 offen offset:2048 nt lds\n"
    "buffer_load_dwordx4 v15, s[4:7], s49 offen offset:3072 nt lds\n"
    "s_add_u32 m0, s43, 131072\n"
    "s_nop 0\n"
    "buffer_load_dwordx4 v16, s[4:7], s49 offen nt lds\n"
    "s_mov_b32 s49, 225280\n"
    "s_add_u32 m0, s42, 102400\n"
    "s_nop 0\n"
    "buffer_load_dwordx4 v12, s[4:7], s49 offen offset:0 nt lds\n"
    "buffer_load_dwordx4 v13, s[4:7], s49 offen offset:1024 nt lds\n"
    "buffer_load_dwordx4 v14, s[4:7], s49 offen offset:2048 nt lds\n"
    "buffer_load_dwordx4 v15, s[4:7], s49 offen offset:3072 nt lds\n"
    "s_add_u32 m0, s43, 135168\n"
    "s_nop 0\n"
    "buffer_load_dwordx4 v16, s[4:7], s49 offen nt lds\n"
    "s_waitcnt vmcnt(20)\n"
    "s_barrier\n"
    "v_add_u32 v24, 0, v17\n"
    "v_add_u32 v32, 0, v18\n"
    "v_xor_b32 v33, 32, v32\n"
    "v_xor_b32 v25, 32, v24\n"
    "v_xor_b32 v26, 64, v24\n"
    "v_xor_b32 v27, 96, v24\n"
    "v_xor_b32 v28, 128, v24\n"
    "v_xor_b32 v29, 160, v24\n"
    "v_xor_b32 v30, 192, v24\n"
    "v_xor_b32 v31, 224, v24\n"
    "ds_read_b64_tr_b16 a[12:13], v24 offset:0\n"
    "ds_read_b64_tr_b16 a[14:15], v24 offset:4096\n"
    "ds_read_b64_tr_b16 a[36:37], v25 offset:0\n"
    "ds_read_b64_tr_b16 a[38:39], v25 offset:4096\n"
    "ds_read_b64_tr_b16 a[60:61], v26 offset:0\n"
    "ds_read_b64_tr_b16 a[62:63], v26 offset:4096\n"
    "ds_read_b64_tr_b16 a[84:85], v27 offset:0\n"
    "ds_read_b64_tr_b16 a[86:87], v27 offset:4096\n"
    "ds_read_b64_tr_b16 a[108:109], v28 offset:0\n"
    "ds_read_b64_tr_b16 a[110:111], v28 offset:4096\n"
    "ds_read_b64_tr_b16 a[132:133], v29 offset:0\n"
    "ds_read_b64_tr_b16 a[134:135], v29 offset:4096\n"
    "ds_read_b64_tr_b16 a[156:157], v30 offset:0\n"
    "ds_read_b64_tr_b16 a[158:159], v30 offset:4096\n"
    "ds_read_b64_tr_b16 a[180:181], v31 offset:0\n"
    "ds_read_b64_tr_b16 a[182:183], v31 offset:4096\n"
    "ds_read_b64_tr_b16 a[204:205], v32 offset:0\n"
    "ds_read_b64_tr_b16 a[206:207], v32 offset:4096\n"
    "ds_read_b64_tr_b16 a[228:229], v33 offset:0\n"
    "ds_read_b64_tr_b16 a[230:231], v33 offset:4096\n"
    "s_waitcnt lgkmcnt(0)\n"
    "s_barrier\n"
    "s_mov_b32 s49, 0\n"
    "s_add_u32 m0, s41, 0\n"
    "s_nop 0\n"
    "buffer_load_dwordx4 v0, s[8:11], s49 offen offset:0 nt lds\n"
    "buffer_load_dwordx4 v1, s[8:11], s49 offen offset:1024 nt lds\n"
    "buffer_load_dwordx4 v2, s[8:11], s49 offen offset:2048 nt lds\n"
    "s_mov_b32 s49, 1572864\n"
    "s_add_u32 m0, s41, 12288\n"
    "s_nop 0\n"
    "buffer_load_dwordx4 v0, s[8:11], s49 offen offset:0 nt lds\n"
    "buffer_load_dwordx4 v1, s[8:11], s49 offen offset:1024 nt lds\n"
    "buffer_load_dwordx4 v2, s[8:11], s49 offen offset:2048 nt lds\n"
    "s_mov_b32 s49, 3145728\n"
    "s_add_u32 m0, s41, 24576\n"
    "s_nop 0\n"
    "buffer_load_dwordx4 v0, s[8:11], s49 offen offset:0 nt lds\n"
    "buffer_load_dwordx4 v1, s[8:11], s49 offen offset:1024 nt lds\n"
    "buffer_load_dwordx4 v2, s[8:11], s49 offen offset:2048 nt lds\n"
    "s_mov_b32 s49, 4718592\n"
    "s_add_u32 m0, s41, 36864\n"
    "s_nop 0\n"
    "buffer_load_dwordx4 v0, s[8:11], s49 offen offset:0 nt lds\n"
    "buffer_load_dwordx4 v1, s[8:11], s49 offen offset:1024 nt lds\n"
    "buffer_load_dwordx4 v2, s[8:11], s49 offen offset:2048 nt lds\n"
    "s_waitcnt vmcnt(22)\n"
    "s_barrier\n"
    "v_add_u32 v24, 49152, v17\n"
    "v_add_u32 v32, 49152, v18\n"
    "v_xor_b32 v33, 32, v32\n"
    "v_xor_b32 v25, 32, v24\n"
    "v_xor_b32 v26, 64, v24\n"
    "v_xor_b32 v27, 96, v24\n"
    "v_xor_b32 v28, 128, v24\n"
    "v_xor_b32 v29, 160, v24\n"
    "v_xor_b32 v30, 192, v24\n"
    "v_xor_b32 v31, 224, v24\n"
    "ds_read_b64_tr_b16 a[16:17], v24 offset:0\n"
    "ds_read_b64_tr_b16 a[18:19], v24 offset:4096\n"
    "ds_read_b64_tr_b16 a[40:41], v25 offset:0\n"
    "ds_read_b64_tr_b16 a[42:43], v25 offset:4096\n"
    "ds_read_b64_tr_b16 a[64:65], v26 offset:0\n"
    "ds_read_b64_tr_b16 a[66:67], v26 offset:4096\n"
    "ds_read_b64_tr_b16 a[88:89], v27 offset:0\n"
    "ds_read_b64_tr_b16 a[90:91], v27 offset:4096\n"
    "ds_read_b64_tr_b16 a[112:113], v28 offset:0\n"
    "ds_read_b64_tr_b16 a[114:115], v28 offset:4096\n"
    "ds_read_b64_tr_b16 a[136:137], v29 offset:0\n"
    "ds_read_b64_tr_b16 a[138:139], v29 offset:4096\n"
    "ds_read_b64_tr_b16 a[160:161], v30 offset:0\n"
    "ds_read_b64_tr_b16 a[162:163], v30 offset:4096\n"
    "ds_read_b64_tr_b16 a[184:185], v31 offset:0\n"
    "ds_read_b64_tr_b16 a[186:187], v31 offset:4096\n"
    "ds_read_b64_tr_b16 a[208:209], v32 offset:0\n"
    "ds_read_b64_tr_b16 a[210:211], v32 offset:4096\n"
    "ds_read_b64_tr_b16 a[232:233], v33 offset:0\n"
    "ds_read_b64_tr_b16 a[234:235], v33 offset:4096\n"
    "s_waitcnt lgkmcnt(0)\n"
    "s_barrier\n"
    "s_mov_b32 s49, 393216\n"
    "s_add_u32 m0, s41, 49152\n"
    "s_nop 0\n"
    "buffer_load_dwordx4 v0, s[8:11], s49 offen offset:0 nt lds\n"
    "buffer_load_dwordx4 v1, s[8:11], s49 offen offset:1024 nt lds\n"
    "buffer_load_dwordx4 v2, s[8:11], s49 offen offset:2048 nt lds\n"
    "s_mov_b32 s49, 1966080\n"
    "s_add_u32 m0, s41, 61440\n"
    "s_nop 0\n"
    "buffer_load_dwordx4 v0, s[8:11], s49 offen offset:0 nt lds\n"
    "buffer_load_dwordx4 v1, s[8:11], s49 offen offset:1024 nt lds\n"
    "buffer_load_dwordx4 v2, s[8:11], s49 offen offset:2048 nt lds\n"
    "s_mov_b32 s49, 3538944\n"
    "s_add_u32 m0, s41, 73728\n"
    "s_nop 0\n"
    "buffer_load_dwordx4 v0, s[8:11], s49 offen offset:0 nt lds\n"
    "buffer_load_dwordx4 v1, s[8:11], s49 offen offset:1024 nt lds\n"
    "buffer_load_dwordx4 v2, s[8:11], s49 offen offset:2048 nt lds\n"
    "s_mov_b32 s49, 5111808\n"
    "s_add_u32 m0, s41, 86016\n"
    "s_nop 0\n"
    "buffer_load_dwordx4 v0, s[8:11], s49 offen offset:0 nt lds\n"
    "buffer_load_dwordx4 v1, s[8:11], s49 offen offset:1024 nt lds\n"
    "buffer_load_dwordx4 v2, s[8:11], s49 offen offset:2048 nt lds\n"
    "s_waitcnt vmcnt(24)\n"
    "s_barrier\n"
    "v_add_u32 v24, 98304, v17\n"
    "v_add_u32 v32, 98304, v18\n"
    "v_xor_b32 v33, 32, v32\n"
    "v_xor_b32 v25, 32, v24\n"
    "v_xor_b32 v26, 64, v24\n"
    "v_xor_b32 v27, 96, v24\n"
    "v_xor_b32 v28, 128, v24\n"
    "v_xor_b32 v29, 160, v24\n"
    "v_xor_b32 v30, 192, v24\n"
    "v_xor_b32 v31, 224, v24\n"
    "ds_read_b64_tr_b16 a[20:21], v24 offset:0\n"
    "ds_read_b64_tr_b16 a[22:23], v24 offset:4096\n"
    "ds_read_b64_tr_b16 a[44:45], v25 offset:0\n"
    "ds_read_b64_tr_b16 a[46:47], v25 offset:4096\n"
    "ds_read_b64_tr_b16 a[68:69], v26 offset:0\n"
    "ds_read_b64_tr_b16 a[70:71], v26 offset:4096\n"
    "ds_read_b64_tr_b16 a[92:93], v27 offset:0\n"
    "ds_read_b64_tr_b16 a[94:95], v27 offset:4096\n"
    "ds_read_b64_tr_b16 a[116:117], v28 offset:0\n"
    "ds_read_b64_tr_b16 a[118:119], v28 offset:4096\n"
    "ds_read_b64_tr_b16 a[140:141], v29 offset:0\n"
    "ds_read_b64_tr_b16 a[142:143], v29 offset:4096\n"
    "ds_read_b64_tr_b16 a[164:165], v30 offset:0\n"
    "ds_read_b64_tr_b16 a[166:167], v30 offset:4096\n"
    "ds_read_b64_tr_b16 a[188:189], v31 offset:0\n"
    "ds_read_b64_tr_b16 a[190:191], v31 offset:4096\n"
    "ds_read_b64_tr_b16 a[212:213], v32 offset:0\n"
    "ds_read_b64_tr_b16 a[214:215], v32 offset:4096\n"
    "ds_read_b64_tr_b16 a[236:237], v33 offset:0\n"
    "ds_read_b64_tr_b16 a[238:239], v33 offset:4096\n"
    "s_waitcnt lgkmcnt(0)\n"
    "s_barrier\n"
    "s_mov_b32 s49, 786432\n"
    "s_add_u32 m0, s41, 98304\n"
    "s_nop 0\n"
    "buffer_load_dwordx4 v0, s[8:11], s49 offen offset:0 nt lds\n"
    "buffer_load_dwordx4 v1, s[8:11], s49 offen offset:1024 nt lds\n"
    "buffer_load_dwordx4 v2, s[8:11], s49 offen offset:2048 nt lds\n"
    "s_mov_b32 s49, 2359296\n"
    "s_add_u32 m0, s41, 110592\n"
    "s_nop 0\n"
    "buffer_load_dwordx4 v0, s[8:11], s49 offen offset:0 nt lds\n"
    "buffer_load_dwordx4 v1, s[8:11], s49 offen offset:1024 nt lds\n"
    "buffer_load_dwordx4 v2, s[8:11], s49 offen offset:2048 nt lds\n"
    "s_mov_b32 s49, 3932160\n"
    "s_add_u32 m0, s41, 122880\n"
    "s_nop 0\n"
    "buffer_load_dwordx4 v0, s[8:11], s49 offen offset:0 nt lds\n"
    "buffer_load_dwordx4 v1, s[8:11], s49 offen offset:1024 nt lds\n"
    "buffer_load_dwordx4 v2, s[8:11], s49 offen offset:2048 nt lds\n"
    "s_mov_b32 s49, 5505024\n"
    "s_add_u32 m0, s41, 135168\n"
    "s_nop 0\n"
    "buffer_load_dwordx4 v0, s[8:11], s49 offen offset:0 nt lds\n"
    "buffer_load_dwordx4 v1, s[8:11], s49 offen offset:1024 nt lds\n"
    "buffer_load_dwordx4 v2, s[8:11], s49 offen offset:2048 nt lds\n"
    "s_waitcnt vmcnt(24)\n"
    "s_barrier\n"
    "v_add_u32 v8, 0, v3\n"
    "ds_read_b64_tr_b16 a[240:241], v8 offset:0\n"
    "ds_read_b64_tr_b16 a[242:243], v8 offset:768\n"
    "ds_read_b64_tr_b16 v[176:177], v8 offset:128\n"
    "ds_read_b64_tr_b16 v[178:179], v8 offset:896\n"
    "ds_read_b64_tr_b16 v[12:13], v8 offset:256\n"
    "ds_read_b64_tr_b16 v[14:15], v8 offset:1024\n"
    "v_add_u32 v9, 0, v4\n"
    "ds_read_b64_tr_b16 v[112:113], v9 offset:0\n"
    "ds_read_b64_tr_b16 v[114:115], v9 offset:768\n"
    "ds_read_b64_tr_b16 v[192:193], v9 offset:128\n"
    "ds_read_b64_tr_b16 v[194:195], v9 offset:896\n"
    "ds_read_b64_tr_b16 v[28:29], v9 offset:256\n"
    "ds_read_b64_tr_b16 v[30:31], v9 offset:1024\n"
    "v_add_u32 v10, 0, v5\n"
    "ds_read_b64_tr_b16 v[128:129], v10 offset:0\n"
    "ds_read_b64_tr_b16 v[130:131], v10 offset:768\n"
    "ds_read_b64_tr_b16 v[208:209], v10 offset:128\n"
    "ds_read_b64_tr_b16 v[210:211], v10 offset:896\n"
    "ds_read_b64_tr_b16 v[44:45], v10 offset:256\n"
    "ds_read_b64_tr_b16 v[46:47], v10 offset:1024\n"
    "v_add_u32 v11, 0, v6\n"
    "ds_read_b64_tr_b16 v[144:145], v11 offset:0\n"
    "ds_read_b64_tr_b16 v[146:147], v11 offset:768\n"
    "ds_read_b64_tr_b16 v[224:225], v11 offset:128\n"
    "ds_read_b64_tr_b16 v[226:227], v11 offset:896\n"
    "ds_read_b64_tr_b16 v[60:61], v11 offset:256\n"
    "ds_read_b64_tr_b16 v[62:63], v11 offset:1024\n"
    "s_waitcnt lgkmcnt(0)\n"
    "s_barrier\n"
    "s_mov_b32 s49, 1179648\n"
    "s_add_u32 m0, s41, 0\n"
    "s_nop 0\n"
    "buffer_load_dwordx4 v0, s[8:11], s49 offen offset:0 nt lds\n"
    "buffer_load_dwordx4 v1, s[8:11], s49 offen offset:1024 nt lds\n"
    "buffer_load_dwordx4 v2, s[8:11], s49 offen offset:2048 nt lds\n"
    "s_mov_b32 s49, 2752512\n"
    "s_add_u32 m0, s41, 12288\n"
    "s_nop 0\n"
    "buffer_load_dwordx4 v0, s[8:11], s49 offen offset:0 nt lds\n"
    "buffer_load_dwordx4 v1, s[8:11], s49 offen offset:1024 nt lds\n"
    "buffer_load_dwordx4 v2, s[8:11], s49 offen offset:2048 nt lds\n"
    "s_mov_b32 s49, 4325376\n"
    "s_add_u32 m0, s41, 24576\n"
    "s_nop 0\n"
    "buffer_load_dwordx4 v0, s[8:11], s49 offen offset:0 nt lds\n"
    "buffer_load_dwordx4 v1, s[8:11], s49 offen offset:1024 nt lds\n"
    "buffer_load_dwordx4 v2, s[8:11], s49 offen offset:2048 nt lds\n"
    "s_mov_b32 s49, 5898240\n"
    "s_add_u32 m0, s41, 36864\n"
    "s_nop 0\n"
    "buffer_load_dwordx4 v0, s[8:11], s49 offen offset:0 nt lds\n"
    "buffer_load_dwordx4 v1, s[8:11], s49 offen offset:1024 nt lds\n"
    "buffer_load_dwordx4 v2, s[8:11], s49 offen offset:2048 nt lds\n"
    "s_waitcnt vmcnt(24)\n"
    "s_barrier\n"
    "v_add_u32 v8, 49152, v3\n"
    "ds_read_b64_tr_b16 a[244:245], v8 offset:0\n"
    "ds_read_b64_tr_b16 a[246:247], v8 offset:768\n"
    "ds_read_b64_tr_b16 v[180:181], v8 offset:128\n"
    "ds_read_b64_tr_b16 v[182:183], v8 offset:896\n"
    "ds_read_b64_tr_b16 v[16:17], v8 offset:256\n"
    "ds_read_b64_tr_b16 v[18:19], v8 offset:1024\n"
    "v_add_u32 v9, 49152, v4\n"
    "ds_read_b64_tr_b16 v[116:117], v9 offset:0\n"
    "ds_read_b64_tr_b16 v[118:119], v9 offset:768\n"
    "ds_read_b64_tr_b16 v[196:197], v9 offset:128\n"
    "ds_read_b64_tr_b16 v[198:199], v9 offset:896\n"
    "ds_read_b64_tr_b16 v[32:33], v9 offset:256\n"
    "ds_read_b64_tr_b16 v[34:35], v9 offset:1024\n"
    "v_add_u32 v10, 49152, v5\n"
    "ds_read_b64_tr_b16 v[132:133], v10 offset:0\n"
    "ds_read_b64_tr_b16 v[134:135], v10 offset:768\n"
    "ds_read_b64_tr_b16 v[212:213], v10 offset:128\n"
    "ds_read_b64_tr_b16 v[214:215], v10 offset:896\n"
    "ds_read_b64_tr_b16 v[48:49], v10 offset:256\n"
    "ds_read_b64_tr_b16 v[50:51], v10 offset:1024\n"
    "v_add_u32 v11, 49152, v6\n"
    "ds_read_b64_tr_b16 v[148:149], v11 offset:0\n"
    "ds_read_b64_tr_b16 v[150:151], v11 offset:768\n"
    "ds_read_b64_tr_b16 v[228:229], v11 offset:128\n"
    "ds_read_b64_tr_b16 v[230:231], v11 offset:896\n"
    "ds_read_b64_tr_b16 v[64:65], v11 offset:256\n"
    "ds_read_b64_tr_b16 v[66:67], v11 offset:1024\n"
    "s_waitcnt lgkmcnt(0)\n"
    "s_barrier\n"
    "s_mov_b32 s49, 6291456\n"
    "s_add_u32 m0, s41, 49152\n"
    "s_nop 0\n"
    "buffer_load_dwordx4 v0, s[8:11], s49 offen offset:0 nt lds\n"
    "buffer_load_dwordx4 v1, s[8:11], s49 offen offset:1024 nt lds\n"
    "buffer_load_dwordx4 v2, s[8:11], s49 offen offset:2048 nt lds\n"
    "s_mov_b32 s49, 6684672\n"
    "s_add_u32 m0, s41, 61440\n"
    "s_nop 0\n"
    "buffer_load_dwordx4 v0, s[8:11], s49 offen offset:0 nt lds\n"
    "buffer_load_dwordx4 v1, s[8:11], s49 offen offset:1024 nt lds\n"
    "buffer_load_dwordx4 v2, s[8:11], s49 offen offset:2048 nt lds\n"
    "s_mov_b32 s49, 7077888\n"
    "s_add_u32 m0, s41, 73728\n"
    "s_nop 0\n"
    "buffer_load_dwordx4 v0, s[8:11], s49 offen offset:0 nt lds\n"
    "buffer_load_dwordx4 v1, s[8:11], s49 offen offset:1024 nt lds\n"
    "buffer_load_dwordx4 v2, s[8:11], s49 offen offset:2048 nt lds\n"
    "s_mov_b32 s49, 7471104\n"
    "s_add_u32 m0, s41, 86016\n"
    "s_nop 0\n"
    "buffer_load_dwordx4 v0, s[8:11], s49 offen offset:0 nt lds\n"
    "buffer_load_dwordx4 v1, s[8:11], s49 offen offset:1024 nt lds\n"
    "buffer_load_dwordx4 v2, s[8:11], s49 offen offset:2048 nt lds\n"
    "s_waitcnt vmcnt(24)\n"
    "s_barrier\n"
    "v_add_u32 v8, 98304, v3\n"
    "ds_read_b64_tr_b16 a[248:249], v8 offset:0\n"
    "ds_read_b64_tr_b16 a[250:251], v8 offset:768\n"
    "ds_read_b64_tr_b16 v[184:185], v8 offset:128\n"
    "ds_read_b64_tr_b16 v[186:187], v8 offset:896\n"
    "ds_read_b64_tr_b16 v[20:21], v8 offset:256\n"
    "ds_read_b64_tr_b16 v[22:23], v8 offset:1024\n"
    "v_add_u32 v9, 98304, v4\n"
    "ds_read_b64_tr_b16 v[120:121], v9 offset:0\n"
    "ds_read_b64_tr_b16 v[122:123], v9 offset:768\n"
    "ds_read_b64_tr_b16 v[200:201], v9 offset:128\n"
    "ds_read_b64_tr_b16 v[202:203], v9 offset:896\n"
    "ds_read_b64_tr_b16 v[36:37], v9 offset:256\n"
    "ds_read_b64_tr_b16 v[38:39], v9 offset:1024\n"
    "v_add_u32 v10, 98304, v5\n"
    "ds_read_b64_tr_b16 v[136:137], v10 offset:0\n"
    "ds_read_b64_tr_b16 v[138:139], v10 offset:768\n"
    "ds_read_b64_tr_b16 v[216:217], v10 offset:128\n"
    "ds_read_b64_tr_b16 v[218:219], v10 offset:896\n"
    "ds_read_b64_tr_b16 v[52:53], v10 offset:256\n"
    "ds_read_b64_tr_b16 v[54:55], v10 offset:1024\n"
    "v_add_u32 v11, 98304, v6\n"
    "ds_read_b64_tr_b16 v[152:153], v11 offset:0\n"
    "ds_read_b64_tr_b16 v[154:155], v11 offset:768\n"
    "ds_read_b64_tr_b16 v[232:233], v11 offset:128\n"
    "ds_read_b64_tr_b16 v[234:235], v11 offset:896\n"
    "ds_read_b64_tr_b16 v[68:69], v11 offset:256\n"
    "ds_read_b64_tr_b16 v[70:71], v11 offset:1024\n"
    "s_waitcnt lgkmcnt(0)\n"
    "s_barrier\n"
    "s_waitcnt vmcnt(12)\n"
    "s_barrier\n"
    "v_add_u32 v8, 0, v3\n"
    "ds_read_b64_tr_b16 a[252:253], v8 offset:0\n"
    "ds_read_b64_tr_b16 a[254:255], v8 offset:768\n"
    "ds_read_b64_tr_b16 v[188:189], v8 offset:128\n"
    "ds_read_b64_tr_b16 v[190:191], v8 offset:896\n"
    "ds_read_b64_tr_b16 v[24:25], v8 offset:256\n"
    "ds_read_b64_tr_b16 v[26:27], v8 offset:1024\n"
    "v_add_u32 v9, 0, v4\n"
    "ds_read_b64_tr_b16 v[124:125], v9 offset:0\n"
    "ds_read_b64_tr_b16 v[126:127], v9 offset:768\n"
    "ds_read_b64_tr_b16 v[204:205], v9 offset:128\n"
    "ds_read_b64_tr_b16 v[206:207], v9 offset:896\n"
    "ds_read_b64_tr_b16 v[40:41], v9 offset:256\n"
    "ds_read_b64_tr_b16 v[42:43], v9 offset:1024\n"
    "v_add_u32 v10, 0, v5\n"
    "ds_read_b64_tr_b16 v[140:141], v10 offset:0\n"
    "ds_read_b64_tr_b16 v[142:143], v10 offset:768\n"
    "ds_read_b64_tr_b16 v[220:221], v10 offset:128\n"
    "ds_read_b64_tr_b16 v[222:223], v10 offset:896\n"
    "ds_read_b64_tr_b16 v[56:57], v10 offset:256\n"
    "ds_read_b64_tr_b16 v[58:59], v10 offset:1024\n"
    "v_add_u32 v11, 0, v6\n"
    "ds_read_b64_tr_b16 v[156:157], v11 offset:0\n"
    "ds_read_b64_tr_b16 v[158:159], v11 offset:768\n"
    "ds_read_b64_tr_b16 v[236:237], v11 offset:128\n"
    "ds_read_b64_tr_b16 v[238:239], v11 offset:896\n"
    "ds_read_b64_tr_b16 v[72:73], v11 offset:256\n"
    "ds_read_b64_tr_b16 v[74:75], v11 offset:1024\n"
    "s_waitcnt lgkmcnt(0)\n"
    "s_barrier\n"
    "s_waitcnt vmcnt(0)\n"
    "s_barrier\n"
    "v_add_u32 v8, 49152, v7\n"
    "ds_read_b64_tr_b16 v[160:161], v8 offset:0\n"
    "ds_read_b64_tr_b16 v[162:163], v8 offset:768\n"
    "ds_read_b64_tr_b16 v[240:241], v8 offset:128\n"
    "ds_read_b64_tr_b16 v[242:243], v8 offset:896\n"
    "ds_read_b64_tr_b16 v[76:77], v8 offset:256\n"
    "ds_read_b64_tr_b16 v[78:79], v8 offset:1024\n"
    "v_add_u32 v9, 61440, v7\n"
    "ds_read_b64_tr_b16 v[164:165], v9 offset:0\n"
    "ds_read_b64_tr_b16 v[166:167], v9 offset:768\n"
    "ds_read_b64_tr_b16 v[244:245], v9 offset:128\n"
    "ds_read_b64_tr_b16 v[246:247], v9 offset:896\n"
    "ds_read_b64_tr_b16 v[80:81], v9 offset:256\n"
    "ds_read_b64_tr_b16 v[82:83], v9 offset:1024\n"
    "v_add_u32 v10, 73728, v7\n"
    "ds_read_b64_tr_b16 v[168:169], v10 offset:0\n"
    "ds_read_b64_tr_b16 v[170:171], v10 offset:768\n"
    "ds_read_b64_tr_b16 v[248:249], v10 offset:128\n"
    "ds_read_b64_tr_b16 v[250:251], v10 offset:896\n"
    "ds_read_b64_tr_b16 v[84:85], v10 offset:256\n"
    "ds_read_b64_tr_b16 v[86:87], v10 offset:1024\n"
    "v_add_u32 v11, 86016, v7\n"
    "ds_read_b64_tr_b16 v[172:173], v11 offset:0\n"
    "ds_read_b64_tr_b16 v[174:175], v11 offset:768\n"
    "ds_read_b64_tr_b16 v[252:253], v11 offset:128\n"
    "ds_read_b64_tr_b16 v[254:255], v11 offset:896\n"
    "ds_read_b64_tr_b16 v[88:89], v11 offset:256\n"
    "ds_read_b64_tr_b16 v[90:91], v11 offset:1024\n"
    "s_waitcnt lgkmcnt(0)\n"
    "s_barrier\n"
    "ds_write_b128 v97, v[12:15] offset:0\n"
    "ds_write_b128 v97, v[16:19] offset:1024\n"
    "ds_write_b128 v97, v[20:23] offset:2048\n"
    "ds_write_b128 v97, v[24:27] offset:3072\n"
    "ds_write_b128 v97, v[28:31] offset:4096\n"
    "ds_write_b128 v97, v[32:35] offset:5120\n"
    "ds_write_b128 v97, v[36:39] offset:6144\n"
    "ds_write_b128 v97, v[40:43] offset:7168\n"
    "ds_write_b128 v97, v[44:47] offset:8192\n"
    "ds_write_b128 v97, v[48:51] offset:9216\n"
    "ds_write_b128 v97, v[52:55] offset:10240\n"
    "ds_write_b128 v97, v[56:59] offset:11264\n"
    "ds_write_b128 v97, v[60:63] offset:12288\n"
    "ds_write_b128 v97, v[64:67] offset:13312\n"
    "ds_write_b128 v97, v[68:71] offset:14336\n"
    "ds_write_b128 v97, v[72:75] offset:15360\n"
    "ds_write_b128 v97, v[76:79] offset:16384\n"
    "ds_write_b128 v97, v[80:83] offset:17408\n"
    "ds_write_b128 v97, v[84:87] offset:18432\n"
    "ds_write_b128 v97, v[88:91] offset:19456\n"
    "s_mov_b32 s34, -4\n"
    "s_lshl_b32 s48, s12, 11\n"
    "v_lshrrev_b32 v4, 2, v96\n"
    ".Lvo_po2_%=:\n"
    "buffer_load_dword v8, v4, s[0:3], s21 offen sc1\n"
    "s_waitcnt vmcnt(0)\n"
    "v_cmp_gt_i32 vcc, 1, v8\n"
    "s_nop 0\n"
    "s_cbranch_vccz .Lvo_pd3_%=\n"
    "s_add_u32 s28, s28, 1\n"
    "s_cmp_lt_u32 s28, 0x40000\n"
    "s_cbranch_scc1 .Lvo_sp4_%=\n"
    "v_mov_b32 v8, 30\n"
    "s_mov_b32 s32, 33728\n"
    "buffer_store_dword v8, off, s[0:3], s32 sc1\n"
    "s_waitcnt vmcnt(0)\n"
    "s_branch .Lvo_pd3_%=\n"
    ".Lvo_sp4_%=:\n"
    "s_sleep 4\n"
    "s_branch .Lvo_po2_%=\n"
    ".Lvo_pd3_%=:\n"
    ".Lvo_po5_%=:\n"
    "buffer_load_dword v8, v105, s[0:3], s22 offen sc1\n"
    "s_waitcnt vmcnt(0)\n"
    "v_cmp_gt_i32 vcc, s14, v8\n"
    "s_nop 0\n"
    "s_cbranch_vccz .Lvo_pd6_%=\n"
    "s_add_u32 s28, s28, 1\n"
    "s_cmp_lt_u32 s28, 0x40000\n"
    "s_cbranch_scc1 .Lvo_sp7_%=\n"
    "v_mov_b32 v8, 31\n"
    "s_mov_b32 s32, 33728\n"
    "buffer_store_dword v8, off, s[0:3], s32 sc1\n"
    "s_waitcnt vmcnt(0)\n"
    "s_branch .Lvo_pd6_%=\n"
    ".Lvo_sp7_%=:\n"
    "s_sleep 4\n"
    "s_branch .Lvo_po5_%=\n"
    ".Lvo_pd6_%=:\n"
    "s_add_u32 s43, s17, s13\n"
    "s_add_u32 m0, s48, 0\n"
    "s_nop 0\n"
    "buffer_load_dwordx4 v96, s[0:3], s43 offen sc1 lds\n"
    "buffer_load_dwordx4 v96, s[0:3], s43 offen offset:1024 sc1 lds\n"
    "s_add_u32 m0, s48, 13312\n"
    "s_add_u32 s43, s43, 0x4000\n"
    "buffer_load_dwordx4 v96, s[0:3], s43 offen sc1 lds\n"
    "buffer_load_dwordx4 v96, s[0:3], s43 offen offset:1024 sc1 lds\n"
    "s_add_u32 m0, s48, 26624\n"
    "s_add_u32 s43, s43, 0x4000\n"
    "buffer_load_dwordx4 v96, s[0:3], s43 offen sc1 lds\n"
    "buffer_load_dwordx4 v96, s[0:3], s43 offen offset:1024 sc1 lds\n"
    "s_add_u32 m0, s48, 39936\n"
    "s_add_u32 s43, s43, 0x4000\n"
    "buffer_load_dwordx4 v96, s[0:3], s43 offen sc1 lds\n"
    "buffer_load_dwordx4 v96, s[0:3], s43 offen offset:1024 sc1 lds\n"
    "s_add_u32 m0, s48, 53248\n"
    "s_add_u32 s43, s43, 0x4000\n"
    "buffer_load_dwordx4 v96, s[0:3], s43 offen sc1 lds\n"
    "buffer_load_dwordx4 v96, s[0:3], s43 offen offset:1024 sc1 lds\n"
    "s_memrealtime s[30:31]\n"
    "s_waitcnt vmcnt(0) lgkmcnt(0)\n"
    "s_sub_u32 s46, s30, 1520\n"
    "s_mov_b32 s34, -3\n"
    "s_mov_b32 s35, -5\n"
    "s_mov_b32 s45, s20\n"
    "s_mov_b32 s38, 2130706432\n"
    "s_mov_b32 s39, 2130706432\n"
    "s_branch .Lvo_vph_%=\n"
    ".Lvo_take_%=:\n"
    "s_waitcnt vmcnt(4) lgkmcnt(0)\n"
    "ds_write_b128 v92, v[72:75] offset:8192\n"
    "v_smfmac_f32_16x16x64_bf16 v[84:87], v[72:75], v[0:7], v98\n"
    "s_waitcnt vmcnt(3)\n"
    "ds_write_b128 v92, v[76:79] offset:9216\n"
    "v_max3_u32 v108, v72, v74, v76\n"
    "s_waitcnt vmcnt(2)\n"
    "ds_write_b128 v92, v[80:83] offset:10240\n"
    "v_max3_u32 v108, v108, v78, v80\n"
    "v_smfmac_f32_16x16x64_bf16 v[64:67], v[72:75], a[240:247], v98\n"
    "v_max_u32 v108, v108, v82\n"
    "v_cmp_eq_u32 vcc, -1, v108\n"
    "s_nop 0\n"
    "s_cbranch_vccnz .Lvo_retry_%=\n"
    ".Lvo_go_%=:\n"
    "s_waitcnt lgkmcnt(0)\n"
    "s_barrier\n"
    "ds_read_b128 v[88:91], v93 offset:8192\n"
    "ds_read_b128 v[80:83], v93 offset:9216\n"
    "v_smfmac_f32_16x16x64_bf16 v[68:71], v[72:75], v[176:183], v98\n"
    "ds_read_b128 v[72:75], v94 offset:8192\n"
    "v_smfmac_f32_16x16x64_bf16 v[84:87], v[76:79], v[8:15], v98\n"
    "s_waitcnt vmcnt(0)\n"
    "v_cmp_gt_i32 vcc, s50, v110\n"
    "v_smfmac_f32_16x16x64_bf16 v[64:67], v[76:79], a[248:255], v98\n"
    "s_cbranch_vccnz .Lvo_wq_%=\n"
    ".Lvo_wqb_%=:\n"
    "buffer_load_dwordx4 v[32:35], v96, s[0:3], s44 offen offset:0 sc1\n"
    "v_smfmac_f32_16x16x64_bf16 v[68:71], v[76:79], v[184:191], v98\n"
    "ds_read_b128 v[76:79], v94 offset:9216\n"
    "s_waitcnt lgkmcnt(3)\n"
    "v_smfmac_f32_16x16x64_bf16 v[84:87], v[88:91], v[16:23], v98\n"
    "ds_read_b128 v[0:3], v97 offset:12288\n"
    "v_smfmac_f32_16x16x64_bf16 v[64:67], v[88:91], v[112:119], v98\n"
    "ds_read_b128 v[4:7], v97 offset:13312\n"
    "v_smfmac_f32_16x16x64_bf16 v[68:71], v[88:91], v[192:199], v98\n"
    "ds_read_b128 v[88:91], v95 offset:8192\n"
    "s_waitcnt lgkmcnt(5)\n"
    "v_smfmac_f32_16x16x64_bf16 v[84:87], v[80:83], v[24:31], v98\n"
    "ds_read_b128 v[8:11], v97 offset:14336\n"
    "buffer_load_dwordx4 v[36:39], v96, s[0:3], s44 offen offset:1024 sc1\n"
    "v_smfmac_f32_16x16x64_bf16 v[64:67], v[80:83], v[120:127], v98\n"
    "ds_read_b128 v[12:15], v97 offset:15360\n"
    "v_smfmac_f32_16x16x64_bf16 v[68:71], v[80:83], v[200:207], v98\n"
    "ds_read_b128 v[80:83], v95 offset:9216\n"
    "s_waitcnt lgkmcnt(7)\n"
    "v_smfmac_f32_16x16x64_bf16 v[84:87], v[72:75], v[48:55], v98\n"
    "ds_read_b128 v[16:19], v97 offset:16384\n"
    "v_smfmac_f32_16x16x64_bf16 v[64:67], v[72:75], v[128:135], v98\n"
    "ds_read_b128 v[20:23], v97 offset:17408\n"
    "buffer_load_dwordx4 v[40:43], v96, s[0:3], s44 offen offset:2048 sc1\n"
    "v_smfmac_f32_16x16x64_bf16 v[68:71], v[72:75], v[208:215], v98\n"
    "ds_read_b128 v[72:75], v96 offset:10240\n"
    "s_waitcnt lgkmcnt(9)\n"
    "v_smfmac_f32_16x16x64_bf16 v[84:87], v[76:79], v[56:63], v98\n"
    "ds_read_b128 v[24:27], v97 offset:18432\n"
    "v_smfmac_f32_16x16x64_bf16 v[64:67], v[76:79], v[136:143], v98\n"
    "ds_read_b128 v[28:31], v97 offset:19456\n"
    "v_mov_b64 v[48:49], 0\n"
    "v_smfmac_f32_16x16x64_bf16 v[68:71], v[76:79], v[216:223], v98\n"
    "ds_read_b128 v[76:79], v96 offset:23552\n"
    "s_waitcnt lgkmcnt(9)\n"
    "v_smfmac_f32_16x16x64_bf16 v[84:87], v[88:91], v[0:7], v98\n"
    "buffer_load_dwordx4 v[44:47], v96, s[0:3], s44 offen offset:3072 sc1\n"
    "v_cmp_gt_i32 vcc, s51, v111\n"
    "v_smfmac_f32_16x16x64_bf16 v[64:67], v[88:91], v[144:151], v98\n"
    "s_cbranch_vccnz .Lvo_wx_%=\n"
    ".Lvo_wxb_%=:\n"
    "s_add_u32 m0, s48, 0\n"
    "v_smfmac_f32_16x16x64_bf16 v[68:71], v[88:91], v[224:231], v98\n"
    "s_waitcnt lgkmcnt(3)\n"
    "v_mov_b64 v[50:51], 0\n"
    "v_smfmac_f32_16x16x64_bf16 v[84:87], v[80:83], v[8:15], v98\n"
    "v_mov_b64 v[52:53], 0\n"
    "v_mov_b64 v[54:55], 0\n"
    "v_smfmac_f32_16x16x64_bf16 v[84:87], v[72:75], v[16:23], v98\n"
    "s_waitcnt lgkmcnt(0)\n"
    "buffer_load_dwordx4 v96, s[0:3], s43 offen sc1 lds\n"
    "v_smfmac_f32_16x16x64_bf16 v[84:87], v[76:79], v[24:31], v98\n"
    "v_mov_b64 v[56:57], 0\n"
    "v_mov_b64 v[58:59], 0\n"
    "v_smfmac_f32_16x16x64_bf16 v[64:67], v[80:83], v[152:159], v98\n"
    "v_mov_b64 v[60:61], 0\n"
    "v_mov_b64 v[62:63], 0\n"
    "v_smfmac_f32_16x16x64_bf16 v[64:67], v[72:75], v[160:167], v98\n"
    "v_mov_b64 v[24:25], 0\n"
    "v_mov_b64 v[26:27], 0\n"
    "v_smfmac_f32_16x16x64_bf16 v[64:67], v[76:79], v[168:175], v98\n"
    "buffer_load_dwordx4 v96, s[0:3], s43 offen offset:1024 sc1 lds\n"
    "v_mov_b64 v[28:29], 0\n"
    "v_smfmac_f32_16x16x64_bf16 v[68:71], v[80:83], v[232:239], v98\n"
    "v_mov_b64 v[30:31], 0\n"
    "v_smfmac_f32_16x16x64_bf16 v[68:71], v[72:75], v[240:247], v98\n"
    "v_smfmac_f32_16x16x64_bf16 v[68:71], v[76:79], v[248:255], v98\n"
    "v_pk_add_f32 v[84:85], v[84:85], v[86:87]\n"
    "v_cvt_pk_bf16_f32 v84, v84, v85\n"
    "ds_write_b32 v100, v84 offset:0\n"
    "v_pk_add_f32 v[64:65], v[64:65], v[66:67]\n"
    "v_cvt_pk_bf16_f32 v64, v64, v65\n"
    "ds_write_b32 v99, v64 offset:0\n"
    "s_nop 1\n"
    "v_pk_add_f32 v[68:69], v[68:69], v[70:71]\n"
    "v_cvt_pk_bf16_f32 v68, v68, v69\n"
    "ds_write_b32 v99, v68 offset:256\n"
    "s_waitcnt lgkmcnt(0)\n"
    "s_barrier\n"
    "ds_read_b64_tr_b16 v[72:73], v101 offset:0\n"
    "ds_read_b64_tr_b16 v[74:75], v101 offset:64\n"
    "ds_read_b64_tr_b16 v[76:77], v101 offset:1024\n"
    "ds_read_b64_tr_b16 v[78:79], v101 offset:1088\n"
    "ds_read_b64_tr_b16 v[80:81], v101 offset:2048\n"
    "ds_read_b64_tr_b16 v[82:83], v101 offset:2112\n"
    "v_mov_b64 v[64:65], 0\n"
    "v_mov_b64 v[66:67], 0\n"
    "v_mov_b64 v[68:69], 0\n"
    "v_mov_b64 v[70:71], 0\n"
    "v_mov_b64 v[84:85], 0\n"
    "v_mov_b64 v[86:87], 0\n"
    "v_mov_b64 v[88:89], 0\n"
    "v_mov_b64 v[90:91], 0\n"
    "s_waitcnt lgkmcnt(4)\n"
    "v_smfmac_f32_16x16x64_bf16 v[24:27], v[72:75], a[192:199], v98\n"
    "s_add_u32 m0, s48, 13312\n"
    "s_add_u32 s43, s43, 0x4000\n"
    "v_smfmac_f32_16x16x64_bf16 v[28:31], v[72:75], a[216:223], v98\n"
    "s_waitcnt lgkmcnt(2)\n"
    "buffer_load_dwordx4 v96, s[0:3], s43 offen sc1 lds\n"
    "v_smfmac_f32_16x16x64_bf16 v[24:27], v[76:79], a[200:207], v98\n"
    "ds_read_b128 v[0:3], v96 offset:11264\n"
    "v_smfmac_f32_16x16x64_bf16 v[28:31], v[76:79], a[224:231], v98\n"
    "s_waitcnt lgkmcnt(1)\n"
    "ds_read_b128 v[4:7], v96 offset:12288\n"
    "v_smfmac_f32_16x16x64_bf16 v[24:27], v[80:83], a[208:215], v98\n"
    "ds_read_b128 v[8:11], v96 offset:24576\n"
    "v_smfmac_f32_16x16x64_bf16 v[28:31], v[80:83], a[232:239], v98\n"
    "buffer_load_dwordx4 v96, s[0:3], s43 offen offset:1024 sc1 lds\n"
    "s_add_u32 m0, s48, 26624\n"
    "v_smfmac_f32_16x16x64_bf16 v[48:51], v[72:75], a[0:7], v98\n"
    "s_add_u32 s43, s43, 0x4000\n"
    "ds_read_b128 v[12:15], v96 offset:25600\n"
    "v_smfmac_f32_16x16x64_bf16 v[48:51], v[76:79], a[8:15], v98\n"
    "ds_read_b128 v[16:19], v96 offset:37888\n"
    "v_smfmac_f32_16x16x64_bf16 v[48:51], v[80:83], a[16:23], v98\n"
    "ds_read_b128 v[20:23], v96 offset:38912\n"
    "v_smfmac_f32_16x16x64_bf16 v[52:55], v[72:75], a[24:31], v98\n"
    "buffer_load_dwordx4 v96, s[0:3], s43 offen sc1 lds\n"
    "v_smfmac_f32_16x16x64_bf16 v[52:55], v[76:79], a[32:39], v98\n"
    "v_smfmac_f32_16x16x64_bf16 v[52:55], v[80:83], a[40:47], v98\n"
    "v_smfmac_f32_16x16x64_bf16 v[56:59], v[72:75], a[48:55], v98\n"
    "v_add_f32 v24, v24, v26\n"
    "v_add_f32 v25, v25, v27\n"
    "v_smfmac_f32_16x16x64_bf16 v[56:59], v[76:79], a[56:63], v98\n"
    "v_cvt_pk_bf16_f32 v108, v24, v25\n"
    "v_add_f32 v28, v28, v30\n"
    "v_smfmac_f32_16x16x64_bf16 v[56:59], v[80:83], a[64:71], v98\n"
    "v_add_f32 v29, v29, v31\n"
    "v_cvt_pk_bf16_f32 v109, v28, v29\n"
    "v_smfmac_f32_16x16x64_bf16 v[60:63], v[72:75], a[72:79], v98\n"
    "v_smfmac_f32_16x16x64_bf16 v[60:63], v[76:79], a[80:87], v98\n"
    "v_permlane16_swap_b32 v108, v109\n"
    "v_smfmac_f32_16x16x64_bf16 v[60:63], v[80:83], a[88:95], v98\n"
    "v_min_u32 v108, -2, v108\n"
    "buffer_store_dwordx2 v[108:109], v103, s[0:3], s36 offen sc1\n"
    "v_smfmac_f32_16x16x64_bf16 v[64:67], v[72:75], a[96:103], v98\n"
    "v_add_f32 v48, v48, v50\n"
    "v_add_f32 v49, v49, v51\n"
    "v_smfmac_f32_16x16x64_bf16 v[64:67], v[76:79], a[104:111], v98\n"
    "v_cvt_pk_bf16_f32 v48, v48, v49\n"
    "v_add_f32 v52, v52, v54\n"
    "v_smfmac_f32_16x16x64_bf16 v[64:67], v[80:83], a[112:119], v98\n"
    "v_add_f32 v53, v53, v55\n"
    "v_cvt_pk_bf16_f32 v49, v52, v53\n"
    "v_smfmac_f32_16x16x64_bf16 v[68:71], v[72:75], a[120:127], v98\n"
    "v_add_f32 v56, v56, v58\n"
    "v_add_f32 v57, v57, v59\n"
    "v_smfmac_f32_16x16x64_bf16 v[68:71], v[76:79], a[128:135], v98\n"
    "v_cvt_pk_bf16_f32 v50, v56, v57\n"
    "ds_read_b128 v[24:27], v96 offset:51200\n"
    "v_smfmac_f32_16x16x64_bf16 v[68:71], v[80:83], a[136:143], v98\n"
    "ds_read_b128 v[28:31], v96 offset:52224\n"
    "v_smfmac_f32_16x16x64_bf16 v[84:87], v[72:75], a[144:151], v98\n"
    "v_permlane32_swap_b32 v48, v50\n"
    "v_smfmac_f32_16x16x64_bf16 v[84:87], v[76:79], a[152:159], v98\n"
    "v_add_f32 v60, v60, v62\n"
    "v_add_f32 v61, v61, v63\n"
    "v_smfmac_f32_16x16x64_bf16 v[84:87], v[80:83], a[160:167], v98\n"
    "v_cvt_pk_bf16_f32 v51, v60, v61\n"
    "buffer_load_dwordx4 v96, s[0:3], s43 offen offset:1024 sc1 lds\n"
    "v_smfmac_f32_16x16x64_bf16 v[88:91], v[72:75], a[168:175], v98\n"
    "v_smfmac_f32_16x16x64_bf16 v[88:91], v[76:79], a[176:183], v98\n"
    "v_permlane32_swap_b32 v49, v51\n"
    "v_smfmac_f32_16x16x64_bf16 v[88:91], v[80:83], a[184:191], v98\n"
    ".Lvo_uph_%=:\n"
    "s_waitcnt lgkmcnt(0)\n"
    "v_mfma_f32_16x16x32_bf16 v[56:59], v[0:3], a[0:3], 0\n"
    "v_permlane16_swap_b32 v48, v49\n"
    "v_mfma_f32_16x16x32_bf16 v[60:63], v[4:7], a[0:3], 0\n"
    "v_permlane16_swap_b32 v50, v51\n"
    "v_mfma_f32_16x16x32_bf16 v[56:59], v[8:11], a[4:7], v[56:59]\n"
    "v_min_u32 v48, -2, v48\n"
    "v_min_u32 v50, -2, v50\n"
    "v_mfma_f32_16x16x32_bf16 v[60:63], v[12:15], a[4:7], v[60:63]\n"
    "buffer_store_dwordx4 v[48:51], v102, s[0:3], s36 offen sc1\n"
    "v_add_f32 v64, v64, v66\n"
    "v_mfma_f32_16x16x32_bf16 v[56:59], v[16:19], a[8:11], v[56:59]\n"
    "v_add_f32 v65, v65, v67\n"
    "v_cvt_pk_bf16_f32 v64, v64, v65\n"
    "v_mfma_f32_16x16x32_bf16 v[60:63], v[20:23], a[8:11], v[60:63]\n"
    "v_add_f32 v68, v68, v70\n"
    "v_add_f32 v69, v69, v71\n"
    "v_mfma_f32_16x16x32_bf16 v[56:59], v[24:27], a[12:15], v[56:59]\n"
    "v_cvt_pk_bf16_f32 v65, v68, v69\n"
    "v_add_f32 v84, v84, v86\n"
    "s_waitcnt vmcnt(8)\n"
    "v_swap_b32 v34, v36\n"
    "v_swap_b32 v35, v37\n"
    "v_swap_b32 v42, v44\n"
    "v_swap_b32 v43, v45\n"
    "v_mfma_f32_16x16x32_bf16 v[60:63], v[28:31], a[12:15], v[60:63]\n"
    "v_add_f32 v85, v85, v87\n"
    "v_cvt_pk_bf16_f32 v66, v84, v85\n"
    "v_mfma_f32_16x16x32_bf16 v[56:59], v[32:35], a[16:19], v[56:59]\n"
    "s_add_u32 m0, s48, 39936\n"
    "s_add_u32 s43, s43, 0x4000\n"
    "v_mfma_f32_16x16x32_bf16 v[60:63], v[36:39], a[16:19], v[60:63]\n"
    "v_permlane32_swap_b32 v64, v66\n"
    "v_mfma_f32_16x16x32_bf16 v[56:59], v[40:43], a[20:23], v[56:59]\n"
    "v_add_f32 v88, v88, v90\n"
    "v_add_f32 v89, v89, v91\n"
    "v_mfma_f32_16x16x32_bf16 v[60:63], v[44:47], a[20:23], v[60:63]\n"
    "v_cvt_pk_bf16_f32 v67, v88, v89\n"
    "buffer_load_dwordx4 v96, s[0:3], s43 offen sc1 lds\n"
    "v_mfma_f32_16x16x32_bf16 v[48:51], v[0:3], a[24:27], 0\n"
    "v_mfma_f32_16x16x32_bf16 v[52:55], v[4:7], a[24:27], 0\n"
    "v_permlane32_swap_b32 v65, v67\n"
    "v_mfma_f32_16x16x32_bf16 v[48:51], v[8:11], a[28:31], v[48:51]\n"
    "v_cvt_pk_bf16_f32 v72, v56, v57\n"
    "v_cvt_pk_bf16_f32 v74, v58, v59\n"
    "v_mfma_f32_16x16x32_bf16 v[52:55], v[12:15], a[28:31], v[52:55]\n"
    "v_permlane16_swap_b32 v64, v65\n"
    "v_mfma_f32_16x16x32_bf16 v[48:51], v[16:19], a[32:35], v[48:51]\n"
    "v_permlane16_swap_b32 v66, v67\n"
    "v_mfma_f32_16x16x32_bf16 v[52:55], v[20:23], a[32:35], v[52:55]\n"
    "v_min_u32 v64, -2, v64\n"
    "v_min_u32 v66, -2, v66\n"
    "v_mfma_f32_16x16x32_bf16 v[48:51], v[24:27], a[36:39], v[48:51]\n"
    "buffer_store_dwordx4 v[64:67], v102, s[0:3], s37 offen sc1\n"
    "v_cvt_pk_bf16_f32 v73, v60, v61\n"
    "v_mfma_f32_16x16x32_bf16 v[52:55], v[28:31], a[36:39], v[52:55]\n"
    "s_cmp_eq_u32 s34, 63\n"
    "s_cbranch_scc1 .Lvo_end_%=\n"
    "v_mfma_f32_16x16x32_bf16 v[48:51], v[32:35], a[40:43], v[48:51]\n"
    "v_cvt_pk_bf16_f32 v75, v62, v63\n"
    "v_mfma_f32_16x16x32_bf16 v[52:55], v[36:39], a[40:43], v[52:55]\n"
    "v_mfma_f32_16x16x32_bf16 v[48:51], v[40:43], a[44:47], v[48:51]\n"
    "v_mfma_f32_16x16x32_bf16 v[52:55], v[44:47], a[44:47], v[52:55]\n"
    "v_mfma_f32_16x16x32_bf16 v[56:59], v[0:3], a[48:51], 0\n"
    "v_mfma_f32_16x16x32_bf16 v[60:63], v[4:7], a[48:51], 0\n"
    "v_mfma_f32_16x16x32_bf16 v[56:59], v[8:11], a[52:55], v[56:59]\n"
    "buffer_load_dwordx4 v96, s[0:3], s43 offen offset:1024 sc1 lds\n"
    "s_add_u32 m0, s48, 53248\n"
    "v_mfma_f32_16x16x32_bf16 v[60:63], v[12:15], a[52:55], v[60:63]\n"
    "s_add_u32 s43, s43, 0x4000\n"
    "v_mfma_f32_16x16x32_bf16 v[56:59], v[16:19], a[56:59], v[56:59]\n"
    "v_mfma_f32_16x16x32_bf16 v[60:63], v[20:23], a[56:59], v[60:63]\n"
    "v_mfma_f32_16x16x32_bf16 v[56:59], v[24:27], a[60:63], v[56:59]\n"
    "v_cvt_pk_bf16_f32 v76, v48, v49\n"
    "v_cvt_pk_bf16_f32 v78, v50, v51\n"
    "v_mfma_f32_16x16x32_bf16 v[60:63], v[28:31], a[60:63], v[60:63]\n"
    "v_cvt_pk_bf16_f32 v77, v52, v53\n"
    "v_cvt_pk_bf16_f32 v79, v54, v55\n"
    "v_mfma_f32_16x16x32_bf16 v[56:59], v[32:35], a[64:67], v[56:59]\n"
    "buffer_load_dwordx4 v96, s[0:3], s43 offen sc1 lds\n"
    "v_mov_b64 v[88:89], -1\n"
    "v_mfma_f32_16x16x32_bf16 v[60:63], v[36:39], a[64:67], v[60:63]\n"
    "v_mov_b64 v[90:91], -1\n"
    "v_mfma_f32_16x16x32_bf16 v[56:59], v[40:43], a[68:71], v[56:59]\n"
    "v_mfma_f32_16x16x32_bf16 v[60:63], v[44:47], a[68:71], v[60:63]\n"
    "v_mfma_f32_16x16x32_bf16 v[48:51], v[0:3], a[72:75], 0\n"
    "buffer_load_dwordx4 v96, s[0:3], s43 offen offset:1024 sc1 lds\n"
    "v_mfma_f32_16x16x32_bf16 v[52:55], v[4:7], a[72:75], 0\n"
    "v_mfma_f32_16x16x32_bf16 v[48:51], v[8:11], a[76:79], v[48:51]\n"
    "v_mfma_f32_16x16x32_bf16 v[52:55], v[12:15], a[76:79], v[52:55]\n"
    "v_mfma_f32_16x16x32_bf16 v[48:51], v[16:19], a[80:83], v[48:51]\n"
    "buffer_store_dwordx4 v[72:75], v96, s[0:3], s40 offen sc1\n"
    "s_add_u32 s40, s40, 0x10000\n"
    "v_mfma_f32_16x16x32_bf16 v[52:55], v[20:23], a[80:83], v[52:55]\n"
    "v_mfma_f32_16x16x32_bf16 v[48:51], v[24:27], a[84:87], v[48:51]\n"
    "v_cvt_pk_bf16_f32 v80, v56, v57\n"
    "v_cvt_pk_bf16_f32 v82, v58, v59\n"
    "v_mfma_f32_16x16x32_bf16 v[52:55], v[28:31], a[84:87], v[52:55]\n"
    "v_cvt_pk_bf16_f32 v81, v60, v61\n"
    "v_cvt_pk_bf16_f32 v83, v62, v63\n"
    "v_mfma_f32_16x16x32_bf16 v[48:51], v[32:35], a[88:91], v[48:51]\n"
    "v_mfma_f32_16x16x32_bf16 v[52:55], v[36:39], a[88:91], v[52:55]\n"
    "v_mfma_f32_16x16x32_bf16 v[48:51], v[40:43], a[92:95], v[48:51]\n"
    "v_mfma_f32_16x16x32_bf16 v[52:55], v[44:47], a[92:95], v[52:55]\n"
    "v_mfma_f32_16x16x32_bf16 v[56:59], v[0:3], a[96:99], 0\n"
    "buffer_store_dwordx4 v[76:79], v96, s[0:3], s40 offen sc1\n"
    "s_add_u32 s40, s40, 0x10000\n"
    "v_mfma_f32_16x16x32_bf16 v[60:63], v[4:7], a[96:99], 0\n"
    "v_mfma_f32_16x16x32_bf16 v[56:59], v[8:11], a[100:103], v[56:59]\n"
    "v_mfma_f32_16x16x32_bf16 v[60:63], v[12:15], a[100:103], v[60:63]\n"
    "v_mfma_f32_16x16x32_bf16 v[56:59], v[16:19], a[104:107], v[56:59]\n"
    "v_mfma_f32_16x16x32_bf16 v[60:63], v[20:23], a[104:107], v[60:63]\n"
    "v_mfma_f32_16x16x32_bf16 v[56:59], v[24:27], a[108:111], v[56:59]\n"
    "v_cvt_pk_bf16_f32 v72, v48, v49\n"
    "v_cvt_pk_bf16_f32 v74, v50, v51\n"
    "v_mfma_f32_16x16x32_bf16 v[60:63], v[28:31], a[108:111], v[60:63]\n"
    "v_cvt_pk_bf16_f32 v73, v52, v53\n"
    "v_cvt_pk_bf16_f32 v75, v54, v55\n"
    "v_mfma_f32_16x16x32_bf16 v[56:59], v[32:35], a[112:115], v[56:59]\n"
    "buffer_store_dwordx4 v[80:83], v96, s[0:3], s40 offen sc1\n"
    "s_add_u32 s40, s40, 0x10000\n"
    "v_mfma_f32_16x16x32_bf16 v[60:63], v[36:39], a[112:115], v[60:63]\n"
    "v_mfma_f32_16x16x32_bf16 v[56:59], v[40:43], a[116:119], v[56:59]\n"
    "v_mfma_f32_16x16x32_bf16 v[60:63], v[44:47], a[116:119], v[60:63]\n"
    "v_mfma_f32_16x16x32_bf16 v[48:51], v[0:3], a[120:123], 0\n"
    "v_mfma_f32_16x16x32_bf16 v[52:55], v[4:7], a[120:123], 0\n"
    "v_mfma_f32_16x16x32_bf16 v[48:51], v[8:11], a[124:127], v[48:51]\n"
    "v_mfma_f32_16x16x32_bf16 v[52:55], v[12:15], a[124:127], v[52:55]\n"
    "v_mfma_f32_16x16x32_bf16 v[48:51], v[16:19], a[128:131], v[48:51]\n"
    "buffer_store_dwordx4 v[72:75], v96, s[0:3], s40 offen sc1\n"
    "s_add_u32 s40, s40, 0x10000\n"
    "v_mfma_f32_16x16x32_bf16 v[52:55], v[20:23], a[128:131], v[52:55]\n"
    "v_mfma_f32_16x16x32_bf16 v[48:51], v[24:27], a[132:135], v[48:51]\n"
    "v_cvt_pk_bf16_f32 v76, v56, v57\n"
    "v_cvt_pk_bf16_f32 v78, v58, v59\n"
    "v_mfma_f32_16x16x32_bf16 v[52:55], v[28:31], a[132:135], v[52:55]\n"
    "v_cvt_pk_bf16_f32 v77, v60, v61\n"
    "v_cvt_pk_bf16_f32 v79, v62, v63\n"
    "v_mfma_f32_16x16x32_bf16 v[48:51], v[32:35], a[136:139], v[48:51]\n"
    "v_mfma_f32_16x16x32_bf16 v[52:55], v[36:39], a[136:139], v[52:55]\n"
    "v_mfma_f32_16x16x32_bf16 v[48:51], v[40:43], a[140:143], v[48:51]\n"
    "v_mfma_f32_16x16x32_bf16 v[52:55], v[44:47], a[140:143], v[52:55]\n"
    "v_mfma_f32_16x16x32_bf16 v[56:59], v[0:3], a[144:147], 0\n"
    "buffer_store_dwordx4 v[76:79], v96, s[0:3], s40 offen sc1\n"
    "s_add_u32 s40, s40, 0x10000\n"
    "v_mfma_f32_16x16x32_bf16 v[60:63], v[4:7], a[144:147], 0\n"
    "v_mfma_f32_16x16x32_bf16 v[56:59], v[8:11], a[148:151], v[56:59]\n"
    "v_mfma_f32_16x16x32_bf16 v[60:63], v[12:15], a[148:151], v[60:63]\n"
    "v_mfma_f32_16x16x32_bf16 v[56:59], v[16:19], a[152:155], v[56:59]\n"
    "v_mfma_f32_16x16x32_bf16 v[60:63], v[20:23], a[152:155], v[60:63]\n"
    "v_mfma_f32_16x16x32_bf16 v[56:59], v[24:27], a[156:159], v[56:59]\n"
    "v_cvt_pk_bf16_f32 v80, v48, v49\n"
    "v_cvt_pk_bf16_f32 v82, v50, v51\n"
    "v_mfma_f32_16x16x32_bf16 v[60:63], v[28:31], a[156:159], v[60:63]\n"
    "v_cvt_pk_bf16_f32 v81, v52, v53\n"
    "v_cvt_pk_bf16_f32 v83, v54, v55\n"
    "v_mfma_f32_16x16x32_bf16 v[56:59], v[32:35], a[160:163], v[56:59]\n"
    "buffer_store_dwordx4 v[80:83], v96, s[0:3], s40 offen sc1\n"
    "s_add_u32 s40, s40, 0x10000\n"
    "v_mfma_f32_16x16x32_bf16 v[60:63], v[36:39], a[160:163], v[60:63]\n"
    "v_mfma_f32_16x16x32_bf16 v[56:59], v[40:43], a[164:167], v[56:59]\n"
    "v_mfma_f32_16x16x32_bf16 v[60:63], v[44:47], a[164:167], v[60:63]\n"
    "v_mfma_f32_16x16x32_bf16 v[48:51], v[0:3], a[168:171], 0\n"
    "v_mfma_f32_16x16x32_bf16 v[52:55], v[4:7], a[168:171], 0\n"
    "v_mfma_f32_16x16x32_bf16 v[48:51], v[8:11], a[172:175], v[48:51]\n"
    "v_mfma_f32_16x16x32_bf16 v[52:55], v[12:15], a[172:175], v[52:55]\n"
    "v_mfma_f32_16x16x32_bf16 v[48:51], v[16:19], a[176:179], v[48:51]\n"
    "buffer_store_dwordx4 v[88:91], v102, s[0:3], s38 offen sc1\n"
    "v_mfma_f32_16x16x32_bf16 v[52:55], v[20:23], a[176:179], v[52:55]\n"
    "v_mfma_f32_16x16x32_bf16 v[48:51], v[24:27], a[180:183], v[48:51]\n"
    "v_cvt_pk_bf16_f32 v72, v56, v57\n"
    "v_cvt_pk_bf16_f32 v74, v58, v59\n"
    "v_mfma_f32_16x16x32_bf16 v[52:55], v[28:31], a[180:183], v[52:55]\n"
    "v_cvt_pk_bf16_f32 v73, v60, v61\n"
    "v_cvt_pk_bf16_f32 v75, v62, v63\n"
    "v_mfma_f32_16x16x32_bf16 v[48:51], v[32:35], a[184:187], v[48:51]\n"
    "v_mfma_f32_16x16x32_bf16 v[52:55], v[36:39], a[184:187], v[52:55]\n"
    "v_mfma_f32_16x16x32_bf16 v[48:51], v[40:43], a[188:191], v[48:51]\n"
    "v_mfma_f32_16x16x32_bf16 v[52:55], v[44:47], a[188:191], v[52:55]\n"
    "v_mfma_f32_16x16x32_bf16 v[56:59], v[0:3], a[192:195], 0\n"
    "buffer_store_dwordx4 v[72:75], v96, s[0:3], s40 offen sc1\n"
    "s_add_u32 s40, s40, 0x10000\n"
    "v_mfma_f32_16x16x32_bf16 v[60:63], v[4:7], a[192:195], 0\n"
    "v_mfma_f32_16x16x32_bf16 v[56:59], v[8:11], a[196:199], v[56:59]\n"
    "v_mfma_f32_16x16x32_bf16 v[60:63], v[12:15], a[196:199], v[60:63]\n"
    "v_mfma_f32_16x16x32_bf16 v[56:59], v[16:19], a[200:203], v[56:59]\n"
    "v_mfma_f32_16x16x32_bf16 v[60:63], v[20:23], a[200:203], v[60:63]\n"
    "v_mfma_f32_16x16x32_bf16 v[56:59], v[24:27], a[204:207], v[56:59]\n"
    "v_cvt_pk_bf16_f32 v76, v48, v49\n"
    "v_cvt_pk_bf16_f32 v78, v50, v51\n"
    "v_mfma_f32_16x16x32_bf16 v[60:63], v[28:31], a[204:207], v[60:63]\n"
    "v_cvt_pk_bf16_f32 v77, v52, v53\n"
    "v_cvt_pk_bf16_f32 v79, v54, v55\n"
    "v_mfma_f32_16x16x32_bf16 v[56:59], v[32:35], a[208:211], v[56:59]\n"
    "buffer_store_dwordx4 v[76:79], v96, s[0:3], s40 offen sc1\n"
    "s_add_u32 s40, s40, 0x10000\n"
    "v_mfma_f32_16x16x32_bf16 v[60:63], v[36:39], a[208:211], v[60:63]\n"
    "v_mfma_f32_16x16x32_bf16 v[56:59], v[40:43], a[212:215], v[56:59]\n"
    "v_mfma_f32_16x16x32_bf16 v[60:63], v[44:47], a[212:215], v[60:63]\n"
    "v_mfma_f32_16x16x32_bf16 v[48:51], v[0:3], a[216:219], 0\n"
    "v_mfma_f32_16x16x32_bf16 v[52:55], v[4:7], a[216:219], 0\n"
    "v_mfma_f32_16x16x32_bf16 v[48:51], v[8:11], a[220:223], v[48:51]\n"
    "v_mfma_f32_16x16x32_bf16 v[52:55], v[12:15], a[220:223], v[52:55]\n"
    "v_mfma_f32_16x16x32_bf16 v[48:51], v[16:19], a[224:227], v[48:51]\n"
    "buffer_store_dwordx4 v[88:91], v102, s[0:3], s39 offen sc1\n"
    "v_mfma_f32_16x16x32_bf16 v[52:55], v[20:23], a[224:227], v[52:55]\n"
    "v_mfma_f32_16x16x32_bf16 v[48:51], v[24:27], a[228:231], v[48:51]\n"
    "v_cvt_pk_bf16_f32 v80, v56, v57\n"
    "v_cvt_pk_bf16_f32 v82, v58, v59\n"
    "v_mfma_f32_16x16x32_bf16 v[52:55], v[28:31], a[228:231], v[52:55]\n"
    "v_cvt_pk_bf16_f32 v81, v60, v61\n"
    "v_cvt_pk_bf16_f32 v83, v62, v63\n"
    "v_mfma_f32_16x16x32_bf16 v[48:51], v[32:35], a[232:235], v[48:51]\n"
    "v_mfma_f32_16x16x32_bf16 v[52:55], v[36:39], a[232:235], v[52:55]\n"
    "v_mfma_f32_16x16x32_bf16 v[48:51], v[40:43], a[236:239], v[48:51]\n"
    "v_mfma_f32_16x16x32_bf16 v[52:55], v[44:47], a[236:239], v[52:55]\n"
    "s_nop 6\n"
    "v_cvt_pk_bf16_f32 v72, v48, v49\n"
    "v_cvt_pk_bf16_f32 v74, v50, v51\n"
    "v_cvt_pk_bf16_f32 v73, v52, v53\n"
    "v_cvt_pk_bf16_f32 v75, v54, v55\n"
    "buffer_store_dwordx4 v[80:83], v104, s[0:3], s38 offen sc1\n"
    ".Lvo_vph_%=:\n"
    "s_waitcnt vmcnt(11) lgkmcnt(0)\n"
    "s_barrier\n"
    "ds_read_b128 v[24:27], v92 offset:0\n"
    "ds_read_b128 v[28:31], v92 offset:1024\n"
    "ds_read_b128 v[32:35], v92 offset:2048\n"
    "ds_read_b128 v[36:39], v92 offset:3072\n"
    "ds_read_b128 v[40:43], v92 offset:4096\n"
    "ds_read_b128 v[44:47], v92 offset:5120\n"
    "s_waitcnt lgkmcnt(4)\n"
    "v_mfma_f32_16x16x32_bf16 v[48:51], a[240:243], v[24:27], 0\n"
    "buffer_store_dwordx4 v[72:75], v104, s[0:3], s39 offen sc1\n"
    "v_mfma_f32_16x16x32_bf16 v[52:55], a[240:243], v[28:31], 0\n"
    "s_add_u32 s34, s34, 1\n"
    "s_add_u32 s36, s34, 0\n"
    "s_and_b32 s36, s36, 3\n"
    "v_mfma_f32_16x16x32_bf16 v[56:59], v[176:179], v[24:27], 0\n"
    "ds_read_b128 v[24:27], v92 offset:6144\n"
    "s_lshl_b32 s36, s36, 22\n"
    "v_mfma_f32_16x16x32_bf16 v[60:63], v[176:179], v[28:31], 0\n"
    "ds_read_b128 v[28:31], v92 offset:7168\n"
    "s_waitcnt lgkmcnt(4)\n"
    "v_mfma_f32_16x16x32_bf16 v[48:51], a[244:247], v[32:35], v[48:51]\n"
    "s_add_u32 s41, s15, s36\n"
    "s_add_u32 s42, s16, s36\n"
    "s_add_u32 s37, s36, 0x10000\n"
    "v_mfma_f32_16x16x32_bf16 v[52:55], a[244:247], v[36:39], v[52:55]\n"
    "s_add_u32 s50, s34, 3\n"
    "s_cmp_gt_i32 s50, 64\n"
    "s_cselect_b32 s50, 0, s50\n"
    "v_mfma_f32_16x16x32_bf16 v[56:59], v[180:183], v[32:35], v[56:59]\n"
    "ds_read_b128 v[32:35], v93 offset:0\n"
    "s_add_u32 s51, s34, 4\n"
    "v_mfma_f32_16x16x32_bf16 v[60:63], v[180:183], v[36:39], v[60:63]\n"
    "ds_read_b128 v[36:39], v93 offset:1024\n"
    "s_waitcnt lgkmcnt(4)\n"
    "v_mfma_f32_16x16x32_bf16 v[48:51], a[248:251], v[40:43], v[48:51]\n"
    "buffer_load_dwordx2 v[108:109], off, s[0:3], s45 sc1\n"
    "v_mfma_f32_16x16x32_bf16 v[52:55], a[248:251], v[44:47], v[52:55]\n"
    "s_cmp_gt_i32 s51, 64\n"
    "s_cselect_b32 s51, 0, s51\n"
    "s_mul_i32 s51, s51, s14\n"
    "v_mfma_f32_16x16x32_bf16 v[56:59], v[184:187], v[40:43], v[56:59]\n"
    "ds_read_b128 v[40:43], v93 offset:2048\n"
    "v_mov_b64 v[64:65], 0\n"
    "v_mfma_f32_16x16x32_bf16 v[60:63], v[184:187], v[44:47], v[60:63]\n"
    "ds_read_b128 v[44:47], v93 offset:3072\n"
    "s_waitcnt lgkmcnt(4)\n"
    "v_mfma_f32_16x16x32_bf16 v[48:51], a[252:255], v[24:27], v[48:51]\n"
    "buffer_store_dwordx2 v[88:89], v103, s[0:3], s38 offen sc1\n"
    "v_mov_b64 v[66:67], 0\n"
    "v_mfma_f32_16x16x32_bf16 v[52:55], a[252:255], v[28:31], v[52:55]\n"
    "v_mov_b64 v[68:69], 0\n"
    "v_mov_b64 v[70:71], 0\n"
    "v_mfma_f32_16x16x32_bf16 v[56:59], v[188:191], v[24:27], v[56:59]\n"
    "ds_read_b128 v[24:27], v93 offset:4096\n"
    "v_mov_b64 v[84:85], 0\n"
    "v_mfma_f32_16x16x32_bf16 v[60:63], v[188:191], v[28:31], v[60:63]\n"
    "ds_read_b128 v[28:31], v93 offset:5120\n"
    "s_waitcnt lgkmcnt(4)\n"
    "v_mfma_f32_16x16x32_bf16 v[48:51], v[112:115], v[32:35], v[48:51]\n"
    "v_mov_b64 v[86:87], 0\n"
    "v_mfma_f32_16x16x32_bf16 v[52:55], v[112:115], v[36:39], v[52:55]\n"
    "v_mfma_f32_16x16x32_bf16 v[56:59], v[192:195], v[32:35], v[56:59]\n"
    "ds_read_b128 v[32:35], v93 offset:6144\n"
    "v_mfma_f32_16x16x32_bf16 v[60:63], v[192:195], v[36:39], v[60:63]\n"
    "ds_read_b128 v[36:39], v93 offset:7168\n"
    "s_waitcnt lgkmcnt(4)\n"
    "v_mfma_f32_16x16x32_bf16 v[48:51], v[116:119], v[40:43], v[48:51]\n"
    "v_mfma_f32_16x16x32_bf16 v[52:55], v[116:119], v[44:47], v[52:55]\n"
    "v_mfma_f32_16x16x32_bf16 v[56:59], v[196:199], v[40:43], v[56:59]\n"
    "ds_read_b128 v[40:43], v94 offset:0\n"
    "v_mfma_f32_16x16x32_bf16 v[60:63], v[196:199], v[44:47], v[60:63]\n"
    "ds_read_b128 v[44:47], v94 offset:1024\n"
    "s_waitcnt lgkmcnt(4)\n"
    "v_mfma_f32_16x16x32_bf16 v[48:51], v[120:123], v[24:27], v[48:51]\n"
    "v_mfma_f32_16x16x32_bf16 v[52:55], v[120:123], v[28:31], v[52:55]\n"
    "v_mfma_f32_16x16x32_bf16 v[56:59], v[200:203], v[24:27], v[56:59]\n"
    "ds_read_b128 v[24:27], v94 offset:2048\n"
    "v_mfma_f32_16x16x32_bf16 v[60:63], v[200:203], v[28:31], v[60:63]\n"
    "ds_read_b128 v[28:31], v94 offset:3072\n"
    "s_waitcnt lgkmcnt(4)\n"
    "v_mfma_f32_16x16x32_bf16 v[48:51], v[124:127], v[32:35], v[48:51]\n"
    "v_mfma_f32_16x16x32_bf16 v[52:55], v[124:127], v[36:39], v[52:55]\n"
    "v_mfma_f32_16x16x32_bf16 v[56:59], v[204:207], v[32:35], v[56:59]\n"
    "ds_read_b128 v[32:35], v94 offset:4096\n"
    "v_mfma_f32_16x16x32_bf16 v[60:63], v[204:207], v[36:39], v[60:63]\n"
    "ds_read_b128 v[36:39], v94 offset:5120\n"
    "s_waitcnt lgkmcnt(4)\n"
    "v_mfma_f32_16x16x32_bf16 v[48:51], v[128:131], v[40:43], v[48:51]\n"
    "v_mfma_f32_16x16x32_bf16 v[52:55], v[128:131], v[44:47], v[52:55]\n"
    "v_mfma_f32_16x16x32_bf16 v[56:59], v[208:211], v[40:43], v[56:59]\n"
    "ds_read_b128 v[40:43], v94 offset:6144\n"
    "v_mfma_f32_16x16x32_bf16 v[60:63], v[208:211], v[44:47], v[60:63]\n"
    "ds_read_b128 v[44:47], v94 offset:7168\n"
    "s_waitcnt lgkmcnt(4)\n"
    "v_mfma_f32_16x16x32_bf16 v[48:51], v[132:135], v[24:27], v[48:51]\n"
    "s_waitcnt vmcnt(1)\n"
    "v_readfirstlane_b32 s32, v108\n"
    "v_mfma_f32_16x16x32_bf16 v[52:55], v[132:135], v[28:31], v[52:55]\n"
    "v_readfirstlane_b32 s33, v109\n"
    "v_mfma_f32_16x16x32_bf16 v[56:59], v[212:215], v[24:27], v[56:59]\n"
    "ds_read_b128 v[24:27], v95 offset:0\n"
    "v_mfma_f32_16x16x32_bf16 v[60:63], v[212:215], v[28:31], v[60:63]\n"
    "ds_read_b128 v[28:31], v95 offset:1024\n"
    "s_waitcnt lgkmcnt(4)\n"
    "v_mfma_f32_16x16x32_bf16 v[48:51], v[136:139], v[32:35], v[48:51]\n"
    "s_cmp_eq_u32 s33, s35\n"
    "s_cselect_b32 s46, s32, s46\n"
    "s_add_u32 s46, s46, 304\n"
    "v_mfma_f32_16x16x32_bf16 v[52:55], v[136:139], v[36:39], v[52:55]\n"
    "s_sub_u32 s47, s46, 63\n"
    "v_mfma_f32_16x16x32_bf16 v[56:59], v[216:219], v[32:35], v[56:59]\n"
    "ds_read_b128 v[32:35], v95 offset:2048\n"
    "v_mfma_f32_16x16x32_bf16 v[60:63], v[216:219], v[36:39], v[60:63]\n"
    "ds_read_b128 v[36:39], v95 offset:3072\n"
    "s_waitcnt lgkmcnt(4)\n"
    ".Lvo_rt8_%=:\n"
    "s_memrealtime s[30:31]\n"
    "s_waitcnt lgkmcnt(0)\n"
    "s_sub_u32 s32, s47, s30\n"
    "s_cmp_lt_u32 s32, 600\n"
    "s_cbranch_scc1 .Lvo_rt8_%=\n"
    "buffer_load_dwordx4 v[72:75], v96, s[0:3], s41 offen sc1\n"
    "buffer_load_dwordx4 v[76:79], v96, s[0:3], s41 offen offset:1024 sc1\n"
    "buffer_load_dwordx4 v[80:83], v96, s[0:3], s42 offen sc1\n"
    "v_mfma_f32_16x16x32_bf16 v[48:51], v[140:143], v[40:43], v[48:51]\n"
    "s_add_u32 s49, s34, 3\n"
    "s_lshl_b32 s43, s49, 19\n"
    "s_lshl_b32 s33, s49, 11\n"
    "v_mfma_f32_16x16x32_bf16 v[52:55], v[140:143], v[44:47], v[52:55]\n"
    "s_xor_b32 s33, s33, s13\n"
    "s_and_b32 s33, s33, 0x2000\n"
    "s_add_u32 s43, s43, s33\n"
    "v_mfma_f32_16x16x32_bf16 v[56:59], v[220:223], v[40:43], v[56:59]\n"
    "ds_read_b128 v[40:43], v95 offset:4096\n"
    "s_add_u32 s43, s43, s17\n"
    "v_mfma_f32_16x16x32_bf16 v[60:63], v[220:223], v[44:47], v[60:63]\n"
    "ds_read_b128 v[44:47], v95 offset:5120\n"
    "s_waitcnt lgkmcnt(4)\n"
    "v_mfma_f32_16x16x32_bf16 v[48:51], v[144:147], v[24:27], v[48:51]\n"
    "s_add_u32 s49, s34, 2\n"
    "s_lshl_b32 s44, s49, 20\n"
    "s_lshl_b32 s33, s49, 12\n"
    "v_mfma_f32_16x16x32_bf16 v[52:55], v[144:147], v[28:31], v[52:55]\n"
    "s_xor_b32 s33, s33, s13\n"
    "s_and_b32 s33, s33, 0x2000\n"
    "s_add_u32 s44, s44, s33\n"
    "v_mfma_f32_16x16x32_bf16 v[56:59], v[224:227], v[24:27], v[56:59]\n"
    "ds_read_b128 v[24:27], v95 offset:6144\n"
    "s_add_u32 s44, s44, s18\n"
    "v_mfma_f32_16x16x32_bf16 v[60:63], v[224:227], v[28:31], v[60:63]\n"
    "ds_read_b128 v[28:31], v95 offset:7168\n"
    "s_waitcnt lgkmcnt(4)\n"
    "v_mfma_f32_16x16x32_bf16 v[48:51], v[148:151], v[32:35], v[48:51]\n"
    "s_add_u32 s38, s34, 2\n"
    "s_and_b32 s38, s38, 3\n"
    "s_lshl_b32 s38, s38, 22\n"
    "v_mfma_f32_16x16x32_bf16 v[52:55], v[148:151], v[36:39], v[52:55]\n"
    "s_add_u32 s39, s38, 0x10000\n"
    "s_add_u32 s40, s19, s38\n"
    "s_and_b32 s45, s34, 63\n"
    "v_mfma_f32_16x16x32_bf16 v[56:59], v[228:231], v[32:35], v[56:59]\n"
    "ds_read_b128 v[32:35], v96 offset:53248\n"
    "s_lshl_b32 s45, s45, 7\n"
    "v_mfma_f32_16x16x32_bf16 v[60:63], v[228:231], v[36:39], v[60:63]\n"
    "ds_read_b128 v[36:39], v96 offset:54272\n"
    "s_waitcnt lgkmcnt(4)\n"
    "v_mfma_f32_16x16x32_bf16 v[48:51], v[152:155], v[40:43], v[48:51]\n"
    "s_add_u32 s45, s20, s45\n"
    "s_add_u32 s35, s34, 1\n"
    "v_mfma_f32_16x16x32_bf16 v[52:55], v[152:155], v[44:47], v[52:55]\n"
    "s_cmp_lt_i32 s34, 0\n"
    "s_cselect_b32 s35, -5, s35\n"
    "v_mfma_f32_16x16x32_bf16 v[56:59], v[232:235], v[40:43], v[56:59]\n"
    "ds_read_b128 v[40:43], v96 offset:55296\n"
    "buffer_load_dword v110, v106, s[0:3], s23 offen sc1\n"
    "v_mfma_f32_16x16x32_bf16 v[60:63], v[232:235], v[44:47], v[60:63]\n"
    "ds_read_b128 v[44:47], v96 offset:56320\n"
    "s_waitcnt lgkmcnt(4)\n"
    "v_mfma_f32_16x16x32_bf16 v[48:51], v[156:159], v[24:27], v[48:51]\n"
    "ds_read_b128 v[0:3], v97 offset:0\n"
    "v_mfma_f32_16x16x32_bf16 v[52:55], v[156:159], v[28:31], v[52:55]\n"
    "ds_read_b128 v[4:7], v97 offset:1024\n"
    "v_mfma_f32_16x16x32_bf16 v[56:59], v[236:239], v[24:27], v[56:59]\n"
    "ds_read_b128 v[24:27], v96 offset:57344\n"
    "v_mfma_f32_16x16x32_bf16 v[60:63], v[236:239], v[28:31], v[60:63]\n"
    "ds_read_b128 v[28:31], v96 offset:58368\n"
    "s_waitcnt lgkmcnt(6)\n"
    "v_mfma_f32_16x16x32_bf16 v[48:51], v[160:163], v[32:35], v[48:51]\n"
    "buffer_load_dword v111, v105, s[0:3], s22 offen sc1\n"
    "ds_read_b128 v[8:11], v97 offset:2048\n"
    "v_mfma_f32_16x16x32_bf16 v[52:55], v[160:163], v[36:39], v[52:55]\n"
    "ds_read_b128 v[12:15], v97 offset:3072\n"
    "v_mfma_f32_16x16x32_bf16 v[56:59], v[240:243], v[32:35], v[56:59]\n"
    "ds_read_b128 v[32:35], v96 offset:59392\n"
    "v_mfma_f32_16x16x32_bf16 v[60:63], v[240:243], v[36:39], v[60:63]\n"
    "ds_read_b128 v[36:39], v96 offset:60416\n"
    "s_waitcnt lgkmcnt(8)\n"
    "v_mfma_f32_16x16x32_bf16 v[48:51], v[164:167], v[40:43], v[48:51]\n"
    "ds_read_b128 v[16:19], v97 offset:4096\n"
    "v_mfma_f32_16x16x32_bf16 v[52:55], v[164:167], v[44:47], v[52:55]\n"
    "ds_read_b128 v[20:23], v97 offset:5120\n"
    "v_mfma_f32_16x16x32_bf16 v[56:59], v[244:247], v[40:43], v[56:59]\n"
    "v_mfma_f32_16x16x32_bf16 v[60:63], v[244:247], v[44:47], v[60:63]\n"
    "s_waitcnt lgkmcnt(6)\n"
    "v_mfma_f32_16x16x32_bf16 v[48:51], v[168:171], v[24:27], v[48:51]\n"
    "v_mfma_f32_16x16x32_bf16 v[52:55], v[168:171], v[28:31], v[52:55]\n"
    "v_mfma_f32_16x16x32_bf16 v[56:59], v[248:251], v[24:27], v[56:59]\n"
    "v_mfma_f32_16x16x32_bf16 v[60:63], v[248:251], v[28:31], v[60:63]\n"
    "s_waitcnt lgkmcnt(2)\n"
    "v_mfma_f32_16x16x32_bf16 v[48:51], v[172:175], v[32:35], v[48:51]\n"
    "v_mfma_f32_16x16x32_bf16 v[52:55], v[172:175], v[36:39], v[52:55]\n"
    "v_mfma_f32_16x16x32_bf16 v[56:59], v[252:255], v[32:35], v[56:59]\n"
    "v_mfma_f32_16x16x32_bf16 v[60:63], v[252:255], v[36:39], v[60:63]\n"
    "ds_read_b128 v[24:27], v97 offset:6144\n"
    "ds_read_b128 v[28:31], v97 offset:7168\n"
    "s_nop 2\n"
    "v_cvt_pk_bf16_f32 v32, v48, v49\n"
    "v_cvt_pk_bf16_f32 v33, v50, v51\n"
    "v_cvt_pk_bf16_f32 v34, v56, v57\n"
    "v_cvt_pk_bf16_f32 v35, v58, v59\n"
    "ds_write_b128 v92, v[32:35] offset:11264\n"
    "v_cvt_pk_bf16_f32 v36, v52, v53\n"
    "v_cvt_pk_bf16_f32 v37, v54, v55\n"
    "v_cvt_pk_bf16_f32 v38, v60, v61\n"
    "v_cvt_pk_bf16_f32 v39, v62, v63\n"
    "ds_write_b128 v92, v[36:39] offset:12288\n"
    "ds_read_b128 v[48:51], v97 offset:8192\n"
    "ds_read_b128 v[52:55], v97 offset:9216\n"
    "ds_read_b128 v[56:59], v97 offset:10240\n"
    "ds_read_b128 v[60:63], v97 offset:11264\n"
    "s_cmp_lt_i32 s34, 0\n"
    "s_cbranch_scc0 .Lvo_take_%=\n"
    "s_waitcnt vmcnt(0) lgkmcnt(0)\n"
    "s_barrier\n"
    ".Lvo_po9_%=:\n"
    "buffer_load_dword v110, v106, s[0:3], s23 offen sc1\n"
    "s_waitcnt vmcnt(0)\n"
    "v_cmp_gt_i32 vcc, s50, v110\n"
    "s_nop 0\n"
    "s_cbranch_vccz .Lvo_pd10_%=\n"
    "s_add_u32 s28, s28, 1\n"
    "s_cmp_lt_u32 s28, 0x40000\n"
    "s_cbranch_scc1 .Lvo_sp11_%=\n"
    "v_mov_b32 v110, 32\n"
    "s_mov_b32 s32, 33728\n"
    "buffer_store_dword v110, off, s[0:3], s32 sc1\n"
    "s_waitcnt vmcnt(0)\n"
    "s_branch .Lvo_pd10_%=\n"
    ".Lvo_sp11_%=:\n"
    "s_sleep 4\n"
    "s_branch .Lvo_po9_%=\n"
    ".Lvo_pd10_%=:\n"
    ".Lvo_po12_%=:\n"
    "buffer_load_dword v111, v105, s[0:3], s22 offen sc1\n"
    "s_waitcnt vmcnt(0)\n"
    "v_cmp_gt_i32 vcc, s51, v111\n"
    "s_nop 0\n"
    "s_cbranch_vccz .Lvo_pd13_%=\n"
    "s_add_u32 s28, s28, 1\n"
    "s_cmp_lt_u32 s28, 0x40000\n"
    "s_cbranch_scc1 .Lvo_sp14_%=\n"
    "v_mov_b32 v111, 35\n"
    "s_mov_b32 s32, 33728\n"
    "buffer_store_dword v111, off, s[0:3], s32 sc1\n"
    "s_waitcnt vmcnt(0)\n"
    "s_branch .Lvo_pd13_%=\n"
    ".Lvo_sp14_%=:\n"
    "s_sleep 4\n"
    "s_branch .Lvo_po12_%=\n"
    ".Lvo_pd13_%=:\n"
    "buffer_load_dwordx4 v[32:35], v96, s[0:3], s44 offen offset:0 sc1\n"
    "buffer_load_dwordx4 v[36:39], v96, s[0:3], s44 offen offset:1024 sc1\n"
    "buffer_load_dwordx4 v[40:43], v96, s[0:3], s44 offen offset:2048 sc1\n"
    "buffer_load_dwordx4 v[44:47], v96, s[0:3], s44 offen offset:3072 sc1\n"
    "s_add_u32 m0, s48, 0\n"
    "s_nop 0\n"
    "buffer_load_dwordx4 v96, s[0:3], s43 offen sc1 lds\n"
    "buffer_load_dwordx4 v96, s[0:3], s43 offen offset:1024 sc1 lds\n"
    "s_add_u32 m0, s48, 13312\n"
    "s_add_u32 s43, s43, 0x4000\n"
    "buffer_load_dwordx4 v96, s[0:3], s43 offen sc1 lds\n"
    "buffer_load_dwordx4 v96, s[0:3], s43 offen offset:1024 sc1 lds\n"
    "s_add_u32 m0, s48, 26624\n"
    "s_add_u32 s43, s43, 0x4000\n"
    "buffer_load_dwordx4 v96, s[0:3], s43 offen sc1 lds\n"
    "buffer_load_dwordx4 v96, s[0:3], s43 offen offset:1024 sc1 lds\n"
    "ds_read_b128 v[0:3], v96 offset:11264\n"
    "ds_read_b128 v[4:7], v96 offset:12288\n"
    "ds_read_b128 v[8:11], v96 offset:24576\n"
    "ds_read_b128 v[12:15], v96 offset:25600\n"
    "ds_read_b128 v[16:19], v96 offset:37888\n"
    "ds_read_b128 v[20:23], v96 offset:38912\n"
    "ds_read_b128 v[24:27], v96 offset:51200\n"
    "ds_read_b128 v[28:31], v96 offset:52224\n"
    "s_waitcnt vmcnt(0)\n"
    "s_mov_b32 s37, 2130706432\n"
    "s_mov_b32 s36, 2130706432\n"
    "s_branch .Lvo_uph_%=\n"
    ".Lvo_retry_%=:\n"
    "s_add_u32 s28, s28, 1\n"
    "s_cmp_lt_u32 s28, 0x40000\n"
    "s_cbranch_scc1 .Lvo_sp15_%=\n"
    "v_mov_b32 v108, 2\n"
    "s_mov_b32 s32, 33728\n"
    "buffer_store_dword v108, off, s[0:3], s32 sc1\n"
    "s_waitcnt vmcnt(0)\n"
    "s_branch .Lvo_go_%=\n"
    ".Lvo_sp15_%=:\n"
    "s_nop 7\n"
    "v_mov_b64 v[64:65], 0\n"
    "v_mov_b64 v[66:67], 0\n"
    "v_mov_b64 v[68:69], 0\n"
    "v_mov_b64 v[70:71], 0\n"
    "v_mov_b64 v[84:85], 0\n"
    "v_mov_b64 v[86:87], 0\n"
    "buffer_load_dwordx4 v[72:75], v96, s[0:3], s41 offen sc1\n"
    "buffer_load_dwordx4 v[76:79], v96, s[0:3], s41 offen offset:1024 sc1\n"
    "buffer_load_dwordx4 v[80:83], v96, s[0:3], s42 offen sc1\n"
    "s_waitcnt vmcnt(0)\n"
    "s_branch .Lvo_take_%=\n"
    ".Lvo_wq_%=:\n"
    ".Lvo_po16_%=:\n"
    "buffer_load_dword v110, v106, s[0:3], s23 offen sc1\n"
    "s_waitcnt vmcnt(0)\n"
    "v_cmp_gt_i32 vcc, s50, v110\n"
    "s_nop 0\n"
    "s_cbranch_vccz .Lvo_pd17_%=\n"
    "s_add_u32 s28, s28, 1\n"
    "s_cmp_lt_u32 s28, 0x40000\n"
    "s_cbranch_scc1 .Lvo_sp18_%=\n"
    "v_mov_b32 v110, 33\n"
    "s_mov_b32 s32, 33728\n"
    "buffer_store_dword v110, off, s[0:3], s32 sc1\n"
    "s_waitcnt vmcnt(0)\n"
    "s_branch .Lvo_pd17_%=\n"
    ".Lvo_sp18_%=:\n"
    "s_sleep 4\n"
    "s_branch .Lvo_po16_%=\n"
    ".Lvo_pd17_%=:\n"
    "s_branch .Lvo_wqb_%=\n"
    ".Lvo_wx_%=:\n"
    ".Lvo_po19_%=:\n"
    "buffer_load_dword v111, v105, s[0:3], s22 offen sc1\n"
    "s_waitcnt vmcnt(0)\n"
    "v_cmp_gt_i32 vcc, s51, v111\n"
    "s_nop 0\n"
    "s_cbranch_vccz .Lvo_pd20_%=\n"
    "s_add_u32 s28, s28, 1\n"
    "s_cmp_lt_u32 s28, 0x40000\n"
    "s_cbranch_scc1 .Lvo_sp21_%=\n"
    "v_mov_b32 v111, 34\n"
    "s_mov_b32 s32, 33728\n"
    "buffer_store_dword v111, off, s[0:3], s32 sc1\n"
    "s_waitcnt vmcnt(0)\n"
    "s_branch .Lvo_pd20_%=\n"
    ".Lvo_sp21_%=:\n"
    "s_sleep 4\n"
    "s_branch .Lvo_po19_%=\n"
    ".Lvo_pd20_%=:\n"
    "s_branch .Lvo_wxb_%=\n"
    ".Lvo_end_%=:\n"
    "s_waitcnt vmcnt(0) lgkmcnt(0)\n"
    "s_endpgm\n"
    :: "{s[0:3]}"(s0_), "{s[4:7]}"(s4_), "{s[8:11]}"(s8_), "{s[12:15]}"(s12_), "{s[16:19]}"(s16_), "{s[20:23]}"(s20_), "{s[24:27]}"(s24_), "{v[0:3]}"(v0_), "{v[4:7]}"(v4_), "{v[8:11]}"(v8_), "{v[12:15]}"(v12_), "{v[16:19]}"(v16_), "{v[92:95]}"(v92_), "{v[96:99]}"(v96_), "{v[100:103]}"(v100_), "{v[104:107]}"(v104_) : "memory", "v255", "a255", "s95");
    __builtin_unreachable();
}
DEV void ct_role(const MegaArgs& A, const int L, const int u) {
    ROLE_HEAD;
    {
        const int sg = 2 * u + L;
        u32x4 xa[8], xb[8];
#pragma unroll
        for (int r = 0; r < 8; ++r) {
            const bf16_t* xr = A.x0 + (size_t)(32 * sg + 8 * w + r) * D_;
            xa[r] = *(const u32x4*)(xr + 8 * l);
            xb[r] = *(const u32x4*)(xr + 512 + 8 * l16);
        }
        const float mk = l < 16 ? 1.f : 0.f;
#pragma unroll
        for (int r = 0; r < 8; ++r) {
            float v[16], s = 0.f, s2 = 0.f, q = 0.f, q2 = 0.f;
#pragma unroll
            for (int e = 0; e < 4; ++e) { v[2 * e] = lo16(xa[r][e]); v[2 * e + 1] = hi16(xa[r][e]); v[8 + 2 * e] = lo16(xb[r][e]); v[9 + 2 * e] = hi16(xb[r][e]); }
#pragma unroll
            for (int e = 0; e < 8; ++e) { s += v[e]; s2 += v[8 + e]; }
            s += l < 16 ? s2 : 0.f;
            for (int o = 32; o > 0; o >>= 1) s += __shfl_xor(s, o);
            const float mean = s * (1.0f / D_);
#pragma unroll
            for (int e = 0; e < 8; ++e) { const float d = v[e] - mean, d2 = v[8 + e] - mean; q += d * d; q2 += d2 * d2; }
            q += l < 16 ? q2 : 0.f;
            for (int o = 32; o > 0; o >>= 1) q += __shfl_xor(q, o);
            const float mb = rb(mean), rd = __builtin_amdgcn_rcpf(rb(sqrtf(rb(rb(q * (1.0f / D_)) + 1e-5f))));
            u32x4 oa, ob;
#pragma unroll
            for (int e = 0; e < 4; ++e) {
                oa[sw2(e)] = pk2(rb(v[2 * e] - mb) * rd, rb(v[2 * e + 1] - mb) * rd);
                ob[sw2(e)] = pk2(rb(v[8 + 2 * e] - mb) * rd, rb(v[9 + 2 * e] - mb) * rd);
            }
            const int row = 8 * w + r;
            const unsigned o = rec(sg, 0) + (row >> 4) * 1024 + (row & 15) * 16 + (l16 >> 2) * 2048 + (l & 3) * 256;
            bst16(rr, o + lq * 16384, oa);
            bst16(rr, l < 16 ? o + 65536 : OOB, ob);
        }
    }
    const int r5 = l / 5, q5 = l - 5 * r5, c0 = q5 < 4 ? 16 * u + 4 * q5 : 512 + 4 * u, tk = 8 * w + r5;
    const unsigned mpos = l < 40 ? xl(RG_MEM + w * 16384 + (c0 >> 6) * 1024 + (((c0 >> 4) & 3) * 16 + 4 * (r5 >> 1) + ((c0 >> 1) & 2) + (r5 & 1)) * 16 + (c0 & 8), L) : OOB;
    const unsigned xpos = l < 40 ? (c0 >> 7) * 16384 + ((c0 >> 5) & 3) * 2048 + (tk >> 4) * 1024 + (((c0 >> 3) & 3) * 16 + (tk & 15)) * 16 + (c0 & 4) * 2 : OOB;
    const unsigned rpos = (L || l >= 40) ? xpos + RC_X2 : tk * 1280 + c0 * 2;
    const u32x4 rs = L ? rx : u32x4{lo(A.x0), hi(A.x0), 2048u * 1280u, 0x20000u};
    const u32x2 m0 = *(const u32x2*)(A.mem0 + (r5 & 7) * D_ + c0);
    bst8(rr, mpos, u32x2{nosent(m0[0]), m0[1]}); bst8(rr, mpos + SLOT, ones2); bst8(rr, mpos + 2 * SLOT, ones2);
    DRAIN_BARRIER();
    bst4(rr, threadIdx.x < 16 ? xl(Z_CT + (l & 7) * 256 + (32 * L + u) * 4, l >> 3) : OOB, 1u);
    if (!((A.layers >> L) & 1)) return;
    const u32x4 s0_ = {RFL(rx[0]), RFL(rx[1]), RFL(rx[2]), RFL(rx[3])};
    const u32x4 s4_ = {RFL(rs[0]), RFL(rs[1]), RFL(rs[2]), RFL(rs[3])};
    const u32x4 s8_ = {RFL(L << 13), RFL(REC0), RFL(xl(RG_UT + (u * 4 + w) * 8192, L)), RFL(xl(RG_UTX + (u * 4 + w) * 2048, L))};
    const u32x4 s12_ = {RFL(xl(RG_UM + (u * 4 + w) * 2048, L)), RFL(xl(RG_UMX + (u * 4 + w) * 512, L)), RFL(xl(RG_PT + w * 16384, L)), RFL(xl(RG_PM + w * 8192, L))};
    const u32x4 s16_ = {RFL(xl(Z_X + 16384 + (u & 1) * 8192, L)), RFL(xl(Z_QV + (u & 3) * 256, L)), RFL(DBG0 + ((L * 128 + 64 + u) * 4 + w) * 2048), RFL(0u)};
    const u32x4 v192_ = {(unsigned)(l * 16), (unsigned)(l * 16 + 4096), (unsigned)((l & 2) ? 0xeeeeu : 0x4444u), (unsigned)(0x4444u)};
    const u32x4 v196_ = {(unsigned)((l16 >> 2) * 256 + lq * 64 + (l & 3) * 16), (unsigned)(lq * 64 + (l & 3) * 16), (unsigned)((lq >> 1) * 2048 + 1024 + (lq & 1) * 256 + l16 * 16), (unsigned)(lq * 2048 + 1536 + l16 * 16)};
    const u32x4 v200_ = {(unsigned)(0 + w * 640 + lq * 160 + l16 * 4), (unsigned)(l16 < 4 ? 0 + w * 640 + lq * 160 + 64 + l16 * 4 : 12800), (unsigned)(0 + (l < 40 ? l : 0) * 16), (unsigned)(mpos)};
    const u32x4 v204_ = {(unsigned)(2560 + w * 2560 + lq * 320 + l16 * 4), (unsigned)(l16 < 4 ? 2560 + w * 2560 + lq * 320 + 64 + l16 * 4 : 12800), (unsigned)(3840 + w * 2560 + lq * 320 + l16 * 4), (unsigned)(l16 < 4 ? 3840 + w * 2560 + lq * 320 + 64 + l16 * 4 : 12800)};
    const u32x4 v208_ = {(unsigned)(2560 + w * 640 + (l < 40 ? l : 0) * 16), (unsigned)(xpos + RC_X1), (unsigned)(rpos), (unsigned)(l < 4 ? xl(Z_X1 + l * 4096 + u * 128 + w * 4, L) : OOB)};
    asm volatile(
    "s_mov_b32 s20, 0\n"
    "v_lshrrev_b32 v4, 2, v192\n"
    ".Lct_po1_%=:\n"
    "buffer_load_dword v8, v4, s[0:3], s17 offen sc1\n"
    "s_waitcnt vmcnt(0)\n"
    "v_cmp_gt_i32 vcc, 1, v8\n"
    "s_nop 0\n"
    "s_cbranch_vccz .Lct_pd2_%=\n"
    "s_add_u32 s20, s20, 1\n"
    "s_cmp_lt_u32 s20, 0x40000\n"
    "s_cbranch_scc1 .Lct_sp3_%=\n"
    "v_mov_b32 v8, 40\n"
    "s_mov_b32 s24, 33728\n"
    "buffer_store_dword v8, off, s[0:3], s24 sc1\n"
    "s_waitcnt vmcnt(0)\n"
    "s_branch .Lct_pd2_%=\n"
    ".Lct_sp3_%=:\n"
    "s_sleep 4\n"
    "s_branch .Lct_po1_%=\n"
    ".Lct_pd2_%=:\n"
    "s_memrealtime s[22:23]\n"
    "s_waitcnt lgkmcnt(0)\n"
    "s_sub_u32 s41, s22, 912\n"
    "s_mov_b32 s26, -1\n"
    "s_add_u32 s26, s26, 1\n"
    "s_add_u32 s27, s26, 1\n"
    "s_add_u32 s28, s26, 0\n"
    "s_and_b32 s28, s28, 3\n"
    "s_lshl_b32 s28, s28, 22\n"
    "s_add_u32 s29, s26, 1\n"
    "s_and_b32 s29, s29, 3\n"
    "s_lshl_b32 s29, s29, 22\n"
    "s_add_u32 s30, s26, 3\n"
    "s_and_b32 s30, s30, 3\n"
    "s_lshl_b32 s30, s30, 22\n"
    "s_and_b32 s40, s26, 63\n"
    "s_lshl_b32 s40, s40, 7\n"
    "s_add_u32 s31, s12, s28\n"
    "s_add_u32 s32, s13, s28\n"
    "s_add_u32 s33, s15, s28\n"
    "s_add_u32 s34, s14, s28\n"
    "s_add_u32 s38, s10, s29\n"
    "s_add_u32 s39, s11, s29\n"
    "s_add_u32 s40, s16, s40\n"
    "s_add_u32 s35, s34, 4096\n"
    "s_add_u32 s36, s34, 16384\n"
    "s_add_u32 s37, s34, 20480\n"
    "s_add_u32 s43, s41, 356\n"
    "s_lshl_b32 s45, s26, 19\n"
    "s_lshl_b32 s25, s26, 11\n"
    "s_xor_b32 s25, s25, s8\n"
    "s_and_b32 s25, s25, 0x2000\n"
    "s_add_u32 s45, s45, s25\n"
    "s_add_u32 s45, s45, s9\n"
    "s_mul_i32 s46, s26, 40960\n"
    "s_cmp_eq_u32 s8, 0\n"
    "s_cselect_b32 s46, s46, s45\n"
    "v_mov_b64 v[76:77], 0\n"
    "v_mov_b64 v[78:79], 0\n"
    "v_mov_b64 v[80:81], 0\n"
    "v_mov_b64 v[82:83], 0\n"
    ".Lct_p0_%=:\n"
    "buffer_load_dwordx4 v[40:43], v192, s[0:3], s31 offen offset:0 sc1\n"
    "buffer_load_dwordx4 v[44:47], v192, s[0:3], s31 offen offset:1024 sc1\n"
    "buffer_load_dwordx4 v[48:51], v197, s[0:3], s32 offen offset:0 sc1\n"
    "buffer_load_dwordx4 v[52:55], v197, s[0:3], s32 offen offset:256 sc1\n"
    "s_waitcnt vmcnt(0)\n"
    "v_max3_u32 v184, v40, v42, v40\n"
    "v_max3_u32 v184, v44, v46, v184\n"
    "v_max3_u32 v184, v48, v50, v184\n"
    "v_max3_u32 v184, v52, v54, v184\n"
    "v_cmp_eq_u32 vcc, -1, v184\n"
    "s_nop 0\n"
    "s_cbranch_vccz .Lct_p1_%=\n"
    "s_add_u32 s20, s20, 1\n"
    "s_cmp_lt_u32 s20, 0x40000\n"
    "s_cbranch_scc1 .Lct_sp4_%=\n"
    "v_mov_b32 v184, 41\n"
    "s_mov_b32 s24, 33728\n"
    "buffer_store_dword v184, off, s[0:3], s24 sc1\n"
    "s_waitcnt vmcnt(0)\n"
    "s_branch .Lct_p1_%=\n"
    ".Lct_sp4_%=:\n"
    "s_sleep 8\n"
    "s_branch .Lct_p0_%=\n"
    ".Lct_p1_%=:\n"
    "s_mov_b32 s38, s10\n"
    "s_mov_b32 s39, s11\n"
    "buffer_load_dwordx4 a[0:3], v192, s[0:3], s38 offen offset:0 sc1\n"
    "buffer_load_dwordx4 a[4:7], v192, s[0:3], s38 offen offset:1024 sc1\n"
    "buffer_load_dwordx4 a[8:11], v192, s[0:3], s38 offen offset:2048 sc1\n"
    "buffer_load_dwordx4 a[12:15], v192, s[0:3], s38 offen offset:3072 sc1\n"
    "buffer_load_dwordx4 a[16:19], v193, s[0:3], s38 offen offset:0 sc1\n"
    "buffer_load_dwordx4 a[20:23], v193, s[0:3], s38 offen offset:1024 sc1\n"
    "buffer_load_dwordx4 a[24:27], v193, s[0:3], s38 offen offset:2048 sc1\n"
    "buffer_load_dwordx4 a[28:31], v193, s[0:3], s38 offen offset:3072 sc1\n"
    "buffer_load_dwordx4 v[0:3], v196, s[0:3], s39 offen offset:0 sc1\n"
    "buffer_load_dwordx4 v[4:7], v196, s[0:3], s39 offen offset:1024 sc1\n"
    "s_add_u32 s38, s10, s29\n"
    "s_add_u32 s39, s11, s29\n"
    "v_mov_b32 v189, 2130706432\n"
    "s_waitcnt vmcnt(0)\n"
    ".Lct_top_%=:\n"
    ".Lct_rt5_%=:\n"
    "s_memrealtime s[22:23]\n"
    "s_waitcnt lgkmcnt(0)\n"
    "s_sub_u32 s24, s43, s22\n"
    "s_cmp_lt_u32 s24, 600\n"
    "s_cbranch_scc1 .Lct_rt5_%=\n"
    ".Lct_x0_%=:\n"
    "buffer_load_dwordx2 v[190:191], off, s[0:3], s40 sc1\n"
    "s_waitcnt vmcnt(2)\n"
    "v_mov_b64 v[8:9], v[0:1]\n"
    "v_mov_b64 v[10:11], v[2:3]\n"
    "v_mov_b64 v[12:13], v[4:5]\n"
    "v_mov_b64 v[14:15], v[6:7]\n"
    "v_mov_b32_dpp v16, v0 row_shl:4 row_mask:0xf bank_mask:0xf bound_ctrl:1\n"
    "v_mov_b32_dpp v17, v1 row_shl:4 row_mask:0xf bank_mask:0xf bound_ctrl:1\n"
    "v_mov_b32_dpp v18, v2 row_shl:4 row_mask:0xf bank_mask:0xf bound_ctrl:1\n"
    "v_mov_b32_dpp v19, v3 row_shl:4 row_mask:0xf bank_mask:0xf bound_ctrl:1\n"
    "v_mov_b32_dpp v20, v4 row_shl:4 row_mask:0xf bank_mask:0xf bound_ctrl:1\n"
    "v_mov_b32_dpp v21, v5 row_shl:4 row_mask:0xf bank_mask:0xf bound_ctrl:1\n"
    "v_mov_b32_dpp v22, v6 row_shl:4 row_mask:0xf bank_mask:0xf bound_ctrl:1\n"
    "v_mov_b32_dpp v23, v7 row_shl:4 row_mask:0xf bank_mask:0xf bound_ctrl:1\n"
    "v_mov_b32_dpp v24, v0 row_shl:8 row_mask:0xf bank_mask:0xf bound_ctrl:1\n"
    "v_mov_b32_dpp v25, v1 row_shl:8 row_mask:0xf bank_mask:0xf bound_ctrl:1\n"
    "v_mov_b32_dpp v26, v2 row_shl:8 row_mask:0xf bank_mask:0xf bound_ctrl:1\n"
    "v_mov_b32_dpp v27, v3 row_shl:8 row_mask:0xf bank_mask:0xf bound_ctrl:1\n"
    "v_mov_b32_dpp v28, v4 row_shl:8 row_mask:0xf bank_mask:0xf bound_ctrl:1\n"
    "v_mov_b32_dpp v29, v5 row_shl:8 row_mask:0xf bank_mask:0xf bound_ctrl:1\n"
    "v_mov_b32_dpp v30, v6 row_shl:8 row_mask:0xf bank_mask:0xf bound_ctrl:1\n"
    "v_mov_b32_dpp v31, v7 row_shl:8 row_mask:0xf bank_mask:0xf bound_ctrl:1\n"
    "v_mov_b32_dpp v32, v0 row_shl:12 row_mask:0xf bank_mask:0xf bound_ctrl:1\n"
    "v_mov_b32_dpp v33, v1 row_shl:12 row_mask:0xf bank_mask:0xf bound_ctrl:1\n"
    "v_mov_b32_dpp v34, v2 row_shl:12 row_mask:0xf bank_mask:0xf bound_ctrl:1\n"
    "v_mov_b32_dpp v35, v3 row_shl:12 row_mask:0xf bank_mask:0xf bound_ctrl:1\n"
    "v_mov_b32_dpp v36, v4 row_shl:12 row_mask:0xf bank_mask:0xf bound_ctrl:1\n"
    "v_mov_b32_dpp v37, v5 row_shl:12 row_mask:0xf bank_mask:0xf bound_ctrl:1\n"
    "v_mov_b32_dpp v38, v6 row_shl:12 row_mask:0xf bank_mask:0xf bound_ctrl:1\n"
    "v_mov_b32_dpp v39, v7 row_shl:12 row_mask:0xf bank_mask:0xf bound_ctrl:1\n"
    "s_waitcnt vmcnt(0)\n"
    "buffer_store_dword v188, v189, s[0:3], 0 offen sc1\n"
    "v_readfirstlane_b32 s25, v191\n"
    "v_readfirstlane_b32 s41, v190\n"
    "s_cmp_eq_u32 s25, s27\n"
    "s_cbranch_scc0 .Lct_xr0_%=\n"
    ".Lct_xg0_%=:\n"
    "s_add_u32 s43, s41, 99\n"
    "s_add_u32 s44, s41, 115\n"
    ".Lct_rt6_%=:\n"
    "s_memrealtime s[22:23]\n"
    "s_waitcnt lgkmcnt(0)\n"
    "s_sub_u32 s24, s43, s22\n"
    "s_cmp_lt_u32 s24, 600\n"
    "s_cbranch_scc1 .Lct_rt6_%=\n"
    "buffer_load_dwordx4 v[40:43], v192, s[0:3], s31 offen offset:0 sc1\n"
    "buffer_load_dwordx4 v[44:47], v192, s[0:3], s31 offen offset:1024 sc1\n"
    "buffer_load_dwordx4 v[48:51], v197, s[0:3], s32 offen offset:0 sc1\n"
    "buffer_load_dwordx4 v[52:55], v197, s[0:3], s32 offen offset:256 sc1\n"
    ".Lct_rt7_%=:\n"
    "s_memrealtime s[22:23]\n"
    "s_waitcnt lgkmcnt(0)\n"
    "s_sub_u32 s24, s44, s22\n"
    "s_cmp_lt_u32 s24, 600\n"
    "s_cbranch_scc1 .Lct_rt7_%=\n"
    "buffer_load_dwordx4 v[72:75], v193, s[0:3], s33 offen sc1\n"
    "buffer_load_dwordx4 v[56:59], v192, s[0:3], s33 offen offset:0 sc1\n"
    "buffer_load_dwordx4 v[60:63], v192, s[0:3], s33 offen offset:1024 sc1\n"
    "buffer_load_dwordx4 v[64:67], v192, s[0:3], s33 offen offset:2048 sc1\n"
    "buffer_load_dwordx4 v[68:71], v192, s[0:3], s33 offen offset:3072 sc1\n"
    ".Lct_take0_%=:\n"
    "s_waitcnt vmcnt(5)\n"
    "v_max3_u32 v184, v40, v42, v40\n"
    "v_max3_u32 v184, v44, v46, v184\n"
    "v_max3_u32 v184, v48, v50, v184\n"
    "v_max3_u32 v184, v52, v54, v184\n"
    "s_waitcnt vmcnt(4)\n"
    "v_smfmac_f32_16x16x64_bf16 v[76:79], v[72:75], v[40:47], v194\n"
    "v_max3_u32 v184, v72, v74, v184\n"
    "v_smfmac_f32_16x16x64_bf16 v[80:83], v[72:75], v[48:55], v194\n"
    "s_waitcnt vmcnt(3)\n"
    "v_smfmac_f32_16x16x64_bf16 v[80:83], v[56:59], v[8:15], v194\n"
    "v_max3_u32 v184, v56, v58, v184\n"
    "v_smfmac_f32_16x16x64_bf16 v[76:79], v[56:59], a[0:7], v194\n"
    "s_waitcnt vmcnt(2)\n"
    "v_smfmac_f32_16x16x64_bf16 v[80:83], v[60:63], v[16:23], v194\n"
    "v_max3_u32 v184, v60, v62, v184\n"
    "v_smfmac_f32_16x16x64_bf16 v[76:79], v[60:63], a[8:15], v194\n"
    "s_waitcnt vmcnt(1)\n"
    "v_smfmac_f32_16x16x64_bf16 v[80:83], v[64:67], v[24:31], v194\n"
    "v_max3_u32 v184, v64, v66, v184\n"
    "v_smfmac_f32_16x16x64_bf16 v[76:79], v[64:67], a[16:23], v194\n"
    "s_waitcnt vmcnt(0)\n"
    "v_smfmac_f32_16x16x64_bf16 v[80:83], v[68:71], v[32:39], v194\n"
    "v_max3_u32 v184, v68, v70, v184\n"
    "v_smfmac_f32_16x16x64_bf16 v[76:79], v[68:71], a[24:31], v194\n"
    "v_cmp_eq_u32 vcc, -1, v184\n"
    "s_nop 0\n"
    "s_cbranch_vccnz .Lct_retry0_%=\n"
    ".Lct_go0_%=:\n"
    "s_nop 2\n"
    "v_pk_add_f32 v[80:81], v[80:81], v[82:83]\n"
    "ds_write2_b32 v201, v80, v81 offset1:20\n"
    "v_pk_add_f32 v[76:77], v[76:77], v[78:79]\n"
    "ds_write2_b32 v200, v76, v77 offset1:20\n"
    "s_waitcnt lgkmcnt(0)\n"
    "s_barrier\n"
    "ds_read_b128 v[164:167], v202 offset:0\n"
    "ds_read_b128 v[168:171], v202 offset:640\n"
    "ds_read_b128 v[172:175], v202 offset:1280\n"
    "ds_read_b128 v[176:179], v202 offset:1920\n"
    "s_waitcnt lgkmcnt(2)\n"
    "v_pk_add_f32 v[164:165], v[164:165], v[168:169]\n"
    "v_pk_add_f32 v[166:167], v[166:167], v[170:171]\n"
    "s_waitcnt lgkmcnt(0)\n"
    "v_pk_add_f32 v[172:173], v[172:173], v[176:177]\n"
    "v_pk_add_f32 v[174:175], v[174:175], v[178:179]\n"
    "v_pk_add_f32 v[164:165], v[164:165], v[172:173]\n"
    "v_pk_add_f32 v[166:167], v[166:167], v[174:175]\n"
    "v_cvt_pk_bf16_f32 v164, v164, v165\n"
    "v_cvt_pk_bf16_f32 v165, v166, v167\n"
    "v_min_u32 v164, -2, v164\n"
    "buffer_store_dwordx2 v[164:165], v203, s[0:3], s29 offen sc1\n"
    "s_sleep 2\n"
    "buffer_load_dwordx4 v[132:135], v199, s[0:3], s34 offen offset:0 sc1\n"
    "buffer_load_dwordx4 v[136:139], v199, s[0:3], s36 offen offset:0 sc1\n"
    "buffer_load_dwordx4 v[140:143], v199, s[0:3], s34 offen offset:256 sc1\n"
    "buffer_load_dwordx4 v[144:147], v199, s[0:3], s36 offen offset:256 sc1\n"
    "buffer_load_dwordx4 v[116:119], v198, s[0:3], s34 offen sc1\n"
    "buffer_load_dwordx4 v[84:87], v192, s[0:3], s34 offen offset:0 sc1\n"
    "buffer_load_dwordx4 v[88:91], v192, s[0:3], s34 offen offset:2048 sc1\n"
    "buffer_load_dwordx4 v[120:123], v198, s[0:3], s35 offen sc1\n"
    "buffer_load_dwordx4 v[92:95], v192, s[0:3], s35 offen offset:0 sc1\n"
    "buffer_load_dwordx4 v[96:99], v192, s[0:3], s35 offen offset:2048 sc1\n"
    "buffer_load_dwordx4 v[124:127], v198, s[0:3], s36 offen sc1\n"
    "buffer_load_dwordx4 v[100:103], v192, s[0:3], s36 offen offset:0 sc1\n"
    "buffer_load_dwordx4 v[104:107], v192, s[0:3], s36 offen offset:2048 sc1\n"
    "buffer_load_dwordx4 v[128:131], v198, s[0:3], s37 offen sc1\n"
    "buffer_load_dwordx4 v[108:111], v192, s[0:3], s37 offen offset:0 sc1\n"
    "buffer_load_dwordx4 v[112:115], v192, s[0:3], s37 offen offset:2048 sc1\n"
    "buffer_load_dwordx2 v[186:187], v210, s[4:7], s46 offen sc1\n"
    "v_mov_b64 v[166:167], -1\n"
    "buffer_store_dwordx2 v[166:167], v203, s[0:3], s30 offen sc1\n"
    "s_waitcnt vmcnt(17)\n"
    "v_max3_u32 v184, v132, v134, v132\n"
    "v_swap_b32 v133, v134\n"
    "s_nop 1\n"
    "v_mfma_f32_16x16x32_bf16 v[148:151], v[132:135], v[40:43], 0\n"
    "v_mfma_f32_16x16x32_bf16 v[152:155], v[132:135], v[48:51], 0\n"
    "buffer_load_dwordx4 a[32:35], v192, s[0:3], s38 offen offset:0 sc1\n"
    "s_waitcnt vmcnt(17)\n"
    "v_max3_u32 v184, v136, v138, v184\n"
    "v_swap_b32 v137, v138\n"
    "s_nop 1\n"
    "v_mfma_f32_16x16x32_bf16 v[148:151], v[136:139], v[44:47], v[148:151]\n"
    "v_mfma_f32_16x16x32_bf16 v[152:155], v[136:139], v[52:55], v[152:155]\n"
    "buffer_load_dwordx4 a[36:39], v192, s[0:3], s38 offen offset:1024 sc1\n"
    "s_waitcnt vmcnt(17)\n"
    "v_max3_u32 v184, v140, v142, v184\n"
    "v_swap_b32 v141, v142\n"
    "s_nop 1\n"
    "v_mfma_f32_16x16x32_bf16 v[156:159], v[140:143], v[40:43], 0\n"
    "v_mfma_f32_16x16x32_bf16 v[160:163], v[140:143], v[48:51], 0\n"
    "buffer_load_dwordx4 a[40:43], v192, s[0:3], s38 offen offset:2048 sc1\n"
    "s_waitcnt vmcnt(17)\n"
    "v_max3_u32 v184, v144, v146, v184\n"
    "v_swap_b32 v145, v146\n"
    "s_nop 1\n"
    "v_mfma_f32_16x16x32_bf16 v[156:159], v[144:147], v[44:47], v[156:159]\n"
    "v_mfma_f32_16x16x32_bf16 v[160:163], v[144:147], v[52:55], v[160:163]\n"
    "buffer_load_dwordx4 a[44:47], v192, s[0:3], s38 offen offset:3072 sc1\n"
    "s_waitcnt vmcnt(17)\n"
    "v_max3_u32 v184, v116, v118, v184\n"
    "v_smfmac_f32_16x16x64_bf16 v[148:151], v[116:119], a[0:7], v195\n"
    "v_smfmac_f32_16x16x64_bf16 v[152:155], v[116:119], v[8:15], v195\n"
    "buffer_load_dwordx4 a[48:51], v193, s[0:3], s38 offen offset:0 sc1\n"
    "s_waitcnt vmcnt(17)\n"
    "v_max3_u32 v184, v84, v86, v184\n"
    "v_swap_b32 v85, v86\n"
    "s_nop 1\n"
    "v_mfma_f32_16x16x32_bf16 v[156:159], v[84:87], a[0:3], v[156:159]\n"
    "v_mfma_f32_16x16x32_bf16 v[160:163], v[84:87], v[8:11], v[160:163]\n"
    "buffer_load_dwordx4 a[52:55], v193, s[0:3], s38 offen offset:1024 sc1\n"
    "s_waitcnt vmcnt(17)\n"
    "v_max3_u32 v184, v88, v90, v184\n"
    "v_swap_b32 v89, v90\n"
    "s_nop 1\n"
    "v_mfma_f32_16x16x32_bf16 v[156:159], v[88:91], a[4:7], v[156:159]\n"
    "v_mfma_f32_16x16x32_bf16 v[160:163], v[88:91], v[12:15], v[160:163]\n"
    "buffer_load_dwordx4 a[56:59], v193, s[0:3], s38 offen offset:2048 sc1\n"
    "s_waitcnt vmcnt(17)\n"
    "v_max3_u32 v184, v120, v122, v184\n"
    "v_smfmac_f32_16x16x64_bf16 v[148:151], v[120:123], a[8:15], v195\n"
    "v_smfmac_f32_16x16x64_bf16 v[152:155], v[120:123], v[16:23], v195\n"
    "buffer_load_dwordx4 a[60:63], v193, s[0:3], s38 offen offset:3072 sc1\n"
    "s_waitcnt vmcnt(17)\n"
    "v_max3_u32 v184, v92, v94, v184\n"
    "v_swap_b32 v93, v94\n"
    "s_nop 1\n"
    "v_mfma_f32_16x16x32_bf16 v[156:159], v[92:95], a[8:11], v[156:159]\n"
    "v_mfma_f32_16x16x32_bf16 v[160:163], v[92:95], v[16:19], v[160:163]\n"
    "buffer_load_dwordx4 v[0:3], v196, s[0:3], s39 offen offset:0 sc1\n"
    "s_waitcnt vmcnt(17)\n"
    "v_max3_u32 v184, v96, v98, v184\n"
    "v_swap_b32 v97, v98\n"
    "s_nop 1\n"
    "v_mfma_f32_16x16x32_bf16 v[156:159], v[96:99], a[12:15], v[156:159]\n"
    "v_mfma_f32_16x16x32_bf16 v[160:163], v[96:99], v[20:23], v[160:163]\n"
    "buffer_load_dwordx4 v[4:7], v196, s[0:3], s39 offen offset:1024 sc1\n"
    "s_waitcnt vmcnt(17)\n"
    "v_max3_u32 v184, v124, v126, v184\n"
    "v_smfmac_f32_16x16x64_bf16 v[148:151], v[124:127], a[16:23], v195\n"
    "v_smfmac_f32_16x16x64_bf16 v[152:155], v[124:127], v[24:31], v195\n"
    "s_waitcnt vmcnt(16)\n"
    "v_max3_u32 v184, v100, v102, v184\n"
    "v_swap_b32 v101, v102\n"
    "s_nop 1\n"
    "v_mfma_f32_16x16x32_bf16 v[156:159], v[100:103], a[16:19], v[156:159]\n"
    "v_mfma_f32_16x16x32_bf16 v[160:163], v[100:103], v[24:27], v[160:163]\n"
    "s_waitcnt vmcnt(15)\n"
    "v_max3_u32 v184, v104, v106, v184\n"
    "v_swap_b32 v105, v106\n"
    "s_nop 1\n"
    "v_mfma_f32_16x16x32_bf16 v[156:159], v[104:107], a[20:23], v[156:159]\n"
    "v_mfma_f32_16x16x32_bf16 v[160:163], v[104:107], v[28:31], v[160:163]\n"
    "s_waitcnt vmcnt(14)\n"
    "v_max3_u32 v184, v128, v130, v184\n"
    "v_smfmac_f32_16x16x64_bf16 v[148:151], v[128:131], a[24:31], v195\n"
    "v_smfmac_f32_16x16x64_bf16 v[152:155], v[128:131], v[32:39], v195\n"
    "s_waitcnt vmcnt(13)\n"
    "v_max3_u32 v184, v108, v110, v184\n"
    "v_swap_b32 v109, v110\n"
    "s_nop 1\n"
    "v_mfma_f32_16x16x32_bf16 v[156:159], v[108:111], a[24:27], v[156:159]\n"
    "v_mfma_f32_16x16x32_bf16 v[160:163], v[108:111], v[32:35], v[160:163]\n"
    "s_waitcnt vmcnt(12)\n"
    "v_max3_u32 v184, v112, v114, v184\n"
    "v_swap_b32 v113, v114\n"
    "s_nop 1\n"
    "v_mfma_f32_16x16x32_bf16 v[156:159], v[112:115], a[28:31], v[156:159]\n"
    "v_mfma_f32_16x16x32_bf16 v[160:163], v[112:115], v[36:39], v[160:163]\n"
    "v_cmp_eq_u32 vcc, -1, v184\n"
    "s_nop 0\n"
    "s_cbranch_vccnz .Lct_tr0_%=\n"
    ".Lct_tg0_%=:\n"
    "ds_write2_b32 v204, v148, v149 offset0:0 offset1:20\n"
    "ds_write2_b32 v204, v150, v151 offset0:40 offset1:60\n"
    "ds_write2_b32 v205, v152, v153 offset0:0 offset1:20\n"
    "ds_write2_b32 v205, v154, v155 offset0:40 offset1:60\n"
    "ds_write2_b32 v206, v156, v157 offset0:0 offset1:20\n"
    "ds_write2_b32 v206, v158, v159 offset0:40 offset1:60\n"
    "ds_write2_b32 v207, v160, v161 offset0:0 offset1:20\n"
    "ds_write2_b32 v207, v162, v163 offset0:40 offset1:60\n"
    "v_mov_b32 v188, s27\n"
    "v_mov_b32 v189, v211\n"
    "s_waitcnt vmcnt(11)\n"
    "v_lshlrev_b32 v148, 16, v186\n"
    "v_and_b32 v149, 0xffff0000, v186\n"
    "v_lshlrev_b32 v150, 16, v187\n"
    "v_and_b32 v151, 0xffff0000, v187\n"
    "s_waitcnt lgkmcnt(0)\n"
    "s_barrier\n"
    "ds_read_b128 v[164:167], v208 offset:0\n"
    "ds_read_b128 v[168:171], v208 offset:2560\n"
    "ds_read_b128 v[172:175], v208 offset:5120\n"
    "ds_read_b128 v[176:179], v208 offset:7680\n"
    "s_waitcnt lgkmcnt(2)\n"
    "v_pk_add_f32 v[164:165], v[164:165], v[168:169]\n"
    "v_pk_add_f32 v[166:167], v[166:167], v[170:171]\n"
    "s_waitcnt lgkmcnt(0)\n"
    "v_pk_add_f32 v[172:173], v[172:173], v[176:177]\n"
    "v_pk_add_f32 v[174:175], v[174:175], v[178:179]\n"
    "v_pk_add_f32 v[164:165], v[164:165], v[172:173]\n"
    "v_pk_add_f32 v[166:167], v[166:167], v[174:175]\n"
    "v_cvt_pk_bf16_f32 v164, v164, v165\n"
    "v_cvt_pk_bf16_f32 v165, v166, v167\n"
    "v_lshlrev_b32 v168, 16, v164\n"
    "v_and_b32 v169, 0xffff0000, v164\n"
    "v_lshlrev_b32 v170, 16, v165\n"
    "v_and_b32 v171, 0xffff0000, v165\n"
    "v_pk_add_f32 v[168:169], v[168:169], v[148:149]\n"
    "v_pk_add_f32 v[170:171], v[170:171], v[150:151]\n"
    "v_cvt_pk_bf16_f32 v164, v168, v169\n"
    "v_cvt_pk_bf16_f32 v165, v170, v171\n"
    "buffer_store_dwordx2 v[164:165], v209, s[0:3], s45 offen sc1\n"
    "s_add_u32 s26, s26, 1\n"
    "s_add_u32 s27, s26, 1\n"
    "s_add_u32 s28, s26, 0\n"
    "s_and_b32 s28, s28, 3\n"
    "s_lshl_b32 s28, s28, 22\n"
    "s_add_u32 s29, s26, 1\n"
    "s_and_b32 s29, s29, 3\n"
    "s_lshl_b32 s29, s29, 22\n"
    "s_add_u32 s30, s26, 3\n"
    "s_and_b32 s30, s30, 3\n"
    "s_lshl_b32 s30, s30, 22\n"
    "s_and_b32 s40, s26, 63\n"
    "s_lshl_b32 s40, s40, 7\n"
    "s_add_u32 s31, s12, s28\n"
    "s_add_u32 s32, s13, s28\n"
    "s_add_u32 s33, s15, s28\n"
    "s_add_u32 s34, s14, s28\n"
    "s_add_u32 s38, s10, s29\n"
    "s_add_u32 s39, s11, s29\n"
    "s_add_u32 s40, s16, s40\n"
    "s_add_u32 s35, s34, 4096\n"
    "s_add_u32 s36, s34, 16384\n"
    "s_add_u32 s37, s34, 20480\n"
    "s_add_u32 s43, s41, 356\n"
    "s_lshl_b32 s45, s26, 19\n"
    "s_lshl_b32 s25, s26, 11\n"
    "s_xor_b32 s25, s25, s8\n"
    "s_and_b32 s25, s25, 0x2000\n"
    "s_add_u32 s45, s45, s25\n"
    "s_add_u32 s45, s45, s9\n"
    "s_mul_i32 s46, s26, 40960\n"
    "s_cmp_eq_u32 s8, 0\n"
    "s_cselect_b32 s46, s46, s45\n"
    "v_mov_b64 v[76:77], 0\n"
    "v_mov_b64 v[78:79], 0\n"
    "v_mov_b64 v[80:81], 0\n"
    "v_mov_b64 v[82:83], 0\n"
    ".Lct_rt11_%=:\n"
    "s_memrealtime s[22:23]\n"
    "s_waitcnt lgkmcnt(0)\n"
    "s_sub_u32 s24, s43, s22\n"
    "s_cmp_lt_u32 s24, 600\n"
    "s_cbranch_scc1 .Lct_rt11_%=\n"
    ".Lct_x1_%=:\n"
    "buffer_load_dwordx2 v[190:191], off, s[0:3], s40 sc1\n"
    "s_waitcnt vmcnt(2)\n"
    "v_mov_b64 v[8:9], v[0:1]\n"
    "v_mov_b64 v[10:11], v[2:3]\n"
    "v_mov_b64 v[12:13], v[4:5]\n"
    "v_mov_b64 v[14:15], v[6:7]\n"
    "v_mov_b32_dpp v16, v0 row_shl:4 row_mask:0xf bank_mask:0xf bound_ctrl:1\n"
    "v_mov_b32_dpp v17, v1 row_shl:4 row_mask:0xf bank_mask:0xf bound_ctrl:1\n"
    "v_mov_b32_dpp v18, v2 row_shl:4 row_mask:0xf bank_mask:0xf bound_ctrl:1\n"
    "v_mov_b32_dpp v19, v3 row_shl:4 row_mask:0xf bank_mask:0xf bound_ctrl:1\n"
    "v_mov_b32_dpp v20, v4 row_shl:4 row_mask:0xf bank_mask:0xf bound_ctrl:1\n"
    "v_mov_b32_dpp v21, v5 row_shl:4 row_mask:0xf bank_mask:0xf bound_ctrl:1\n"
    "v_mov_b32_dpp v22, v6 row_shl:4 row_mask:0xf bank_mask:0xf bound_ctrl:1\n"
    "v_mov_b32_dpp v23, v7 row_shl:4 row_mask:0xf bank_mask:0xf bound_ctrl:1\n"
    "v_mov_b32_dpp v24, v0 row_shl:8 row_mask:0xf bank_mask:0xf bound_ctrl:1\n"
    "v_mov_b32_dpp v25, v1 row_shl:8 row_mask:0xf bank_mask:0xf bound_ctrl:1\n"
    "v_mov_b32_dpp v26, v2 row_shl:8 row_mask:0xf bank_mask:0xf bound_ctrl:1\n"
    "v_mov_b32_dpp v27, v3 row_shl:8 row_mask:0xf bank_mask:0xf bound_ctrl:1\n"
    "v_mov_b32_dpp v28, v4 row_shl:8 row_mask:0xf bank_mask:0xf bound_ctrl:1\n"
    "v_mov_b32_dpp v29, v5 row_shl:8 row_mask:0xf bank_mask:0xf bound_ctrl:1\n"
    "v_mov_b32_dpp v30, v6 row_shl:8 row_mask:0xf bank_mask:0xf bound_ctrl:1\n"
    "v_mov_b32_dpp v31, v7 row_shl:8 row_mask:0xf bank_mask:0xf bound_ctrl:1\n"
    "v_mov_b32_dpp v32, v0 row_shl:12 row_mask:0xf bank_mask:0xf bound_ctrl:1\n"
    "v_mov_b32_dpp v33, v1 row_shl:12 row_mask:0xf bank_mask:0xf bound_ctrl:1\n"
    "v_mov_b32_dpp v34, v2 row_shl:12 row_mask:0xf bank_mask:0xf bound_ctrl:1\n"
    "v_mov_b32_dpp v35, v3 row_shl:12 row_mask:0xf bank_mask:0xf bound_ctrl:1\n"
    "v_mov_b32_dpp v36, v4 row_shl:12 row_mask:0xf bank_mask:0xf bound_ctrl:1\n"
    "v_mov_b32_dpp v37, v5 row_shl:12 row_mask:0xf bank_mask:0xf bound_ctrl:1\n"
    "v_mov_b32_dpp v38, v6 row_shl:12 row_mask:0xf bank_mask:0xf bound_ctrl:1\n"
    "v_mov_b32_dpp v39, v7 row_shl:12 row_mask:0xf bank_mask:0xf bound_ctrl:1\n"
    "s_waitcnt vmcnt(0)\n"
    "buffer_store_dword v188, v189, s[0:3], 0 offen sc1\n"
    "v_readfirstlane_b32 s25, v191\n"
    "v_readfirstlane_b32 s41, v190\n"
    "s_cmp_eq_u32 s25, s27\n"
    "s_cbranch_scc0 .Lct_xr1_%=\n"
    ".Lct_xg1_%=:\n"
    "s_add_u32 s43, s41, 99\n"
    "s_add_u32 s44, s41, 115\n"
    ".Lct_rt12_%=:\n"
    "s_memrealtime s[22:23]\n"
    "s_waitcnt lgkmcnt(0)\n"
    "s_sub_u32 s24, s43, s22\n"
    "s_cmp_lt_u32 s24, 600\n"
    "s_cbranch_scc1 .Lct_rt12_%=\n"
    "buffer_load_dwordx4 v[40:43], v192, s[0:3], s31 offen offset:0 sc1\n"
    "buffer_load_dwordx4 v[44:47], v192, s[0:3], s31 offen offset:1024 sc1\n"
    "buffer_load_dwordx4 v[48:51], v197, s[0:3], s32 offen offset:0 sc1\n"
    "buffer_load_dwordx4 v[52:55], v197, s[0:3], s32 offen offset:256 sc1\n"
    ".Lct_rt13_%=:\n"
    "s_memrealtime s[22:23]\n"
    "s_waitcnt lgkmcnt(0)\n"
    "s_sub_u32 s24, s44, s22\n"
    "s_cmp_lt_u32 s24, 600\n"
    "s_cbranch_scc1 .Lct_rt13_%=\n"
    "buffer_load_dwordx4 v[72:75], v193, s[0:3], s33 offen sc1\n"
    "buffer_load_dwordx4 v[56:59], v192, s[0:3], s33 offen offset:0 sc1\n"
    "buffer_load_dwordx4 v[60:63], v192, s[0:3], s33 offen offset:1024 sc1\n"
    "buffer_load_dwordx4 v[64:67], v192, s[0:3], s33 offen offset:2048 sc1\n"
    "buffer_load_dwordx4 v[68:71], v192, s[0:3], s33 offen offset:3072 sc1\n"
    ".Lct_take1_%=:\n"
    "s_waitcnt vmcnt(5)\n"
    "v_max3_u32 v184, v40, v42, v40\n"
    "v_max3_u32 v184, v44, v46, v184\n"
    "v_max3_u32 v184, v48, v50, v184\n"
    "v_max3_u32 v184, v52, v54, v184\n"
    "s_waitcnt vmcnt(4)\n"
    "v_smfmac_f32_16x16x64_bf16 v[76:79], v[72:75], v[40:47], v194\n"
    "v_max3_u32 v184, v72, v74, v184\n"
    "v_smfmac_f32_16x16x64_bf16 v[80:83], v[72:75], v[48:55], v194\n"
    "s_waitcnt vmcnt(3)\n"
    "v_smfmac_f32_16x16x64_bf16 v[80:83], v[56:59], v[8:15], v194\n"
    "v_max3_u32 v184, v56, v58, v184\n"
    "v_smfmac_f32_16x16x64_bf16 v[76:79], v[56:59], a[32:39], v194\n"
    "s_waitcnt vmcnt(2)\n"
    "v_smfmac_f32_16x16x64_bf16 v[80:83], v[60:63], v[16:23], v194\n"
    "v_max3_u32 v184, v60, v62, v184\n"
    "v_smfmac_f32_16x16x64_bf16 v[76:79], v[60:63], a[40:47], v194\n"
    "s_waitcnt vmcnt(1)\n"
    "v_smfmac_f32_16x16x64_bf16 v[80:83], v[64:67], v[24:31], v194\n"
    "v_max3_u32 v184, v64, v66, v184\n"
    "v_smfmac_f32_16x16x64_bf16 v[76:79], v[64:67], a[48:55], v194\n"
    "s_waitcnt vmcnt(0)\n"
    "v_smfmac_f32_16x16x64_bf16 v[80:83], v[68:71], v[32:39], v194\n"
    "v_max3_u32 v184, v68, v70, v184\n"
    "v_smfmac_f32_16x16x64_bf16 v[76:79], v[68:71], a[56:63], v194\n"
    "v_cmp_eq_u32 vcc, -1, v184\n"
    "s_nop 0\n"
    "s_cbranch_vccnz .Lct_retry1_%=\n"
    ".Lct_go1_%=:\n"
    "s_nop 2\n"
    "v_pk_add_f32 v[80:81], v[80:81], v[82:83]\n"
    "ds_write2_b32 v201, v80, v81 offset1:20\n"
    "v_pk_add_f32 v[76:77], v[76:77], v[78:79]\n"
    "ds_write2_b32 v200, v76, v77 offset1:20\n"
    "s_waitcnt lgkmcnt(0)\n"
    "s_barrier\n"
    "ds_read_b128 v[164:167], v202 offset:0\n"
    "ds_read_b128 v[168:171], v202 offset:640\n"
    "ds_read_b128 v[172:175], v202 offset:1280\n"
    "ds_read_b128 v[176:179], v202 offset:1920\n"
    "s_waitcnt lgkmcnt(2)\n"
    "v_pk_add_f32 v[164:165], v[164:165], v[168:169]\n"
    "v_pk_add_f32 v[166:167], v[166:167], v[170:171]\n"
    "s_waitcnt lgkmcnt(0)\n"
    "v_pk_add_f32 v[172:173], v[172:173], v[176:177]\n"
    "v_pk_add_f32 v[174:175], v[174:175], v[178:179]\n"
    "v_pk_add_f32 v[164:165], v[164:165], v[172:173]\n"
    "v_pk_add_f32 v[166:167], v[166:167], v[174:175]\n"
    "v_cvt_pk_bf16_f32 v164, v164, v165\n"
    "v_cvt_pk_bf16_f32 v165, v166, v167\n"
    "v_min_u32 v164, -2, v164\n"
    "buffer_store_dwordx2 v[164:165], v203, s[0:3], s29 offen sc1\n"
    "s_sleep 2\n"
    "buffer_load_dwordx4 v[132:135], v199, s[0:3], s34 offen offset:0 sc1\n"
    "buffer_load_dwordx4 v[136:139], v199, s[0:3], s36 offen offset:0 sc1\n"
    "buffer_load_dwordx4 v[140:143], v199, s[0:3], s34 offen offset:256 sc1\n"
    "buffer_load_dwordx4 v[144:147], v199, s[0:3], s36 offen offset:256 sc1\n"
    "buffer_load_dwordx4 v[116:119], v198, s[0:3], s34 offen sc1\n"
    "buffer_load_dwordx4 v[84:87], v192, s[0:3], s34 offen offset:0 sc1\n"
    "buffer_load_dwordx4 v[88:91], v192, s[0:3], s34 offen offset:2048 sc1\n"
    "buffer_load_dwordx4 v[120:123], v198, s[0:3], s35 offen sc1\n"
    "buffer_load_dwordx4 v[92:95], v192, s[0:3], s35 offen offset:0 sc1\n"
    "buffer_load_dwordx4 v[96:99], v192, s[0:3], s35 offen offset:2048 sc1\n"
    "buffer_load_dwordx4 v[124:127], v198, s[0:3], s36 offen sc1\n"
    "buffer_load_dwordx4 v[100:103], v192, s[0:3], s36 offen offset:0 sc1\n"
    "buffer_load_dwordx4 v[104:107], v192, s[0:3], s36 offen offset:2048 sc1\n"
    "buffer_load_dwordx4 v[128:131], v198, s[0:3], s37 offen sc1\n"
    "buffer_load_dwordx4 v[108:111], v192, s[0:3], s37 offen offset:0 sc1\n"
    "buffer_load_dwordx4 v[112:115], v192, s[0:3], s37 offen offset:2048 sc1\n"
    "buffer_load_dwordx2 v[186:187], v210, s[4:7], s46 offen sc1\n"
    "v_mov_b64 v[166:167], -1\n"
    "buffer_store_dwordx2 v[166:167], v203, s[0:3], s30 offen sc1\n"
    "s_waitcnt vmcnt(17)\n"
    "v_max3_u32 v184, v132, v134, v132\n"
    "v_swap_b32 v133, v134\n"
    "s_nop 1\n"
    "v_mfma_f32_16x16x32_bf16 v[148:151], v[132:135], v[40:43], 0\n"
    "v_mfma_f32_16x16x32_bf16 v[152:155], v[132:135], v[48:51], 0\n"
    "buffer_load_dwordx4 a[0:3], v192, s[0:3], s38 offen offset:0 sc1\n"
    "s_waitcnt vmcnt(17)\n"
    "v_max3_u32 v184, v136, v138, v184\n"
    "v_swap_b32 v137, v138\n"
    "s_nop 1\n"
    "v_mfma_f32_16x16x32_bf16 v[148:151], v[136:139], v[44:47], v[148:151]\n"
    "v_mfma_f32_16x16x32_bf16 v[152:155], v[136:139], v[52:55], v[152:155]\n"
    "buffer_load_dwordx4 a[4:7], v192, s[0:3], s38 offen offset:1024 sc1\n"
    "s_waitcnt vmcnt(17)\n"
    "v_max3_u32 v184, v140, v142, v184\n"
    "v_swap_b32 v141, v142\n"
    "s_nop 1\n"
    "v_mfma_f32_16x16x32_bf16 v[156:159], v[140:143], v[40:43], 0\n"
    "v_mfma_f32_16x16x32_bf16 v[160:163], v[140:143], v[48:51], 0\n"
    "buffer_load_dwordx4 a[8:11], v192, s[0:3], s38 offen offset:2048 sc1\n"
    "s_waitcnt vmcnt(17)\n"
    "v_max3_u32 v184, v144, v146, v184\n"
    "v_swap_b32 v145, v146\n"
    "s_nop 1\n"
    "v_mfma_f32_16x16x32_bf16 v[156:159], v[144:147], v[44:47], v[156:159]\n"
    "v_mfma_f32_16x16x32_bf16 v[160:163], v[144:147], v[52:55], v[160:163]\n"
    "buffer_load_dwordx4 a[12:15], v192, s[0:3], s38 offen offset:3072 sc1\n"
    "s_waitcnt vmcnt(17)\n"
    "v_max3_u32 v184, v116, v118, v184\n"
    "v_smfmac_f32_16x16x64_bf16 v[148:151], v[116:119], a[32:39], v195\n"
    "v_smfmac_f32_16x16x64_bf16 v[152:155], v[116:119], v[8:15], v195\n"
    "buffer_load_dwordx4 a[16:19], v193, s[0:3], s38 offen offset:0 sc1\n"
    "s_waitcnt vmcnt(17)\n"
    "v_max3_u32 v184, v84, v86, v184\n"
    "v_swap_b32 v85, v86\n"
    "s_nop 1\n"
    "v_mfma_f32_16x16x32_bf16 v[156:159], v[84:87], a[32:35], v[156:159]\n"
    "v_mfma_f32_16x16x32_bf16 v[160:163], v[84:87], v[8:11], v[160:163]\n"
    "buffer_load_dwordx4 a[20:23], v193, s[0:3], s38 offen offset:1024 sc1\n"
    "s_waitcnt vmcnt(17)\n"
    "v_max3_u32 v184, v88, v90, v184\n"
    "v_swap_b32 v89, v90\n"
    "s_nop 1\n"
    "v_mfma_f32_16x16x32_bf16 v[156:159], v[88:91], a[36:39], v[156:159]\n"
    "v_mfma_f32_16x16x32_bf16 v[160:163], v[88:91], v[12:15], v[160:163]\n"
    "buffer_load_dwordx4 a[24:27], v193, s[0:3], s38 offen offset:2048 sc1\n"
    "s_waitcnt vmcnt(17)\n"
    "v_max3_u32 v184, v120, v122, v184\n"
    "v_smfmac_f32_16x16x64_bf16 v[148:151], v[120:123], a[40:47], v195\n"
    "v_smfmac_f32_16x16x64_bf16 v[152:155], v[120:123], v[16:23], v195\n"
    "buffer_load_dwordx4 a[28:31], v193, s[0:3], s38 offen offset:3072 sc1\n"
    "s_waitcnt vmcnt(17)\n"
    "v_max3_u32 v184, v92, v94, v184\n"
    "v_swap_b32 v93, v94\n"
    "s_nop 1\n"
    "v_mfma_f32_16x16x32_bf16 v[156:159], v[92:95], a[40:43], v[156:159]\n"
    "v_mfma_f32_16x16x32_bf16 v[160:163], v[92:95], v[16:19], v[160:163]\n"
    "buffer_load_dwordx4 v[0:3], v196, s[0:3], s39 offen offset:0 sc1\n"
    "s_waitcnt vmcnt(17)\n"
    "v_max3_u32 v184, v96, v98, v184\n"
    "v_swap_b32 v97, v98\n"
    "s_nop 1\n"
    "v_mfma_f32_16x16x32_bf16 v[156:159], v[96:99], a[44:47], v[156:159]\n"
    "v_mfma_f32_16x16x32_bf16 v[160:163], v[96:99], v[20:23], v[160:163]\n"
    "buffer_load_dwordx4 v[4:7], v196, s[0:3], s39 offen offset:1024 sc1\n"
    "s_waitcnt vmcnt(17)\n"
    "v_max3_u32 v184, v124, v126, v184\n"
    "v_smfmac_f32_16x16x64_bf16 v[148:151], v[124:127], a[48:55], v195\n"
    "v_smfmac_f32_16x16x64_bf16 v[152:155], v[124:127], v[24:31], v195\n"
    "s_waitcnt vmcnt(16)\n"
    "v_max3_u32 v184, v100, v102, v184\n"
    "v_swap_b32 v101, v102\n"
    "s_nop 1\n"
    "v_mfma_f32_16x16x32_bf16 v[156:159], v[100:103], a[48:51], v[156:159]\n"
    "v_mfma_f32_16x16x32_bf16 v[160:163], v[100:103], v[24:27], v[160:163]\n"
    "s_waitcnt vmcnt(15)\n"
    "v_max3_u32 v184, v104, v106, v184\n"
    "v_swap_b32 v105, v106\n"
    "s_nop 1\n"
    "v_mfma_f32_16x16x32_bf16 v[156:159], v[104:107], a[52:55], v[156:159]\n"
    "v_mfma_f32_16x16x32_bf16 v[160:163], v[104:107], v[28:31], v[160:163]\n"
    "s_waitcnt vmcnt(14)\n"
    "v_max3_u32 v184, v128, v130, v184\n"
    "v_smfmac_f32_16x16x64_bf16 v[148:151], v[128:131], a[56:63], v195\n"
    "v_smfmac_f32_16x16x64_bf16 v[152:155], v[128:131], v[32:39], v195\n"
    "s_waitcnt vmcnt(13)\n"
    "v_max3_u32 v184, v108, v110, v184\n"
    "v_swap_b32 v109, v110\n"
    "s_nop 1\n"
    "v_mfma_f32_16x16x32_bf16 v[156:159], v[108:111], a[56:59], v[156:159]\n"
    "v_mfma_f32_16x16x32_bf16 v[160:163], v[108:111], v[32:35], v[160:163]\n"
    "s_waitcnt vmcnt(12)\n"
    "v_max3_u32 v184, v112, v114, v184\n"
    "v_swap_b32 v113, v114\n"
    "s_nop 1\n"
    "v_mfma_f32_16x16x32_bf16 v[156:159], v[112:115], a[60:63], v[156:159]\n"
    "v_mfma_f32_16x16x32_bf16 v[160:163], v[112:115], v[36:39], v[160:163]\n"
    "v_cmp_eq_u32 vcc, -1, v184\n"
    "s_nop 0\n"
    "s_cbranch_vccnz .Lct_tr1_%=\n"
    ".Lct_tg1_%=:\n"
    "ds_write2_b32 v204, v148, v149 offset0:0 offset1:20\n"
    "ds_write2_b32 v204, v150, v151 offset0:40 offset1:60\n"
    "ds_write2_b32 v205, v152, v153 offset0:0 offset1:20\n"
    "ds_write2_b32 v205, v154, v155 offset0:40 offset1:60\n"
    "ds_write2_b32 v206, v156, v157 offset0:0 offset1:20\n"
    "ds_write2_b32 v206, v158, v159 offset0:40 offset1:60\n"
    "ds_write2_b32 v207, v160, v161 offset0:0 offset1:20\n"
    "ds_write2_b32 v207, v162, v163 offset0:40 offset1:60\n"
    "v_mov_b32 v188, s27\n"
    "v_mov_b32 v189, v211\n"
    "s_waitcnt vmcnt(11)\n"
    "v_lshlrev_b32 v148, 16, v186\n"
    "v_and_b32 v149, 0xffff0000, v186\n"
    "v_lshlrev_b32 v150, 16, v187\n"
    "v_and_b32 v151, 0xffff0000, v187\n"
    "s_waitcnt lgkmcnt(0)\n"
    "s_barrier\n"
    "ds_read_b128 v[164:167], v208 offset:0\n"
    "ds_read_b128 v[168:171], v208 offset:2560\n"
    "ds_read_b128 v[172:175], v208 offset:5120\n"
    "ds_read_b128 v[176:179], v208 offset:7680\n"
    "s_waitcnt lgkmcnt(2)\n"
    "v_pk_add_f32 v[164:165], v[164:165], v[168:169]\n"
    "v_pk_add_f32 v[166:167], v[166:167], v[170:171]\n"
    "s_waitcnt lgkmcnt(0)\n"
    "v_pk_add_f32 v[172:173], v[172:173], v[176:177]\n"
    "v_pk_add_f32 v[174:175], v[174:175], v[178:179]\n"
    "v_pk_add_f32 v[164:165], v[164:165], v[172:173]\n"
    "v_pk_add_f32 v[166:167], v[166:167], v[174:175]\n"
    "v_cvt_pk_bf16_f32 v164, v164, v165\n"
    "v_cvt_pk_bf16_f32 v165, v166, v167\n"
    "v_lshlrev_b32 v168, 16, v164\n"
    "v_and_b32 v169, 0xffff0000, v164\n"
    "v_lshlrev_b32 v170, 16, v165\n"
    "v_and_b32 v171, 0xffff0000, v165\n"
    "v_pk_add_f32 v[168:169], v[168:169], v[148:149]\n"
    "v_pk_add_f32 v[170:171], v[170:171], v[150:151]\n"
    "v_cvt_pk_bf16_f32 v164, v168, v169\n"
    "v_cvt_pk_bf16_f32 v165, v170, v171\n"
    "buffer_store_dwordx2 v[164:165], v209, s[0:3], s45 offen sc1\n"
    "s_add_u32 s26, s26, 1\n"
    "s_add_u32 s27, s26, 1\n"
    "s_add_u32 s28, s26, 0\n"
    "s_and_b32 s28, s28, 3\n"
    "s_lshl_b32 s28, s28, 22\n"
    "s_add_u32 s29, s26, 1\n"
    "s_and_b32 s29, s29, 3\n"
    "s_lshl_b32 s29, s29, 22\n"
    "s_add_u32 s30, s26, 3\n"
    "s_and_b32 s30, s30, 3\n"
    "s_lshl_b32 s30, s30, 22\n"
    "s_and_b32 s40, s26, 63\n"
    "s_lshl_b32 s40, s40, 7\n"
    "s_add_u32 s31, s12, s28\n"
    "s_add_u32 s32, s13, s28\n"
    "s_add_u32 s33, s15, s28\n"
    "s_add_u32 s34, s14, s28\n"
    "s_add_u32 s38, s10, s29\n"
    "s_add_u32 s39, s11, s29\n"
    "s_add_u32 s40, s16, s40\n"
    "s_add_u32 s35, s34, 4096\n"
    "s_add_u32 s36, s34, 16384\n"
    "s_add_u32 s37, s34, 20480\n"
    "s_add_u32 s43, s41, 356\n"
    "s_lshl_b32 s45, s26, 19\n"
    "s_lshl_b32 s25, s26, 11\n"
    "s_xor_b32 s25, s25, s8\n"
    "s_and_b32 s25, s25, 0x2000\n"
    "s_add_u32 s45, s45, s25\n"
    "s_add_u32 s45, s45, s9\n"
    "s_mul_i32 s46, s26, 40960\n"
    "s_cmp_eq_u32 s8, 0\n"
    "s_cselect_b32 s46, s46, s45\n"
    "v_mov_b64 v[76:77], 0\n"
    "v_mov_b64 v[78:79], 0\n"
    "v_mov_b64 v[80:81], 0\n"
    "v_mov_b64 v[82:83], 0\n"
    "s_cmp_lt_u32 s26, 64\n"
    "s_cbranch_scc1 .Lct_top_%=\n"
    "s_waitcnt vmcnt(0)\n"
    "buffer_store_dword v188, v189, s[0:3], 0 offen sc1\n"
    "s_waitcnt vmcnt(0) lgkmcnt(0)\n"
    "s_endpgm\n"
    ".Lct_xr0_%=:\n"
    "s_add_u32 s20, s20, 1\n"
    "s_cmp_lt_u32 s20, 0x40000\n"
    "s_cbranch_scc1 .Lct_sp8_%=\n"
    "v_mov_b32 v184, 42\n"
    "s_mov_b32 s24, 33728\n"
    "buffer_store_dword v184, off, s[0:3], s24 sc1\n"
    "s_waitcnt vmcnt(0)\n"
    "s_branch .Lct_xg0_%=\n"
    ".Lct_sp8_%=:\n"
    "s_sleep 4\n"
    "s_branch .Lct_x0_%=\n"
    ".Lct_retry0_%=:\n"
    "s_add_u32 s20, s20, 1\n"
    "s_cmp_lt_u32 s20, 0x40000\n"
    "s_cbranch_scc1 .Lct_sp9_%=\n"
    "v_mov_b32 v184, 3\n"
    "s_mov_b32 s24, 33728\n"
    "buffer_store_dword v184, off, s[0:3], s24 sc1\n"
    "s_waitcnt vmcnt(0)\n"
    "s_branch .Lct_go0_%=\n"
    ".Lct_sp9_%=:\n"
    "s_waitcnt vmcnt(0)\n"
    "v_mov_b64 v[76:77], 0\n"
    "v_mov_b64 v[78:79], 0\n"
    "v_mov_b64 v[80:81], 0\n"
    "v_mov_b64 v[82:83], 0\n"
    "buffer_load_dwordx4 v[40:43], v192, s[0:3], s31 offen offset:0 sc1\n"
    "buffer_load_dwordx4 v[44:47], v192, s[0:3], s31 offen offset:1024 sc1\n"
    "buffer_load_dwordx4 v[48:51], v197, s[0:3], s32 offen offset:0 sc1\n"
    "buffer_load_dwordx4 v[52:55], v197, s[0:3], s32 offen offset:256 sc1\n"
    "buffer_load_dwordx4 v[72:75], v193, s[0:3], s33 offen sc1\n"
    "buffer_load_dwordx4 v[56:59], v192, s[0:3], s33 offen offset:0 sc1\n"
    "buffer_load_dwordx4 v[60:63], v192, s[0:3], s33 offen offset:1024 sc1\n"
    "buffer_load_dwordx4 v[64:67], v192, s[0:3], s33 offen offset:2048 sc1\n"
    "buffer_load_dwordx4 v[68:71], v192, s[0:3], s33 offen offset:3072 sc1\n"
    "s_waitcnt vmcnt(0)\n"
    "s_branch .Lct_take0_%=\n"
    ".Lct_tr0_%=:\n"
    "s_add_u32 s20, s20, 1\n"
    "s_cmp_lt_u32 s20, 0x40000\n"
    "s_cbranch_scc1 .Lct_sp10_%=\n"
    "v_mov_b32 v184, 4\n"
    "s_mov_b32 s24, 33728\n"
    "buffer_store_dword v184, off, s[0:3], s24 sc1\n"
    "s_waitcnt vmcnt(0)\n"
    "s_branch .Lct_tg0_%=\n"
    ".Lct_sp10_%=:\n"
    "buffer_load_dwordx4 v[132:135], v199, s[0:3], s34 offen offset:0 sc1\n"
    "buffer_load_dwordx4 v[136:139], v199, s[0:3], s36 offen offset:0 sc1\n"
    "buffer_load_dwordx4 v[140:143], v199, s[0:3], s34 offen offset:256 sc1\n"
    "buffer_load_dwordx4 v[144:147], v199, s[0:3], s36 offen offset:256 sc1\n"
    "buffer_load_dwordx4 v[116:119], v198, s[0:3], s34 offen sc1\n"
    "buffer_load_dwordx4 v[84:87], v192, s[0:3], s34 offen offset:0 sc1\n"
    "buffer_load_dwordx4 v[88:91], v192, s[0:3], s34 offen offset:2048 sc1\n"
    "buffer_load_dwordx4 v[120:123], v198, s[0:3], s35 offen sc1\n"
    "buffer_load_dwordx4 v[92:95], v192, s[0:3], s35 offen offset:0 sc1\n"
    "buffer_load_dwordx4 v[96:99], v192, s[0:3], s35 offen offset:2048 sc1\n"
    "buffer_load_dwordx4 v[124:127], v198, s[0:3], s36 offen sc1\n"
    "buffer_load_dwordx4 v[100:103], v192, s[0:3], s36 offen offset:0 sc1\n"
    "buffer_load_dwordx4 v[104:107], v192, s[0:3], s36 offen offset:2048 sc1\n"
    "buffer_load_dwordx4 v[128:131], v198, s[0:3], s37 offen sc1\n"
    "buffer_load_dwordx4 v[108:111], v192, s[0:3], s37 offen offset:0 sc1\n"
    "buffer_load_dwordx4 v[112:115], v192, s[0:3], s37 offen offset:2048 sc1\n"
    "s_waitcnt vmcnt(0)\n"
    "v_max3_u32 v184, v132, v134, v132\n"
    "v_swap_b32 v133, v134\n"
    "s_nop 1\n"
    "v_mfma_f32_16x16x32_bf16 v[148:151], v[132:135], v[40:43], 0\n"
    "v_mfma_f32_16x16x32_bf16 v[152:155], v[132:135], v[48:51], 0\n"
    "v_max3_u32 v184, v136, v138, v184\n"
    "v_swap_b32 v137, v138\n"
    "s_nop 1\n"
    "v_mfma_f32_16x16x32_bf16 v[148:151], v[136:139], v[44:47], v[148:151]\n"
    "v_mfma_f32_16x16x32_bf16 v[152:155], v[136:139], v[52:55], v[152:155]\n"
    "v_max3_u32 v184, v140, v142, v184\n"
    "v_swap_b32 v141, v142\n"
    "s_nop 1\n"
    "v_mfma_f32_16x16x32_bf16 v[156:159], v[140:143], v[40:43], 0\n"
    "v_mfma_f32_16x16x32_bf16 v[160:163], v[140:143], v[48:51], 0\n"
    "v_max3_u32 v184, v144, v146, v184\n"
    "v_swap_b32 v145, v146\n"
    "s_nop 1\n"
    "v_mfma_f32_16x16x32_bf16 v[156:159], v[144:147], v[44:47], v[156:159]\n"
    "v_mfma_f32_16x16x32_bf16 v[160:163], v[144:147], v[52:55], v[160:163]\n"
    "v_max3_u32 v184, v116, v118, v184\n"
    "v_smfmac_f32_16x16x64_bf16 v[148:151], v[116:119], a[0:7], v195\n"
    "v_smfmac_f32_16x16x64_bf16 v[152:155], v[116:119], v[8:15], v195\n"
    "v_max3_u32 v184, v84, v86, v184\n"
    "v_swap_b32 v85, v86\n"
    "s_nop 1\n"
    "v_mfma_f32_16x16x32_bf16 v[156:159], v[84:87], a[0:3], v[156:159]\n"
    "v_mfma_f32_16x16x32_bf16 v[160:163], v[84:87], v[8:11], v[160:163]\n"
    "v_max3_u32 v184, v88, v90, v184\n"
    "v_swap_b32 v89, v90\n"
    "s_nop 1\n"
    "v_mfma_f32_16x16x32_bf16 v[156:159], v[88:91], a[4:7], v[156:159]\n"
    "v_mfma_f32_16x16x32_bf16 v[160:163], v[88:91], v[12:15], v[160:163]\n"
    "v_max3_u32 v184, v120, v122, v184\n"
    "v_smfmac_f32_16x16x64_bf16 v[148:151], v[120:123], a[8:15], v195\n"
    "v_smfmac_f32_16x16x64_bf16 v[152:155], v[120:123], v[16:23], v195\n"
    "v_max3_u32 v184, v92, v94, v184\n"
    "v_swap_b32 v93, v94\n"
    "s_nop 1\n"
    "v_mfma_f32_16x16x32_bf16 v[156:159], v[92:95], a[8:11], v[156:159]\n"
    "v_mfma_f32_16x16x32_bf16 v[160:163], v[92:95], v[16:19], v[160:163]\n"
    "v_max3_u32 v184, v96, v98, v184\n"
    "v_swap_b32 v97, v98\n"
    "s_nop 1\n"
    "v_mfma_f32_16x16x32_bf16 v[156:159], v[96:99], a[12:15], v[156:159]\n"
    "v_mfma_f32_16x16x32_bf16 v[160:163], v[96:99], v[20:23], v[160:163]\n"
    "v_max3_u32 v184, v124, v126, v184\n"
    "v_smfmac_f32_16x16x64_bf16 v[148:151], v[124:127], a[16:23], v195\n"
    "v_smfmac_f32_16x16x64_bf16 v[152:155], v[124:127], v[24:31], v195\n"
    "v_max3_u32 v184, v100, v102, v184\n"
    "v_swap_b32 v101, v102\n"
    "s_nop 1\n"
    "v_mfma_f32_16x16x32_bf16 v[156:159], v[100:103], a[16:19], v[156:159]\n"
    "v_mfma_f32_16x16x32_bf16 v[160:163], v[100:103], v[24:27], v[160:163]\n"
    "v_max3_u32 v184, v104, v106, v184\n"
    "v_swap_b32 v105, v106\n"
    "s_nop 1\n"
    "v_mfma_f32_16x16x32_bf16 v[156:159], v[104:107], a[20:23], v[156:159]\n"
    "v_mfma_f32_16x16x32_bf16 v[160:163], v[104:107], v[28:31], v[160:163]\n"
    "v_max3_u32 v184, v128, v130, v184\n"
    "v_smfmac_f32_16x16x64_bf16 v[148:151], v[128:131], a[24:31], v195\n"
    "v_smfmac_f32_16x16x64_bf16 v[152:155], v[128:131], v[32:39], v195\n"
    "v_max3_u32 v184, v108, v110, v184\n"
    "v_swap_b32 v109, v110\n"
    "s_nop 1\n"
    "v_mfma_f32_16x16x32_bf16 v[156:159], v[108:111], a[24:27], v[156:159]\n"
    "v_mfma_f32_16x16x32_bf16 v[160:163], v[108:111], v[32:35], v[160:163]\n"
    "v_max3_u32 v184, v112, v114, v184\n"
    "v_swap_b32 v113, v114\n"
    "s_nop 1\n"
    "v_mfma_f32_16x16x32_bf16 v[156:159], v[112:115], a[28:31], v[156:159]\n"
    "v_mfma_f32_16x16x32_bf16 v[160:163], v[112:115], v[36:39], v[160:163]\n"
    "v_cmp_eq_u32 vcc, -1, v184\n"
    "s_nop 0\n"
    "s_cbranch_vccnz .Lct_tr0_%=\n"
    "s_nop 7\n"
    "s_branch .Lct_tg0_%=\n"
    ".Lct_xr1_%=:\n"
    "s_add_u32 s20, s20, 1\n"
    "s_cmp_lt_u32 s20, 0x40000\n"
    "s_cbranch_scc1 .Lct_sp14_%=\n"
    "v_mov_b32 v184, 42\n"
    "s_mov_b32 s24, 33728\n"
    "buffer_store_dword v184, off, s[0:3], s24 sc1\n"
    "s_waitcnt vmcnt(0)\n"
    "s_branch .Lct_xg1_%=\n"
    ".Lct_sp14_%=:\n"
    "s_sleep 4\n"
    "s_branch .Lct_x1_%=\n"
    ".Lct_retry1_%=:\n"
    "s_add_u32 s20, s20, 1\n"
    "s_cmp_lt_u32 s20, 0x40000\n"
    "s_cbranch_scc1 .Lct_sp15_%=\n"
    "v_mov_b32 v184, 3\n"
    "s_mov_b32 s24, 33728\n"
    "buffer_store_dword v184, off, s[0:3], s24 sc1\n"
    "s_waitcnt vmcnt(0)\n"
    "s_branch .Lct_go1_%=\n"
    ".Lct_sp15_%=:\n"
    "s_waitcnt vmcnt(0)\n"
    "v_mov_b64 v[76:77], 0\n"
    "v_mov_b64 v[78:79], 0\n"
    "v_mov_b64 v[80:81], 0\n"
    "v_mov_b64 v[82:83], 0\n"
    "buffer_load_dwordx4 v[40:43], v192, s[0:3], s31 offen offset:0 sc1\n"
    "buffer_load_dwordx4 v[44:47], v192, s[0:3], s31 offen offset:1024 sc1\n"
    "buffer_load_dwordx4 v[48:51], v197, s[0:3], s32 offen offset:0 sc1\n"
    "buffer_load_dwordx4 v[52:55], v197, s[0:3], s32 offen offset:256 sc1\n"
    "buffer_load_dwordx4 v[72:75], v193, s[0:3], s33 offen sc1\n"
    "buffer_load_dwordx4 v[56:59], v192, s[0:3], s33 offen offset:0 sc1\n"
    "buffer_load_dwordx4 v[60:63], v192, s[0:3], s33 offen offset:1024 sc1\n"
    "buffer_load_dwordx4 v[64:67], v192, s[0:3], s33 offen offset:2048 sc1\n"
    "buffer_load_dwordx4 v[68:71], v192, s[0:3], s33 offen offset:3072 sc1\n"
    "s_waitcnt vmcnt(0)\n"
    "s_branch .Lct_take1_%=\n"
    ".Lct_tr1_%=:\n"
    "s_add_u32 s20, s20, 1\n"
    "s_cmp_lt_u32 s20, 0x40000\n"
    "s_cbranch_scc1 .Lct_sp16_%=\n"
    "v_mov_b32 v184, 4\n"
    "s_mov_b32 s24, 33728\n"
    "buffer_store_dword v184, off, s[0:3], s24 sc1\n"
    "s_waitcnt vmcnt(0)\n"
    "s_branch .Lct_tg1_%=\n"
    ".Lct_sp16_%=:\n"
    "buffer_load_dwordx4 v[132:135], v199, s[0:3], s34 offen offset:0 sc1\n"
    "buffer_load_dwordx4 v[136:139], v199, s[0:3], s36 offen offset:0 sc1\n"
    "buffer_load_dwordx4 v[140:143], v199, s[0:3], s34 offen offset:256 sc1\n"
    "buffer_load_dwordx4 v[144:147], v199, s[0:3], s36 offen offset:256 sc1\n"
    "buffer_load_dwordx4 v[116:119], v198, s[0:3], s34 offen sc1\n"
    "buffer_load_dwordx4 v[84:87], v192, s[0:3], s34 offen offset:0 sc1\n"
    "buffer_load_dwordx4 v[88:91], v192, s[0:3], s34 offen offset:2048 sc1\n"
    "buffer_load_dwordx4 v[120:123], v198, s[0:3], s35 offen sc1\n"
    "buffer_load_dwordx4 v[92:95], v192, s[0:3], s35 offen offset:0 sc1\n"
    "buffer_load_dwordx4 v[96:99], v192, s[0:3], s35 offen offset:2048 sc1\n"
    "buffer_load_dwordx4 v[124:127], v198, s[0:3], s36 offen sc1\n"
    "buffer_load_dwordx4 v[100:103], v192, s[0:3], s36 offen offset:0 sc1\n"
    "buffer_load_dwordx4 v[104:107], v192, s[0:3], s36 offen offset:2048 sc1\n"
    "buffer_load_dwordx4 v[128:131], v198, s[0:3], s37 offen sc1\n"
    "buffer_load_dwordx4 v[108:111], v192, s[0:3], s37 offen offset:0 sc1\n"
    "buffer_load_dwordx4 v[112:115], v192, s[0:3], s37 offen offset:2048 sc1\n"
    "s_waitcnt vmcnt(0)\n"
    "v_max3_u32 v184, v132, v134, v132\n"
    "v_swap_b32 v133, v134\n"
    "s_nop 1\n"
    "v_mfma_f32_16x16x32_bf16 v[148:151], v[132:135], v[40:43], 0\n"
    "v_mfma_f32_16x16x32_bf16 v[152:155], v[132:135], v[48:51], 0\n"
    "v_max3_u32 v184, v136, v138, v184\n"
    "v_swap_b32 v137, v138\n"
    "s_nop 1\n"
    "v_mfma_f32_16x16x32_bf16 v[148:151], v[136:139], v[44:47], v[148:151]\n"
    "v_mfma_f32_16x16x32_bf16 v[152:155], v[136:139], v[52:55], v[152:155]\n"
    "v_max3_u32 v184, v140, v142, v184\n"
    "v_swap_b32 v141, v142\n"
    "s_nop 1\n"
    "v_mfma_f32_16x16x32_bf16 v[156:159], v[140:143], v[40:43], 0\n"
    "v_mfma_f32_16x16x32_bf16 v[160:163], v[140:143], v[48:51], 0\n"
    "v_max3_u32 v184, v144, v146, v184\n"
    "v_swap_b32 v145, v146\n"
    "s_nop 1\n"
    "v_mfma_f32_16x16x32_bf16 v[156:159], v[144:147], v[44:47], v[156:159]\n"
    "v_mfma_f32_16x16x32_bf16 v[160:163], v[144:147], v[52:55], v[160:163]\n"
    "v_max3_u32 v184, v116, v118, v184\n"
    "v_smfmac_f32_16x16x64_bf16 v[148:151], v[116:119], a[32:39], v195\n"
    "v_smfmac_f32_16x16x64_bf16 v[152:155], v[116:119], v[8:15], v195\n"
    "v_max3_u32 v184, v84, v86, v184\n"
    "v_swap_b32 v85, v86\n"
    "s_nop 1\n"
    "v_mfma_f32_16x16x32_bf16 v[156:159], v[84:87], a[32:35], v[156:159]\n"
    "v_mfma_f32_16x16x32_bf16 v[160:163], v[84:87], v[8:11], v[160:163]\n"
    "v_max3_u32 v184, v88, v90, v184\n"
    "v_swap_b32 v89, v90\n"
    "s_nop 1\n"
    "v_mfma_f32_16x16x32_bf16 v[156:159], v[88:91], a[36:39], v[156:159]\n"
    "v_mfma_f32_16x16x32_bf16 v[160:163], v[88:91], v[12:15], v[160:163]\n"
    "v_max3_u32 v184, v120, v122, v184\n"
    "v_smfmac_f32_16x16x64_bf16 v[148:151], v[120:123], a[40:47], v195\n"
    "v_smfmac_f32_16x16x64_bf16 v[152:155], v[120:123], v[16:23], v195\n"
    "v_max3_u32 v184, v92, v94, v184\n"
    "v_swap_b32 v93, v94\n"
    "s_nop 1\n"
    "v_mfma_f32_16x16x32_bf16 v[156:159], v[92:95], a[40:43], v[156:159]\n"
    "v_mfma_f32_16x16x32_bf16 v[160:163], v[92:95], v[16:19], v[160:163]\n"
    "v_max3_u32 v184, v96, v98, v184\n"
    "v_swap_b32 v97, v98\n"
    "s_nop 1\n"
    "v_mfma_f32_16x16x32_bf16 v[156:159], v[96:99], a[44:47], v[156:159]\n"
    "v_mfma_f32_16x16x32_bf16 v[160:163], v[96:99], v[20:23], v[160:163]\n"
    "v_max3_u32 v184, v124, v126, v184\n"
    "v_smfmac_f32_16x16x64_bf16 v[148:151], v[124:127], a[48:55], v195\n"
    "v_smfmac_f32_16x16x64_bf16 v[152:155], v[124:127], v[24:31], v195\n"
    "v_max3_u32 v184, v100, v102, v184\n"
    "v_swap_b32 v101, v102\n"
    "s_nop 1\n"
    "v_mfma_f32_16x16x32_bf16 v[156:159], v[100:103], a[48:51], v[156:159]\n"
    "v_mfma_f32_16x16x32_bf16 v[160:163], v[100:103], v[24:27], v[160:163]\n"
    "v_max3_u32 v184, v104, v106, v184\n"
    "v_swap_b32 v105, v106\n"
    "s_nop 1\n"
    "v_mfma_f32_16x16x32_bf16 v[156:159], v[104:107], a[52:55], v[156:159]\n"
    "v_mfma_f32_16x16x32_bf16 v[160:163], v[104:107], v[28:31], v[160:163]\n"
    "v_max3_u32 v184, v128, v130, v184\n"
    "v_smfmac_f32_16x16x64_bf16 v[148:151], v[128:131], a[56:63], v195\n"
    "v_smfmac_f32_16x16x64_bf16 v[152:155], v[128:131], v[32:39], v195\n"
    "v_max3_u32 v184, v108, v110, v184\n"
    "v_swap_b32 v109, v110\n"
    "s_nop 1\n"
    "v_mfma_f32_16x16x32_bf16 v[156:159], v[108:111], a[56:59], v[156:159]\n"
    "v_mfma_f32_16x16x32_bf16 v[160:163], v[108:111], v[32:35], v[160:163]\n"
    "v_max3_u32 v184, v112, v114, v184\n"
    "v_swap_b32 v113, v114\n"
    "s_nop 1\n"
    "v_mfma_f32_16x16x32_bf16 v[156:159], v[112:115], a[60:63], v[156:159]\n"
    "v_mfma_f32_16x16x32_bf16 v[160:163], v[112:115], v[36:39], v[160:163]\n"
    "v_cmp_eq_u32 vcc, -1, v184\n"
    "s_nop 0\n"
    "s_cbranch_vccnz .Lct_tr1_%=\n"
    "s_nop 7\n"
    "s_branch .Lct_tg1_%=\n"
    :: "{s[0:3]}"(s0_), "{s[4:7]}"(s4_), "{s[8:11]}"(s8_), "{s[12:15]}"(s12_), "{s[16:19]}"(s16_), "{v[192:195]}"(v192_), "{v[196:199]}"(v196_), "{v[200:203]}"(v200_), "{v[204:207]}"(v204_), "{v[208:211]}"(v208_) : "memory", "v255", "a255", "s95");
    __builtin_unreachable();
}
DEV void qw_role(const MegaArgs& A, const int L, const int h, const int rep) {
    ROLE_HEAD;
    const u32x4 s0_ = {RFL(rx[0]), RFL(rx[1]), RFL(rx[2]), RFL(rx[3])};
    const u32x4 s4_ = {RFL(lo(A.Wq + h * 192 + 64)), RFL(hi(A.Wq + h * 192 + 64)), RFL(WBYTES - h * 384 - 128), RFL(0x20000u)};
    const u32x4 s8_ = {RFL(lo(A.Wv + h * 192 + 128)), RFL(hi(A.Wv + h * 192 + 128)), RFL(WBYTES - h * 384 - 256), RFL(0x20000u)};
    const u32x4 s12_ = {RFL(w), RFL(REC0 + w * 2048), RFL(QT0 + h * 32768), RFL((A.layers & 2) ? 64 : 0)};
    const u32x4 s16_ = {RFL(xl(Z_CT + (h & 7) * 256, L)), RFL(xl(Z_XN + rep * 2048, 1)), RFL(DBG0 + ((L * 128 + 112 + (h >> 3) * 4 + (h & 3)) * 4 + w) * 2048), RFL(0u)};
    const u32x4 v0_ = {(unsigned)((8 * w + (l >> 3)) * 12288 + ((l & 7) ^ (((8 * w + (l >> 3)) >> 1) & 6)) * 16), (unsigned)((8 * w + (l >> 3)) * 12288 + ((l & 7) ^ (((8 * w + (l >> 3)) >> 1) & 6)) * 16 + 128), (unsigned)((8 * lq + ((l >> 2) & 1) + ((l >> 1) & 4)) * 128 + (w ^ (((8 * lq + ((l >> 2) & 1) + ((l >> 1) & 4)) >> 2) & 3)) * 32 + (l & 3) * 8), (unsigned)(0u)};
    const u32x4 v64_ = {(unsigned)(l * 16), (unsigned)(l * 16 + 40960), (unsigned)(l * 16 + w * 2048), (unsigned)(l * 16 + w * 1024 + 16384)};
    const u32x4 v68_ = {(unsigned)(l < 20 ? (l >> 2) * 128 + (l & 3) * 4 : 0), (unsigned)(l == 0 ? xl(Z_QW + h * 128 + w * 4, 0) : OOB), (unsigned)(0u), (unsigned)(0u)};
    asm volatile(
    "s_mov_b32 s20, 0\n"
    "s_lshl_b32 s32, s12, 10\n"
    "s_mov_b32 s38, 0\n"
    "s_add_u32 m0, s32, 0\n"
    "s_nop 0\n"
    "buffer_load_dwordx4 v0, s[4:7], s38 offen nt lds\n"
    "s_add_u32 m0, s32, 4096\n"
    "s_nop 0\n"
    "buffer_load_dwordx4 v1, s[4:7], s38 offen nt lds\n"
    "s_add_u32 m0, s32, 8192\n"
    "s_nop 0\n"
    "buffer_load_dwordx4 v0, s[8:11], s38 offen nt lds\n"
    "s_mov_b32 s38, 393216\n"
    "s_add_u32 m0, s32, 12288\n"
    "s_nop 0\n"
    "buffer_load_dwordx4 v0, s[4:7], s38 offen nt lds\n"
    "s_add_u32 m0, s32, 16384\n"
    "s_nop 0\n"
    "buffer_load_dwordx4 v1, s[4:7], s38 offen nt lds\n"
    "s_add_u32 m0, s32, 20480\n"
    "s_nop 0\n"
    "buffer_load_dwordx4 v0, s[8:11], s38 offen nt lds\n"
    "s_mov_b32 s38, 786432\n"
    "s_add_u32 m0, s32, 24576\n"
    "s_nop 0\n"
    "buffer_load_dwordx4 v0, s[4:7], s38 offen nt lds\n"
    "s_add_u32 m0, s32, 28672\n"
    "s_nop 0\n"
    "buffer_load_dwordx4 v1, s[4:7], s38 offen nt lds\n"
    "s_add_u32 m0, s32, 32768\n"
    "s_nop 0\n"
    "buffer_load_dwordx4 v0, s[8:11], s38 offen nt lds\n"
    "s_mov_b32 s38, 1179648\n"
    "s_add_u32 m0, s32, 36864\n"
    "s_nop 0\n"
    "buffer_load_dwordx4 v0, s[4:7], s38 offen nt lds\n"
    "s_add_u32 m0, s32, 40960\n"
    "s_nop 0\n"
    "buffer_load_dwordx4 v1, s[4:7], s38 offen nt lds\n"
    "s_add_u32 m0, s32, 45056\n"
    "s_nop 0\n"
    "buffer_load_dwordx4 v0, s[8:11], s38 offen nt lds\n"
    "s_mov_b32 s38, 1572864\n"
    "s_add_u32 m0, s32, 49152\n"
    "s_nop 0\n"
    "buffer_load_dwordx4 v0, s[4:7], s38 offen nt lds\n"
    "s_add_u32 m0, s32, 53248\n"
    "s_nop 0\n"
    "buffer_load_dwordx4 v1, s[4:7], s38 offen nt lds\n"
    "s_add_u32 m0, s32, 57344\n"
    "s_nop 0\n"
    "buffer_load_dwordx4 v0, s[8:11], s38 offen nt lds\n"
    "s_mov_b32 s38, 1966080\n"
    "s_add_u32 m0, s32, 61440\n"
    "s_nop 0\n"
    "buffer_load_dwordx4 v0, s[4:7], s38 offen nt lds\n"
    "s_add_u32 m0, s32, 65536\n"
    "s_nop 0\n"
    "buffer_load_dwordx4 v1, s[4:7], s38 offen nt lds\n"
    "s_add_u32 m0, s32, 69632\n"
    "s_nop 0\n"
    "buffer_load_dwordx4 v0, s[8:11], s38 offen nt lds\n"
    "s_mov_b32 s38, 2359296\n"
    "s_add_u32 m0, s32, 73728\n"
    "s_nop 0\n"
    "buffer_load_dwordx4 v0, s[4:7], s38 offen nt lds\n"
    "s_add_u32 m0, s32, 77824\n"
    "s_nop 0\n"
    "buffer_load_dwordx4 v1, s[4:7], s38 offen nt lds\n"
    "s_add_u32 m0, s32, 81920\n"
    "s_nop 0\n"
    "buffer_load_dwordx4 v0, s[8:11], s38 offen nt lds\n"
    "s_mov_b32 s38, 2752512\n"
    "s_add_u32 m0, s32, 86016\n"
    "s_nop 0\n"
    "buffer_load_dwordx4 v0, s[4:7], s38 offen nt lds\n"
    "s_add_u32 m0, s32, 90112\n"
    "s_nop 0\n"
    "buffer_load_dwordx4 v1, s[4:7], s38 offen nt lds\n"
    "s_add_u32 m0, s32, 94208\n"
    "s_nop 0\n"
    "buffer_load_dwordx4 v0, s[8:11], s38 offen nt lds\n"
    "s_mov_b32 s38, 3145728\n"
    "s_add_u32 m0, s32, 98304\n"
    "s_nop 0\n"
    "buffer_load_dwordx4 v0, s[4:7], s38 offen nt lds\n"
    "s_add_u32 m0, s32, 102400\n"
    "s_nop 0\n"
    "buffer_load_dwordx4 v1, s[4:7], s38 offen nt lds\n"
    "s_add_u32 m0, s32, 106496\n"
    "s_nop 0\n"
    "buffer_load_dwordx4 v0, s[8:11], s38 offen nt lds\n"
    "s_mov_b32 s38, 3538944\n"
    "s_add_u32 m0, s32, 110592\n"
    "s_nop 0\n"
    "buffer_load_dwordx4 v0, s[4:7], s38 offen nt lds\n"
    "s_add_u32 m0, s32, 114688\n"
    "s_nop 0\n"
    "buffer_load_dwordx4 v1, s[4:7], s38 offen nt lds\n"
    "s_add_u32 m0, s32, 118784\n"
    "s_nop 0\n"
    "buffer_load_dwordx4 v0, s[8:11], s38 offen nt lds\n"
    "s_mov_b32 s38, 3932160\n"
    "s_add_u32 m0, s32, 122880\n"
    "s_nop 0\n"
    "buffer_load_dwordx4 v0, s[4:7], s38 offen nt lds\n"
    "s_add_u32 m0, s32, 126976\n"
    "s_nop 0\n"
    "buffer_load_dwordx4 v1, s[4:7], s38 offen nt lds\n"
    "s_add_u32 m0, s32, 131072\n"
    "s_nop 0\n"
    "buffer_load_dwordx4 v0, s[8:11], s38 offen nt lds\n"
    "s_mov_b32 s38, 4325376\n"
    "s_add_u32 m0, s32, 135168\n"
    "s_nop 0\n"
    "buffer_load_dwordx4 v0, s[4:7], s38 offen nt lds\n"
    "s_add_u32 m0, s32, 139264\n"
    "s_nop 0\n"
    "buffer_load_dwordx4 v1, s[4:7], s38 offen nt lds\n"
    "s_add_u32 m0, s32, 143360\n"
    "s_nop 0\n"
    "buffer_load_dwordx4 v0, s[8:11], s38 offen nt lds\n"
    "s_waitcnt vmcnt(24)\n"
    "s_barrier\n"
    "v_add_u32 v8, 0, v2\n"
    "ds_read_b64_tr_b16 a[0:1], v8 offset:0\n"
    "ds_read_b64_tr_b16 a[2:3], v8 offset:256\n"
    "ds_read_b64_tr_b16 a[80:81], v8 offset:4096\n"
    "ds_read_b64_tr_b16 a[82:83], v8 offset:4352\n"
    "ds_read_b64_tr_b16 a[160:161], v8 offset:8192\n"
    "ds_read_b64_tr_b16 a[162:163], v8 offset:8448\n"
    "ds_read_b64_tr_b16 a[4:5], v8 offset:12288\n"
    "ds_read_b64_tr_b16 a[6:7], v8 offset:12544\n"
    "ds_read_b64_tr_b16 a[84:85], v8 offset:16384\n"
    "ds_read_b64_tr_b16 a[86:87], v8 offset:16640\n"
    "ds_read_b64_tr_b16 a[164:165], v8 offset:20480\n"
    "ds_read_b64_tr_b16 a[166:167], v8 offset:20736\n"
    "ds_read_b64_tr_b16 a[8:9], v8 offset:24576\n"
    "ds_read_b64_tr_b16 a[10:11], v8 offset:24832\n"
    "ds_read_b64_tr_b16 a[88:89], v8 offset:28672\n"
    "ds_read_b64_tr_b16 a[90:91], v8 offset:28928\n"
    "ds_read_b64_tr_b16 a[168:169], v8 offset:32768\n"
    "ds_read_b64_tr_b16 a[170:171], v8 offset:33024\n"
    "ds_read_b64_tr_b16 a[12:13], v8 offset:36864\n"
    "ds_read_b64_tr_b16 a[14:15], v8 offset:37120\n"
    "ds_read_b64_tr_b16 a[92:93], v8 offset:40960\n"
    "ds_read_b64_tr_b16 a[94:95], v8 offset:41216\n"
    "ds_read_b64_tr_b16 a[172:173], v8 offset:45056\n"
    "ds_read_b64_tr_b16 a[174:175], v8 offset:45312\n"
    "s_waitcnt lgkmcnt(0)\n"
    "s_barrier\n"
    "s_mov_b32 s38, 4718592\n"
    "s_add_u32 m0, s32, 0\n"
    "s_nop 0\n"
    "buffer_load_dwordx4 v0, s[4:7], s38 offen nt lds\n"
    "s_add_u32 m0, s32, 4096\n"
    "s_nop 0\n"
    "buffer_load_dwordx4 v1, s[4:7], s38 offen nt lds\n"
    "s_add_u32 m0, s32, 8192\n"
    "s_nop 0\n"
    "buffer_load_dwordx4 v0, s[8:11], s38 offen nt lds\n"
    "s_mov_b32 s38, 5111808\n"
    "s_add_u32 m0, s32, 12288\n"
    "s_nop 0\n"
    "buffer_load_dwordx4 v0, s[4:7], s38 offen nt lds\n"
    "s_add_u32 m0, s32, 16384\n"
    "s_nop 0\n"
    "buffer_load_dwordx4 v1, s[4:7], s38 offen nt lds\n"
    "s_add_u32 m0, s32, 20480\n"
    "s_nop 0\n"
    "buffer_load_dwordx4 v0, s[8:11], s38 offen nt lds\n"
    "s_mov_b32 s38, 5505024\n"
    "s_add_u32 m0, s32, 24576\n"
    "s_nop 0\n"
    "buffer_load_dwordx4 v0, s[4:7], s38 offen nt lds\n"
    "s_add_u32 m0, s32, 28672\n"
    "s_nop 0\n"
    "buffer_load_dwordx4 v1, s[4:7], s38 offen nt lds\n"
    "s_add_u32 m0, s32, 32768\n"
    "s_nop 0\n"
    "buffer_load_dwordx4 v0, s[8:11], s38 offen nt lds\n"
    "s_mov_b32 s38, 5898240\n"
    "s_add_u32 m0, s32, 36864\n"
    "s_nop 0\n"
    "buffer_load_dwordx4 v0, s[4:7], s38 offen nt lds\n"
    "s_add_u32 m0, s32, 40960\n"
    "s_nop 0\n"
    "buffer_load_dwordx4 v1, s[4:7], s38 offen nt lds\n"
    "s_add_u32 m0, s32, 45056\n"
    "s_nop 0\n"
    "buffer_load_dwordx4 v0, s[8:11], s38 offen nt lds\n"
    "s_waitcnt vmcnt(24)\n"
    "s_barrier\n"
    "v_add_u32 v8, 49152, v2\n"
    "ds_read_b64_tr_b16 a[16:17], v8 offset:0\n"
    "ds_read_b64_tr_b16 a[18:19], v8 offset:256\n"
    "ds_read_b64_tr_b16 a[96:97], v8 offset:4096\n"
    "ds_read_b64_tr_b16 a[98:99], v8 offset:4352\n"
    "ds_read_b64_tr_b16 a[176:177], v8 offset:8192\n"
    "ds_read_b64_tr_b16 a[178:179], v8 offset:8448\n"
    "ds_read_b64_tr_b16 a[20:21], v8 offset:12288\n"
    "ds_read_b64_tr_b16 a[22:23], v8 offset:12544\n"
    "ds_read_b64_tr_b16 a[100:101], v8 offset:16384\n"
    "ds_read_b64_tr_b16 a[102:103], v8 offset:16640\n"
    "ds_read_b64_tr_b16 a[180:181], v8 offset:20480\n"
    "ds_read_b64_tr_b16 a[182:183], v8 offset:20736\n"
    "ds_read_b64_tr_b16 a[24:25], v8 offset:24576\n"
    "ds_read_b64_tr_b16 a[26:27], v8 offset:24832\n"
    "ds_read_b64_tr_b16 a[104:105], v8 offset:28672\n"
    "ds_read_b64_tr_b16 a[106:107], v8 offset:28928\n"
    "ds_read_b64_tr_b16 a[184:185], v8 offset:32768\n"
    "ds_read_b64_tr_b16 a[186:187], v8 offset:33024\n"
    "ds_read_b64_tr_b16 a[28:29], v8 offset:36864\n"
    "ds_read_b64_tr_b16 a[30:31], v8 offset:37120\n"
    "ds_read_b64_tr_b16 a[108:109], v8 offset:40960\n"
    "ds_read_b64_tr_b16 a[110:111], v8 offset:41216\n"
    "ds_read_b64_tr_b16 a[188:189], v8 offset:45056\n"
    "ds_read_b64_tr_b16 a[190:191], v8 offset:45312\n"
    "s_waitcnt lgkmcnt(0)\n"
    "s_barrier\n"
    "s_mov_b32 s38, 6291456\n"
    "s_add_u32 m0, s32, 49152\n"
    "s_nop 0\n"
    "buffer_load_dwordx4 v0, s[4:7], s38 offen nt lds\n"
    "s_add_u32 m0, s32, 53248\n"
    "s_nop 0\n"
    "buffer_load_dwordx4 v1, s[4:7], s38 offen nt lds\n"
    "s_add_u32 m0, s32, 57344\n"
    "s_nop 0\n"
    "buffer_load_dwordx4 v0, s[8:11], s38 offen nt lds\n"
    "s_mov_b32 s38, 6684672\n"
    "s_add_u32 m0, s32, 61440\n"
    "s_nop 0\n"
    "buffer_load_dwordx4 v0, s[4:7], s38 offen nt lds\n"
    "s_add_u32 m0, s32, 65536\n"
    "s_nop 0\n"
    "buffer_load_dwordx4 v1, s[4:7], s38 offen nt lds\n"
    "s_add_u32 m0, s32, 69632\n"
    "s_nop 0\n"
    "buffer_load_dwordx4 v0, s[8:11], s38 offen nt lds\n"
    "s_mov_b32 s38, 7077888\n"
    "s_add_u32 m0, s32, 73728\n"
    "s_nop 0\n"
    "buffer_load_dwordx4 v0, s[4:7], s38 offen nt lds\n"
    "s_add_u32 m0, s32, 77824\n"
    "s_nop 0\n"
    "buffer_load_dwordx4 v1, s[4:7], s38 offen nt lds\n"
    "s_add_u32 m0, s32, 81920\n"
    "s_nop 0\n"
    "buffer_load_dwordx4 v0, s[8:11], s38 offen nt lds\n"
    "s_mov_b32 s38, 7471104\n"
    "s_add_u32 m0, s32, 86016\n"
    "s_nop 0\n"
    "buffer_load_dwordx4 v0, s[4:7], s38 offen nt lds\n"
    "s_add_u32 m0, s32, 90112\n"
    "s_nop 0\n"
    "buffer_load_dwordx4 v1, s[4:7], s38 offen nt lds\n"
    "s_add_u32 m0, s32, 94208\n"
    "s_nop 0\n"
    "buffer_load_dwordx4 v0, s[8:11], s38 offen nt lds\n"
    "s_waitcnt vmcnt(24)\n"
    "s_barrier\n"
    "v_add_u32 v8, 98304, v2\n"
    "ds_read_b64_tr_b16 a[32:33], v8 offset:0\n"
    "ds_read_b64_tr_b16 a[34:35], v8 offset:256\n"
    "ds_read_b64_tr_b16 a[112:113], v8 offset:4096\n"
    "ds_read_b64_tr_b16 a[114:115], v8 offset:4352\n"
    "ds_read_b64_tr_b16 a[192:193], v8 offset:8192\n"
    "ds_read_b64_tr_b16 a[194:195], v8 offset:8448\n"
    "ds_read_b64_tr_b16 a[36:37], v8 offset:12288\n"
    "ds_read_b64_tr_b16 a[38:39], v8 offset:12544\n"
    "ds_read_b64_tr_b16 a[116:117], v8 offset:16384\n"
    "ds_read_b64_tr_b16 a[118:119], v8 offset:16640\n"
    "ds_read_b64_tr_b16 a[196:197], v8 offset:20480\n"
    "ds_read_b64_tr_b16 a[198:199], v8 offset:20736\n"
    "ds_read_b64_tr_b16 a[40:41], v8 offset:24576\n"
    "ds_read_b64_tr_b16 a[42:43], v8 offset:24832\n"
    "ds_read_b64_tr_b16 a[120:121], v8 offset:28672\n"
    "ds_read_b64_tr_b16 a[122:123], v8 offset:28928\n"
    "ds_read_b64_tr_b16 a[200:201], v8 offset:32768\n"
    "ds_read_b64_tr_b16 a[202:203], v8 offset:33024\n"
    "ds_read_b64_tr_b16 a[44:45], v8 offset:36864\n"
    "ds_read_b64_tr_b16 a[46:47], v8 offset:37120\n"
    "ds_read_b64_tr_b16 a[124:125], v8 offset:40960\n"
    "ds_read_b64_tr_b16 a[126:127], v8 offset:41216\n"
    "ds_read_b64_tr_b16 a[204:205], v8 offset:45056\n"
    "ds_read_b64_tr_b16 a[206:207], v8 offset:45312\n"
    "s_waitcnt lgkmcnt(0)\n"
    "s_barrier\n"
    "s_waitcnt vmcnt(12)\n"
    "s_barrier\n"
    "v_add_u32 v8, 0, v2\n"
    "ds_read_b64_tr_b16 a[48:49], v8 offset:0\n"
    "ds_read_b64_tr_b16 a[50:51], v8 offset:256\n"
    "ds_read_b64_tr_b16 a[128:129], v8 offset:4096\n"
    "ds_read_b64_tr_b16 a[130:131], v8 offset:4352\n"
    "ds_read_b64_tr_b16 a[208:209], v8 offset:8192\n"
    "ds_read_b64_tr_b16 a[210:211], v8 offset:8448\n"
    "ds_read_b64_tr_b16 a[52:53], v8 offset:12288\n"
    "ds_read_b64_tr_b16 a[54:55], v8 offset:12544\n"
    "ds_read_b64_tr_b16 a[132:133], v8 offset:16384\n"
    "ds_read_b64_tr_b16 a[134:135], v8 offset:16640\n"
    "ds_read_b64_tr_b16 a[212:213], v8 offset:20480\n"
    "ds_read_b64_tr_b16 a[214:215], v8 offset:20736\n"
    "ds_read_b64_tr_b16 a[56:57], v8 offset:24576\n"
    "ds_read_b64_tr_b16 a[58:59], v8 offset:24832\n"
    "ds_read_b64_tr_b16 a[136:137], v8 offset:28672\n"
    "ds_read_b64_tr_b16 a[138:139], v8 offset:28928\n"
    "ds_read_b64_tr_b16 a[216:217], v8 offset:32768\n"
    "ds_read_b64_tr_b16 a[218:219], v8 offset:33024\n"
    "ds_read_b64_tr_b16 a[60:61], v8 offset:36864\n"
    "ds_read_b64_tr_b16 a[62:63], v8 offset:37120\n"
    "ds_read_b64_tr_b16 a[140:141], v8 offset:40960\n"
    "ds_read_b64_tr_b16 a[142:143], v8 offset:41216\n"
    "ds_read_b64_tr_b16 a[220:221], v8 offset:45056\n"
    "ds_read_b64_tr_b16 a[222:223], v8 offset:45312\n"
    "s_waitcnt lgkmcnt(0)\n"
    "s_barrier\n"
    "s_waitcnt vmcnt(0)\n"
    "s_barrier\n"
    "v_add_u32 v8, 49152, v2\n"
    "ds_read_b64_tr_b16 a[64:65], v8 offset:0\n"
    "ds_read_b64_tr_b16 a[66:67], v8 offset:256\n"
    "ds_read_b64_tr_b16 a[144:145], v8 offset:4096\n"
    "ds_read_b64_tr_b16 a[146:147], v8 offset:4352\n"
    "ds_read_b64_tr_b16 a[224:225], v8 offset:8192\n"
    "ds_read_b64_tr_b16 a[226:227], v8 offset:8448\n"
    "ds_read_b64_tr_b16 a[68:69], v8 offset:12288\n"
    "ds_read_b64_tr_b16 a[70:71], v8 offset:12544\n"
    "ds_read_b64_tr_b16 a[148:149], v8 offset:16384\n"
    "ds_read_b64_tr_b16 a[150:151], v8 offset:16640\n"
    "ds_read_b64_tr_b16 a[228:229], v8 offset:20480\n"
    "ds_read_b64_tr_b16 a[230:231], v8 offset:20736\n"
    "ds_read_b64_tr_b16 a[72:73], v8 offset:24576\n"
    "ds_read_b64_tr_b16 a[74:75], v8 offset:24832\n"
    "ds_read_b64_tr_b16 a[152:153], v8 offset:28672\n"
    "ds_read_b64_tr_b16 a[154:155], v8 offset:28928\n"
    "ds_read_b64_tr_b16 a[232:233], v8 offset:32768\n"
    "ds_read_b64_tr_b16 a[234:235], v8 offset:33024\n"
    "ds_read_b64_tr_b16 a[76:77], v8 offset:36864\n"
    "ds_read_b64_tr_b16 a[78:79], v8 offset:37120\n"
    "ds_read_b64_tr_b16 a[156:157], v8 offset:40960\n"
    "ds_read_b64_tr_b16 a[158:159], v8 offset:41216\n"
    "ds_read_b64_tr_b16 a[236:237], v8 offset:45056\n"
    "ds_read_b64_tr_b16 a[238:239], v8 offset:45312\n"
    "s_waitcnt lgkmcnt(0)\n"
    "s_barrier\n"
    "s_mov_b32 s26, -2\n"
    "v_lshrrev_b32 v4, 2, v64\n"
    ".Lqw_po1_%=:\n"
    "buffer_load_dword v8, v4, s[0:3], s16 offen sc1\n"
    "s_waitcnt vmcnt(0)\n"
    "v_cmp_gt_i32 vcc, 1, v8\n"
    "s_nop 0\n"
    "s_cbranch_vccz .Lqw_pd2_%=\n"
    "s_add_u32 s20, s20, 1\n"
    "s_cmp_lt_u32 s20, 0x40000\n"
    "s_cbranch_scc1 .Lqw_sp3_%=\n"
    "v_mov_b32 v8, 50\n"
    "s_mov_b32 s24, 33728\n"
    "buffer_store_dword v8, off, s[0:3], s24 sc1\n"
    "s_waitcnt vmcnt(0)\n"
    "s_branch .Lqw_pd2_%=\n"
    ".Lqw_sp3_%=:\n"
    "s_sleep 4\n"
    "s_branch .Lqw_po1_%=\n"
    ".Lqw_pd2_%=:\n"
    "s_mov_b32 s26, -1\n"
    "s_mov_b32 s35, 0\n"
    "s_lshl_b32 s37, s12, 11\n"
    "s_mov_b32 s43, 2130706432\n"
    "s_mov_b32 s44, 0\n"
    "s_mov_b32 s41, 2130706432\n"
    "s_mov_b32 s42, 0\n"
    ".Lqw_top_%=:\n"
    "s_add_u32 s26, s26, 1\n"
    "s_sub_u32 s27, s26, 10\n"
    "s_ashr_i32 s28, s27, 1\n"
    "s_and_b32 s29, s27, 1\n"
    "s_add_u32 s30, s28, 10\n"
    "s_cmp_lt_i32 s27, 0\n"
    "s_cselect_b32 s29, 0, s29\n"
    "s_cselect_b32 s30, s26, s30\n"
    "s_cmp_eq_u32 s29, 1\n"
    "s_cselect_b32 s30, s28, s30\n"
    "s_cselect_b32 s27, s15, 64\n"
    "s_lshl_b32 s31, s29, 13\n"
    "s_add_u32 s40, s30, 1\n"
    "s_lshl_b32 s32, s30, 19\n"
    "s_lshl_b32 s25, s30, 11\n"
    "s_xor_b32 s25, s25, s31\n"
    "s_and_b32 s25, s25, 0x2000\n"
    "s_add_u32 s32, s32, s25\n"
    "s_add_u32 s32, s32, s13\n"
    "s_lshl_b32 s33, s30, 20\n"
    "s_lshl_b32 s25, s30, 12\n"
    "s_xor_b32 s25, s25, s31\n"
    "s_and_b32 s25, s25, 0x2000\n"
    "s_add_u32 s33, s33, s25\n"
    "s_add_u32 s33, s33, s14\n"
    "s_cmp_lt_u32 s30, s27\n"
    "s_cselect_b32 s36, 1, 0\n"
    "s_cselect_b32 s32, s32, 2130706432\n"
    "s_cselect_b32 s39, s31, 2130706432\n"
    "s_and_b32 s27, s36, s29\n"
    "s_cbranch_scc1 .Lqw_wx_%=\n"
    ".Lqw_wxb_%=:\n"
    "s_cmp_eq_u32 s35, 0\n"
    "s_cbranch_scc1 .Lqw_skip_%=\n"
    "ds_read_b128 v[24:27], v65 offset:0\n"
    "ds_read_b128 v[28:31], v65 offset:1024\n"
    "ds_read_b128 v[32:35], v65 offset:2048\n"
    "ds_read_b128 v[36:39], v65 offset:3072\n"
    "ds_read_b128 v[40:43], v65 offset:4096\n"
    "ds_read_b128 v[44:47], v65 offset:5120\n"
    "s_waitcnt lgkmcnt(4)\n"
    "v_mfma_f32_16x16x32_bf16 v[0:3], a[0:3], v[24:27], 0\n"
    "s_add_u32 m0, s37, 0\n"
    "v_mfma_f32_16x16x32_bf16 v[4:7], a[0:3], v[28:31], 0\n"
    "v_mfma_f32_16x16x32_bf16 v[8:11], a[80:83], v[24:27], 0\n"
    "v_mfma_f32_16x16x32_bf16 v[12:15], a[80:83], v[28:31], 0\n"
    "v_mfma_f32_16x16x32_bf16 v[16:19], a[160:163], v[24:27], 0\n"
    "ds_read_b128 v[24:27], v65 offset:6144\n"
    "v_mfma_f32_16x16x32_bf16 v[20:23], a[160:163], v[28:31], 0\n"
    "ds_read_b128 v[28:31], v65 offset:7168\n"
    "s_waitcnt lgkmcnt(4)\n"
    "v_mfma_f32_16x16x32_bf16 v[0:3], a[4:7], v[32:35], v[0:3]\n"
    "v_mfma_f32_16x16x32_bf16 v[4:7], a[4:7], v[36:39], v[4:7]\n"
    "v_mfma_f32_16x16x32_bf16 v[8:11], a[84:87], v[32:35], v[8:11]\n"
    "v_mfma_f32_16x16x32_bf16 v[12:15], a[84:87], v[36:39], v[12:15]\n"
    "v_mfma_f32_16x16x32_bf16 v[16:19], a[164:167], v[32:35], v[16:19]\n"
    "ds_read_b128 v[32:35], v65 offset:8192\n"
    "v_mfma_f32_16x16x32_bf16 v[20:23], a[164:167], v[36:39], v[20:23]\n"
    "ds_read_b128 v[36:39], v65 offset:9216\n"
    "s_waitcnt lgkmcnt(4)\n"
    "v_mfma_f32_16x16x32_bf16 v[0:3], a[8:11], v[40:43], v[0:3]\n"
    "buffer_load_dwordx4 v64, s[0:3], s32 offen sc1 lds\n"
    "v_mfma_f32_16x16x32_bf16 v[4:7], a[8:11], v[44:47], v[4:7]\n"
    "v_mfma_f32_16x16x32_bf16 v[8:11], a[88:91], v[40:43], v[8:11]\n"
    "v_mfma_f32_16x16x32_bf16 v[12:15], a[88:91], v[44:47], v[12:15]\n"
    "v_mfma_f32_16x16x32_bf16 v[16:19], a[168:171], v[40:43], v[16:19]\n"
    "ds_read_b128 v[40:43], v65 offset:10240\n"
    "buffer_load_dwordx4 v64, s[0:3], s32 offen offset:1024 sc1 lds\n"
    "v_mfma_f32_16x16x32_bf16 v[20:23], a[168:171], v[44:47], v[20:23]\n"
    "ds_read_b128 v[44:47], v65 offset:11264\n"
    "s_waitcnt lgkmcnt(4)\n"
    "v_mfma_f32_16x16x32_bf16 v[0:3], a[12:15], v[24:27], v[0:3]\n"
    "s_add_u32 m0, s37, 8192\n"
    "s_add_u32 s32, s32, 0x4000\n"
    "v_mfma_f32_16x16x32_bf16 v[4:7], a[12:15], v[28:31], v[4:7]\n"
    "v_mfma_f32_16x16x32_bf16 v[8:11], a[92:95], v[24:27], v[8:11]\n"
    "buffer_load_dwordx4 v64, s[0:3], s32 offen sc1 lds\n"
    "v_mfma_f32_16x16x32_bf16 v[12:15], a[92:95], v[28:31], v[12:15]\n"
    "v_mfma_f32_16x16x32_bf16 v[16:19], a[172:175], v[24:27], v[16:19]\n"
    "ds_read_b128 v[24:27], v65 offset:12288\n"
    "v_mfma_f32_16x16x32_bf16 v[20:23], a[172:175], v[28:31], v[20:23]\n"
    "ds_read_b128 v[28:31], v65 offset:13312\n"
    "s_waitcnt lgkmcnt(4)\n"
    "v_mfma_f32_16x16x32_bf16 v[0:3], a[16:19], v[32:35], v[0:3]\n"
    "buffer_load_dwordx4 v64, s[0:3], s32 offen offset:1024 sc1 lds\n"
    "s_add_u32 m0, s37, 16384\n"
    "v_mfma_f32_16x16x32_bf16 v[4:7], a[16:19], v[36:39], v[4:7]\n"
    "s_add_u32 s32, s32, 0x4000\n"
    "v_mfma_f32_16x16x32_bf16 v[8:11], a[96:99], v[32:35], v[8:11]\n"
    "v_mfma_f32_16x16x32_bf16 v[12:15], a[96:99], v[36:39], v[12:15]\n"
    "v_mfma_f32_16x16x32_bf16 v[16:19], a[176:179], v[32:35], v[16:19]\n"
    "ds_read_b128 v[32:35], v65 offset:14336\n"
    "buffer_load_dwordx4 v64, s[0:3], s32 offen sc1 lds\n"
    "v_mfma_f32_16x16x32_bf16 v[20:23], a[176:179], v[36:39], v[20:23]\n"
    "ds_read_b128 v[36:39], v65 offset:15360\n"
    "s_waitcnt lgkmcnt(4)\n"
    "v_mfma_f32_16x16x32_bf16 v[0:3], a[20:23], v[40:43], v[0:3]\n"
    "v_mfma_f32_16x16x32_bf16 v[4:7], a[20:23], v[44:47], v[4:7]\n"
    "v_mfma_f32_16x16x32_bf16 v[8:11], a[100:103], v[40:43], v[8:11]\n"
    "buffer_load_dwordx4 v64, s[0:3], s32 offen offset:1024 sc1 lds\n"
    "s_add_u32 m0, s37, 24576\n"
    "v_mfma_f32_16x16x32_bf16 v[12:15], a[100:103], v[44:47], v[12:15]\n"
    "s_add_u32 s32, s32, 0x4000\n"
    "v_mfma_f32_16x16x32_bf16 v[16:19], a[180:183], v[40:43], v[16:19]\n"
    "ds_read_b128 v[40:43], v65 offset:16384\n"
    "v_mfma_f32_16x16x32_bf16 v[20:23], a[180:183], v[44:47], v[20:23]\n"
    "ds_read_b128 v[44:47], v65 offset:17408\n"
    "s_waitcnt lgkmcnt(4)\n"
    "v_mfma_f32_16x16x32_bf16 v[0:3], a[24:27], v[24:27], v[0:3]\n"
    "s_waitcnt vmcnt(6)\n"
    "v_mov_b32 v61, s44\n"
    "v_mfma_f32_16x16x32_bf16 v[4:7], a[24:27], v[28:31], v[4:7]\n"
    "buffer_store_dword v61, v69, s[0:3], s43 offen sc1\n"
    "v_mfma_f32_16x16x32_bf16 v[8:11], a[104:107], v[24:27], v[8:11]\n"
    "v_mfma_f32_16x16x32_bf16 v[12:15], a[104:107], v[28:31], v[12:15]\n"
    "v_mfma_f32_16x16x32_bf16 v[16:19], a[184:187], v[24:27], v[16:19]\n"
    "ds_read_b128 v[24:27], v65 offset:18432\n"
    "v_mfma_f32_16x16x32_bf16 v[20:23], a[184:187], v[28:31], v[20:23]\n"
    "ds_read_b128 v[28:31], v65 offset:19456\n"
    "s_waitcnt lgkmcnt(4)\n"
    "v_mfma_f32_16x16x32_bf16 v[0:3], a[28:31], v[32:35], v[0:3]\n"
    "v_mfma_f32_16x16x32_bf16 v[4:7], a[28:31], v[36:39], v[4:7]\n"
    "v_mfma_f32_16x16x32_bf16 v[8:11], a[108:111], v[32:35], v[8:11]\n"
    "v_mfma_f32_16x16x32_bf16 v[12:15], a[108:111], v[36:39], v[12:15]\n"
    "v_mfma_f32_16x16x32_bf16 v[16:19], a[188:191], v[32:35], v[16:19]\n"
    "ds_read_b128 v[32:35], v65 offset:20480\n"
    "buffer_load_dwordx4 v64, s[0:3], s32 offen sc1 lds\n"
    "v_mfma_f32_16x16x32_bf16 v[20:23], a[188:191], v[36:39], v[20:23]\n"
    "ds_read_b128 v[36:39], v65 offset:21504\n"
    "s_waitcnt lgkmcnt(4)\n"
    "v_mfma_f32_16x16x32_bf16 v[0:3], a[32:35], v[40:43], v[0:3]\n"
    "v_mfma_f32_16x16x32_bf16 v[4:7], a[32:35], v[44:47], v[4:7]\n"
    "v_mfma_f32_16x16x32_bf16 v[8:11], a[112:115], v[40:43], v[8:11]\n"
    "buffer_load_dwordx4 v64, s[0:3], s32 offen offset:1024 sc1 lds\n"
    "s_add_u32 m0, s37, 32768\n"
    "v_mfma_f32_16x16x32_bf16 v[12:15], a[112:115], v[44:47], v[12:15]\n"
    "s_add_u32 s32, s32, 0x4000\n"
    "v_mfma_f32_16x16x32_bf16 v[16:19], a[192:195], v[40:43], v[16:19]\n"
    "ds_read_b128 v[40:43], v65 offset:22528\n"
    "v_mfma_f32_16x16x32_bf16 v[20:23], a[192:195], v[44:47], v[20:23]\n"
    "ds_read_b128 v[44:47], v65 offset:23552\n"
    "s_waitcnt lgkmcnt(4)\n"
    "v_mfma_f32_16x16x32_bf16 v[0:3], a[36:39], v[24:27], v[0:3]\n"
    "buffer_load_dwordx4 v64, s[0:3], s32 offen sc1 lds\n"
    "v_mfma_f32_16x16x32_bf16 v[4:7], a[36:39], v[28:31], v[4:7]\n"
    "v_mfma_f32_16x16x32_bf16 v[8:11], a[116:119], v[24:27], v[8:11]\n"
    "v_mfma_f32_16x16x32_bf16 v[12:15], a[116:119], v[28:31], v[12:15]\n"
    "v_mfma_f32_16x16x32_bf16 v[16:19], a[196:199], v[24:27], v[16:19]\n"
    "ds_read_b128 v[24:27], v65 offset:24576\n"
    "buffer_load_dwordx4 v64, s[0:3], s32 offen offset:1024 sc1 lds\n"
    "v_mfma_f32_16x16x32_bf16 v[20:23], a[196:199], v[28:31], v[20:23]\n"
    "ds_read_b128 v[28:31], v65 offset:25600\n"
    "s_waitcnt lgkmcnt(4)\n"
    "v_mfma_f32_16x16x32_bf16 v[0:3], a[40:43], v[32:35], v[0:3]\n"
    "v_mfma_f32_16x16x32_bf16 v[4:7], a[40:43], v[36:39], v[4:7]\n"
    "v_mfma_f32_16x16x32_bf16 v[8:11], a[120:123], v[32:35], v[8:11]\n"
    "v_mfma_f32_16x16x32_bf16 v[12:15], a[120:123], v[36:39], v[12:15]\n"
    "v_mfma_f32_16x16x32_bf16 v[16:19], a[200:203], v[32:35], v[16:19]\n"
    "ds_read_b128 v[32:35], v65 offset:26624\n"
    "v_mfma_f32_16x16x32_bf16 v[20:23], a[200:203], v[36:39], v[20:23]\n"
    "ds_read_b128 v[36:39], v65 offset:27648\n"
    "s_waitcnt lgkmcnt(4)\n"
    "v_mfma_f32_16x16x32_bf16 v[0:3], a[44:47], v[40:43], v[0:3]\n"
    "v_mfma_f32_16x16x32_bf16 v[4:7], a[44:47], v[44:47], v[4:7]\n"
    "v_mfma_f32_16x16x32_bf16 v[8:11], a[124:127], v[40:43], v[8:11]\n"
    "v_mfma_f32_16x16x32_bf16 v[12:15], a[124:127], v[44:47], v[12:15]\n"
    "v_mfma_f32_16x16x32_bf16 v[16:19], a[204:207], v[40:43], v[16:19]\n"
    "ds_read_b128 v[40:43], v65 offset:28672\n"
    "v_mfma_f32_16x16x32_bf16 v[20:23], a[204:207], v[44:47], v[20:23]\n"
    "ds_read_b128 v[44:47], v65 offset:29696\n"
    "s_waitcnt lgkmcnt(4)\n"
    "v_mfma_f32_16x16x32_bf16 v[0:3], a[48:51], v[24:27], v[0:3]\n"
    "v_mfma_f32_16x16x32_bf16 v[4:7], a[48:51], v[28:31], v[4:7]\n"
    "v_mfma_f32_16x16x32_bf16 v[8:11], a[128:131], v[24:27], v[8:11]\n"
    "v_mfma_f32_16x16x32_bf16 v[12:15], a[128:131], v[28:31], v[12:15]\n"
    "v_mfma_f32_16x16x32_bf16 v[16:19], a[208:211], v[24:27], v[16:19]\n"
    "ds_read_b128 v[24:27], v65 offset:30720\n"
    "v_mfma_f32_16x16x32_bf16 v[20:23], a[208:211], v[28:31], v[20:23]\n"
    "ds_read_b128 v[28:31], v65 offset:31744\n"
    "s_waitcnt lgkmcnt(4)\n"
    "v_mfma_f32_16x16x32_bf16 v[0:3], a[52:55], v[32:35], v[0:3]\n"
    "v_mfma_f32_16x16x32_bf16 v[4:7], a[52:55], v[36:39], v[4:7]\n"
    "v_mfma_f32_16x16x32_bf16 v[8:11], a[132:135], v[32:35], v[8:11]\n"
    "v_mfma_f32_16x16x32_bf16 v[12:15], a[132:135], v[36:39], v[12:15]\n"
    "v_mfma_f32_16x16x32_bf16 v[16:19], a[212:215], v[32:35], v[16:19]\n"
    "ds_read_b128 v[32:35], v65 offset:32768\n"
    "v_mfma_f32_16x16x32_bf16 v[20:23], a[212:215], v[36:39], v[20:23]\n"
    "ds_read_b128 v[36:39], v65 offset:33792\n"
    "s_waitcnt lgkmcnt(4)\n"
    "v_mfma_f32_16x16x32_bf16 v[0:3], a[56:59], v[40:43], v[0:3]\n"
    "v_mfma_f32_16x16x32_bf16 v[4:7], a[56:59], v[44:47], v[4:7]\n"
    "v_mfma_f32_16x16x32_bf16 v[8:11], a[136:139], v[40:43], v[8:11]\n"
    "v_mfma_f32_16x16x32_bf16 v[12:15], a[136:139], v[44:47], v[12:15]\n"
    "v_mfma_f32_16x16x32_bf16 v[16:19], a[216:219], v[40:43], v[16:19]\n"
    "ds_read_b128 v[40:43], v65 offset:34816\n"
    "v_mfma_f32_16x16x32_bf16 v[20:23], a[216:219], v[44:47], v[20:23]\n"
    "ds_read_b128 v[44:47], v65 offset:35840\n"
    "s_waitcnt lgkmcnt(4)\n"
    "v_mfma_f32_16x16x32_bf16 v[0:3], a[60:63], v[24:27], v[0:3]\n"
    "v_mfma_f32_16x16x32_bf16 v[4:7], a[60:63], v[28:31], v[4:7]\n"
    "v_mfma_f32_16x16x32_bf16 v[8:11], a[140:143], v[24:27], v[8:11]\n"
    "v_mfma_f32_16x16x32_bf16 v[12:15], a[140:143], v[28:31], v[12:15]\n"
    "v_mfma_f32_16x16x32_bf16 v[16:19], a[220:223], v[24:27], v[16:19]\n"
    "ds_read_b128 v[24:27], v65 offset:36864\n"
    "v_mfma_f32_16x16x32_bf16 v[20:23], a[220:223], v[28:31], v[20:23]\n"
    "ds_read_b128 v[28:31], v65 offset:37888\n"
    "s_waitcnt lgkmcnt(4)\n"
    "v_mfma_f32_16x16x32_bf16 v[0:3], a[64:67], v[32:35], v[0:3]\n"
    "v_mfma_f32_16x16x32_bf16 v[4:7], a[64:67], v[36:39], v[4:7]\n"
    "v_mfma_f32_16x16x32_bf16 v[8:11], a[144:147], v[32:35], v[8:11]\n"
    "v_mfma_f32_16x16x32_bf16 v[12:15], a[144:147], v[36:39], v[12:15]\n"
    "v_mfma_f32_16x16x32_bf16 v[16:19], a[224:227], v[32:35], v[16:19]\n"
    "ds_read_b128 v[32:35], v65 offset:38912\n"
    "v_mfma_f32_16x16x32_bf16 v[20:23], a[224:227], v[36:39], v[20:23]\n"
    "ds_read_b128 v[36:39], v65 offset:39936\n"
    "s_waitcnt lgkmcnt(4)\n"
    "v_mfma_f32_16x16x32_bf16 v[0:3], a[68:71], v[40:43], v[0:3]\n"
    "v_mfma_f32_16x16x32_bf16 v[4:7], a[68:71], v[44:47], v[4:7]\n"
    "v_mfma_f32_16x16x32_bf16 v[8:11], a[148:151], v[40:43], v[8:11]\n"
    "v_mfma_f32_16x16x32_bf16 v[12:15], a[148:151], v[44:47], v[12:15]\n"
    "v_mfma_f32_16x16x32_bf16 v[16:19], a[228:231], v[40:43], v[16:19]\n"
    "v_mfma_f32_16x16x32_bf16 v[20:23], a[228:231], v[44:47], v[20:23]\n"
    "s_waitcnt lgkmcnt(2)\n"
    "v_mfma_f32_16x16x32_bf16 v[0:3], a[72:75], v[24:27], v[0:3]\n"
    "v_mfma_f32_16x16x32_bf16 v[4:7], a[72:75], v[28:31], v[4:7]\n"
    "v_mfma_f32_16x16x32_bf16 v[8:11], a[152:155], v[24:27], v[8:11]\n"
    "v_mfma_f32_16x16x32_bf16 v[12:15], a[152:155], v[28:31], v[12:15]\n"
    "v_mfma_f32_16x16x32_bf16 v[16:19], a[232:235], v[24:27], v[16:19]\n"
    "v_mfma_f32_16x16x32_bf16 v[20:23], a[232:235], v[28:31], v[20:23]\n"
    "s_waitcnt lgkmcnt(0)\n"
    "v_mfma_f32_16x16x32_bf16 v[0:3], a[76:79], v[32:35], v[0:3]\n"
    "v_mfma_f32_16x16x32_bf16 v[4:7], a[76:79], v[36:39], v[4:7]\n"
    "v_mfma_f32_16x16x32_bf16 v[8:11], a[156:159], v[32:35], v[8:11]\n"
    "v_mfma_f32_16x16x32_bf16 v[12:15], a[156:159], v[36:39], v[12:15]\n"
    "v_mfma_f32_16x16x32_bf16 v[16:19], a[236:239], v[32:35], v[16:19]\n"
    "v_mfma_f32_16x16x32_bf16 v[20:23], a[236:239], v[36:39], v[20:23]\n"
    "s_nop 2\n"
    "v_cvt_pk_bf16_f32 v48, v0, v1\n"
    "v_cvt_pk_bf16_f32 v49, v2, v3\n"
    "v_cvt_pk_bf16_f32 v50, v8, v9\n"
    "v_cvt_pk_bf16_f32 v51, v10, v11\n"
    "buffer_store_dwordx4 v[48:51], v66, s[0:3], s34 offen offset:0 sc1\n"
    "v_cvt_pk_bf16_f32 v52, v4, v5\n"
    "v_cvt_pk_bf16_f32 v53, v6, v7\n"
    "v_cvt_pk_bf16_f32 v54, v12, v13\n"
    "v_cvt_pk_bf16_f32 v55, v14, v15\n"
    "buffer_store_dwordx4 v[52:55], v66, s[0:3], s34 offen offset:1024 sc1\n"
    "v_cvt_pk_bf16_f32 v56, v16, v17\n"
    "v_cvt_pk_bf16_f32 v57, v18, v19\n"
    "v_cvt_pk_bf16_f32 v58, v20, v21\n"
    "v_cvt_pk_bf16_f32 v59, v22, v23\n"
    "buffer_store_dwordx4 v[56:59], v67, s[0:3], s34 offen sc1\n"
    ".Lqw_join_%=:\n"
    "s_mov_b32 s43, s41\n"
    "s_mov_b32 s44, s42\n"
    "s_mov_b32 s41, s39\n"
    "s_mov_b32 s42, s40\n"
    "s_mov_b32 s34, s33\n"
    "s_mov_b32 s35, s36\n"
    "s_xor_b32 s37, s37, 40960\n"
    "v_xor_b32 v65, 40960, v65\n"
    "s_waitcnt vmcnt(3) lgkmcnt(0)\n"
    "s_barrier\n"
    "s_cmp_lt_u32 s26, 138\n"
    "s_cbranch_scc1 .Lqw_top_%=\n"
    "s_waitcnt vmcnt(0)\n"
    "v_mov_b32 v61, s44\n"
    "buffer_store_dword v61, v69, s[0:3], s43 offen sc1\n"
    "s_waitcnt vmcnt(0) lgkmcnt(0)\n"
    "s_endpgm\n"
    ".Lqw_wx_%=:\n"
    ".Lqw_po4_%=:\n"
    "buffer_load_dword v60, v68, s[0:3], s17 offen sc1\n"
    "s_waitcnt vmcnt(0)\n"
    "v_mov_b32 v61, s44\n"
    "buffer_store_dword v61, v69, s[0:3], s43 offen sc1\n"
    "s_mov_b32 s43, 2130706432\n"
    "v_cmp_gt_i32 vcc, s40, v60\n"
    "s_nop 0\n"
    "s_cbranch_vccz .Lqw_pd5_%=\n"
    "s_add_u32 s20, s20, 1\n"
    "s_cmp_lt_u32 s20, 0x40000\n"
    "s_cbranch_scc1 .Lqw_sp6_%=\n"
    "v_mov_b32 v60, 51\n"
    "s_mov_b32 s24, 33728\n"
    "buffer_store_dword v60, off, s[0:3], s24 sc1\n"
    "s_waitcnt vmcnt(0)\n"
    "s_branch .Lqw_pd5_%=\n"
    ".Lqw_sp6_%=:\n"
    "s_sleep 4\n"
    "s_branch .Lqw_po4_%=\n"
    ".Lqw_pd5_%=:\n"
    "s_branch .Lqw_wxb_%=\n"
    ".Lqw_skip_%=:\n"
    "s_add_u32 m0, s37, 0\n"
    "s_nop 0\n"
    "buffer_load_dwordx4 v64, s[0:3], s32 offen sc1 lds\n"
    "buffer_load_dwordx4 v64, s[0:3], s32 offen offset:1024 sc1 lds\n"
    "s_add_u32 m0, s37, 8192\n"
    "s_add_u32 s32, s32, 0x4000\n"
    "buffer_load_dwordx4 v64, s[0:3], s32 offen sc1 lds\n"
    "buffer_load_dwordx4 v64, s[0:3], s32 offen offset:1024 sc1 lds\n"
    "s_add_u32 m0, s37, 16384\n"
    "s_add_u32 s32, s32, 0x4000\n"
    "buffer_load_dwordx4 v64, s[0:3], s32 offen sc1 lds\n"
    "buffer_load_dwordx4 v64, s[0:3], s32 offen offset:1024 sc1 lds\n"
    "s_add_u32 m0, s37, 24576\n"
    "s_add_u32 s32, s32, 0x4000\n"
    "buffer_load_dwordx4 v64, s[0:3], s32 offen sc1 lds\n"
    "buffer_load_dwordx4 v64, s[0:3], s32 offen offset:1024 sc1 lds\n"
    "s_add_u32 m0, s37, 32768\n"
    "s_add_u32 s32, s32, 0x4000\n"
    "buffer_load_dwordx4 v64, s[0:3], s32 offen sc1 lds\n"
    "buffer_load_dwordx4 v64, s[0:3], s32 offen offset:1024 sc1 lds\n"
    "s_waitcnt vmcnt(0)\n"
    "v_mov_b32 v61, s44\n"
    "buffer_store_dword v61, v69, s[0:3], s43 offen sc1\n"
    "s_branch .Lqw_join_%=\n"
    :: "{s[0:3]}"(s0_), "{s[4:7]}"(s4_), "{s[8:11]}"(s8_), "{s[12:15]}"(s12_), "{s[16:19]}"(s16_), "{v[0:3]}"(v0_), "{v[64:67]}"(v64_), "{v[68:71]}"(v68_) : "memory", "v255", "a255", "s95");
    __builtin_unreachable();
}
DEV void b1_role(const MegaArgs& A, const int L, const int i, const int rep) {
    ROLE_HEAD;
    const int qd = 4 * w + lq, sw = (l16 & 6) << 3, hw = w < 3 ? w : 2, hd = 3 * i + w;
    const bool live = w < 3 && hd < 32;
    const unsigned om = live ? RC_O - RC_X1 - w * 2048 + (hd >> 3) * 16384 + ((hd >> 1) & 3) * 2048 + (lq & 1) * 1024 + (2 * (hd & 1) + (lq >> 1)) * 256 + l16 * 16 : OOB;
    const unsigned ox = live && l < 16 ? RC_O - RC_X1 - w * 2048 + 65536 + hd * 256 + l16 * 16 : OOB;
    u64 wm[4];
#pragma unroll
    for (int e = 0; e < 4; ++e) { const int d = l16 + 16 - 16 * (e >> 1) - 4 * lq - 2 * (e & 1) - (l16 & 1); wm[e] = __ballot(d >= 0 && d <= 14); }
    const u32x4 s0_ = {RFL(rx[0]), RFL(rx[1]), RFL(rx[2]), RFL(rx[3])};
    const u32x4 s4_ = {RFL(lo(A.wqkv + 60 * i + 16 * w)), RFL(hi(A.wqkv + 60 * i + 16 * w)), RFL((640 * 1920 - 60 * i - 16 * w) * 2), RFL(0x20000u)};
    const u32x4 s8_ = {RFL((unsigned)wm[0]), RFL((unsigned)(wm[0] >> 32)), RFL((unsigned)wm[1]), RFL((unsigned)(wm[1] >> 32))};
    const u32x4 s12_ = {RFL((unsigned)wm[2]), RFL((unsigned)(wm[2] >> 32)), RFL((unsigned)wm[3]), RFL((unsigned)(wm[3] >> 32))};
    const u32x4 s16_ = {RFL(w == 3 ? 0xf000f000u : 0u), RFL(w == 3 ? 0xf000f000u : 0u), RFL(w), RFL(L << 13)};
    const u32x4 s20_ = {RFL(REC0 + RC_X1 + w * 2048), RFL(xl(Z_X1 + rep * 4096, L)), RFL(DBG0 + ((L * 128 + 96 + i) * 4 + w) * 2048), RFL(0u)};
    const u32x4 v0_ = {(unsigned)((l >> 1) * 3840 + (l & 1) * 16 + 0), (unsigned)((l >> 1) * 3840 + (l & 1) * 16 + 256), (unsigned)((l >> 1) * 3840 + (l & 1) * 16 + 512), (unsigned)(w * 40960 + (8 * lq + (l16 >> 2)) * 32 + (l & 3) * 8)};
    const u32x4 v224_ = {(unsigned)(l * 16 + ((w + 0) & 3) * 2048), (unsigned)(l * 16 + ((w + 1) & 3) * 2048), (unsigned)(l * 16 + ((w + 2) & 3) * 2048), (unsigned)(l * 16 + ((w + 3) & 3) * 2048)};
    const u32x4 v228_ = {(unsigned)(l * 16), (unsigned)((l & 31) * 128), (unsigned)(l < 4 ? xl(Z_O + l * 2048 + i * 128 + w * 4, L) : OOB), (unsigned)(qd == 15 ? 86016 : 40960 + (qd / 5) * 2048 + l16 * 64 + (((qd % 5) * 8) ^ sw))};
    const u32x4 v232_ = {(unsigned)(qd == 15 ? 86016 : 49152 + (qd / 5) * 4096 + l16 * 64 + (((qd % 5) * 8) ^ sw)), (unsigned)(qd == 15 ? 86016 : 61440 + (qd / 5) * 4096 + l16 * 64 + (((qd % 5) * 8) ^ sw)), (unsigned)(40960 + hw * 2048 + l16 * 64 + ((lq * 16) ^ sw)), (unsigned)(49152 + hw * 4096 + l16 * 64 + ((lq * 16) ^ sw))};
    const u32x4 v236_ = {(unsigned)(51200 + hw * 4096 + l16 * 64 + ((lq * 16) ^ sw)), (unsigned)(61440 + hw * 4096 + (l >> 2) * 64 + ((0 + (l & 3) * 8) ^ (((l >> 2) & 6) << 3))), (unsigned)(63488 + hw * 4096 + (l >> 2) * 64 + ((0 + (l & 3) * 8) ^ (((l >> 2) & 6) << 3))), (unsigned)(61440 + hw * 4096 + (l >> 2) * 64 + ((32 + (l & 3) * 8) ^ (((l >> 2) & 6) << 3)))};
    const u32x4 v240_ = {(unsigned)(63488 + hw * 4096 + (l >> 2) * 64 + ((32 + (l & 3) * 8) ^ (((l >> 2) & 6) << 3))), (unsigned)(73728 + w * 2048 + l * 16), (unsigned)(73728 + (l >> 5) * 1024 + ((l16 >> 2) * 16 + l16) * 16 + (l & 3) * 4), (unsigned)(81920 + w * 512 + l * 4)};
    const u32x4 v244_ = {(unsigned)(81920 + 3 * 512 + (l >> 5) * 256 + (48 + l16) * 4), (unsigned)(om), (unsigned)(ox), (unsigned)((l & 1) ? 0x01000c0cu : 0x0c0c0100u)};
    const u32x4 v248_ = {(unsigned)((l & 1) ? 0x03020c0cu : 0x0c0c0302u), (unsigned)(0xff800000u), (unsigned)(0u), (unsigned)(0u)};
    asm volatile(
    "s_mov_b32 s24, 0\n"
    "s_sleep 35\n"
    "s_sleep 35\n"
    "s_sleep 35\n"
    "s_sleep 35\n"
    "s_sleep 35\n"
    "s_sleep 35\n"
    "s_sleep 35\n"
    "s_sleep 35\n"
    "s_sleep 35\n"
    "s_sleep 35\n"
    "s_sleep 35\n"
    "s_sleep 35\n"
    "s_sleep 35\n"
    "s_sleep 35\n"
    ".Lb1_ho1_%=:\n"
    "s_mul_i32 s32, s18, 40960\n"
    "s_add_u32 s35, s18, 0\n"
    "s_and_b32 s35, s35, 3\n"
    "s_add_u32 s35, s35, 0\n"
    "s_mul_i32 s35, s35, 122880\n"
    "s_add_u32 m0, s32, 0\n"
    "s_nop 0\n"
    "buffer_load_dwordx4 v0, s[4:7], s35 offen offset:0 lds\n"
    "buffer_load_dwordx4 v1, s[4:7], s35 offen offset:1024 lds\n"
    "buffer_load_dwordx4 v2, s[4:7], s35 offen offset:2048 lds\n"
    "s_add_u32 s35, s18, 1\n"
    "s_and_b32 s35, s35, 3\n"
    "s_add_u32 s35, s35, 0\n"
    "s_mul_i32 s35, s35, 122880\n"
    "s_add_u32 m0, s32, 3072\n"
    "s_nop 0\n"
    "buffer_load_dwordx4 v0, s[4:7], s35 offen offset:0 lds\n"
    "buffer_load_dwordx4 v1, s[4:7], s35 offen offset:1024 lds\n"
    "buffer_load_dwordx4 v2, s[4:7], s35 offen offset:2048 lds\n"
    "s_add_u32 s35, s18, 2\n"
    "s_and_b32 s35, s35, 3\n"
    "s_add_u32 s35, s35, 0\n"
    "s_mul_i32 s35, s35, 122880\n"
    "s_add_u32 m0, s32, 6144\n"
    "s_nop 0\n"
    "buffer_load_dwordx4 v0, s[4:7], s35 offen offset:0 lds\n"
    "buffer_load_dwordx4 v1, s[4:7], s35 offen offset:1024 lds\n"
    "buffer_load_dwordx4 v2, s[4:7], s35 offen offset:2048 lds\n"
    "s_add_u32 s35, s18, 3\n"
    "s_and_b32 s35, s35, 3\n"
    "s_add_u32 s35, s35, 0\n"
    "s_mul_i32 s35, s35, 122880\n"
    "s_add_u32 m0, s32, 9216\n"
    "s_nop 0\n"
    "buffer_load_dwordx4 v0, s[4:7], s35 offen offset:0 lds\n"
    "buffer_load_dwordx4 v1, s[4:7], s35 offen offset:1024 lds\n"
    "buffer_load_dwordx4 v2, s[4:7], s35 offen offset:2048 lds\n"
    "s_add_u32 s35, s18, 0\n"
    "s_and_b32 s35, s35, 3\n"
    "s_add_u32 s35, s35, 4\n"
    "s_mul_i32 s35, s35, 122880\n"
    "s_add_u32 m0, s32, 12288\n"
    "s_nop 0\n"
    "buffer_load_dwordx4 v0, s[4:7], s35 offen offset:0 lds\n"
    "buffer_load_dwordx4 v1, s[4:7], s35 offen offset:1024 lds\n"
    "buffer_load_dwordx4 v2, s[4:7], s35 offen offset:2048 lds\n"
    "s_add_u32 s35, s18, 1\n"
    "s_and_b32 s35, s35, 3\n"
    "s_add_u32 s35, s35, 4\n"
    "s_mul_i32 s35, s35, 122880\n"
    "s_add_u32 m0, s32, 15360\n"
    "s_nop 0\n"
    "buffer_load_dwordx4 v0, s[4:7], s35 offen offset:0 lds\n"
    "buffer_load_dwordx4 v1, s[4:7], s35 offen offset:1024 lds\n"
    "buffer_load_dwordx4 v2, s[4:7], s35 offen offset:2048 lds\n"
    "s_add_u32 s35, s18, 2\n"
    "s_and_b32 s35, s35, 3\n"
    "s_add_u32 s35, s35, 4\n"
    "s_mul_i32 s35, s35, 122880\n"
    "s_add_u32 m0, s32, 18432\n"
    "s_nop 0\n"
    "buffer_load_dwordx4 v0, s[4:7], s35 offen offset:0 lds\n"
    "buffer_load_dwordx4 v1, s[4:7], s35 offen offset:1024 lds\n"
    "buffer_load_dwordx4 v2, s[4:7], s35 offen offset:2048 lds\n"
    "s_add_u32 s35, s18, 3\n"
    "s_and_b32 s35, s35, 3\n"
    "s_add_u32 s35, s35, 4\n"
    "s_mul_i32 s35, s35, 122880\n"
    "s_add_u32 m0, s32, 21504\n"
    "s_nop 0\n"
    "buffer_load_dwordx4 v0, s[4:7], s35 offen offset:0 lds\n"
    "buffer_load_dwordx4 v1, s[4:7], s35 offen offset:1024 lds\n"
    "buffer_load_dwordx4 v2, s[4:7], s35 offen offset:2048 lds\n"
    "s_add_u32 s35, s18, 0\n"
    "s_and_b32 s35, s35, 3\n"
    "s_add_u32 s35, s35, 8\n"
    "s_mul_i32 s35, s35, 122880\n"
    "s_add_u32 m0, s32, 24576\n"
    "s_nop 0\n"
    "buffer_load_dwordx4 v0, s[4:7], s35 offen offset:0 lds\n"
    "buffer_load_dwordx4 v1, s[4:7], s35 offen offset:1024 lds\n"
    "buffer_load_dwordx4 v2, s[4:7], s35 offen offset:2048 lds\n"
    "s_add_u32 s35, s18, 1\n"
    "s_and_b32 s35, s35, 3\n"
    "s_add_u32 s35, s35, 8\n"
    "s_mul_i32 s35, s35, 122880\n"
    "s_add_u32 m0, s32, 27648\n"
    "s_nop 0\n"
    "buffer_load_dwordx4 v0, s[4:7], s35 offen offset:0 lds\n"
    "buffer_load_dwordx4 v1, s[4:7], s35 offen offset:1024 lds\n"
    "buffer_load_dwordx4 v2, s[4:7], s35 offen offset:2048 lds\n"
    "s_add_u32 s35, s18, 2\n"
    "s_and_b32 s35, s35, 3\n"
    "s_add_u32 s35, s35, 8\n"
    "s_mul_i32 s35, s35, 122880\n"
    "s_add_u32 m0, s32, 30720\n"
    "s_nop 0\n"
    "buffer_load_dwordx4 v0, s[4:7], s35 offen offset:0 lds\n"
    "buffer_load_dwordx4 v1, s[4:7], s35 offen offset:1024 lds\n"
    "buffer_load_dwordx4 v2, s[4:7], s35 offen offset:2048 lds\n"
    "s_add_u32 s35, s18, 3\n"
    "s_and_b32 s35, s35, 3\n"
    "s_add_u32 s35, s35, 8\n"
    "s_mul_i32 s35, s35, 122880\n"
    "s_add_u32 m0, s32, 33792\n"
    "s_nop 0\n"
    "buffer_load_dwordx4 v0, s[4:7], s35 offen offset:0 lds\n"
    "buffer_load_dwordx4 v1, s[4:7], s35 offen offset:1024 lds\n"
    "buffer_load_dwordx4 v2, s[4:7], s35 offen offset:2048 lds\n"
    "s_add_u32 s35, s18, 0\n"
    "s_and_b32 s35, s35, 3\n"
    "s_add_u32 s35, s35, 12\n"
    "s_mul_i32 s35, s35, 122880\n"
    "s_add_u32 m0, s32, 36864\n"
    "s_nop 0\n"
    "buffer_load_dwordx4 v0, s[4:7], s35 offen offset:0 lds\n"
    "buffer_load_dwordx4 v1, s[4:7], s35 offen offset:1024 lds\n"
    "buffer_load_dwordx4 v2, s[4:7], s35 offen offset:2048 lds\n"
    "s_waitcnt vmcnt(36)\n"
    "ds_read_b64_tr_b16 a[0:1], v3 offset:0\n"
    "ds_read_b64_tr_b16 a[2:3], v3 offset:128\n"
    "ds_read_b64_tr_b16 a[80:81], v3 offset:1024\n"
    "ds_read_b64_tr_b16 a[82:83], v3 offset:1152\n"
    "ds_read_b64_tr_b16 a[160:161], v3 offset:2048\n"
    "ds_read_b64_tr_b16 a[162:163], v3 offset:2176\n"
    "s_waitcnt lgkmcnt(0)\n"
    "s_add_u32 s35, s18, 1\n"
    "s_and_b32 s35, s35, 3\n"
    "s_add_u32 s35, s35, 12\n"
    "s_mul_i32 s35, s35, 122880\n"
    "s_add_u32 m0, s32, 0\n"
    "s_nop 0\n"
    "buffer_load_dwordx4 v0, s[4:7], s35 offen offset:0 lds\n"
    "buffer_load_dwordx4 v1, s[4:7], s35 offen offset:1024 lds\n"
    "buffer_load_dwordx4 v2, s[4:7], s35 offen offset:2048 lds\n"
    "s_waitcnt vmcnt(36)\n"
    "ds_read_b64_tr_b16 a[4:5], v3 offset:3072\n"
    "ds_read_b64_tr_b16 a[6:7], v3 offset:3200\n"
    "ds_read_b64_tr_b16 a[84:85], v3 offset:4096\n"
    "ds_read_b64_tr_b16 a[86:87], v3 offset:4224\n"
    "ds_read_b64_tr_b16 a[164:165], v3 offset:5120\n"
    "ds_read_b64_tr_b16 a[166:167], v3 offset:5248\n"
    "s_waitcnt lgkmcnt(0)\n"
    "s_add_u32 s35, s18, 2\n"
    "s_and_b32 s35, s35, 3\n"
    "s_add_u32 s35, s35, 12\n"
    "s_mul_i32 s35, s35, 122880\n"
    "s_add_u32 m0, s32, 3072\n"
    "s_nop 0\n"
    "buffer_load_dwordx4 v0, s[4:7], s35 offen offset:0 lds\n"
    "buffer_load_dwordx4 v1, s[4:7], s35 offen offset:1024 lds\n"
    "buffer_load_dwordx4 v2, s[4:7], s35 offen offset:2048 lds\n"
    "s_waitcnt vmcnt(36)\n"
    "ds_read_b64_tr_b16 a[8:9], v3 offset:6144\n"
    "ds_read_b64_tr_b16 a[10:11], v3 offset:6272\n"
    "ds_read_b64_tr_b16 a[88:89], v3 offset:7168\n"
    "ds_read_b64_tr_b16 a[90:91], v3 offset:7296\n"
    "ds_read_b64_tr_b16 a[168:169], v3 offset:8192\n"
    "ds_read_b64_tr_b16 a[170:171], v3 offset:8320\n"
    "s_waitcnt lgkmcnt(0)\n"
    "s_add_u32 s35, s18, 3\n"
    "s_and_b32 s35, s35, 3\n"
    "s_add_u32 s35, s35, 12\n"
    "s_mul_i32 s35, s35, 122880\n"
    "s_add_u32 m0, s32, 6144\n"
    "s_nop 0\n"
    "buffer_load_dwordx4 v0, s[4:7], s35 offen offset:0 lds\n"
    "buffer_load_dwordx4 v1, s[4:7], s35 offen offset:1024 lds\n"
    "buffer_load_dwordx4 v2, s[4:7], s35 offen offset:2048 lds\n"
    "s_waitcnt vmcnt(36)\n"
    "ds_read_b64_tr_b16 a[12:13], v3 offset:9216\n"
    "ds_read_b64_tr_b16 a[14:15], v3 offset:9344\n"
    "ds_read_b64_tr_b16 a[92:93], v3 offset:10240\n"
    "ds_read_b64_tr_b16 a[94:95], v3 offset:10368\n"
    "ds_read_b64_tr_b16 a[172:173], v3 offset:11264\n"
    "ds_read_b64_tr_b16 a[174:175], v3 offset:11392\n"
    "s_waitcnt lgkmcnt(0)\n"
    "s_add_u32 s35, s18, 0\n"
    "s_and_b32 s35, s35, 3\n"
    "s_add_u32 s35, s35, 16\n"
    "s_mul_i32 s35, s35, 122880\n"
    "s_add_u32 m0, s32, 9216\n"
    "s_nop 0\n"
    "buffer_load_dwordx4 v0, s[4:7], s35 offen offset:0 lds\n"
    "buffer_load_dwordx4 v1, s[4:7], s35 offen offset:1024 lds\n"
    "buffer_load_dwordx4 v2, s[4:7], s35 offen offset:2048 lds\n"
    "s_waitcnt vmcnt(36)\n"
    "ds_read_b64_tr_b16 a[16:17], v3 offset:12288\n"
    "ds_read_b64_tr_b16 a[18:19], v3 offset:12416\n"
    "ds_read_b64_tr_b16 a[96:97], v3 offset:13312\n"
    "ds_read_b64_tr_b16 a[98:99], v3 offset:13440\n"
    "ds_read_b64_tr_b16 a[176:177], v3 offset:14336\n"
    "ds_read_b64_tr_b16 a[178:179], v3 offset:14464\n"
    "s_waitcnt lgkmcnt(0)\n"
    "s_add_u32 s35, s18, 1\n"
    "s_and_b32 s35, s35, 3\n"
    "s_add_u32 s35, s35, 16\n"
    "s_mul_i32 s35, s35, 122880\n"
    "s_add_u32 m0, s32, 12288\n"
    "s_nop 0\n"
    "buffer_load_dwordx4 v0, s[4:7], s35 offen offset:0 lds\n"
    "buffer_load_dwordx4 v1, s[4:7], s35 offen offset:1024 lds\n"
    "buffer_load_dwordx4 v2, s[4:7], s35 offen offset:2048 lds\n"
    "s_waitcnt vmcnt(36)\n"
    "ds_read_b64_tr_b16 a[20:21], v3 offset:15360\n"
    "ds_read_b64_tr_b16 a[22:23], v3 offset:15488\n"
    "ds_read_b64_tr_b16 a[100:101], v3 offset:16384\n"
    "ds_read_b64_tr_b16 a[102:103], v3 offset:16512\n"
    "ds_read_b64_tr_b16 a[180:181], v3 offset:17408\n"
    "ds_read_b64_tr_b16 a[182:183], v3 offset:17536\n"
    "s_waitcnt lgkmcnt(0)\n"
    "s_add_u32 s35, s18, 2\n"
    "s_and_b32 s35, s35, 3\n"
    "s_add_u32 s35, s35, 16\n"
    "s_mul_i32 s35, s35, 122880\n"
    "s_add_u32 m0, s32, 15360\n"
    "s_nop 0\n"
    "buffer_load_dwordx4 v0, s[4:7], s35 offen offset:0 lds\n"
    "buffer_load_dwordx4 v1, s[4:7], s35 offen offset:1024 lds\n"
    "buffer_load_dwordx4 v2, s[4:7], s35 offen offset:2048 lds\n"
    "s_waitcnt vmcnt(36)\n"
    "ds_read_b64_tr_b16 a[24:25], v3 offset:18432\n"
    "ds_read_b64_tr_b16 a[26:27], v3 offset:18560\n"
    "ds_read_b64_tr_b16 a[104:105], v3 offset:19456\n"
    "ds_read_b64_tr_b16 a[106:107], v3 offset:19584\n"
    "ds_read_b64_tr_b16 a[184:185], v3 offset:20480\n"
    "ds_read_b64_tr_b16 a[186:187], v3 offset:20608\n"
    "s_waitcnt lgkmcnt(0)\n"
    "s_add_u32 s35, s18, 3\n"
    "s_and_b32 s35, s35, 3\n"
    "s_add_u32 s35, s35, 16\n"
    "s_mul_i32 s35, s35, 122880\n"
    "s_add_u32 m0, s32, 18432\n"
    "s_nop 0\n"
    "buffer_load_dwordx4 v0, s[4:7], s35 offen offset:0 lds\n"
    "buffer_load_dwordx4 v1, s[4:7], s35 offen offset:1024 lds\n"
    "buffer_load_dwordx4 v2, s[4:7], s35 offen offset:2048 lds\n"
    "s_waitcnt vmcnt(36)\n"
    "ds_read_b64_tr_b16 a[28:29], v3 offset:21504\n"
    "ds_read_b64_tr_b16 a[30:31], v3 offset:21632\n"
    "ds_read_b64_tr_b16 a[108:109], v3 offset:22528\n"
    "ds_read_b64_tr_b16 a[110:111], v3 offset:22656\n"
    "ds_read_b64_tr_b16 a[188:189], v3 offset:23552\n"
    "ds_read_b64_tr_b16 a[190:191], v3 offset:23680\n"
    "s_waitcnt lgkmcnt(0)\n"
    "s_waitcnt vmcnt(33)\n"
    "ds_read_b64_tr_b16 a[32:33], v3 offset:24576\n"
    "ds_read_b64_tr_b16 a[34:35], v3 offset:24704\n"
    "ds_read_b64_tr_b16 a[112:113], v3 offset:25600\n"
    "ds_read_b64_tr_b16 a[114:115], v3 offset:25728\n"
    "ds_read_b64_tr_b16 a[192:193], v3 offset:26624\n"
    "ds_read_b64_tr_b16 a[194:195], v3 offset:26752\n"
    "s_waitcnt lgkmcnt(0)\n"
    "s_waitcnt vmcnt(30)\n"
    "ds_read_b64_tr_b16 a[36:37], v3 offset:27648\n"
    "ds_read_b64_tr_b16 a[38:39], v3 offset:27776\n"
    "ds_read_b64_tr_b16 a[116:117], v3 offset:28672\n"
    "ds_read_b64_tr_b16 a[118:119], v3 offset:28800\n"
    "ds_read_b64_tr_b16 a[196:197], v3 offset:29696\n"
    "ds_read_b64_tr_b16 a[198:199], v3 offset:29824\n"
    "s_waitcnt lgkmcnt(0)\n"
    "s_waitcnt vmcnt(27)\n"
    "ds_read_b64_tr_b16 a[40:41], v3 offset:30720\n"
    "ds_read_b64_tr_b16 a[42:43], v3 offset:30848\n"
    "ds_read_b64_tr_b16 a[120:121], v3 offset:31744\n"
    "ds_read_b64_tr_b16 a[122:123], v3 offset:31872\n"
    "ds_read_b64_tr_b16 a[200:201], v3 offset:32768\n"
    "ds_read_b64_tr_b16 a[202:203], v3 offset:32896\n"
    "s_waitcnt lgkmcnt(0)\n"
    "s_waitcnt vmcnt(24)\n"
    "ds_read_b64_tr_b16 a[44:45], v3 offset:33792\n"
    "ds_read_b64_tr_b16 a[46:47], v3 offset:33920\n"
    "ds_read_b64_tr_b16 a[124:125], v3 offset:34816\n"
    "ds_read_b64_tr_b16 a[126:127], v3 offset:34944\n"
    "ds_read_b64_tr_b16 a[204:205], v3 offset:35840\n"
    "ds_read_b64_tr_b16 a[206:207], v3 offset:35968\n"
    "s_waitcnt lgkmcnt(0)\n"
    "s_waitcnt vmcnt(21)\n"
    "ds_read_b64_tr_b16 a[48:49], v3 offset:36864\n"
    "ds_read_b64_tr_b16 a[50:51], v3 offset:36992\n"
    "ds_read_b64_tr_b16 a[128:129], v3 offset:37888\n"
    "ds_read_b64_tr_b16 a[130:131], v3 offset:38016\n"
    "ds_read_b64_tr_b16 a[208:209], v3 offset:38912\n"
    "ds_read_b64_tr_b16 a[210:211], v3 offset:39040\n"
    "s_waitcnt lgkmcnt(0)\n"
    "s_waitcnt vmcnt(18)\n"
    "ds_read_b64_tr_b16 a[52:53], v3 offset:0\n"
    "ds_read_b64_tr_b16 a[54:55], v3 offset:128\n"
    "ds_read_b64_tr_b16 a[132:133], v3 offset:1024\n"
    "ds_read_b64_tr_b16 a[134:135], v3 offset:1152\n"
    "ds_read_b64_tr_b16 a[212:213], v3 offset:2048\n"
    "ds_read_b64_tr_b16 a[214:215], v3 offset:2176\n"
    "s_waitcnt lgkmcnt(0)\n"
    "s_waitcnt vmcnt(15)\n"
    "ds_read_b64_tr_b16 a[56:57], v3 offset:3072\n"
    "ds_read_b64_tr_b16 a[58:59], v3 offset:3200\n"
    "ds_read_b64_tr_b16 a[136:137], v3 offset:4096\n"
    "ds_read_b64_tr_b16 a[138:139], v3 offset:4224\n"
    "ds_read_b64_tr_b16 a[216:217], v3 offset:5120\n"
    "ds_read_b64_tr_b16 a[218:219], v3 offset:5248\n"
    "s_waitcnt lgkmcnt(0)\n"
    "s_waitcnt vmcnt(12)\n"
    "ds_read_b64_tr_b16 a[60:61], v3 offset:6144\n"
    "ds_read_b64_tr_b16 a[62:63], v3 offset:6272\n"
    "ds_read_b64_tr_b16 a[140:141], v3 offset:7168\n"
    "ds_read_b64_tr_b16 a[142:143], v3 offset:7296\n"
    "ds_read_b64_tr_b16 a[220:221], v3 offset:8192\n"
    "ds_read_b64_tr_b16 a[222:223], v3 offset:8320\n"
    "s_waitcnt lgkmcnt(0)\n"
    "s_waitcnt vmcnt(9)\n"
    "ds_read_b64_tr_b16 a[64:65], v3 offset:9216\n"
    "ds_read_b64_tr_b16 a[66:67], v3 offset:9344\n"
    "ds_read_b64_tr_b16 a[144:145], v3 offset:10240\n"
    "ds_read_b64_tr_b16 a[146:147], v3 offset:10368\n"
    "ds_read_b64_tr_b16 a[224:225], v3 offset:11264\n"
    "ds_read_b64_tr_b16 a[226:227], v3 offset:11392\n"
    "s_waitcnt lgkmcnt(0)\n"
    "s_waitcnt vmcnt(6)\n"
    "ds_read_b64_tr_b16 a[68:69], v3 offset:12288\n"
    "ds_read_b64_tr_b16 a[70:71], v3 offset:12416\n"
    "ds_read_b64_tr_b16 a[148:149], v3 offset:13312\n"
    "ds_read_b64_tr_b16 a[150:151], v3 offset:13440\n"
    "ds_read_b64_tr_b16 a[228:229], v3 offset:14336\n"
    "ds_read_b64_tr_b16 a[230:231], v3 offset:14464\n"
    "s_waitcnt lgkmcnt(0)\n"
    "s_waitcnt vmcnt(3)\n"
    "ds_read_b64_tr_b16 a[72:73], v3 offset:15360\n"
    "ds_read_b64_tr_b16 a[74:75], v3 offset:15488\n"
    "ds_read_b64_tr_b16 a[152:153], v3 offset:16384\n"
    "ds_read_b64_tr_b16 a[154:155], v3 offset:16512\n"
    "ds_read_b64_tr_b16 a[232:233], v3 offset:17408\n"
    "ds_read_b64_tr_b16 a[234:235], v3 offset:17536\n"
    "s_waitcnt lgkmcnt(0)\n"
    "s_waitcnt vmcnt(0)\n"
    "ds_read_b64_tr_b16 a[76:77], v3 offset:18432\n"
    "ds_read_b64_tr_b16 a[78:79], v3 offset:18560\n"
    "ds_read_b64_tr_b16 a[156:157], v3 offset:19456\n"
    "ds_read_b64_tr_b16 a[158:159], v3 offset:19584\n"
    "ds_read_b64_tr_b16 a[236:237], v3 offset:20480\n"
    "ds_read_b64_tr_b16 a[238:239], v3 offset:20608\n"
    "s_waitcnt lgkmcnt(0)\n"
    "s_waitcnt lgkmcnt(0)\n"
    "s_barrier\n"
    "v_mov_b32 v32, 0x3f803f80\n"
    "v_mov_b32 v36, 0\n"
    "v_mov_b32 v33, 0x3f803f80\n"
    "v_mov_b32 v37, 0\n"
    "v_mov_b32 v34, 0x3f803f80\n"
    "v_mov_b32 v38, 0\n"
    "v_mov_b32 v35, 0x3f803f80\n"
    "v_mov_b32 v39, 0\n"
    "s_nop 0\n"
    "v_mfma_f32_16x16x32_bf16 v[96:99], a[0:3], v[32:35], 0\n"
    "v_mfma_f32_16x16x32_bf16 v[100:103], a[80:83], v[32:35], 0\n"
    "v_mfma_f32_16x16x32_bf16 v[104:107], a[160:163], v[32:35], 0\n"
    "v_mfma_f32_16x16x32_bf16 v[96:99], a[4:7], v[32:35], v[96:99]\n"
    "v_mfma_f32_16x16x32_bf16 v[100:103], a[84:87], v[32:35], v[100:103]\n"
    "v_mfma_f32_16x16x32_bf16 v[104:107], a[164:167], v[32:35], v[104:107]\n"
    "v_mfma_f32_16x16x32_bf16 v[96:99], a[8:11], v[32:35], v[96:99]\n"
    "v_mfma_f32_16x16x32_bf16 v[100:103], a[88:91], v[32:35], v[100:103]\n"
    "v_mfma_f32_16x16x32_bf16 v[104:107], a[168:171], v[32:35], v[104:107]\n"
    "v_mfma_f32_16x16x32_bf16 v[96:99], a[12:15], v[32:35], v[96:99]\n"
    "v_mfma_f32_16x16x32_bf16 v[100:103], a[92:95], v[32:35], v[100:103]\n"
    "v_mfma_f32_16x16x32_bf16 v[104:107], a[172:175], v[32:35], v[104:107]\n"
    "v_mfma_f32_16x16x32_bf16 v[96:99], a[16:19], v[32:35], v[96:99]\n"
    "v_mfma_f32_16x16x32_bf16 v[100:103], a[96:99], v[32:35], v[100:103]\n"
    "v_mfma_f32_16x16x32_bf16 v[104:107], a[176:179], v[32:35], v[104:107]\n"
    "v_mfma_f32_16x16x32_bf16 v[96:99], a[20:23], v[32:35], v[96:99]\n"
    "v_mfma_f32_16x16x32_bf16 v[100:103], a[100:103], v[32:35], v[100:103]\n"
    "v_mfma_f32_16x16x32_bf16 v[104:107], a[180:183], v[32:35], v[104:107]\n"
    "v_mfma_f32_16x16x32_bf16 v[96:99], a[24:27], v[32:35], v[96:99]\n"
    "v_mfma_f32_16x16x32_bf16 v[100:103], a[104:107], v[32:35], v[100:103]\n"
    "v_mfma_f32_16x16x32_bf16 v[104:107], a[184:187], v[32:35], v[104:107]\n"
    "v_mfma_f32_16x16x32_bf16 v[96:99], a[28:31], v[32:35], v[96:99]\n"
    "v_mfma_f32_16x16x32_bf16 v[100:103], a[108:111], v[32:35], v[100:103]\n"
    "v_mfma_f32_16x16x32_bf16 v[104:107], a[188:191], v[32:35], v[104:107]\n"
    "v_mfma_f32_16x16x32_bf16 v[96:99], a[32:35], v[32:35], v[96:99]\n"
    "v_mfma_f32_16x16x32_bf16 v[100:103], a[112:115], v[32:35], v[100:103]\n"
    "v_mfma_f32_16x16x32_bf16 v[104:107], a[192:195], v[32:35], v[104:107]\n"
    "v_mfma_f32_16x16x32_bf16 v[96:99], a[36:39], v[32:35], v[96:99]\n"
    "v_mfma_f32_16x16x32_bf16 v[100:103], a[116:119], v[32:35], v[100:103]\n"
    "v_mfma_f32_16x16x32_bf16 v[104:107], a[196:199], v[32:35], v[104:107]\n"
    "v_mfma_f32_16x16x32_bf16 v[96:99], a[40:43], v[32:35], v[96:99]\n"
    "v_mfma_f32_16x16x32_bf16 v[100:103], a[120:123], v[32:35], v[100:103]\n"
    "v_mfma_f32_16x16x32_bf16 v[104:107], a[200:203], v[32:35], v[104:107]\n"
    "v_mfma_f32_16x16x32_bf16 v[96:99], a[44:47], v[32:35], v[96:99]\n"
    "v_mfma_f32_16x16x32_bf16 v[100:103], a[124:127], v[32:35], v[100:103]\n"
    "v_mfma_f32_16x16x32_bf16 v[104:107], a[204:207], v[32:35], v[104:107]\n"
    "v_mfma_f32_16x16x32_bf16 v[96:99], a[48:51], v[32:35], v[96:99]\n"
    "v_mfma_f32_16x16x32_bf16 v[100:103], a[128:131], v[32:35], v[100:103]\n"
    "v_mfma_f32_16x16x32_bf16 v[104:107], a[208:211], v[32:35], v[104:107]\n"
    "v_mfma_f32_16x16x32_bf16 v[96:99], a[52:55], v[32:35], v[96:99]\n"
    "v_mfma_f32_16x16x32_bf16 v[100:103], a[132:135], v[32:35], v[100:103]\n"
    "v_mfma_f32_16x16x32_bf16 v[104:107], a[212:215], v[32:35], v[104:107]\n"
    "v_mfma_f32_16x16x32_bf16 v[96:99], a[56:59], v[32:35], v[96:99]\n"
    "v_mfma_f32_16x16x32_bf16 v[100:103], a[136:139], v[32:35], v[100:103]\n"
    "v_mfma_f32_16x16x32_bf16 v[104:107], a[216:219], v[32:35], v[104:107]\n"
    "v_mfma_f32_16x16x32_bf16 v[96:99], a[60:63], v[32:35], v[96:99]\n"
    "v_mfma_f32_16x16x32_bf16 v[100:103], a[140:143], v[32:35], v[100:103]\n"
    "v_mfma_f32_16x16x32_bf16 v[104:107], a[220:223], v[32:35], v[104:107]\n"
    "v_mfma_f32_16x16x32_bf16 v[96:99], a[64:67], v[32:35], v[96:99]\n"
    "v_mfma_f32_16x16x32_bf16 v[100:103], a[144:147], v[32:35], v[100:103]\n"
    "v_mfma_f32_16x16x32_bf16 v[104:107], a[224:227], v[32:35], v[104:107]\n"
    "v_mfma_f32_16x16x32_bf16 v[96:99], a[68:71], v[32:35], v[96:99]\n"
    "v_mfma_f32_16x16x32_bf16 v[100:103], a[148:151], v[32:35], v[100:103]\n"
    "v_mfma_f32_16x16x32_bf16 v[104:107], a[228:231], v[32:35], v[104:107]\n"
    "v_mfma_f32_16x16x32_bf16 v[96:99], a[72:75], v[32:35], v[96:99]\n"
    "v_mfma_f32_16x16x32_bf16 v[100:103], a[152:155], v[32:35], v[100:103]\n"
    "v_mfma_f32_16x16x32_bf16 v[104:107], a[232:235], v[32:35], v[104:107]\n"
    "v_mfma_f32_16x16x32_bf16 v[96:99], a[76:79], v[32:35], v[96:99]\n"
    "v_mfma_f32_16x16x32_bf16 v[100:103], a[156:159], v[32:35], v[100:103]\n"
    "v_mfma_f32_16x16x32_bf16 v[104:107], a[236:239], v[32:35], v[104:107]\n"
    "s_lshl_b32 s35, s18, 13\n"
    "v_add_u32 v8, s35, v228\n"
    "ds_write_b128 v8, v[36:39] offset:40960\n"
    "ds_write_b128 v8, v[36:39] offset:41984\n"
    "ds_write_b128 v8, v[36:39] offset:43008\n"
    "ds_write_b128 v8, v[36:39] offset:44032\n"
    "ds_write_b128 v8, v[36:39] offset:45056\n"
    "ds_write_b128 v8, v[36:39] offset:46080\n"
    "ds_write_b128 v8, v[36:39] offset:47104\n"
    "ds_write_b128 v8, v[36:39] offset:48128\n"
    "s_nop 7\n"
    "s_nop 7\n"
    "s_mov_b64 exec, s[16:17]\n"
    "v_accvgpr_write_b32 a0, v32\n"
    "v_accvgpr_write_b32 a1, v32\n"
    "v_accvgpr_write_b32 a2, v32\n"
    "v_accvgpr_write_b32 a3, v32\n"
    "v_accvgpr_write_b32 a4, v32\n"
    "v_accvgpr_write_b32 a5, v32\n"
    "v_accvgpr_write_b32 a6, v32\n"
    "v_accvgpr_write_b32 a7, v32\n"
    "v_accvgpr_write_b32 a8, v32\n"
    "v_accvgpr_write_b32 a9, v32\n"
    "v_accvgpr_write_b32 a10, v32\n"
    "v_accvgpr_write_b32 a11, v32\n"
    "v_accvgpr_write_b32 a12, v32\n"
    "v_accvgpr_write_b32 a13, v32\n"
    "v_accvgpr_write_b32 a14, v32\n"
    "v_accvgpr_write_b32 a15, v32\n"
    "v_accvgpr_write_b32 a16, v32\n"
    "v_accvgpr_write_b32 a17, v32\n"
    "v_accvgpr_write_b32 a18, v32\n"
    "v_accvgpr_write_b32 a19, v32\n"
    "v_accvgpr_write_b32 a20, v32\n"
    "v_accvgpr_write_b32 a21, v32\n"
    "v_accvgpr_write_b32 a22, v32\n"
    "v_accvgpr_write_b32 a23, v32\n"
    "v_accvgpr_write_b32 a24, v32\n"
    "v_accvgpr_write_b32 a25, v32\n"
    "v_accvgpr_write_b32 a26, v32\n"
    "v_accvgpr_write_b32 a27, v32\n"
    "v_accvgpr_write_b32 a28, v32\n"
    "v_accvgpr_write_b32 a29, v32\n"
    "v_accvgpr_write_b32 a30, v32\n"
    "v_accvgpr_write_b32 a31, v32\n"
    "v_accvgpr_write_b32 a32, v32\n"
    "v_accvgpr_write_b32 a33, v32\n"
    "v_accvgpr_write_b32 a34, v32\n"
    "v_accvgpr_write_b32 a35, v32\n"
    "v_accvgpr_write_b32 a36, v32\n"
    "v_accvgpr_write_b32 a37, v32\n"
    "v_accvgpr_write_b32 a38, v32\n"
    "v_accvgpr_write_b32 a39, v32\n"
    "v_accvgpr_write_b32 a40, v32\n"
    "v_accvgpr_write_b32 a41, v32\n"
    "v_accvgpr_write_b32 a42, v32\n"
    "v_accvgpr_write_b32 a43, v32\n"
    "v_accvgpr_write_b32 a44, v32\n"
    "v_accvgpr_write_b32 a45, v32\n"
    "v_accvgpr_write_b32 a46, v32\n"
    "v_accvgpr_write_b32 a47, v32\n"
    "v_accvgpr_write_b32 a48, v32\n"
    "v_accvgpr_write_b32 a49, v32\n"
    "v_accvgpr_write_b32 a50, v32\n"
    "v_accvgpr_write_b32 a51, v32\n"
    "v_accvgpr_write_b32 a52, v32\n"
    "v_accvgpr_write_b32 a53, v32\n"
    "v_accvgpr_write_b32 a54, v32\n"
    "v_accvgpr_write_b32 a55, v32\n"
    "v_accvgpr_write_b32 a56, v32\n"
    "v_accvgpr_write_b32 a57, v32\n"
    "v_accvgpr_write_b32 a58, v32\n"
    "v_accvgpr_write_b32 a59, v32\n"
    "v_accvgpr_write_b32 a60, v32\n"
    "v_accvgpr_write_b32 a61, v32\n"
    "v_accvgpr_write_b32 a62, v32\n"
    "v_accvgpr_write_b32 a63, v32\n"
    "v_accvgpr_write_b32 a64, v32\n"
    "v_accvgpr_write_b32 a65, v32\n"
    "v_accvgpr_write_b32 a66, v32\n"
    "v_accvgpr_write_b32 a67, v32\n"
    "v_accvgpr_write_b32 a68, v32\n"
    "v_accvgpr_write_b32 a69, v32\n"
    "v_accvgpr_write_b32 a70, v32\n"
    "v_accvgpr_write_b32 a71, v32\n"
    "v_accvgpr_write_b32 a72, v32\n"
    "v_accvgpr_write_b32 a73, v32\n"
    "v_accvgpr_write_b32 a74, v32\n"
    "v_accvgpr_write_b32 a75, v32\n"
    "v_accvgpr_write_b32 a76, v32\n"
    "v_accvgpr_write_b32 a77, v32\n"
    "v_accvgpr_write_b32 a78, v32\n"
    "v_accvgpr_write_b32 a79, v32\n"
    "s_mov_b64 exec, -1\n"
    "s_mov_b32 s30, -2\n"
    "s_mov_b32 s30, 0\n"
    "s_mov_b32 s31, 1\n"
    "s_lshl_b32 s34, s18, 11\n"
    "s_add_u32 s32, s20, s19\n"
    "s_mov_b32 s37, 0x3ea52b5c\n"
    "s_mov_b64 s[38:39], 0\n"
    "s_mov_b64 s[40:41], 0\n"
    "s_mov_b32 s36, 0\n"
    "s_mov_b32 s42, 0xaaaaaaaa\n"
    "s_mov_b32 s43, 0xaaaaaaaa\n"
    "s_waitcnt lgkmcnt(0)\n"
    ".Lb1_top_%=:\n"
    "s_cmp_lg_u32 s18, 0\n"
    "s_cbranch_scc1 .Lb1_pd3_%=\n"
    ".Lb1_rt4_%=:\n"
    "s_memrealtime s[26:27]\n"
    "s_waitcnt lgkmcnt(0)\n"
    "s_sub_u32 s28, s36, s26\n"
    "s_cmp_lt_u32 s28, 600\n"
    "s_cbranch_scc1 .Lb1_rt4_%=\n"
    ".Lb1_po2_%=:\n"
    "buffer_load_dwordx4 v[108:111], v229, s[0:3], s21 offen sc1\n"
    "s_memrealtime s[26:27]\n"
    "s_waitcnt vmcnt(0) lgkmcnt(0)\n"
    "s_add_u32 s36, s26, 274\n"
    "v_min3_i32 v108, v108, v109, v110\n"
    "v_min_i32 v108, v108, v111\n"
    "v_cmp_gt_i32 vcc, s31, v108\n"
    "s_nop 0\n"
    "s_cbranch_vccz .Lb1_pd3_%=\n"
    "s_add_u32 s24, s24, 1\n"
    "s_cmp_lt_u32 s24, 0x40000\n"
    "s_cbranch_scc1 .Lb1_sp5_%=\n"
    "v_mov_b32 v108, 60\n"
    "s_mov_b32 s28, 33728\n"
    "buffer_store_dword v108, off, s[0:3], s28 sc1\n"
    "s_waitcnt vmcnt(0)\n"
    "s_branch .Lb1_pd3_%=\n"
    ".Lb1_sp5_%=:\n"
    "s_sleep 2\n"
    "s_branch .Lb1_po2_%=\n"
    ".Lb1_pd3_%=:\n"
    "s_barrier\n"
    "s_mov_b32 s33, s32\n"
    "s_add_u32 m0, s34, 0\n"
    "s_nop 0\n"
    "buffer_load_dwordx4 v228, s[0:3], s32 offen sc1 lds\n"
    "buffer_load_dwordx4 v228, s[0:3], s32 offen offset:1024 sc1 lds\n"
    "s_add_u32 m0, s34, 8192\n"
    "s_add_u32 s32, s32, 0x4000\n"
    "buffer_load_dwordx4 v228, s[0:3], s32 offen sc1 lds\n"
    "buffer_load_dwordx4 v228, s[0:3], s32 offen offset:1024 sc1 lds\n"
    "s_add_u32 m0, s34, 16384\n"
    "s_add_u32 s32, s32, 0x4000\n"
    "buffer_load_dwordx4 v228, s[0:3], s32 offen sc1 lds\n"
    "buffer_load_dwordx4 v228, s[0:3], s32 offen offset:1024 sc1 lds\n"
    "s_add_u32 m0, s34, 24576\n"
    "s_add_u32 s32, s32, 0x4000\n"
    "buffer_load_dwordx4 v228, s[0:3], s32 offen sc1 lds\n"
    "buffer_load_dwordx4 v228, s[0:3], s32 offen offset:1024 sc1 lds\n"
    "s_add_u32 m0, s34, 32768\n"
    "s_add_u32 s32, s32, 0x4000\n"
    "buffer_load_dwordx4 v228, s[0:3], s32 offen sc1 lds\n"
    "buffer_load_dwordx4 v228, s[0:3], s32 offen offset:1024 sc1 lds\n"
    "s_waitcnt vmcnt(8)\n"
    "s_barrier\n"
    "ds_read_b128 v[32:35], v224 offset:0\n"
    "ds_read_b128 v[36:39], v224 offset:1024\n"
    "ds_read_b128 v[40:43], v225 offset:0\n"
    "ds_read_b128 v[44:47], v225 offset:1024\n"
    "s_waitcnt lgkmcnt(2)\n"
    "v_mfma_f32_16x16x32_bf16 v[24:27], v[32:35], v[32:35], 0\n"
    "ds_read_b128 v[48:51], v226 offset:0\n"
    "v_mfma_f32_16x16x32_bf16 v[28:31], v[36:39], v[36:39], 0\n"
    "ds_read_b128 v[52:55], v226 offset:1024\n"
    "v_mfma_f32_16x16x32_bf16 v[0:3], a[0:3], v[32:35], 0\n"
    "v_mfma_f32_16x16x32_bf16 v[4:7], a[0:3], v[36:39], 0\n"
    "v_mfma_f32_16x16x32_bf16 v[8:11], a[80:83], v[32:35], 0\n"
    "s_add_u32 s35, s30, 1\n"
    "s_lshl_b32 s32, s35, 19\n"
    "s_lshl_b32 s29, s35, 11\n"
    "v_mfma_f32_16x16x32_bf16 v[12:15], a[80:83], v[36:39], 0\n"
    "s_xor_b32 s29, s29, s19\n"
    "s_and_b32 s29, s29, 0x2000\n"
    "s_add_u32 s32, s32, s29\n"
    "v_mfma_f32_16x16x32_bf16 v[16:19], a[160:163], v[32:35], 0\n"
    "s_add_u32 s32, s32, s20\n"
    "v_xor_b32 v232, 0x800, v232\n"
    "v_mfma_f32_16x16x32_bf16 v[20:23], a[160:163], v[36:39], 0\n"
    "s_waitcnt lgkmcnt(2)\n"
    "v_xor_b32 v233, 0x800, v233\n"
    "v_mfma_f32_16x16x32_bf16 v[0:3], a[4:7], v[40:43], v[0:3]\n"
    "ds_read_b128 v[56:59], v227 offset:0\n"
    "v_xor_b32 v235, 0x800, v235\n"
    "v_mfma_f32_16x16x32_bf16 v[4:7], a[4:7], v[44:47], v[4:7]\n"
    "ds_read_b128 v[60:63], v227 offset:1024\n"
    "v_xor_b32 v236, 0x800, v236\n"
    "v_mfma_f32_16x16x32_bf16 v[8:11], a[84:87], v[40:43], v[8:11]\n"
    "v_xor_b32 v237, 0x800, v237\n"
    "v_xor_b32 v238, 0x800, v238\n"
    "v_mfma_f32_16x16x32_bf16 v[12:15], a[84:87], v[44:47], v[12:15]\n"
    "v_xor_b32 v239, 0x800, v239\n"
    "v_xor_b32 v240, 0x800, v240\n"
    "v_mfma_f32_16x16x32_bf16 v[16:19], a[164:167], v[40:43], v[16:19]\n"
    "v_mov_b32 v116, s31\n"
    "v_mfma_f32_16x16x32_bf16 v[20:23], a[164:167], v[44:47], v[20:23]\n"
    "s_waitcnt lgkmcnt(2)\n"
    "v_mfma_f32_16x16x32_bf16 v[0:3], a[8:11], v[48:51], v[0:3]\n"
    "s_waitcnt vmcnt(6)\n"
    "s_barrier\n"
    "v_mfma_f32_16x16x32_bf16 v[4:7], a[8:11], v[52:55], v[4:7]\n"
    "ds_read_b128 v[64:67], v224 offset:8192\n"
    "v_mfma_f32_16x16x32_bf16 v[8:11], a[88:91], v[48:51], v[8:11]\n"
    "ds_read_b128 v[68:71], v224 offset:9216\n"
    "v_mfma_f32_16x16x32_bf16 v[12:15], a[88:91], v[52:55], v[12:15]\n"
    "v_mfma_f32_16x16x32_bf16 v[16:19], a[168:171], v[48:51], v[16:19]\n"
    "v_mfma_f32_16x16x32_bf16 v[20:23], a[168:171], v[52:55], v[20:23]\n"
    "s_waitcnt lgkmcnt(2)\n"
    "v_mfma_f32_16x16x32_bf16 v[0:3], a[12:15], v[56:59], v[0:3]\n"
    "ds_read_b128 v[72:75], v225 offset:8192\n"
    "v_mfma_f32_16x16x32_bf16 v[4:7], a[12:15], v[60:63], v[4:7]\n"
    "ds_read_b128 v[76:79], v225 offset:9216\n"
    "v_mfma_f32_16x16x32_bf16 v[8:11], a[92:95], v[56:59], v[8:11]\n"
    "v_mfma_f32_16x16x32_bf16 v[12:15], a[92:95], v[60:63], v[12:15]\n"
    "v_mfma_f32_16x16x32_bf16 v[16:19], a[172:175], v[56:59], v[16:19]\n"
    "v_mfma_f32_16x16x32_bf16 v[20:23], a[172:175], v[60:63], v[20:23]\n"
    "s_waitcnt lgkmcnt(2)\n"
    "v_mfma_f32_16x16x32_bf16 v[24:27], v[64:67], v[64:67], v[24:27]\n"
    "ds_read_b128 v[80:83], v226 offset:8192\n"
    "v_mfma_f32_16x16x32_bf16 v[28:31], v[68:71], v[68:71], v[28:31]\n"
    "ds_read_b128 v[84:87], v226 offset:9216\n"
    "v_mfma_f32_16x16x32_bf16 v[0:3], a[16:19], v[64:67], v[0:3]\n"
    "v_mfma_f32_16x16x32_bf16 v[4:7], a[16:19], v[68:71], v[4:7]\n"
    "v_mfma_f32_16x16x32_bf16 v[8:11], a[96:99], v[64:67], v[8:11]\n"
    "v_mfma_f32_16x16x32_bf16 v[12:15], a[96:99], v[68:71], v[12:15]\n"
    "v_mfma_f32_16x16x32_bf16 v[16:19], a[176:179], v[64:67], v[16:19]\n"
    "v_mfma_f32_16x16x32_bf16 v[20:23], a[176:179], v[68:71], v[20:23]\n"
    "s_waitcnt lgkmcnt(2)\n"
    "v_mfma_f32_16x16x32_bf16 v[0:3], a[20:23], v[72:75], v[0:3]\n"
    "ds_read_b128 v[88:91], v227 offset:8192\n"
    "v_mfma_f32_16x16x32_bf16 v[4:7], a[20:23], v[76:79], v[4:7]\n"
    "ds_read_b128 v[92:95], v227 offset:9216\n"
    "v_mfma_f32_16x16x32_bf16 v[8:11], a[100:103], v[72:75], v[8:11]\n"
    "v_mfma_f32_16x16x32_bf16 v[12:15], a[100:103], v[76:79], v[12:15]\n"
    "v_mfma_f32_16x16x32_bf16 v[16:19], a[180:183], v[72:75], v[16:19]\n"
    "v_mfma_f32_16x16x32_bf16 v[20:23], a[180:183], v[76:79], v[20:23]\n"
    "s_waitcnt lgkmcnt(2)\n"
    "v_mfma_f32_16x16x32_bf16 v[0:3], a[24:27], v[80:83], v[0:3]\n"
    "s_waitcnt vmcnt(4)\n"
    "s_barrier\n"
    "v_mfma_f32_16x16x32_bf16 v[4:7], a[24:27], v[84:87], v[4:7]\n"
    "ds_read_b128 v[32:35], v224 offset:16384\n"
    "v_mfma_f32_16x16x32_bf16 v[8:11], a[104:107], v[80:83], v[8:11]\n"
    "ds_read_b128 v[36:39], v224 offset:17408\n"
    "v_mfma_f32_16x16x32_bf16 v[12:15], a[104:107], v[84:87], v[12:15]\n"
    "v_mfma_f32_16x16x32_bf16 v[16:19], a[184:187], v[80:83], v[16:19]\n"
    "v_mfma_f32_16x16x32_bf16 v[20:23], a[184:187], v[84:87], v[20:23]\n"
    "s_waitcnt lgkmcnt(2)\n"
    "v_mfma_f32_16x16x32_bf16 v[0:3], a[28:31], v[88:91], v[0:3]\n"
    "ds_read_b128 v[40:43], v225 offset:16384\n"
    "v_mfma_f32_16x16x32_bf16 v[4:7], a[28:31], v[92:95], v[4:7]\n"
    "ds_read_b128 v[44:47], v225 offset:17408\n"
    "v_mfma_f32_16x16x32_bf16 v[8:11], a[108:111], v[88:91], v[8:11]\n"
    "v_mfma_f32_16x16x32_bf16 v[12:15], a[108:111], v[92:95], v[12:15]\n"
    "v_mfma_f32_16x16x32_bf16 v[16:19], a[188:191], v[88:91], v[16:19]\n"
    "v_mfma_f32_16x16x32_bf16 v[20:23], a[188:191], v[92:95], v[20:23]\n"
    "s_waitcnt lgkmcnt(2)\n"
    "v_mfma_f32_16x16x32_bf16 v[24:27], v[32:35], v[32:35], v[24:27]\n"
    "ds_read_b128 v[48:51], v226 offset:16384\n"
    "v_mfma_f32_16x16x32_bf16 v[28:31], v[36:39], v[36:39], v[28:31]\n"
    "ds_read_b128 v[52:55], v226 offset:17408\n"
    "v_mfma_f32_16x16x32_bf16 v[0:3], a[32:35], v[32:35], v[0:3]\n"
    "v_mfma_f32_16x16x32_bf16 v[4:7], a[32:35], v[36:39], v[4:7]\n"
    "v_mfma_f32_16x16x32_bf16 v[8:11], a[112:115], v[32:35], v[8:11]\n"
    "v_mfma_f32_16x16x32_bf16 v[12:15], a[112:115], v[36:39], v[12:15]\n"
    "v_mfma_f32_16x16x32_bf16 v[16:19], a[192:195], v[32:35], v[16:19]\n"
    "v_mfma_f32_16x16x32_bf16 v[20:23], a[192:195], v[36:39], v[20:23]\n"
    "s_waitcnt lgkmcnt(2)\n"
    "v_mfma_f32_16x16x32_bf16 v[0:3], a[36:39], v[40:43], v[0:3]\n"
    "ds_read_b128 v[56:59], v227 offset:16384\n"
    "v_mfma_f32_16x16x32_bf16 v[4:7], a[36:39], v[44:47], v[4:7]\n"
    "ds_read_b128 v[60:63], v227 offset:17408\n"
    "v_mfma_f32_16x16x32_bf16 v[8:11], a[116:119], v[40:43], v[8:11]\n"
    "v_mfma_f32_16x16x32_bf16 v[12:15], a[116:119], v[44:47], v[12:15]\n"
    "v_mfma_f32_16x16x32_bf16 v[16:19], a[196:199], v[40:43], v[16:19]\n"
    "v_mfma_f32_16x16x32_bf16 v[20:23], a[196:199], v[44:47], v[20:23]\n"
    "s_waitcnt lgkmcnt(2)\n"
    "v_mfma_f32_16x16x32_bf16 v[0:3], a[40:43], v[48:51], v[0:3]\n"
    "s_waitcnt vmcnt(2)\n"
    "s_barrier\n"
    "v_mfma_f32_16x16x32_bf16 v[4:7], a[40:43], v[52:55], v[4:7]\n"
    "ds_read_b128 v[64:67], v224 offset:24576\n"
    "v_mfma_f32_16x16x32_bf16 v[8:11], a[120:123], v[48:51], v[8:11]\n"
    "ds_read_b128 v[68:71], v224 offset:25600\n"
    "v_mfma_f32_16x16x32_bf16 v[12:15], a[120:123], v[52:55], v[12:15]\n"
    "ds_read_b128 v[72:75], v225 offset:24576\n"
    "v_mfma_f32_16x16x32_bf16 v[16:19], a[200:203], v[48:51], v[16:19]\n"
    "ds_read_b128 v[76:79], v225 offset:25600\n"
    "v_mfma_f32_16x16x32_bf16 v[20:23], a[200:203], v[52:55], v[20:23]\n"
    "s_waitcnt lgkmcnt(4)\n"
    "ds_read_b128 v[80:83], v226 offset:24576\n"
    "v_mfma_f32_16x16x32_bf16 v[0:3], a[44:47], v[56:59], v[0:3]\n"
    "ds_read_b128 v[84:87], v226 offset:25600\n"
    "v_mfma_f32_16x16x32_bf16 v[4:7], a[44:47], v[60:63], v[4:7]\n"
    "ds_read_b128 v[88:91], v227 offset:24576\n"
    "v_mfma_f32_16x16x32_bf16 v[8:11], a[124:127], v[56:59], v[8:11]\n"
    "ds_read_b128 v[92:95], v227 offset:25600\n"
    "s_waitcnt vmcnt(0)\n"
    "v_mfma_f32_16x16x32_bf16 v[12:15], a[124:127], v[60:63], v[12:15]\n"
    "s_barrier\n"
    "ds_read_b128 v[32:35], v224 offset:32768\n"
    "v_mfma_f32_16x16x32_bf16 v[16:19], a[204:207], v[56:59], v[16:19]\n"
    "ds_read_b128 v[36:39], v224 offset:33792\n"
    "v_mfma_f32_16x16x32_bf16 v[20:23], a[204:207], v[60:63], v[20:23]\n"
    "s_waitcnt lgkmcnt(8)\n"
    "ds_read_b128 v[40:43], v225 offset:32768\n"
    "v_mfma_f32_16x16x32_bf16 v[24:27], v[64:67], v[64:67], v[24:27]\n"
    "ds_read_b128 v[44:47], v225 offset:33792\n"
    "v_mfma_f32_16x16x32_bf16 v[28:31], v[68:71], v[68:71], v[28:31]\n"
    "ds_read_b128 v[48:51], v226 offset:32768\n"
    "v_mfma_f32_16x16x32_bf16 v[0:3], a[48:51], v[64:67], v[0:3]\n"
    "ds_read_b128 v[52:55], v226 offset:33792\n"
    "v_mfma_f32_16x16x32_bf16 v[4:7], a[48:51], v[68:71], v[4:7]\n"
    "s_waitcnt lgkmcnt(10)\n"
    "ds_read_b128 v[56:59], v227 offset:32768\n"
    "v_mfma_f32_16x16x32_bf16 v[0:3], a[52:55], v[72:75], v[0:3]\n"
    "ds_read_b128 v[60:63], v227 offset:33792\n"
    "v_mfma_f32_16x16x32_bf16 v[4:7], a[52:55], v[76:79], v[4:7]\n"
    "s_waitcnt lgkmcnt(10)\n"
    "v_mfma_f32_16x16x32_bf16 v[0:3], a[56:59], v[80:83], v[0:3]\n"
    "v_mfma_f32_16x16x32_bf16 v[4:7], a[56:59], v[84:87], v[4:7]\n"
    "s_waitcnt lgkmcnt(8)\n"
    "v_mfma_f32_16x16x32_bf16 v[0:3], a[60:63], v[88:91], v[0:3]\n"
    "v_mfma_f32_16x16x32_bf16 v[4:7], a[60:63], v[92:95], v[4:7]\n"
    "s_waitcnt lgkmcnt(6)\n"
    "v_mfma_f32_16x16x32_bf16 v[24:27], v[32:35], v[32:35], v[24:27]\n"
    "v_mfma_f32_16x16x32_bf16 v[28:31], v[36:39], v[36:39], v[28:31]\n"
    "v_mfma_f32_16x16x32_bf16 v[0:3], a[64:67], v[32:35], v[0:3]\n"
    "v_mfma_f32_16x16x32_bf16 v[4:7], a[64:67], v[36:39], v[4:7]\n"
    "s_waitcnt lgkmcnt(4)\n"
    "v_mfma_f32_16x16x32_bf16 v[0:3], a[68:71], v[40:43], v[0:3]\n"
    "v_mfma_f32_16x16x32_bf16 v[4:7], a[68:71], v[44:47], v[4:7]\n"
    "s_waitcnt lgkmcnt(2)\n"
    "v_mfma_f32_16x16x32_bf16 v[0:3], a[72:75], v[48:51], v[0:3]\n"
    "v_mfma_f32_16x16x32_bf16 v[4:7], a[72:75], v[52:55], v[4:7]\n"
    "s_waitcnt lgkmcnt(0)\n"
    "v_mfma_f32_16x16x32_bf16 v[0:3], a[76:79], v[56:59], v[0:3]\n"
    "v_mfma_f32_16x16x32_bf16 v[4:7], a[76:79], v[60:63], v[4:7]\n"
    "ds_write_b128 v241, v[24:27]\n"
    "ds_write_b128 v241, v[28:31] offset:1024\n"
    "v_mfma_f32_16x16x32_bf16 v[8:11], a[128:131], v[64:67], v[8:11]\n"
    "v_mfma_f32_16x16x32_bf16 v[12:15], a[128:131], v[68:71], v[12:15]\n"
    "v_mfma_f32_16x16x32_bf16 v[8:11], a[132:135], v[72:75], v[8:11]\n"
    "v_mfma_f32_16x16x32_bf16 v[12:15], a[132:135], v[76:79], v[12:15]\n"
    "v_mfma_f32_16x16x32_bf16 v[8:11], a[136:139], v[80:83], v[8:11]\n"
    "v_mfma_f32_16x16x32_bf16 v[12:15], a[136:139], v[84:87], v[12:15]\n"
    "ds_write2st64_b32 v243, v0, v4 offset1:1\n"
    "s_waitcnt lgkmcnt(0)\n"
    "v_mfma_f32_16x16x32_bf16 v[8:11], a[140:143], v[88:91], v[8:11]\n"
    "s_barrier\n"
    "ds_read2st64_b32 v[24:25], v242 offset1:8\n"
    "v_mfma_f32_16x16x32_bf16 v[12:15], a[140:143], v[92:95], v[12:15]\n"
    "ds_read2st64_b32 v[26:27], v242 offset0:16 offset1:24\n"
    "ds_read_b32 v108, v244\n"
    "v_mfma_f32_16x16x32_bf16 v[8:11], a[144:147], v[32:35], v[8:11]\n"
    "v_mfma_f32_16x16x32_bf16 v[12:15], a[144:147], v[36:39], v[12:15]\n"
    "v_mfma_f32_16x16x32_bf16 v[8:11], a[148:151], v[40:43], v[8:11]\n"
    "v_mfma_f32_16x16x32_bf16 v[12:15], a[148:151], v[44:47], v[12:15]\n"
    "v_mfma_f32_16x16x32_bf16 v[8:11], a[152:155], v[48:51], v[8:11]\n"
    "s_waitcnt lgkmcnt(0)\n"
    "v_add_f32 v24, v24, v26\n"
    "v_mfma_f32_16x16x32_bf16 v[12:15], a[152:155], v[52:55], v[12:15]\n"
    "v_add_f32 v25, v25, v27\n"
    "v_add_f32 v112, v24, v25\n"
    "v_mfma_f32_16x16x32_bf16 v[8:11], a[156:159], v[56:59], v[8:11]\n"
    "v_mul_f32 v108, 0x3acccccd, v108\n"
    "v_mul_f32 v112, 0x3acccccd, v112\n"
    "v_mfma_f32_16x16x32_bf16 v[12:15], a[156:159], v[60:63], v[12:15]\n"
    "v_fma_f32 v112, -v108, v108, v112\n"
    "v_max_f32 v112, 0, v112\n"
    "v_mfma_f32_16x16x32_bf16 v[16:19], a[208:211], v[64:67], v[16:19]\n"
    "v_cvt_pk_bf16_f32 v108, v108, v112\n"
    "v_and_b32 v112, 0xffff0000, v108\n"
    "v_mfma_f32_16x16x32_bf16 v[20:23], a[208:211], v[68:71], v[20:23]\n"
    "v_lshlrev_b32 v108, 16, v108\n"
    "v_add_f32 v112, 0x3727c5ac, v112\n"
    "v_mfma_f32_16x16x32_bf16 v[16:19], a[212:215], v[72:75], v[16:19]\n"
    "v_mov_b32 v110, v108\n"
    "v_cvt_pk_bf16_f32 v112, v112, v112\n"
    "v_mfma_f32_16x16x32_bf16 v[20:23], a[212:215], v[76:79], v[20:23]\n"
    "v_lshlrev_b32 v112, 16, v112\n"
    "v_mfma_f32_16x16x32_bf16 v[16:19], a[216:219], v[80:83], v[16:19]\n"
    "v_sqrt_f32 v112, v112\n"
    "v_mfma_f32_16x16x32_bf16 v[20:23], a[216:219], v[84:87], v[20:23]\n"
    "v_permlane32_swap_b32 v108, v110\n"
    "v_mfma_f32_16x16x32_bf16 v[16:19], a[220:223], v[88:91], v[16:19]\n"
    "v_cvt_pk_bf16_f32 v112, v112, v112\n"
    "v_lshlrev_b32 v112, 16, v112\n"
    "v_mfma_f32_16x16x32_bf16 v[20:23], a[220:223], v[92:95], v[20:23]\n"
    "v_rcp_f32 v112, v112\n"
    "v_mfma_f32_16x16x32_bf16 v[16:19], a[224:227], v[32:35], v[16:19]\n"
    "v_mov_b32 v114, v112\n"
    "v_mfma_f32_16x16x32_bf16 v[20:23], a[224:227], v[36:39], v[20:23]\n"
    "v_mfma_f32_16x16x32_bf16 v[16:19], a[228:231], v[40:43], v[16:19]\n"
    "v_permlane32_swap_b32 v112, v114\n"
    "v_mfma_f32_16x16x32_bf16 v[20:23], a[228:231], v[44:47], v[20:23]\n"
    "v_fma_f32 v0, v96, -v108, v0\n"
    "v_fma_f32 v1, v97, -v108, v1\n"
    "v_mfma_f32_16x16x32_bf16 v[16:19], a[232:235], v[48:51], v[16:19]\n"
    "v_fma_f32 v2, v98, -v108, v2\n"
    "v_fma_f32 v3, v99, -v108, v3\n"
    "v_mfma_f32_16x16x32_bf16 v[20:23], a[232:235], v[52:55], v[20:23]\n"
    "v_fma_f32 v4, v96, -v110, v4\n"
    "v_fma_f32 v5, v97, -v110, v5\n"
    "v_mfma_f32_16x16x32_bf16 v[16:19], a[236:239], v[56:59], v[16:19]\n"
    "v_fma_f32 v6, v98, -v110, v6\n"
    "v_fma_f32 v7, v99, -v110, v7\n"
    "v_mfma_f32_16x16x32_bf16 v[20:23], a[236:239], v[60:63], v[20:23]\n"
    "v_mul_f32 v0, v0, v112\n"
    "v_mul_f32 v1, v1, v112\n"
    "v_mul_f32 v2, v2, v112\n"
    "v_mul_f32 v3, v3, v112\n"
    "v_mul_f32 v4, v4, v114\n"
    "v_mul_f32 v5, v5, v114\n"
    "v_mul_f32 v6, v6, v114\n"
    "v_mul_f32 v7, v7, v114\n"
    "v_cvt_pk_bf16_f32 v0, v0, v1\n"
    "v_cvt_pk_bf16_f32 v1, v2, v3\n"
    "ds_write_b64 v231, v[0:1] offset:0\n"
    "v_cvt_pk_bf16_f32 v4, v4, v5\n"
    "v_cvt_pk_bf16_f32 v5, v6, v7\n"
    "ds_write_b64 v231, v[4:5] offset:1024\n"
    "v_pk_fma_f32 v[8:9], v[100:101], v[108:109], v[8:9] op_sel_hi:[1,0,1] neg_lo:[0,1,0] neg_hi:[0,1,0]\n"
    "v_pk_fma_f32 v[10:11], v[102:103], v[108:109], v[10:11] op_sel_hi:[1,0,1] neg_lo:[0,1,0] neg_hi:[0,1,0]\n"
    "v_pk_fma_f32 v[12:13], v[100:101], v[110:111], v[12:13] op_sel_hi:[1,0,1] neg_lo:[0,1,0] neg_hi:[0,1,0]\n"
    "v_pk_fma_f32 v[14:15], v[102:103], v[110:111], v[14:15] op_sel_hi:[1,0,1] neg_lo:[0,1,0] neg_hi:[0,1,0]\n"
    "v_pk_mul_f32 v[8:9], v[8:9], v[112:113] op_sel_hi:[1,0]\n"
    "v_pk_mul_f32 v[10:11], v[10:11], v[112:113] op_sel_hi:[1,0]\n"
    "v_pk_mul_f32 v[12:13], v[12:13], v[114:115] op_sel_hi:[1,0]\n"
    "v_pk_mul_f32 v[14:15], v[14:15], v[114:115] op_sel_hi:[1,0]\n"
    "v_cvt_pk_bf16_f32 v8, v8, v9\n"
    "v_cvt_pk_bf16_f32 v9, v10, v11\n"
    "ds_write_b64 v232, v[8:9] offset:0\n"
    "v_cvt_pk_bf16_f32 v12, v12, v13\n"
    "v_cvt_pk_bf16_f32 v13, v14, v15\n"
    "ds_write_b64 v232, v[12:13] offset:1024\n"
    "v_pk_fma_f32 v[16:17], v[104:105], v[108:109], v[16:17] op_sel_hi:[1,0,1] neg_lo:[0,1,0] neg_hi:[0,1,0]\n"
    "v_pk_fma_f32 v[18:19], v[106:107], v[108:109], v[18:19] op_sel_hi:[1,0,1] neg_lo:[0,1,0] neg_hi:[0,1,0]\n"
    "v_pk_fma_f32 v[20:21], v[104:105], v[110:111], v[20:21] op_sel_hi:[1,0,1] neg_lo:[0,1,0] neg_hi:[0,1,0]\n"
    "v_pk_fma_f32 v[22:23], v[106:107], v[110:111], v[22:23] op_sel_hi:[1,0,1] neg_lo:[0,1,0] neg_hi:[0,1,0]\n"
    "v_pk_mul_f32 v[16:17], v[16:17], v[112:113] op_sel_hi:[1,0]\n"
    "v_pk_mul_f32 v[18:19], v[18:19], v[112:113] op_sel_hi:[1,0]\n"
    "v_pk_mul_f32 v[20:21], v[20:21], v[114:115] op_sel_hi:[1,0]\n"
    "v_pk_mul_f32 v[22:23], v[22:23], v[114:115] op_sel_hi:[1,0]\n"
    "v_cvt_pk_bf16_f32 v16, v16, v17\n"
    "v_cvt_pk_bf16_f32 v17, v18, v19\n"
    "ds_write_b64 v233, v[16:17] offset:0\n"
    "v_cvt_pk_bf16_f32 v20, v20, v21\n"
    "v_cvt_pk_bf16_f32 v21, v22, v23\n"
    "ds_write_b64 v233, v[20:21] offset:1024\n"
    "s_waitcnt lgkmcnt(0)\n"
    "s_barrier\n"
    "ds_read_b128 v[32:35], v236 offset:1024\n"
    "ds_read_b128 v[44:47], v234 offset:0\n"
    "ds_read_b128 v[36:39], v235 offset:0\n"
    "ds_read_b128 v[48:51], v234 offset:1024\n"
    "ds_read_b128 v[40:43], v235 offset:1024\n"
    "ds_read_b64_tr_b16 v[52:53], v238 offset:1024\n"
    "ds_read_b64_tr_b16 v[54:55], v237 offset:0\n"
    "ds_read_b64_tr_b16 v[56:57], v237 offset:1024\n"
    "ds_read_b64_tr_b16 v[58:59], v240 offset:1024\n"
    "ds_read_b64_tr_b16 v[60:61], v239 offset:0\n"
    "ds_read_b64_tr_b16 v[62:63], v239 offset:1024\n"
    "s_waitcnt lgkmcnt(9)\n"
    "v_mfma_f32_16x16x32_bf16 v[64:67], v[32:35], v[44:47], 0\n"
    "s_waitcnt lgkmcnt(8)\n"
    "v_mfma_f32_16x16x32_bf16 v[68:71], v[36:39], v[44:47], 0\n"
    "s_waitcnt lgkmcnt(7)\n"
    "v_mfma_f32_16x16x32_bf16 v[72:75], v[36:39], v[48:51], 0\n"
    "s_waitcnt lgkmcnt(6)\n"
    "v_mfma_f32_16x16x32_bf16 v[76:79], v[40:43], v[48:51], 0\n"
    "s_nop 1\n"
    "v_cndmask_b32 v64, v64, v65, s[42:43]\n"
    "v_cndmask_b32 v65, v66, v67, s[42:43]\n"
    "v_cndmask_b32 v66, v68, v69, s[42:43]\n"
    "v_cndmask_b32 v67, v70, v71, s[42:43]\n"
    "v_cndmask_b32 v68, v72, v73, s[42:43]\n"
    "v_cndmask_b32 v69, v74, v75, s[42:43]\n"
    "v_cndmask_b32 v70, v76, v77, s[42:43]\n"
    "v_cndmask_b32 v71, v78, v79, s[42:43]\n"
    "v_cndmask_b32 v64, v249, v64, s[38:39]\n"
    "v_cndmask_b32 v65, v249, v65, s[40:41]\n"
    "v_cndmask_b32 v66, v249, v66, s[12:13]\n"
    "v_cndmask_b32 v67, v249, v67, s[14:15]\n"
    "v_cndmask_b32 v68, v249, v68, s[8:9]\n"
    "v_cndmask_b32 v69, v249, v69, s[10:11]\n"
    "v_cndmask_b32 v70, v249, v70, s[12:13]\n"
    "v_cndmask_b32 v71, v249, v71, s[14:15]\n"
    "v_max3_f32 v72, v64, v65, v66\n"
    "v_max3_f32 v74, v68, v69, v70\n"
    "v_max_f32 v72, v72, v67\n"
    "v_max_f32 v74, v74, v71\n"
    "s_nop 1\n"
    "v_permlane16_swap_b32 v72, v74\n"
    "v_max_f32 v72, v72, v74\n"
    "v_mov_b32 v74, v72\n"
    "s_nop 1\n"
    "v_permlane32_swap_b32 v72, v74\n"
    "v_max_f32 v72, v72, v74\n"
    "v_mul_f32 v72, s37, v72\n"
    "v_mov_b32 v74, v72\n"
    "s_nop 1\n"
    "v_permlane16_swap_b32 v72, v74\n"
    "v_fma_f32 v64, v64, s37, -v72\n"
    "v_fma_f32 v65, v65, s37, -v72\n"
    "v_fma_f32 v66, v66, s37, -v72\n"
    "v_fma_f32 v67, v67, s37, -v72\n"
    "v_fma_f32 v68, v68, s37, -v74\n"
    "v_fma_f32 v69, v69, s37, -v74\n"
    "v_fma_f32 v70, v70, s37, -v74\n"
    "v_fma_f32 v71, v71, s37, -v74\n"
    "v_exp_f32 v64, v64\n"
    "v_exp_f32 v65, v65\n"
    "v_exp_f32 v66, v66\n"
    "v_exp_f32 v67, v67\n"
    "v_exp_f32 v68, v68\n"
    "v_exp_f32 v69, v69\n"
    "v_exp_f32 v70, v70\n"
    "v_exp_f32 v71, v71\n"
    "v_pk_add_f32 v[72:73], v[64:65], v[66:67]\n"
    "v_pk_add_f32 v[74:75], v[68:69], v[70:71]\n"
    "v_add_f32 v72, v72, v73\n"
    "v_add_f32 v74, v74, v75\n"
    "s_nop 1\n"
    "v_permlane16_swap_b32 v72, v74\n"
    "v_add_f32 v72, v72, v74\n"
    "v_mov_b32 v74, v72\n"
    "s_nop 1\n"
    "v_permlane32_swap_b32 v72, v74\n"
    "v_add_f32 v72, v72, v74\n"
    "v_rcp_f32 v72, v72\n"
    "s_nop 0\n"
    "v_mov_b32 v74, v72\n"
    "s_nop 1\n"
    "v_permlane16_swap_b32 v72, v74\n"
    "v_pk_mul_f32 v[64:65], v[64:65], v[72:73] op_sel_hi:[1,0]\n"
    "v_pk_mul_f32 v[66:67], v[66:67], v[72:73] op_sel_hi:[1,0]\n"
    "v_pk_mul_f32 v[68:69], v[68:69], v[74:75] op_sel_hi:[1,0]\n"
    "v_pk_mul_f32 v[70:71], v[70:71], v[74:75] op_sel_hi:[1,0]\n"
    "v_cvt_pk_bf16_f32 v64, v64, v65\n"
    "v_cvt_pk_bf16_f32 v66, v66, v67\n"
    "v_cvt_pk_bf16_f32 v68, v68, v69\n"
    "v_cvt_pk_bf16_f32 v70, v70, v71\n"
    "v_perm_b32 v80, v64, v64, v247\n"
    "v_perm_b32 v81, v64, v64, v248\n"
    "v_perm_b32 v82, v66, v66, v247\n"
    "v_perm_b32 v83, v66, v66, v248\n"
    "v_perm_b32 v84, v68, v68, v247\n"
    "v_perm_b32 v85, v68, v68, v248\n"
    "v_perm_b32 v86, v70, v70, v247\n"
    "v_perm_b32 v87, v70, v70, v248\n"
    "s_waitcnt lgkmcnt(0)\n"
    "v_mfma_f32_16x16x32_bf16 v[0:3], v[52:55], v[80:83], 0\n"
    "v_mfma_f32_16x16x32_bf16 v[4:7], v[54:57], v[84:87], 0\n"
    "v_mfma_f32_16x16x32_bf16 v[8:11], v[58:61], v[80:83], 0\n"
    "v_mfma_f32_16x16x32_bf16 v[12:15], v[60:63], v[84:87], 0\n"
    "s_mov_b64 s[38:39], s[8:9]\n"
    "s_mov_b64 s[40:41], s[10:11]\n"
    "s_nop 2\n"
    "v_cvt_pk_bf16_f32 v16, v0, v1\n"
    "v_cvt_pk_bf16_f32 v17, v2, v3\n"
    "v_cvt_pk_bf16_f32 v18, v4, v5\n"
    "v_cvt_pk_bf16_f32 v19, v6, v7\n"
    "s_nop 0\n"
    "v_permlane16_swap_b32 v16, v18\n"
    "v_permlane16_swap_b32 v17, v19\n"
    "buffer_store_dwordx4 v[16:19], v245, s[0:3], s33 offen sc1\n"
    "v_cvt_pk_bf16_f32 v20, v8, v9\n"
    "v_cvt_pk_bf16_f32 v21, v10, v11\n"
    "v_cvt_pk_bf16_f32 v22, v12, v13\n"
    "v_cvt_pk_bf16_f32 v23, v14, v15\n"
    "buffer_store_dwordx4 v[20:23], v246, s[0:3], s33 offen sc1\n"
    "s_waitcnt vmcnt(0)\n"
    "buffer_store_dword v116, v230, s[0:3], 0 offen sc1\n"
    "s_mov_b32 s30, s31\n"
    "s_add_u32 s31, s31, 1\n"
    "s_cmp_lt_u32 s30, 64\n"
    "s_cbranch_scc1 .Lb1_top_%=\n"
    "s_waitcnt vmcnt(0) lgkmcnt(0)\n"
    "s_endpgm\n"
    :: "{s[0:3]}"(s0_), "{s[4:7]}"(s4_), "{s[8:11]}"(s8_), "{s[12:15]}"(s12_), "{s[16:19]}"(s16_), "{s[20:23]}"(s20_), "{v[0:3]}"(v0_), "{v[224:227]}"(v224_), "{v[228:231]}"(v228_), "{v[232:235]}"(v232_), "{v[236:239]}"(v236_), "{v[240:243]}"(v240_), "{v[244:247]}"(v244_), "{v[248:251]}"(v248_) : "memory", "v255", "a255", "s95");
    __builtin_unreachable();
}
DEV void b2_role(const MegaArgs& A, const int L, const int c, const int rep) {
    ROLE_HEAD;
    const int b = 4 * c + w;
    const u32x4 ro = L ? u32x4{lo(A.out), hi(A.out), 2048u * 1280u, 0x20000u} : rx;
    if (L == 0) {
#pragma unroll
        for (int k = 0; k < 16; ++k) bst16(rr, ST0 + b * 32768 + (k >> 3) * 16384 + (k & 7) * 1024 + l * 16, ones);
        DRAIN_BARRIER();
        bst4(rr, threadIdx.x == 0 ? xl(Z_ST + c * 128, 0) : OOB, 1u);
    }
    const u32x4 s0_ = {RFL(rx[0]), RFL(rx[1]), RFL(rx[2]), RFL(rx[3])};
    const u32x4 s4_ = {RFL(lo(A.wo + 32 * b)), RFL(hi(A.wo + 32 * b)), RFL((640 * 640 - 32 * b) * 2), RFL(0x20000u)};
    const u32x4 s8_ = {RFL(ro[0]), RFL(ro[1]), RFL(ro[2]), RFL(ro[3])};
    const u32x4 s12_ = {RFL(w), RFL(L << 13), RFL(REC0 + RC_O + w * 2048), RFL(xl(Z_O + rep * 2048, L))};
    const u32x4 s16_ = {RFL(xl(Z_ST, 0)), RFL(ST0), RFL(DBG0 + ((L * 128 + 107 + c) * 4 + w) * 2048), RFL(0u)};
    const u32x4 v0_ = {(unsigned)((0 + (l >> 2)) * 1280 + (l & 3) * 16 - 0), (unsigned)((16 + (l >> 2)) * 1280 + (l & 3) * 16 - 1024), (unsigned)((32 + (l >> 2)) * 1280 + (l & 3) * 16 - 2048), (unsigned)((48 + (l >> 2)) * 1280 + (l & 3) * 16 - 3072)};
    const u32x4 v4_ = {(unsigned)(w * 40960 + (8 * (lq & 1) + 20 * (lq >> 1) + (l16 >> 2)) * 64 + (l & 3) * 16), (unsigned)(w * 40960 + (20 * lq + 16 + (l16 >> 2)) * 64 + (l & 3) * 16), (unsigned)((l < 5 ? l : 0) * 128), (unsigned)(0u)};
    const u32x4 v224_ = {(unsigned)(l * 16), (unsigned)((l < 11 ? l : 0) * 128), (unsigned)(l < 4 ? xl(Z_XN + l * 2048 + c * 128 + w * 4, 1) : OOB), (unsigned)(c * 16384 + l * 16)};
    const u32x4 v228_ = {(unsigned)(L ? (l16 + 0) * 1280 + b * 64 + lq * 16 : RC_X2 + c * 16384 + 0 + l * 16), (unsigned)(L ? (l16 + 16) * 1280 + b * 64 + lq * 16 : RC_X2 + c * 16384 + 1024 + l * 16), (unsigned)(b * 32768 + l16 * 16 + lq * 4), (unsigned)((0 + lq) * 32768 + l16 * 16)};
    const u32x4 v232_ = {(unsigned)((4 + lq) * 32768 + l16 * 16), (unsigned)((8 + lq) * 32768 + l16 * 16), (unsigned)((12 + lq) * 32768 + l16 * 16), (unsigned)((16 + lq) * 32768 + l16 * 16)};
    asm volatile(
    "s_mov_b32 s20, 0\n"
    "s_sleep 35\n"
    "s_sleep 35\n"
    "s_sleep 35\n"
    "s_sleep 35\n"
    "s_sleep 35\n"
    "s_sleep 35\n"
    "s_sleep 35\n"
    "s_sleep 35\n"
    "s_sleep 35\n"
    "s_sleep 35\n"
    "s_sleep 35\n"
    "s_sleep 35\n"
    "s_sleep 35\n"
    "s_sleep 35\n"
    ".Lb2_ho1_%=:\n"
    "s_mul_i32 s28, s12, 40960\n"
    "s_mov_b32 s34, 0\n"
    "s_add_u32 m0, s28, 0\n"
    "s_nop 0\n"
    "buffer_load_dwordx4 v0, s[4:7], s34 offen offset:0 lds\n"
    "buffer_load_dwordx4 v1, s[4:7], s34 offen offset:1024 lds\n"
    "buffer_load_dwordx4 v2, s[4:7], s34 offen offset:2048 lds\n"
    "buffer_load_dwordx4 v3, s[4:7], s34 offen offset:3072 lds\n"
    "s_mov_b32 s34, 81920\n"
    "s_add_u32 m0, s28, 4096\n"
    "s_nop 0\n"
    "buffer_load_dwordx4 v0, s[4:7], s34 offen offset:0 lds\n"
    "buffer_load_dwordx4 v1, s[4:7], s34 offen offset:1024 lds\n"
    "buffer_load_dwordx4 v2, s[4:7], s34 offen offset:2048 lds\n"
    "buffer_load_dwordx4 v3, s[4:7], s34 offen offset:3072 lds\n"
    "s_mov_b32 s34, 163840\n"
    "s_add_u32 m0, s28, 8192\n"
    "s_nop 0\n"
    "buffer_load_dwordx4 v0, s[4:7], s34 offen offset:0 lds\n"
    "buffer_load_dwordx4 v1, s[4:7], s34 offen offset:1024 lds\n"
    "buffer_load_dwordx4 v2, s[4:7], s34 offen offset:2048 lds\n"
    "buffer_load_dwordx4 v3, s[4:7], s34 offen offset:3072 lds\n"
    "s_mov_b32 s34, 245760\n"
    "s_add_u32 m0, s28, 12288\n"
    "s_nop 0\n"
    "buffer_load_dwordx4 v0, s[4:7], s34 offen offset:0 lds\n"
    "buffer_load_dwordx4 v1, s[4:7], s34 offen offset:1024 lds\n"
    "buffer_load_dwordx4 v2, s[4:7], s34 offen offset:2048 lds\n"
    "buffer_load_dwordx4 v3, s[4:7], s34 offen offset:3072 lds\n"
    "s_mov_b32 s34, 327680\n"
    "s_add_u32 m0, s28, 16384\n"
    "s_nop 0\n"
    "buffer_load_dwordx4 v0, s[4:7], s34 offen offset:0 lds\n"
    "buffer_load_dwordx4 v1, s[4:7], s34 offen offset:1024 lds\n"
    "buffer_load_dwordx4 v2, s[4:7], s34 offen offset:2048 lds\n"
    "buffer_load_dwordx4 v3, s[4:7], s34 offen offset:3072 lds\n"
    "s_mov_b32 s34, 409600\n"
    "s_add_u32 m0, s28, 20480\n"
    "s_nop 0\n"
    "buffer_load_dwordx4 v0, s[4:7], s34 offen offset:0 lds\n"
    "buffer_load_dwordx4 v1, s[4:7], s34 offen offset:1024 lds\n"
    "buffer_load_dwordx4 v2, s[4:7], s34 offen offset:2048 lds\n"
    "buffer_load_dwordx4 v3, s[4:7], s34 offen offset:3072 lds\n"
    "s_mov_b32 s34, 491520\n"
    "s_add_u32 m0, s28, 24576\n"
    "s_nop 0\n"
    "buffer_load_dwordx4 v0, s[4:7], s34 offen offset:0 lds\n"
    "buffer_load_dwordx4 v1, s[4:7], s34 offen offset:1024 lds\n"
    "buffer_load_dwordx4 v2, s[4:7], s34 offen offset:2048 lds\n"
    "buffer_load_dwordx4 v3, s[4:7], s34 offen offset:3072 lds\n"
    "s_mov_b32 s34, 573440\n"
    "s_add_u32 m0, s28, 28672\n"
    "s_nop 0\n"
    "buffer_load_dwordx4 v0, s[4:7], s34 offen offset:0 lds\n"
    "buffer_load_dwordx4 v1, s[4:7], s34 offen offset:1024 lds\n"
    "buffer_load_dwordx4 v2, s[4:7], s34 offen offset:2048 lds\n"
    "buffer_load_dwordx4 v3, s[4:7], s34 offen offset:3072 lds\n"
    "s_mov_b32 s34, 655360\n"
    "s_add_u32 m0, s28, 32768\n"
    "s_nop 0\n"
    "buffer_load_dwordx4 v0, s[4:7], s34 offen offset:0 lds\n"
    "buffer_load_dwordx4 v1, s[4:7], s34 offen offset:1024 lds\n"
    "buffer_load_dwordx4 v2, s[4:7], s34 offen offset:2048 lds\n"
    "buffer_load_dwordx4 v3, s[4:7], s34 offen offset:3072 lds\n"
    "s_mov_b32 s34, 737280\n"
    "s_add_u32 m0, s28, 36864\n"
    "s_nop 0\n"
    "buffer_load_dwordx4 v0, s[4:7], s34 offen offset:0 lds\n"
    "buffer_load_dwordx4 v1, s[4:7], s34 offen offset:1024 lds\n"
    "buffer_load_dwordx4 v2, s[4:7], s34 offen offset:2048 lds\n"
    "buffer_load_dwordx4 v3, s[4:7], s34 offen offset:3072 lds\n"
    "s_waitcnt vmcnt(0)\n"
    "ds_read_b64_tr_b16 a[0:1], v4 offset:0\n"
    "ds_read_b64_tr_b16 a[2:3], v4 offset:256\n"
    "ds_read_b64_tr_b16 a[80:81], v4 offset:8\n"
    "ds_read_b64_tr_b16 a[82:83], v4 offset:264\n"
    "s_waitcnt lgkmcnt(0)\n"
    "ds_read_b64_tr_b16 a[4:5], v4 offset:2560\n"
    "ds_read_b64_tr_b16 a[6:7], v4 offset:2816\n"
    "ds_read_b64_tr_b16 a[84:85], v4 offset:2568\n"
    "ds_read_b64_tr_b16 a[86:87], v4 offset:2824\n"
    "s_waitcnt lgkmcnt(0)\n"
    "ds_read_b64_tr_b16 a[8:9], v4 offset:5120\n"
    "ds_read_b64_tr_b16 a[10:11], v4 offset:5376\n"
    "ds_read_b64_tr_b16 a[88:89], v4 offset:5128\n"
    "ds_read_b64_tr_b16 a[90:91], v4 offset:5384\n"
    "s_waitcnt lgkmcnt(0)\n"
    "ds_read_b64_tr_b16 a[12:13], v4 offset:7680\n"
    "ds_read_b64_tr_b16 a[14:15], v4 offset:7936\n"
    "ds_read_b64_tr_b16 a[92:93], v4 offset:7688\n"
    "ds_read_b64_tr_b16 a[94:95], v4 offset:7944\n"
    "s_waitcnt lgkmcnt(0)\n"
    "ds_read_b64_tr_b16 a[16:17], v4 offset:10240\n"
    "ds_read_b64_tr_b16 a[18:19], v4 offset:10496\n"
    "ds_read_b64_tr_b16 a[96:97], v4 offset:10248\n"
    "ds_read_b64_tr_b16 a[98:99], v4 offset:10504\n"
    "s_waitcnt lgkmcnt(0)\n"
    "ds_read_b64_tr_b16 a[20:21], v4 offset:12800\n"
    "ds_read_b64_tr_b16 a[22:23], v4 offset:13056\n"
    "ds_read_b64_tr_b16 a[100:101], v4 offset:12808\n"
    "ds_read_b64_tr_b16 a[102:103], v4 offset:13064\n"
    "s_waitcnt lgkmcnt(0)\n"
    "ds_read_b64_tr_b16 a[24:25], v4 offset:15360\n"
    "ds_read_b64_tr_b16 a[26:27], v4 offset:15616\n"
    "ds_read_b64_tr_b16 a[104:105], v4 offset:15368\n"
    "ds_read_b64_tr_b16 a[106:107], v4 offset:15624\n"
    "s_waitcnt lgkmcnt(0)\n"
    "ds_read_b64_tr_b16 a[28:29], v4 offset:17920\n"
    "ds_read_b64_tr_b16 a[30:31], v4 offset:18176\n"
    "ds_read_b64_tr_b16 a[108:109], v4 offset:17928\n"
    "ds_read_b64_tr_b16 a[110:111], v4 offset:18184\n"
    "s_waitcnt lgkmcnt(0)\n"
    "ds_read_b64_tr_b16 a[32:33], v4 offset:20480\n"
    "ds_read_b64_tr_b16 a[34:35], v4 offset:20736\n"
    "ds_read_b64_tr_b16 a[112:113], v4 offset:20488\n"
    "ds_read_b64_tr_b16 a[114:115], v4 offset:20744\n"
    "s_waitcnt lgkmcnt(0)\n"
    "ds_read_b64_tr_b16 a[36:37], v4 offset:23040\n"
    "ds_read_b64_tr_b16 a[38:39], v4 offset:23296\n"
    "ds_read_b64_tr_b16 a[116:117], v4 offset:23048\n"
    "ds_read_b64_tr_b16 a[118:119], v4 offset:23304\n"
    "s_waitcnt lgkmcnt(0)\n"
    "ds_read_b64_tr_b16 a[40:41], v4 offset:25600\n"
    "ds_read_b64_tr_b16 a[42:43], v4 offset:25856\n"
    "ds_read_b64_tr_b16 a[120:121], v4 offset:25608\n"
    "ds_read_b64_tr_b16 a[122:123], v4 offset:25864\n"
    "s_waitcnt lgkmcnt(0)\n"
    "ds_read_b64_tr_b16 a[44:45], v4 offset:28160\n"
    "ds_read_b64_tr_b16 a[46:47], v4 offset:28416\n"
    "ds_read_b64_tr_b16 a[124:125], v4 offset:28168\n"
    "ds_read_b64_tr_b16 a[126:127], v4 offset:28424\n"
    "s_waitcnt lgkmcnt(0)\n"
    "ds_read_b64_tr_b16 a[48:49], v4 offset:30720\n"
    "ds_read_b64_tr_b16 a[50:51], v4 offset:30976\n"
    "ds_read_b64_tr_b16 a[128:129], v4 offset:30728\n"
    "ds_read_b64_tr_b16 a[130:131], v4 offset:30984\n"
    "s_waitcnt lgkmcnt(0)\n"
    "ds_read_b64_tr_b16 a[52:53], v4 offset:33280\n"
    "ds_read_b64_tr_b16 a[54:55], v4 offset:33536\n"
    "ds_read_b64_tr_b16 a[132:133], v4 offset:33288\n"
    "ds_read_b64_tr_b16 a[134:135], v4 offset:33544\n"
    "s_waitcnt lgkmcnt(0)\n"
    "ds_read_b64_tr_b16 a[56:57], v4 offset:35840\n"
    "ds_read_b64_tr_b16 a[58:59], v4 offset:36096\n"
    "ds_read_b64_tr_b16 a[136:137], v4 offset:35848\n"
    "ds_read_b64_tr_b16 a[138:139], v4 offset:36104\n"
    "s_waitcnt lgkmcnt(0)\n"
    "ds_read_b64_tr_b16 a[60:61], v4 offset:38400\n"
    "ds_read_b64_tr_b16 a[62:63], v4 offset:38656\n"
    "ds_read_b64_tr_b16 a[140:141], v4 offset:38408\n"
    "ds_read_b64_tr_b16 a[142:143], v4 offset:38664\n"
    "s_waitcnt lgkmcnt(0)\n"
    "ds_read_b64_tr_b16 a[64:65], v5 offset:0\n"
    "ds_read_b64_tr_b16 a[66:67], v5 offset:5120\n"
    "ds_read_b64_tr_b16 a[144:145], v5 offset:8\n"
    "ds_read_b64_tr_b16 a[146:147], v5 offset:5128\n"
    "s_waitcnt lgkmcnt(0)\n"
    "ds_read_b64_tr_b16 a[68:69], v5 offset:10240\n"
    "ds_read_b64_tr_b16 a[70:71], v5 offset:15360\n"
    "ds_read_b64_tr_b16 a[148:149], v5 offset:10248\n"
    "ds_read_b64_tr_b16 a[150:151], v5 offset:15368\n"
    "s_waitcnt lgkmcnt(0)\n"
    "ds_read_b64_tr_b16 a[72:73], v5 offset:20480\n"
    "ds_read_b64_tr_b16 a[74:75], v5 offset:25600\n"
    "ds_read_b64_tr_b16 a[152:153], v5 offset:20488\n"
    "ds_read_b64_tr_b16 a[154:155], v5 offset:25608\n"
    "s_waitcnt lgkmcnt(0)\n"
    "ds_read_b64_tr_b16 a[76:77], v5 offset:30720\n"
    "ds_read_b64_tr_b16 a[78:79], v5 offset:35840\n"
    "ds_read_b64_tr_b16 a[156:157], v5 offset:30728\n"
    "ds_read_b64_tr_b16 a[158:159], v5 offset:35848\n"
    "s_waitcnt lgkmcnt(0)\n"
    "s_mov_b32 s26, -2\n"
    ".Lb2_po2_%=:\n"
    "buffer_load_dword v8, v6, s[0:3], s16 offen sc1\n"
    "s_waitcnt vmcnt(0)\n"
    "v_cmp_gt_i32 vcc, 1, v8\n"
    "s_nop 0\n"
    "s_cbranch_vccz .Lb2_pd3_%=\n"
    "s_add_u32 s20, s20, 1\n"
    "s_cmp_lt_u32 s20, 0x40000\n"
    "s_cbranch_scc1 .Lb2_sp4_%=\n"
    "v_mov_b32 v8, 70\n"
    "s_mov_b32 s24, 33728\n"
    "buffer_store_dword v8, off, s[0:3], s24 sc1\n"
    "s_waitcnt vmcnt(0)\n"
    "s_branch .Lb2_pd3_%=\n"
    ".Lb2_sp4_%=:\n"
    "s_sleep 4\n"
    "s_branch .Lb2_po2_%=\n"
    ".Lb2_pd3_%=:\n"
    "s_mov_b32 s26, 0\n"
    "s_lshl_b32 s33, s12, 11\n"
    "s_mov_b32 s35, 0\n"
    ".Lb2_top_%=:\n"
    "s_lshl_b32 s28, s26, 19\n"
    "s_lshl_b32 s25, s26, 11\n"
    "s_xor_b32 s25, s25, s13\n"
    "s_and_b32 s25, s25, 0x2000\n"
    "s_add_u32 s28, s28, s25\n"
    "s_add_u32 s28, s28, s14\n"
    "s_add_u32 s27, s26, 1\n"
    "s_sub_u32 s29, s28, 81920\n"
    "s_sub_u32 s31, s28, 163840\n"
    "s_xor_b32 s31, s31, 0x2000\n"
    "s_mul_i32 s30, s26, 40960\n"
    "s_cmp_eq_u32 s13, 0\n"
    "s_cselect_b32 s30, s31, s30\n"
    "s_and_b32 s32, s26, 32\n"
    "s_add_u32 s32, s32, s26\n"
    "s_lshl_b32 s32, s32, 8\n"
    "s_add_u32 s32, s32, s17\n"
    "s_cmp_lg_u32 s12, 0\n"
    "s_cbranch_scc1 .Lb2_pd6_%=\n"
    ".Lb2_rt7_%=:\n"
    "s_memrealtime s[22:23]\n"
    "s_waitcnt lgkmcnt(0)\n"
    "s_sub_u32 s24, s35, s22\n"
    "s_cmp_lt_u32 s24, 600\n"
    "s_cbranch_scc1 .Lb2_rt7_%=\n"
    ".Lb2_po5_%=:\n"
    "buffer_load_dwordx4 v[84:87], v225, s[0:3], s15 offen sc1\n"
    "s_memrealtime s[22:23]\n"
    "s_waitcnt vmcnt(0) lgkmcnt(0)\n"
    "s_add_u32 s35, s22, 274\n"
    "v_min3_i32 v84, v84, v85, v86\n"
    "v_min_i32 v84, v84, v87\n"
    "v_cmp_gt_i32 vcc, s27, v84\n"
    "s_nop 0\n"
    "s_cbranch_vccz .Lb2_pd6_%=\n"
    "s_add_u32 s20, s20, 1\n"
    "s_cmp_lt_u32 s20, 0x40000\n"
    "s_cbranch_scc1 .Lb2_sp8_%=\n"
    "v_mov_b32 v84, 71\n"
    "s_mov_b32 s24, 33728\n"
    "buffer_store_dword v84, off, s[0:3], s24 sc1\n"
    "s_waitcnt vmcnt(0)\n"
    "s_branch .Lb2_pd6_%=\n"
    ".Lb2_sp8_%=:\n"
    "s_sleep 2\n"
    "s_branch .Lb2_po5_%=\n"
    ".Lb2_pd6_%=:\n"
    "s_barrier\n"
    "s_add_u32 m0, s33, 0\n"
    "s_nop 0\n"
    "buffer_load_dwordx4 v224, s[0:3], s28 offen sc1 lds\n"
    "buffer_load_dwordx4 v224, s[0:3], s28 offen offset:1024 sc1 lds\n"
    "s_add_u32 m0, s33, 8192\n"
    "s_add_u32 s28, s28, 0x4000\n"
    "buffer_load_dwordx4 v224, s[0:3], s28 offen sc1 lds\n"
    "buffer_load_dwordx4 v224, s[0:3], s28 offen offset:1024 sc1 lds\n"
    "s_add_u32 m0, s33, 16384\n"
    "s_add_u32 s28, s28, 0x4000\n"
    "buffer_load_dwordx4 v224, s[0:3], s28 offen sc1 lds\n"
    "buffer_load_dwordx4 v224, s[0:3], s28 offen offset:1024 sc1 lds\n"
    "s_add_u32 m0, s33, 24576\n"
    "s_add_u32 s28, s28, 0x4000\n"
    "buffer_load_dwordx4 v224, s[0:3], s28 offen sc1 lds\n"
    "buffer_load_dwordx4 v224, s[0:3], s28 offen offset:1024 sc1 lds\n"
    "s_add_u32 m0, s33, 32768\n"
    "s_add_u32 s28, s28, 0x4000\n"
    "buffer_load_dwordx4 v224, s[0:3], s28 offen sc1 lds\n"
    "buffer_load_dwordx4 v224, s[0:3], s28 offen offset:1024 sc1 lds\n"
    "buffer_load_dwordx4 v[48:51], v227, s[0:3], s29 offen offset:0 sc1\n"
    "buffer_load_dwordx4 v[52:55], v227, s[0:3], s29 offen offset:1024 sc1\n"
    "s_waitcnt vmcnt(10)\n"
    "s_barrier\n"
    "ds_read_b128 v[16:19], v224 offset:0\n"
    "ds_read_b128 v[20:23], v224 offset:1024\n"
    "ds_read_b128 v[24:27], v224 offset:2048\n"
    "ds_read_b128 v[28:31], v224 offset:3072\n"
    "ds_read_b128 v[32:35], v224 offset:4096\n"
    "ds_read_b128 v[36:39], v224 offset:5120\n"
    "s_waitcnt lgkmcnt(4)\n"
    "v_mfma_f32_16x16x32_bf16 v[0:3], a[0:3], v[16:19], 0\n"
    "ds_read_b128 v[40:43], v224 offset:6144\n"
    "v_mfma_f32_16x16x32_bf16 v[8:11], a[0:3], v[20:23], 0\n"
    "ds_read_b128 v[44:47], v224 offset:7168\n"
    "v_mfma_f32_16x16x32_bf16 v[4:7], a[80:83], v[16:19], 0\n"
    "v_mfma_f32_16x16x32_bf16 v[12:15], a[80:83], v[20:23], 0\n"
    "s_waitcnt lgkmcnt(4)\n"
    "v_mfma_f32_16x16x32_bf16 v[0:3], a[4:7], v[24:27], v[0:3]\n"
    "s_waitcnt vmcnt(8)\n"
    "s_barrier\n"
    "v_mfma_f32_16x16x32_bf16 v[8:11], a[4:7], v[28:31], v[8:11]\n"
    "ds_read_b128 v[16:19], v224 offset:8192\n"
    "v_mfma_f32_16x16x32_bf16 v[4:7], a[84:87], v[24:27], v[4:7]\n"
    "ds_read_b128 v[20:23], v224 offset:9216\n"
    "v_mfma_f32_16x16x32_bf16 v[12:15], a[84:87], v[28:31], v[12:15]\n"
    "s_waitcnt lgkmcnt(4)\n"
    "v_mfma_f32_16x16x32_bf16 v[0:3], a[8:11], v[32:35], v[0:3]\n"
    "ds_read_b128 v[24:27], v224 offset:10240\n"
    "v_mfma_f32_16x16x32_bf16 v[8:11], a[8:11], v[36:39], v[8:11]\n"
    "ds_read_b128 v[28:31], v224 offset:11264\n"
    "v_mfma_f32_16x16x32_bf16 v[4:7], a[88:91], v[32:35], v[4:7]\n"
    "v_mfma_f32_16x16x32_bf16 v[12:15], a[88:91], v[36:39], v[12:15]\n"
    "s_waitcnt lgkmcnt(4)\n"
    "v_mfma_f32_16x16x32_bf16 v[0:3], a[12:15], v[40:43], v[0:3]\n"
    "ds_read_b128 v[32:35], v224 offset:12288\n"
    "v_mfma_f32_16x16x32_bf16 v[8:11], a[12:15], v[44:47], v[8:11]\n"
    "ds_read_b128 v[36:39], v224 offset:13312\n"
    "v_mfma_f32_16x16x32_bf16 v[4:7], a[92:95], v[40:43], v[4:7]\n"
    "v_mfma_f32_16x16x32_bf16 v[12:15], a[92:95], v[44:47], v[12:15]\n"
    "s_waitcnt lgkmcnt(4)\n"
    "v_mfma_f32_16x16x32_bf16 v[0:3], a[16:19], v[16:19], v[0:3]\n"
    "ds_read_b128 v[40:43], v224 offset:14336\n"
    "v_mfma_f32_16x16x32_bf16 v[8:11], a[16:19], v[20:23], v[8:11]\n"
    "ds_read_b128 v[44:47], v224 offset:15360\n"
    "v_mfma_f32_16x16x32_bf16 v[4:7], a[96:99], v[16:19], v[4:7]\n"
    "v_mfma_f32_16x16x32_bf16 v[12:15], a[96:99], v[20:23], v[12:15]\n"
    "s_waitcnt lgkmcnt(4)\n"
    "v_mfma_f32_16x16x32_bf16 v[0:3], a[20:23], v[24:27], v[0:3]\n"
    "s_waitcnt vmcnt(6)\n"
    "s_barrier\n"
    "v_mfma_f32_16x16x32_bf16 v[8:11], a[20:23], v[28:31], v[8:11]\n"
    "ds_read_b128 v[16:19], v224 offset:16384\n"
    "v_mfma_f32_16x16x32_bf16 v[4:7], a[100:103], v[24:27], v[4:7]\n"
    "ds_read_b128 v[20:23], v224 offset:17408\n"
    "v_mfma_f32_16x16x32_bf16 v[12:15], a[100:103], v[28:31], v[12:15]\n"
    "s_waitcnt lgkmcnt(4)\n"
    "v_mfma_f32_16x16x32_bf16 v[0:3], a[24:27], v[32:35], v[0:3]\n"
    "ds_read_b128 v[24:27], v224 offset:18432\n"
    "v_mfma_f32_16x16x32_bf16 v[8:11], a[24:27], v[36:39], v[8:11]\n"
    "ds_read_b128 v[28:31], v224 offset:19456\n"
    "v_mfma_f32_16x16x32_bf16 v[4:7], a[104:107], v[32:35], v[4:7]\n"
    "v_mfma_f32_16x16x32_bf16 v[12:15], a[104:107], v[36:39], v[12:15]\n"
    "s_waitcnt lgkmcnt(4)\n"
    "v_mfma_f32_16x16x32_bf16 v[0:3], a[28:31], v[40:43], v[0:3]\n"
    "ds_read_b128 v[32:35], v224 offset:20480\n"
    "v_mfma_f32_16x16x32_bf16 v[8:11], a[28:31], v[44:47], v[8:11]\n"
    "ds_read_b128 v[36:39], v224 offset:21504\n"
    "v_mfma_f32_16x16x32_bf16 v[4:7], a[108:111], v[40:43], v[4:7]\n"
    "v_mfma_f32_16x16x32_bf16 v[12:15], a[108:111], v[44:47], v[12:15]\n"
    "s_waitcnt lgkmcnt(4)\n"
    "v_mfma_f32_16x16x32_bf16 v[0:3], a[32:35], v[16:19], v[0:3]\n"
    "ds_read_b128 v[40:43], v224 offset:22528\n"
    "v_mfma_f32_16x16x32_bf16 v[8:11], a[32:35], v[20:23], v[8:11]\n"
    "ds_read_b128 v[44:47], v224 offset:23552\n"
    "v_mfma_f32_16x16x32_bf16 v[4:7], a[112:115], v[16:19], v[4:7]\n"
    "v_mfma_f32_16x16x32_bf16 v[12:15], a[112:115], v[20:23], v[12:15]\n"
    "s_waitcnt lgkmcnt(4)\n"
    "v_mfma_f32_16x16x32_bf16 v[0:3], a[36:39], v[24:27], v[0:3]\n"
    "s_waitcnt vmcnt(4)\n"
    "s_barrier\n"
    "v_mfma_f32_16x16x32_bf16 v[8:11], a[36:39], v[28:31], v[8:11]\n"
    "ds_read_b128 v[16:19], v224 offset:24576\n"
    "v_mfma_f32_16x16x32_bf16 v[4:7], a[116:119], v[24:27], v[4:7]\n"
    "ds_read_b128 v[20:23], v224 offset:25600\n"
    "v_mfma_f32_16x16x32_bf16 v[12:15], a[116:119], v[28:31], v[12:15]\n"
    "s_waitcnt lgkmcnt(4)\n"
    "v_mfma_f32_16x16x32_bf16 v[0:3], a[40:43], v[32:35], v[0:3]\n"
    "ds_read_b128 v[24:27], v224 offset:26624\n"
    "v_mfma_f32_16x16x32_bf16 v[8:11], a[40:43], v[36:39], v[8:11]\n"
    "ds_read_b128 v[28:31], v224 offset:27648\n"
    "v_mfma_f32_16x16x32_bf16 v[4:7], a[120:123], v[32:35], v[4:7]\n"
    "v_mfma_f32_16x16x32_bf16 v[12:15], a[120:123], v[36:39], v[12:15]\n"
    "s_waitcnt lgkmcnt(4)\n"
    "v_mfma_f32_16x16x32_bf16 v[0:3], a[44:47], v[40:43], v[0:3]\n"
    "ds_read_b128 v[32:35], v224 offset:28672\n"
    "v_mfma_f32_16x16x32_bf16 v[8:11], a[44:47], v[44:47], v[8:11]\n"
    "ds_read_b128 v[36:39], v224 offset:29696\n"
    "v_mfma_f32_16x16x32_bf16 v[4:7], a[124:127], v[40:43], v[4:7]\n"
    "v_mfma_f32_16x16x32_bf16 v[12:15], a[124:127], v[44:47], v[12:15]\n"
    "s_waitcnt lgkmcnt(4)\n"
    "v_mfma_f32_16x16x32_bf16 v[0:3], a[48:51], v[16:19], v[0:3]\n"
    "ds_read_b128 v[40:43], v224 offset:30720\n"
    "v_mfma_f32_16x16x32_bf16 v[8:11], a[48:51], v[20:23], v[8:11]\n"
    "ds_read_b128 v[44:47], v224 offset:31744\n"
    "v_mfma_f32_16x16x32_bf16 v[4:7], a[128:131], v[16:19], v[4:7]\n"
    "v_mfma_f32_16x16x32_bf16 v[12:15], a[128:131], v[20:23], v[12:15]\n"
    "s_waitcnt lgkmcnt(4)\n"
    "v_mfma_f32_16x16x32_bf16 v[0:3], a[52:55], v[24:27], v[0:3]\n"
    "s_waitcnt vmcnt(2)\n"
    "s_barrier\n"
    "v_mfma_f32_16x16x32_bf16 v[8:11], a[52:55], v[28:31], v[8:11]\n"
    "ds_read_b128 v[16:19], v224 offset:32768\n"
    "v_mfma_f32_16x16x32_bf16 v[4:7], a[132:135], v[24:27], v[4:7]\n"
    "ds_read_b128 v[20:23], v224 offset:33792\n"
    "v_mfma_f32_16x16x32_bf16 v[12:15], a[132:135], v[28:31], v[12:15]\n"
    "s_waitcnt lgkmcnt(4)\n"
    "v_mfma_f32_16x16x32_bf16 v[0:3], a[56:59], v[32:35], v[0:3]\n"
    "ds_read_b128 v[24:27], v224 offset:34816\n"
    "v_mfma_f32_16x16x32_bf16 v[8:11], a[56:59], v[36:39], v[8:11]\n"
    "ds_read_b128 v[28:31], v224 offset:35840\n"
    "v_mfma_f32_16x16x32_bf16 v[4:7], a[136:139], v[32:35], v[4:7]\n"
    "v_mfma_f32_16x16x32_bf16 v[12:15], a[136:139], v[36:39], v[12:15]\n"
    "s_waitcnt lgkmcnt(4)\n"
    "v_mfma_f32_16x16x32_bf16 v[0:3], a[60:63], v[40:43], v[0:3]\n"
    "s_waitcnt lgkmcnt(2)\n"
    "ds_read_b128 v[32:35], v224 offset:36864\n"
    "v_mfma_f32_16x16x32_bf16 v[8:11], a[60:63], v[44:47], v[8:11]\n"
    "v_swap_b32 v18, v20\n"
    "v_mfma_f32_16x16x32_bf16 v[4:7], a[140:143], v[40:43], v[4:7]\n"
    "v_swap_b32 v19, v21\n"
    "v_mfma_f32_16x16x32_bf16 v[12:15], a[140:143], v[44:47], v[12:15]\n"
    "ds_read_b128 v[36:39], v224 offset:37888\n"
    "v_mfma_f32_16x16x32_bf16 v[0:3], a[64:67], v[16:19], v[0:3]\n"
    "s_waitcnt lgkmcnt(2)\n"
    "ds_read_b128 v[40:43], v224 offset:38912\n"
    "v_mfma_f32_16x16x32_bf16 v[8:11], a[64:67], v[20:23], v[8:11]\n"
    "v_swap_b32 v26, v28\n"
    "v_mfma_f32_16x16x32_bf16 v[4:7], a[144:147], v[16:19], v[4:7]\n"
    "v_swap_b32 v27, v29\n"
    "v_mfma_f32_16x16x32_bf16 v[12:15], a[144:147], v[20:23], v[12:15]\n"
    "ds_read_b128 v[44:47], v224 offset:39936\n"
    "v_mfma_f32_16x16x32_bf16 v[0:3], a[68:71], v[24:27], v[0:3]\n"
    "s_waitcnt lgkmcnt(2)\n"
    "v_mfma_f32_16x16x32_bf16 v[8:11], a[68:71], v[28:31], v[8:11]\n"
    "v_swap_b32 v34, v36\n"
    "v_mfma_f32_16x16x32_bf16 v[4:7], a[148:151], v[24:27], v[4:7]\n"
    "v_swap_b32 v35, v37\n"
    "v_mfma_f32_16x16x32_bf16 v[12:15], a[148:151], v[28:31], v[12:15]\n"
    "s_nop 0\n"
    "v_mfma_f32_16x16x32_bf16 v[0:3], a[72:75], v[32:35], v[0:3]\n"
    "s_waitcnt lgkmcnt(0)\n"
    "v_mfma_f32_16x16x32_bf16 v[8:11], a[72:75], v[36:39], v[8:11]\n"
    "v_swap_b32 v42, v44\n"
    "v_mfma_f32_16x16x32_bf16 v[4:7], a[152:155], v[32:35], v[4:7]\n"
    "v_swap_b32 v43, v45\n"
    "v_mfma_f32_16x16x32_bf16 v[12:15], a[152:155], v[36:39], v[12:15]\n"
    "s_nop 0\n"
    "v_mfma_f32_16x16x32_bf16 v[0:3], a[76:79], v[40:43], v[0:3]\n"
    "v_mfma_f32_16x16x32_bf16 v[8:11], a[76:79], v[44:47], v[8:11]\n"
    "v_mfma_f32_16x16x32_bf16 v[4:7], a[156:159], v[40:43], v[4:7]\n"
    "v_mfma_f32_16x16x32_bf16 v[12:15], a[156:159], v[44:47], v[12:15]\n"
    "s_waitcnt vmcnt(0)\n"
    "v_lshlrev_b32 v16, 16, v48\n"
    "v_and_b32 v17, 0xffff0000, v48\n"
    "v_lshlrev_b32 v18, 16, v49\n"
    "v_and_b32 v19, 0xffff0000, v49\n"
    "v_lshlrev_b32 v20, 16, v50\n"
    "v_and_b32 v21, 0xffff0000, v50\n"
    "v_lshlrev_b32 v22, 16, v51\n"
    "v_and_b32 v23, 0xffff0000, v51\n"
    "v_lshlrev_b32 v24, 16, v52\n"
    "v_and_b32 v25, 0xffff0000, v52\n"
    "v_lshlrev_b32 v26, 16, v53\n"
    "v_and_b32 v27, 0xffff0000, v53\n"
    "v_lshlrev_b32 v28, 16, v54\n"
    "v_and_b32 v29, 0xffff0000, v54\n"
    "v_lshlrev_b32 v30, 16, v55\n"
    "v_and_b32 v31, 0xffff0000, v55\n"
    "v_cvt_pk_bf16_f32 v56, v0, v1\n"
    "v_cvt_pk_bf16_f32 v57, v2, v3\n"
    "v_cvt_pk_bf16_f32 v58, v4, v5\n"
    "v_cvt_pk_bf16_f32 v59, v6, v7\n"
    "v_lshlrev_b32 v0, 16, v56\n"
    "v_and_b32 v1, 0xffff0000, v56\n"
    "v_lshlrev_b32 v2, 16, v57\n"
    "v_and_b32 v3, 0xffff0000, v57\n"
    "v_lshlrev_b32 v4, 16, v58\n"
    "v_and_b32 v5, 0xffff0000, v58\n"
    "v_lshlrev_b32 v6, 16, v59\n"
    "v_and_b32 v7, 0xffff0000, v59\n"
    "v_pk_add_f32 v[0:1], v[0:1], v[16:17]\n"
    "v_pk_add_f32 v[2:3], v[2:3], v[18:19]\n"
    "v_pk_add_f32 v[4:5], v[4:5], v[20:21]\n"
    "v_pk_add_f32 v[6:7], v[6:7], v[22:23]\n"
    "v_cvt_pk_bf16_f32 v56, v0, v1\n"
    "v_cvt_pk_bf16_f32 v57, v2, v3\n"
    "v_cvt_pk_bf16_f32 v58, v4, v5\n"
    "v_cvt_pk_bf16_f32 v59, v6, v7\n"
    "v_cvt_pk_bf16_f32 v60, v8, v9\n"
    "v_cvt_pk_bf16_f32 v61, v10, v11\n"
    "v_cvt_pk_bf16_f32 v62, v12, v13\n"
    "v_cvt_pk_bf16_f32 v63, v14, v15\n"
    "v_lshlrev_b32 v8, 16, v60\n"
    "v_and_b32 v9, 0xffff0000, v60\n"
    "v_lshlrev_b32 v10, 16, v61\n"
    "v_and_b32 v11, 0xffff0000, v61\n"
    "v_lshlrev_b32 v12, 16, v62\n"
    "v_and_b32 v13, 0xffff0000, v62\n"
    "v_lshlrev_b32 v14, 16, v63\n"
    "v_and_b32 v15, 0xffff0000, v63\n"
    "v_pk_add_f32 v[8:9], v[8:9], v[24:25]\n"
    "v_pk_add_f32 v[10:11], v[10:11], v[26:27]\n"
    "v_pk_add_f32 v[12:13], v[12:13], v[28:29]\n"
    "v_pk_add_f32 v[14:15], v[14:15], v[30:31]\n"
    "v_cvt_pk_bf16_f32 v60, v8, v9\n"
    "v_cvt_pk_bf16_f32 v61, v10, v11\n"
    "v_cvt_pk_bf16_f32 v62, v12, v13\n"
    "v_cvt_pk_bf16_f32 v63, v14, v15\n"
    "s_cmp_lg_u32 s13, 0\n"
    "s_cbranch_scc1 .Lb2_out_%=\n"
    "v_lshlrev_b32 v0, 16, v56\n"
    "v_and_b32 v1, 0xffff0000, v56\n"
    "v_lshlrev_b32 v2, 16, v57\n"
    "v_and_b32 v3, 0xffff0000, v57\n"
    "v_lshlrev_b32 v4, 16, v58\n"
    "v_and_b32 v5, 0xffff0000, v58\n"
    "v_lshlrev_b32 v6, 16, v59\n"
    "v_and_b32 v7, 0xffff0000, v59\n"
    "v_lshlrev_b32 v8, 16, v60\n"
    "v_and_b32 v9, 0xffff0000, v60\n"
    "v_lshlrev_b32 v10, 16, v61\n"
    "v_and_b32 v11, 0xffff0000, v61\n"
    "v_lshlrev_b32 v12, 16, v62\n"
    "v_and_b32 v13, 0xffff0000, v62\n"
    "v_lshlrev_b32 v14, 16, v63\n"
    "v_and_b32 v15, 0xffff0000, v63\n"
    "v_pk_add_f32 v[64:65], v[0:1], v[2:3]\n"
    "v_pk_mul_f32 v[66:67], v[0:1], v[0:1]\n"
    "v_pk_add_f32 v[16:17], v[4:5], v[6:7]\n"
    "v_pk_fma_f32 v[66:67], v[2:3], v[2:3], v[66:67]\n"
    "v_pk_add_f32 v[64:65], v[64:65], v[16:17]\n"
    "v_pk_fma_f32 v[66:67], v[4:5], v[4:5], v[66:67]\n"
    "v_add_f32 v64, v64, v65\n"
    "v_pk_fma_f32 v[66:67], v[6:7], v[6:7], v[66:67]\n"
    "v_add_f32 v66, v66, v67\n"
    "v_pk_add_f32 v[68:69], v[8:9], v[10:11]\n"
    "v_pk_mul_f32 v[70:71], v[8:9], v[8:9]\n"
    "v_pk_add_f32 v[16:17], v[12:13], v[14:15]\n"
    "v_pk_fma_f32 v[70:71], v[10:11], v[10:11], v[70:71]\n"
    "v_pk_add_f32 v[68:69], v[68:69], v[16:17]\n"
    "v_pk_fma_f32 v[70:71], v[12:13], v[12:13], v[70:71]\n"
    "v_add_f32 v68, v68, v69\n"
    "v_pk_fma_f32 v[70:71], v[14:15], v[14:15], v[70:71]\n"
    "v_add_f32 v70, v70, v71\n"
    "v_permlane16_swap_b32 v64, v66\n"
    "s_nop 0\n"
    "v_permlane16_swap_b32 v68, v70\n"
    "v_add_f32 v64, v64, v66\n"
    "v_add_f32 v68, v68, v70\n"
    "s_nop 1\n"
    "v_permlane32_swap_b32 v64, v68\n"
    "v_add_f32 v64, v64, v68\n"
    "v_min_u32 v64, -2, v64\n"
    "buffer_store_dword v64, v230, s[0:3], s32 offen sc1\n"
    "buffer_store_dwordx4 v[56:59], v228, s[8:11], s30 offen sc1\n"
    "buffer_store_dwordx4 v[60:63], v229, s[8:11], s30 offen sc1\n"
    "s_sleep 6\n"
    ".Lb2_sl_%=:\n"
    "buffer_load_dwordx4 v[64:67], v231, s[0:3], s32 offen sc1\n"
    "buffer_load_dwordx4 v[68:71], v232, s[0:3], s32 offen sc1\n"
    "buffer_load_dwordx4 v[72:75], v233, s[0:3], s32 offen sc1\n"
    "buffer_load_dwordx4 v[76:79], v234, s[0:3], s32 offen sc1\n"
    "buffer_load_dwordx4 v[80:83], v235, s[0:3], s32 offen sc1\n"
    "s_waitcnt vmcnt(4)\n"
    "v_max3_u32 v84, v64, v65, v64\n"
    "v_max3_u32 v84, v66, v67, v84\n"
    "s_waitcnt vmcnt(3)\n"
    "v_max3_u32 v84, v68, v69, v84\n"
    "v_max3_u32 v84, v70, v71, v84\n"
    "v_pk_add_f32 v[64:65], v[64:65], v[68:69]\n"
    "v_pk_add_f32 v[66:67], v[66:67], v[70:71]\n"
    "s_waitcnt vmcnt(2)\n"
    "v_max3_u32 v84, v72, v73, v84\n"
    "v_max3_u32 v84, v74, v75, v84\n"
    "v_pk_add_f32 v[64:65], v[64:65], v[72:73]\n"
    "v_pk_add_f32 v[66:67], v[66:67], v[74:75]\n"
    "s_waitcnt vmcnt(1)\n"
    "v_max3_u32 v84, v76, v77, v84\n"
    "v_max3_u32 v84, v78, v79, v84\n"
    "v_pk_add_f32 v[64:65], v[64:65], v[76:77]\n"
    "v_pk_add_f32 v[66:67], v[66:67], v[78:79]\n"
    "s_waitcnt vmcnt(0)\n"
    "v_max3_u32 v84, v80, v81, v84\n"
    "v_max3_u32 v84, v82, v83, v84\n"
    "v_pk_add_f32 v[64:65], v[64:65], v[80:81]\n"
    "v_pk_add_f32 v[66:67], v[66:67], v[82:83]\n"
    "v_cmp_eq_u32 vcc, -1, v84\n"
    "s_nop 0\n"
    "s_cbranch_vccnz .Lb2_sr_%=\n"
    ".Lb2_sg_%=:\n"
    "v_permlane16_swap_b32 v64, v65\n"
    "v_permlane16_swap_b32 v66, v67\n"
    "v_add_f32 v64, v64, v65\n"
    "v_add_f32 v66, v66, v67\n"
    "s_nop 1\n"
    "v_permlane32_swap_b32 v64, v66\n"
    "v_add_f32 v64, v64, v66\n"
    "v_mov_b32 v66, v64\n"
    "s_nop 1\n"
    "v_permlane16_swap_b32 v64, v66\n"
    "v_mul_f32 v64, 0x3acccccd, v64\n"
    "v_mul_f32 v66, 0x3acccccd, v66\n"
    "v_fma_f32 v66, -v64, v64, v66\n"
    "v_max_f32 v66, 0, v66\n"
    "v_cvt_pk_bf16_f32 v64, v64, v66\n"
    "v_and_b32 v66, 0xffff0000, v64\n"
    "v_lshlrev_b32 v64, 16, v64\n"
    "v_add_f32 v66, 0x3727c5ac, v66\n"
    "v_mov_b32 v68, v64\n"
    "v_cvt_pk_bf16_f32 v66, v66, v66\n"
    "v_lshlrev_b32 v66, 16, v66\n"
    "v_sqrt_f32 v66, v66\n"
    "v_permlane32_swap_b32 v64, v68\n"
    "v_cvt_pk_bf16_f32 v66, v66, v66\n"
    "v_lshlrev_b32 v66, 16, v66\n"
    "v_rcp_f32 v66, v66\n"
    "s_nop 0\n"
    "v_mov_b32 v70, v66\n"
    "s_nop 1\n"
    "v_permlane32_swap_b32 v66, v70\n"
    "v_pk_add_f32 v[0:1], v[0:1], v[64:65] op_sel_hi:[1,0] neg_lo:[0,1] neg_hi:[0,1]\n"
    "v_pk_add_f32 v[2:3], v[2:3], v[64:65] op_sel_hi:[1,0] neg_lo:[0,1] neg_hi:[0,1]\n"
    "v_pk_add_f32 v[4:5], v[4:5], v[64:65] op_sel_hi:[1,0] neg_lo:[0,1] neg_hi:[0,1]\n"
    "v_pk_add_f32 v[6:7], v[6:7], v[64:65] op_sel_hi:[1,0] neg_lo:[0,1] neg_hi:[0,1]\n"
    "v_cvt_pk_bf16_f32 v16, v0, v1\n"
    "v_cvt_pk_bf16_f32 v17, v2, v3\n"
    "v_cvt_pk_bf16_f32 v18, v4, v5\n"
    "v_cvt_pk_bf16_f32 v19, v6, v7\n"
    "v_lshlrev_b32 v0, 16, v16\n"
    "v_and_b32 v1, 0xffff0000, v16\n"
    "v_lshlrev_b32 v2, 16, v17\n"
    "v_and_b32 v3, 0xffff0000, v17\n"
    "v_lshlrev_b32 v4, 16, v18\n"
    "v_and_b32 v5, 0xffff0000, v18\n"
    "v_lshlrev_b32 v6, 16, v19\n"
    "v_and_b32 v7, 0xffff0000, v19\n"
    "v_pk_mul_f32 v[0:1], v[0:1], v[66:67] op_sel_hi:[1,0]\n"
    "v_pk_mul_f32 v[2:3], v[2:3], v[66:67] op_sel_hi:[1,0]\n"
    "v_pk_mul_f32 v[4:5], v[4:5], v[66:67] op_sel_hi:[1,0]\n"
    "v_pk_mul_f32 v[6:7], v[6:7], v[66:67] op_sel_hi:[1,0]\n"
    "v_cvt_pk_bf16_f32 v56, v0, v1\n"
    "v_cvt_pk_bf16_f32 v58, v2, v3\n"
    "v_cvt_pk_bf16_f32 v57, v4, v5\n"
    "v_cvt_pk_bf16_f32 v59, v6, v7\n"
    "buffer_store_dwordx4 v[56:59], v227, s[0:3], s31 offen offset:0 sc1\n"
    "v_pk_add_f32 v[8:9], v[8:9], v[68:69] op_sel_hi:[1,0] neg_lo:[0,1] neg_hi:[0,1]\n"
    "v_pk_add_f32 v[10:11], v[10:11], v[68:69] op_sel_hi:[1,0] neg_lo:[0,1] neg_hi:[0,1]\n"
    "v_pk_add_f32 v[12:13], v[12:13], v[68:69] op_sel_hi:[1,0] neg_lo:[0,1] neg_hi:[0,1]\n"
    "v_pk_add_f32 v[14:15], v[14:15], v[68:69] op_sel_hi:[1,0] neg_lo:[0,1] neg_hi:[0,1]\n"
    "v_cvt_pk_bf16_f32 v16, v8, v9\n"
    "v_cvt_pk_bf16_f32 v17, v10, v11\n"
    "v_cvt_pk_bf16_f32 v18, v12, v13\n"
    "v_cvt_pk_bf16_f32 v19, v14, v15\n"
    "v_lshlrev_b32 v8, 16, v16\n"
    "v_and_b32 v9, 0xffff0000, v16\n"
    "v_lshlrev_b32 v10, 16, v17\n"
    "v_and_b32 v11, 0xffff0000, v17\n"
    "v_lshlrev_b32 v12, 16, v18\n"
    "v_and_b32 v13, 0xffff0000, v18\n"
    "v_lshlrev_b32 v14, 16, v19\n"
    "v_and_b32 v15, 0xffff0000, v19\n"
    "v_pk_mul_f32 v[8:9], v[8:9], v[70:71] op_sel_hi:[1,0]\n"
    "v_pk_mul_f32 v[10:11], v[10:11], v[70:71] op_sel_hi:[1,0]\n"
    "v_pk_mul_f32 v[12:13], v[12:13], v[70:71] op_sel_hi:[1,0]\n"
    "v_pk_mul_f32 v[14:15], v[14:15], v[70:71] op_sel_hi:[1,0]\n"
    "v_cvt_pk_bf16_f32 v60, v8, v9\n"
    "v_cvt_pk_bf16_f32 v62, v10, v11\n"
    "v_cvt_pk_bf16_f32 v61, v12, v13\n"
    "v_cvt_pk_bf16_f32 v63, v14, v15\n"
    "buffer_store_dwordx4 v[60:63], v227, s[0:3], s31 offen offset:1024 sc1\n"
    "v_mov_b32 v84, s27\n"
    "s_waitcnt vmcnt(0)\n"
    "buffer_store_dword v84, v226, s[0:3], 0 offen sc1\n"
    ".Lb2_nx_%=:\n"
    "s_mov_b32 s26, s27\n"
    "s_cmp_lt_u32 s26, 64\n"
    "s_cbranch_scc1 .Lb2_top_%=\n"
    "s_waitcnt vmcnt(0) lgkmcnt(0)\n"
    "s_endpgm\n"
    ".Lb2_out_%=:\n"
    "buffer_store_dwordx4 v[56:59], v228, s[8:11], s30 offen\n"
    "buffer_store_dwordx4 v[60:63], v229, s[8:11], s30 offen\n"
    "s_branch .Lb2_nx_%=\n"
    ".Lb2_sr_%=:\n"
    "s_add_u32 s20, s20, 1\n"
    "s_cmp_lt_u32 s20, 0x40000\n"
    "s_cbranch_scc1 .Lb2_sp9_%=\n"
    "v_mov_b32 v84, 72\n"
    "s_mov_b32 s24, 33728\n"
    "buffer_store_dword v84, off, s[0:3], s24 sc1\n"
    "s_waitcnt vmcnt(0)\n"
    "s_branch .Lb2_sg_%=\n"
    ".Lb2_sp9_%=:\n"
    "s_sleep 2\n"
    "s_branch .Lb2_sl_%=\n"
    :: "{s[0:3]}"(s0_), "{s[4:7]}"(s4_), "{s[8:11]}"(s8_), "{s[12:15]}"(s12_), "{s[16:19]}"(s16_), "{v[0:3]}"(v0_), "{v[4:7]}"(v4_), "{v[224:227]}"(v224_), "{v[228:231]}"(v228_), "{v[232:235]}"(v232_) : "memory", "v255", "a255", "s95");
    __builtin_unreachable();
}
constexpr int MEGA_LDS = 163840;
__global__ void __launch_bounds__(256, 1) mega_kernel(MegaArgs A) {
    extern __shared__ __attribute__((aligned(16))) char smem[];
    int* sRole = (int*)smem;
    const int x4 = (int)(__builtin_amdgcn_s_getreg((3 << 11) | 20) & 3u);
    if (threadIdx.x < 64) {
        const rsrc_t rf = mk_rsrc(arena_base(A.ar), ARENA);
        unsigned tm[2] = {~0u, ~0u};
#pragma unroll
        for (int k = 0; k < 4; ++k) {
            const unsigned long long t0 = __builtin_amdgcn_s_memtime();
            if (threadIdx.x == 0) bst4(rf, (blockIdx.x >> 7) * 16384 + (blockIdx.x & 127) * 64 + (k & 1) * 8192, 0u);
            DRAIN();
            const unsigned d = (unsigned)(__builtin_amdgcn_s_memtime() - t0);
            tm[k & 1] = d < tm[k & 1] ? d : tm[k & 1];
        }
        if (threadIdx.x == 0) {
            unsigned* cnt = (unsigned*)(arena_base(A.ar) + xl(Z_TK, 0));
            int role = -1;
            auto take = [&](int pool) {
                if (role >= 0) return;
                const int k = (int)__hip_atomic_fetch_add(cnt + 16 * pool, 1u, RLX_AGENT), x = pool & 3;
                if (k < 32) role = (pool >> 2) * 128 + (k < 24 ? (k >> 3) * 32 + 8 * x + (k & 7) : 96 + ((k - 24) >> 2) * 16 + 4 * x + (k & 3));
            };
            take((tm[1] < tm[0] ? 4 : 0) + x4);
            for (int p = 0; p < 8; ++p) take(p);
            *sRole = role;
        }
    }
    __syncthreads();
    const int role = RFL(*sRole), L = role >> 7, r = role & 127;
    __syncthreads();
    const bool on = (A.layers >> L) & 1;
    if (r < 32) { if (on) qk_role(A, L, r, x4); }
    else if (r < 64) { if (on) vo_role(A, L, r - 32, x4); }
    else if (r < 96) ct_role(A, L, r - 64);
    else if (r < 112) { if (on) { if (r < 107) b1_role(A, L, r - 96, x4); else b2_role(A, L, r - 107, x4); } }
    else qw_role(A, L, 8 * ((r - 112) >> 2) + 4 * L + (r & 3), x4);
}

#define LAYERS 3
extern "C" void kernel_launch(void* const* d_in, const int* in_sizes, int n_in, void* d_out, int out_size, void* d_ws, size_t ws_size, hipStream_t stream) {
    MegaArgs A;
    A.x0 = (const bf16_t*)d_in[0]; A.mem0 = (const bf16_t*)d_in[1]; A.Wq = (const bf16_t*)d_in[2]; A.Wk = (const bf16_t*)d_in[3]; A.Wv = (const bf16_t*)d_in[4];
    A.Wo = (const bf16_t*)d_in[5]; A.wqkv = (const bf16_t*)d_in[8]; A.wo = (const bf16_t*)d_in[9];
    A.ar = (char*)d_ws + 266240;
    A.out = (bf16_t*)d_out; A.layers = LAYERS; A.pad = 0;
    (void)hipFuncSetAttribute((const void*)mega_kernel, hipFuncAttributeMaxDynamicSharedMemorySize, MEGA_LDS);
    k_zero<<<2 * (Z_END - Z_TK) / 4096, 256, 0, stream>>>(A.ar);
    mega_kernel<<<256, 256, MEGA_LDS, stream>>>(A);
}
```

```cpp
#include <hip/hip_runtime.h>
#include <hip/hip_bf16.h>
#include <stdint.h>

typedef unsigned short bf16_t;
typedef short bf16x8 __attribute__((ext_vector_type(8)));
typedef short bf16x4 __attribute__((ext_vector_type(4)));
typedef float f32x4 __attribute__((ext_vector_type(4)));
typedef unsigned u32x4 __attribute__((ext_vector_type(4)));
typedef unsigned u32x2 __attribute__((ext_vector_type(2)));
typedef unsigned long long u64;
typedef __bf16 bf16v2 __attribute__((ext_vector_type(2)));
typedef float f32v2 __attribute__((ext_vector_type(2)));
#define DEV __device__ __forceinline__
DEV unsigned pk2(float a, float b) { f32v2 f = {a, b}; bf16v2 r = __builtin_convertvector(f, bf16v2); return __builtin_bit_cast(unsigned, r); }
DEV float rb(float a) { return __uint_as_float((pk2(a, 0.f) & 0xffffu) << 16); }
DEV float lo16(unsigned u) { return __uint_as_float(u << 16); }
DEV float hi16(unsigned u) { return __uint_as_float(u & 0xffff0000u); }
constexpr int D_ = 640;
#define RLX_AGENT __ATOMIC_RELAXED, __HIP_MEMORY_SCOPE_AGENT
#define AUX_SC1 16
typedef __amdgpu_buffer_rsrc_t rsrc_t;
DEV rsrc_t mk_rsrc(const void* p, unsigned bytes) { return __builtin_amdgcn_make_buffer_rsrc((void*)p, 0, (int)bytes, 0x00020000); }
DEV void bst16(rsrc_t r, unsigned off, u32x4 v) { __builtin_amdgcn_raw_buffer_store_b128(v, r, off, 0, AUX_SC1); }
DEV void bst8(rsrc_t r, unsigned off, u32x2 v) { __builtin_amdgcn_raw_buffer_store_b64(v, r, off, 0, AUX_SC1); }
DEV void bst4(rsrc_t r, unsigned off, unsigned v) { __builtin_amdgcn_raw_buffer_store_b32(v, r, off, 0, AUX_SC1); }
#define DRAIN() asm volatile("s_waitcnt vmcnt(0)" ::: "memory")
#define DRAIN_BARRIER() asm volatile("s_waitcnt vmcnt(0) lgkmcnt(0)\n\ts_barrier" ::: "memory")
#define RFL(x) ((unsigned)__builtin_amdgcn_readfirstlane((int)(x)))
DEV unsigned lo(const void* p) { return (unsigned)(size_t)p; }
DEV unsigned hi(const void* p) { return (unsigned)((size_t)p >> 32) & 0xffffu; }
DEV unsigned nosent(unsigned x) { return min(x, 0xfffffffeu); }
DEV int sw2(int x) { return ((x & 1) << 1) | (x >> 1); }

DEV unsigned xl(unsigned o, int side) { const unsigned ph = ((o >> 13) << 14) | (o & 8191u); return ph | ((((ph >> 21) ^ side) & 1u) << 13); }
constexpr unsigned Z_TK = 16384, Z_QV = Z_TK + 1024, Z_CT = Z_QV + 1024, Z_QW = Z_CT + 2048, Z_X = 3 * 8192, Z_X1 = 7 * 8192, Z_XN = 9 * 8192, Z_O = 10 * 8192, Z_ST = 11 * 8192, Z_END = 12 * 8192;
constexpr unsigned RG_UT = 1u << 21, RG_UTX = RG_UT + (1u << 20), RG_UM = RG_UTX + 262144, RG_UMX = RG_UM + 262144, RG_PT = RG_UMX + 65536, RG_PM = RG_PT + 65536, RG_MEM = RG_PM + 32768;
constexpr unsigned SLOT = 1u << 22;
constexpr unsigned REC0 = 20u << 20, RC_X1 = 5 * 16384, RC_O = 10 * 16384, RC_X2 = 15 * 16384, ST0 = 1u << 20, QT0 = 52u << 20, DBG0 = 116u << 20, ARENA = 120u << 20;
DEV unsigned rec(int s, int L) { return REC0 + (s << 19) + ((((s >> 2) ^ L) & 1) << 13); }
constexpr unsigned OOB = 0x7f000000u, WBYTES = 640u * 6144u * 2u;
DEV char* arena_base(char* p) { return (char*)(((size_t)p + 0x3fffff) & ~(size_t)0x3fffff); }

struct MegaArgs {
    const bf16_t* x0; const bf16_t* mem0; const bf16_t* Wq; const bf16_t* Wk; const bf16_t* Wv; const bf16_t* Wo; const bf16_t* wqkv; const bf16_t* wo;
    char* ar; bf16_t* out; int layers, pad;
};
__global__ void __launch_bounds__(256) k_zero(char* ar) {
    *(u32x4*)(arena_base(ar) + 2 * Z_TK + (blockIdx.x * 256 + threadIdx.x) * 16) = u32x4{0u, 0u, 0u, 0u};
}
#define ROLE_HEAD const int l = threadIdx.x & 63, l16 = l & 15, lq = l >> 4, w = RFL(threadIdx.x >> 6); char* const ab = arena_base(A.ar); \
    const rsrc_t rr = mk_rsrc(ab, ARENA); const u32x4 rx = {lo(ab), hi(ab), ARENA, 0x20000u}; const u32x4 ones = {~0u, ~0u, ~0u, ~0u}; const u32x2 ones2 = {~0u, ~0u}
DEV void qk_role(const MegaArgs& A, const int L, const int h, const int rep) {
    ROLE_HEAD;
    const int i = 2 * w + (lq & 1), p = l16 < 8 ? l16 >> 2 : l16 & 1, rho = 4 * (i >> 1) + 2 * p + (i & 1), tp = 4 * w + lq;
    const unsigned pm = (l >= 32 || l16 >= 10) ? OOB : xl(RG_PM + (h >> 3) * 8192 +
        (l16 < 8 ? ((h & 7) >> 1) * 1024 + ((2 * (h & 1) + ((l16 >> 1) & 1)) * 16 + rho) * 16 + (l16 & 1) * 8 : 4096 + (((h & 7) >> 1) * 16 + rho) * 16 + (h & 1) * 8), L);
    const unsigned pt = xl(RG_PT + h * 2048 + tp * 16 + (l16 < 8 ? (l16 & 3) * 256 + (l16 >> 2) * 8 : (l16 & 1) * 8 + (l16 < 10 ? 1792 : (l16 < 14 ? 1024 + ((l16 - 10) >> 1) * 256 : 1536))), L);
    u64 cm[4];
    const int k0 = (l16 >= 4 && l16 < 8) ? 4 * (l16 - 4) : ((l16 >= 10 && l16 < 14) ? 4 * (l16 - 10) : -4);
#pragma unroll
    for (int r = 0; r < 4; ++r) cm[r] = __ballot(k0 + r <= tp);
    bst8(rr, pm, ones2); bst8(rr, pt, ones2);
    DRAIN_BARRIER();
    bst4(rr, threadIdx.x < 4 ? xl(Z_QV + l * 256 + h * 4, L) : OOB, 1u);
    const u32x4 s0_ = {RFL(rx[0]), RFL(rx[1]), RFL(rx[2]), RFL(rx[3])};
    const u32x4 s4_ = {RFL(lo(A.Wk + h * 192)), RFL(hi(A.Wk + h * 192)), RFL(WBYTES - h * 384), RFL(0x20000u)};
    const u32x4 s8_ = {RFL(lo(A.Wq + h * 192)), RFL(hi(A.Wq + h * 192)), RFL(WBYTES - h * 384), RFL(0x20000u)};
    const u32x4 s12_ = {RFL((unsigned)cm[0]), RFL((unsigned)(cm[0] >> 32)), RFL((unsigned)cm[1]), RFL((unsigned)(cm[1] >> 32))};
    const u32x4 s16_ = {RFL((unsigned)cm[2]), RFL((unsigned)(cm[2] >> 32)), RFL((unsigned)cm[3]), RFL((unsigned)(cm[3] >> 32))};
    const u32x4 s20_ = {RFL(w), RFL(L << 13), RFL(xl(RG_MEM + rep * 16384, L) + w * 2048), RFL(w < 2 ? xl(RG_MEM + rep * 16384 + 8192, L) + w * 1024 : OOB)};
    const u32x4 s24_ = {RFL(REC0 + w * 2048), RFL(QT0 + h * 32768 + w * 2048), RFL(h == 0 && w == 0 ? xl(Z_X, L) : OOB), RFL(xl(Z_X, L))};
    const u32x4 s28_ = {RFL(xl(Z_CT + (h & 7) * 256, L)), RFL(xl(Z_XN + rep * 2048, 0)), RFL(xl(Z_QW + h * 128, L)), RFL(DBG0 + ((L * 128 + h) * 4 + w) * 2048)};
    const u32x4 v0_ = {(unsigned)(((3 * w + 0) * 64 + l) / 24 * 12288 + ((((3 * w + 0) * 64 + l) % 24) ^ ((((3 * w + 0) * 64 + l) / 48) & 6)) * 16 - 0), (unsigned)(((3 * w + 1) * 64 + l) / 24 * 12288 + ((((3 * w + 1) * 64 + l) % 24) ^ ((((3 * w + 1) * 64 + l) / 48) & 6)) * 16 - 1024), (unsigned)(((3 * w + 2) * 64 + l) / 24 * 12288 + ((((3 * w + 2) * 64 + l) % 24) ^ ((((3 * w + 2) * 64 + l) / 48) & 6)) * 16 - 2048), (unsigned)((8 * lq + ((l >> 2) & 1) + ((l >> 1) & 4)) * 384 + (w ^ (((8 * lq + ((l >> 2) & 1) + ((l >> 1) & 4)) >> 2) & 3)) * 32 + (l & 3) * 8 + ((w + 0) & 3) * 12288)};
    const u32x4 v4_ = {(unsigned)((8 * lq + ((l >> 2) & 1) + ((l >> 1) & 4)) * 384 + (w ^ (((8 * lq + ((l >> 2) & 1) + ((l >> 1) & 4)) >> 2) & 3)) * 32 + (l & 3) * 8 + ((w + 1) & 3) * 12288), (unsigned)((8 * lq + ((l >> 2) & 1) + ((l >> 1) & 4)) * 384 + (w ^ (((8 * lq + ((l >> 2) & 1) + ((l >> 1) & 4)) >> 2) & 3)) * 32 + (l & 3) * 8 + ((w + 2) & 3) * 12288), (unsigned)((8 * lq + ((l >> 2) & 1) + ((l >> 1) & 4)) * 384 + (w ^ (((8 * lq + ((l >> 2) & 1) + ((l >> 1) & 4)) >> 2) & 3)) * 32 + (l & 3) * 8 + ((w + 3) & 3) * 12288), (unsigned)((8 * lq + ((l >> 2) & 1) + ((l >> 1) & 4)) * 384 + (w ^ (((8 * lq + ((l >> 2) & 1) + ((l >> 1) & 4)) >> 2) & 3)) * 32 + (l & 3) * 8)};
    const u32x4 v92_ = {(unsigned)(l * 16 + ((w + 0) & 3) * 11264), (unsigned)(l * 16 + ((w + 1) & 3) * 11264), (unsigned)(l * 16 + ((w + 2) & 3) * 11264), (unsigned)(l * 16 + ((w + 3) & 3) * 11264)};
    const u32x4 v96_ = {(unsigned)(l * 16), (unsigned)(l * 16 + 53248 + w * 20480), (unsigned)((l & 2) ? 0xeeeeu : 0x4444u), (unsigned)(135168 + w * 1808 + l16 * 112 + lq * 4)};
    const u32x4 v100_ = {(unsigned)(135168 + w * 1808 + (l >> 2) * 112 + (l & 3) * 8), (unsigned)(135168 + w * 1808 + l16 * 176 + lq * 16), (unsigned)(135168 + (l16 < 10 ? (2 * w + (lq & 1)) * 176 + l16 * 16 : 1792)), (unsigned)(142400 + w * 4352 + l16 * 272 + lq * 16)};
    const u32x4 v104_ = {(unsigned)(142400 + (4 * w + lq) * 272 + l16 * 16), (unsigned)(pm), (unsigned)(pt), (unsigned)(0xff800000u)};
    asm volatile(
    "s_mov_b32 s32, 0\n"
    "s_cmp_eq_u32 s21, 0\n"
    "s_cbranch_scc1 .Lqk_ho1_%=\n"
    "s_sleep 35\n"
    "s_sleep 35\n"
    "s_sleep 35\n"
    "s_sleep 35\n"
    "s_sleep 35\n"
    "s_sleep 35\n"
    "s_sleep 35\n"
    "s_sleep 35\n"
    "s_sleep 35\n"
    "s_sleep 35\n"
    "s_sleep 35\n"
    "s_sleep 35\n"
    "s_sleep 35\n"
    "s_sleep 35\n"
    ".Lqk_ho1_%=:\n"
    "s_mul_i32 s43, s20, 3072\n"
    "s_mov_b32 s52, 0\n"
    "s_add_u32 m0, s43, 0\n"
    "s_nop 0\n"
    "buffer_load_dwordx4 v0, s[4:7], s52 offen offset:0 nt lds\n"
    "buffer_load_dwordx4 v1, s[4:7], s52 offen offset:1024 nt lds\n"
    "buffer_load_dwordx4 v2, s[4:7], s52 offen offset:2048 nt lds\n"
    "s_mov_b32 s52, 1572864\n"
    "s_add_u32 m0, s43, 12288\n"
    "s_nop 0\n"
    "buffer_load_dwordx4 v0, s[4:7], s52 offen offset:0 nt lds\n"
    "buffer_load_dwordx4 v1, s[4:7], s52 offen offset:1024 nt lds\n"
    "buffer_load_dwordx4 v2, s[4:7], s52 offen offset:2048 nt lds\n"
    "s_mov_b32 s52, 3145728\n"
    "s_add_u32 m0, s43, 24576\n"
    "s_nop 0\n"
    "buffer_load_dwordx4 v0, s[4:7], s52 offen offset:0 nt lds\n"
    "buffer_load_dwordx4 v1, s[4:7], s52 offen offset:1024 nt lds\n"
    "buffer_load_dwordx4 v2, s[4:7], s52 offen offset:2048 nt lds\n"
    "s_mov_b32 s52, 4718592\n"
    "s_add_u32 m0, s43, 36864\n"
    "s_nop 0\n"
    "buffer_load_dwordx4 v0, s[4:7], s52 offen offset:0 nt lds\n"
    "buffer_load_dwordx4 v1, s[4:7], s52 offen offset:1024 nt lds\n"
    "buffer_load_dwordx4 v2, s[4:7], s52 offen offset:2048 nt lds\n"
    "s_mov_b32 s52, 393216\n"
    "s_add_u32 m0, s43, 49152\n"
    "s_nop 0\n"
    "buffer_load_dwordx4 v0, s[4:7], s52 offen offset:0 nt lds\n"
    "buffer_load_dwordx4 v1, s[4:7], s52 offen offset:1024 nt lds\n"
    "buffer_load_dwordx4 v2, s[4:7], s52 offen offset:2048 nt lds\n"
    "s_mov_b32 s52, 1966080\n"
    "s_add_u32 m0, s43, 61440\n"
    "s_nop 0\n"
    "buffer_load_dwordx4 v0, s[4:7], s52 offen offset:0 nt lds\n"
    "buffer_load_dwordx4 v1, s[4:7], s52 offen offset:1024 nt lds\n"
    "buffer_load_dwordx4 v2, s[4:7], s52 offen offset:2048 nt lds\n"
    "s_mov_b32 s52, 3538944\n"
    "s_add_u32 m0, s43, 73728\n"
    "s_nop 0\n"
    "buffer_load_dwordx4 v0, s[4:7], s52 offen offset:0 nt lds\n"
    "buffer_load_dwordx4 v1, s[4:7], s52 offen offset:1024 nt lds\n"
    "buffer_load_dwordx4 v2, s[4:7], s52 offen offset:2048 nt lds\n"
    "s_mov_b32 s52, 5111808\n"
    "s_add_u32 m0, s43, 86016\n"
    "s_nop 0\n"
    "buffer_load_dwordx4 v0, s[4:7], s52 offen offset:0 nt lds\n"
    "buffer_load_dwordx4 v1, s[4:7], s52 offen offset:1024 nt lds\n"
    "buffer_load_dwordx4 v2, s[4:7], s52 offen offset:2048 nt lds\n"
    "s_mov_b32 s52, 786432\n"
    "s_add_u32 m0, s43, 98304\n"
    "s_nop 0\n"
    "buffer_load_dwordx4 v0, s[4:7], s52 offen offset:0 nt lds\n"
    "buffer_load_dwordx4 v1, s[4:7], s52 offen offset:1024 nt lds\n"
    "buffer_load_dwordx4 v2, s[4:7], s52 offen offset:2048 nt lds\n"
    "s_mov_b32 s52, 2359296\n"
    "s_add_u32 m0, s43, 110592\n"
    "s_nop 0\n"
    "buffer_load_dwordx4 v0, s[4:7], s52 offen offset:0 nt lds\n"
    "buffer_load_dwordx4 v1, s[4:7], s52 offen offset:1024 nt lds\n"
    "buffer_load_dwordx4 v2, s[4:7], s52 offen offset:2048 nt lds\n"
    "s_mov_b32 s52, 3932160\n"
    "s_add_u32 m0, s43, 122880\n"
    "s_nop 0\n"
    "buffer_load_dwordx4 v0, s[4:7], s52 offen offset:0 nt lds\n"
    "buffer_load_dwordx4 v1, s[4:7], s52 offen offset:1024 nt lds\n"
    "buffer_load_dwordx4 v2, s[4:7], s52 offen offset:2048 nt lds\n"
    "s_mov_b32 s52, 5505024\n"
    "s_add_u32 m0, s43, 135168\n"
    "s_nop 0\n"
    "buffer_load_dwordx4 v0, s[4:7], s52 offen offset:0 nt lds\n"
    "buffer_load_dwordx4 v1, s[4:7], s52 offen offset:1024 nt lds\n"
    "buffer_load_dwordx4 v2, s[4:7], s52 offen offset:2048 nt lds\n"
    "s_waitcnt vmcnt(24)\n"
    "s_barrier\n"
    "v_add_u32 v8, 0, v3\n"
    "ds_read_b64_tr_b16 a[0:1], v8 offset:0\n"
    "ds_read_b64_tr_b16 a[2:3], v8 offset:768\n"
    "ds_read_b64_tr_b16 a[80:81], v8 offset:128\n"
    "ds_read_b64_tr_b16 a[82:83], v8 offset:896\n"
    "ds_read_b64_tr_b16 a[160:161], v8 offset:256\n"
    "ds_read_b64_tr_b16 a[162:163], v8 offset:1024\n"
    "v_add_u32 v9, 0, v4\n"
    "ds_read_b64_tr_b16 a[16:17], v9 offset:0\n"
    "ds_read_b64_tr_b16 a[18:19], v9 offset:768\n"
    "ds_read_b64_tr_b16 a[96:97], v9 offset:128\n"
    "ds_read_b64_tr_b16 a[98:99], v9 offset:896\n"
    "ds_read_b64_tr_b16 a[176:177], v9 offset:256\n"
    "ds_read_b64_tr_b16 a[178:179], v9 offset:1024\n"
    "v_add_u32 v10, 0, v5\n"
    "ds_read_b64_tr_b16 a[32:33], v10 offset:0\n"
    "ds_read_b64_tr_b16 a[34:35], v10 offset:768\n"
    "ds_read_b64_tr_b16 a[112:113], v10 offset:128\n"
    "ds_read_b64_tr_b16 a[114:115], v10 offset:896\n"
    "ds_read_b64_tr_b16 a[192:193], v10 offset:256\n"
    "ds_read_b64_tr_b16 a[194:195], v10 offset:1024\n"
    "v_add_u32 v11, 0, v6\n"
    "ds_read_b64_tr_b16 a[48:49], v11 offset:0\n"
    "ds_read_b64_tr_b16 a[50:51], v11 offset:768\n"
    "ds_read_b64_tr_b16 a[128:129], v11 offset:128\n"
    "ds_read_b64_tr_b16 a[130:131], v11 offset:896\n"
    "ds_read_b64_tr_b16 a[208:209], v11 offset:256\n"
    "ds_read_b64_tr_b16 a[210:211], v11 offset:1024\n"
    "s_waitcnt lgkmcnt(0)\n"
    "s_barrier\n"
    "s_mov_b32 s52, 1179648\n"
    "s_add_u32 m0, s43, 0\n"
    "s_nop 0\n"
    "buffer_load_dwordx4 v0, s[4:7], s52 offen offset:0 nt lds\n"
    "buffer_load_dwordx4 v1, s[4:7], s52 offen offset:1024 nt lds\n"
    "buffer_load_dwordx4 v2, s[4:7], s52 offen offset:2048 nt lds\n"
    "s_mov_b32 s52, 2752512\n"
    "s_add_u32 m0, s43, 12288\n"
    "s_nop 0\n"
    "buffer_load_dwordx4 v0, s[4:7], s52 offen offset:0 nt lds\n"
    "buffer_load_dwordx4 v1, s[4:7], s52 offen offset:1024 nt lds\n"
    "buffer_load_dwordx4 v2, s[4:7], s52 offen offset:2048 nt lds\n"
    "s_mov_b32 s52, 4325376\n"
    "s_add_u32 m0, s43, 24576\n"
    "s_nop 0\n"
    "buffer_load_dwordx4 v0, s[4:7], s52 offen offset:0 nt lds\n"
    "buffer_load_dwordx4 v1, s[4:7], s52 offen offset:1024 nt lds\n"
    "buffer_load_dwordx4 v2, s[4:7], s52 offen offset:2048 nt lds\n"
    "s_mov_b32 s52, 5898240\n"
    "s_add_u32 m0, s43, 36864\n"
    "s_nop 0\n"
    "buffer_load_dwordx4 v0, s[4:7], s52 offen offset:0 nt lds\n"
    "buffer_load_dwordx4 v1, s[4:7], s52 offen offset:1024 nt lds\n"
    "buffer_load_dwordx4 v2, s[4:7], s52 offen offset:2048 nt lds\n"
    "s_waitcnt vmcnt(24)\n"
    "s_barrier\n"
    "v_add_u32 v8, 49152, v3\n"
    "ds_read_b64_tr_b16 a[4:5], v8 offset:0\n"
    "ds_read_b64_tr_b16 a[6:7], v8 offset:768\n"
    "ds_read_b64_tr_b16 a[84:85], v8 offset:128\n"
    "ds_read_b64_tr_b16 a[86:87], v8 offset:896\n"
    "ds_read_b64_tr_b16 a[164:165], v8 offset:256\n"
    "ds_read_b64_tr_b16 a[166:167], v8 offset:1024\n"
    "v_add_u32 v9, 49152, v4\n"
    "ds_read_b64_tr_b16 a[20:21], v9 offset:0\n"
    "ds_read_b64_tr_b16 a[22:23], v9 offset:768\n"
    "ds_read_b64_tr_b16 a[100:101], v9 offset:128\n"
    "ds_read_b64_tr_b16 a[102:103], v9 offset:896\n"
    "ds_read_b64_tr_b16 a[180:181], v9 offset:256\n"
    "ds_read_b64_tr_b16 a[182:183], v9 offset:1024\n"
    "v_add_u32 v10, 49152, v5\n"
    "ds_read_b64_tr_b16 a[36:37], v10 offset:0\n"
    "ds_read_b64_tr_b16 a[38:39], v10 offset:768\n"
    "ds_read_b64_tr_b16 a[116:117], v10 offset:128\n"
    "ds_read_b64_tr_b16 a[118:119], v10 offset:896\n"
    "ds_read_b64_tr_b16 a[196:197], v10 offset:256\n"
    "ds_read_b64_tr_b16 a[198:199], v10 offset:1024\n"
    "v_add_u32 v11, 49152, v6\n"
    "ds_read_b64_tr_b16 a[52:53], v11 offset:0\n"
    "ds_read_b64_tr_b16 a[54:55], v11 offset:768\n"
    "ds_read_b64_tr_b16 a[132:133], v11 offset:128\n"
    "ds_read_b64_tr_b16 a[134:135], v11 offset:896\n"
    "ds_read_b64_tr_b16 a[212:213], v11 offset:256\n"
    "ds_read_b64_tr_b16 a[214:215], v11 offset:1024\n"
    "s_waitcnt lgkmcnt(0)\n"
    "s_barrier\n"
    "s_mov_b32 s52, 6291456\n"
    "s_add_u32 m0, s43, 49152\n"
    "s_nop 0\n"
    "buffer_load_dwordx4 v0, s[4:7], s52 offen offset:0 nt lds\n"
    "buffer_load_dwordx4 v1, s[4:7], s52 offen offset:1024 nt lds\n"
    "buffer_load_dwordx4 v2, s[4:7], s52 offen offset:2048 nt lds\n"
    "s_mov_b32 s52, 6684672\n"
    "s_add_u32 m0, s43, 61440\n"
    "s_nop 0\n"
    "buffer_load_dwordx4 v0, s[4:7], s52 offen offset:0 nt lds\n"
    "buffer_load_dwordx4 v1, s[4:7], s52 offen offset:1024 nt lds\n"
    "buffer_load_dwordx4 v2, s[4:7], s52 offen offset:2048 nt lds\n"
    "s_mov_b32 s52, 7077888\n"
    "s_add_u32 m0, s43, 73728\n"
    "s_nop 0\n"
    "buffer_load_dwordx4 v0, s[4:7], s52 offen offset:0 nt lds\n"
    "buffer_load_dwordx4 v1, s[4:7], s52 offen offset:1024 nt lds\n"
    "buffer_load_dwordx4 v2, s[4:7], s52 offen offset:2048 nt lds\n"
    "s_mov_b32 s52, 7471104\n"
    "s_add_u32 m0, s43, 86016\n"
    "s_nop 0\n"
    "buffer_load_dwordx4 v0, s[4:7], s52 offen offset:0 nt lds\n"
    "buffer_load_dwordx4 v1, s[4:7], s52 offen offset:1024 nt lds\n"
    "buffer_load_dwordx4 v2, s[4:7], s52 offen offset:2048 nt lds\n"
    "s_waitcnt vmcnt(24)\n"
    "s_barrier\n"
    "v_add_u32 v8, 98304, v3\n"
    "ds_read_b64_tr_b16 a[8:9], v8 offset:0\n"
    "ds_read_b64_tr_b16 a[10:11], v8 offset:768\n"
    "ds_read_b64_tr_b16 a[88:89], v8 offset:128\n"
    "ds_read_b64_tr_b16 a[90:91], v8 offset:896\n"
    "ds_read_b64_tr_b16 a[168:169], v8 offset:256\n"
    "ds_read_b64_tr_b16 a[170:171], v8 offset:1024\n"
    "v_add_u32 v9, 98304, v4\n"
    "ds_read_b64_tr_b16 a[24:25], v9 offset:0\n"
    "ds_read_b64_tr_b16 a[26:27], v9 offset:768\n"
    "ds_read_b64_tr_b16 a[104:105], v9 offset:128\n"
    "ds_read_b64_tr_b16 a[106:107], v9 offset:896\n"
    "ds_read_b64_tr_b16 a[184:185], v9 offset:256\n"
    "ds_read_b64_tr_b16 a[186:187], v9 offset:1024\n"
    "v_add_u32 v10, 98304, v5\n"
    "ds_read_b64_tr_b16 a[40:41], v10 offset:0\n"
    "ds_read_b64_tr_b16 a[42:43], v10 offset:768\n"
    "ds_read_b64_tr_b16 a[120:121], v10 offset:128\n"
    "ds_read_b64_tr_b16 a[122:123], v10 offset:896\n"
    "ds_read_b64_tr_b16 a[200:201], v10 offset:256\n"
    "ds_read_b64_tr_b16 a[202:203], v10 offset:1024\n"
    "v_add_u32 v11, 98304, v6\n"
    "ds_read_b64_tr_b16 a[56:57], v11 offset:0\n"
    "ds_read_b64_tr_b16 a[58:59], v11 offset:768\n"
    "ds_read_b64_tr_b16 a[136:137], v11 offset:128\n"
    "ds_read_b64_tr_b16 a[138:139], v11 offset:896\n"
    "ds_read_b64_tr_b16 a[216:217], v11 offset:256\n"
    "ds_read_b64_tr_b16 a[218:219], v11 offset:1024\n"
    "s_waitcnt lgkmcnt(0)\n"
    "s_barrier\n"
    "s_mov_b32 s52, 0\n"
    "s_add_u32 m0, s43, 98304\n"
    "s_nop 0\n"
    "buffer_load_dwordx4 v0, s[8:11], s52 offen offset:0 nt lds\n"
    "buffer_load_dwordx4 v1, s[8:11], s52 offen offset:1024 nt lds\n"
    "buffer_load_dwordx4 v2, s[8:11], s52 offen offset:2048 nt lds\n"
    "s_mov_b32 s52, 1572864\n"
    "s_add_u32 m0, s43, 110592\n"
    "s_nop 0\n"
    "buffer_load_dwordx4 v0, s[8:11], s52 offen offset:0 nt lds\n"
    "buffer_load_dwordx4 v1, s[8:11], s52 offen offset:1024 nt lds\n"
    "buffer_load_dwordx4 v2, s[8:11], s52 offen offset:2048 nt lds\n"
    "s_mov_b32 s52, 3145728\n"
    "s_add_u32 m0, s43, 122880\n"
    "s_nop 0\n"
    "buffer_load_dwordx4 v0, s[8:11], s52 offen offset:0 nt lds\n"
    "buffer_load_dwordx4 v1, s[8:11], s52 offen offset:1024 nt lds\n"
    "buffer_load_dwordx4 v2, s[8:11], s52 offen offset:2048 nt lds\n"
    "s_mov_b32 s52, 4718592\n"
    "s_add_u32 m0, s43, 135168\n"
    "s_nop 0\n"
    "buffer_load_dwordx4 v0, s[8:11], s52 offen offset:0 nt lds\n"
    "buffer_load_dwordx4 v1, s[8:11], s52 offen offset:1024 nt lds\n"
    "buffer_load_dwordx4 v2, s[8:11], s52 offen offset:2048 nt lds\n"
    "s_waitcnt vmcnt(24)\n"
    "s_barrier\n"
    "v_add_u32 v8, 0, v3\n"
    "ds_read_b64_tr_b16 a[12:13], v8 offset:0\n"
    "ds_read_b64_tr_b16 a[14:15], v8 offset:768\n"
    "ds_read_b64_tr_b16 a[92:93], v8 offset:128\n"
    "ds_read_b64_tr_b16 a[94:95], v8 offset:896\n"
    "ds_read_b64_tr_b16 a[172:173], v8 offset:256\n"
    "ds_read_b64_tr_b16 a[174:175], v8 offset:1024\n"
    "v_add_u32 v9, 0, v4\n"
    "ds_read_b64_tr_b16 a[28:29], v9 offset:0\n"
    "ds_read_b64_tr_b16 a[30:31], v9 offset:768\n"
    "ds_read_b64_tr_b16 a[108:109], v9 offset:128\n"
    "ds_read_b64_tr_b16 a[110:111], v9 offset:896\n"
    "ds_read_b64_tr_b16 a[188:189], v9 offset:256\n"
    "ds_read_b64_tr_b16 a[190:191], v9 offset:1024\n"
    "v_add_u32 v10, 0, v5\n"
    "ds_read_b64_tr_b16 a[44:45], v10 offset:0\n"
    "ds_read_b64_tr_b16 a[46:47], v10 offset:768\n"
    "ds_read_b64_tr_b16 a[124:125], v10 offset:128\n"
    "ds_read_b64_tr_b16 a[126:127], v10 offset:896\n"
    "ds_read_b64_tr_b16 a[204:205], v10 offset:256\n"
    "ds_read_b64_tr_b16 a[206:207], v10 offset:1024\n"
    "v_add_u32 v11, 0, v6\n"
    "ds_read_b64_tr_b16 a[60:61], v11 offset:0\n"
    "ds_read_b64_tr_b16 a[62:63], v11 offset:768\n"
    "ds_read_b64_tr_b16 a[140:141], v11 offset:128\n"
    "ds_read_b64_tr_b16 a[142:143], v11 offset:896\n"
    "ds_read_b64_tr_b16 a[220:221], v11 offset:256\n"
    "ds_read_b64_tr_b16 a[222:223], v11 offset:1024\n"
    "s_waitcnt lgkmcnt(0)\n"
    "s_barrier\n"
    "s_mov_b32 s52, 393216\n"
    "s_add_u32 m0, s43, 0\n"
    "s_nop 0\n"
    "buffer_load_dwordx4 v0, s[8:11], s52 offen offset:0 nt lds\n"
    "buffer_load_dwordx4 v1, s[8:11], s52 offen offset:1024 nt lds\n"
    "buffer_load_dwordx4 v2, s[8:11], s52 offen offset:2048 nt lds\n"
    "s_mov_b32 s52, 1966080\n"
    "s_add_u32 m0, s43, 12288\n"
    "s_nop 0\n"
    "buffer_load_dwordx4 v0, s[8:11], s52 offen offset:0 nt lds\n"
    "buffer_load_dwordx4 v1, s[8:11], s52 offen offset:1024 nt lds\n"
    "buffer_load_dwordx4 v2, s[8:11], s52 offen offset:2048 nt lds\n"
    "s_mov_b32 s52, 3538944\n"
    "s_add_u32 m0, s43, 24576\n"
    "s_nop 0\n"
    "buffer_load_dwordx4 v0, s[8:11], s52 offen offset:0 nt lds\n"
    "buffer_load_dwordx4 v1, s[8:11], s52 offen offset:1024 nt lds\n"
    "buffer_load_dwordx4 v2, s[8:11], s52 offen offset:2048 nt lds\n"
    "s_mov_b32 s52, 5111808\n"
    "s_add_u32 m0, s43, 36864\n"
    "s_nop 0\n"
    "buffer_load_dwordx4 v0, s[8:11], s52 offen offset:0 nt lds\n"
    "buffer_load_dwordx4 v1, s[8:11], s52 offen offset:1024 nt lds\n"
    "buffer_load_dwordx4 v2, s[8:11], s52 offen offset:2048 nt lds\n"
    "s_waitcnt vmcnt(24)\n"
    "s_barrier\n"
    "v_add_u32 v8, 49152, v7\n"
    "ds_read_b64_tr_b16 a[64:65], v8 offset:0\n"
    "ds_read_b64_tr_b16 a[66:67], v8 offset:768\n"
    "ds_read_b64_tr_b16 a[144:145], v8 offset:128\n"
    "ds_read_b64_tr_b16 a[146:147], v8 offset:896\n"
    "ds_read_b64_tr_b16 a[224:225], v8 offset:256\n"
    "ds_read_b64_tr_b16 a[226:227], v8 offset:1024\n"
    "v_add_u32 v9, 61440, v7\n"
    "ds_read_b64_tr_b16 a[68:69], v9 offset:0\n"
    "ds_read_b64_tr_b16 a[70:71], v9 offset:768\n"
    "ds_read_b64_tr_b16 a[148:149], v9 offset:128\n"
    "ds_read_b64_tr_b16 a[150:151], v9 offset:896\n"
    "ds_read_b64_tr_b16 a[228:229], v9 offset:256\n"
    "ds_read_b64_tr_b16 a[230:231], v9 offset:1024\n"
    "v_add_u32 v10, 73728, v7\n"
    "ds_read_b64_tr_b16 a[72:73], v10 offset:0\n"
    "ds_read_b64_tr_b16 a[74:75], v10 offset:768\n"
    "ds_read_b64_tr_b16 a[152:153], v10 offset:128\n"
    "ds_read_b64_tr_b16 a[154:155], v10 offset:896\n"
    "ds_read_b64_tr_b16 a[232:233], v10 offset:256\n"
    "ds_read_b64_tr_b16 a[234:235], v10 offset:1024\n"
    "v_add_u32 v11, 86016, v7\n"
    "ds_read_b64_tr_b16 a[76:77], v11 offset:0\n"
    "ds_read_b64_tr_b16 a[78:79], v11 offset:768\n"
    "ds_read_b64_tr_b16 a[156:157], v11 offset:128\n"
    "ds_read_b64_tr_b16 a[158:159], v11 offset:896\n"
    "ds_read_b64_tr_b16 a[236:237], v11 offset:256\n"
    "ds_read_b64_tr_b16 a[238:239], v11 offset:1024\n"
    "s_waitcnt lgkmcnt(0)\n"
    "s_barrier\n"
    "s_mov_b32 s52, 786432\n"
    "s_add_u32 m0, s43, 49152\n"
    "s_nop 0\n"
    "buffer_load_dwordx4 v0, s[8:11], s52 offen offset:0 nt lds\n"
    "buffer_load_dwordx4 v1, s[8:11], s52 offen offset:1024 nt lds\n"
    "buffer_load_dwordx4 v2, s[8:11], s52 offen offset:2048 nt lds\n"
    "s_mov_b32 s52, 2359296\n"
    "s_add_u32 m0, s43, 61440\n"
    "s_nop 0\n"
    "buffer_load_dwordx4 v0, s[8:11], s52 offen offset:0 nt lds\n"
    "buffer_load_dwordx4 v1, s[8:11], s52 offen offset:1024 nt lds\n"
    "buffer_load_dwordx4 v2, s[8:11], s52 offen offset:2048 nt lds\n"
    "s_mov_b32 s52, 3932160\n"
    "s_add_u32 m0, s43, 73728\n"
    "s_nop 0\n"
    "buffer_load_dwordx4 v0, s[8:11], s52 offen offset:0 nt lds\n"
    "buffer_load_dwordx4 v1, s[8:11], s52 offen offset:1024 nt lds\n"
    "buffer_load_dwordx4 v2, s[8:11], s52 offen offset:2048 nt lds\n"
    "s_mov_b32 s52, 5505024\n"
    "s_add_u32 m0, s43, 86016\n"
    "s_nop 0\n"
    "buffer_load_dwordx4 v0, s[8:11], s52 offen offset:0 nt lds\n"
    "buffer_load_dwordx4 v1, s[8:11], s52 offen offset:1024 nt lds\n"
    "buffer_load_dwordx4 v2, s[8:11], s52 offen offset:2048 nt lds\n"
    "s_waitcnt vmcnt(24)\n"
    "s_barrier\n"
    "v_add_u32 v8, 98304, v3\n"
    "ds_read_b64_tr_b16 a[240:241], v8 offset:0\n"
    "ds_read_b64_tr_b16 a[242:243], v8 offset:768\n"
    "ds_read_b64_tr_b16 v[176:177], v8 offset:128\n"
    "ds_read_b64_tr_b16 v[178:179], v8 offset:896\n"
    "ds_read_b64_tr_b16 v[12:13], v8 offset:256\n"
    "ds_read_b64_tr_b16 v[14:15], v8 offset:1024\n"
    "v_add_u32 v9, 98304, v4\n"
    "ds_read_b64_tr_b16 v[112:113], v9 offset:0\n"
    "ds_read_b64_tr_b16 v[114:115], v9 offset:768\n"
    "ds_read_b64_tr_b16 v[192:193], v9 offset:128\n"
    "ds_read_b64_tr_b16 v[194:195], v9 offset:896\n"
    "ds_read_b64_tr_b16 v[28:29], v9 offset:256\n"
    "ds_read_b64_tr_b16 v[30:31], v9 offset:1024\n"
    "v_add_u32 v10, 98304, v5\n"
    "ds_read_b64_tr_b16 v[128:129], v10 offset:0\n"
    "ds_read_b64_tr_b16 v[130:131], v10 offset:768\n"
    "ds_read_b64_tr_b16 v[208:209], v10 offset:128\n"
    "ds_read_b64_tr_b16 v[210:211], v10 offset:896\n"
    "ds_read_b64_tr_b16 v[44:45], v10 offset:256\n"
    "ds_read_b64_tr_b16 v[46:47], v10 offset:1024\n"
    "v_add_u32 v11, 98304, v6\n"
    "ds_read_b64_tr_b16 v[144:145], v11 offset:0\n"
    "ds_read_b64_tr_b16 v[146:147], v11 offset:768\n"
    "ds_read_b64_tr_b16 v[224:225], v11 offset:128\n"
    "ds_read_b64_tr_b16 v[226:227], v11 offset:896\n"
    "ds_read_b64_tr_b16 v[60:61], v11 offset:256\n"
    "ds_read_b64_tr_b16 v[62:63], v11 offset:1024\n"
    "s_waitcnt lgkmcnt(0)\n"
    "s_barrier\n"
    "s_mov_b32 s52, 1179648\n"
    "s_add_u32 m0, s43, 98304\n"
    "s_nop 0\n"
    "buffer_load_dwordx4 v0, s[8:11], s52 offen offset:0 nt lds\n"
    "buffer_load_dwordx4 v1, s[8:11], s52 offen offset:1024 nt lds\n"
    "buffer_load_dwordx4 v2, s[8:11], s52 offen offset:2048 nt lds\n"
    "s_mov_b32 s52, 2752512\n"
    "s_add_u32 m0, s43, 110592\n"
    "s_nop 0\n"
    "buffer_load_dwordx4 v0, s[8:11], s52 offen offset:0 nt lds\n"
    "buffer_load_dwordx4 v1, s[8:11], s52 offen offset:1024 nt lds\n"
    "buffer_load_dwordx4 v2, s[8:11], s52 offen offset:2048 nt lds\n"
    "s_mov_b32 s52, 4325376\n"
    "s_add_u32 m0, s43, 122880\n"
    "s_nop 0\n"
    "buffer_load_dwordx4 v0, s[8:11], s52 offen offset:0 nt lds\n"
    "buffer_load_dwordx4 v1, s[8:11], s52 offen offset:1024 nt lds\n"
    "buffer_load_dwordx4 v2, s[8:11], s52 offen offset:2048 nt lds\n"
    "s_mov_b32 s52, 5898240\n"
    "s_add_u32 m0, s43, 135168\n"
    "s_nop 0\n"
    "buffer_load_dwordx4 v0, s[8:11], s52 offen offset:0 nt lds\n"
    "buffer_load_dwordx4 v1, s[8:11], s52 offen offset:1024 nt lds\n"
    "buffer_load_dwordx4 v2, s[8:11], s52 offen offset:2048 nt lds\n"
    "s_waitcnt vmcnt(24)\n"
    "s_barrier\n"
    "v_add_u32 v8, 0, v3\n"
    "ds_read_b64_tr_b16 a[244:245], v8 offset:0\n"
    "ds_read_b64_tr_b16 a[246:247], v8 offset:768\n"
    "ds_read_b64_tr_b16 v[180:181], v8 offset:128\n"
    "ds_read_b64_tr_b16 v[182:183], v8 offset:896\n"
    "ds_read_b64_tr_b16 v[16:17], v8 offset:256\n"
    "ds_read_b64_tr_b16 v[18:19], v8 offset:1024\n"
    "v_add_u32 v9, 0, v4\n"
    "ds_read_b64_tr_b16 v[116:117], v9 offset:0\n"
    "ds_read_b64_tr_b16 v[118:119], v9 offset:768\n"
    "ds_read_b64_tr_b16 v[196:197], v9 offset:128\n"
    "ds_read_b64_tr_b16 v[198:199], v9 offset:896\n"
    "ds_read_b64_tr_b16 v[32:33], v9 offset:256\n"
    "ds_read_b64_tr_b16 v[34:35], v9 offset:1024\n"
    "v_add_u32 v10, 0, v5\n"
    "ds_read_b64_tr_b16 v[132:133], v10 offset:0\n"
    "ds_read_b64_tr_b16 v[134:135], v10 offset:768\n"
    "ds_read_b64_tr_b16 v[212:213], v10 offset:128\n"
    "ds_read_b64_tr_b16 v[214:215], v10 offset:896\n"
    "ds_read_b64_tr_b16 v[48:49], v10 offset:256\n"
    "ds_read_b64_tr_b16 v[50:51], v10 offset:1024\n"
    "v_add_u32 v11, 0, v6\n"
    "ds_read_b64_tr_b16 v[148:149], v11 offset:0\n"
    "ds_read_b64_tr_b16 v[150:151], v11 offset:768\n"
    "ds_read_b64_tr_b16 v[228:229], v11 offset:128\n"
    "ds_read_b64_tr_b16 v[230:231], v11 offset:896\n"
    "ds_read_b64_tr_b16 v[64:65], v11 offset:256\n"
    "ds_read_b64_tr_b16 v[66:67], v11 offset:1024\n"
    "s_waitcnt lgkmcnt(0)\n"
    "s_barrier\n"
    "s_mov_b32 s52, 6291456\n"
    "s_add_u32 m0, s43, 0\n"
    "s_nop 0\n"
    "buffer_load_dwordx4 v0, s[8:11], s52 offen offset:0 nt lds\n"
    "buffer_load_dwordx4 v1, s[8:11], s52 offen offset:1024 nt lds\n"
    "buffer_load_dwordx4 v2, s[8:11], s52 offen offset:2048 nt lds\n"
    "s_mov_b32 s52, 6684672\n"
    "s_add_u32 m0, s43, 12288\n"
    "s_nop 0\n"
    "buffer_load_dwordx4 v0, s[8:11], s52 offen offset:0 nt lds\n"
    "buffer_load_dwordx4 v1, s[8:11], s52 offen offset:1024 nt lds\n"
    "buffer_load_dwordx4 v2, s[8:11], s52 offen offset:2048 nt lds\n"
    "s_mov_b32 s52, 7077888\n"
    "s_add_u32 m0, s43, 24576\n"
    "s_nop 0\n"
    "buffer_load_dwordx4 v0, s[8:11], s52 offen offset:0 nt lds\n"
    "buffer_load_dwordx4 v1, s[8:11], s52 offen offset:1024 nt lds\n"
    "buffer_load_dwordx4 v2, s[8:11], s52 offen offset:2048 nt lds\n"
    "s_mov_b32 s52, 7471104\n"
    "s_add_u32 m0, s43, 36864\n"
    "s_nop 0\n"
    "buffer_load_dwordx4 v0, s[8:11], s52 offen offset:0 nt lds\n"
    "buffer_load_dwordx4 v1, s[8:11], s52 offen offset:1024 nt lds\n"
    "buffer_load_dwordx4 v2, s[8:11], s52 offen offset:2048 nt lds\n"
    "s_waitcnt vmcnt(24)\n"
    "s_barrier\n"
    "v_add_u32 v8, 49152, v3\n"
    "ds_read_b64_tr_b16 a[248:249], v8 offset:0\n"
    "ds_read_b64_tr_b16 a[250:251], v8 offset:768\n"
    "ds_read_b64_tr_b16 v[184:185], v8 offset:128\n"
    "ds_read_b64_tr_b16 v[186:187], v8 offset:896\n"
    "ds_read_b64_tr_b16 v[20:21], v8 offset:256\n"
    "ds_read_b64_tr_b16 v[22:23], v8 offset:1024\n"
    "v_add_u32 v9, 49152, v4\n"
    "ds_read_b64_tr_b16 v[120:121], v9 offset:0\n"
    "ds_read_b64_tr_b16 v[122:123], v9 offset:768\n"
    "ds_read_b64_tr_b16 v[200:201], v9 offset:128\n"
    "ds_read_b64_tr_b16 v[202:203], v9 offset:896\n"
    "ds_read_b64_tr_b16 v[36:37], v9 offset:256\n"
    "ds_read_b64_tr_b16 v[38:39], v9 offset:1024\n"
    "v_add_u32 v10, 49152, v5\n"
    "ds_read_b64_tr_b16 v[136:137], v10 offset:0\n"
    "ds_read_b64_tr_b16 v[138:139], v10 offset:768\n"
    "ds_read_b64_tr_b16 v[216:217], v10 offset:128\n"
    "ds_read_b64_tr_b16 v[218:219], v10 offset:896\n"
    "ds_read_b64_tr_b16 v[52:53], v10 offset:256\n"
    "ds_read_b64_tr_b16 v[54:55], v10 offset:1024\n"
    "v_add_u32 v11, 49152, v6\n"
    "ds_read_b64_tr_b16 v[152:153], v11 offset:0\n"
    "ds_read_b64_tr_b16 v[154:155], v11 offset:768\n"
    "ds_read_b64_tr_b16 v[232:233], v11 offset:128\n"
    "ds_read_b64_tr_b16 v[234:235], v11 offset:896\n"
    "ds_read_b64_tr_b16 v[68:69], v11 offset:256\n"
    "ds_read_b64_tr_b16 v[70:71], v11 offset:1024\n"
    "s_waitcnt lgkmcnt(0)\n"
    "s_barrier\n"
    "s_waitcnt vmcnt(12)\n"
    "s_barrier\n"
    "v_add_u32 v8, 98304, v3\n"
    "ds_read_b64_tr_b16 a[252:253], v8 offset:0\n"
    "ds_read_b64_tr_b16 a[254:255], v8 offset:768\n"
    "ds_read_b64_tr_b16 v[188:189], v8 offset:128\n"
    "ds_read_b64_tr_b16 v[190:191], v8 offset:896\n"
    "ds_read_b64_tr_b16 v[24:25], v8 offset:256\n"
    "ds_read_b64_tr_b16 v[26:27], v8 offset:1024\n"
    "v_add_u32 v9, 98304, v4\n"
    "ds_read_b64_tr_b16 v[124:125], v9 offset:0\n"
    "ds_read_b64_tr_b16 v[126:127], v9 offset:768\n"
    "ds_read_b64_tr_b16 v[204:205], v9 offset:128\n"
    "ds_read_b64_tr_b16 v[206:207], v9 offset:896\n"
    "ds_read_b64_tr_b16 v[40:41], v9 offset:256\n"
    "ds_read_b64_tr_b16 v[42:43], v9 offset:1024\n"
    "v_add_u32 v10, 98304, v5\n"
    "ds_read_b64_tr_b16 v[140:141], v10 offset:0\n"
    "ds_read_b64_tr_b16 v[142:143], v10 offset:768\n"
    "ds_read_b64_tr_b16 v[220:221], v10 offset:128\n"
    "ds_read_b64_tr_b16 v[222:223], v10 offset:896\n"
    "ds_read_b64_tr_b16 v[56:57], v10 offset:256\n"
    "ds_read_b64_tr_b16 v[58:59], v10 offset:1024\n"
    "v_add_u32 v11, 98304, v6\n"
    "ds_read_b64_tr_b16 v[156:157], v11 offset:0\n"
    "ds_read_b64_tr_b16 v[158:159], v11 offset:768\n"
    "ds_read_b64_tr_b16 v[236:237], v11 offset:128\n"
    "ds_read_b64_tr_b16 v[238:239], v11 offset:896\n"
    "ds_read_b64_tr_b16 v[72:73], v11 offset:256\n"
    "ds_read_b64_tr_b16 v[74:75], v11 offset:1024\n"
    "s_waitcnt lgkmcnt(0)\n"
    "s_barrier\n"
    "s_waitcnt vmcnt(0)\n"
    "s_barrier\n"
    "v_add_u32 v8, 0, v7\n"
    "ds_read_b64_tr_b16 v[160:161], v8 offset:0\n"
    "ds_read_b64_tr_b16 v[162:163], v8 offset:768\n"
    "ds_read_b64_tr_b16 v[240:241], v8 offset:128\n"
    "ds_read_b64_tr_b16 v[242:243], v8 offset:896\n"
    "ds_read_b64_tr_b16 v[76:77], v8 offset:256\n"
    "ds_read_b64_tr_b16 v[78:79], v8 offset:1024\n"
    "v_add_u32 v9, 12288, v7\n"
    "ds_read_b64_tr_b16 v[164:165], v9 offset:0\n"
    "ds_read_b64_tr_b16 v[166:167], v9 offset:768\n"
    "ds_read_b64_tr_b16 v[244:245], v9 offset:128\n"
    "ds_read_b64_tr_b16 v[246:247], v9 offset:896\n"
    "ds_read_b64_tr_b16 v[80:81], v9 offset:256\n"
    "ds_read_b64_tr_b16 v[82:83], v9 offset:1024\n"
    "v_add_u32 v10, 24576, v7\n"
    "ds_read_b64_tr_b16 v[168:169], v10 offset:0\n"
    "ds_read_b64_tr_b16 v[170:171], v10 offset:768\n"
    "ds_read_b64_tr_b16 v[248:249], v10 offset:128\n"
    "ds_read_b64_tr_b16 v[250:251], v10 offset:896\n"
    "ds_read_b64_tr_b16 v[84:85], v10 offset:256\n"
    "ds_read_b64_tr_b16 v[86:87], v10 offset:1024\n"
    "v_add_u32 v11, 36864, v7\n"
    "ds_read_b64_tr_b16 v[172:173], v11 offset:0\n"
    "ds_read_b64_tr_b16 v[174:175], v11 offset:768\n"
    "ds_read_b64_tr_b16 v[252:253], v11 offset:128\n"
    "ds_read_b64_tr_b16 v[254:255], v11 offset:896\n"
    "ds_read_b64_tr_b16 v[88:89], v11 offset:256\n"
    "ds_read_b64_tr_b16 v[90:91], v11 offset:1024\n"
    "s_waitcnt lgkmcnt(0)\n"
    "s_barrier\n"
    "ds_write_b128 v97, v[12:15] offset:0\n"
    "ds_write_b128 v97, v[16:19] offset:1024\n"
    "ds_write_b128 v97, v[20:23] offset:2048\n"
    "ds_write_b128 v97, v[24:27] offset:3072\n"
    "ds_write_b128 v97, v[28:31] offset:4096\n"
    "ds_write_b128 v97, v[32:35] offset:5120\n"
    "ds_write_b128 v97, v[36:39] offset:6144\n"
    "ds_write_b128 v97, v[40:43] offset:7168\n"
    "ds_write_b128 v97, v[44:47] offset:8192\n"
    "ds_write_b128 v97, v[48:51] offset:9216\n"
    "ds_write_b128 v97, v[52:55] offset:10240\n"
    "ds_write_b128 v97, v[56:59] offset:11264\n"
    "ds_write_b128 v97, v[60:63] offset:12288\n"
    "ds_write_b128 v97, v[64:67] offset:13312\n"
    "ds_write_b128 v97, v[68:71] offset:14336\n"
    "ds_write_b128 v97, v[72:75] offset:15360\n"
    "ds_write_b128 v97, v[76:79] offset:16384\n"
    "ds_write_b128 v97, v[80:83] offset:17408\n"
    "ds_write_b128 v97, v[84:87] offset:18432\n"
    "ds_write_b128 v97, v[88:91] offset:19456\n"
    "s_mov_b32 s38, -2\n"
    "s_mov_b32 s54, 1037384597\n"
    "s_mov_b32 s55, 1037384597\n"
    "s_mov_b32 s56, 16711935\n"
    "s_mov_b32 s57, 16711935\n"
    "s_mov_b32 s58, 0\n"
    "s_mov_b32 s59, 16711935\n"
    "s_mov_b32 s60, 0\n"
    "s_mov_b32 s61, -1\n"
    "s_mov_b32 s62, 67044351\n"
    "s_mov_b32 s63, 67044351\n"
    "v_mov_b32 v8, v107\n"
    "v_mov_b32 v9, v107\n"
    "v_mov_b32 v10, 136960\n"
    "ds_write_b64 v10, v[8:9] offset:0\n"
    "ds_write_b64 v10, v[8:9] offset:8\n"
    "ds_write_b64 v10, v[8:9] offset:1808\n"
    "ds_write_b64 v10, v[8:9] offset:1816\n"
    "ds_write_b64 v10, v[8:9] offset:3616\n"
    "ds_write_b64 v10, v[8:9] offset:3624\n"
    "ds_write_b64 v10, v[8:9] offset:5424\n"
    "ds_write_b64 v10, v[8:9] offset:5432\n"
    "s_lshl_b32 s51, s20, 11\n"
    "v_lshrrev_b32 v4, 2, v96\n"
    "v_and_b32 v6, 12, v4\n"
    "v_lshlrev_b32 v5, 1, v96\n"
    "v_and_b32 v5, 0x380, v5\n"
    "v_min_u32 v5, 0x200, v5\n"
    "v_add_u32 v5, v5, v6\n"
    "s_lshr_b32 s52, s21, 13\n"
    "s_mul_i32 s52, s52, 3\n"
    ".Lqk_po2_%=:\n"
    "buffer_load_dword v8, v4, s[0:3], s28 offen sc1\n"
    "s_waitcnt vmcnt(0)\n"
    "v_cmp_gt_i32 vcc, 1, v8\n"
    "s_nop 0\n"
    "s_cbranch_vccz .Lqk_pd3_%=\n"
    "s_add_u32 s32, s32, 1\n"
    "s_cmp_lt_u32 s32, 0x40000\n"
    "s_cbranch_scc1 .Lqk_sp4_%=\n"
    "v_mov_b32 v8, 20\n"
    "s_mov_b32 s36, 33728\n"
    "buffer_store_dword v8, off, s[0:3], s36 sc1\n"
    "s_waitcnt vmcnt(0)\n"
    "s_branch .Lqk_pd3_%=\n"
    ".Lqk_sp4_%=:\n"
    "s_sleep 4\n"
    "s_branch .Lqk_po2_%=\n"
    ".Lqk_pd3_%=:\n"
    ".Lqk_po5_%=:\n"
    "buffer_load_dword v8, v5, s[0:3], s29 offen sc1\n"
    "s_waitcnt vmcnt(0)\n"
    "v_cmp_gt_i32 vcc, s52, v8\n"
    "s_nop 0\n"
    "s_cbranch_vccz .Lqk_pd6_%=\n"
    "s_add_u32 s32, s32, 1\n"
    "s_cmp_lt_u32 s32, 0x40000\n"
    "s_cbranch_scc1 .Lqk_sp7_%=\n"
    "v_mov_b32 v8, 21\n"
    "s_mov_b32 s36, 33728\n"
    "buffer_store_dword v8, off, s[0:3], s36 sc1\n"
    "s_waitcnt vmcnt(0)\n"
    "s_branch .Lqk_pd6_%=\n"
    ".Lqk_sp7_%=:\n"
    "s_sleep 4\n"
    "s_branch .Lqk_po5_%=\n"
    ".Lqk_pd6_%=:\n"
    "s_add_u32 s45, s24, s21\n"
    "s_add_u32 m0, s51, 0\n"
    "s_nop 0\n"
    "buffer_load_dwordx4 v96, s[0:3], s45 offen sc1 lds\n"
    "buffer_load_dwordx4 v96, s[0:3], s45 offen offset:1024 sc1 lds\n"
    "s_add_u32 m0, s51, 11264\n"
    "s_add_u32 s45, s45, 0x4000\n"
    "buffer_load_dwordx4 v96, s[0:3], s45 offen sc1 lds\n"
    "buffer_load_dwordx4 v96, s[0:3], s45 offen offset:1024 sc1 lds\n"
    "s_add_u32 m0, s51, 22528\n"
    "s_add_u32 s45, s45, 0x4000\n"
    "buffer_load_dwordx4 v96, s[0:3], s45 offen sc1 lds\n"
    "buffer_load_dwordx4 v96, s[0:3], s45 offen offset:1024 sc1 lds\n"
    "s_add_u32 m0, s51, 33792\n"
    "s_add_u32 s45, s45, 0x4000\n"
    "buffer_load_dwordx4 v96, s[0:3], s45 offen sc1 lds\n"
    "buffer_load_dwordx4 v96, s[0:3], s45 offen offset:1024 sc1 lds\n"
    "s_add_u32 m0, s51, 45056\n"
    "s_add_u32 s45, s45, 0x4000\n"
    "buffer_load_dwordx4 v96, s[0:3], s45 offen sc1 lds\n"
    "buffer_load_dwordx4 v96, s[0:3], s45 offen offset:1024 sc1 lds\n"
    ".Lqk_po8_%=:\n"
    "buffer_load_dword v8, v6, s[0:3], s30 offen sc1\n"
    "s_waitcnt vmcnt(0)\n"
    "v_cmp_gt_i32 vcc, 2, v8\n"
    "s_nop 0\n"
    "s_cbranch_vccz .Lqk_pd9_%=\n"
    "s_add_u32 s32, s32, 1\n"
    "s_cmp_lt_u32 s32, 0x40000\n"
    "s_cbranch_scc1 .Lqk_sp10_%=\n"
    "v_mov_b32 v8, 22\n"
    "s_mov_b32 s36, 33728\n"
    "buffer_store_dword v8, off, s[0:3], s36 sc1\n"
    "s_waitcnt vmcnt(0)\n"
    "s_branch .Lqk_pd9_%=\n"
    ".Lqk_sp10_%=:\n"
    "s_sleep 4\n"
    "s_branch .Lqk_po8_%=\n"
    ".Lqk_pd9_%=:\n"
    "s_memrealtime s[34:35]\n"
    "s_waitcnt lgkmcnt(0)\n"
    "s_sub_u32 s49, s34, 864\n"
    "s_mov_b32 s38, -1\n"
    "s_mov_b32 s39, -5\n"
    "s_mov_b32 s42, 2130706432\n"
    "s_mov_b32 s41, 4194304\n"
    "s_mov_b32 s47, s27\n"
    "s_add_u32 s46, s25, s21\n"
    "s_branch .Lqk_bulk_%=\n"
    ".Lqk_take_%=:\n"
    "s_waitcnt vmcnt(2) lgkmcnt(0)\n"
    "ds_write_b128 v92, v[56:59] offset:8192\n"
    "v_smfmac_f32_16x16x64_bf16 v[8:11], v[56:59], v[32:39], v98\n"
    "v_smfmac_f32_16x16x64_bf16 v[12:15], v[56:59], a[160:167], v98\n"
    "s_waitcnt vmcnt(1)\n"
    "ds_write_b128 v92, v[60:63] offset:9216\n"
    "v_max3_u32 v108, v56, v58, v60\n"
    "v_smfmac_f32_16x16x64_bf16 v[0:3], v[56:59], v[176:183], v98\n"
    "s_waitcnt vmcnt(0)\n"
    "ds_write_b128 v92, v[64:67] offset:10240\n"
    "v_max3_u32 v108, v108, v62, v64\n"
    "v_smfmac_f32_16x16x64_bf16 v[4:7], v[56:59], a[80:87], v98\n"
    "v_max_u32 v108, v108, v66\n"
    "v_cmp_eq_u32 vcc, -1, v108\n"
    "v_smfmac_f32_16x16x64_bf16 v[16:19], v[56:59], a[240:247], v98\n"
    "s_cbranch_vccnz .Lqk_retry_%=\n"
    ".Lqk_go_%=:\n"
    "v_smfmac_f32_16x16x64_bf16 v[20:23], v[56:59], a[0:7], v98\n"
    "s_waitcnt lgkmcnt(0)\n"
    "s_barrier\n"
    "ds_read_b128 v[24:27], v93 offset:8192\n"
    "s_memrealtime s[64:65]\n"
    "ds_read_b128 v[28:31], v93 offset:9216\n"
    "v_smfmac_f32_16x16x64_bf16 v[8:11], v[60:63], v[40:47], v98\n"
    "ds_read_b128 v[32:35], v97 offset:6144\n"
    "s_add_u32 m0, s51, 0\n"
    "v_smfmac_f32_16x16x64_bf16 v[12:15], v[60:63], a[168:175], v98\n"
    "ds_read_b128 v[36:39], v97 offset:7168\n"
    "buffer_load_dwordx4 v96, s[0:3], s45 offen sc1 lds\n"
    "v_smfmac_f32_16x16x64_bf16 v[0:3], v[60:63], v[184:191], v98\n"
    "ds_read_b128 v[64:67], v94 offset:8192\n"
    "v_smfmac_f32_16x16x64_bf16 v[4:7], v[60:63], a[88:95], v98\n"
    "ds_read_b128 v[40:43], v97 offset:8192\n"
    "v_smfmac_f32_16x16x64_bf16 v[16:19], v[60:63], a[248:255], v98\n"
    "ds_read_b128 v[44:47], v97 offset:9216\n"
    "v_smfmac_f32_16x16x64_bf16 v[20:23], v[60:63], a[8:15], v98\n"
    "ds_read_b128 v[56:59], v94 offset:9216\n"
    "s_waitcnt lgkmcnt(7)\n"
    "v_smfmac_f32_16x16x64_bf16 v[8:11], v[24:27], v[48:55], v98\n"
    "ds_read_b128 v[48:51], v97 offset:10240\n"
    "buffer_load_dwordx4 v96, s[0:3], s45 offen offset:1024 sc1 lds\n"
    "v_smfmac_f32_16x16x64_bf16 v[12:15], v[24:27], a[176:183], v98\n"
    "ds_read_b128 v[52:55], v97 offset:11264\n"
    "s_add_u32 m0, s51, 11264\n"
    "v_smfmac_f32_16x16x64_bf16 v[0:3], v[24:27], v[192:199], v98\n"
    "s_add_u32 s45, s45, 0x4000\n"
    "v_smfmac_f32_16x16x64_bf16 v[4:7], v[24:27], a[96:103], v98\n"
    "v_smfmac_f32_16x16x64_bf16 v[16:19], v[24:27], v[112:119], v98\n"
    "buffer_load_dwordx4 v96, s[0:3], s45 offen sc1 lds\n"
    "v_smfmac_f32_16x16x64_bf16 v[20:23], v[24:27], a[16:23], v98\n"
    "s_waitcnt lgkmcnt(0)\n"
    "v_smfmac_f32_16x16x64_bf16 v[8:11], v[28:31], v[32:39], v98\n"
    "ds_read_b128 v[32:35], v97 offset:12288\n"
    "s_sub_u32 s36, s64, s49\n"
    "v_smfmac_f32_16x16x64_bf16 v[12:15], v[28:31], a[184:191], v98\n"
    "ds_read_b128 v[36:39], v97 offset:13312\n"
    "s_add_u32 s37, s64, 24\n"
    "v_smfmac_f32_16x16x64_bf16 v[0:3], v[28:31], v[200:207], v98\n"
    "ds_read_b128 v[60:63], v95 offset:8192\n"
    "buffer_load_dwordx4 v96, s[0:3], s45 offen offset:1024 sc1 lds\n"
    "v_smfmac_f32_16x16x64_bf16 v[4:7], v[28:31], a[104:111], v98\n"
    "ds_read_b128 v[24:27], v95 offset:9216\n"
    "s_add_u32 m0, s51, 22528\n"
    "v_smfmac_f32_16x16x64_bf16 v[16:19], v[28:31], v[120:127], v98\n"
    "s_add_u32 s45, s45, 0x4000\n"
    "s_cmp_gt_i32 s36, 0\n"
    "s_cselect_b32 s49, s37, s49\n"
    "v_smfmac_f32_16x16x64_bf16 v[20:23], v[28:31], a[24:31], v98\n"
    "ds_read_b128 v[28:31], v96 offset:10240\n"
    "v_mov_b32 v108, s49\n"
    "v_smfmac_f32_16x16x64_bf16 v[8:11], v[64:67], v[40:47], v98\n"
    "ds_read_b128 v[40:43], v97 offset:14336\n"
    "buffer_load_dwordx4 v96, s[0:3], s45 offen sc1 lds\n"
    "v_smfmac_f32_16x16x64_bf16 v[12:15], v[64:67], a[192:199], v98\n"
    "ds_read_b128 v[44:47], v97 offset:15360\n"
    "v_mov_b32 v109, s39\n"
    "v_smfmac_f32_16x16x64_bf16 v[0:3], v[64:67], v[208:215], v98\n"
    "v_lshlrev_b32 v110, 10, v96\n"
    "v_smfmac_f32_16x16x64_bf16 v[4:7], v[64:67], a[112:119], v98\n"
    "v_smfmac_f32_16x16x64_bf16 v[16:19], v[64:67], v[128:135], v98\n"
    "buffer_load_dwordx4 v96, s[0:3], s45 offen offset:1024 sc1 lds\n"
    "s_add_u32 m0, s51, 33792\n"
    "v_smfmac_f32_16x16x64_bf16 v[20:23], v[64:67], a[32:39], v98\n"
    "ds_read_b128 v[64:67], v96 offset:21504\n"
    "s_add_u32 s45, s45, 0x4000\n"
    "v_smfmac_f32_16x16x64_bf16 v[8:11], v[56:59], v[48:55], v98\n"
    "ds_read_b128 v[48:51], v97 offset:16384\n"
    "v_smfmac_f32_16x16x64_bf16 v[12:15], v[56:59], a[200:207], v98\n"
    "ds_read_b128 v[52:55], v97 offset:17408\n"
    "s_waitcnt lgkmcnt(3)\n"
    "v_smfmac_f32_16x16x64_bf16 v[8:11], v[60:63], v[32:39], v98\n"
    "ds_read_b128 v[32:35], v97 offset:18432\n"
    "buffer_load_dwordx4 v96, s[0:3], s45 offen sc1 lds\n"
    "v_smfmac_f32_16x16x64_bf16 v[12:15], v[60:63], a[208:215], v98\n"
    "ds_read_b128 v[36:39], v97 offset:19456\n"
    "v_smfmac_f32_16x16x64_bf16 v[8:11], v[24:27], v[40:47], v98\n"
    "v_smfmac_f32_16x16x64_bf16 v[12:15], v[24:27], a[216:223], v98\n"
    "v_smfmac_f32_16x16x64_bf16 v[0:3], v[56:59], v[216:223], v98\n"
    "buffer_load_dwordx4 v96, s[0:3], s45 offen offset:1024 sc1 lds\n"
    "s_add_u32 m0, s51, 45056\n"
    "v_smfmac_f32_16x16x64_bf16 v[4:7], v[56:59], a[120:127], v98\n"
    "s_add_u32 s45, s45, 0x4000\n"
    "v_smfmac_f32_16x16x64_bf16 v[0:3], v[60:63], v[224:231], v98\n"
    "v_smfmac_f32_16x16x64_bf16 v[4:7], v[60:63], a[128:135], v98\n"
    "s_waitcnt lgkmcnt(0)\n"
    "v_smfmac_f32_16x16x64_bf16 v[8:11], v[28:31], v[48:55], v98\n"
    "buffer_load_dwordx4 v96, s[0:3], s45 offen sc1 lds\n"
    "v_smfmac_f32_16x16x64_bf16 v[12:15], v[28:31], a[224:231], v98\n"
    "v_smfmac_f32_16x16x64_bf16 v[8:11], v[64:67], v[32:39], v98\n"
    "v_smfmac_f32_16x16x64_bf16 v[12:15], v[64:67], a[232:239], v98\n"
    "v_smfmac_f32_16x16x64_bf16 v[0:3], v[24:27], v[232:239], v98\n"
    "buffer_load_dwordx4 v96, s[0:3], s45 offen offset:1024 sc1 lds\n"
    "v_smfmac_f32_16x16x64_bf16 v[4:7], v[24:27], a[136:143], v98\n"
    "v_smfmac_f32_16x16x64_bf16 v[0:3], v[28:31], v[240:247], v98\n"
    "v_smfmac_f32_16x16x64_bf16 v[4:7], v[28:31], a[144:151], v98\n"
    "v_smfmac_f32_16x16x64_bf16 v[0:3], v[64:67], v[248:255], v98\n"
    "s_mov_b64 exec, 15\n"
    "buffer_store_dwordx2 v[108:109], v110, s[0:3], s48 offen sc1\n"
    "s_mov_b64 exec, -1\n"
    "v_smfmac_f32_16x16x64_bf16 v[4:7], v[64:67], a[152:159], v98\n"
    "v_smfmac_f32_16x16x64_bf16 v[16:19], v[56:59], v[136:143], v98\n"
    "v_add_f32 v8, v8, v10\n"
    "v_add_f32 v9, v9, v11\n"
    "v_smfmac_f32_16x16x64_bf16 v[20:23], v[56:59], a[40:47], v98\n"
    "v_add_f32 v12, v12, v14\n"
    "v_add_f32 v13, v13, v15\n"
    "v_smfmac_f32_16x16x64_bf16 v[16:19], v[60:63], v[144:151], v98\n"
    "v_cvt_pk_bf16_f32 v8, v8, v9\n"
    "v_cvt_pk_bf16_f32 v12, v12, v13\n"
    "v_smfmac_f32_16x16x64_bf16 v[20:23], v[60:63], a[48:55], v98\n"
    "ds_write2_b32 v99, v8, v12 offset0:8 offset1:12\n"
    "ds_read_b64_tr_b16 v[14:15], v100 offset:32\n"
    "v_smfmac_f32_16x16x64_bf16 v[16:19], v[24:27], v[152:159], v98\n"
    "v_add_f32 v0, v0, v2\n"
    "v_add_f32 v1, v1, v3\n"
    "v_smfmac_f32_16x16x64_bf16 v[20:23], v[24:27], a[56:63], v98\n"
    "v_add_f32 v4, v4, v6\n"
    "v_add_f32 v5, v5, v7\n"
    "v_smfmac_f32_16x16x64_bf16 v[16:19], v[28:31], v[160:167], v98\n"
    "v_cvt_pk_bf16_f32 v0, v0, v1\n"
    "v_cvt_pk_bf16_f32 v4, v4, v5\n"
    "v_smfmac_f32_16x16x64_bf16 v[20:23], v[28:31], a[64:71], v98\n"
    "ds_write2_b32 v99, v0, v4 offset0:0 offset1:4\n"
    "ds_read_b64_tr_b16 v[12:13], v100 offset:0\n"
    "v_smfmac_f32_16x16x64_bf16 v[16:19], v[64:67], v[168:175], v98\n"
    "v_smfmac_f32_16x16x64_bf16 v[20:23], v[64:67], a[72:79], v98\n"
    "s_nop 6\n"
    "v_pk_add_f32 v[16:17], v[16:17], v[18:19]\n"
    "v_pk_add_f32 v[20:21], v[20:21], v[22:23]\n"
    "v_cvt_pk_bf16_f32 v16, v16, v17\n"
    "v_cvt_pk_bf16_f32 v20, v20, v21\n"
    "ds_write2_b32 v99, v16, v20 offset0:16 offset1:20\n"
    "ds_read_b64_tr_b16 v[4:5], v100 offset:64\n"
    "s_waitcnt lgkmcnt(2)\n"
    "v_mfma_f32_16x16x32_bf16 v[8:11], v[68:71], v[12:15], 0\n"
    "v_mfma_f32_16x16x32_bf16 v[0:3], v[72:75], v[12:15], 0\n"
    "v_mfma_f32_16x16x32_bf16 v[108:111], v[12:15], v[12:15], 0\n"
    "v_mfma_f32_16x16x32_bf16 v[32:35], v[12:15], v[80:83], 0\n"
    "v_mfma_f32_16x16x32_bf16 v[36:39], v[12:15], v[84:87], 0\n"
    "s_waitcnt lgkmcnt(0)\n"
    "s_nop 0\n"
    "v_mfma_f32_16x16x16_bf16 v[8:11], v[76:77], v[4:5], v[8:11]\n"
    "v_mfma_f32_16x16x16_bf16 v[0:3], v[78:79], v[4:5], v[0:3]\n"
    "v_mfma_f32_16x16x16_bf16 v[108:111], v[4:5], v[4:5], v[108:111]\n"
    "v_mfma_f32_16x16x16_bf16 v[32:35], v[4:5], v[88:89], v[32:35]\n"
    "v_mfma_f32_16x16x16_bf16 v[36:39], v[4:5], v[90:91], v[36:39]\n"
    "s_mov_b64 exec, s[56:57]\n"
    "s_nop 2\n"
    "ds_write_b128 v101, v[8:11]\n"
    "ds_write_b128 v101, v[0:3] offset:64\n"
    "s_mov_b64 exec, s[58:59]\n"
    "ds_write_b128 v101, v[108:111] offset:96\n"
    "s_mov_b64 exec, -1\n"
    "s_waitcnt lgkmcnt(0)\n"
    "s_barrier\n"
    "ds_read_b128 v[40:43], v102 offset:0\n"
    "ds_read_b128 v[44:47], v102 offset:1808\n"
    "ds_read_b128 v[48:51], v102 offset:3616\n"
    "ds_read_b128 v[52:55], v102 offset:5424\n"
    "s_mov_b64 exec, s[60:61]\n"
    "ds_write_b128 v103, v[36:39] offset:96\n"
    "ds_write_b128 v103, v[32:35] offset:192\n"
    "s_mov_b64 exec, -1\n"
    "s_waitcnt lgkmcnt(4)\n"
    "v_pk_add_f32 v[40:41], v[40:41], v[44:45]\n"
    "v_pk_add_f32 v[42:43], v[42:43], v[46:47]\n"
    "s_waitcnt lgkmcnt(2)\n"
    "v_pk_add_f32 v[48:49], v[48:49], v[52:53]\n"
    "v_pk_add_f32 v[50:51], v[50:51], v[54:55]\n"
    "v_pk_add_f32 v[40:41], v[40:41], v[48:49]\n"
    "v_pk_add_f32 v[42:43], v[42:43], v[50:51]\n"
    "v_max3_f32 v16, v40, v41, v42\n"
    "v_max_f32 v16, v16, v43\n"
    "s_nop 1\n"
    "v_max_f32_dpp v16, v16, v16 row_ror:8 row_mask:0xf bank_mask:0xf\n"
    "s_nop 1\n"
    "v_max_f32_dpp v16, v16, v16 row_ror:4 row_mask:0xf bank_mask:0xf\n"
    "s_nop 1\n"
    "v_max_f32_dpp v16, v16, v16 row_ror:2 row_mask:0xf bank_mask:0xf\n"
    "s_nop 1\n"
    "v_max_f32_dpp v16, v16, v16 row_ror:1 row_mask:0xf bank_mask:0xf\n"
    "v_mul_f32 v16, s54, v16\n"
    "v_pk_fma_f32 v[40:41], v[40:41], s[54:55], v[16:17] op_sel_hi:[1,1,0] neg_lo:[0,0,1] neg_hi:[0,0,1]\n"
    "v_pk_fma_f32 v[42:43], v[42:43], s[54:55], v[16:17] op_sel_hi:[1,1,0] neg_lo:[0,0,1] neg_hi:[0,0,1]\n"
    "v_exp_f32 v40, v40\n"
    "v_exp_f32 v41, v41\n"
    "v_exp_f32 v42, v42\n"
    "v_exp_f32 v43, v43\n"
    "s_nop 0\n"
    "v_pk_add_f32 v[16:17], v[40:41], v[42:43]\n"
    "v_add_f32 v16, v16, v17\n"
    "s_nop 1\n"
    "v_add_f32_dpp v16, v16, v16 row_ror:8 row_mask:0xf bank_mask:0xf\n"
    "s_nop 1\n"
    "v_add_f32_dpp v16, v16, v16 row_ror:4 row_mask:0xf bank_mask:0xf\n"
    "s_nop 1\n"
    "v_add_f32_dpp v16, v16, v16 row_ror:2 row_mask:0xf bank_mask:0xf\n"
    "s_nop 1\n"
    "v_add_f32_dpp v16, v16, v16 row_ror:1 row_mask:0xf bank_mask:0xf\n"
    "v_rcp_f32 v16, v16\n"
    "s_nop 0\n"
    "v_pk_mul_f32 v[40:41], v[40:41], v[16:17] op_sel_hi:[1,0]\n"
    "v_pk_mul_f32 v[42:43], v[42:43], v[16:17] op_sel_hi:[1,0]\n"
    "v_cvt_pk_bf16_f32 v40, v40, v41\n"
    "v_cvt_pk_bf16_f32 v41, v42, v43\n"
    "v_min_u32 v40, -2, v40\n"
    "buffer_store_dwordx2 v[40:41], v105, s[0:3], s40 offen sc1\n"
    ".Lqk_bulk_%=:\n"
    "s_waitcnt vmcnt(2) lgkmcnt(0)\n"
    "s_barrier\n"
    "ds_read_b128 v[32:35], v92 offset:0\n"
    "ds_read_b128 v[36:39], v92 offset:1024\n"
    "ds_read_b128 v[56:59], v104 offset:0\n"
    "ds_read_b128 v[60:63], v104 offset:4352\n"
    "ds_read_b128 v[64:67], v104 offset:8704\n"
    "ds_read_b128 v[68:71], v104 offset:13056\n"
    "ds_read_b128 v[40:43], v92 offset:2048\n"
    "ds_read_b128 v[44:47], v92 offset:3072\n"
    "ds_read_b128 v[48:51], v92 offset:4096\n"
    "ds_read_b128 v[52:55], v92 offset:5120\n"
    "s_waitcnt lgkmcnt(8)\n"
    "v_mfma_f32_16x16x32_bf16 v[0:3], a[80:83], v[32:35], 0\n"
    "s_waitcnt lgkmcnt(6)\n"
    "v_add_f32 v56, v56, v60\n"
    "v_mfma_f32_16x16x32_bf16 v[4:7], a[80:83], v[36:39], 0\n"
    "v_add_f32 v57, v57, v61\n"
    "v_add_f32 v58, v58, v62\n"
    "v_mfma_f32_16x16x32_bf16 v[8:11], a[160:163], v[32:35], 0\n"
    "v_add_f32 v59, v59, v63\n"
    "s_waitcnt lgkmcnt(4)\n"
    "v_mfma_f32_16x16x32_bf16 v[12:15], a[160:163], v[36:39], 0\n"
    "v_add_f32 v64, v64, v68\n"
    "v_add_f32 v65, v65, v69\n"
    "v_mfma_f32_16x16x32_bf16 v[16:19], a[0:3], v[32:35], 0\n"
    "v_add_f32 v66, v66, v70\n"
    "v_add_f32 v67, v67, v71\n"
    "v_mfma_f32_16x16x32_bf16 v[20:23], a[0:3], v[36:39], 0\n"
    "v_add_f32 v56, v56, v64\n"
    "v_add_f32 v57, v57, v65\n"
    "v_mfma_f32_16x16x32_bf16 v[24:27], a[240:243], v[32:35], 0\n"
    "ds_read_b128 v[32:35], v92 offset:6144\n"
    "v_add_f32 v58, v58, v66\n"
    "v_mfma_f32_16x16x32_bf16 v[28:31], a[240:243], v[36:39], 0\n"
    "ds_read_b128 v[36:39], v92 offset:7168\n"
    "s_waitcnt lgkmcnt(4)\n"
    "v_mfma_f32_16x16x32_bf16 v[0:3], a[84:87], v[40:43], v[0:3]\n"
    "v_add_f32 v59, v59, v67\n"
    "s_barrier\n"
    "v_mfma_f32_16x16x32_bf16 v[4:7], a[84:87], v[44:47], v[4:7]\n"
    "v_cndmask_b32 v56, v107, v56, s[12:13]\n"
    "v_cndmask_b32 v57, v107, v57, s[14:15]\n"
    "v_mfma_f32_16x16x32_bf16 v[8:11], a[164:167], v[40:43], v[8:11]\n"
    "v_cndmask_b32 v58, v107, v58, s[16:17]\n"
    "v_cndmask_b32 v59, v107, v59, s[18:19]\n"
    "v_mfma_f32_16x16x32_bf16 v[12:15], a[164:167], v[44:47], v[12:15]\n"
    "v_max3_f32 v72, v56, v57, v58\n"
    "v_max_f32 v72, v72, v59\n"
    "v_mfma_f32_16x16x32_bf16 v[16:19], a[4:7], v[40:43], v[16:19]\n"
    "v_cndmask_b32 v74, v107, v72, s[62:63]\n"
    "v_cndmask_b32 v75, v72, v107, s[62:63]\n"
    "v_mfma_f32_16x16x32_bf16 v[20:23], a[4:7], v[44:47], v[20:23]\n"
    "v_max_f32_dpp v74, v74, v74 row_ror:8 row_mask:0xf bank_mask:0xf\n"
    "v_max_f32_dpp v75, v75, v75 row_ror:8 row_mask:0xf bank_mask:0xf\n"
    "v_mfma_f32_16x16x32_bf16 v[24:27], a[244:247], v[40:43], v[24:27]\n"
    "ds_read_b128 v[40:43], v93 offset:0\n"
    "v_max_f32_dpp v74, v74, v74 row_ror:4 row_mask:0xf bank_mask:0xf\n"
    "v_mfma_f32_16x16x32_bf16 v[28:31], a[244:247], v[44:47], v[28:31]\n"
    "ds_read_b128 v[44:47], v93 offset:1024\n"
    "s_waitcnt lgkmcnt(4)\n"
    "v_mfma_f32_16x16x32_bf16 v[0:3], a[88:91], v[48:51], v[0:3]\n"
    "v_max_f32_dpp v75, v75, v75 row_ror:4 row_mask:0xf bank_mask:0xf\n"
    "v_max_f32_dpp v74, v74, v74 row_ror:2 row_mask:0xf bank_mask:0xf\n"
    "v_mfma_f32_16x16x32_bf16 v[4:7], a[88:91], v[52:55], v[4:7]\n"
    "v_max_f32_dpp v75, v75, v75 row_ror:2 row_mask:0xf bank_mask:0xf\n"
    "v_max_f32_dpp v74, v74, v74 row_ror:1 row_mask:0xf bank_mask:0xf\n"
    "v_mfma_f32_16x16x32_bf16 v[8:11], a[168:171], v[48:51], v[8:11]\n"
    "v_max_f32_dpp v75, v75, v75 row_ror:1 row_mask:0xf bank_mask:0xf\n"
    "v_cndmask_b32 v72, v75, v74, s[62:63]\n"
    "v_mfma_f32_16x16x32_bf16 v[12:15], a[168:171], v[52:55], v[12:15]\n"
    "v_mul_f32 v72, s54, v72\n"
    "v_fma_f32 v56, v56, s54, -v72\n"
    "v_mfma_f32_16x16x32_bf16 v[16:19], a[8:11], v[48:51], v[16:19]\n"
    "v_fma_f32 v57, v57, s54, -v72\n"
    "v_fma_f32 v58, v58, s54, -v72\n"
    "v_mfma_f32_16x16x32_bf16 v[20:23], a[8:11], v[52:55], v[20:23]\n"
    "v_fma_f32 v59, v59, s54, -v72\n"
    "v_mfma_f32_16x16x32_bf16 v[24:27], a[248:251], v[48:51], v[24:27]\n"
    "ds_read_b128 v[48:51], v93 offset:2048\n"
    "v_mfma_f32_16x16x32_bf16 v[28:31], a[248:251], v[52:55], v[28:31]\n"
    "ds_read_b128 v[52:55], v93 offset:3072\n"
    "s_waitcnt lgkmcnt(4)\n"
    "v_mfma_f32_16x16x32_bf16 v[0:3], a[92:95], v[32:35], v[0:3]\n"
    "v_exp_f32 v56, v56\n"
    "v_mfma_f32_16x16x32_bf16 v[4:7], a[92:95], v[36:39], v[4:7]\n"
    "v_exp_f32 v57, v57\n"
    "v_mfma_f32_16x16x32_bf16 v[8:11], a[172:175], v[32:35], v[8:11]\n"
    "v_exp_f32 v58, v58\n"
    "v_mfma_f32_16x16x32_bf16 v[12:15], a[172:175], v[36:39], v[12:15]\n"
    "v_exp_f32 v59, v59\n"
    "v_mfma_f32_16x16x32_bf16 v[16:19], a[12:15], v[32:35], v[16:19]\n"
    "v_add_f32 v72, v56, v58\n"
    "v_add_f32 v73, v57, v59\n"
    "v_mfma_f32_16x16x32_bf16 v[20:23], a[12:15], v[36:39], v[20:23]\n"
    "v_add_f32 v72, v72, v73\n"
    "v_cndmask_b32 v74, 0, v72, s[62:63]\n"
    "v_mfma_f32_16x16x32_bf16 v[24:27], a[252:255], v[32:35], v[24:27]\n"
    "ds_read_b128 v[32:35], v93 offset:4096\n"
    "v_cndmask_b32 v75, v72, 0, s[62:63]\n"
    "v_mfma_f32_16x16x32_bf16 v[28:31], a[252:255], v[36:39], v[28:31]\n"
    "ds_read_b128 v[36:39], v93 offset:5120\n"
    "s_waitcnt lgkmcnt(4)\n"
    "v_mfma_f32_16x16x32_bf16 v[0:3], a[96:99], v[40:43], v[0:3]\n"
    "v_add_f32_dpp v74, v74, v74 row_ror:8 row_mask:0xf bank_mask:0xf\n"
    "v_add_f32_dpp v75, v75, v75 row_ror:8 row_mask:0xf bank_mask:0xf\n"
    "v_mfma_f32_16x16x32_bf16 v[4:7], a[96:99], v[44:47], v[4:7]\n"
    "v_add_f32_dpp v74, v74, v74 row_ror:4 row_mask:0xf bank_mask:0xf\n"
    "v_add_f32_dpp v75, v75, v75 row_ror:4 row_mask:0xf bank_mask:0xf\n"
    "v_mfma_f32_16x16x32_bf16 v[8:11], a[176:179], v[40:43], v[8:11]\n"
    "v_add_f32_dpp v74, v74, v74 row_ror:2 row_mask:0xf bank_mask:0xf\n"
    "v_add_f32_dpp v75, v75, v75 row_ror:2 row_mask:0xf bank_mask:0xf\n"
    "v_mfma_f32_16x16x32_bf16 v[12:15], a[176:179], v[44:47], v[12:15]\n"
    "v_add_f32_dpp v74, v74, v74 row_ror:1 row_mask:0xf bank_mask:0xf\n"
    "v_add_f32_dpp v75, v75, v75 row_ror:1 row_mask:0xf bank_mask:0xf\n"
    "v_mfma_f32_16x16x32_bf16 v[16:19], a[16:19], v[40:43], v[16:19]\n"
    "v_cndmask_b32 v72, v75, v74, s[62:63]\n"
    "v_mfma_f32_16x16x32_bf16 v[20:23], a[16:19], v[44:47], v[20:23]\n"
    "v_rcp_f32 v72, v72\n"
    "v_mfma_f32_16x16x32_bf16 v[24:27], v[112:115], v[40:43], v[24:27]\n"
    "ds_read_b128 v[40:43], v93 offset:6144\n"
    "v_mul_f32 v56, v56, v72\n"
    "v_mfma_f32_16x16x32_bf16 v[28:31], v[112:115], v[44:47], v[28:31]\n"
    "ds_read_b128 v[44:47], v93 offset:7168\n"
    "s_waitcnt lgkmcnt(4)\n"
    "v_mfma_f32_16x16x32_bf16 v[0:3], a[100:103], v[48:51], v[0:3]\n"
    "v_mul_f32 v57, v57, v72\n"
    "v_mul_f32 v58, v58, v72\n"
    "v_mfma_f32_16x16x32_bf16 v[4:7], a[100:103], v[52:55], v[4:7]\n"
    "v_mul_f32 v59, v59, v72\n"
    "v_cvt_pk_bf16_f32 v56, v56, v57\n"
    "v_mfma_f32_16x16x32_bf16 v[8:11], a[180:183], v[48:51], v[8:11]\n"
    "v_cvt_pk_bf16_f32 v57, v58, v59\n"
    "v_min_u32 v56, -2, v56\n"
    "v_mfma_f32_16x16x32_bf16 v[12:15], a[180:183], v[52:55], v[12:15]\n"
    "buffer_store_dwordx2 v[56:57], v106, s[0:3], s42 offen sc1\n"
    "v_mfma_f32_16x16x32_bf16 v[16:19], a[20:23], v[48:51], v[16:19]\n"
    "s_cmp_eq_u32 s38, 63\n"
    "s_cbranch_scc1 .Lqk_end_%=\n"
    "v_mfma_f32_16x16x32_bf16 v[20:23], a[20:23], v[52:55], v[20:23]\n"
    "v_mov_b64 v[72:73], -1\n"
    "v_mfma_f32_16x16x32_bf16 v[24:27], v[116:119], v[48:51], v[24:27]\n"
    "ds_read_b128 v[48:51], v94 offset:0\n"
    "v_mfma_f32_16x16x32_bf16 v[28:31], v[116:119], v[52:55], v[28:31]\n"
    "ds_read_b128 v[52:55], v94 offset:1024\n"
    "s_waitcnt lgkmcnt(4)\n"
    "v_mfma_f32_16x16x32_bf16 v[0:3], a[104:107], v[32:35], v[0:3]\n"
    "v_mfma_f32_16x16x32_bf16 v[4:7], a[104:107], v[36:39], v[4:7]\n"
    "v_mfma_f32_16x16x32_bf16 v[8:11], a[184:187], v[32:35], v[8:11]\n"
    "v_mfma_f32_16x16x32_bf16 v[12:15], a[184:187], v[36:39], v[12:15]\n"
    "v_mfma_f32_16x16x32_bf16 v[16:19], a[24:27], v[32:35], v[16:19]\n"
    "buffer_store_dwordx2 v[72:73], v105, s[0:3], s41 offen sc1\n"
    "v_mfma_f32_16x16x32_bf16 v[20:23], a[24:27], v[36:39], v[20:23]\n"
    "v_mfma_f32_16x16x32_bf16 v[24:27], v[120:123], v[32:35], v[24:27]\n"
    "ds_read_b128 v[32:35], v94 offset:2048\n"
    "v_mfma_f32_16x16x32_bf16 v[28:31], v[120:123], v[36:39], v[28:31]\n"
    "ds_read_b128 v[36:39], v94 offset:3072\n"
    "s_waitcnt lgkmcnt(4)\n"
    "v_mfma_f32_16x16x32_bf16 v[0:3], a[108:111], v[40:43], v[0:3]\n"
    "v_mfma_f32_16x16x32_bf16 v[4:7], a[108:111], v[44:47], v[4:7]\n"
    "v_mfma_f32_16x16x32_bf16 v[8:11], a[188:191], v[40:43], v[8:11]\n"
    "v_mfma_f32_16x16x32_bf16 v[12:15], a[188:191], v[44:47], v[12:15]\n"
    "v_mfma_f32_16x16x32_bf16 v[16:19], a[28:31], v[40:43], v[16:19]\n"
    "v_mfma_f32_16x16x32_bf16 v[20:23], a[28:31], v[44:47], v[20:23]\n"
    "buffer_store_dwordx2 v[72:73], v106, s[0:3], s41 offen sc1\n"
    "v_mfma_f32_16x16x32_bf16 v[24:27], v[124:127], v[40:43], v[24:27]\n"
    "ds_read_b128 v[40:43], v94 offset:4096\n"
    "v_mfma_f32_16x16x32_bf16 v[28:31], v[124:127], v[44:47], v[28:31]\n"
    "ds_read_b128 v[44:47], v94 offset:5120\n"
    "s_waitcnt lgkmcnt(4)\n"
    "v_mfma_f32_16x16x32_bf16 v[0:3], a[112:115], v[48:51], v[0:3]\n"
    "v_mfma_f32_16x16x32_bf16 v[4:7], a[112:115], v[52:55], v[4:7]\n"
    "v_mfma_f32_16x16x32_bf16 v[8:11], a[192:195], v[48:51], v[8:11]\n"
    "v_mfma_f32_16x16x32_bf16 v[12:15], a[192:195], v[52:55], v[12:15]\n"
    "v_mfma_f32_16x16x32_bf16 v[16:19], a[32:35], v[48:51], v[16:19]\n"
    "v_mfma_f32_16x16x32_bf16 v[20:23], a[32:35], v[52:55], v[20:23]\n"
    "v_mfma_f32_16x16x32_bf16 v[24:27], v[128:131], v[48:51], v[24:27]\n"
    "ds_read_b128 v[48:51], v94 offset:6144\n"
    "buffer_load_dwordx4 v[80:83], v96, s[0:3], s46 offen sc1\n"
    "v_mfma_f32_16x16x32_bf16 v[28:31], v[128:131], v[52:55], v[28:31]\n"
    "ds_read_b128 v[52:55], v94 offset:7168\n"
    "s_waitcnt lgkmcnt(4)\n"
    "v_mfma_f32_16x16x32_bf16 v[0:3], a[116:119], v[32:35], v[0:3]\n"
    "v_mfma_f32_16x16x32_bf16 v[4:7], a[116:119], v[36:39], v[4:7]\n"
    "v_mfma_f32_16x16x32_bf16 v[8:11], a[196:199], v[32:35], v[8:11]\n"
    "buffer_load_dwordx4 v[84:87], v96, s[0:3], s46 offen offset:1024 sc1\n"
    "v_mfma_f32_16x16x32_bf16 v[12:15], a[196:199], v[36:39], v[12:15]\n"
    "v_mfma_f32_16x16x32_bf16 v[16:19], a[36:39], v[32:35], v[16:19]\n"
    "v_mfma_f32_16x16x32_bf16 v[20:23], a[36:39], v[36:39], v[20:23]\n"
    "v_mfma_f32_16x16x32_bf16 v[24:27], v[132:135], v[32:35], v[24:27]\n"
    "ds_read_b128 v[32:35], v95 offset:0\n"
    "buffer_load_dwordx2 v[108:109], off, s[0:3], s47 sc1\n"
    "v_mfma_f32_16x16x32_bf16 v[28:31], v[132:135], v[36:39], v[28:31]\n"
    "ds_read_b128 v[36:39], v95 offset:1024\n"
    "s_waitcnt lgkmcnt(4)\n"
    "v_mfma_f32_16x16x32_bf16 v[0:3], a[120:123], v[40:43], v[0:3]\n"
    "s_add_u32 s38, s38, 1\n"
    "s_add_u32 s52, s38, 1\n"
    "s_add_u32 s40, s38, 0\n"
    "v_mfma_f32_16x16x32_bf16 v[4:7], a[120:123], v[44:47], v[4:7]\n"
    "s_and_b32 s40, s40, 3\n"
    "s_lshl_b32 s40, s40, 22\n"
    "s_add_u32 s41, s38, 2\n"
    "v_mfma_f32_16x16x32_bf16 v[8:11], a[200:203], v[40:43], v[8:11]\n"
    "s_and_b32 s41, s41, 3\n"
    "s_lshl_b32 s41, s41, 22\n"
    "s_and_b32 s47, s38, 63\n"
    "v_mfma_f32_16x16x32_bf16 v[12:15], a[200:203], v[44:47], v[12:15]\n"
    "s_lshl_b32 s47, s47, 7\n"
    "s_mov_b32 s42, s40\n"
    "s_add_u32 s43, s22, s40\n"
    "v_mfma_f32_16x16x32_bf16 v[16:19], a[40:43], v[40:43], v[16:19]\n"
    "s_add_u32 s44, s23, s40\n"
    "s_add_u32 s48, s26, s47\n"
    "s_add_u32 s47, s27, s47\n"
    "v_mfma_f32_16x16x32_bf16 v[20:23], a[40:43], v[44:47], v[20:23]\n"
    "s_lshl_b32 s45, s52, 19\n"
    "s_lshl_b32 s37, s52, 11\n"
    "s_xor_b32 s37, s37, s21\n"
    "v_mfma_f32_16x16x32_bf16 v[24:27], v[136:139], v[40:43], v[24:27]\n"
    "ds_read_b128 v[40:43], v95 offset:2048\n"
    "s_and_b32 s37, s37, 0x2000\n"
    "v_mfma_f32_16x16x32_bf16 v[28:31], v[136:139], v[44:47], v[28:31]\n"
    "ds_read_b128 v[44:47], v95 offset:3072\n"
    "s_waitcnt lgkmcnt(4)\n"
    "v_mfma_f32_16x16x32_bf16 v[0:3], a[124:127], v[48:51], v[0:3]\n"
    "s_add_u32 s45, s45, s37\n"
    "s_add_u32 s45, s45, s24\n"
    "s_lshl_b32 s46, s52, 20\n"
    "v_mfma_f32_16x16x32_bf16 v[4:7], a[124:127], v[52:55], v[4:7]\n"
    "s_lshl_b32 s37, s52, 12\n"
    "s_xor_b32 s37, s37, s21\n"
    "s_and_b32 s37, s37, 0x2000\n"
    "v_mfma_f32_16x16x32_bf16 v[8:11], a[204:207], v[48:51], v[8:11]\n"
    "s_add_u32 s46, s46, s37\n"
    "s_add_u32 s46, s46, s25\n"
    "v_mfma_f32_16x16x32_bf16 v[12:15], a[204:207], v[52:55], v[12:15]\n"
    "v_mfma_f32_16x16x32_bf16 v[16:19], a[44:47], v[48:51], v[16:19]\n"
    "v_mfma_f32_16x16x32_bf16 v[20:23], a[44:47], v[52:55], v[20:23]\n"
    "v_mfma_f32_16x16x32_bf16 v[24:27], v[140:143], v[48:51], v[24:27]\n"
    "ds_read_b128 v[48:51], v95 offset:4096\n"
    "v_mfma_f32_16x16x32_bf16 v[28:31], v[140:143], v[52:55], v[28:31]\n"
    "ds_read_b128 v[52:55], v95 offset:5120\n"
    "s_waitcnt lgkmcnt(4)\n"
    "v_mfma_f32_16x16x32_bf16 v[0:3], a[128:131], v[32:35], v[0:3]\n"
    "v_mfma_f32_16x16x32_bf16 v[4:7], a[128:131], v[36:39], v[4:7]\n"
    "v_mfma_f32_16x16x32_bf16 v[8:11], a[208:211], v[32:35], v[8:11]\n"
    "v_mfma_f32_16x16x32_bf16 v[12:15], a[208:211], v[36:39], v[12:15]\n"
    "v_mfma_f32_16x16x32_bf16 v[16:19], a[48:51], v[32:35], v[16:19]\n"
    "v_mfma_f32_16x16x32_bf16 v[20:23], a[48:51], v[36:39], v[20:23]\n"
    "v_mfma_f32_16x16x32_bf16 v[24:27], v[144:147], v[32:35], v[24:27]\n"
    "ds_read_b128 v[32:35], v95 offset:6144\n"
    "v_mfma_f32_16x16x32_bf16 v[28:31], v[144:147], v[36:39], v[28:31]\n"
    "ds_read_b128 v[36:39], v95 offset:7168\n"
    "s_waitcnt lgkmcnt(4)\n"
    "v_mfma_f32_16x16x32_bf16 v[0:3], a[132:135], v[40:43], v[0:3]\n"
    "v_mfma_f32_16x16x32_bf16 v[4:7], a[132:135], v[44:47], v[4:7]\n"
    "v_mfma_f32_16x16x32_bf16 v[8:11], a[212:215], v[40:43], v[8:11]\n"
    "v_mfma_f32_16x16x32_bf16 v[12:15], a[212:215], v[44:47], v[12:15]\n"
    "v_mfma_f32_16x16x32_bf16 v[16:19], a[52:55], v[40:43], v[16:19]\n"
    "v_mfma_f32_16x16x32_bf16 v[20:23], a[52:55], v[44:47], v[20:23]\n"
    "v_mfma_f32_16x16x32_bf16 v[24:27], v[148:151], v[40:43], v[24:27]\n"
    "ds_read_b128 v[40:43], v96 offset:45056\n"
    "v_mfma_f32_16x16x32_bf16 v[28:31], v[148:151], v[44:47], v[28:31]\n"
    "ds_read_b128 v[44:47], v96 offset:46080\n"
    "s_waitcnt lgkmcnt(4)\n"
    "v_mfma_f32_16x16x32_bf16 v[0:3], a[136:139], v[48:51], v[0:3]\n"
    "v_mfma_f32_16x16x32_bf16 v[4:7], a[136:139], v[52:55], v[4:7]\n"
    "v_mfma_f32_16x16x32_bf16 v[8:11], a[216:219], v[48:51], v[8:11]\n"
    "v_mfma_f32_16x16x32_bf16 v[12:15], a[216:219], v[52:55], v[12:15]\n"
    "v_mfma_f32_16x16x32_bf16 v[16:19], a[56:59], v[48:51], v[16:19]\n"
    "v_mfma_f32_16x16x32_bf16 v[20:23], a[56:59], v[52:55], v[20:23]\n"
    "v_mfma_f32_16x16x32_bf16 v[24:27], v[152:155], v[48:51], v[24:27]\n"
    "ds_read_b128 v[48:51], v96 offset:47104\n"
    "v_mfma_f32_16x16x32_bf16 v[28:31], v[152:155], v[52:55], v[28:31]\n"
    "ds_read_b128 v[52:55], v96 offset:48128\n"
    "s_waitcnt lgkmcnt(4)\n"
    "v_mfma_f32_16x16x32_bf16 v[0:3], a[140:143], v[32:35], v[0:3]\n"
    "v_mfma_f32_16x16x32_bf16 v[4:7], a[140:143], v[36:39], v[4:7]\n"
    "v_mfma_f32_16x16x32_bf16 v[8:11], a[220:223], v[32:35], v[8:11]\n"
    "s_waitcnt vmcnt(0)\n"
    "v_readfirstlane_b32 s36, v108\n"
    "v_mfma_f32_16x16x32_bf16 v[12:15], a[220:223], v[36:39], v[12:15]\n"
    "v_readfirstlane_b32 s37, v109\n"
    "v_mfma_f32_16x16x32_bf16 v[16:19], a[60:63], v[32:35], v[16:19]\n"
    "s_cmp_eq_u32 s37, s39\n"
    "s_cselect_b32 s49, s36, s49\n"
    "s_add_u32 s49, s49, 288\n"
    "v_mfma_f32_16x16x32_bf16 v[20:23], a[60:63], v[36:39], v[20:23]\n"
    "s_sub_u32 s50, s49, 63\n"
    "s_mov_b32 s39, s52\n"
    "v_mfma_f32_16x16x32_bf16 v[24:27], v[156:159], v[32:35], v[24:27]\n"
    "ds_read_b128 v[32:35], v96 offset:49152\n"
    "v_mfma_f32_16x16x32_bf16 v[28:31], v[156:159], v[36:39], v[28:31]\n"
    "ds_read_b128 v[36:39], v96 offset:50176\n"
    "s_waitcnt lgkmcnt(4)\n"
    "v_mfma_f32_16x16x32_bf16 v[0:3], a[144:147], v[40:43], v[0:3]\n"
    "v_mfma_f32_16x16x32_bf16 v[4:7], a[144:147], v[44:47], v[4:7]\n"
    ".Lqk_rt11_%=:\n"
    "s_memrealtime s[34:35]\n"
    "s_waitcnt lgkmcnt(0)\n"
    "s_sub_u32 s36, s50, s34\n"
    "s_cmp_lt_u32 s36, 600\n"
    "s_cbranch_scc1 .Lqk_rt11_%=\n"
    "buffer_load_dwordx4 v[56:59], v96, s[0:3], s43 offen sc1\n"
    "buffer_load_dwordx4 v[60:63], v96, s[0:3], s43 offen offset:1024 sc1\n"
    "buffer_load_dwordx4 v[64:67], v96, s[0:3], s44 offen sc1\n"
    "v_mfma_f32_16x16x32_bf16 v[8:11], a[224:227], v[40:43], v[8:11]\n"
    "v_mfma_f32_16x16x32_bf16 v[12:15], a[224:227], v[44:47], v[12:15]\n"
    "v_mfma_f32_16x16x32_bf16 v[16:19], a[64:67], v[40:43], v[16:19]\n"
    "v_mfma_f32_16x16x32_bf16 v[20:23], a[64:67], v[44:47], v[20:23]\n"
    "v_mfma_f32_16x16x32_bf16 v[24:27], v[160:163], v[40:43], v[24:27]\n"
    "ds_read_b128 v[40:43], v96 offset:51200\n"
    "v_mfma_f32_16x16x32_bf16 v[28:31], v[160:163], v[44:47], v[28:31]\n"
    "ds_read_b128 v[44:47], v96 offset:52224\n"
    "s_waitcnt lgkmcnt(4)\n"
    "v_mfma_f32_16x16x32_bf16 v[0:3], a[148:151], v[48:51], v[0:3]\n"
    "v_mfma_f32_16x16x32_bf16 v[4:7], a[148:151], v[52:55], v[4:7]\n"
    "v_mfma_f32_16x16x32_bf16 v[8:11], a[228:231], v[48:51], v[8:11]\n"
    "v_mfma_f32_16x16x32_bf16 v[12:15], a[228:231], v[52:55], v[12:15]\n"
    "v_mfma_f32_16x16x32_bf16 v[16:19], a[68:71], v[48:51], v[16:19]\n"
    "v_mfma_f32_16x16x32_bf16 v[20:23], a[68:71], v[52:55], v[20:23]\n"
    "v_mfma_f32_16x16x32_bf16 v[24:27], v[164:167], v[48:51], v[24:27]\n"
    "v_mfma_f32_16x16x32_bf16 v[28:31], v[164:167], v[52:55], v[28:31]\n"
    "s_waitcnt lgkmcnt(2)\n"
    "v_mfma_f32_16x16x32_bf16 v[0:3], a[152:155], v[32:35], v[0:3]\n"
    "v_mfma_f32_16x16x32_bf16 v[4:7], a[152:155], v[36:39], v[4:7]\n"
    "v_mfma_f32_16x16x32_bf16 v[8:11], a[232:235], v[32:35], v[8:11]\n"
    "v_mfma_f32_16x16x32_bf16 v[12:15], a[232:235], v[36:39], v[12:15]\n"
    "v_mfma_f32_16x16x32_bf16 v[16:19], a[72:75], v[32:35], v[16:19]\n"
    "v_mfma_f32_16x16x32_bf16 v[20:23], a[72:75], v[36:39], v[20:23]\n"
    "v_mfma_f32_16x16x32_bf16 v[24:27], v[168:171], v[32:35], v[24:27]\n"
    "v_mfma_f32_16x16x32_bf16 v[28:31], v[168:171], v[36:39], v[28:31]\n"
    "s_waitcnt lgkmcnt(0)\n"
    "v_mfma_f32_16x16x32_bf16 v[0:3], a[156:159], v[40:43], v[0:3]\n"
    "v_mfma_f32_16x16x32_bf16 v[4:7], a[156:159], v[44:47], v[4:7]\n"
    "v_mfma_f32_16x16x32_bf16 v[8:11], a[236:239], v[40:43], v[8:11]\n"
    "v_mfma_f32_16x16x32_bf16 v[12:15], a[236:239], v[44:47], v[12:15]\n"
    "v_mfma_f32_16x16x32_bf16 v[16:19], a[76:79], v[40:43], v[16:19]\n"
    "v_mfma_f32_16x16x32_bf16 v[20:23], a[76:79], v[44:47], v[20:23]\n"
    "v_mfma_f32_16x16x32_bf16 v[24:27], v[172:175], v[40:43], v[24:27]\n"
    "v_mfma_f32_16x16x32_bf16 v[28:31], v[172:175], v[44:47], v[28:31]\n"
    "ds_read_b128 v[32:35], v97 offset:0\n"
    "ds_read_b128 v[36:39], v97 offset:1024\n"
    "ds_read_b128 v[40:43], v97 offset:2048\n"
    "ds_read_b128 v[44:47], v97 offset:3072\n"
    "ds_read_b128 v[48:51], v97 offset:4096\n"
    "ds_read_b128 v[52:55], v97 offset:5120\n"
    "v_cvt_pk_bf16_f32 v68, v0, v1\n"
    "v_cvt_pk_bf16_f32 v69, v2, v3\n"
    "v_cvt_pk_bf16_f32 v70, v8, v9\n"
    "v_cvt_pk_bf16_f32 v71, v10, v11\n"
    "v_cvt_pk_bf16_f32 v72, v4, v5\n"
    "v_cvt_pk_bf16_f32 v73, v6, v7\n"
    "v_cvt_pk_bf16_f32 v74, v12, v13\n"
    "v_cvt_pk_bf16_f32 v75, v14, v15\n"
    "v_mfma_f32_16x16x32_bf16 v[108:111], v[68:71], v[84:87], 0\n"
    "v_cvt_pk_bf16_f32 v88, v24, v25\n"
    "v_cvt_pk_bf16_f32 v89, v26, v27\n"
    "v_cvt_pk_bf16_f32 v90, v28, v29\n"
    "v_cvt_pk_bf16_f32 v91, v30, v31\n"
    "v_mfma_f32_16x16x32_bf16 v[24:27], v[72:75], v[84:87], 0\n"
    "v_cvt_pk_bf16_f32 v76, v16, v17\n"
    "v_cvt_pk_bf16_f32 v77, v18, v19\n"
    "v_cvt_pk_bf16_f32 v78, v20, v21\n"
    "v_cvt_pk_bf16_f32 v79, v22, v23\n"
    "v_mfma_f32_16x16x32_bf16 v[28:31], v[68:71], v[80:83], 0\n"
    "v_mov_b64 v[0:1], 0\n"
    "v_mov_b64 v[2:3], 0\n"
    "v_mfma_f32_16x16x16_bf16 v[108:111], v[76:77], v[90:91], v[108:111]\n"
    "v_mov_b64 v[4:5], 0\n"
    "v_mov_b64 v[6:7], 0\n"
    "v_mfma_f32_16x16x16_bf16 v[24:27], v[78:79], v[90:91], v[24:27]\n"
    "v_mov_b64 v[8:9], 0\n"
    "v_mov_b64 v[10:11], 0\n"
    "v_mfma_f32_16x16x16_bf16 v[28:31], v[76:77], v[88:89], v[28:31]\n"
    "v_mov_b64 v[12:13], 0\n"
    "v_mov_b64 v[14:15], 0\n"
    "v_mov_b64 v[16:17], 0\n"
    "v_mov_b64 v[18:19], 0\n"
    "v_mov_b64 v[20:21], 0\n"
    "v_mov_b64 v[22:23], 0\n"
    "ds_write_b128 v103, v[108:111]\n"
    "ds_write_b128 v103, v[24:27] offset:64\n"
    "ds_write_b128 v103, v[28:31] offset:160\n"
    "s_branch .Lqk_take_%=\n"
    ".Lqk_retry_%=:\n"
    "s_add_u32 s32, s32, 1\n"
    "s_cmp_lt_u32 s32, 0x40000\n"
    "s_cbranch_scc1 .Lqk_sp12_%=\n"
    "v_mov_b32 v108, 1\n"
    "s_mov_b32 s36, 33728\n"
    "buffer_store_dword v108, off, s[0:3], s36 sc1\n"
    "s_waitcnt vmcnt(0)\n"
    "s_branch .Lqk_go_%=\n"
    ".Lqk_sp12_%=:\n"
    "s_nop 7\n"
    "v_mov_b64 v[0:1], 0\n"
    "v_mov_b64 v[2:3], 0\n"
    "v_mov_b64 v[4:5], 0\n"
    "v_mov_b64 v[6:7], 0\n"
    "v_mov_b64 v[8:9], 0\n"
    "v_mov_b64 v[10:11], 0\n"
    "v_mov_b64 v[12:13], 0\n"
    "v_mov_b64 v[14:15], 0\n"
    "v_mov_b64 v[16:17], 0\n"
    "v_mov_b64 v[18:19], 0\n"
    "v_mov_b64 v[20:21], 0\n"
    "v_mov_b64 v[22:23], 0\n"
    "buffer_load_dwordx4 v[56:59], v96, s[0:3], s43 offen sc1\n"
    "buffer_load_dwordx4 v[60:63], v96, s[0:3], s43 offen offset:1024 sc1\n"
    "buffer_load_dwordx4 v[64:67], v96, s[0:3], s44 offen sc1\n"
    "s_waitcnt vmcnt(0)\n"
    "s_branch .Lqk_take_%=\n"
    ".Lqk_end_%=:\n"
    "s_waitcnt vmcnt(0) lgkmcnt(0)\n"
    "s_endpgm\n"
    :: "{s[0:3]}"(s0_), "{s[4:7]}"(s4_), "{s[8:11]}"(s8_), "{s[12:15]}"(s12_), "{s[16:19]}"(s16_), "{s[20:23]}"(s20_), "{s[24:27]}"(s24_), "{s[28:31]}"(s28_), "{v[0:3]}"(v0_), "{v[4:7]}"(v4_), "{v[92:95]}"(v92_), "{v[96:99]}"(v96_), "{v[100:103]}"(v100_), "{v[104:107]}"(v104_) : "memory", "v255", "a255", "s95");
    __builtin_unreachable();
}
DEV void vo_role(const MegaArgs& A, const int L, const int h, const int rep) {
    ROLE_HEAD;
    const unsigned um = xl(RG_UM + ((8 * w + lq) * 4 + (h >> 3)) * 2048 + (h & 7) * 256 + l16 * 16, L);
    const unsigned umx = xl(RG_UMX + ((4 * (2 * w + (lq & 1)) + (l16 >> 2)) * 4 + (h >> 3)) * 512 + (h & 7) * 64 + (l & 3) * 16 + (lq >> 1) * 8, L);
    bst16(rr, um, ones); bst16(rr, um + 65536, ones); bst8(rr, umx, ones2);
    DRAIN_BARRIER();
    bst4(rr, threadIdx.x < 4 ? xl(Z_QV + l * 256 + (32 + h) * 4, L) : OOB, 1u);
    const u32x4 s0_ = {RFL(rx[0]), RFL(rx[1]), RFL(rx[2]), RFL(rx[3])};
    const u32x4 s4_ = {RFL(lo(A.Wo + h * 192 * 640)), RFL(hi(A.Wo + h * 192 * 640)), RFL(192u * 1280u), RFL(0x20000u)};
    const u32x4 s8_ = {RFL(lo(A.Wv + h * 192)), RFL(hi(A.Wv + h * 192)), RFL(WBYTES - h * 384), RFL(0x20000u)};
    const u32x4 s12_ = {RFL(w), RFL(L << 13), RFL(L), RFL(xl(RG_MEM + rep * 16384, L) + w * 2048)};
    const u32x4 s16_ = {RFL(w < 2 ? xl(RG_MEM + rep * 16384 + 8192, L) + w * 1024 : OOB), RFL(REC0 + w * 2048), RFL(QT0 + h * 32768 + 16384), RFL(xl(RG_UT + (32 * w + (h >> 3)) * 8192 + (h & 7) * 1024, L))};
    const u32x4 s20_ = {RFL(xl(Z_X + 8192, L)), RFL(xl(Z_CT + (h & 7) * 256, L)), RFL(L ? xl(Z_XN + rep * 2048, 0) : OOB), RFL(xl(Z_QW + h * 128, L))};
    const u32x4 s24_ = {RFL(DBG0 + ((L * 128 + 32 + h) * 4 + w) * 2048), RFL(0u), RFL(0u), RFL(0u)};
    const u32x4 v0_ = {(unsigned)(((3 * w + 0) * 64 + l) / 24 * 12288 + ((((3 * w + 0) * 64 + l) % 24) ^ ((((3 * w + 0) * 64 + l) / 48) & 6)) * 16 - 0), (unsigned)(((3 * w + 1) * 64 + l) / 24 * 12288 + ((((3 * w + 1) * 64 + l) % 24) ^ ((((3 * w + 1) * 64 + l) / 48) & 6)) * 16 - 1024), (unsigned)(((3 * w + 2) * 64 + l) / 24 * 12288 + ((((3 * w + 2) * 64 + l) % 24) ^ ((((3 * w + 2) * 64 + l) / 48) & 6)) * 16 - 2048), (unsigned)((8 * lq + ((l >> 2) & 1) + ((l >> 1) & 4)) * 384 + (w ^ (((8 * lq + ((l >> 2) & 1) + ((l >> 1) & 4)) >> 2) & 3)) * 32 + (l & 3) * 8 + ((w + 0) & 3) * 12288)};
    const u32x4 v4_ = {(unsigned)((8 * lq + ((l >> 2) & 1) + ((l >> 1) & 4)) * 384 + (w ^ (((8 * lq + ((l >> 2) & 1) + ((l >> 1) & 4)) >> 2) & 3)) * 32 + (l & 3) * 8 + ((w + 1) & 3) * 12288), (unsigned)((8 * lq + ((l >> 2) & 1) + ((l >> 1) & 4)) * 384 + (w ^ (((8 * lq + ((l >> 2) & 1) + ((l >> 1) & 4)) >> 2) & 3)) * 32 + (l & 3) * 8 + ((w + 2) & 3) * 12288), (unsigned)((8 * lq + ((l >> 2) & 1) + ((l >> 1) & 4)) * 384 + (w ^ (((8 * lq + ((l >> 2) & 1) + ((l >> 1) & 4)) >> 2) & 3)) * 32 + (l & 3) * 8 + ((w + 3) & 3) * 12288), (unsigned)((8 * lq + ((l >> 2) & 1) + ((l >> 1) & 4)) * 384 + (w ^ (((8 * lq + ((l >> 2) & 1) + ((l >> 1) & 4)) >> 2) & 3)) * 32 + (l & 3) * 8)};
    const u32x4 v8_ = {(unsigned)(0), (unsigned)(0), (unsigned)(0), (unsigned)(0)};
    const u32x4 v12_ = {(unsigned)((4 * 0 + lq) * 1280 + 256 * w + ((l16 ^ (2 * ((4 * 0 + lq) & 7))) * 16) - 0), (unsigned)((4 * 1 + lq) * 1280 + 256 * w + ((l16 ^ (2 * ((4 * 1 + lq) & 7))) * 16) - 1024), (unsigned)((4 * 2 + lq) * 1280 + 256 * w + ((l16 ^ (2 * ((4 * 2 + lq) & 7))) * 16) - 2048), (unsigned)((4 * 3 + lq) * 1280 + 256 * w + ((l16 ^ (2 * ((4 * 3 + lq) & 7))) * 16) - 3072)};
    const u32x4 v16_ = {(unsigned)((4 * w + lq) * 1280 + 1024 + ((l16 ^ (2 * ((4 * w + lq) & 7))) * 16)), (unsigned)(w * 8192 + (l >> 2) * 256 + ((l >> 2) & 7) * 32 + (l & 3) * 8), (unsigned)(32768 + (l >> 2) * 256 + (((l >> 2) & 7) ^ (2 * w)) * 32 + (l & 3) * 8), (unsigned)(0u)};
    const u32x4 v92_ = {(unsigned)(l * 16 + ((w + 0) & 3) * 13312), (unsigned)(l * 16 + ((w + 1) & 3) * 13312), (unsigned)(l * 16 + ((w + 2) & 3) * 13312), (unsigned)(l * 16 + ((w + 3) & 3) * 13312)};
    const u32x4 v96_ = {(unsigned)(l * 16), (unsigned)(l * 16 + 64512 + w * 20480), (unsigned)((l & 2) ? 0xeeeeu : 0x4444u), (unsigned)(61440 + 512 * w + (l16 >> 2) * 64 + (l & 1) * 32 + sw2(lq) * 8 + (l & 2) * 2)};
    const u32x4 v100_ = {(unsigned)(61440 + 2048 + 256 * w + (l16 >> 2) * 64 + (l & 1) * 32 + sw2(lq) * 8 + (l & 2) * 2), (unsigned)(61440 + 512 * (lq >> 1) + 128 * (lq & 1) + (l & 4) * 8 + (l & 8) * 32 + (l & 3) * 8), (unsigned)(um), (unsigned)(umx)};
    const u32x4 v104_ = {(unsigned)(xl(RG_UTX + ((8 * w + (l16 >> 2)) * 4 + (h >> 3)) * 2048 + (h & 1) * 1024 + ((h & 7) >> 1) * 256 + lq * 64 + (l & 3) * 16, L)), (unsigned)(l < 20 ? (l >> 2) * 128 + (l & 3) * 4 : 0), (unsigned)((l & 3) * 4), (unsigned)(0u)};
    asm volatile(
    "s_mov_b32 s28, 0\n"
    "s_cmp_eq_u32 s13, 0\n"
    "s_cbranch_scc1 .Lvo_ho1_%=\n"
    "s_sleep 35\n"
    "s_sleep 35\n"
    "s_sleep 35\n"
    "s_sleep 35\n"
    "s_sleep 35\n"
    "s_sleep 35\n"
    "s_sleep 35\n"
    "s_sleep 35\n"
    "s_sleep 35\n"
    "s_sleep 35\n"
    "s_sleep 35\n"
    "s_sleep 35\n"
    "s_sleep 35\n"
    "s_sleep 35\n"
    ".Lvo_ho1_%=:\n"
    "s_mul_i32 s41, s12, 3072\n"
    "s_lshl_b32 s42, s12, 13\n"
    "s_lshl_b32 s43, s12, 10\n"
    "s_mov_b32 s49, 0\n"
    "s_add_u32 m0, s42, 0\n"
    "s_nop 0\n"
    "buffer_load_dwordx4 v12, s[4:7], s49 offen offset:0 nt lds\n"
    "buffer_load_dwordx4 v13, s[4:7], s49 offen offset:1024 nt lds\n"
    "buffer_load_dwordx4 v14, s[4:7], s49 offen offset:2048 nt lds\n"
    "buffer_load_dwordx4 v15, s[4:7], s49 offen offset:3072 nt lds\n"
    "s_add_u32 m0, s43, 32768\n"
    "s_nop 0\n"
    "buffer_load_dwordx4 v16, s[4:7], s49 offen nt lds\n"
    "s_mov_b32 s49, 81920\n"
    "s_add_u32 m0, s42, 4096\n"
    "s_nop 0\n"
    "buffer_load_dwordx4 v12, s[4:7], s49 offen offset:0 nt lds\n"
    "buffer_load_dwordx4 v13, s[4:7], s49 offen offset:1024 nt lds\n"
    "buffer_load_dwordx4 v14, s[4:7], s49 offen offset:2048 nt lds\n"
    "buffer_load_dwordx4 v15, s[4:7], s49 offen offset:3072 nt lds\n"
    "s_add_u32 m0, s43, 36864\n"
    "s_nop 0\n"
    "buffer_load_dwordx4 v16, s[4:7], s49 offen nt lds\n"
    "s_mov_b32 s49, 20480\n"
    "s_add_u32 m0, s42, 49152\n"
    "s_nop 0\n"
    "buffer_load_dwordx4 v12, s[4:7], s49 offen offset:0 nt lds\n"
    "buffer_load_dwordx4 v13, s[4:7], s49 offen offset:1024 nt lds\n"
    "buffer_load_dwordx4 v14, s[4:7], s49 offen offset:2048 nt lds\n"
    "buffer_load_dwordx4 v15, s[4:7], s49 offen offset:3072 nt lds\n"
    "s_add_u32 m0, s43, 81920\n"
    "s_nop 0\n"
    "buffer_load_dwordx4 v16, s[4:7], s49 offen nt lds\n"
    "s_mov_b32 s49, 102400\n"
    "s_add_u32 m0, s42, 53248\n"
    "s_nop 0\n"
    "buffer_load_dwordx4 v12, s[4:7], s49 offen offset:0 nt lds\n"
    "buffer_load_dwordx4 v13, s[4:7], s49 offen offset:1024 nt lds\n"
    "buffer_load_dwordx4 v14, s[4:7], s49 offen offset:2048 nt lds\n"
    "buffer_load_dwordx4 v15, s[4:7], s49 offen offset:3072 nt lds\n"
    "s_add_u32 m0, s43, 86016\n"
    "s_nop 0\n"
    "buffer_load_dwordx4 v16, s[4:7], s49 offen nt lds\n"
    "s_mov_b32 s49, 40960\n"
    "s_add_u32 m0, s42, 98304\n"
    "s_nop 0\n"
    "buffer_load_dwordx4 v12, s[4:7], s49 offen offset:0 nt lds\n"
    "buffer_load_dwordx4 v13, s[4:7], s49 offen offset:1024 nt lds\n"
    "buffer_load_dwordx4 v14, s[4:7], s49 offen offset:2048 nt lds\n"
    "buffer_load_dwordx4 v15, s[4:7], s49 offen offset:3072 nt lds\n"
    "s_add_u32 m0, s43, 131072\n"
    "s_nop 0\n"
    "buffer_load_dwordx4 v16, s[4:7], s49 offen nt lds\n"
    "s_mov_b32 s49, 122880\n"
    "s_add_u32 m0, s42, 102400\n"
    "s_nop 0\n"
    "buffer_load_dwordx4 v12, s[4:7], s49 offen offset:0 nt lds\n"
    "buffer_load_dwordx4 v13, s[4:7], s49 offen offset:1024 nt lds\n"
    "buffer_load_dwordx4 v14, s[4:7], s49 offen offset:2048 nt lds\n"
    "buffer_load_dwordx4 v15, s[4:7], s49 offen offset:3072 nt lds\n"
    "s_add_u32 m0, s43, 135168\n"
    "s_nop 0\n"
    "buffer_load_dwordx4 v16, s[4:7], s49 offen nt lds\n"
    "s_waitcnt vmcnt(20)\n"
    "s_barrier\n"
    "v_add_u32 v24, 0, v17\n"
    "v_add_u32 v32, 0, v18\n"
    "v_xor_b32 v33, 32, v32\n"
    "v_xor_b32 v25, 32, v24\n"
    "v_xor_b32 v26, 64, v24\n"
    "v_xor_b32 v27, 96, v24\n"
    "v_xor_b32 v28, 128, v24\n"
    "v_xor_b32 v29, 160, v24\n"
    "v_xor_b32 v30, 192, v24\n"
    "v_xor_b32 v31, 224, v24\n"
    "ds_read_b64_tr_b16 a[0:1], v24 offset:0\n"
    "ds_read_b64_tr_b16 a[2:3], v24 offset:4096\n"
    "ds_read_b64_tr_b16 a[24:25], v25 offset:0\n"
    "ds_read_b64_tr_b16 a[26:27], v25 offset:4096\n"
    "ds_read_b64_tr_b16 a[48:49], v26 offset:0\n"
    "ds_read_b64_tr_b16 a[50:51], v26 offset:4096\n"
    "ds_read_b64_tr_b16 a[72:73], v27 offset:0\n"
    "ds_read_b64_tr_b16 a[74:75], v27 offset:4096\n"
    "ds_read_b64_tr_b16 a[96:97], v28 offset:0\n"
    "ds_read_b64_tr_b16 a[98:99], v28 offset:4096\n"
    "ds_read_b64_tr_b16 a[120:121], v29 offset:0\n"
    "ds_read_b64_tr_b16 a[122:123], v29 offset:4096\n"
    "ds_read_b64_tr_b16 a[144:145], v30 offset:0\n"
    "ds_read_b64_tr_b16 a[146:147], v30 offset:4096\n"
    "ds_read_b64_tr_b16 a[168:169], v31 offset:0\n"
    "ds_read_b64_tr_b16 a[170:171], v31 offset:4096\n"
    "ds_read_b64_tr_b16 a[192:193], v32 offset:0\n"
    "ds_read_b64_tr_b16 a[194:195], v32 offset:4096\n"
    "ds_read_b64_tr_b16 a[216:217], v33 offset:0\n"
    "ds_read_b64_tr_b16 a[218:219], v33 offset:4096\n"
    "s_waitcnt lgkmcnt(0)\n"
    "s_barrier\n"
    "s_mov_b32 s49, 61440\n"
    "s_add_u32 m0, s42, 0\n"
    "s_nop 0\n"
    "buffer_load_dwordx4 v12, s[4:7], s49 offen offset:0 nt lds\n"
    "buffer_load_dwordx4 v13, s[4:7], s49 offen offset:1024 nt lds\n"
    "buffer_load_dwordx4 v14, s[4:7], s49 offen offset:2048 nt lds\n"
    "buffer_load_dwordx4 v15, s[4:7], s49 offen offset:3072 nt lds\n"
    "s_add_u32 m0, s43, 32768\n"
    "s_nop 0\n"
    "buffer_load_dwordx4 v16, s[4:7], s49 offen nt lds\n"
    "s_mov_b32 s49, 143360\n"
    "s_add_u32 m0, s42, 4096\n"
    "s_nop 0\n"
    "buffer_load_dwordx4 v12, s[4:7], s49 offen offset:0 nt lds\n"
    "buffer_load_dwordx4 v13, s[4:7], s49 offen offset:1024 nt lds\n"
    "buffer_load_dwordx4 v14, s[4:7], s49 offen offset:2048 nt lds\n"
    "buffer_load_dwordx4 v15, s[4:7], s49 offen offset:3072 nt lds\n"
    "s_add_u32 m0, s43, 36864\n"
    "s_nop 0\n"
    "buffer_load_dwordx4 v16, s[4:7], s49 offen nt lds\n"
    "s_waitcnt vmcnt(20)\n"
    "s_barrier\n"
    "v_add_u32 v24, 49152, v17\n"
    "v_add_u32 v32, 49152, v18\n"
    "v_xor_b32 v33, 32, v32\n"
    "v_xor_b32 v25, 32, v24\n"
    "v_xor_b32 v26, 64, v24\n"
    "v_xor_b32 v27, 96, v24\n"
    "v_xor_b32 v28, 128, v24\n"
    "v_xor_b32 v29, 160, v24\n"
    "v_xor_b32 v30, 192, v24\n"
    "v_xor_b32 v31, 224, v24\n"
    "ds_read_b64_tr_b16 a[4:5], v24 offset:0\n"
    "ds_read_b64_tr_b16 a[6:7], v24 offset:4096\n"
    "ds_read_b64_tr_b16 a[28:29], v25 offset:0\n"
    "ds_read_b64_tr_b16 a[30:31], v25 offset:4096\n"
    "ds_read_b64_tr_b16 a[52:53], v26 offset:0\n"
    "ds_read_b64_tr_b16 a[54:55], v26 offset:4096\n"
    "ds_read_b64_tr_b16 a[76:77], v27 offset:0\n"
    "ds_read_b64_tr_b16 a[78:79], v27 offset:4096\n"
    "ds_read_b64_tr_b16 a[100:101], v28 offset:0\n"
    "ds_read_b64_tr_b16 a[102:103], v28 offset:4096\n"
    "ds_read_b64_tr_b16 a[124:125], v29 offset:0\n"
    "ds_read_b64_tr_b16 a[126:127], v29 offset:4096\n"
    "ds_read_b64_tr_b16 a[148:149], v30 offset:0\n"
    "ds_read_b64_tr_b16 a[150:151], v30 offset:4096\n"
    "ds_read_b64_tr_b16 a[172:173], v31 offset:0\n"
    "ds_read_b64_tr_b16 a[174:175], v31 offset:4096\n"
    "ds_read_b64_tr_b16 a[196:197], v32 offset:0\n"
    "ds_read_b64_tr_b16 a[198:199], v32 offset:4096\n"
    "ds_read_b64_tr_b16 a[220:221], v33 offset:0\n"
    "ds_read_b64_tr_b16 a[222:223], v33 offset:4096\n"
    "s_waitcnt lgkmcnt(0)\n"
    "s_barrier\n"
    "s_mov_b32 s49, 163840\n"
    "s_add_u32 m0, s42, 49152\n"
    "s_nop 0\n"
    "buffer_load_dwordx4 v12, s[4:7], s49 offen offset:0 nt lds\n"
    "buffer_load_dwordx4 v13, s[4:7], s49 offen offset:1024 nt lds\n"
    "buffer_load_dwordx4 v14, s[4:7], s49 offen offset:2048 nt lds\n"
    "buffer_load_dwordx4 v15, s[4:7], s49 offen offset:3072 nt lds\n"
    "s_add_u32 m0, s43, 81920\n"
    "s_nop 0\n"
    "buffer_load_dwordx4 v16, s[4:7], s49 offen nt lds\n"
    "s_mov_b32 s49, 184320\n"
    "s_add_u32 m0, s42, 53248\n"
    "s_nop 0\n"
    "buffer_load_dwordx4 v12, s[4:7], s49 offen offset:0 nt lds\n"
    "buffer_load_dwordx4 v13, s[4:7], s49 offen offset:1024 nt lds\n"
    "buffer_load_dwordx4 v14, s[4:7], s49 offen offset:2048 nt lds\n"
    "buffer_load_dwordx4 v15, s[4:7], s49 offen offset:3072 nt lds\n"
    "s_add_u32 m0, s43, 86016\n"
    "s_nop 0\n"
    "buffer_load_dwordx4 v16, s[4:7], s49 offen nt lds\n"
    "s_waitcnt vmcnt(20)\n"
    "s_barrier\n"
    "v_add_u32 v24, 98304, v17\n"
    "v_add_u32 v32, 98304, v18\n"
    "v_xor_b32 v33, 32, v32\n"
    "v_xor_b32 v25, 32, v24\n"
    "v_xor_b32 v26, 64, v24\n"
    "v_xor_b32 v27, 96, v24\n"
    "v_xor_b32 v28, 128, v24\n"
    "v_xor_b32 v29, 160, v24\n"
    "v_xor_b32 v30, 192, v24\n"
    "v_xor_b32 v31, 224, v24\n"
    "ds_read_b64_tr_b16 a[8:9], v24 offset:0\n"
    "ds_read_b64_tr_b16 a[10:11], v24 offset:4096\n"
    "ds_read_b64_tr_b16 a[32:33], v25 offset:0\n"
    "ds_read_b64_tr_b16 a[34:35], v25 offset:4096\n"
    "ds_read_b64_tr_b16 a[56:57], v26 offset:0\n"
    "ds_read_b64_tr_b16 a[58:59], v26 offset:4096\n"
    "ds_read_b64_tr_b16 a[80:81], v27 offset:0\n"
    "ds_read_b64_tr_b16 a[82:83], v27 offset:4096\n"
    "ds_read_b64_tr_b16 a[104:105], v28 offset:0\n"
    "ds_read_b64_tr_b16 a[106:107], v28 offset:4096\n"
    "ds_read_b64_tr_b16 a[128:129], v29 offset:0\n"
    "ds_read_b64_tr_b16 a[130:131], v29 offset:4096\n"
    "ds_read_b64_tr_b16 a[152:153], v30 offset:0\n"
    "ds_read_b64_tr_b16 a[154:155], v30 offset:4096\n"
    "ds_read_b64_tr_b16 a[176:177], v31 offset:0\n"
    "ds_read_b64_tr_b16 a[178:179], v31 offset:4096\n"
    "ds_read_b64_tr_b16 a[200:201], v32 offset:0\n"
    "ds_read_b64_tr_b16 a[202:203], v32 offset:4096\n"
    "ds_read_b64_tr_b16 a[224:225], v33 offset:0\n"
    "ds_read_b64_tr_b16 a[226:227], v33 offset:4096\n"
    "s_waitcnt lgkmcnt(0)\n"
    "s_barrier\n"
    "s_mov_b32 s49, 204800\n"
    "s_add_u32 m0, s42, 98304\n"
    "s_nop 0\n"
    "buffer_load_dwordx4 v12, s[4:7], s49 offen offset:0 nt lds\n"
    "buffer_load_dwordx4 v13, s[4:7], s49 offen offset:1024 nt lds\n"
    "buffer_load_dwordx4 v14, s[4:7], s49 offen offset:2048 nt lds\n"
    "buffer_load_dwordx4 v15, s[4:7], s49 offen offset:3072 nt lds\n"
    "s_add_u32 m0, s43, 131072\n"
    "s_nop 0\n"
    "buffer_load_dwordx4 v16, s[4:7], s49 offen nt lds\n"
    "s_mov_b32 s49, 225280\n"
    "s_add_u32 m0, s42, 102400\n"
    "s_nop 0\n"
    "buffer_load_dwordx4 v12, s[4:7], s49 offen offset:0 nt lds\n"
    "buffer_load_dwordx4 v13, s[4:7], s49 offen offset:1024 nt lds\n"
    "buffer_load_dwordx4 v14, s[4:7], s49 offen offset:2048 nt lds\n"
    "buffer_load_dwordx4 v15, s[4:7], s49 offen offset:3072 nt lds\n"
    "s_add_u32 m0, s43, 135168\n"
    "s_nop 0\n"
    "buffer_load_dwordx4 v16, s[4:7], s49 offen nt lds\n"
    "s_waitcnt vmcnt(20)\n"
    "s_barrier\n"
    "v_add_u32 v24, 0, v17\n"
    "v_add_u32 v32, 0, v18\n"
    "v_xor_b32 v33, 32, v32\n"
    "v_xor_b32 v25, 32, v24\n"
    "v_xor_b32 v26, 64, v24\n"
    "v_xor_b32 v27, 96, v24\n"
    "v_xor_b32 v28, 128, v24\n"
    "v_xor_b32 v29, 160, v24\n"
    "v_xor_b32 v30, 192, v24\n"
    "v_xor_b32 v31, 224, v24\n"
    "ds_read_b64_tr_b16 a[12:13], v24 offset:0\n"
    "ds_read_b64_tr_b16 a[14:15], v24 offset:4096\n"
    "ds_read_b64_tr_b16 a[36:37], v25 offset:0\n"
    "ds_read_b64_tr_b16 a[38:39], v25 offset:4096\n"
    "ds_read_b64_tr_b16 a[60:61], v26 offset:0\n"
    "ds_read_b64_tr_b16 a[62:63], v26 offset:4096\n"
    "ds_read_b64_tr_b16 a[84:85], v27 offset:0\n"
    "ds_read_b64_tr_b16 a[86:87], v27 offset:4096\n"
    "ds_read_b64_tr_b16 a[108:109], v28 offset:0\n"
    "ds_read_b64_tr_b16 a[110:111], v28 offset:4096\n"
    "ds_read_b64_tr_b16 a[132:133], v29 offset:0\n"
    "ds_read_b64_tr_b16 a[134:135], v29 offset:4096\n"
    "ds_read_b64_tr_b16 a[156:157], v30 offset:0\n"
    "ds_read_b64_tr_b16 a[158:159], v30 offset:4096\n"
    "ds_read_b64_tr_b16 a[180:181], v31 offset:0\n"
    "ds_read_b64_tr_b16 a[182:183], v31 offset:4096\n"
    "ds_read_b64_tr_b16 a[204:205], v32 offset:0\n"
    "ds_read_b64_tr_b16 a[206:207], v32 offset:4096\n"
    "ds_read_b64_tr_b16 a[228:229], v33 offset:0\n"
    "ds_read_b64_tr_b16 a[230:231], v33 offset:4096\n"
    "s_waitcnt lgkmcnt(0)\n"
    "s_barrier\n"
    "s_mov_b32 s49, 0\n"
    "s_add_u32 m0, s41, 0\n"
    "s_nop 0\n"
    "buffer_load_dwordx4 v0, s[8:11], s49 offen offset:0 nt lds\n"
    "buffer_load_dwordx4 v1, s[8:11], s49 offen offset:1024 nt lds\n"
    "buffer_load_dwordx4 v2, s[8:11], s49 offen offset:2048 nt lds\n"
    "s_mov_b32 s49, 1572864\n"
    "s_add_u32 m0, s41, 12288\n"
    "s_nop 0\n"
    "buffer_load_dwordx4 v0, s[8:11], s49 offen offset:0 nt lds\n"
    "buffer_load_dwordx4 v1, s[8:11], s49 offen offset:1024 nt lds\n"
    "buffer_load_dwordx4 v2, s[8:11], s49 offen offset:2048 nt lds\n"
    "s_mov_b32 s49, 3145728\n"
    "s_add_u32 m0, s41, 24576\n"
    "s_nop 0\n"
    "buffer_load_dwordx4 v0, s[8:11], s49 offen offset:0 nt lds\n"
    "buffer_load_dwordx4 v1, s[8:11], s49 offen offset:1024 nt lds\n"
    "buffer_load_dwordx4 v2, s[8:11], s49 offen offset:2048 nt lds\n"
    "s_mov_b32 s49, 4718592\n"
    "s_add_u32 m0, s41, 36864\n"
    "s_nop 0\n"
    "buffer_load_dwordx4 v0, s[8:11], s49 offen offset:0 nt lds\n"
    "buffer_load_dwordx4 v1, s[8:11], s49 offen offset:1024 nt lds\n"
    "buffer_load_dwordx4 v2, s[8:11], s49 offen offset:2048 nt lds\n"
    "s_waitcnt vmcnt(22)\n"
    "s_barrier\n"
    "v_add_u32 v24, 49152, v17\n"
    "v_add_u32 v32, 49152, v18\n"
    "v_xor_b32 v33, 32, v32\n"
    "v_xor_b32 v25, 32, v24\n"
    "v_xor_b32 v26, 64, v24\n"
    "v_xor_b32 v27, 96, v24\n"
    "v_xor_b32 v28, 128, v24\n"
    "v_xor_b32 v29, 160, v24\n"
    "v_xor_b32 v30, 192, v24\n"
    "v_xor_b32 v31, 224, v24\n"
    "ds_read_b64_tr_b16 a[16:17], v24 offset:0\n"
    "ds_read_b64_tr_b16 a[18:19], v24 offset:4096\n"
    "ds_read_b64_tr_b16 a[40:41], v25 offset:0\n"
    "ds_read_b64_tr_b16 a[42:43], v25 offset:4096\n"
    "ds_read_b64_tr_b16 a[64:65], v26 offset:0\n"
    "ds_read_b64_tr_b16 a[66:67], v26 offset:4096\n"
    "ds_read_b64_tr_b16 a[88:89], v27 offset:0\n"
    "ds_read_b64_tr_b16 a[90:91], v27 offset:4096\n"
    "ds_read_b64_tr_b16 a[112:113], v28 offset:0\n"
    "ds_read_b64_tr_b16 a[114:115], v28 offset:4096\n"
    "ds_read_b64_tr_b16 a[136:137], v29 offset:0\n"
    "ds_read_b64_tr_b16 a[138:139], v29 offset:4096\n"
    "ds_read_b64_tr_b16 a[160:161], v30 offset:0\n"
    "ds_read_b64_tr_b16 a[162:163], v30 offset:4096\n"
    "ds_read_b64_tr_b16 a[184:185], v31 offset:0\n"
    "ds_read_b64_tr_b16 a[186:187], v31 offset:4096\n"
    "ds_read_b64_tr_b16 a[208:209], v32 offset:0\n"
    "ds_read_b64_tr_b16 a[210:211], v32 offset:4096\n"
    "ds_read_b64_tr_b16 a[232:233], v33 offset:0\n"
    "ds_read_b64_tr_b16 a[234:235], v33 offset:4096\n"
    "s_waitcnt lgkmcnt(0)\n"
    "s_barrier\n"
    "s_mov_b32 s49, 393216\n"
    "s_add_u32 m0, s41, 49152\n"
    "s_nop 0\n"
    "buffer_load_dwordx4 v0, s[8:11], s49 offen offset:0 nt lds\n"
    "buffer_load_dwordx4 v1, s[8:11], s49 offen offset:1024 nt lds\n"
    "buffer_load_dwordx4 v2, s[8:11], s49 offen offset:2048 nt lds\n"
    "s_mov_b32 s49, 1966080\n"
    "s_add_u32 m0, s41, 61440\n"
    "s_nop 0\n"
    "buffer_load_dwordx4 v0, s[8:11], s49 offen offset:0 nt lds\n"
    "buffer_load_dwordx4 v1, s[8:11], s49 offen offset:1024 nt lds\n"
    "buffer_load_dwordx4 v2, s[8:11], s49 offen offset:2048 nt lds\n"
    "s_mov_b32 s49, 3538944\n"
    "s_add_u32 m0, s41, 73728\n"
    "s_nop 0\n"
    "buffer_load_dwordx4 v0, s[8:11], s49 offen offset:0 nt lds\n"
    "buffer_load_dwordx4 v1, s[8:11], s49 offen offset:1024 nt lds\n"
    "buffer_load_dwordx4 v2, s[8:11], s49 offen offset:2048 nt lds\n"
    "s_mov_b32 s49, 5111808\n"
    "s_add_u32 m0, s41, 86016\n"
    "s_nop 0\n"
    "buffer_load_dwordx4 v0, s[8:11], s49 offen offset:0 nt lds\n"
    "buffer_load_dwordx4 v1, s[8:11], s49 offen offset:1024 nt lds\n"
    "buffer_load_dwordx4 v2, s[8:11], s49 offen offset:2048 nt lds\n"
    "s_waitcnt vmcnt(24)\n"
    "s_barrier\n"
    "v_add_u32 v24, 98304, v17\n"
    "v_add_u32 v32, 98304, v18\n"
    "v_xor_b32 v33, 32, v32\n"
    "v_xor_b32 v25, 32, v24\n"
    "v_xor_b32 v26, 64, v24\n"
    "v_xor_b32 v27, 96, v24\n"
    "v_xor_b32 v28, 128, v24\n"
    "v_xor_b32 v29, 160, v24\n"
    "v_xor_b32 v30, 192, v24\n"
    "v_xor_b32 v31, 224, v24\n"
    "ds_read_b64_tr_b16 a[20:21], v24 offset:0\n"
    "ds_read_b64_tr_b16 a[22:23], v24 offset:4096\n"
    "ds_read_b64_tr_b16 a[44:45], v25 offset:0\n"
    "ds_read_b64_tr_b16 a[46:47], v25 offset:4096\n"
    "ds_read_b64_tr_b16 a[68:69], v26 offset:0\n"
    "ds_read_b64_tr_b16 a[70:71], v26 offset:4096\n"
    "ds_read_b64_tr_b16 a[92:93], v27 offset:0\n"
    "ds_read_b64_tr_b16 a[94:95], v27 offset:4096\n"
    "ds_read_b64_tr_b16 a[116:117], v28 offset:0\n"
    "ds_read_b64_tr_b16 a[118:119], v28 offset:4096\n"
    "ds_read_b64_tr_b16 a[140:141], v29 offset:0\n"
    "ds_read_b64_tr_b16 a[142:143], v29 offset:4096\n"
    "ds_read_b64_tr_b16 a[164:165], v30 offset:0\n"
    "ds_read_b64_tr_b16 a[166:167], v30 offset:4096\n"
    "ds_read_b64_tr_b16 a[188:189], v31 offset:0\n"
    "ds_read_b64_tr_b16 a[190:191], v31 offset:4096\n"
    "ds_read_b64_tr_b16 a[212:213], v32 offset:0\n"
    "ds_read_b64_tr_b16 a[214:215], v32 offset:4096\n"
    "ds_read_b64_tr_b16 a[236:237], v33 offset:0\n"
    "ds_read_b64_tr_b16 a[238:239], v33 offset:4096\n"
    "s_waitcnt lgkmcnt(0)\n"
    "s_barrier\n"
    "s_mov_b32 s49, 786432\n"
    "s_add_u32 m0, s41, 98304\n"
    "s_nop 0\n"
    "buffer_load_dwordx4 v0, s[8:11], s49 offen offset:0 nt lds\n"
    "buffer_load_dwordx4 v1, s[8:11], s49 offen offset:1024 nt lds\n"
    "buffer_load_dwordx4 v2, s[8:11], s49 offen offset:2048 nt lds\n"
    "s_mov_b32 s49, 2359296\n"
    "s_add_u32 m0, s41, 110592\n"
    "s_nop 0\n"
    "buffer_load_dwordx4 v0, s[8:11], s49 offen offset:0 nt lds\n"
    "buffer_load_dwordx4 v1, s[8:11], s49 offen offset:1024 nt lds\n"
    "buffer_load_dwordx4 v2, s[8:11], s49 offen offset:2048 nt lds\n"
    "s_mov_b32 s49, 3932160\n"
    "s_add_u32 m0, s41, 122880\n"
    "s_nop 0\n"
    "buffer_load_dwordx4 v0, s[8:11], s49 offen offset:0 nt lds\n"
    "buffer_load_dwordx4 v1, s[8:11], s49 offen offset:1024 nt lds\n"
    "buffer_load_dwordx4 v2, s[8:11], s49 offen offset:2048 nt lds\n"
    "s_mov_b32 s49, 5505024\n"
    "s_add_u32 m0, s41, 135168\n"
    "s_nop 0\n"
    "buffer_load_dwordx4 v0, s[8:11], s49 offen offset:0 nt lds\n"
    "buffer_load_dwordx4 v1, s[8:11], s49 offen offset:1024 nt lds\n"
    "buffer_load_dwordx4 v2, s[8:11], s49 offen offset:2048 nt lds\n"
    "s_waitcnt vmcnt(24)\n"
    "s_barrier\n"
    "v_add_u32 v8, 0, v3\n"
    "ds_read_b64_tr_b16 a[240:241], v8 offset:0\n"
    "ds_read_b64_tr_b16 a[242:243], v8 offset:768\n"
    "ds_read_b64_tr_b16 v[176:177], v8 offset:128\n"
    "ds_read_b64_tr_b16 v[178:179], v8 offset:896\n"
    "ds_read_b64_tr_b16 v[12:13], v8 offset:256\n"
    "ds_read_b64_tr_b16 v[14:15], v8 offset:1024\n"
    "v_add_u32 v9, 0, v4\n"
    "ds_read_b64_tr_b16 v[112:113], v9 offset:0\n"
    "ds_read_b64_tr_b16 v[114:115], v9 offset:768\n"
    "ds_read_b64_tr_b16 v[192:193], v9 offset:128\n"
    "ds_read_b64_tr_b16 v[194:195], v9 offset:896\n"
    "ds_read_b64_tr_b16 v[28:29], v9 offset:256\n"
    "ds_read_b64_tr_b16 v[30:31], v9 offset:1024\n"
    "v_add_u32 v10, 0, v5\n"
    "ds_read_b64_tr_b16 v[128:129], v10 offset:0\n"
    "ds_read_b64_tr_b16 v[130:131], v10 offset:768\n"
    "ds_read_b64_tr_b16 v[208:209], v10 offset:128\n"
    "ds_read_b64_tr_b16 v[210:211], v10 offset:896\n"
    "ds_read_b64_tr_b16 v[44:45], v10 offset:256\n"
    "ds_read_b64_tr_b16 v[46:47], v10 offset:1024\n"
    "v_add_u32 v11, 0, v6\n"
    "ds_read_b64_tr_b16 v[144:145], v11 offset:0\n"
    "ds_read_b64_tr_b16 v[146:147], v11 offset:768\n"
    "ds_read_b64_tr_b16 v[224:225], v11 offset:128\n"
    "ds_read_b64_tr_b16 v[226:227], v11 offset:896\n"
    "ds_read_b64_tr_b16 v[60:61], v11 offset:256\n"
    "ds_read_b64_tr_b16 v[62:63], v11 offset:1024\n"
    "s_waitcnt lgkmcnt(0)\n"
    "s_barrier\n"
    "s_mov_b32 s49, 1179648\n"
    "s_add_u32 m0, s41, 0\n"
    "s_nop 0\n"
    "buffer_load_dwordx4 v0, s[8:11], s49 offen offset:0 nt lds\n"
    "buffer_load_dwordx4 v1, s[8:11], s49 offen offset:1024 nt lds\n"
    "buffer_load_dwordx4 v2, s[8:11], s49 offen offset:2048 nt lds\n"
    "s_mov_b32 s49, 2752512\n"
    "s_add_u32 m0, s41, 12288\n"
    "s_nop 0\n"
    "buffer_load_dwordx4 v0, s[8:11], s49 offen offset:0 nt lds\n"
    "buffer_load_dwordx4 v1, s[8:11], s49 offen offset:1024 nt lds\n"
    "buffer_load_dwordx4 v2, s[8:11], s49 offen offset:2048 nt lds\n"
    "s_mov_b32 s49, 4325376\n"
    "s_add_u32 m0, s41, 24576\n"
    "s_nop 0\n"
    "buffer_load_dwordx4 v0, s[8:11], s49 offen offset:0 nt lds\n"
    "buffer_load_dwordx4 v1, s[8:11], s49 offen offset:1024 nt lds\n"
    "buffer_load_dwordx4 v2, s[8:11], s49 offen offset:2048 nt lds\n"
    "s_mov_b32 s49, 5898240\n"
    "s_add_u32 m0, s41, 36864\n"
    "s_nop 0\n"
    "buffer_load_dwordx4 v0, s[8:11], s49 offen offset:0 nt lds\n"
    "buffer_load_dwordx4 v1, s[8:11], s49 offen offset:1024 nt lds\n"
    "buffer_load_dwordx4 v2, s[8:11], s49 offen offset:2048 nt lds\n"
    "s_waitcnt vmcnt(24)\n"
    "s_barrier\n"
    "v_add_u32 v8, 49152, v3\n"
    "ds_read_b64_tr_b16 a[244:245], v8 offset:0\n"
    "ds_read_b64_tr_b16 a[246:247], v8 offset:768\n"
    "ds_read_b64_tr_b16 v[180:181], v8 offset:128\n"
    "ds_read_b64_tr_b16 v[182:183], v8 offset:896\n"
    "ds_read_b64_tr_b16 v[16:17], v8 offset:256\n"
    "ds_read_b64_tr_b16 v[18:19], v8 offset:1024\n"
    "v_add_u32 v9, 49152, v4\n"
    "ds_read_b64_tr_b16 v[116:117], v9 offset:0\n"
    "ds_read_b64_tr_b16 v[118:119], v9 offset:768\n"
    "ds_read_b64_tr_b16 v[196:197], v9 offset:128\n"
    "ds_read_b64_tr_b16 v[198:199], v9 offset:896\n"
    "ds_read_b64_tr_b16 v[32:33], v9 offset:256\n"
    "ds_read_b64_tr_b16 v[34:35], v9 offset:1024\n"
    "v_add_u32 v10, 49152, v5\n"
    "ds_read_b64_tr_b16 v[132:133], v10 offset:0\n"
    "ds_read_b64_tr_b16 v[134:135], v10 offset:768\n"
    "ds_read_b64_tr_b16 v[212:213], v10 offset:128\n"
    "ds_read_b64_tr_b16 v[214:215], v10 offset:896\n"
    "ds_read_b64_tr_b16 v[48:49], v10 offset:256\n"
    "ds_read_b64_tr_b16 v[50:51], v10 offset:1024\n"
    "v_add_u32 v11, 49152, v6\n"
    "ds_read_b64_tr_b16 v[148:149], v11 offset:0\n"
    "ds_read_b64_tr_b16 v[150:151], v11 offset:768\n"
    "ds_read_b64_tr_b16 v[228:229], v11 offset:128\n"
    "ds_read_b64_tr_b16 v[230:231], v11 offset:896\n"
    "ds_read_b64_tr_b16 v[64:65], v11 offset:256\n"
    "ds_read_b64_tr_b16 v[66:67], v11 offset:1024\n"
    "s_waitcnt lgkmcnt(0)\n"
    "s_barrier\n"
    "s_mov_b32 s49, 6291456\n"
    "s_add_u32 m0, s41, 49152\n"
    "s_nop 0\n"
    "buffer_load_dwordx4 v0, s[8:11], s49 offen offset:0 nt lds\n"
    "buffer_load_dwordx4 v1, s[8:11], s49 offen offset:1024 nt lds\n"
    "buffer_load_dwordx4 v2, s[8:11], s49 offen offset:2048 nt lds\n"
    "s_mov_b32 s49, 6684672\n"
    "s_add_u32 m0, s41, 61440\n"
    "s_nop 0\n"
    "buffer_load_dwordx4 v0, s[8:11], s49 offen offset:0 nt lds\n"
    "buffer_load_dwordx4 v1, s[8:11], s49 offen offset:1024 nt lds\n"
    "buffer_load_dwordx4 v2, s[8:11], s49 offen offset:2048 nt lds\n"
    "s_mov_b32 s49, 7077888\n"
    "s_add_u32 m0, s41, 73728\n"
    "s_nop 0\n"
    "buffer_load_dwordx4 v0, s[8:11], s49 offen offset:0 nt lds\n"
    "buffer_load_dwordx4 v1, s[8:11], s49 offen offset:1024 nt lds\n"
    "buffer_load_dwordx4 v2, s[8:11], s49 offen offset:2048 nt lds\n"
    "s_mov_b32 s49, 7471104\n"
    "s_add_u32 m0, s41, 86016\n"
    "s_nop 0\n"
    "buffer_load_dwordx4 v0, s[8:11], s49 offen offset:0 nt lds\n"
    "buffer_load_dwordx4 v1, s[8:11], s49 offen offset:1024 nt lds\n"
    "buffer_load_dwordx4 v2, s[8:11], s49 offen offset:2048 nt lds\n"
    "s_waitcnt vmcnt(24)\n"
    "s_barrier\n"
    "v_add_u32 v8, 98304, v3\n"
    "ds_read_b64_tr_b16 a[248:249], v8 offset:0\n"
    "ds_read_b64_tr_b16 a[250:251], v8 offset:768\n"
    "ds_read_b64_tr_b16 v[184:185], v8 offset:128\n"
    "ds_read_b64_tr_b16 v[186:187], v8 offset:896\n"
    "ds_read_b64_tr_b16 v[20:21], v8 offset:256\n"
    "ds_read_b64_tr_b16 v[22:23], v8 offset:1024\n"
    "v_add_u32 v9, 98304, v4\n"
    "ds_read_b64_tr_b16 v[120:121], v9 offset:0\n"
    "ds_read_b64_tr_b16 v[122:123], v9 offset:768\n"
    "ds_read_b64_tr_b16 v[200:201], v9 offset:128\n"
    "ds_read_b64_tr_b16 v[202:203], v9 offset:896\n"
    "ds_read_b64_tr_b16 v[36:37], v9 offset:256\n"
    "ds_read_b64_tr_b16 v[38:39], v9 offset:1024\n"
    "v_add_u32 v10, 98304, v5\n"
    "ds_read_b64_tr_b16 v[136:137], v10 offset:0\n"
    "ds_read_b64_tr_b16 v[138:139], v10 offset:768\n"
    "ds_read_b64_tr_b16 v[216:217], v10 offset:128\n"
    "ds_read_b64_tr_b16 v[218:219], v10 offset:896\n"
    "ds_read_b64_tr_b16 v[52:53], v10 offset:256\n"
    "ds_read_b64_tr_b16 v[54:55], v10 offset:1024\n"
    "v_add_u32 v11, 98304, v6\n"
    "ds_read_b64_tr_b16 v[152:153], v11 offset:0\n"
    "ds_read_b64_tr_b16 v[154:155], v11 offset:768\n"
    "ds_read_b64_tr_b16 v[232:233], v11 offset:128\n"
    "ds_read_b64_tr_b16 v[234:235], v11 offset:896\n"
    "ds_read_b64_tr_b16 v[68:69], v11 offset:256\n"
    "ds_read_b64_tr_b16 v[70:71], v11 offset:1024\n"
    "s_waitcnt lgkmcnt(0)\n"
    "s_barrier\n"
    "s_waitcnt vmcnt(12)\n"
    "s_barrier\n"
    "v_add_u32 v8, 0, v3\n"
    "ds_read_b64_tr_b16 a[252:253], v8 offset:0\n"
    "ds_read_b64_tr_b16 a[254:255], v8 offset:768\n"
    "ds_read_b64_tr_b16 v[188:189], v8 offset:128\n"
    "ds_read_b64_tr_b16 v[190:191], v8 offset:896\n"
    "ds_read_b64_tr_b16 v[24:25], v8 offset:256\n"
    "ds_read_b64_tr_b16 v[26:27], v8 offset:1024\n"
    "v_add_u32 v9, 0, v4\n"
    "ds_read_b64_tr_b16 v[124:125], v9 offset:0\n"
    "ds_read_b64_tr_b16 v[126:127], v9 offset:768\n"
    "ds_read_b64_tr_b16 v[204:205], v9 offset:128\n"
    "ds_read_b64_tr_b16 v[206:207], v9 offset:896\n"
    "ds_read_b64_tr_b16 v[40:41], v9 offset:256\n"
    "ds_read_b64_tr_b16 v[42:43], v9 offset:1024\n"
    "v_add_u32 v10, 0, v5\n"
    "ds_read_b64_tr_b16 v[140:141], v10 offset:0\n"
    "ds_read_b64_tr_b16 v[142:143], v10 offset:768\n"
    "ds_read_b64_tr_b16 v[220:221], v10 offset:128\n"
    "ds_read_b64_tr_b16 v[222:223], v10 offset:896\n"
    "ds_read_b64_tr_b16 v[56:57], v10 offset:256\n"
    "ds_read_b64_tr_b16 v[58:59], v10 offset:1024\n"
    "v_add_u32 v11, 0, v6\n"
    "ds_read_b64_tr_b16 v[156:157], v11 offset:0\n"
    "ds_read_b64_tr_b16 v[158:159], v11 offset:768\n"
    "ds_read_b64_tr_b16 v[236:237], v11 offset:128\n"
    "ds_read_b64_tr_b16 v[238:239], v11 offset:896\n"
    "ds_read_b64_tr_b16 v[72:73], v11 offset:256\n"
    "ds_read_b64_tr_b16 v[74:75], v11 offset:1024\n"
    "s_waitcnt lgkmcnt(0)\n"
    "s_barrier\n"
    "s_waitcnt vmcnt(0)\n"
    "s_barrier\n"
    "v_add_u32 v8, 49152, v7\n"
    "ds_read_b64_tr_b16 v[160:161], v8 offset:0\n"
    "ds_read_b64_tr_b16 v[162:163], v8 offset:768\n"
    "ds_read_b64_tr_b16 v[240:241], v8 offset:128\n"
    "ds_read_b64_tr_b16 v[242:243], v8 offset:896\n"
    "ds_read_b64_tr_b16 v[76:77], v8 offset:256\n"
    "ds_read_b64_tr_b16 v[78:79], v8 offset:1024\n"
    "v_add_u32 v9, 61440, v7\n"
    "ds_read_b64_tr_b16 v[164:165], v9 offset:0\n"
    "ds_read_b64_tr_b16 v[166:167], v9 offset:768\n"
    "ds_read_b64_tr_b16 v[244:245], v9 offset:128\n"
    "ds_read_b64_tr_b16 v[246:247], v9 offset:896\n"
    "ds_read_b64_tr_b16 v[80:81], v9 offset:256\n"
    "ds_read_b64_tr_b16 v[82:83], v9 offset:1024\n"
    "v_add_u32 v10, 73728, v7\n"
    "ds_read_b64_tr_b16 v[168:169], v10 offset:0\n"
    "ds_read_b64_tr_b16 v[170:171], v10 offset:768\n"
    "ds_read_b64_tr_b16 v[248:249], v10 offset:128\n"
    "ds_read_b64_tr_b16 v[250:251], v10 offset:896\n"
    "ds_read_b64_tr_b16 v[84:85], v10 offset:256\n"
    "ds_read_b64_tr_b16 v[86:87], v10 offset:1024\n"
    "v_add_u32 v11, 86016, v7\n"
    "ds_read_b64_tr_b16 v[172:173], v11 offset:0\n"
    "ds_read_b64_tr_b16 v[174:175], v11 offset:768\n"
    "ds_read_b64_tr_b16 v[252:253], v11 offset:128\n"
    "ds_read_b64_tr_b16 v[254:255], v11 offset:896\n"
    "ds_read_b64_tr_b16 v[88:89], v11 offset:256\n"
    "ds_read_b64_tr_b16 v[90:91], v11 offset:1024\n"
    "s_waitcnt lgkmcnt(0)\n"
    "s_barrier\n"
    "ds_write_b128 v97, v[12:15] offset:0\n"
    "ds_write_b128 v97, v[16:19] offset:1024\n"
    "ds_write_b128 v97, v[20:23] offset:2048\n"
    "ds_write_b128 v97, v[24:27] offset:3072\n"
    "ds_write_b128 v97, v[28:31] offset:4096\n"
    "ds_write_b128 v97, v[32:35] offset:5120\n"
    "ds_write_b128 v97, v[36:39] offset:6144\n"
    "ds_write_b128 v97, v[40:43] offset:7168\n"
    "ds_write_b128 v97, v[44:47] offset:8192\n"
    "ds_write_b128 v97, v[48:51] offset:9216\n"
    "ds_write_b128 v97, v[52:55] offset:10240\n"
    "ds_write_b128 v97, v[56:59] offset:11264\n"
    "ds_write_b128 v97, v[60:63] offset:12288\n"
    "ds_write_b128 v97, v[64:67] offset:13312\n"
    "ds_write_b128 v97, v[68:71] offset:14336\n"
    "ds_write_b128 v97, v[72:75] offset:15360\n"
    "ds_write_b128 v97, v[76:79] offset:16384\n"
    "ds_write_b128 v97, v[80:83] offset:17408\n"
    "ds_write_b128 v97, v[84:87] offset:18432\n"
    "ds_write_b128 v97, v[88:91] offset:19456\n"
    "s_mov_b32 s34, -4\n"
    "s_lshl_b32 s48, s12, 11\n"
    "v_lshrrev_b32 v4, 2, v96\n"
    ".Lvo_po2_%=:\n"
    "buffer_load_dword v8, v4, s[0:3], s21 offen sc1\n"
    "s_waitcnt vmcnt(0)\n"
    "v_cmp_gt_i32 vcc, 1, v8\n"
    "s_nop 0\n"
    "s_cbranch_vccz .Lvo_pd3_%=\n"
    "s_add_u32 s28, s28, 1\n"
    "s_cmp_lt_u32 s28, 0x40000\n"
    "s_cbranch_scc1 .Lvo_sp4_%=\n"
    "v_mov_b32 v8, 30\n"
    "s_mov_b32 s32, 33728\n"
    "buffer_store_dword v8, off, s[0:3], s32 sc1\n"
    "s_waitcnt vmcnt(0)\n"
    "s_branch .Lvo_pd3_%=\n"
    ".Lvo_sp4_%=:\n"
    "s_sleep 4\n"
    "s_branch .Lvo_po2_%=\n"
    ".Lvo_pd3_%=:\n"
    ".Lvo_po5_%=:\n"
    "buffer_load_dword v8, v105, s[0:3], s22 offen sc1\n"
    "s_waitcnt vmcnt(0)\n"
    "v_cmp_gt_i32 vcc, s14, v8\n"
    "s_nop 0\n"
    "s_cbranch_vccz .Lvo_pd6_%=\n"
    "s_add_u32 s28, s28, 1\n"
    "s_cmp_lt_u32 s28, 0x40000\n"
    "s_cbranch_scc1 .Lvo_sp7_%=\n"
    "v_mov_b32 v8, 31\n"
    "s_mov_b32 s32, 33728\n"
    "buffer_store_dword v8, off, s[0:3], s32 sc1\n"
    "s_waitcnt vmcnt(0)\n"
    "s_branch .Lvo_pd6_%=\n"
    ".Lvo_sp7_%=:\n"
    "s_sleep 4\n"
    "s_branch .Lvo_po5_%=\n"
    ".Lvo_pd6_%=:\n"
    "s_add_u32 s43, s17, s13\n"
    "s_add_u32 m0, s48, 0\n"
    "s_nop 0\n"
    "buffer_load_dwordx4 v96, s[0:3], s43 offen sc1 lds\n"
    "buffer_load_dwordx4 v96, s[0:3], s43 offen offset:1024 sc1 lds\n"
    "s_add_u32 m0, s48, 13312\n"
    "s_add_u32 s43, s43, 0x4000\n"
    "buffer_load_dwordx4 v96, s[0:3], s43 offen sc1 lds\n"
    "buffer_load_dwordx4 v96, s[0:3], s43 offen offset:1024 sc1 lds\n"
    "s_add_u32 m0, s48, 26624\n"
    "s_add_u32 s43, s43, 0x4000\n"
    "buffer_load_dwordx4 v96, s[0:3], s43 offen sc1 lds\n"
    "buffer_load_dwordx4 v96, s[0:3], s43 offen offset:1024 sc1 lds\n"
    "s_add_u32 m0, s48, 39936\n"
    "s_add_u32 s43, s43, 0x4000\n"
    "buffer_load_dwordx4 v96, s[0:3], s43 offen sc1 lds\n"
    "buffer_load_dwordx4 v96, s[0:3], s43 offen offset:1024 sc1 lds\n"
    "s_add_u32 m0, s48, 53248\n"
    "s_add_u32 s43, s43, 0x4000\n"
    "buffer_load_dwordx4 v96, s[0:3], s43 offen sc1 lds\n"
    "buffer_load_dwordx4 v96, s[0:3], s43 offen offset:1024 sc1 lds\n"
    "s_memrealtime s[30:31]\n"
    "s_waitcnt vmcnt(0) lgkmcnt(0)\n"
    "s_sub_u32 s46, s30, 1440\n"
    "s_mov_b32 s34, -3\n"
    "s_mov_b32 s35, -5\n"
    "s_mov_b32 s45, s20\n"
    "s_mov_b32 s38, 2130706432\n"
    "s_mov_b32 s39, 2130706432\n"
    "s_branch .Lvo_vph_%=\n"
    ".Lvo_take_%=:\n"
    "s_waitcnt vmcnt(4) lgkmcnt(0)\n"
    "ds_write_b128 v92, v[72:75] offset:8192\n"
    "v_smfmac_f32_16x16x64_bf16 v[84:87], v[72:75], v[0:7], v98\n"
    "s_waitcnt vmcnt(3)\n"
    "ds_write_b128 v92, v[76:79] offset:9216\n"
    "v_max3_u32 v108, v72, v74, v76\n"
    "s_waitcnt vmcnt(2)\n"
    "ds_write_b128 v92, v[80:83] offset:10240\n"
    "v_max3_u32 v108, v108, v78, v80\n"
    "v_smfmac_f32_16x16x64_bf16 v[64:67], v[72:75], a[240:247], v98\n"
    "v_max_u32 v108, v108, v82\n"
    "v_cmp_eq_u32 vcc, -1, v108\n"
    "s_nop 0\n"
    "s_cbranch_vccnz .Lvo_retry_%=\n"
    ".Lvo_go_%=:\n"
    "s_waitcnt lgkmcnt(0)\n"
    "s_barrier\n"
    "ds_read_b128 v[88:91], v93 offset:8192\n"
    "ds_read_b128 v[80:83], v93 offset:9216\n"
    "v_smfmac_f32_16x16x64_bf16 v[68:71], v[72:75], v[176:183], v98\n"
    "ds_read_b128 v[72:75], v94 offset:8192\n"
    "v_smfmac_f32_16x16x64_bf16 v[84:87], v[76:79], v[8:15], v98\n"
    "s_waitcnt vmcnt(0)\n"
    "v_cmp_gt_i32 vcc, s50, v110\n"
    "v_smfmac_f32_16x16x64_bf16 v[64:67], v[76:79], a[248:255], v98\n"
    "s_cbranch_vccnz .Lvo_wq_%=\n"
    ".Lvo_wqb_%=:\n"
    "buffer_load_dwordx4 v[32:35], v96, s[0:3], s44 offen offset:0 sc1\n"
    "v_smfmac_f32_16x16x64_bf16 v[68:71], v[76:79], v[184:191], v98\n"
    "ds_read_b128 v[76:79], v94 offset:9216\n"
    "s_waitcnt lgkmcnt(3)\n"
    "v_smfmac_f32_16x16x64_bf16 v[84:87], v[88:91], v[16:23], v98\n"
    "ds_read_b128 v[0:3], v97 offset:12288\n"
    "v_smfmac_f32_16x16x64_bf16 v[64:67], v[88:91], v[112:119], v98\n"
    "ds_read_b128 v[4:7], v97 offset:13312\n"
    "v_smfmac_f32_16x16x64_bf16 v[68:71], v[88:91], v[192:199], v98\n"
    "ds_read_b128 v[88:91], v95 offset:8192\n"
    "s_waitcnt lgkmcnt(5)\n"
    "v_smfmac_f32_16x16x64_bf16 v[84:87], v[80:83], v[24:31], v98\n"
    "ds_read_b128 v[8:11], v97 offset:14336\n"
    "buffer_load_dwordx4 v[36:39], v96, s[0:3], s44 offen offset:1024 sc1\n"
    "v_smfmac_f32_16x16x64_bf16 v[64:67], v[80:83], v[120:127], v98\n"
    "ds_read_b128 v[12:15], v97 offset:15360\n"
    "v_smfmac_f32_16x16x64_bf16 v[68:71], v[80:83], v[200:207], v98\n"
    "ds_read_b128 v[80:83], v95 offset:9216\n"
    "s_waitcnt lgkmcnt(7)\n"
    "v_smfmac_f32_16x16x64_bf16 v[84:87], v[72:75], v[48:55], v98\n"
    "ds_read_b128 v[16:19], v97 offset:16384\n"
    "v_smfmac_f32_16x16x64_bf16 v[64:67], v[72:75], v[128:135], v98\n"
    "ds_read_b128 v[20:23], v97 offset:17408\n"
    "buffer_load_dwordx4 v[40:43], v96, s[0:3], s44 offen offset:2048 sc1\n"
    "v_smfmac_f32_16x16x64_bf16 v[68:71], v[72:75], v[208:215], v98\n"
    "ds_read_b128 v[72:75], v96 offset:10240\n"
    "s_waitcnt lgkmcnt(9)\n"
    "v_smfmac_f32_16x16x64_bf16 v[84:87], v[76:79], v[56:63], v98\n"
    "ds_read_b128 v[24:27], v97 offset:18432\n"
    "v_smfmac_f32_16x16x64_bf16 v[64:67], v[76:79], v[136:143], v98\n"
    "ds_read_b128 v[28:31], v97 offset:19456\n"
    "v_mov_b64 v[48:49], 0\n"
    "v_smfmac_f32_16x16x64_bf16 v[68:71], v[76:79], v[216:223], v98\n"
    "ds_read_b128 v[76:79], v96 offset:23552\n"
    "s_waitcnt lgkmcnt(9)\n"
    "v_smfmac_f32_16x16x64_bf16 v[84:87], v[88:91], v[0:7], v98\n"
    "buffer_load_dwordx4 v[44:47], v96, s[0:3], s44 offen offset:3072 sc1\n"
    "v_cmp_gt_i32 vcc, s51, v111\n"
    "v_smfmac_f32_16x16x64_bf16 v[64:67], v[88:91], v[144:151], v98\n"
    "s_cbranch_vccnz .Lvo_wx_%=\n"
    ".Lvo_wxb_%=:\n"
    "s_add_u32 m0, s48, 0\n"
    "v_smfmac_f32_16x16x64_bf16 v[68:71], v[88:91], v[224:231], v98\n"
    "s_waitcnt lgkmcnt(3)\n"
    "v_mov_b64 v[50:51], 0\n"
    "v_smfmac_f32_16x16x64_bf16 v[84:87], v[80:83], v[8:15], v98\n"
    "v_mov_b64 v[52:53], 0\n"
    "v_mov_b64 v[54:55], 0\n"
    "v_smfmac_f32_16x16x64_bf16 v[84:87], v[72:75], v[16:23], v98\n"
    "s_waitcnt lgkmcnt(0)\n"
    "buffer_load_dwordx4 v96, s[0:3], s43 offen sc1 lds\n"
    "v_smfmac_f32_16x16x64_bf16 v[84:87], v[76:79], v[24:31], v98\n"
    "v_mov_b64 v[56:57], 0\n"
    "v_mov_b64 v[58:59], 0\n"
    "v_smfmac_f32_16x16x64_bf16 v[64:67], v[80:83], v[152:159], v98\n"
    "v_mov_b64 v[60:61], 0\n"
    "v_mov_b64 v[62:63], 0\n"
    "v_smfmac_f32_16x16x64_bf16 v[64:67], v[72:75], v[160:167], v98\n"
    "v_mov_b64 v[24:25], 0\n"
    "v_mov_b64 v[26:27], 0\n"
    "v_smfmac_f32_16x16x64_bf16 v[64:67], v[76:79], v[168:175], v98\n"
    "buffer_load_dwordx4 v96, s[0:3], s43 offen offset:1024 sc1 lds\n"
    "v_mov_b64 v[28:29], 0\n"
    "v_smfmac_f32_16x16x64_bf16 v[68:71], v[80:83], v[232:239], v98\n"
    "v_mov_b64 v[30:31], 0\n"
    "v_smfmac_f32_16x16x64_bf16 v[68:71], v[72:75], v[240:247], v98\n"
    "v_smfmac_f32_16x16x64_bf16 v[68:71], v[76:79], v[248:255], v98\n"
    "v_pk_add_f32 v[84:85], v[84:85], v[86:87]\n"
    "v_cvt_pk_bf16_f32 v84, v84, v85\n"
    "ds_write_b32 v100, v84 offset:0\n"
    "v_pk_add_f32 v[64:65], v[64:65], v[66:67]\n"
    "v_cvt_pk_bf16_f32 v64, v64, v65\n"
    "ds_write_b32 v99, v64 offset:0\n"
    "s_nop 1\n"
    "v_pk_add_f32 v[68:69], v[68:69], v[70:71]\n"
    "v_cvt_pk_bf16_f32 v68, v68, v69\n"
    "ds_write_b32 v99, v68 offset:256\n"
    "s_waitcnt lgkmcnt(0)\n"
    "s_barrier\n"
    "ds_read_b64_tr_b16 v[72:73], v101 offset:0\n"
    "ds_read_b64_tr_b16 v[74:75], v101 offset:64\n"
    "ds_read_b64_tr_b16 v[76:77], v101 offset:1024\n"
    "ds_read_b64_tr_b16 v[78:79], v101 offset:1088\n"
    "ds_read_b64_tr_b16 v[80:81], v101 offset:2048\n"
    "ds_read_b64_tr_b16 v[82:83], v101 offset:2112\n"
    "v_mov_b64 v[64:65], 0\n"
    "v_mov_b64 v[66:67], 0\n"
    "v_mov_b64 v[68:69], 0\n"
    "v_mov_b64 v[70:71], 0\n"
    "v_mov_b64 v[84:85], 0\n"
    "v_mov_b64 v[86:87], 0\n"
    "v_mov_b64 v[88:89], 0\n"
    "v_mov_b64 v[90:91], 0\n"
    "s_waitcnt lgkmcnt(4)\n"
    "v_smfmac_f32_16x16x64_bf16 v[24:27], v[72:75], a[192:199], v98\n"
    "s_add_u32 m0, s48, 13312\n"
    "s_add_u32 s43, s43, 0x4000\n"
    "v_smfmac_f32_16x16x64_bf16 v[28:31], v[72:75], a[216:223], v98\n"
    "s_waitcnt lgkmcnt(2)\n"
    "buffer_load_dwordx4 v96, s[0:3], s43 offen sc1 lds\n"
    "v_smfmac_f32_16x16x64_bf16 v[24:27], v[76:79], a[200:207], v98\n"
    "ds_read_b128 v[0:3], v96 offset:11264\n"
    "v_smfmac_f32_16x16x64_bf16 v[28:31], v[76:79], a[224:231], v98\n"
    "s_waitcnt lgkmcnt(1)\n"
    "ds_read_b128 v[4:7], v96 offset:12288\n"
    "v_smfmac_f32_16x16x64_bf16 v[24:27], v[80:83], a[208:215], v98\n"
    "ds_read_b128 v[8:11], v96 offset:24576\n"
    "v_smfmac_f32_16x16x64_bf16 v[28:31], v[80:83], a[232:239], v98\n"
    "buffer_load_dwordx4 v96, s[0:3], s43 offen offset:1024 sc1 lds\n"
    "s_add_u32 m0, s48, 26624\n"
    "v_smfmac_f32_16x16x64_bf16 v[48:51], v[72:75], a[0:7], v98\n"
    "s_add_u32 s43, s43, 0x4000\n"
    "ds_read_b128 v[12:15], v96 offset:25600\n"
    "v_smfmac_f32_16x16x64_bf16 v[48:51], v[76:79], a[8:15], v98\n"
    "ds_read_b128 v[16:19], v96 offset:37888\n"
    "v_smfmac_f32_16x16x64_bf16 v[48:51], v[80:83], a[16:23], v98\n"
    "ds_read_b128 v[20:23], v96 offset:38912\n"
    "v_smfmac_f32_16x16x64_bf16 v[52:55], v[72:75], a[24:31], v98\n"
    "buffer_load_dwordx4 v96, s[0:3], s43 offen sc1 lds\n"
    "v_smfmac_f32_16x16x64_bf16 v[52:55], v[76:79], a[32:39], v98\n"
    "v_smfmac_f32_16x16x64_bf16 v[52:55], v[80:83], a[40:47], v98\n"
    "v_smfmac_f32_16x16x64_bf16 v[56:59], v[72:75], a[48:55], v98\n"
    "v_add_f32 v24, v24, v26\n"
    "v_add_f32 v25, v25, v27\n"
    "v_smfmac_f32_16x16x64_bf16 v[56:59], v[76:79], a[56:63], v98\n"
    "v_cvt_pk_bf16_f32 v108, v24, v25\n"
    "v_add_f32 v28, v28, v30\n"
    "v_smfmac_f32_16x16x64_bf16 v[56:59], v[80:83], a[64:71], v98\n"
    "v_add_f32 v29, v29, v31\n"
    "v_cvt_pk_bf16_f32 v109, v28, v29\n"
    "v_smfmac_f32_16x16x64_bf16 v[60:63], v[72:75], a[72:79], v98\n"
    "v_smfmac_f32_16x16x64_bf16 v[60:63], v[76:79], a[80:87], v98\n"
    "v_permlane16_swap_b32 v108, v109\n"
    "v_smfmac_f32_16x16x64_bf16 v[60:63], v[80:83], a[88:95], v98\n"
    "v_min_u32 v108, -2, v108\n"
    "buffer_store_dwordx2 v[108:109], v103, s[0:3], s36 offen sc1\n"
    "v_smfmac_f32_16x16x64_bf16 v[64:67], v[72:75], a[96:103], v98\n"
    "v_add_f32 v48, v48, v50\n"
    "v_add_f32 v49, v49, v51\n"
    "v_smfmac_f32_16x16x64_bf16 v[64:67], v[76:79], a[104:111], v98\n"
    "v_cvt_pk_bf16_f32 v48, v48, v49\n"
    "v_add_f32 v52, v52, v54\n"
    "v_smfmac_f32_16x16x64_bf16 v[64:67], v[80:83], a[112:119], v98\n"
    "v_add_f32 v53, v53, v55\n"
    "v_cvt_pk_bf16_f32 v49, v52, v53\n"
    "v_smfmac_f32_16x16x64_bf16 v[68:71], v[72:75], a[120:127], v98\n"
    "v_add_f32 v56, v56, v58\n"
    "v_add_f32 v57, v57, v59\n"
    "v_smfmac_f32_16x16x64_bf16 v[68:71], v[76:79], a[128:135], v98\n"
    "v_cvt_pk_bf16_f32 v50, v56, v57\n"
    "ds_read_b128 v[24:27], v96 offset:51200\n"
    "v_smfmac_f32_16x16x64_bf16 v[68:71], v[80:83], a[136:143], v98\n"
    "ds_read_b128 v[28:31], v96 offset:52224\n"
    "v_smfmac_f32_16x16x64_bf16 v[84:87], v[72:75], a[144:151], v98\n"
    "v_permlane32_swap_b32 v48, v50\n"
    "v_smfmac_f32_16x16x64_bf16 v[84:87], v[76:79], a[152:159], v98\n"
    "v_add_f32 v60, v60, v62\n"
    "v_add_f32 v61, v61, v63\n"
    "v_smfmac_f32_16x16x64_bf16 v[84:87], v[80:83], a[160:167], v98\n"
    "v_cvt_pk_bf16_f32 v51, v60, v61\n"
    "buffer_load_dwordx4 v96, s[0:3], s43 offen offset:1024 sc1 lds\n"
    "v_smfmac_f32_16x16x64_bf16 v[88:91], v[72:75], a[168:175], v98\n"
    "v_smfmac_f32_16x16x64_bf16 v[88:91], v[76:79], a[176:183], v98\n"
    "v_permlane32_swap_b32 v49, v51\n"
    "v_smfmac_f32_16x16x64_bf16 v[88:91], v[80:83], a[184:191], v98\n"
    ".Lvo_uph_%=:\n"
    "s_waitcnt lgkmcnt(0)\n"
    "v_mfma_f32_16x16x32_bf16 v[56:59], v[0:3], a[0:3], 0\n"
    "v_permlane16_swap_b32 v48, v49\n"
    "v_mfma_f32_16x16x32_bf16 v[60:63], v[4:7], a[0:3], 0\n"
    "v_permlane16_swap_b32 v50, v51\n"
    "v_mfma_f32_16x16x32_bf16 v[56:59], v[8:11], a[4:7], v[56:59]\n"
    "v_min_u32 v48, -2, v48\n"
    "v_min_u32 v50, -2, v50\n"
    "v_mfma_f32_16x16x32_bf16 v[60:63], v[12:15], a[4:7], v[60:63]\n"
    "buffer_store_dwordx4 v[48:51], v102, s[0:3], s36 offen sc1\n"
    "v_add_f32 v64, v64, v66\n"
    "v_mfma_f32_16x16x32_bf16 v[56:59], v[16:19], a[8:11], v[56:59]\n"
    "v_add_f32 v65, v65, v67\n"
    "v_cvt_pk_bf16_f32 v64, v64, v65\n"
    "v_mfma_f32_16x16x32_bf16 v[60:63], v[20:23], a[8:11], v[60:63]\n"
    "v_add_f32 v68, v68, v70\n"
    "v_add_f32 v69, v69, v71\n"
    "v_mfma_f32_16x16x32_bf16 v[56:59], v[24:27], a[12:15], v[56:59]\n"
    "v_cvt_pk_bf16_f32 v65, v68, v69\n"
    "v_add_f32 v84, v84, v86\n"
    "s_waitcnt vmcnt(8)\n"
    "v_swap_b32 v34, v36\n"
    "v_swap_b32 v35, v37\n"
    "v_swap_b32 v42, v44\n"
    "v_swap_b32 v43, v45\n"
    "v_mfma_f32_16x16x32_bf16 v[60:63], v[28:31], a[12:15], v[60:63]\n"
    "v_add_f32 v85, v85, v87\n"
    "v_cvt_pk_bf16_f32 v66, v84, v85\n"
    "v_mfma_f32_16x16x32_bf16 v[56:59], v[32:35], a[16:19], v[56:59]\n"
    "s_add_u32 m0, s48, 39936\n"
    "s_add_u32 s43, s43, 0x4000\n"
    "v_mfma_f32_16x16x32_bf16 v[60:63], v[36:39], a[16:19], v[60:63]\n"
    "v_permlane32_swap_b32 v64, v66\n"
    "v_mfma_f32_16x16x32_bf16 v[56:59], v[40:43], a[20:23], v[56:59]\n"
    "v_add_f32 v88, v88, v90\n"
    "v_add_f32 v89, v89, v91\n"
    "v_mfma_f32_16x16x32_bf16 v[60:63], v[44:47], a[20:23], v[60:63]\n"
    "v_cvt_pk_bf16_f32 v67, v88, v89\n"
    "buffer_load_dwordx4 v96, s[0:3], s43 offen sc1 lds\n"
    "v_mfma_f32_16x16x32_bf16 v[48:51], v[0:3], a[24:27], 0\n"
    "v_mfma_f32_16x16x32_bf16 v[52:55], v[4:7], a[24:27], 0\n"
    "v_permlane32_swap_b32 v65, v67\n"
    "v_mfma_f32_16x16x32_bf16 v[48:51], v[8:11], a[28:31], v[48:51]\n"
    "v_cvt_pk_bf16_f32 v72, v56, v57\n"
    "v_cvt_pk_bf16_f32 v74, v58, v59\n"
    "v_mfma_f32_16x16x32_bf16 v[52:55], v[12:15], a[28:31], v[52:55]\n"
    "v_permlane16_swap_b32 v64, v65\n"
    "v_mfma_f32_16x16x32_bf16 v[48:51], v[16:19], a[32:35], v[48:51]\n"
    "v_permlane16_swap_b32 v66, v67\n"
    "v_mfma_f32_16x16x32_bf16 v[52:55], v[20:23], a[32:35], v[52:55]\n"
    "v_min_u32 v64, -2, v64\n"
    "v_min_u32 v66, -2, v66\n"
    "v_mfma_f32_16x16x32_bf16 v[48:51], v[24:27], a[36:39], v[48:51]\n"
    "buffer_store_dwordx4 v[64:67], v102, s[0:3], s37 offen sc1\n"
    "v_cvt_pk_bf16_f32 v73, v60, v61\n"
    "v_mfma_f32_16x16x32_bf16 v[52:55], v[28:31], a[36:39], v[52:55]\n"
    "s_cmp_eq_u32 s34, 63\n"
    "s_cbranch_scc1 .Lvo_end_%=\n"
    "v_mfma_f32_16x16x32_bf16 v[48:51], v[32:35], a[40:43], v[48:51]\n"
    "v_cvt_pk_bf16_f32 v75, v62, v63\n"
    "v_mfma_f32_16x16x32_bf16 v[52:55], v[36:39], a[40:43], v[52:55]\n"
    "v_mfma_f32_16x16x32_bf16 v[48:51], v[40:43], a[44:47], v[48:51]\n"
    "v_mfma_f32_16x16x32_bf16 v[52:55], v[44:47], a[44:47], v[52:55]\n"
    "v_mfma_f32_16x16x32_bf16 v[56:59], v[0:3], a[48:51], 0\n"
    "v_mfma_f32_16x16x32_bf16 v[60:63], v[4:7], a[48:51], 0\n"
    "v_mfma_f32_16x16x32_bf16 v[56:59], v[8:11], a[52:55], v[56:59]\n"
    "buffer_load_dwordx4 v96, s[0:3], s43 offen offset:1024 sc1 lds\n"
    "s_add_u32 m0, s48, 53248\n"
    "v_mfma_f32_16x16x32_bf16 v[60:63], v[12:15], a[52:55], v[60:63]\n"
    "s_add_u32 s43, s43, 0x4000\n"
    "v_mfma_f32_16x16x32_bf16 v[56:59], v[16:19], a[56:59], v[56:59]\n"
    "v_mfma_f32_16x16x32_bf16 v[60:63], v[20:23], a[56:59], v[60:63]\n"
    "v_mfma_f32_16x16x32_bf16 v[56:59], v[24:27], a[60:63], v[56:59]\n"
    "v_cvt_pk_bf16_f32 v76, v48, v49\n"
    "v_cvt_pk_bf16_f32 v78, v50, v51\n"
    "v_mfma_f32_16x16x32_bf16 v[60:63], v[28:31], a[60:63], v[60:63]\n"
    "v_cvt_pk_bf16_f32 v77, v52, v53\n"
    "v_cvt_pk_bf16_f32 v79, v54, v55\n"
    "v_mfma_f32_16x16x32_bf16 v[56:59], v[32:35], a[64:67], v[56:59]\n"
    "buffer_load_dwordx4 v96, s[0:3], s43 offen sc1 lds\n"
    "v_mov_b64 v[88:89], -1\n"
    "v_mfma_f32_16x16x32_bf16 v[60:63], v[36:39], a[64:67], v[60:63]\n"
    "v_mov_b64 v[90:91], -1\n"
    "v_mfma_f32_16x16x32_bf16 v[56:59], v[40:43], a[68:71], v[56:59]\n"
    "v_mfma_f32_16x16x32_bf16 v[60:63], v[44:47], a[68:71], v[60:63]\n"
    "v_mfma_f32_16x16x32_bf16 v[48:51], v[0:3], a[72:75], 0\n"
    "buffer_load_dwordx4 v96, s[0:3], s43 offen offset:1024 sc1 lds\n"
    "v_mfma_f32_16x16x32_bf16 v[52:55], v[4:7], a[72:75], 0\n"
    "v_mfma_f32_16x16x32_bf16 v[48:51], v[8:11], a[76:79], v[48:51]\n"
    "v_mfma_f32_16x16x32_bf16 v[52:55], v[12:15], a[76:79], v[52:55]\n"
    "v_mfma_f32_16x16x32_bf16 v[48:51], v[16:19], a[80:83], v[48:51]\n"
    "buffer_store_dwordx4 v[72:75], v96, s[0:3], s40 offen sc1\n"
    "s_add_u32 s40, s40, 0x10000\n"
    "v_mfma_f32_16x16x32_bf16 v[52:55], v[20:23], a[80:83], v[52:55]\n"
    "v_mfma_f32_16x16x32_bf16 v[48:51], v[24:27], a[84:87], v[48:51]\n"
    "v_cvt_pk_bf16_f32 v80, v56, v57\n"
    "v_cvt_pk_bf16_f32 v82, v58, v59\n"
    "v_mfma_f32_16x16x32_bf16 v[52:55], v[28:31], a[84:87], v[52:55]\n"
    "v_cvt_pk_bf16_f32 v81, v60, v61\n"
    "v_cvt_pk_bf16_f32 v83, v62, v63\n"
    "v_mfma_f32_16x16x32_bf16 v[48:51], v[32:35], a[88:91], v[48:51]\n"
    "v_mfma_f32_16x16x32_bf16 v[52:55], v[36:39], a[88:91], v[52:55]\n"
    "v_mfma_f32_16x16x32_bf16 v[48:51], v[40:43], a[92:95], v[48:51]\n"
    "v_mfma_f32_16x16x32_bf16 v[52:55], v[44:47], a[92:95], v[52:55]\n"
    "v_mfma_f32_16x16x32_bf16 v[56:59], v[0:3], a[96:99], 0\n"
    "buffer_store_dwordx4 v[76:79], v96, s[0:3], s40 offen sc1\n"
    "s_add_u32 s40, s40, 0x10000\n"
    "v_mfma_f32_16x16x32_bf16 v[60:63], v[4:7], a[96:99], 0\n"
    "v_mfma_f32_16x16x32_bf16 v[56:59], v[8:11], a[100:103], v[56:59]\n"
    "v_mfma_f32_16x16x32_bf16 v[60:63], v[12:15], a[100:103], v[60:63]\n"
    "v_mfma_f32_16x16x32_bf16 v[56:59], v[16:19], a[104:107], v[56:59]\n"
    "v_mfma_f32_16x16x32_bf16 v[60:63], v[20:23], a[104:107], v[60:63]\n"
    "v_mfma_f32_16x16x32_bf16 v[56:59], v[24:27], a[108:111], v[56:59]\n"
    "v_cvt_pk_bf16_f32 v72, v48, v49\n"
    "v_cvt_pk_bf16_f32 v74, v50, v51\n"
    "v_mfma_f32_16x16x32_bf16 v[60:63], v[28:31], a[108:111], v[60:63]\n"
    "v_cvt_pk_bf16_f32 v73, v52, v53\n"
    "v_cvt_pk_bf16_f32 v75, v54, v55\n"
    "v_mfma_f32_16x16x32_bf16 v[56:59], v[32:35], a[112:115], v[56:59]\n"
    "buffer_store_dwordx4 v[80:83], v96, s[0:3], s40 offen sc1\n"
    "s_add_u32 s40, s40, 0x10000\n"
    "v_mfma_f32_16x16x32_bf16 v[60:63], v[36:39], a[112:115], v[60:63]\n"
    "v_mfma_f32_16x16x32_bf16 v[56:59], v[40:43], a[116:119], v[56:59]\n"
    "v_mfma_f32_16x16x32_bf16 v[60:63], v[44:47], a[116:119], v[60:63]\n"
    "v_mfma_f32_16x16x32_bf16 v[48:51], v[0:3], a[120:123], 0\n"
    "v_mfma_f32_16x16x32_bf16 v[52:55], v[4:7], a[120:123], 0\n"
    "v_mfma_f32_16x16x32_bf16 v[48:51], v[8:11], a[124:127], v[48:51]\n"
    "v_mfma_f32_16x16x32_bf16 v[52:55], v[12:15], a[124:127], v[52:55]\n"
    "v_mfma_f32_16x16x32_bf16 v[48:51], v[16:19], a[128:131], v[48:51]\n"
    "buffer_store_dwordx4 v[72:75], v96, s[0:3], s40 offen sc1\n"
    "s_add_u32 s40, s40, 0x10000\n"
    "v_mfma_f32_16x16x32_bf16 v[52:55], v[20:23], a[128:131], v[52:55]\n"
    "v_mfma_f32_16x16x32_bf16 v[48:51], v[24:27], a[132:135], v[48:51]\n"
    "v_cvt_pk_bf16_f32 v76, v56, v57\n"
    "v_cvt_pk_bf16_f32 v78, v58, v59\n"
    "v_mfma_f32_16x16x32_bf16 v[52:55], v[28:31], a[132:135], v[52:55]\n"
    "v_cvt_pk_bf16_f32 v77, v60, v61\n"
    "v_cvt_pk_bf16_f32 v79, v62, v63\n"
    "v_mfma_f32_16x16x32_bf16 v[48:51], v[32:35], a[136:139], v[48:51]\n"
    "v_mfma_f32_16x16x32_bf16 v[52:55], v[36:39], a[136:139], v[52:55]\n"
    "v_mfma_f32_16x16x32_bf16 v[48:51], v[40:43], a[140:143], v[48:51]\n"
    "v_mfma_f32_16x16x32_bf16 v[52:55], v[44:47], a[140:143], v[52:55]\n"
    "v_mfma_f32_16x16x32_bf16 v[56:59], v[0:3], a[144:147], 0\n"
    "buffer_store_dwordx4 v[76:79], v96, s[0:3], s40 offen sc1\n"
    "s_add_u32 s40, s40, 0x10000\n"
    "v_mfma_f32_16x16x32_bf16 v[60:63], v[4:7], a[144:147], 0\n"
    "v_mfma_f32_16x16x32_bf16 v[56:59], v[8:11], a[148:151], v[56:59]\n"
    "v_mfma_f32_16x16x32_bf16 v[60:63], v[12:15], a[148:151], v[60:63]\n"
    "v_mfma_f32_16x16x32_bf16 v[56:59], v[16:19], a[152:155], v[56:59]\n"
    "v_mfma_f32_16x16x32_bf16 v[60:63], v[20:23], a[152:155], v[60:63]\n"
    "v_mfma_f32_16x16x32_bf16 v[56:59], v[24:27], a[156:159], v[56:59]\n"
    "v_cvt_pk_bf16_f32 v80, v48, v49\n"
    "v_cvt_pk_bf16_f32 v82, v50, v51\n"
    "v_mfma_f32_16x16x32_bf16 v[60:63], v[28:31], a[156:159], v[60:63]\n"
    "v_cvt_pk_bf16_f32 v81, v52, v53\n"
    "v_cvt_pk_bf16_f32 v83, v54, v55\n"
    "v_mfma_f32_16x16x32_bf16 v[56:59], v[32:35], a[160:163], v[56:59]\n"
    "buffer_store_dwordx4 v[80:83], v96, s[0:3], s40 offen sc1\n"
    "s_add_u32 s40, s40, 0x10000\n"
    "v_mfma_f32_16x16x32_bf16 v[60:63], v[36:39], a[160:163], v[60:63]\n"
    "v_mfma_f32_16x16x32_bf16 v[56:59], v[40:43], a[164:167], v[56:59]\n"
    "v_mfma_f32_16x16x32_bf16 v[60:63], v[44:47], a[164:167], v[60:63]\n"
    "v_mfma_f32_16x16x32_bf16 v[48:51], v[0:3], a[168:171], 0\n"
    "v_mfma_f32_16x16x32_bf16 v[52:55], v[4:7], a[168:171], 0\n"
    "v_mfma_f32_16x16x32_bf16 v[48:51], v[8:11], a[172:175], v[48:51]\n"
    "v_mfma_f32_16x16x32_bf16 v[52:55], v[12:15], a[172:175], v[52:55]\n"
    "v_mfma_f32_16x16x32_bf16 v[48:51], v[16:19], a[176:179], v[48:51]\n"
    "buffer_store_dwordx4 v[88:91], v102, s[0:3], s38 offen sc1\n"
    "v_mfma_f32_16x16x32_bf16 v[52:55], v[20:23], a[176:179], v[52:55]\n"
    "v_mfma_f32_16x16x32_bf16 v[48:51], v[24:27], a[180:183], v[48:51]\n"
    "v_cvt_pk_bf16_f32 v72, v56, v57\n"
    "v_cvt_pk_bf16_f32 v74, v58, v59\n"
    "v_mfma_f32_16x16x32_bf16 v[52:55], v[28:31], a[180:183], v[52:55]\n"
    "v_cvt_pk_bf16_f32 v73, v60, v61\n"
    "v_cvt_pk_bf16_f32 v75, v62, v63\n"
    "v_mfma_f32_16x16x32_bf16 v[48:51], v[32:35], a[184:187], v[48:51]\n"
    "v_mfma_f32_16x16x32_bf16 v[52:55], v[36:39], a[184:187], v[52:55]\n"
    "v_mfma_f32_16x16x32_bf16 v[48:51], v[40:43], a[188:191], v[48:51]\n"
    "v_mfma_f32_16x16x32_bf16 v[52:55], v[44:47], a[188:191], v[52:55]\n"
    "v_mfma_f32_16x16x32_bf16 v[56:59], v[0:3], a[192:195], 0\n"
    "buffer_store_dwordx4 v[72:75], v96, s[0:3], s40 offen sc1\n"
    "s_add_u32 s40, s40, 0x10000\n"
    "v_mfma_f32_16x16x32_bf16 v[60:63], v[4:7], a[192:195], 0\n"
    "v_mfma_f32_16x16x32_bf16 v[56:59], v[8:11], a[196:199], v[56:59]\n"
    "v_mfma_f32_16x16x32_bf16 v[60:63], v[12:15], a[196:199], v[60:63]\n"
    "v_mfma_f32_16x16x32_bf16 v[56:59], v[16:19], a[200:203], v[56:59]\n"
    "v_mfma_f32_16x16x32_bf16 v[60:63], v[20:23], a[200:203], v[60:63]\n"
    "v_mfma_f32_16x16x32_bf16 v[56:59], v[24:27], a[204:207], v[56:59]\n"
    "v_cvt_pk_bf16_f32 v76, v48, v49\n"
    "v_cvt_pk_bf16_f32 v78, v50, v51\n"
    "v_mfma_f32_16x16x32_bf16 v[60:63], v[28:31], a[204:207], v[60:63]\n"
    "v_cvt_pk_bf16_f32 v77, v52, v53\n"
    "v_cvt_pk_bf16_f32 v79, v54, v55\n"
    "v_mfma_f32_16x16x32_bf16 v[56:59], v[32:35], a[208:211], v[56:59]\n"
    "buffer_store_dwordx4 v[76:79], v96, s[0:3], s40 offen sc1\n"
    "s_add_u32 s40, s40, 0x10000\n"
    "v_mfma_f32_16x16x32_bf16 v[60:63], v[36:39], a[208:211], v[60:63]\n"
    "v_mfma_f32_16x16x32_bf16 v[56:59], v[40:43], a[212:215], v[56:59]\n"
    "v_mfma_f32_16x16x32_bf16 v[60:63], v[44:47], a[212:215], v[60:63]\n"
    "v_mfma_f32_16x16x32_bf16 v[48:51], v[0:3], a[216:219], 0\n"
    "v_mfma_f32_16x16x32_bf16 v[52:55], v[4:7], a[216:219], 0\n"
    "v_mfma_f32_16x16x32_bf16 v[48:51], v[8:11], a[220:223], v[48:51]\n"
    "v_mfma_f32_16x16x32_bf16 v[52:55], v[12:15], a[220:223], v[52:55]\n"
    "v_mfma_f32_16x16x32_bf16 v[48:51], v[16:19], a[224:227], v[48:51]\n"
    "buffer_store_dwordx4 v[88:91], v102, s[0:3], s39 offen sc1\n"
    "v_mfma_f32_16x16x32_bf16 v[52:55], v[20:23], a[224:227], v[52:55]\n"
    "v_mfma_f32_16x16x32_bf16 v[48:51], v[24:27], a[228:231], v[48:51]\n"
    "v_cvt_pk_bf16_f32 v80, v56, v57\n"
    "v_cvt_pk_bf16_f32 v82, v58, v59\n"
    "v_mfma_f32_16x16x32_bf16 v[52:55], v[28:31], a[228:231], v[52:55]\n"
    "v_cvt_pk_bf16_f32 v81, v60, v61\n"
    "v_cvt_pk_bf16_f32 v83, v62, v63\n"
    "v_mfma_f32_16x16x32_bf16 v[48:51], v[32:35], a[232:235], v[48:51]\n"
    "v_mfma_f32_16x16x32_bf16 v[52:55], v[36:39], a[232:235], v[52:55]\n"
    "v_mfma_f32_16x16x32_bf16 v[48:51], v[40:43], a[236:239], v[48:51]\n"
    "v_mfma_f32_16x16x32_bf16 v[52:55], v[44:47], a[236:239], v[52:55]\n"
    "s_nop 6\n"
    "v_cvt_pk_bf16_f32 v72, v48, v49\n"
    "v_cvt_pk_bf16_f32 v74, v50, v51\n"
    "v_cvt_pk_bf16_f32 v73, v52, v53\n"
    "v_cvt_pk_bf16_f32 v75, v54, v55\n"
    "buffer_store_dwordx4 v[80:83], v104, s[0:3], s38 offen sc1\n"
    ".Lvo_vph_%=:\n"
    "s_waitcnt vmcnt(11) lgkmcnt(0)\n"
    "s_barrier\n"
    "ds_read_b128 v[24:27], v92 offset:0\n"
    "ds_read_b128 v[28:31], v92 offset:1024\n"
    "ds_read_b128 v[32:35], v92 offset:2048\n"
    "ds_read_b128 v[36:39], v92 offset:3072\n"
    "ds_read_b128 v[40:43], v92 offset:4096\n"
    "ds_read_b128 v[44:47], v92 offset:5120\n"
    "s_waitcnt lgkmcnt(4)\n"
    "v_mfma_f32_16x16x32_bf16 v[48:51], a[240:243], v[24:27], 0\n"
    "buffer_store_dwordx4 v[72:75], v104, s[0:3], s39 offen sc1\n"
    "v_mfma_f32_16x16x32_bf16 v[52:55], a[240:243], v[28:31], 0\n"
    "s_add_u32 s34, s34, 1\n"
    "s_add_u32 s36, s34, 0\n"
    "s_and_b32 s36, s36, 3\n"
    "v_mfma_f32_16x16x32_bf16 v[56:59], v[176:179], v[24:27], 0\n"
    "ds_read_b128 v[24:27], v92 offset:6144\n"
    "s_lshl_b32 s36, s36, 22\n"
    "v_mfma_f32_16x16x32_bf16 v[60:63], v[176:179], v[28:31], 0\n"
    "ds_read_b128 v[28:31], v92 offset:7168\n"
    "s_waitcnt lgkmcnt(4)\n"
    "v_mfma_f32_16x16x32_bf16 v[48:51], a[244:247], v[32:35], v[48:51]\n"
    "s_add_u32 s41, s15, s36\n"
    "s_add_u32 s42, s16, s36\n"
    "s_add_u32 s37, s36, 0x10000\n"
    "v_mfma_f32_16x16x32_bf16 v[52:55], a[244:247], v[36:39], v[52:55]\n"
    "s_add_u32 s50, s34, 3\n"
    "s_cmp_gt_i32 s50, 64\n"
    "s_cselect_b32 s50, 0, s50\n"
    "v_mfma_f32_16x16x32_bf16 v[56:59], v[180:183], v[32:35], v[56:59]\n"
    "ds_read_b128 v[32:35], v93 offset:0\n"
    "s_add_u32 s51, s34, 4\n"
    "v_mfma_f32_16x16x32_bf16 v[60:63], v[180:183], v[36:39], v[60:63]\n"
    "ds_read_b128 v[36:39], v93 offset:1024\n"
    "s_waitcnt lgkmcnt(4)\n"
    "v_mfma_f32_16x16x32_bf16 v[48:51], a[248:251], v[40:43], v[48:51]\n"
    "buffer_load_dwordx2 v[108:109], off, s[0:3], s45 sc1\n"
    "v_mfma_f32_16x16x32_bf16 v[52:55], a[248:251], v[44:47], v[52:55]\n"
    "s_cmp_gt_i32 s51, 64\n"
    "s_cselect_b32 s51, 0, s51\n"
    "s_mul_i32 s51, s51, s14\n"
    "v_mfma_f32_16x16x32_bf16 v[56:59], v[184:187], v[40:43], v[56:59]\n"
    "ds_read_b128 v[40:43], v93 offset:2048\n"
    "v_mov_b64 v[64:65], 0\n"
    "v_mfma_f32_16x16x32_bf16 v[60:63], v[184:187], v[44:47], v[60:63]\n"
    "ds_read_b128 v[44:47], v93 offset:3072\n"
    "s_waitcnt lgkmcnt(4)\n"
    "v_mfma_f32_16x16x32_bf16 v[48:51], a[252:255], v[24:27], v[48:51]\n"
    "buffer_store_dwordx2 v[88:89], v103, s[0:3], s38 offen sc1\n"
    "v_mov_b64 v[66:67], 0\n"
    "v_mfma_f32_16x16x32_bf16 v[52:55], a[252:255], v[28:31], v[52:55]\n"
    "v_mov_b64 v[68:69], 0\n"
    "v_mov_b64 v[70:71], 0\n"
    "v_mfma_f32_16x16x32_bf16 v[56:59], v[188:191], v[24:27], v[56:59]\n"
    "ds_read_b128 v[24:27], v93 offset:4096\n"
    "v_mov_b64 v[84:85], 0\n"
    "v_mfma_f32_16x16x32_bf16 v[60:63], v[188:191], v[28:31], v[60:63]\n"
    "ds_read_b128 v[28:31], v93 offset:5120\n"
    "s_waitcnt lgkmcnt(4)\n"
    "v_mfma_f32_16x16x32_bf16 v[48:51], v[112:115], v[32:35], v[48:51]\n"
    "v_mov_b64 v[86:87], 0\n"
    "v_mfma_f32_16x16x32_bf16 v[52:55], v[112:115], v[36:39], v[52:55]\n"
    "v_mfma_f32_16x16x32_bf16 v[56:59], v[192:195], v[32:35], v[56:59]\n"
    "ds_read_b128 v[32:35], v93 offset:6144\n"
    "v_mfma_f32_16x16x32_bf16 v[60:63], v[192:195], v[36:39], v[60:63]\n"
    "ds_read_b128 v[36:39], v93 offset:7168\n"
    "s_waitcnt lgkmcnt(4)\n"
    "v_mfma_f32_16x16x32_bf16 v[48:51], v[116:119], v[40:43], v[48:51]\n"
    "v_mfma_f32_16x16x32_bf16 v[52:55], v[116:119], v[44:47], v[52:55]\n"
    "v_mfma_f32_16x16x32_bf16 v[56:59], v[196:199], v[40:43], v[56:59]\n"
    "ds_read_b128 v[40:43], v94 offset:0\n"
    "v_mfma_f32_16x16x32_bf16 v[60:63], v[196:199], v[44:47], v[60:63]\n"
    "ds_read_b128 v[44:47], v94 offset:1024\n"
    "s_waitcnt lgkmcnt(4)\n"
    "v_mfma_f32_16x16x32_bf16 v[48:51], v[120:123], v[24:27], v[48:51]\n"
    "v_mfma_f32_16x16x32_bf16 v[52:55], v[120:123], v[28:31], v[52:55]\n"
    "v_mfma_f32_16x16x32_bf16 v[56:59], v[200:203], v[24:27], v[56:59]\n"
    "ds_read_b128 v[24:27], v94 offset:2048\n"
    "v_mfma_f32_16x16x32_bf16 v[60:63], v[200:203], v[28:31], v[60:63]\n"
    "ds_read_b128 v[28:31], v94 offset:3072\n"
    "s_waitcnt lgkmcnt(4)\n"
    "v_mfma_f32_16x16x32_bf16 v[48:51], v[124:127], v[32:35], v[48:51]\n"
    "v_mfma_f32_16x16x32_bf16 v[52:55], v[124:127], v[36:39], v[52:55]\n"
    "v_mfma_f32_16x16x32_bf16 v[56:59], v[204:207], v[32:35], v[56:59]\n"
    "ds_read_b128 v[32:35], v94 offset:4096\n"
    "v_mfma_f32_16x16x32_bf16 v[60:63], v[204:207], v[36:39], v[60:63]\n"
    "ds_read_b128 v[36:39], v94 offset:5120\n"
    "s_waitcnt lgkmcnt(4)\n"
    "v_mfma_f32_16x16x32_bf16 v[48:51], v[128:131], v[40:43], v[48:51]\n"
    "v_mfma_f32_16x16x32_bf16 v[52:55], v[128:131], v[44:47], v[52:55]\n"
    "v_mfma_f32_16x16x32_bf16 v[56:59], v[208:211], v[40:43], v[56:59]\n"
    "ds_read_b128 v[40:43], v94 offset:6144\n"
    "v_mfma_f32_16x16x32_bf16 v[60:63], v[208:211], v[44:47], v[60:63]\n"
    "ds_read_b128 v[44:47], v94 offset:7168\n"
    "s_waitcnt lgkmcnt(4)\n"
    "v_mfma_f32_16x16x32_bf16 v[48:51], v[132:135], v[24:27], v[48:51]\n"
    "s_waitcnt vmcnt(1)\n"
    "v_readfirstlane_b32 s32, v108\n"
    "v_mfma_f32_16x16x32_bf16 v[52:55], v[132:135], v[28:31], v[52:55]\n"
    "v_readfirstlane_b32 s33, v109\n"
    "v_mfma_f32_16x16x32_bf16 v[56:59], v[212:215], v[24:27], v[56:59]\n"
    "ds_read_b128 v[24:27], v95 offset:0\n"
    "v_mfma_f32_16x16x32_bf16 v[60:63], v[212:215], v[28:31], v[60:63]\n"
    "ds_read_b128 v[28:31], v95 offset:1024\n"
    "s_waitcnt lgkmcnt(4)\n"
    "v_mfma_f32_16x16x32_bf16 v[48:51], v[136:139], v[32:35], v[48:51]\n"
    "s_cmp_eq_u32 s33, s35\n"
    "s_cselect_b32 s46, s32, s46\n"
    "s_add_u32 s46, s46, 288\n"
    "v_mfma_f32_16x16x32_bf16 v[52:55], v[136:139], v[36:39], v[52:55]\n"
    "s_sub_u32 s47, s46, 63\n"
    "v_mfma_f32_16x16x32_bf16 v[56:59], v[216:219], v[32:35], v[56:59]\n"
    "ds_read_b128 v[32:35], v95 offset:2048\n"
    "v_mfma_f32_16x16x32_bf16 v[60:63], v[216:219], v[36:39], v[60:63]\n"
    "ds_read_b128 v[36:39], v95 offset:3072\n"
    "s_waitcnt lgkmcnt(4)\n"
    ".Lvo_rt8_%=:\n"
    "s_memrealtime s[30:31]\n"
    "s_waitcnt lgkmcnt(0)\n"
    "s_sub_u32 s32, s47, s30\n"
    "s_cmp_lt_u32 s32, 600\n"
    "s_cbranch_scc1 .Lvo_rt8_%=\n"
    "buffer_load_dwordx4 v[72:75], v96, s[0:3], s41 offen sc1\n"
    "buffer_load_dwordx4 v[76:79], v96, s[0:3], s41 offen offset:1024 sc1\n"
    "buffer_load_dwordx4 v[80:83], v96, s[0:3], s42 offen sc1\n"
    "v_mfma_f32_16x16x32_bf16 v[48:51], v[140:143], v[40:43], v[48:51]\n"
    "s_add_u32 s49, s34, 3\n"
    "s_lshl_b32 s43, s49, 19\n"
    "s_lshl_b32 s33, s49, 11\n"
    "v_mfma_f32_16x16x32_bf16 v[52:55], v[140:143], v[44:47], v[52:55]\n"
    "s_xor_b32 s33, s33, s13\n"
    "s_and_b32 s33, s33, 0x2000\n"
    "s_add_u32 s43, s43, s33\n"
    "v_mfma_f32_16x16x32_bf16 v[56:59], v[220:223], v[40:43], v[56:59]\n"
    "ds_read_b128 v[40:43], v95 offset:4096\n"
    "s_add_u32 s43, s43, s17\n"
    "v_mfma_f32_16x16x32_bf16 v[60:63], v[220:223], v[44:47], v[60:63]\n"
    "ds_read_b128 v[44:47], v95 offset:5120\n"
    "s_waitcnt lgkmcnt(4)\n"
    "v_mfma_f32_16x16x32_bf16 v[48:51], v[144:147], v[24:27], v[48:51]\n"
    "s_add_u32 s49, s34, 2\n"
    "s_lshl_b32 s44, s49, 20\n"
    "s_lshl_b32 s33, s49, 12\n"
    "v_mfma_f32_16x16x32_bf16 v[52:55], v[144:147], v[28:31], v[52:55]\n"
    "s_xor_b32 s33, s33, s13\n"
    "s_and_b32 s33, s33, 0x2000\n"
    "s_add_u32 s44, s44, s33\n"
    "v_mfma_f32_16x16x32_bf16 v[56:59], v[224:227], v[24:27], v[56:59]\n"
    "ds_read_b128 v[24:27], v95 offset:6144\n"
    "s_add_u32 s44, s44, s18\n"
    "v_mfma_f32_16x16x32_bf16 v[60:63], v[224:227], v[28:31], v[60:63]\n"
    "ds_read_b128 v[28:31], v95 offset:7168\n"
    "s_waitcnt lgkmcnt(4)\n"
    "v_mfma_f32_16x16x32_bf16 v[48:51], v[148:151], v[32:35], v[48:51]\n"
    "s_add_u32 s38, s34, 2\n"
    "s_and_b32 s38, s38, 3\n"
    "s_lshl_b32 s38, s38, 22\n"
    "v_mfma_f32_16x16x32_bf16 v[52:55], v[148:151], v[36:39], v[52:55]\n"
    "s_add_u32 s39, s38, 0x10000\n"
    "s_add_u32 s40, s19, s38\n"
    "s_and_b32 s45, s34, 63\n"
    "v_mfma_f32_16x16x32_bf16 v[56:59], v[228:231], v[32:35], v[56:59]\n"
    "ds_read_b128 v[32:35], v96 offset:53248\n"
    "s_lshl_b32 s45, s45, 7\n"
    "v_mfma_f32_16x16x32_bf16 v[60:63], v[228:231], v[36:39], v[60:63]\n"
    "ds_read_b128 v[36:39], v96 offset:54272\n"
    "s_waitcnt lgkmcnt(4)\n"
    "v_mfma_f32_16x16x32_bf16 v[48:51], v[152:155], v[40:43], v[48:51]\n"
    "s_add_u32 s45, s20, s45\n"
    "s_add_u32 s35, s34, 1\n"
    "v_mfma_f32_16x16x32_bf16 v[52:55], v[152:155], v[44:47], v[52:55]\n"
    "s_cmp_lt_i32 s34, 0\n"
    "s_cselect_b32 s35, -5, s35\n"
    "v_mfma_f32_16x16x32_bf16 v[56:59], v[232:235], v[40:43], v[56:59]\n"
    "ds_read_b128 v[40:43], v96 offset:55296\n"
    "buffer_load_dword v110, v106, s[0:3], s23 offen sc1\n"
    "v_mfma_f32_16x16x32_bf16 v[60:63], v[232:235], v[44:47], v[60:63]\n"
    "ds_read_b128 v[44:47], v96 offset:56320\n"
    "s_waitcnt lgkmcnt(4)\n"
    "v_mfma_f32_16x16x32_bf16 v[48:51], v[156:159], v[24:27], v[48:51]\n"
    "ds_read_b128 v[0:3], v97 offset:0\n"
    "v_mfma_f32_16x16x32_bf16 v[52:55], v[156:159], v[28:31], v[52:55]\n"
    "ds_read_b128 v[4:7], v97 offset:1024\n"
    "v_mfma_f32_16x16x32_bf16 v[56:59], v[236:239], v[24:27], v[56:59]\n"
    "ds_read_b128 v[24:27], v96 offset:57344\n"
    "v_mfma_f32_16x16x32_bf16 v[60:63], v[236:239], v[28:31], v[60:63]\n"
    "ds_read_b128 v[28:31], v96 offset:58368\n"
    "s_waitcnt lgkmcnt(6)\n"
    "v_mfma_f32_16x16x32_bf16 v[48:51], v[160:163], v[32:35], v[48:51]\n"
    "buffer_load_dword v111, v105, s[0:3], s22 offen sc1\n"
    "ds_read_b128 v[8:11], v97 offset:2048\n"
    "v_mfma_f32_16x16x32_bf16 v[52:55], v[160:163], v[36:39], v[52:55]\n"
    "ds_read_b128 v[12:15], v97 offset:3072\n"
    "v_mfma_f32_16x16x32_bf16 v[56:59], v[240:243], v[32:35], v[56:59]\n"
    "ds_read_b128 v[32:35], v96 offset:59392\n"
    "v_mfma_f32_16x16x32_bf16 v[60:63], v[240:243], v[36:39], v[60:63]\n"
    "ds_read_b128 v[36:39], v96 offset:60416\n"
    "s_waitcnt lgkmcnt(8)\n"
    "v_mfma_f32_16x16x32_bf16 v[48:51], v[164:167], v[40:43], v[48:51]\n"
    "ds_read_b128 v[16:19], v97 offset:4096\n"
    "v_mfma_f32_16x16x32_bf16 v[52:55], v[164:167], v[44:47], v[52:55]\n"
    "ds_read_b128 v[20:23], v97 offset:5120\n"
    "v_mfma_f32_16x16x32_bf16 v[56:59], v[244:247], v[40:43], v[56:59]\n"
    "v_mfma_f32_16x16x32_bf16 v[60:63], v[244:247], v[44:47], v[60:63]\n"
    "s_waitcnt lgkmcnt(6)\n"
    "v_mfma_f32_16x16x32_bf16 v[48:51], v[168:171], v[24:27], v[48:51]\n"
    "v_mfma_f32_16x16x32_bf16 v[52:55], v[168:171], v[28:31], v[52:55]\n"
    "v_mfma_f32_16x16x32_bf16 v[56:59], v[248:251], v[24:27], v[56:59]\n"
    "v_mfma_f32_16x16x32_bf16 v[60:63], v[248:251], v[28:31], v[60:63]\n"
    "s_waitcnt lgkmcnt(2)\n"
    "v_mfma_f32_16x16x32_bf16 v[48:51], v[172:175], v[32:35], v[48:51]\n"
    "v_mfma_f32_16x16x32_bf16 v[52:55], v[172:175], v[36:39], v[52:55]\n"
    "v_mfma_f32_16x16x32_bf16 v[56:59], v[252:255], v[32:35], v[56:59]\n"
    "v_mfma_f32_16x16x32_bf16 v[60:63], v[252:255], v[36:39], v[60:63]\n"
    "ds_read_b128 v[24:27], v97 offset:6144\n"
    "ds_read_b128 v[28:31], v97 offset:7168\n"
    "s_nop 2\n"
    "v_cvt_pk_bf16_f32 v32, v48, v49\n"
    "v_cvt_pk_bf16_f32 v33, v50, v51\n"
    "v_cvt_pk_bf16_f32 v34, v56, v57\n"
    "v_cvt_pk_bf16_f32 v35, v58, v59\n"
    "ds_write_b128 v92, v[32:35] offset:11264\n"
    "v_cvt_pk_bf16_f32 v36, v52, v53\n"
    "v_cvt_pk_bf16_f32 v37, v54, v55\n"
    "v_cvt_pk_bf16_f32 v38, v60, v61\n"
    "v_cvt_pk_bf16_f32 v39, v62, v63\n"
    "ds_write_b128 v92, v[36:39] offset:12288\n"
    "ds_read_b128 v[48:51], v97 offset:8192\n"
    "ds_read_b128 v[52:55], v97 offset:9216\n"
    "ds_read_b128 v[56:59], v97 offset:10240\n"
    "ds_read_b128 v[60:63], v97 offset:11264\n"
    "s_cmp_lt_i32 s34, 0\n"
    "s_cbranch_scc0 .Lvo_take_%=\n"
    "s_waitcnt vmcnt(0) lgkmcnt(0)\n"
    "s_barrier\n"
    ".Lvo_po9_%=:\n"
    "buffer_load_dword v110, v106, s[0:3], s23 offen sc1\n"
    "s_waitcnt vmcnt(0)\n"
    "v_cmp_gt_i32 vcc, s50, v110\n"
    "s_nop 0\n"
    "s_cbranch_vccz .Lvo_pd10_%=\n"
    "s_add_u32 s28, s28, 1\n"
    "s_cmp_lt_u32 s28, 0x40000\n"
    "s_cbranch_scc1 .Lvo_sp11_%=\n"
    "v_mov_b32 v110, 32\n"
    "s_mov_b32 s32, 33728\n"
    "buffer_store_dword v110, off, s[0:3], s32 sc1\n"
    "s_waitcnt vmcnt(0)\n"
    "s_branch .Lvo_pd10_%=\n"
    ".Lvo_sp11_%=:\n"
    "s_sleep 4\n"
    "s_branch .Lvo_po9_%=\n"
    ".Lvo_pd10_%=:\n"
    ".Lvo_po12_%=:\n"
    "buffer_load_dword v111, v105, s[0:3], s22 offen sc1\n"
    "s_waitcnt vmcnt(0)\n"
    "v_cmp_gt_i32 vcc, s51, v111\n"
    "s_nop 0\n"
    "s_cbranch_vccz .Lvo_pd13_%=\n"
    "s_add_u32 s28, s28, 1\n"
    "s_cmp_lt_u32 s28, 0x40000\n"
    "s_cbranch_scc1 .Lvo_sp14_%=\n"
    "v_mov_b32 v111, 35\n"
    "s_mov_b32 s32, 33728\n"
    "buffer_store_dword v111, off, s[0:3], s32 sc1\n"
    "s_waitcnt vmcnt(0)\n"
    "s_branch .Lvo_pd13_%=\n"
    ".Lvo_sp14_%=:\n"
    "s_sleep 4\n"
    "s_branch .Lvo_po12_%=\n"
    ".Lvo_pd13_%=:\n"
    "buffer_load_dwordx4 v[32:35], v96, s[0:3], s44 offen offset:0 sc1\n"
    "buffer_load_dwordx4 v[36:39], v96, s[0:3], s44 offen offset:1024 sc1\n"
    "buffer_load_dwordx4 v[40:43], v96, s[0:3], s44 offen offset:2048 sc1\n"
    "buffer_load_dwordx4 v[44:47], v96, s[0:3], s44 offen offset:3072 sc1\n"
    "s_add_u32 m0, s48, 0\n"
    "s_nop 0\n"
    "buffer_load_dwordx4 v96, s[0:3], s43 offen sc1 lds\n"
    "buffer_load_dwordx4 v96, s[0:3], s43 offen offset:1024 sc1 lds\n"
    "s_add_u32 m0, s48, 13312\n"
    "s_add_u32 s43, s43, 0x4000\n"
    "buffer_load_dwordx4 v96, s[0:3], s43 offen sc1 lds\n"
    "buffer_load_dwordx4 v96, s[0:3], s43 offen offset:1024 sc1 lds\n"
    "s_add_u32 m0, s48, 26624\n"
    "s_add_u32 s43, s43, 0x4000\n"
    "buffer_load_dwordx4 v96, s[0:3], s43 offen sc1 lds\n"
    "buffer_load_dwordx4 v96, s[0:3], s43 offen offset:1024 sc1 lds\n"
    "ds_read_b128 v[0:3], v96 offset:11264\n"
    "ds_read_b128 v[4:7], v96 offset:12288\n"
    "ds_read_b128 v[8:11], v96 offset:24576\n"
    "ds_read_b128 v[12:15], v96 offset:25600\n"
    "ds_read_b128 v[16:19], v96 offset:37888\n"
    "ds_read_b128 v[20:23], v96 offset:38912\n"
    "ds_read_b128 v[24:27], v96 offset:51200\n"
    "ds_read_b128 v[28:31], v96 offset:52224\n"
    "s_waitcnt vmcnt(0)\n"
    "s_mov_b32 s37, 2130706432\n"
    "s_mov_b32 s36, 2130706432\n"
    "s_branch .Lvo_uph_%=\n"
    ".Lvo_retry_%=:\n"
    "s_add_u32 s28, s28, 1\n"
    "s_cmp_lt_u32 s28, 0x40000\n"
    "s_cbranch_scc1 .Lvo_sp15_%=\n"
    "v_mov_b32 v108, 2\n"
    "s_mov_b32 s32, 33728\n"
    "buffer_store_dword v108, off, s[0:3], s32 sc1\n"
    "s_waitcnt vmcnt(0)\n"
    "s_branch .Lvo_go_%=\n"
    ".Lvo_sp15_%=:\n"
    "s_nop 7\n"
    "v_mov_b64 v[64:65], 0\n"
    "v_mov_b64 v[66:67], 0\n"
    "v_mov_b64 v[68:69], 0\n"
    "v_mov_b64 v[70:71], 0\n"
    "v_mov_b64 v[84:85], 0\n"
    "v_mov_b64 v[86:87], 0\n"
    "buffer_load_dwordx4 v[72:75], v96, s[0:3], s41 offen sc1\n"
    "buffer_load_dwordx4 v[76:79], v96, s[0:3], s41 offen offset:1024 sc1\n"
    "buffer_load_dwordx4 v[80:83], v96, s[0:3], s42 offen sc1\n"
    "s_waitcnt vmcnt(0)\n"
    "s_branch .Lvo_take_%=\n"
    ".Lvo_wq_%=:\n"
    ".Lvo_po16_%=:\n"
    "buffer_load_dword v110, v106, s[0:3], s23 offen sc1\n"
    "s_waitcnt vmcnt(0)\n"
    "v_cmp_gt_i32 vcc, s50, v110\n"
    "s_nop 0\n"
    "s_cbranch_vccz .Lvo_pd17_%=\n"
    "s_add_u32 s28, s28, 1\n"
    "s_cmp_lt_u32 s28, 0x40000\n"
    "s_cbranch_scc1 .Lvo_sp18_%=\n"
    "v_mov_b32 v110, 33\n"
    "s_mov_b32 s32, 33728\n"
    "buffer_store_dword v110, off, s[0:3], s32 sc1\n"
    "s_waitcnt vmcnt(0)\n"
    "s_branch .Lvo_pd17_%=\n"
    ".Lvo_sp18_%=:\n"
    "s_sleep 4\n"
    "s_branch .Lvo_po16_%=\n"
    ".Lvo_pd17_%=:\n"
    "s_branch .Lvo_wqb_%=\n"
    ".Lvo_wx_%=:\n"
    ".Lvo_po19_%=:\n"
    "buffer_load_dword v111, v105, s[0:3], s22 offen sc1\n"
    "s_waitcnt vmcnt(0)\n"
    "v_cmp_gt_i32 vcc, s51, v111\n"
    "s_nop 0\n"
    "s_cbranch_vccz .Lvo_pd20_%=\n"
    "s_add_u32 s28, s28, 1\n"
    "s_cmp_lt_u32 s28, 0x40000\n"
    "s_cbranch_scc1 .Lvo_sp21_%=\n"
    "v_mov_b32 v111, 34\n"
    "s_mov_b32 s32, 33728\n"
    "buffer_store_dword v111, off, s[0:3], s32 sc1\n"
    "s_waitcnt vmcnt(0)\n"
    "s_branch .Lvo_pd20_%=\n"
    ".Lvo_sp21_%=:\n"
    "s_sleep 4\n"
    "s_branch .Lvo_po19_%=\n"
    ".Lvo_pd20_%=:\n"
    "s_branch .Lvo_wxb_%=\n"
    ".Lvo_end_%=:\n"
    "s_waitcnt vmcnt(0) lgkmcnt(0)\n"
    "s_endpgm\n"
    :: "{s[0:3]}"(s0_), "{s[4:7]}"(s4_), "{s[8:11]}"(s8_), "{s[12:15]}"(s12_), "{s[16:19]}"(s16_), "{s[20:23]}"(s20_), "{s[24:27]}"(s24_), "{v[0:3]}"(v0_), "{v[4:7]}"(v4_), "{v[8:11]}"(v8_), "{v[12:15]}"(v12_), "{v[16:19]}"(v16_), "{v[92:95]}"(v92_), "{v[96:99]}"(v96_), "{v[100:103]}"(v100_), "{v[104:107]}"(v104_) : "memory", "v255", "a255", "s95");
    __builtin_unreachable();
}
DEV void ct_role(const MegaArgs& A, const int L, const int u) {
    ROLE_HEAD;
    {
        const int sg = 2 * u + L;
        u32x4 xa[8], xb[8];
#pragma unroll
        for (int r = 0; r < 8; ++r) {
            const bf16_t* xr = A.x0 + (size_t)(32 * sg + 8 * w + r) * D_;
            xa[r] = *(const u32x4*)(xr + 8 * l);
            xb[r] = *(const u32x4*)(xr + 512 + 8 * l16);
        }
        const float mk = l < 16 ? 1.f : 0.f;
#pragma unroll
        for (int r = 0; r < 8; ++r) {
            float v[16], s = 0.f, s2 = 0.f, q = 0.f, q2 = 0.f;
#pragma unroll
            for (int e = 0; e < 4; ++e) { v[2 * e] = lo16(xa[r][e]); v[2 * e + 1] = hi16(xa[r][e]); v[8 + 2 * e] = lo16(xb[r][e]); v[9 + 2 * e] = hi16(xb[r][e]); }
#pragma unroll
            for (int e = 0; e < 8; ++e) { s += v[e]; s2 += v[8 + e]; }
            s += l < 16 ? s2 : 0.f;
            for (int o = 32; o > 0; o >>= 1) s += __shfl_xor(s, o);
            const float mean = s * (1.0f / D_);
#pragma unroll
            for (int e = 0; e < 8; ++e) { const float d = v[e] - mean, d2 = v[8 + e] - mean; q += d * d; q2 += d2 * d2; }
            q += l < 16 ? q2 : 0.f;
            for (int o = 32; o > 0; o >>= 1) q += __shfl_xor(q, o);
            const float mb = rb(mean), rd = __builtin_amdgcn_rcpf(rb(sqrtf(rb(rb(q * (1.0f / D_)) + 1e-5f))));
            u32x4 oa, ob;
#pragma unroll
            for (int e = 0; e < 4; ++e) {
                oa[sw2(e)] = pk2(rb(v[2 * e] - mb) * rd, rb(v[2 * e + 1] - mb) * rd);
                ob[sw2(e)] = pk2(rb(v[8 + 2 * e] - mb) * rd, rb(v[9 + 2 * e] - mb) * rd);
            }
            const int row = 8 * w + r;
            const unsigned o = rec(sg, 0) + (row >> 4) * 1024 + (row & 15) * 16 + (l16 >> 2) * 2048 + (l & 3) * 256;
            bst16(rr, o + lq * 16384, oa);
            bst16(rr, l < 16 ? o + 65536 : OOB, ob);
        }
    }
    const int r5 = l / 5, q5 = l - 5 * r5, c0 = q5 < 4 ? 16 * u + 4 * q5 : 512 + 4 * u, tk = 8 * w + r5;
    const unsigned mpos = l < 40 ? xl(RG_MEM + w * 16384 + (c0 >> 6) * 1024 + (((c0 >> 4) & 3) * 16 + 4 * (r5 >> 1) + ((c0 >> 1) & 2) + (r5 & 1)) * 16 + (c0 & 8), L) : OOB;
    const unsigned xpos = l < 40 ? (c0 >> 7) * 16384 + ((c0 >> 5) & 3) * 2048 + (tk >> 4) * 1024 + (((c0 >> 3) & 3) * 16 + (tk & 15)) * 16 + (c0 & 4) * 2 : OOB;
    const unsigned rpos = (L || l >= 40) ? xpos + RC_X2 : tk * 1280 + c0 * 2;
    const u32x4 rs = L ? rx : u32x4{lo(A.x0), hi(A.x0), 2048u * 1280u, 0x20000u};
#pragma unroll
    for (int k = 0; k < 4; ++k) bst16(rr, rec(2 * u + (k >> 1), L) + RC_X1 + 65536 + w * 2048 + (k & 1) * 1024 + l * 16, ones);
    const u32x2 m0 = *(const u32x2*)(A.mem0 + (r5 & 7) * D_ + c0);
    bst8(rr, mpos, u32x2{nosent(m0[0]), m0[1]}); bst8(rr, mpos + SLOT, ones2); bst8(rr, mpos + 2 * SLOT, ones2);
    DRAIN_BARRIER();
    bst4(rr, threadIdx.x < 16 ? xl(Z_CT + (l & 7) * 256 + (32 * L + u) * 4, l >> 3) : OOB, 1u);
    if (!((A.layers >> L) & 1)) return;
    const u32x4 s0_ = {RFL(rx[0]), RFL(rx[1]), RFL(rx[2]), RFL(rx[3])};
    const u32x4 s4_ = {RFL(rs[0]), RFL(rs[1]), RFL(rs[2]), RFL(rs[3])};
    const u32x4 s8_ = {RFL(L << 13), RFL(REC0), RFL(xl(RG_UT + (u * 4 + w) * 8192, L)), RFL(xl(RG_UTX + (u * 4 + w) * 2048, L))};
    const u32x4 s12_ = {RFL(xl(RG_UM + (u * 4 + w) * 2048, L)), RFL(xl(RG_UMX + (u * 4 + w) * 512, L)), RFL(xl(RG_PT + w * 16384, L)), RFL(xl(RG_PM + w * 8192, L))};
    const u32x4 s16_ = {RFL(xl(Z_X + 16384 + (u & 1) * 8192, L)), RFL(xl(Z_QV + (u & 3) * 256, L)), RFL(DBG0 + ((L * 128 + 64 + u) * 4 + w) * 2048), RFL(DBG0 + (2 << 20) + ((L * 32 + u) * 4 + w) * 8192)};
    const u32x4 v192_ = {(unsigned)(l * 16), (unsigned)(l * 16 + 4096), (unsigned)((l & 2) ? 0xeeeeu : 0x4444u), (unsigned)(0x4444u)};
    const u32x4 v196_ = {(unsigned)((l16 >> 2) * 256 + lq * 64 + (l & 3) * 16), (unsigned)(lq * 64 + (l & 3) * 16), (unsigned)((lq >> 1) * 2048 + 1024 + (lq & 1) * 256 + l16 * 16), (unsigned)(lq * 2048 + 1536 + l16 * 16)};
    const u32x4 v200_ = {(unsigned)(0 + w * 640 + lq * 160 + l16 * 4), (unsigned)(l16 < 4 ? 0 + w * 640 + lq * 160 + 64 + l16 * 4 : 12800), (unsigned)(0 + (l < 40 ? l : 0) * 16), (unsigned)(mpos)};
    const u32x4 v204_ = {(unsigned)(2560 + w * 2560 + lq * 320 + l16 * 4), (unsigned)(l16 < 4 ? 2560 + w * 2560 + lq * 320 + 64 + l16 * 4 : 12800), (unsigned)(3840 + w * 2560 + lq * 320 + l16 * 4), (unsigned)(l16 < 4 ? 3840 + w * 2560 + lq * 320 + 64 + l16 * 4 : 12800)};
    const u32x4 v208_ = {(unsigned)(2560 + w * 640 + (l < 40 ? l : 0) * 16), (unsigned)(xpos + RC_X1), (unsigned)(rpos), (unsigned)(l < 4 && w == 1 ? xl(Z_X1 + l * 4096 + u * 128, L) : OOB)};
    const u32x4 v212_ = {(unsigned)(l < 16 ? ((u >> 2) & 1) * 16384 + (u & 3) * 2048 + l * 128 : OOB), (unsigned)(0u), (unsigned)(0u), (unsigned)(0u)};
    asm volatile(
    "s_mov_b32 s20, 0\n"
    "v_lshrrev_b32 v4, 2, v192\n"
    ".Lct_po1_%=:\n"
    "buffer_load_dword v8, v4, s[0:3], s17 offen sc1\n"
    "s_waitcnt vmcnt(0)\n"
    "v_cmp_gt_i32 vcc, 1, v8\n"
    "s_nop 0\n"
    "s_cbranch_vccz .Lct_pd2_%=\n"
    "s_add_u32 s20, s20, 1\n"
    "s_cmp_lt_u32 s20, 0x40000\n"
    "s_cbranch_scc1 .Lct_sp3_%=\n"
    "v_mov_b32 v8, 40\n"
    "s_mov_b32 s24, 33728\n"
    "buffer_store_dword v8, off, s[0:3], s24 sc1\n"
    "s_waitcnt vmcnt(0)\n"
    "s_branch .Lct_pd2_%=\n"
    ".Lct_sp3_%=:\n"
    "s_sleep 4\n"
    "s_branch .Lct_po1_%=\n"
    ".Lct_pd2_%=:\n"
    "s_memrealtime s[22:23]\n"
    "s_waitcnt lgkmcnt(0)\n"
    "s_sub_u32 s41, s22, 864\n"
    "s_mov_b32 s26, -1\n"
    "s_add_u32 s26, s26, 1\n"
    "s_add_u32 s27, s26, 1\n"
    "s_add_u32 s28, s26, 0\n"
    "s_and_b32 s28, s28, 3\n"
    "s_lshl_b32 s28, s28, 22\n"
    "s_add_u32 s29, s26, 1\n"
    "s_and_b32 s29, s29, 3\n"
    "s_lshl_b32 s29, s29, 22\n"
    "s_add_u32 s30, s26, 3\n"
    "s_and_b32 s30, s30, 3\n"
    "s_lshl_b32 s30, s30, 22\n"
    "s_and_b32 s40, s26, 63\n"
    "s_lshl_b32 s40, s40, 7\n"
    "s_add_u32 s31, s12, s28\n"
    "s_add_u32 s32, s13, s28\n"
    "s_add_u32 s33, s15, s28\n"
    "s_add_u32 s34, s14, s28\n"
    "s_add_u32 s38, s10, s29\n"
    "s_add_u32 s39, s11, s29\n"
    "s_add_u32 s40, s16, s40\n"
    "s_add_u32 s35, s34, 4096\n"
    "s_add_u32 s36, s34, 16384\n"
    "s_add_u32 s37, s34, 20480\n"
    "s_add_u32 s43, s41, 340\n"
    "s_lshl_b32 s45, s26, 19\n"
    "s_lshl_b32 s25, s26, 11\n"
    "s_xor_b32 s25, s25, s8\n"
    "s_and_b32 s25, s25, 0x2000\n"
    "s_add_u32 s45, s45, s25\n"
    "s_add_u32 s45, s45, s9\n"
    "s_mul_i32 s46, s26, 40960\n"
    "s_cmp_eq_u32 s8, 0\n"
    "s_cselect_b32 s46, s46, s45\n"
    "v_mov_b64 v[76:77], 0\n"
    "v_mov_b64 v[78:79], 0\n"
    "v_mov_b64 v[80:81], 0\n"
    "v_mov_b64 v[82:83], 0\n"
    ".Lct_p0_%=:\n"
    "buffer_load_dwordx4 v[40:43], v192, s[0:3], s31 offen offset:0 sc1\n"
    "buffer_load_dwordx4 v[44:47], v192, s[0:3], s31 offen offset:1024 sc1\n"
    "buffer_load_dwordx4 v[48:51], v197, s[0:3], s32 offen offset:0 sc1\n"
    "buffer_load_dwordx4 v[52:55], v197, s[0:3], s32 offen offset:256 sc1\n"
    "s_waitcnt vmcnt(0)\n"
    "v_max3_u32 v184, v40, v42, v40\n"
    "v_max3_u32 v184, v44, v46, v184\n"
    "v_max3_u32 v184, v48, v50, v184\n"
    "v_max3_u32 v184, v52, v54, v184\n"
    "v_cmp_eq_u32 vcc, -1, v184\n"
    "s_nop 0\n"
    "s_cbranch_vccz .Lct_p1_%=\n"
    "s_add_u32 s20, s20, 1\n"
    "s_cmp_lt_u32 s20, 0x40000\n"
    "s_cbranch_scc1 .Lct_sp4_%=\n"
    "v_mov_b32 v184, 41\n"
    "s_mov_b32 s24, 33728\n"
    "buffer_store_dword v184, off, s[0:3], s24 sc1\n"
    "s_waitcnt vmcnt(0)\n"
    "s_branch .Lct_p1_%=\n"
    ".Lct_sp4_%=:\n"
    "s_sleep 8\n"
    "s_branch .Lct_p0_%=\n"
    ".Lct_p1_%=:\n"
    "s_mov_b32 s38, s10\n"
    "s_mov_b32 s39, s11\n"
    "buffer_load_dwordx4 a[0:3], v192, s[0:3], s38 offen offset:0 sc1\n"
    "buffer_load_dwordx4 a[4:7], v192, s[0:3], s38 offen offset:1024 sc1\n"
    "buffer_load_dwordx4 a[8:11], v192, s[0:3], s38 offen offset:2048 sc1\n"
    "buffer_load_dwordx4 a[12:15], v192, s[0:3], s38 offen offset:3072 sc1\n"
    "buffer_load_dwordx4 a[16:19], v193, s[0:3], s38 offen offset:0 sc1\n"
    "buffer_load_dwordx4 a[20:23], v193, s[0:3], s38 offen offset:1024 sc1\n"
    "buffer_load_dwordx4 a[24:27], v193, s[0:3], s38 offen offset:2048 sc1\n"
    "buffer_load_dwordx4 a[28:31], v193, s[0:3], s38 offen offset:3072 sc1\n"
    "buffer_load_dwordx4 v[0:3], v196, s[0:3], s39 offen offset:0 sc1\n"
    "buffer_load_dwordx4 v[4:7], v196, s[0:3], s39 offen offset:1024 sc1\n"
    "s_add_u32 s38, s10, s29\n"
    "s_add_u32 s39, s11, s29\n"
    "s_waitcnt vmcnt(0)\n"
    ".Lct_top_%=:\n"
    ".Lct_rt5_%=:\n"
    "s_memrealtime s[22:23]\n"
    "s_waitcnt lgkmcnt(0)\n"
    "s_sub_u32 s24, s43, s22\n"
    "s_cmp_lt_u32 s24, 600\n"
    "s_cbranch_scc1 .Lct_rt5_%=\n"
    "buffer_load_dwordx2 v[190:191], off, s[0:3], s40 sc1\n"
    "s_waitcnt vmcnt(2)\n"
    "v_mov_b64 v[8:9], v[0:1]\n"
    "v_mov_b64 v[10:11], v[2:3]\n"
    "v_mov_b64 v[12:13], v[4:5]\n"
    "v_mov_b64 v[14:15], v[6:7]\n"
    "v_mov_b32_dpp v16, v0 row_shl:4 row_mask:0xf bank_mask:0xf bound_ctrl:1\n"
    "v_mov_b32_dpp v17, v1 row_shl:4 row_mask:0xf bank_mask:0xf bound_ctrl:1\n"
    "v_mov_b32_dpp v18, v2 row_shl:4 row_mask:0xf bank_mask:0xf bound_ctrl:1\n"
    "v_mov_b32_dpp v19, v3 row_shl:4 row_mask:0xf bank_mask:0xf bound_ctrl:1\n"
    "v_mov_b32_dpp v20, v4 row_shl:4 row_mask:0xf bank_mask:0xf bound_ctrl:1\n"
    "v_mov_b32_dpp v21, v5 row_shl:4 row_mask:0xf bank_mask:0xf bound_ctrl:1\n"
    "v_mov_b32_dpp v22, v6 row_shl:4 row_mask:0xf bank_mask:0xf bound_ctrl:1\n"
    "v_mov_b32_dpp v23, v7 row_shl:4 row_mask:0xf bank_mask:0xf bound_ctrl:1\n"
    "v_mov_b32_dpp v24, v0 row_shl:8 row_mask:0xf bank_mask:0xf bound_ctrl:1\n"
    "v_mov_b32_dpp v25, v1 row_shl:8 row_mask:0xf bank_mask:0xf bound_ctrl:1\n"
    "v_mov_b32_dpp v26, v2 row_shl:8 row_mask:0xf bank_mask:0xf bound_ctrl:1\n"
    "v_mov_b32_dpp v27, v3 row_shl:8 row_mask:0xf bank_mask:0xf bound_ctrl:1\n"
    "v_mov_b32_dpp v28, v4 row_shl:8 row_mask:0xf bank_mask:0xf bound_ctrl:1\n"
    "v_mov_b32_dpp v29, v5 row_shl:8 row_mask:0xf bank_mask:0xf bound_ctrl:1\n"
    "v_mov_b32_dpp v30, v6 row_shl:8 row_mask:0xf bank_mask:0xf bound_ctrl:1\n"
    "v_mov_b32_dpp v31, v7 row_shl:8 row_mask:0xf bank_mask:0xf bound_ctrl:1\n"
    "v_mov_b32_dpp v32, v0 row_shl:12 row_mask:0xf bank_mask:0xf bound_ctrl:1\n"
    "v_mov_b32_dpp v33, v1 row_shl:12 row_mask:0xf bank_mask:0xf bound_ctrl:1\n"
    "v_mov_b32_dpp v34, v2 row_shl:12 row_mask:0xf bank_mask:0xf bound_ctrl:1\n"
    "v_mov_b32_dpp v35, v3 row_shl:12 row_mask:0xf bank_mask:0xf bound_ctrl:1\n"
    "v_mov_b32_dpp v36, v4 row_shl:12 row_mask:0xf bank_mask:0xf bound_ctrl:1\n"
    "v_mov_b32_dpp v37, v5 row_shl:12 row_mask:0xf bank_mask:0xf bound_ctrl:1\n"
    "v_mov_b32_dpp v38, v6 row_shl:12 row_mask:0xf bank_mask:0xf bound_ctrl:1\n"
    "v_mov_b32_dpp v39, v7 row_shl:12 row_mask:0xf bank_mask:0xf bound_ctrl:1\n"
    "v_mov_b32 v148, s26\n"
    "v_mov_b32 v149, s26\n"
    "v_mov_b32 v150, s26\n"
    "v_mov_b32 v151, s26\n"
    "s_waitcnt vmcnt(0)\n"
    "s_barrier\n"
    "buffer_store_dwordx4 v[148:151], v211, s[0:3], 0 offen sc1\n"
    ".Lct_x0_%=:\n"
    "v_readfirstlane_b32 s25, v191\n"
    "v_readfirstlane_b32 s41, v190\n"
    "s_cmp_eq_u32 s25, s27\n"
    "s_cbranch_scc0 .Lct_xr0_%=\n"
    ".Lct_xg0_%=:\n"
    "s_add_u32 s43, s41, 107\n"
    "s_add_u32 s44, s41, 123\n"
    ".Lct_rt6_%=:\n"
    "s_memrealtime s[22:23]\n"
    "s_waitcnt lgkmcnt(0)\n"
    "s_sub_u32 s24, s43, s22\n"
    "s_cmp_lt_u32 s24, 600\n"
    "s_cbranch_scc1 .Lct_rt6_%=\n"
    "buffer_load_dwordx4 v[40:43], v192, s[0:3], s31 offen offset:0 sc1\n"
    "buffer_load_dwordx4 v[44:47], v192, s[0:3], s31 offen offset:1024 sc1\n"
    "buffer_load_dwordx4 v[48:51], v197, s[0:3], s32 offen offset:0 sc1\n"
    "buffer_load_dwordx4 v[52:55], v197, s[0:3], s32 offen offset:256 sc1\n"
    ".Lct_rt7_%=:\n"
    "s_memrealtime s[22:23]\n"
    "s_waitcnt lgkmcnt(0)\n"
    "s_sub_u32 s24, s44, s22\n"
    "s_cmp_lt_u32 s24, 600\n"
    "s_cbranch_scc1 .Lct_rt7_%=\n"
    "buffer_load_dwordx4 v[72:75], v193, s[0:3], s33 offen sc1\n"
    "buffer_load_dwordx4 v[56:59], v192, s[0:3], s33 offen offset:0 sc1\n"
    "buffer_load_dwordx4 v[60:63], v192, s[0:3], s33 offen offset:1024 sc1\n"
    "buffer_load_dwordx4 v[64:67], v192, s[0:3], s33 offen offset:2048 sc1\n"
    "buffer_load_dwordx4 v[68:71], v192, s[0:3], s33 offen offset:3072 sc1\n"
    ".Lct_take0_%=:\n"
    "s_waitcnt vmcnt(5)\n"
    "v_max3_u32 v184, v40, v42, v40\n"
    "v_max3_u32 v184, v44, v46, v184\n"
    "v_max3_u32 v184, v48, v50, v184\n"
    "v_max3_u32 v184, v52, v54, v184\n"
    "s_waitcnt vmcnt(4)\n"
    "v_smfmac_f32_16x16x64_bf16 v[76:79], v[72:75], v[40:47], v194\n"
    "v_max3_u32 v184, v72, v74, v184\n"
    "v_smfmac_f32_16x16x64_bf16 v[80:83], v[72:75], v[48:55], v194\n"
    "s_waitcnt vmcnt(3)\n"
    "v_smfmac_f32_16x16x64_bf16 v[80:83], v[56:59], v[8:15], v194\n"
    "v_max3_u32 v184, v56, v58, v184\n"
    "v_smfmac_f32_16x16x64_bf16 v[76:79], v[56:59], a[0:7], v194\n"
    "s_waitcnt vmcnt(2)\n"
    "v_smfmac_f32_16x16x64_bf16 v[80:83], v[60:63], v[16:23], v194\n"
    "v_max3_u32 v184, v60, v62, v184\n"
    "v_smfmac_f32_16x16x64_bf16 v[76:79], v[60:63], a[8:15], v194\n"
    "s_waitcnt vmcnt(1)\n"
    "v_smfmac_f32_16x16x64_bf16 v[80:83], v[64:67], v[24:31], v194\n"
    "v_max3_u32 v184, v64, v66, v184\n"
    "v_smfmac_f32_16x16x64_bf16 v[76:79], v[64:67], a[16:23], v194\n"
    "s_waitcnt vmcnt(0)\n"
    "v_smfmac_f32_16x16x64_bf16 v[80:83], v[68:71], v[32:39], v194\n"
    "v_max3_u32 v184, v68, v70, v184\n"
    "v_smfmac_f32_16x16x64_bf16 v[76:79], v[68:71], a[24:31], v194\n"
    "v_cmp_eq_u32 vcc, -1, v184\n"
    "s_nop 0\n"
    "s_cbranch_vccnz .Lct_retry0_%=\n"
    ".Lct_go0_%=:\n"
    "s_nop 2\n"
    "v_pk_add_f32 v[80:81], v[80:81], v[82:83]\n"
    "ds_write2_b32 v201, v80, v81 offset1:20\n"
    "v_pk_add_f32 v[76:77], v[76:77], v[78:79]\n"
    "ds_write2_b32 v200, v76, v77 offset1:20\n"
    "s_waitcnt lgkmcnt(0)\n"
    "s_barrier\n"
    "ds_read_b128 v[164:167], v202 offset:0\n"
    "ds_read_b128 v[168:171], v202 offset:640\n"
    "ds_read_b128 v[172:175], v202 offset:1280\n"
    "ds_read_b128 v[176:179], v202 offset:1920\n"
    "s_waitcnt lgkmcnt(2)\n"
    "v_pk_add_f32 v[164:165], v[164:165], v[168:169]\n"
    "v_pk_add_f32 v[166:167], v[166:167], v[170:171]\n"
    "s_waitcnt lgkmcnt(0)\n"
    "v_pk_add_f32 v[172:173], v[172:173], v[176:177]\n"
    "v_pk_add_f32 v[174:175], v[174:175], v[178:179]\n"
    "v_pk_add_f32 v[164:165], v[164:165], v[172:173]\n"
    "v_pk_add_f32 v[166:167], v[166:167], v[174:175]\n"
    "v_cvt_pk_bf16_f32 v164, v164, v165\n"
    "v_cvt_pk_bf16_f32 v165, v166, v167\n"
    "v_min_u32 v164, -2, v164\n"
    "buffer_store_dwordx2 v[164:165], v203, s[0:3], s29 offen sc1\n"
    "s_sleep 2\n"
    "v_mov_b64 v[166:167], -1\n"
    "buffer_load_dwordx4 v[132:135], v199, s[0:3], s34 offen offset:0 sc1\n"
    "buffer_load_dwordx4 v[136:139], v199, s[0:3], s36 offen offset:0 sc1\n"
    "buffer_load_dwordx4 v[140:143], v199, s[0:3], s34 offen offset:256 sc1\n"
    "buffer_load_dwordx4 v[144:147], v199, s[0:3], s36 offen offset:256 sc1\n"
    "buffer_load_dwordx4 v[116:119], v198, s[0:3], s34 offen sc1\n"
    "buffer_load_dwordx4 v[84:87], v192, s[0:3], s34 offen offset:0 sc1\n"
    "buffer_load_dwordx4 v[88:91], v192, s[0:3], s34 offen offset:2048 sc1\n"
    "buffer_load_dwordx4 v[120:123], v198, s[0:3], s35 offen sc1\n"
    "buffer_load_dwordx4 v[92:95], v192, s[0:3], s35 offen offset:0 sc1\n"
    "buffer_load_dwordx4 v[96:99], v192, s[0:3], s35 offen offset:2048 sc1\n"
    "buffer_load_dwordx4 v[124:127], v198, s[0:3], s36 offen sc1\n"
    "s_waitcnt vmcnt(10)\n"
    "v_max3_u32 v184, v132, v134, v132\n"
    "v_swap_b32 v133, v134\n"
    "s_nop 1\n"
    "v_mfma_f32_16x16x32_bf16 v[148:151], v[132:135], v[40:43], 0\n"
    "v_mfma_f32_16x16x32_bf16 v[152:155], v[132:135], v[48:51], 0\n"
    "buffer_load_dwordx4 v[100:103], v192, s[0:3], s36 offen offset:0 sc1\n"
    "s_waitcnt vmcnt(10)\n"
    "v_max3_u32 v184, v136, v138, v184\n"
    "v_swap_b32 v137, v138\n"
    "s_nop 1\n"
    "v_mfma_f32_16x16x32_bf16 v[148:151], v[136:139], v[44:47], v[148:151]\n"
    "v_mfma_f32_16x16x32_bf16 v[152:155], v[136:139], v[52:55], v[152:155]\n"
    "buffer_load_dwordx4 v[104:107], v192, s[0:3], s36 offen offset:2048 sc1\n"
    "s_waitcnt vmcnt(10)\n"
    "v_max3_u32 v184, v140, v142, v184\n"
    "v_swap_b32 v141, v142\n"
    "s_nop 1\n"
    "v_mfma_f32_16x16x32_bf16 v[156:159], v[140:143], v[40:43], 0\n"
    "v_mfma_f32_16x16x32_bf16 v[160:163], v[140:143], v[48:51], 0\n"
    "buffer_load_dwordx4 v[128:131], v198, s[0:3], s37 offen sc1\n"
    "s_waitcnt vmcnt(10)\n"
    "v_max3_u32 v184, v144, v146, v184\n"
    "v_swap_b32 v145, v146\n"
    "s_nop 1\n"
    "v_mfma_f32_16x16x32_bf16 v[156:159], v[144:147], v[44:47], v[156:159]\n"
    "v_mfma_f32_16x16x32_bf16 v[160:163], v[144:147], v[52:55], v[160:163]\n"
    "buffer_load_dwordx4 v[108:111], v192, s[0:3], s37 offen offset:0 sc1\n"
    "s_waitcnt vmcnt(10)\n"
    "v_max3_u32 v184, v116, v118, v184\n"
    "v_smfmac_f32_16x16x64_bf16 v[148:151], v[116:119], a[0:7], v195\n"
    "v_smfmac_f32_16x16x64_bf16 v[152:155], v[116:119], v[8:15], v195\n"
    "buffer_load_dwordx4 v[112:115], v192, s[0:3], s37 offen offset:2048 sc1\n"
    "s_waitcnt vmcnt(10)\n"
    "v_max3_u32 v184, v84, v86, v184\n"
    "v_swap_b32 v85, v86\n"
    "s_nop 1\n"
    "v_mfma_f32_16x16x32_bf16 v[156:159], v[84:87], a[0:3], v[156:159]\n"
    "v_mfma_f32_16x16x32_bf16 v[160:163], v[84:87], v[8:11], v[160:163]\n"
    "buffer_load_dwordx2 v[186:187], v210, s[4:7], s46 offen sc1\n"
    "s_waitcnt vmcnt(10)\n"
    "v_max3_u32 v184, v88, v90, v184\n"
    "v_swap_b32 v89, v90\n"
    "s_nop 1\n"
    "v_mfma_f32_16x16x32_bf16 v[156:159], v[88:91], a[4:7], v[156:159]\n"
    "v_mfma_f32_16x16x32_bf16 v[160:163], v[88:91], v[12:15], v[160:163]\n"
    "buffer_store_dwordx2 v[166:167], v203, s[0:3], s30 offen sc1\n"
    "s_waitcnt vmcnt(10)\n"
    "v_max3_u32 v184, v120, v122, v184\n"
    "v_smfmac_f32_16x16x64_bf16 v[148:151], v[120:123], a[8:15], v195\n"
    "v_smfmac_f32_16x16x64_bf16 v[152:155], v[120:123], v[16:23], v195\n"
    "buffer_load_dwordx4 a[32:35], v192, s[0:3], s38 offen offset:0 sc1\n"
    "s_waitcnt vmcnt(10)\n"
    "v_max3_u32 v184, v92, v94, v184\n"
    "v_swap_b32 v93, v94\n"
    "s_nop 1\n"
    "v_mfma_f32_16x16x32_bf16 v[156:159], v[92:95], a[8:11], v[156:159]\n"
    "v_mfma_f32_16x16x32_bf16 v[160:163], v[92:95], v[16:19], v[160:163]\n"
    "buffer_load_dwordx4 a[36:39], v192, s[0:3], s38 offen offset:1024 sc1\n"
    "s_waitcnt vmcnt(10)\n"
    "v_max3_u32 v184, v96, v98, v184\n"
    "v_swap_b32 v97, v98\n"
    "s_nop 1\n"
    "v_mfma_f32_16x16x32_bf16 v[156:159], v[96:99], a[12:15], v[156:159]\n"
    "v_mfma_f32_16x16x32_bf16 v[160:163], v[96:99], v[20:23], v[160:163]\n"
    "buffer_load_dwordx4 a[40:43], v192, s[0:3], s38 offen offset:2048 sc1\n"
    "s_waitcnt vmcnt(10)\n"
    "v_max3_u32 v184, v124, v126, v184\n"
    "v_smfmac_f32_16x16x64_bf16 v[148:151], v[124:127], a[16:23], v195\n"
    "v_smfmac_f32_16x16x64_bf16 v[152:155], v[124:127], v[24:31], v195\n"
    "buffer_load_dwordx4 a[44:47], v192, s[0:3], s38 offen offset:3072 sc1\n"
    "s_waitcnt vmcnt(10)\n"
    "v_max3_u32 v184, v100, v102, v184\n"
    "v_swap_b32 v101, v102\n"
    "s_nop 1\n"
    "v_mfma_f32_16x16x32_bf16 v[156:159], v[100:103], a[16:19], v[156:159]\n"
    "v_mfma_f32_16x16x32_bf16 v[160:163], v[100:103], v[24:27], v[160:163]\n"
    "buffer_load_dwordx4 a[48:51], v193, s[0:3], s38 offen offset:0 sc1\n"
    "s_waitcnt vmcnt(10)\n"
    "v_max3_u32 v184, v104, v106, v184\n"
    "v_swap_b32 v105, v106\n"
    "s_nop 1\n"
    "v_mfma_f32_16x16x32_bf16 v[156:159], v[104:107], a[20:23], v[156:159]\n"
    "v_mfma_f32_16x16x32_bf16 v[160:163], v[104:107], v[28:31], v[160:163]\n"
    "buffer_load_dwordx4 a[52:55], v193, s[0:3], s38 offen offset:1024 sc1\n"
    "s_waitcnt vmcnt(10)\n"
    "v_max3_u32 v184, v128, v130, v184\n"
    "v_smfmac_f32_16x16x64_bf16 v[148:151], v[128:131], a[24:31], v195\n"
    "v_smfmac_f32_16x16x64_bf16 v[152:155], v[128:131], v[32:39], v195\n"
    "buffer_load_dwordx4 a[56:59], v193, s[0:3], s38 offen offset:2048 sc1\n"
    "s_waitcnt vmcnt(10)\n"
    "v_max3_u32 v184, v108, v110, v184\n"
    "v_swap_b32 v109, v110\n"
    "s_nop 1\n"
    "v_mfma_f32_16x16x32_bf16 v[156:159], v[108:111], a[24:27], v[156:159]\n"
    "v_mfma_f32_16x16x32_bf16 v[160:163], v[108:111], v[32:35], v[160:163]\n"
    "buffer_load_dwordx4 a[60:63], v193, s[0:3], s38 offen offset:3072 sc1\n"
    "s_waitcnt vmcnt(10)\n"
    "v_max3_u32 v184, v112, v114, v184\n"
    "v_swap_b32 v113, v114\n"
    "s_nop 1\n"
    "v_mfma_f32_16x16x32_bf16 v[156:159], v[112:115], a[28:31], v[156:159]\n"
    "v_mfma_f32_16x16x32_bf16 v[160:163], v[112:115], v[36:39], v[160:163]\n"
    "v_cmp_eq_u32 vcc, -1, v184\n"
    "s_nop 0\n"
    "s_cbranch_vccnz .Lct_tr0_%=\n"
    ".Lct_tg0_%=:\n"
    "ds_write2_b32 v204, v148, v149 offset0:0 offset1:20\n"
    "ds_write2_b32 v204, v150, v151 offset0:40 offset1:60\n"
    "ds_write2_b32 v205, v152, v153 offset0:0 offset1:20\n"
    "ds_write2_b32 v205, v154, v155 offset0:40 offset1:60\n"
    "ds_write2_b32 v206, v156, v157 offset0:0 offset1:20\n"
    "ds_write2_b32 v206, v158, v159 offset0:40 offset1:60\n"
    "ds_write2_b32 v207, v160, v161 offset0:0 offset1:20\n"
    "ds_write2_b32 v207, v162, v163 offset0:40 offset1:60\n"
    "s_mov_b32 s42, s45\n"
    "buffer_load_dwordx4 v[0:3], v196, s[0:3], s39 offen offset:0 sc1\n"
    "buffer_load_dwordx4 v[4:7], v196, s[0:3], s39 offen offset:1024 sc1\n"
    "s_add_u32 s26, s26, 1\n"
    "s_add_u32 s27, s26, 1\n"
    "s_add_u32 s28, s26, 0\n"
    "s_and_b32 s28, s28, 3\n"
    "s_lshl_b32 s28, s28, 22\n"
    "s_add_u32 s29, s26, 1\n"
    "s_and_b32 s29, s29, 3\n"
    "s_lshl_b32 s29, s29, 22\n"
    "s_add_u32 s30, s26, 3\n"
    "s_and_b32 s30, s30, 3\n"
    "s_lshl_b32 s30, s30, 22\n"
    "s_and_b32 s40, s26, 63\n"
    "s_lshl_b32 s40, s40, 7\n"
    "s_add_u32 s31, s12, s28\n"
    "s_add_u32 s32, s13, s28\n"
    "s_add_u32 s33, s15, s28\n"
    "s_add_u32 s34, s14, s28\n"
    "s_add_u32 s38, s10, s29\n"
    "s_add_u32 s39, s11, s29\n"
    "s_add_u32 s40, s16, s40\n"
    "s_add_u32 s35, s34, 4096\n"
    "s_add_u32 s36, s34, 16384\n"
    "s_add_u32 s37, s34, 20480\n"
    "s_add_u32 s43, s41, 340\n"
    "s_lshl_b32 s45, s26, 19\n"
    "s_lshl_b32 s25, s26, 11\n"
    "s_xor_b32 s25, s25, s8\n"
    "s_and_b32 s25, s25, 0x2000\n"
    "s_add_u32 s45, s45, s25\n"
    "s_add_u32 s45, s45, s9\n"
    "s_mul_i32 s46, s26, 40960\n"
    "s_cmp_eq_u32 s8, 0\n"
    "s_cselect_b32 s46, s46, s45\n"
    "v_mov_b64 v[76:77], 0\n"
    "v_mov_b64 v[78:79], 0\n"
    "v_mov_b64 v[80:81], 0\n"
    "v_mov_b64 v[82:83], 0\n"
    "s_waitcnt vmcnt(11)\n"
    "v_lshlrev_b32 v148, 16, v186\n"
    "v_and_b32 v149, 0xffff0000, v186\n"
    "v_lshlrev_b32 v150, 16, v187\n"
    "v_and_b32 v151, 0xffff0000, v187\n"
    "s_waitcnt lgkmcnt(0)\n"
    "s_barrier\n"
    "ds_read_b128 v[164:167], v208 offset:0\n"
    "ds_read_b128 v[168:171], v208 offset:2560\n"
    "ds_read_b128 v[172:175], v208 offset:5120\n"
    "ds_read_b128 v[176:179], v208 offset:7680\n"
    "s_waitcnt lgkmcnt(2)\n"
    "v_pk_add_f32 v[164:165], v[164:165], v[168:169]\n"
    "v_pk_add_f32 v[166:167], v[166:167], v[170:171]\n"
    "s_waitcnt lgkmcnt(0)\n"
    "v_pk_add_f32 v[172:173], v[172:173], v[176:177]\n"
    "v_pk_add_f32 v[174:175], v[174:175], v[178:179]\n"
    "v_pk_add_f32 v[164:165], v[164:165], v[172:173]\n"
    "v_pk_add_f32 v[166:167], v[166:167], v[174:175]\n"
    "v_cvt_pk_bf16_f32 v164, v164, v165\n"
    "v_cvt_pk_bf16_f32 v165, v166, v167\n"
    "v_lshlrev_b32 v168, 16, v164\n"
    "v_and_b32 v169, 0xffff0000, v164\n"
    "v_lshlrev_b32 v170, 16, v165\n"
    "v_and_b32 v171, 0xffff0000, v165\n"
    "v_pk_add_f32 v[168:169], v[168:169], v[148:149]\n"
    "v_pk_add_f32 v[170:171], v[170:171], v[150:151]\n"
    "v_cvt_pk_bf16_f32 v164, v168, v169\n"
    "v_cvt_pk_bf16_f32 v165, v170, v171\n"
    "buffer_store_dwordx2 v[164:165], v209, s[0:3], s42 offen sc1\n"
    ".Lct_rt11_%=:\n"
    "s_memrealtime s[22:23]\n"
    "s_waitcnt lgkmcnt(0)\n"
    "s_sub_u32 s24, s43, s22\n"
    "s_cmp_lt_u32 s24, 600\n"
    "s_cbranch_scc1 .Lct_rt11_%=\n"
    "buffer_load_dwordx2 v[190:191], off, s[0:3], s40 sc1\n"
    "s_waitcnt vmcnt(2)\n"
    "v_mov_b64 v[8:9], v[0:1]\n"
    "v_mov_b64 v[10:11], v[2:3]\n"
    "v_mov_b64 v[12:13], v[4:5]\n"
    "v_mov_b64 v[14:15], v[6:7]\n"
    "v_mov_b32_dpp v16, v0 row_shl:4 row_mask:0xf bank_mask:0xf bound_ctrl:1\n"
    "v_mov_b32_dpp v17, v1 row_shl:4 row_mask:0xf bank_mask:0xf bound_ctrl:1\n"
    "v_mov_b32_dpp v18, v2 row_shl:4 row_mask:0xf bank_mask:0xf bound_ctrl:1\n"
    "v_mov_b32_dpp v19, v3 row_shl:4 row_mask:0xf bank_mask:0xf bound_ctrl:1\n"
    "v_mov_b32_dpp v20, v4 row_shl:4 row_mask:0xf bank_mask:0xf bound_ctrl:1\n"
    "v_mov_b32_dpp v21, v5 row_shl:4 row_mask:0xf bank_mask:0xf bound_ctrl:1\n"
    "v_mov_b32_dpp v22, v6 row_shl:4 row_mask:0xf bank_mask:0xf bound_ctrl:1\n"
    "v_mov_b32_dpp v23, v7 row_shl:4 row_mask:0xf bank_mask:0xf bound_ctrl:1\n"
    "v_mov_b32_dpp v24, v0 row_shl:8 row_mask:0xf bank_mask:0xf bound_ctrl:1\n"
    "v_mov_b32_dpp v25, v1 row_shl:8 row_mask:0xf bank_mask:0xf bound_ctrl:1\n"
    "v_mov_b32_dpp v26, v2 row_shl:8 row_mask:0xf bank_mask:0xf bound_ctrl:1\n"
    "v_mov_b32_dpp v27, v3 row_shl:8 row_mask:0xf bank_mask:0xf bound_ctrl:1\n"
    "v_mov_b32_dpp v28, v4 row_shl:8 row_mask:0xf bank_mask:0xf bound_ctrl:1\n"
    "v_mov_b32_dpp v29, v5 row_shl:8 row_mask:0xf bank_mask:0xf bound_ctrl:1\n"
    "v_mov_b32_dpp v30, v6 row_shl:8 row_mask:0xf bank_mask:0xf bound_ctrl:1\n"
    "v_mov_b32_dpp v31, v7 row_shl:8 row_mask:0xf bank_mask:0xf bound_ctrl:1\n"
    "v_mov_b32_dpp v32, v0 row_shl:12 row_mask:0xf bank_mask:0xf bound_ctrl:1\n"
    "v_mov_b32_dpp v33, v1 row_shl:12 row_mask:0xf bank_mask:0xf bound_ctrl:1\n"
    "v_mov_b32_dpp v34, v2 row_shl:12 row_mask:0xf bank_mask:0xf bound_ctrl:1\n"
    "v_mov_b32_dpp v35, v3 row_shl:12 row_mask:0xf bank_mask:0xf bound_ctrl:1\n"
    "v_mov_b32_dpp v36, v4 row_shl:12 row_mask:0xf bank_mask:0xf bound_ctrl:1\n"
    "v_mov_b32_dpp v37, v5 row_shl:12 row_mask:0xf bank_mask:0xf bound_ctrl:1\n"
    "v_mov_b32_dpp v38, v6 row_shl:12 row_mask:0xf bank_mask:0xf bound_ctrl:1\n"
    "v_mov_b32_dpp v39, v7 row_shl:12 row_mask:0xf bank_mask:0xf bound_ctrl:1\n"
    "v_mov_b32 v148, s26\n"
    "v_mov_b32 v149, s26\n"
    "v_mov_b32 v150, s26\n"
    "v_mov_b32 v151, s26\n"
    "s_waitcnt vmcnt(0)\n"
    "s_barrier\n"
    "buffer_store_dwordx4 v[148:151], v211, s[0:3], 0 offen sc1\n"
    ".Lct_x1_%=:\n"
    "v_readfirstlane_b32 s25, v191\n"
    "v_readfirstlane_b32 s41, v190\n"
    "s_cmp_eq_u32 s25, s27\n"
    "s_cbranch_scc0 .Lct_xr1_%=\n"
    ".Lct_xg1_%=:\n"
    "s_add_u32 s43, s41, 107\n"
    "s_add_u32 s44, s41, 123\n"
    ".Lct_rt12_%=:\n"
    "s_memrealtime s[22:23]\n"
    "s_waitcnt lgkmcnt(0)\n"
    "s_sub_u32 s24, s43, s22\n"
    "s_cmp_lt_u32 s24, 600\n"
    "s_cbranch_scc1 .Lct_rt12_%=\n"
    "buffer_load_dwordx4 v[40:43], v192, s[0:3], s31 offen offset:0 sc1\n"
    "buffer_load_dwordx4 v[44:47], v192, s[0:3], s31 offen offset:1024 sc1\n"
    "buffer_load_dwordx4 v[48:51], v197, s[0:3], s32 offen offset:0 sc1\n"
    "buffer_load_dwordx4 v[52:55], v197, s[0:3], s32 offen offset:256 sc1\n"
    ".Lct_rt13_%=:\n"
    "s_memrealtime s[22:23]\n"
    "s_waitcnt lgkmcnt(0)\n"
    "s_sub_u32 s24, s44, s22\n"
    "s_cmp_lt_u32 s24, 600\n"
    "s_cbranch_scc1 .Lct_rt13_%=\n"
    "buffer_load_dwordx4 v[72:75], v193, s[0:3], s33 offen sc1\n"
    "buffer_load_dwordx4 v[56:59], v192, s[0:3], s33 offen offset:0 sc1\n"
    "buffer_load_dwordx4 v[60:63], v192, s[0:3], s33 offen offset:1024 sc1\n"
    "buffer_load_dwordx4 v[64:67], v192, s[0:3], s33 offen offset:2048 sc1\n"
    "buffer_load_dwordx4 v[68:71], v192, s[0:3], s33 offen offset:3072 sc1\n"
    ".Lct_take1_%=:\n"
    "s_waitcnt vmcnt(5)\n"
    "v_max3_u32 v184, v40, v42, v40\n"
    "v_max3_u32 v184, v44, v46, v184\n"
    "v_max3_u32 v184, v48, v50, v184\n"
    "v_max3_u32 v184, v52, v54, v184\n"
    "s_waitcnt vmcnt(4)\n"
    "v_smfmac_f32_16x16x64_bf16 v[76:79], v[72:75], v[40:47], v194\n"
    "v_max3_u32 v184, v72, v74, v184\n"
    "v_smfmac_f32_16x16x64_bf16 v[80:83], v[72:75], v[48:55], v194\n"
    "s_waitcnt vmcnt(3)\n"
    "v_smfmac_f32_16x16x64_bf16 v[80:83], v[56:59], v[8:15], v194\n"
    "v_max3_u32 v184, v56, v58, v184\n"
    "v_smfmac_f32_16x16x64_bf16 v[76:79], v[56:59], a[32:39], v194\n"
    "s_waitcnt vmcnt(2)\n"
    "v_smfmac_f32_16x16x64_bf16 v[80:83], v[60:63], v[16:23], v194\n"
    "v_max3_u32 v184, v60, v62, v184\n"
    "v_smfmac_f32_16x16x64_bf16 v[76:79], v[60:63], a[40:47], v194\n"
    "s_waitcnt vmcnt(1)\n"
    "v_smfmac_f32_16x16x64_bf16 v[80:83], v[64:67], v[24:31], v194\n"
    "v_max3_u32 v184, v64, v66, v184\n"
    "v_smfmac_f32_16x16x64_bf16 v[76:79], v[64:67], a[48:55], v194\n"
    "s_waitcnt vmcnt(0)\n"
    "v_smfmac_f32_16x16x64_bf16 v[80:83], v[68:71], v[32:39], v194\n"
    "v_max3_u32 v184, v68, v70, v184\n"
    "v_smfmac_f32_16x16x64_bf16 v[76:79], v[68:71], a[56:63], v194\n"
    "v_cmp_eq_u32 vcc, -1, v184\n"
    "s_nop 0\n"
    "s_cbranch_vccnz .Lct_retry1_%=\n"
    ".Lct_go1_%=:\n"
    "s_nop 2\n"
    "v_pk_add_f32 v[80:81], v[80:81], v[82:83]\n"
    "ds_write2_b32 v201, v80, v81 offset1:20\n"
    "v_pk_add_f32 v[76:77], v[76:77], v[78:79]\n"
    "ds_write2_b32 v200, v76, v77 offset1:20\n"
    "s_waitcnt lgkmcnt(0)\n"
    "s_barrier\n"
    "ds_read_b128 v[164:167], v202 offset:0\n"
    "ds_read_b128 v[168:171], v202 offset:640\n"
    "ds_read_b128 v[172:175], v202 offset:1280\n"
    "ds_read_b128 v[176:179], v202 offset:1920\n"
    "s_waitcnt lgkmcnt(2)\n"
    "v_pk_add_f32 v[164:165], v[164:165], v[168:169]\n"
    "v_pk_add_f32 v[166:167], v[166:167], v[170:171]\n"
    "s_waitcnt lgkmcnt(0)\n"
    "v_pk_add_f32 v[172:173], v[172:173], v[176:177]\n"
    "v_pk_add_f32 v[174:175], v[174:175], v[178:179]\n"
    "v_pk_add_f32 v[164:165], v[164:165], v[172:173]\n"
    "v_pk_add_f32 v[166:167], v[166:167], v[174:175]\n"
    "v_cvt_pk_bf16_f32 v164, v164, v165\n"
    "v_cvt_pk_bf16_f32 v165, v166, v167\n"
    "v_min_u32 v164, -2, v164\n"
    "buffer_store_dwordx2 v[164:165], v203, s[0:3], s29 offen sc1\n"
    "s_sleep 2\n"
    "v_mov_b64 v[166:167], -1\n"
    "buffer_load_dwordx4 v[132:135], v199, s[0:3], s34 offen offset:0 sc1\n"
    "buffer_load_dwordx4 v[136:139], v199, s[0:3], s36 offen offset:0 sc1\n"
    "buffer_load_dwordx4 v[140:143], v199, s[0:3], s34 offen offset:256 sc1\n"
    "buffer_load_dwordx4 v[144:147], v199, s[0:3], s36 offen offset:256 sc1\n"
    "buffer_load_dwordx4 v[116:119], v198, s[0:3], s34 offen sc1\n"
    "buffer_load_dwordx4 v[84:87], v192, s[0:3], s34 offen offset:0 sc1\n"
    "buffer_load_dwordx4 v[88:91], v192, s[0:3], s34 offen offset:2048 sc1\n"
    "buffer_load_dwordx4 v[120:123], v198, s[0:3], s35 offen sc1\n"
    "buffer_load_dwordx4 v[92:95], v192, s[0:3], s35 offen offset:0 sc1\n"
    "buffer_load_dwordx4 v[96:99], v192, s[0:3], s35 offen offset:2048 sc1\n"
    "buffer_load_dwordx4 v[124:127], v198, s[0:3], s36 offen sc1\n"
    "s_waitcnt vmcnt(10)\n"
    "v_max3_u32 v184, v132, v134, v132\n"
    "v_swap_b32 v133, v134\n"
    "s_nop 1\n"
    "v_mfma_f32_16x16x32_bf16 v[148:151], v[132:135], v[40:43], 0\n"
    "v_mfma_f32_16x16x32_bf16 v[152:155], v[132:135], v[48:51], 0\n"
    "buffer_load_dwordx4 v[100:103], v192, s[0:3], s36 offen offset:0 sc1\n"
    "s_waitcnt vmcnt(10)\n"
    "v_max3_u32 v184, v136, v138, v184\n"
    "v_swap_b32 v137, v138\n"
    "s_nop 1\n"
    "v_mfma_f32_16x16x32_bf16 v[148:151], v[136:139], v[44:47], v[148:151]\n"
    "v_mfma_f32_16x16x32_bf16 v[152:155], v[136:139], v[52:55], v[152:155]\n"
    "buffer_load_dwordx4 v[104:107], v192, s[0:3], s36 offen offset:2048 sc1\n"
    "s_waitcnt vmcnt(10)\n"
    "v_max3_u32 v184, v140, v142, v184\n"
    "v_swap_b32 v141, v142\n"
    "s_nop 1\n"
    "v_mfma_f32_16x16x32_bf16 v[156:159], v[140:143], v[40:43], 0\n"
    "v_mfma_f32_16x16x32_bf16 v[160:163], v[140:143], v[48:51], 0\n"
    "buffer_load_dwordx4 v[128:131], v198, s[0:3], s37 offen sc1\n"
    "s_waitcnt vmcnt(10)\n"
    "v_max3_u32 v184, v144, v146, v184\n"
    "v_swap_b32 v145, v146\n"
    "s_nop 1\n"
    "v_mfma_f32_16x16x32_bf16 v[156:159], v[144:147], v[44:47], v[156:159]\n"
    "v_mfma_f32_16x16x32_bf16 v[160:163], v[144:147], v[52:55], v[160:163]\n"
    "buffer_load_dwordx4 v[108:111], v192, s[0:3], s37 offen offset:0 sc1\n"
    "s_waitcnt vmcnt(10)\n"
    "v_max3_u32 v184, v116, v118, v184\n"
    "v_smfmac_f32_16x16x64_bf16 v[148:151], v[116:119], a[32:39], v195\n"
    "v_smfmac_f32_16x16x64_bf16 v[152:155], v[116:119], v[8:15], v195\n"
    "buffer_load_dwordx4 v[112:115], v192, s[0:3], s37 offen offset:2048 sc1\n"
    "s_waitcnt vmcnt(10)\n"
    "v_max3_u32 v184, v84, v86, v184\n"
    "v_swap_b32 v85, v86\n"
    "s_nop 1\n"
    "v_mfma_f32_16x16x32_bf16 v[156:159], v[84:87], a[32:35], v[156:159]\n"
    "v_mfma_f32_16x16x32_bf16 v[160:163], v[84:87], v[8:11], v[160:163]\n"
    "buffer_load_dwordx2 v[186:187], v210, s[4:7], s46 offen sc1\n"
    "s_waitcnt vmcnt(10)\n"
    "v_max3_u32 v184, v88, v90, v184\n"
    "v_swap_b32 v89, v90\n"
    "s_nop 1\n"
    "v_mfma_f32_16x16x32_bf16 v[156:159], v[88:91], a[36:39], v[156:159]\n"
    "v_mfma_f32_16x16x32_bf16 v[160:163], v[88:91], v[12:15], v[160:163]\n"
    "buffer_store_dwordx2 v[166:167], v203, s[0:3], s30 offen sc1\n"
    "s_waitcnt vmcnt(10)\n"
    "v_max3_u32 v184, v120, v122, v184\n"
    "v_smfmac_f32_16x16x64_bf16 v[148:151], v[120:123], a[40:47], v195\n"
    "v_smfmac_f32_16x16x64_bf16 v[152:155], v[120:123], v[16:23], v195\n"
    "buffer_load_dwordx4 a[0:3], v192, s[0:3], s38 offen offset:0 sc1\n"
    "s_waitcnt vmcnt(10)\n"
    "v_max3_u32 v184, v92, v94, v184\n"
    "v_swap_b32 v93, v94\n"
    "s_nop 1\n"
    "v_mfma_f32_16x16x32_bf16 v[156:159], v[92:95], a[40:43], v[156:159]\n"
    "v_mfma_f32_16x16x32_bf16 v[160:163], v[92:95], v[16:19], v[160:163]\n"
    "buffer_load_dwordx4 a[4:7], v192, s[0:3], s38 offen offset:1024 sc1\n"
    "s_waitcnt vmcnt(10)\n"
    "v_max3_u32 v184, v96, v98, v184\n"
    "v_swap_b32 v97, v98\n"
    "s_nop 1\n"
    "v_mfma_f32_16x16x32_bf16 v[156:159], v[96:99], a[44:47], v[156:159]\n"
    "v_mfma_f32_16x16x32_bf16 v[160:163], v[96:99], v[20:23], v[160:163]\n"
    "buffer_load_dwordx4 a[8:11], v192, s[0:3], s38 offen offset:2048 sc1\n"
    "s_waitcnt vmcnt(10)\n"
    "v_max3_u32 v184, v124, v126, v184\n"
    "v_smfmac_f32_16x16x64_bf16 v[148:151], v[124:127], a[48:55], v195\n"
    "v_smfmac_f32_16x16x64_bf16 v[152:155], v[124:127], v[24:31], v195\n"
    "buffer_load_dwordx4 a[12:15], v192, s[0:3], s38 offen offset:3072 sc1\n"
    "s_waitcnt vmcnt(10)\n"
    "v_max3_u32 v184, v100, v102, v184\n"
    "v_swap_b32 v101, v102\n"
    "s_nop 1\n"
    "v_mfma_f32_16x16x32_bf16 v[156:159], v[100:103], a[48:51], v[156:159]\n"
    "v_mfma_f32_16x16x32_bf16 v[160:163], v[100:103], v[24:27], v[160:163]\n"
    "buffer_load_dwordx4 a[16:19], v193, s[0:3], s38 offen offset:0 sc1\n"
    "s_waitcnt vmcnt(10)\n"
    "v_max3_u32 v184, v104, v106, v184\n"
    "v_swap_b32 v105, v106\n"
    "s_nop 1\n"
    "v_mfma_f32_16x16x32_bf16 v[156:159], v[104:107], a[52:55], v[156:159]\n"
    "v_mfma_f32_16x16x32_bf16 v[160:163], v[104:107], v[28:31], v[160:163]\n"
    "buffer_load_dwordx4 a[20:23], v193, s[0:3], s38 offen offset:1024 sc1\n"
    "s_waitcnt vmcnt(10)\n"
    "v_max3_u32 v184, v128, v130, v184\n"
    "v_smfmac_f32_16x16x64_bf16 v[148:151], v[128:131], a[56:63], v195\n"
    "v_smfmac_f32_16x16x64_bf16 v[152:155], v[128:131], v[32:39], v195\n"
    "buffer_load_dwordx4 a[24:27], v193, s[0:3], s38 offen offset:2048 sc1\n"
    "s_waitcnt vmcnt(10)\n"
    "v_max3_u32 v184, v108, v110, v184\n"
    "v_swap_b32 v109, v110\n"
    "s_nop 1\n"
    "v_mfma_f32_16x16x32_bf16 v[156:159], v[108:111], a[56:59], v[156:159]\n"
    "v_mfma_f32_16x16x32_bf16 v[160:163], v[108:111], v[32:35], v[160:163]\n"
    "buffer_load_dwordx4 a[28:31], v193, s[0:3], s38 offen offset:3072 sc1\n"
    "s_waitcnt vmcnt(10)\n"
    "v_max3_u32 v184, v112, v114, v184\n"
    "v_swap_b32 v113, v114\n"
    "s_nop 1\n"
    "v_mfma_f32_16x16x32_bf16 v[156:159], v[112:115], a[60:63], v[156:159]\n"
    "v_mfma_f32_16x16x32_bf16 v[160:163], v[112:115], v[36:39], v[160:163]\n"
    "v_cmp_eq_u32 vcc, -1, v184\n"
    "s_nop 0\n"
    "s_cbranch_vccnz .Lct_tr1_%=\n"
    ".Lct_tg1_%=:\n"
    "ds_write2_b32 v204, v148, v149 offset0:0 offset1:20\n"
    "ds_write2_b32 v204, v150, v151 offset0:40 offset1:60\n"
    "ds_write2_b32 v205, v152, v153 offset0:0 offset1:20\n"
    "ds_write2_b32 v205, v154, v155 offset0:40 offset1:60\n"
    "ds_write2_b32 v206, v156, v157 offset0:0 offset1:20\n"
    "ds_write2_b32 v206, v158, v159 offset0:40 offset1:60\n"
    "ds_write2_b32 v207, v160, v161 offset0:0 offset1:20\n"
    "ds_write2_b32 v207, v162, v163 offset0:40 offset1:60\n"
    "s_mov_b32 s42, s45\n"
    "buffer_load_dwordx4 v[0:3], v196, s[0:3], s39 offen offset:0 sc1\n"
    "buffer_load_dwordx4 v[4:7], v196, s[0:3], s39 offen offset:1024 sc1\n"
    "s_add_u32 s26, s26, 1\n"
    "s_add_u32 s27, s26, 1\n"
    "s_add_u32 s28, s26, 0\n"
    "s_and_b32 s28, s28, 3\n"
    "s_lshl_b32 s28, s28, 22\n"
    "s_add_u32 s29, s26, 1\n"
    "s_and_b32 s29, s29, 3\n"
    "s_lshl_b32 s29, s29, 22\n"
    "s_add_u32 s30, s26, 3\n"
    "s_and_b32 s30, s30, 3\n"
    "s_lshl_b32 s30, s30, 22\n"
    "s_and_b32 s40, s26, 63\n"
    "s_lshl_b32 s40, s40, 7\n"
    "s_add_u32 s31, s12, s28\n"
    "s_add_u32 s32, s13, s28\n"
    "s_add_u32 s33, s15, s28\n"
    "s_add_u32 s34, s14, s28\n"
    "s_add_u32 s38, s10, s29\n"
    "s_add_u32 s39, s11, s29\n"
    "s_add_u32 s40, s16, s40\n"
    "s_add_u32 s35, s34, 4096\n"
    "s_add_u32 s36, s34, 16384\n"
    "s_add_u32 s37, s34, 20480\n"
    "s_add_u32 s43, s41, 340\n"
    "s_lshl_b32 s45, s26, 19\n"
    "s_lshl_b32 s25, s26, 11\n"
    "s_xor_b32 s25, s25, s8\n"
    "s_and_b32 s25, s25, 0x2000\n"
    "s_add_u32 s45, s45, s25\n"
    "s_add_u32 s45, s45, s9\n"
    "s_mul_i32 s46, s26, 40960\n"
    "s_cmp_eq_u32 s8, 0\n"
    "s_cselect_b32 s46, s46, s45\n"
    "v_mov_b64 v[76:77], 0\n"
    "v_mov_b64 v[78:79], 0\n"
    "v_mov_b64 v[80:81], 0\n"
    "v_mov_b64 v[82:83], 0\n"
    "s_waitcnt vmcnt(11)\n"
    "v_lshlrev_b32 v148, 16, v186\n"
    "v_and_b32 v149, 0xffff0000, v186\n"
    "v_lshlrev_b32 v150, 16, v187\n"
    "v_and_b32 v151, 0xffff0000, v187\n"
    "s_waitcnt lgkmcnt(0)\n"
    "s_barrier\n"
    "ds_read_b128 v[164:167], v208 offset:0\n"
    "ds_read_b128 v[168:171], v208 offset:2560\n"
    "ds_read_b128 v[172:175], v208 offset:5120\n"
    "ds_read_b128 v[176:179], v208 offset:7680\n"
    "s_waitcnt lgkmcnt(2)\n"
    "v_pk_add_f32 v[164:165], v[164:165], v[168:169]\n"
    "v_pk_add_f32 v[166:167], v[166:167], v[170:171]\n"
    "s_waitcnt lgkmcnt(0)\n"
    "v_pk_add_f32 v[172:173], v[172:173], v[176:177]\n"
    "v_pk_add_f32 v[174:175], v[174:175], v[178:179]\n"
    "v_pk_add_f32 v[164:165], v[164:165], v[172:173]\n"
    "v_pk_add_f32 v[166:167], v[166:167], v[174:175]\n"
    "v_cvt_pk_bf16_f32 v164, v164, v165\n"
    "v_cvt_pk_bf16_f32 v165, v166, v167\n"
    "v_lshlrev_b32 v168, 16, v164\n"
    "v_and_b32 v169, 0xffff0000, v164\n"
    "v_lshlrev_b32 v170, 16, v165\n"
    "v_and_b32 v171, 0xffff0000, v165\n"
    "v_pk_add_f32 v[168:169], v[168:169], v[148:149]\n"
    "v_pk_add_f32 v[170:171], v[170:171], v[150:151]\n"
    "v_cvt_pk_bf16_f32 v164, v168, v169\n"
    "v_cvt_pk_bf16_f32 v165, v170, v171\n"
    "buffer_store_dwordx2 v[164:165], v209, s[0:3], s42 offen sc1\n"
    "s_cmp_lt_u32 s26, 64\n"
    "s_cbranch_scc1 .Lct_top_%=\n"
    "v_mov_b32 v148, s26\n"
    "v_mov_b32 v149, s26\n"
    "v_mov_b32 v150, s26\n"
    "v_mov_b32 v151, s26\n"
    "s_waitcnt vmcnt(0)\n"
    "s_barrier\n"
    "buffer_store_dwordx4 v[148:151], v211, s[0:3], 0 offen sc1\n"
    "s_waitcnt vmcnt(0) lgkmcnt(0)\n"
    "s_endpgm\n"
    ".Lct_xr0_%=:\n"
    "s_add_u32 s20, s20, 1\n"
    "s_cmp_lt_u32 s20, 0x40000\n"
    "s_cbranch_scc1 .Lct_sp8_%=\n"
    "v_mov_b32 v184, 42\n"
    "s_mov_b32 s24, 33728\n"
    "buffer_store_dword v184, off, s[0:3], s24 sc1\n"
    "s_waitcnt vmcnt(0)\n"
    "s_branch .Lct_xg0_%=\n"
    ".Lct_sp8_%=:\n"
    "s_sleep 4\n"
    "buffer_load_dwordx2 v[190:191], off, s[0:3], s40 sc1\n"
    "s_waitcnt vmcnt(0)\n"
    "s_branch .Lct_x0_%=\n"
    ".Lct_retry0_%=:\n"
    "s_add_u32 s20, s20, 1\n"
    "s_cmp_lt_u32 s20, 0x40000\n"
    "s_cbranch_scc1 .Lct_sp9_%=\n"
    "v_mov_b32 v184, 3\n"
    "s_mov_b32 s24, 33728\n"
    "buffer_store_dword v184, off, s[0:3], s24 sc1\n"
    "s_waitcnt vmcnt(0)\n"
    "s_branch .Lct_go0_%=\n"
    ".Lct_sp9_%=:\n"
    "s_waitcnt vmcnt(0)\n"
    "v_mov_b64 v[76:77], 0\n"
    "v_mov_b64 v[78:79], 0\n"
    "v_mov_b64 v[80:81], 0\n"
    "v_mov_b64 v[82:83], 0\n"
    "buffer_load_dwordx4 v[40:43], v192, s[0:3], s31 offen offset:0 sc1\n"
    "buffer_load_dwordx4 v[44:47], v192, s[0:3], s31 offen offset:1024 sc1\n"
    "buffer_load_dwordx4 v[48:51], v197, s[0:3], s32 offen offset:0 sc1\n"
    "buffer_load_dwordx4 v[52:55], v197, s[0:3], s32 offen offset:256 sc1\n"
    "buffer_load_dwordx4 v[72:75], v193, s[0:3], s33 offen sc1\n"
    "buffer_load_dwordx4 v[56:59], v192, s[0:3], s33 offen offset:0 sc1\n"
    "buffer_load_dwordx4 v[60:63], v192, s[0:3], s33 offen offset:1024 sc1\n"
    "buffer_load_dwordx4 v[64:67], v192, s[0:3], s33 offen offset:2048 sc1\n"
    "buffer_load_dwordx4 v[68:71], v192, s[0:3], s33 offen offset:3072 sc1\n"
    "s_waitcnt vmcnt(0)\n"
    "s_branch .Lct_take0_%=\n"
    ".Lct_tr0_%=:\n"
    "s_add_u32 s20, s20, 1\n"
    "s_cmp_lt_u32 s20, 0x40000\n"
    "s_cbranch_scc1 .Lct_sp10_%=\n"
    "v_mov_b32 v184, 4\n"
    "s_mov_b32 s24, 33728\n"
    "buffer_store_dword v184, off, s[0:3], s24 sc1\n"
    "s_waitcnt vmcnt(0)\n"
    "s_branch .Lct_tg0_%=\n"
    ".Lct_sp10_%=:\n"
    "buffer_load_dwordx4 v[132:135], v199, s[0:3], s34 offen offset:0 sc1\n"
    "buffer_load_dwordx4 v[136:139], v199, s[0:3], s36 offen offset:0 sc1\n"
    "buffer_load_dwordx4 v[140:143], v199, s[0:3], s34 offen offset:256 sc1\n"
    "buffer_load_dwordx4 v[144:147], v199, s[0:3], s36 offen offset:256 sc1\n"
    "buffer_load_dwordx4 v[116:119], v198, s[0:3], s34 offen sc1\n"
    "buffer_load_dwordx4 v[84:87], v192, s[0:3], s34 offen offset:0 sc1\n"
    "buffer_load_dwordx4 v[88:91], v192, s[0:3], s34 offen offset:2048 sc1\n"
    "buffer_load_dwordx4 v[120:123], v198, s[0:3], s35 offen sc1\n"
    "buffer_load_dwordx4 v[92:95], v192, s[0:3], s35 offen offset:0 sc1\n"
    "buffer_load_dwordx4 v[96:99], v192, s[0:3], s35 offen offset:2048 sc1\n"
    "buffer_load_dwordx4 v[124:127], v198, s[0:3], s36 offen sc1\n"
    "buffer_load_dwordx4 v[100:103], v192, s[0:3], s36 offen offset:0 sc1\n"
    "buffer_load_dwordx4 v[104:107], v192, s[0:3], s36 offen offset:2048 sc1\n"
    "buffer_load_dwordx4 v[128:131], v198, s[0:3], s37 offen sc1\n"
    "buffer_load_dwordx4 v[108:111], v192, s[0:3], s37 offen offset:0 sc1\n"
    "buffer_load_dwordx4 v[112:115], v192, s[0:3], s37 offen offset:2048 sc1\n"
    "s_waitcnt vmcnt(0)\n"
    "v_max3_u32 v184, v132, v134, v132\n"
    "v_swap_b32 v133, v134\n"
    "s_nop 1\n"
    "v_mfma_f32_16x16x32_bf16 v[148:151], v[132:135], v[40:43], 0\n"
    "v_mfma_f32_16x16x32_bf16 v[152:155], v[132:135], v[48:51], 0\n"
    "v_max3_u32 v184, v136, v138, v184\n"
    "v_swap_b32 v137, v138\n"
    "s_nop 1\n"
    "v_mfma_f32_16x16x32_bf16 v[148:151], v[136:139], v[44:47], v[148:151]\n"
    "v_mfma_f32_16x16x32_bf16 v[152:155], v[136:139], v[52:55], v[152:155]\n"
    "v_max3_u32 v184, v140, v142, v184\n"
    "v_swap_b32 v141, v142\n"
    "s_nop 1\n"
    "v_mfma_f32_16x16x32_bf16 v[156:159], v[140:143], v[40:43], 0\n"
    "v_mfma_f32_16x16x32_bf16 v[160:163], v[140:143], v[48:51], 0\n"
    "v_max3_u32 v184, v144, v146, v184\n"
    "v_swap_b32 v145, v146\n"
    "s_nop 1\n"
    "v_mfma_f32_16x16x32_bf16 v[156:159], v[144:147], v[44:47], v[156:159]\n"
    "v_mfma_f32_16x16x32_bf16 v[160:163], v[144:147], v[52:55], v[160:163]\n"
    "v_max3_u32 v184, v116, v118, v184\n"
    "v_smfmac_f32_16x16x64_bf16 v[148:151], v[116:119], a[0:7], v195\n"
    "v_smfmac_f32_16x16x64_bf16 v[152:155], v[116:119], v[8:15], v195\n"
    "v_max3_u32 v184, v84, v86, v184\n"
    "v_swap_b32 v85, v86\n"
    "s_nop 1\n"
    "v_mfma_f32_16x16x32_bf16 v[156:159], v[84:87], a[0:3], v[156:159]\n"
    "v_mfma_f32_16x16x32_bf16 v[160:163], v[84:87], v[8:11], v[160:163]\n"
    "v_max3_u32 v184, v88, v90, v184\n"
    "v_swap_b32 v89, v90\n"
    "s_nop 1\n"
    "v_mfma_f32_16x16x32_bf16 v[156:159], v[88:91], a[4:7], v[156:159]\n"
    "v_mfma_f32_16x16x32_bf16 v[160:163], v[88:91], v[12:15], v[160:163]\n"
    "v_max3_u32 v184, v120, v122, v184\n"
    "v_smfmac_f32_16x16x64_bf16 v[148:151], v[120:123], a[8:15], v195\n"
    "v_smfmac_f32_16x16x64_bf16 v[152:155], v[120:123], v[16:23], v195\n"
    "v_max3_u32 v184, v92, v94, v184\n"
    "v_swap_b32 v93, v94\n"
    "s_nop 1\n"
    "v_mfma_f32_16x16x32_bf16 v[156:159], v[92:95], a[8:11], v[156:159]\n"
    "v_mfma_f32_16x16x32_bf16 v[160:163], v[92:95], v[16:19], v[160:163]\n"
    "v_max3_u32 v184, v96, v98, v184\n"
    "v_swap_b32 v97, v98\n"
    "s_nop 1\n"
    "v_mfma_f32_16x16x32_bf16 v[156:159], v[96:99], a[12:15], v[156:159]\n"
    "v_mfma_f32_16x16x32_bf16 v[160:163], v[96:99], v[20:23], v[160:163]\n"
    "v_max3_u32 v184, v124, v126, v184\n"
    "v_smfmac_f32_16x16x64_bf16 v[148:151], v[124:127], a[16:23], v195\n"
    "v_smfmac_f32_16x16x64_bf16 v[152:155], v[124:127], v[24:31], v195\n"
    "v_max3_u32 v184, v100, v102, v184\n"
    "v_swap_b32 v101, v102\n"
    "s_nop 1\n"
    "v_mfma_f32_16x16x32_bf16 v[156:159], v[100:103], a[16:19], v[156:159]\n"
    "v_mfma_f32_16x16x32_bf16 v[160:163], v[100:103], v[24:27], v[160:163]\n"
    "v_max3_u32 v184, v104, v106, v184\n"
    "v_swap_b32 v105, v106\n"
    "s_nop 1\n"
    "v_mfma_f32_16x16x32_bf16 v[156:159], v[104:107], a[20:23], v[156:159]\n"
    "v_mfma_f32_16x16x32_bf16 v[160:163], v[104:107], v[28:31], v[160:163]\n"
    "v_max3_u32 v184, v128, v130, v184\n"
    "v_smfmac_f32_16x16x64_bf16 v[148:151], v[128:131], a[24:31], v195\n"
    "v_smfmac_f32_16x16x64_bf16 v[152:155], v[128:131], v[32:39], v195\n"
    "v_max3_u32 v184, v108, v110, v184\n"
    "v_swap_b32 v109, v110\n"
    "s_nop 1\n"
    "v_mfma_f32_16x16x32_bf16 v[156:159], v[108:111], a[24:27], v[156:159]\n"
    "v_mfma_f32_16x16x32_bf16 v[160:163], v[108:111], v[32:35], v[160:163]\n"
    "v_max3_u32 v184, v112, v114, v184\n"
    "v_swap_b32 v113, v114\n"
    "s_nop 1\n"
    "v_mfma_f32_16x16x32_bf16 v[156:159], v[112:115], a[28:31], v[156:159]\n"
    "v_mfma_f32_16x16x32_bf16 v[160:163], v[112:115], v[36:39], v[160:163]\n"
    "v_cmp_eq_u32 vcc, -1, v184\n"
    "s_nop 0\n"
    "s_cbranch_vccnz .Lct_tr0_%=\n"
    "s_nop 7\n"
    "s_branch .Lct_tg0_%=\n"
    ".Lct_xr1_%=:\n"
    "s_add_u32 s20, s20, 1\n"
    "s_cmp_lt_u32 s20, 0x40000\n"
    "s_cbranch_scc1 .Lct_sp14_%=\n"
    "v_mov_b32 v184, 42\n"
    "s_mov_b32 s24, 33728\n"
    "buffer_store_dword v184, off, s[0:3], s24 sc1\n"
    "s_waitcnt vmcnt(0)\n"
    "s_branch .Lct_xg1_%=\n"
    ".Lct_sp14_%=:\n"
    "s_sleep 4\n"
    "buffer_load_dwordx2 v[190:191], off, s[0:3], s40 sc1\n"
    "s_waitcnt vmcnt(0)\n"
    "s_branch .Lct_x1_%=\n"
    ".Lct_retry1_%=:\n"
    "s_add_u32 s20, s20, 1\n"
    "s_cmp_lt_u32 s20, 0x40000\n"
    "s_cbranch_scc1 .Lct_sp15_%=\n"
    "v_mov_b32 v184, 3\n"
    "s_mov_b32 s24, 33728\n"
    "buffer_store_dword v184, off, s[0:3], s24 sc1\n"
    "s_waitcnt vmcnt(0)\n"
    "s_branch .Lct_go1_%=\n"
    ".Lct_sp15_%=:\n"
    "s_waitcnt vmcnt(0)\n"
    "v_mov_b64 v[76:77], 0\n"
    "v_mov_b64 v[78:79], 0\n"
    "v_mov_b64 v[80:81], 0\n"
    "v_mov_b64 v[82:83], 0\n"
    "buffer_load_dwordx4 v[40:43], v192, s[0:3], s31 offen offset:0 sc1\n"
    "buffer_load_dwordx4 v[44:47], v192, s[0:3], s31 offen offset:1024 sc1\n"
    "buffer_load_dwordx4 v[48:51], v197, s[0:3], s32 offen offset:0 sc1\n"
    "buffer_load_dwordx4 v[52:55], v197, s[0:3], s32 offen offset:256 sc1\n"
    "buffer_load_dwordx4 v[72:75], v193, s[0:3], s33 offen sc1\n"
    "buffer_load_dwordx4 v[56:59], v192, s[0:3], s33 offen offset:0 sc1\n"
    "buffer_load_dwordx4 v[60:63], v192, s[0:3], s33 offen offset:1024 sc1\n"
    "buffer_load_dwordx4 v[64:67], v192, s[0:3], s33 offen offset:2048 sc1\n"
    "buffer_load_dwordx4 v[68:71], v192, s[0:3], s33 offen offset:3072 sc1\n"
    "s_waitcnt vmcnt(0)\n"
    "s_branch .Lct_take1_%=\n"
    ".Lct_tr1_%=:\n"
    "s_add_u32 s20, s20, 1\n"
    "s_cmp_lt_u32 s20, 0x40000\n"
    "s_cbranch_scc1 .Lct_sp16_%=\n"
    "v_mov_b32 v184, 4\n"
    "s_mov_b32 s24, 33728\n"
    "buffer_store_dword v184, off, s[0:3], s24 sc1\n"
    "s_waitcnt vmcnt(0)\n"
    "s_branch .Lct_tg1_%=\n"
    ".Lct_sp16_%=:\n"
    "buffer_load_dwordx4 v[132:135], v199, s[0:3], s34 offen offset:0 sc1\n"
    "buffer_load_dwordx4 v[136:139], v199, s[0:3], s36 offen offset:0 sc1\n"
    "buffer_load_dwordx4 v[140:143], v199, s[0:3], s34 offen offset:256 sc1\n"
    "buffer_load_dwordx4 v[144:147], v199, s[0:3], s36 offen offset:256 sc1\n"
    "buffer_load_dwordx4 v[116:119], v198, s[0:3], s34 offen sc1\n"
    "buffer_load_dwordx4 v[84:87], v192, s[0:3], s34 offen offset:0 sc1\n"
    "buffer_load_dwordx4 v[88:91], v192, s[0:3], s34 offen offset:2048 sc1\n"
    "buffer_load_dwordx4 v[120:123], v198, s[0:3], s35 offen sc1\n"
    "buffer_load_dwordx4 v[92:95], v192, s[0:3], s35 offen offset:0 sc1\n"
    "buffer_load_dwordx4 v[96:99], v192, s[0:3], s35 offen offset:2048 sc1\n"
    "buffer_load_dwordx4 v[124:127], v198, s[0:3], s36 offen sc1\n"
    "buffer_load_dwordx4 v[100:103], v192, s[0:3], s36 offen offset:0 sc1\n"
    "buffer_load_dwordx4 v[104:107], v192, s[0:3], s36 offen offset:2048 sc1\n"
    "buffer_load_dwordx4 v[128:131], v198, s[0:3], s37 offen sc1\n"
    "buffer_load_dwordx4 v[108:111], v192, s[0:3], s37 offen offset:0 sc1\n"
    "buffer_load_dwordx4 v[112:115], v192, s[0:3], s37 offen offset:2048 sc1\n"
    "s_waitcnt vmcnt(0)\n"
    "v_max3_u32 v184, v132, v134, v132\n"
    "v_swap_b32 v133, v134\n"
    "s_nop 1\n"
    "v_mfma_f32_16x16x32_bf16 v[148:151], v[132:135], v[40:43], 0\n"
    "v_mfma_f32_16x16x32_bf16 v[152:155], v[132:135], v[48:51], 0\n"
    "v_max3_u32 v184, v136, v138, v184\n"
    "v_swap_b32 v137, v138\n"
    "s_nop 1\n"
    "v_mfma_f32_16x16x32_bf16 v[148:151], v[136:139], v[44:47], v[148:151]\n"
    "v_mfma_f32_16x16x32_bf16 v[152:155], v[136:139], v[52:55], v[152:155]\n"
    "v_max3_u32 v184, v140, v142, v184\n"
    "v_swap_b32 v141, v142\n"
    "s_nop 1\n"
    "v_mfma_f32_16x16x32_bf16 v[156:159], v[140:143], v[40:43], 0\n"
    "v_mfma_f32_16x16x32_bf16 v[160:163], v[140:143], v[48:51], 0\n"
    "v_max3_u32 v184, v144, v146, v184\n"
    "v_swap_b32 v145, v146\n"
    "s_nop 1\n"
    "v_mfma_f32_16x16x32_bf16 v[156:159], v[144:147], v[44:47], v[156:159]\n"
    "v_mfma_f32_16x16x32_bf16 v[160:163], v[144:147], v[52:55], v[160:163]\n"
    "v_max3_u32 v184, v116, v118, v184\n"
    "v_smfmac_f32_16x16x64_bf16 v[148:151], v[116:119], a[32:39], v195\n"
    "v_smfmac_f32_16x16x64_bf16 v[152:155], v[116:119], v[8:15], v195\n"
    "v_max3_u32 v184, v84, v86, v184\n"
    "v_swap_b32 v85, v86\n"
    "s_nop 1\n"
    "v_mfma_f32_16x16x32_bf16 v[156:159], v[84:87], a[32:35], v[156:159]\n"
    "v_mfma_f32_16x16x32_bf16 v[160:163], v[84:87], v[8:11], v[160:163]\n"
    "v_max3_u32 v184, v88, v90, v184\n"
    "v_swap_b32 v89, v90\n"
    "s_nop 1\n"
    "v_mfma_f32_16x16x32_bf16 v[156:159], v[88:91], a[36:39], v[156:159]\n"
    "v_mfma_f32_16x16x32_bf16 v[160:163], v[88:91], v[12:15], v[160:163]\n"
    "v_max3_u32 v184, v120, v122, v184\n"
    "v_smfmac_f32_16x16x64_bf16 v[148:151], v[120:123], a[40:47], v195\n"
    "v_smfmac_f32_16x16x64_bf16 v[152:155], v[120:123], v[16:23], v195\n"
    "v_max3_u32 v184, v92, v94, v184\n"
    "v_swap_b32 v93, v94\n"
    "s_nop 1\n"
    "v_mfma_f32_16x16x32_bf16 v[156:159], v[92:95], a[40:43], v[156:159]\n"
    "v_mfma_f32_16x16x32_bf16 v[160:163], v[92:95], v[16:19], v[160:163]\n"
    "v_max3_u32 v184, v96, v98, v184\n"
    "v_swap_b32 v97, v98\n"
    "s_nop 1\n"
    "v_mfma_f32_16x16x32_bf16 v[156:159], v[96:99], a[44:47], v[156:159]\n"
    "v_mfma_f32_16x16x32_bf16 v[160:163], v[96:99], v[20:23], v[160:163]\n"
    "v_max3_u32 v184, v124, v126, v184\n"
    "v_smfmac_f32_16x16x64_bf16 v[148:151], v[124:127], a[48:55], v195\n"
    "v_smfmac_f32_16x16x64_bf16 v[152:155], v[124:127], v[24:31], v195\n"
    "v_max3_u32 v184, v100, v102, v184\n"
    "v_swap_b32 v101, v102\n"
    "s_nop 1\n"
    "v_mfma_f32_16x16x32_bf16 v[156:159], v[100:103], a[48:51], v[156:159]\n"
    "v_mfma_f32_16x16x32_bf16 v[160:163], v[100:103], v[24:27], v[160:163]\n"
    "v_max3_u32 v184, v104, v106, v184\n"
    "v_swap_b32 v105, v106\n"
    "s_nop 1\n"
    "v_mfma_f32_16x16x32_bf16 v[156:159], v[104:107], a[52:55], v[156:159]\n"
    "v_mfma_f32_16x16x32_bf16 v[160:163], v[104:107], v[28:31], v[160:163]\n"
    "v_max3_u32 v184, v128, v130, v184\n"
    "v_smfmac_f32_16x16x64_bf16 v[148:151], v[128:131], a[56:63], v195\n"
    "v_smfmac_f32_16x16x64_bf16 v[152:155], v[128:131], v[32:39], v195\n"
    "v_max3_u32 v184, v108, v110, v184\n"
    "v_swap_b32 v109, v110\n"
    "s_nop 1\n"
    "v_mfma_f32_16x16x32_bf16 v[156:159], v[108:111], a[56:59], v[156:159]\n"
    "v_mfma_f32_16x16x32_bf16 v[160:163], v[108:111], v[32:35], v[160:163]\n"
    "v_max3_u32 v184, v112, v114, v184\n"
    "v_swap_b32 v113, v114\n"
    "s_nop 1\n"
    "v_mfma_f32_16x16x32_bf16 v[156:159], v[112:115], a[60:63], v[156:159]\n"
    "v_mfma_f32_16x16x32_bf16 v[160:163], v[112:115], v[36:39], v[160:163]\n"
    "v_cmp_eq_u32 vcc, -1, v184\n"
    "s_nop 0\n"
    "s_cbranch_vccnz .Lct_tr1_%=\n"
    "s_nop 7\n"
    "s_branch .Lct_tg1_%=\n"
    :: "{s[0:3]}"(s0_), "{s[4:7]}"(s4_), "{s[8:11]}"(s8_), "{s[12:15]}"(s12_), "{s[16:19]}"(s16_), "{v[192:195]}"(v192_), "{v[196:199]}"(v196_), "{v[200:203]}"(v200_), "{v[204:207]}"(v204_), "{v[208:211]}"(v208_), "{v[212:215]}"(v212_) : "memory", "v255", "a255", "s95");
    __builtin_unreachable();
}
DEV void qw_role(const MegaArgs& A, const int L, const int h, const int rep) {
    ROLE_HEAD;
    const u32x4 s0_ = {RFL(rx[0]), RFL(rx[1]), RFL(rx[2]), RFL(rx[3])};
    const u32x4 s4_ = {RFL(lo(A.Wq + h * 192 + 64)), RFL(hi(A.Wq + h * 192 + 64)), RFL(WBYTES - h * 384 - 128), RFL(0x20000u)};
    const u32x4 s8_ = {RFL(lo(A.Wv + h * 192 + 128)), RFL(hi(A.Wv + h * 192 + 128)), RFL(WBYTES - h * 384 - 256), RFL(0x20000u)};
    const u32x4 s12_ = {RFL(w), RFL(REC0 + w * 2048), RFL(QT0 + h * 32768), RFL((A.layers & 2) ? 64 : 0)};
    const u32x4 s16_ = {RFL(xl(Z_CT + (h & 7) * 256, L)), RFL(xl(Z_XN + rep * 2048, 0)), RFL(DBG0 + ((L * 128 + 112 + (h >> 3) * 4 + (h & 3)) * 4 + w) * 2048), RFL(0u)};
    const u32x4 v0_ = {(unsigned)((8 * w + (l >> 3)) * 12288 + ((l & 7) ^ (((8 * w + (l >> 3)) >> 1) & 6)) * 16), (unsigned)((8 * w + (l >> 3)) * 12288 + ((l & 7) ^ (((8 * w + (l >> 3)) >> 1) & 6)) * 16 + 128), (unsigned)((8 * lq + ((l >> 2) & 1) + ((l >> 1) & 4)) * 128 + (w ^ (((8 * lq + ((l >> 2) & 1) + ((l >> 1) & 4)) >> 2) & 3)) * 32 + (l & 3) * 8), (unsigned)(0u)};
    const u32x4 v64_ = {(unsigned)(l * 16), (unsigned)(l * 16 + 40960), (unsigned)(l * 16 + w * 2048), (unsigned)(l * 16 + w * 1024 + 16384)};
    const u32x4 v68_ = {(unsigned)(l < 20 ? (l >> 2) * 128 + (l & 3) * 4 : 0), (unsigned)(l == 0 ? xl(Z_QW + h * 128 + w * 4, 0) : OOB), (unsigned)(0u), (unsigned)(0u)};
    asm volatile(
    "s_mov_b32 s20, 0\n"
    "s_lshl_b32 s32, s12, 10\n"
    "s_mov_b32 s38, 0\n"
    "s_add_u32 m0, s32, 0\n"
    "s_nop 0\n"
    "buffer_load_dwordx4 v0, s[4:7], s38 offen nt lds\n"
    "s_add_u32 m0, s32, 4096\n"
    "s_nop 0\n"
    "buffer_load_dwordx4 v1, s[4:7], s38 offen nt lds\n"
    "s_add_u32 m0, s32, 8192\n"
    "s_nop 0\n"
    "buffer_load_dwordx4 v0, s[8:11], s38 offen nt lds\n"
    "s_mov_b32 s38, 393216\n"
    "s_add_u32 m0, s32, 12288\n"
    "s_nop 0\n"
    "buffer_load_dwordx4 v0, s[4:7], s38 offen nt lds\n"
    "s_add_u32 m0, s32, 16384\n"
    "s_nop 0\n"
    "buffer_load_dwordx4 v1, s[4:7], s38 offen nt lds\n"
    "s_add_u32 m0, s32, 20480\n"
    "s_nop 0\n"
    "buffer_load_dwordx4 v0, s[8:11], s38 offen nt lds\n"
    "s_mov_b32 s38, 786432\n"
    "s_add_u32 m0, s32, 24576\n"
    "s_nop 0\n"
    "buffer_load_dwordx4 v0, s[4:7], s38 offen nt lds\n"
    "s_add_u32 m0, s32, 28672\n"
    "s_nop 0\n"
    "buffer_load_dwordx4 v1, s[4:7], s38 offen nt lds\n"
    "s_add_u32 m0, s32, 32768\n"
    "s_nop 0\n"
    "buffer_load_dwordx4 v0, s[8:11], s38 offen nt lds\n"
    "s_mov_b32 s38, 1179648\n"
    "s_add_u32 m0, s32, 36864\n"
    "s_nop 0\n"
    "buffer_load_dwordx4 v0, s[4:7], s38 offen nt lds\n"
    "s_add_u32 m0, s32, 40960\n"
    "s_nop 0\n"
    "buffer_load_dwordx4 v1, s[4:7], s38 offen nt lds\n"
    "s_add_u32 m0, s32, 45056\n"
    "s_nop 0\n"
    "buffer_load_dwordx4 v0, s[8:11], s38 offen nt lds\n"
    "s_mov_b32 s38, 1572864\n"
    "s_add_u32 m0, s32, 49152\n"
    "s_nop 0\n"
    "buffer_load_dwordx4 v0, s[4:7], s38 offen nt lds\n"
    "s_add_u32 m0, s32, 53248\n"
    "s_nop 0\n"
    "buffer_load_dwordx4 v1, s[4:7], s38 offen nt lds\n"
    "s_add_u32 m0, s32, 57344\n"
    "s_nop 0\n"
    "buffer_load_dwordx4 v0, s[8:11], s38 offen nt lds\n"
    "s_mov_b32 s38, 1966080\n"
    "s_add_u32 m0, s32, 61440\n"
    "s_nop 0\n"
    "buffer_load_dwordx4 v0, s[4:7], s38 offen nt lds\n"
    "s_add_u32 m0, s32, 65536\n"
    "s_nop 0\n"
    "buffer_load_dwordx4 v1, s[4:7], s38 offen nt lds\n"
    "s_add_u32 m0, s32, 69632\n"
    "s_nop 0\n"
    "buffer_load_dwordx4 v0, s[8:11], s38 offen nt lds\n"
    "s_mov_b32 s38, 2359296\n"
    "s_add_u32 m0, s32, 73728\n"
    "s_nop 0\n"
    "buffer_load_dwordx4 v0, s[4:7], s38 offen nt lds\n"
    "s_add_u32 m0, s32, 77824\n"
    "s_nop 0\n"
    "buffer_load_dwordx4 v1, s[4:7], s38 offen nt lds\n"
    "s_add_u32 m0, s32, 81920\n"
    "s_nop 0\n"
    "buffer_load_dwordx4 v0, s[8:11], s38 offen nt lds\n"
    "s_mov_b32 s38, 2752512\n"
    "s_add_u32 m0, s32, 86016\n"
    "s_nop 0\n"
    "buffer_load_dwordx4 v0, s[4:7], s38 offen nt lds\n"
    "s_add_u32 m0, s32, 90112\n"
    "s_nop 0\n"
    "buffer_load_dwordx4 v1, s[4:7], s38 offen nt lds\n"
    "s_add_u32 m0, s32, 94208\n"
    "s_nop 0\n"
    "buffer_load_dwordx4 v0, s[8:11], s38 offen nt lds\n"
    "s_mov_b32 s38, 3145728\n"
    "s_add_u32 m0, s32, 98304\n"
    "s_nop 0\n"
    "buffer_load_dwordx4 v0, s[4:7], s38 offen nt lds\n"
    "s_add_u32 m0, s32, 102400\n"
    "s_nop 0\n"
    "buffer_load_dwordx4 v1, s[4:7], s38 offen nt lds\n"
    "s_add_u32 m0, s32, 106496\n"
    "s_nop 0\n"
    "buffer_load_dwordx4 v0, s[8:11], s38 offen nt lds\n"
    "s_mov_b32 s38, 3538944\n"
    "s_add_u32 m0, s32, 110592\n"
    "s_nop 0\n"
    "buffer_load_dwordx4 v0, s[4:7], s38 offen nt lds\n"
    "s_add_u32 m0, s32, 114688\n"
    "s_nop 0\n"
    "buffer_load_dwordx4 v1, s[4:7], s38 offen nt lds\n"
    "s_add_u32 m0, s32, 118784\n"
    "s_nop 0\n"
    "buffer_load_dwordx4 v0, s[8:11], s38 offen nt lds\n"
    "s_mov_b32 s38, 3932160\n"
    "s_add_u32 m0, s32, 122880\n"
    "s_nop 0\n"
    "buffer_load_dwordx4 v0, s[4:7], s38 offen nt lds\n"
    "s_add_u32 m0, s32, 126976\n"
    "s_nop 0\n"
    "buffer_load_dwordx4 v1, s[4:7], s38 offen nt lds\n"
    "s_add_u32 m0, s32, 131072\n"
    "s_nop 0\n"
    "buffer_load_dwordx4 v0, s[8:11], s38 offen nt lds\n"
    "s_mov_b32 s38, 4325376\n"
    "s_add_u32 m0, s32, 135168\n"
    "s_nop 0\n"
    "buffer_load_dwordx4 v0, s[4:7], s38 offen nt lds\n"
    "s_add_u32 m0, s32, 139264\n"
    "s_nop 0\n"
    "buffer_load_dwordx4 v1, s[4:7], s38 offen nt lds\n"
    "s_add_u32 m0, s32, 143360\n"
    "s_nop 0\n"
    "buffer_load_dwordx4 v0, s[8:11], s38 offen nt lds\n"
    "s_waitcnt vmcnt(24)\n"
    "s_barrier\n"
    "v_add_u32 v8, 0, v2\n"
    "ds_read_b64_tr_b16 a[0:1], v8 offset:0\n"
    "ds_read_b64_tr_b16 a[2:3], v8 offset:256\n"
    "ds_read_b64_tr_b16 a[80:81], v8 offset:4096\n"
    "ds_read_b64_tr_b16 a[82:83], v8 offset:4352\n"
    "ds_read_b64_tr_b16 a[160:161], v8 offset:8192\n"
    "ds_read_b64_tr_b16 a[162:163], v8 offset:8448\n"
    "ds_read_b64_tr_b16 a[4:5], v8 offset:12288\n"
    "ds_read_b64_tr_b16 a[6:7], v8 offset:12544\n"
    "ds_read_b64_tr_b16 a[84:85], v8 offset:16384\n"
    "ds_read_b64_tr_b16 a[86:87], v8 offset:16640\n"
    "ds_read_b64_tr_b16 a[164:165], v8 offset:20480\n"
    "ds_read_b64_tr_b16 a[166:167], v8 offset:20736\n"
    "ds_read_b64_tr_b16 a[8:9], v8 offset:24576\n"
    "ds_read_b64_tr_b16 a[10:11], v8 offset:24832\n"
    "ds_read_b64_tr_b16 a[88:89], v8 offset:28672\n"
    "ds_read_b64_tr_b16 a[90:91], v8 offset:28928\n"
    "ds_read_b64_tr_b16 a[168:169], v8 offset:32768\n"
    "ds_read_b64_tr_b16 a[170:171], v8 offset:33024\n"
    "ds_read_b64_tr_b16 a[12:13], v8 offset:36864\n"
    "ds_read_b64_tr_b16 a[14:15], v8 offset:37120\n"
    "ds_read_b64_tr_b16 a[92:93], v8 offset:40960\n"
    "ds_read_b64_tr_b16 a[94:95], v8 offset:41216\n"
    "ds_read_b64_tr_b16 a[172:173], v8 offset:45056\n"
    "ds_read_b64_tr_b16 a[174:175], v8 offset:45312\n"
    "s_waitcnt lgkmcnt(0)\n"
    "s_barrier\n"
    "s_mov_b32 s38, 4718592\n"
    "s_add_u32 m0, s32, 0\n"
    "s_nop 0\n"
    "buffer_load_dwordx4 v0, s[4:7], s38 offen nt lds\n"
    "s_add_u32 m0, s32, 4096\n"
    "s_nop 0\n"
    "buffer_load_dwordx4 v1, s[4:7], s38 offen nt lds\n"
    "s_add_u32 m0, s32, 8192\n"
    "s_nop 0\n"
    "buffer_load_dwordx4 v0, s[8:11], s38 offen nt lds\n"
    "s_mov_b32 s38, 5111808\n"
    "s_add_u32 m0, s32, 12288\n"
    "s_nop 0\n"
    "buffer_load_dwordx4 v0, s[4:7], s38 offen nt lds\n"
    "s_add_u32 m0, s32, 16384\n"
    "s_nop 0\n"
    "buffer_load_dwordx4 v1, s[4:7], s38 offen nt lds\n"
    "s_add_u32 m0, s32, 20480\n"
    "s_nop 0\n"
    "buffer_load_dwordx4 v0, s[8:11], s38 offen nt lds\n"
    "s_mov_b32 s38, 5505024\n"
    "s_add_u32 m0, s32, 24576\n"
    "s_nop 0\n"
    "buffer_load_dwordx4 v0, s[4:7], s38 offen nt lds\n"
    "s_add_u32 m0, s32, 28672\n"
    "s_nop 0\n"
    "buffer_load_dwordx4 v1, s[4:7], s38 offen nt lds\n"
    "s_add_u32 m0, s32, 32768\n"
    "s_nop 0\n"
    "buffer_load_dwordx4 v0, s[8:11], s38 offen nt lds\n"
    "s_mov_b32 s38, 5898240\n"
    "s_add_u32 m0, s32, 36864\n"
    "s_nop 0\n"
    "buffer_load_dwordx4 v0, s[4:7], s38 offen nt lds\n"
    "s_add_u32 m0, s32, 40960\n"
    "s_nop 0\n"
    "buffer_load_dwordx4 v1, s[4:7], s38 offen nt lds\n"
    "s_add_u32 m0, s32, 45056\n"
    "s_nop 0\n"
    "buffer_load_dwordx4 v0, s[8:11], s38 offen nt lds\n"
    "s_waitcnt vmcnt(24)\n"
    "s_barrier\n"
    "v_add_u32 v8, 49152, v2\n"
    "ds_read_b64_tr_b16 a[16:17], v8 offset:0\n"
    "ds_read_b64_tr_b16 a[18:19], v8 offset:256\n"
    "ds_read_b64_tr_b16 a[96:97], v8 offset:4096\n"
    "ds_read_b64_tr_b16 a[98:99], v8 offset:4352\n"
    "ds_read_b64_tr_b16 a[176:177], v8 offset:8192\n"
    "ds_read_b64_tr_b16 a[178:179], v8 offset:8448\n"
    "ds_read_b64_tr_b16 a[20:21], v8 offset:12288\n"
    "ds_read_b64_tr_b16 a[22:23], v8 offset:12544\n"
    "ds_read_b64_tr_b16 a[100:101], v8 offset:16384\n"
    "ds_read_b64_tr_b16 a[102:103], v8 offset:16640\n"
    "ds_read_b64_tr_b16 a[180:181], v8 offset:20480\n"
    "ds_read_b64_tr_b16 a[182:183], v8 offset:20736\n"
    "ds_read_b64_tr_b16 a[24:25], v8 offset:24576\n"
    "ds_read_b64_tr_b16 a[26:27], v8 offset:24832\n"
    "ds_read_b64_tr_b16 a[104:105], v8 offset:28672\n"
    "ds_read_b64_tr_b16 a[106:107], v8 offset:28928\n"
    "ds_read_b64_tr_b16 a[184:185], v8 offset:32768\n"
    "ds_read_b64_tr_b16 a[186:187], v8 offset:33024\n"
    "ds_read_b64_tr_b16 a[28:29], v8 offset:36864\n"
    "ds_read_b64_tr_b16 a[30:31], v8 offset:37120\n"
    "ds_read_b64_tr_b16 a[108:109], v8 offset:40960\n"
    "ds_read_b64_tr_b16 a[110:111], v8 offset:41216\n"
    "ds_read_b64_tr_b16 a[188:189], v8 offset:45056\n"
    "ds_read_b64_tr_b16 a[190:191], v8 offset:45312\n"
    "s_waitcnt lgkmcnt(0)\n"
    "s_barrier\n"
    "s_mov_b32 s38, 6291456\n"
    "s_add_u32 m0, s32, 49152\n"
    "s_nop 0\n"
    "buffer_load_dwordx4 v0, s[4:7], s38 offen nt lds\n"
    "s_add_u32 m0, s32, 53248\n"
    "s_nop 0\n"
    "buffer_load_dwordx4 v1, s[4:7], s38 offen nt lds\n"
    "s_add_u32 m0, s32, 57344\n"
    "s_nop 0\n"
    "buffer_load_dwordx4 v0, s[8:11], s38 offen nt lds\n"
    "s_mov_b32 s38, 6684672\n"
    "s_add_u32 m0, s32, 61440\n"
    "s_nop 0\n"
    "buffer_load_dwordx4 v0, s[4:7], s38 offen nt lds\n"
    "s_add_u32 m0, s32, 65536\n"
    "s_nop 0\n"
    "buffer_load_dwordx4 v1, s[4:7], s38 offen nt lds\n"
    "s_add_u32 m0, s32, 69632\n"
    "s_nop 0\n"
    "buffer_load_dwordx4 v0, s[8:11], s38 offen nt lds\n"
    "s_mov_b32 s38, 7077888\n"
    "s_add_u32 m0, s32, 73728\n"
    "s_nop 0\n"
    "buffer_load_dwordx4 v0, s[4:7], s38 offen nt lds\n"
    "s_add_u32 m0, s32, 77824\n"
    "s_nop 0\n"
    "buffer_load_dwordx4 v1, s[4:7], s38 offen nt lds\n"
    "s_add_u32 m0, s32, 81920\n"
    "s_nop 0\n"
    "buffer_load_dwordx4 v0, s[8:11], s38 offen nt lds\n"
    "s_mov_b32 s38, 7471104\n"
    "s_add_u32 m0, s32, 86016\n"
    "s_nop 0\n"
    "buffer_load_dwordx4 v0, s[4:7], s38 offen nt lds\n"
    "s_add_u32 m0, s32, 90112\n"
    "s_nop 0\n"
    "buffer_load_dwordx4 v1, s[4:7], s38 offen nt lds\n"
    "s_add_u32 m0, s32, 94208\n"
    "s_nop 0\n"
    "buffer_load_dwordx4 v0, s[8:11], s38 offen nt lds\n"
    "s_waitcnt vmcnt(24)\n"
    "s_barrier\n"
    "v_add_u32 v8, 98304, v2\n"
    "ds_read_b64_tr_b16 a[32:33], v8 offset:0\n"
    "ds_read_b64_tr_b16 a[34:35], v8 offset:256\n"
    "ds_read_b64_tr_b16 a[112:113], v8 offset:4096\n"
    "ds_read_b64_tr_b16 a[114:115], v8 offset:4352\n"
    "ds_read_b64_tr_b16 a[192:193], v8 offset:8192\n"
    "ds_read_b64_tr_b16 a[194:195], v8 offset:8448\n"
    "ds_read_b64_tr_b16 a[36:37], v8 offset:12288\n"
    "ds_read_b64_tr_b16 a[38:39], v8 offset:12544\n"
    "ds_read_b64_tr_b16 a[116:117], v8 offset:16384\n"
    "ds_read_b64_tr_b16 a[118:119], v8 offset:16640\n"
    "ds_read_b64_tr_b16 a[196:197], v8 offset:20480\n"
    "ds_read_b64_tr_b16 a[198:199], v8 offset:20736\n"
    "ds_read_b64_tr_b16 a[40:41], v8 offset:24576\n"
    "ds_read_b64_tr_b16 a[42:43], v8 offset:24832\n"
    "ds_read_b64_tr_b16 a[120:121], v8 offset:28672\n"
    "ds_read_b64_tr_b16 a[122:123], v8 offset:28928\n"
    "ds_read_b64_tr_b16 a[200:201], v8 offset:32768\n"
    "ds_read_b64_tr_b16 a[202:203], v8 offset:33024\n"
    "ds_read_b64_tr_b16 a[44:45], v8 offset:36864\n"
    "ds_read_b64_tr_b16 a[46:47], v8 offset:37120\n"
    "ds_read_b64_tr_b16 a[124:125], v8 offset:40960\n"
    "ds_read_b64_tr_b16 a[126:127], v8 offset:41216\n"
    "ds_read_b64_tr_b16 a[204:205], v8 offset:45056\n"
    "ds_read_b64_tr_b16 a[206:207], v8 offset:45312\n"
    "s_waitcnt lgkmcnt(0)\n"
    "s_barrier\n"
    "s_waitcnt vmcnt(12)\n"
    "s_barrier\n"
    "v_add_u32 v8, 0, v2\n"
    "ds_read_b64_tr_b16 a[48:49], v8 offset:0\n"
    "ds_read_b64_tr_b16 a[50:51], v8 offset:256\n"
    "ds_read_b64_tr_b16 a[128:129], v8 offset:4096\n"
    "ds_read_b64_tr_b16 a[130:131], v8 offset:4352\n"
    "ds_read_b64_tr_b16 a[208:209], v8 offset:8192\n"
    "ds_read_b64_tr_b16 a[210:211], v8 offset:8448\n"
    "ds_read_b64_tr_b16 a[52:53], v8 offset:12288\n"
    "ds_read_b64_tr_b16 a[54:55], v8 offset:12544\n"
    "ds_read_b64_tr_b16 a[132:133], v8 offset:16384\n"
    "ds_read_b64_tr_b16 a[134:135], v8 offset:16640\n"
    "ds_read_b64_tr_b16 a[212:213], v8 offset:20480\n"
    "ds_read_b64_tr_b16 a[214:215], v8 offset:20736\n"
    "ds_read_b64_tr_b16 a[56:57], v8 offset:24576\n"
    "ds_read_b64_tr_b16 a[58:59], v8 offset:24832\n"
    "ds_read_b64_tr_b16 a[136:137], v8 offset:28672\n"
    "ds_read_b64_tr_b16 a[138:139], v8 offset:28928\n"
    "ds_read_b64_tr_b16 a[216:217], v8 offset:32768\n"
    "ds_read_b64_tr_b16 a[218:219], v8 offset:33024\n"
    "ds_read_b64_tr_b16 a[60:61], v8 offset:36864\n"
    "ds_read_b64_tr_b16 a[62:63], v8 offset:37120\n"
    "ds_read_b64_tr_b16 a[140:141], v8 offset:40960\n"
    "ds_read_b64_tr_b16 a[142:143], v8 offset:41216\n"
    "ds_read_b64_tr_b16 a[220:221], v8 offset:45056\n"
    "ds_read_b64_tr_b16 a[222:223], v8 offset:45312\n"
    "s_waitcnt lgkmcnt(0)\n"
    "s_barrier\n"
    "s_waitcnt vmcnt(0)\n"
    "s_barrier\n"
    "v_add_u32 v8, 49152, v2\n"
    "ds_read_b64_tr_b16 a[64:65], v8 offset:0\n"
    "ds_read_b64_tr_b16 a[66:67], v8 offset:256\n"
    "ds_read_b64_tr_b16 a[144:145], v8 offset:4096\n"
    "ds_read_b64_tr_b16 a[146:147], v8 offset:4352\n"
    "ds_read_b64_tr_b16 a[224:225], v8 offset:8192\n"
    "ds_read_b64_tr_b16 a[226:227], v8 offset:8448\n"
    "ds_read_b64_tr_b16 a[68:69], v8 offset:12288\n"
    "ds_read_b64_tr_b16 a[70:71], v8 offset:12544\n"
    "ds_read_b64_tr_b16 a[148:149], v8 offset:16384\n"
    "ds_read_b64_tr_b16 a[150:151], v8 offset:16640\n"
    "ds_read_b64_tr_b16 a[228:229], v8 offset:20480\n"
    "ds_read_b64_tr_b16 a[230:231], v8 offset:20736\n"
    "ds_read_b64_tr_b16 a[72:73], v8 offset:24576\n"
    "ds_read_b64_tr_b16 a[74:75], v8 offset:24832\n"
    "ds_read_b64_tr_b16 a[152:153], v8 offset:28672\n"
    "ds_read_b64_tr_b16 a[154:155], v8 offset:28928\n"
    "ds_read_b64_tr_b16 a[232:233], v8 offset:32768\n"
    "ds_read_b64_tr_b16 a[234:235], v8 offset:33024\n"
    "ds_read_b64_tr_b16 a[76:77], v8 offset:36864\n"
    "ds_read_b64_tr_b16 a[78:79], v8 offset:37120\n"
    "ds_read_b64_tr_b16 a[156:157], v8 offset:40960\n"
    "ds_read_b64_tr_b16 a[158:159], v8 offset:41216\n"
    "ds_read_b64_tr_b16 a[236:237], v8 offset:45056\n"
    "ds_read_b64_tr_b16 a[238:239], v8 offset:45312\n"
    "s_waitcnt lgkmcnt(0)\n"
    "s_barrier\n"
    "s_mov_b32 s26, -2\n"
    "v_lshrrev_b32 v4, 2, v64\n"
    ".Lqw_po1_%=:\n"
    "buffer_load_dword v8, v4, s[0:3], s16 offen sc1\n"
    "s_waitcnt vmcnt(0)\n"
    "v_cmp_gt_i32 vcc, 1, v8\n"
    "s_nop 0\n"
    "s_cbranch_vccz .Lqw_pd2_%=\n"
    "s_add_u32 s20, s20, 1\n"
    "s_cmp_lt_u32 s20, 0x40000\n"
    "s_cbranch_scc1 .Lqw_sp3_%=\n"
    "v_mov_b32 v8, 50\n"
    "s_mov_b32 s24, 33728\n"
    "buffer_store_dword v8, off, s[0:3], s24 sc1\n"
    "s_waitcnt vmcnt(0)\n"
    "s_branch .Lqw_pd2_%=\n"
    ".Lqw_sp3_%=:\n"
    "s_sleep 4\n"
    "s_branch .Lqw_po1_%=\n"
    ".Lqw_pd2_%=:\n"
    "s_mov_b32 s26, -1\n"
    "s_mov_b32 s35, 0\n"
    "s_lshl_b32 s37, s12, 11\n"
    "s_mov_b32 s45, 0\n"
    "v_mov_b32 v60, 0\n"
    "s_mov_b32 s43, 2130706432\n"
    "s_mov_b32 s41, 2130706432\n"
    "s_mov_b32 s46, 2130706432\n"
    "s_mov_b32 s34, 2130706432\n"
    "s_add_u32 s26, s26, 1\n"
    "s_sub_u32 s27, s26, 10\n"
    "s_ashr_i32 s28, s27, 1\n"
    "s_and_b32 s29, s27, 1\n"
    "s_add_u32 s30, s28, 10\n"
    "s_cmp_lt_i32 s27, 0\n"
    "s_cselect_b32 s29, 0, s29\n"
    "s_cselect_b32 s30, s26, s30\n"
    "s_cmp_eq_u32 s29, 1\n"
    "s_cselect_b32 s30, s28, s30\n"
    "s_cselect_b32 s27, s15, 64\n"
    "s_lshl_b32 s31, s29, 13\n"
    "s_add_u32 s40, s30, 1\n"
    "s_lshl_b32 s32, s30, 19\n"
    "s_lshl_b32 s25, s30, 11\n"
    "s_xor_b32 s25, s25, s31\n"
    "s_and_b32 s25, s25, 0x2000\n"
    "s_add_u32 s32, s32, s25\n"
    "s_add_u32 s32, s32, s13\n"
    "s_lshl_b32 s33, s30, 20\n"
    "s_lshl_b32 s25, s30, 12\n"
    "s_xor_b32 s25, s25, s31\n"
    "s_and_b32 s25, s25, 0x2000\n"
    "s_add_u32 s33, s33, s25\n"
    "s_add_u32 s33, s33, s14\n"
    "s_mul_i32 s47, s40, s29\n"
    "s_cmp_lt_u32 s30, s27\n"
    "s_cselect_b32 s36, 1, 0\n"
    "s_cselect_b32 s32, s32, 2130706432\n"
    "s_cmp_lt_u32 s30, s27\n"
    "s_cselect_b32 s39, s31, 2130706432\n"
    "s_cselect_b32 s47, s47, 0\n"
    "s_cmp_gt_u32 s47, 0\n"
    "s_cselect_b32 s38, s17, 2130706432\n"
    ".Lqw_top_%=:\n"
    "ds_read_b128 v[24:27], v65 offset:0\n"
    "ds_read_b128 v[28:31], v65 offset:1024\n"
    "ds_read_b128 v[32:35], v65 offset:2048\n"
    "ds_read_b128 v[36:39], v65 offset:3072\n"
    "ds_read_b128 v[40:43], v65 offset:4096\n"
    "ds_read_b128 v[44:47], v65 offset:5120\n"
    "v_cmp_gt_i32 vcc, s47, v60\n"
    "s_nop 0\n"
    "s_cbranch_vccnz .Lqw_wx_%=\n"
    ".Lqw_wxb_%=:\n"
    "s_cmp_eq_u32 s35, 0\n"
    "s_cbranch_scc1 .Lqw_skip_%=\n"
    "s_waitcnt lgkmcnt(4)\n"
    "v_mfma_f32_16x16x32_bf16 v[0:3], a[0:3], v[24:27], 0\n"
    "buffer_store_dwordx4 v[48:51], v66, s[0:3], s46 offen offset:0 sc1\n"
    "v_mfma_f32_16x16x32_bf16 v[4:7], a[0:3], v[28:31], 0\n"
    "v_mfma_f32_16x16x32_bf16 v[8:11], a[80:83], v[24:27], 0\n"
    "v_mfma_f32_16x16x32_bf16 v[12:15], a[80:83], v[28:31], 0\n"
    "v_mfma_f32_16x16x32_bf16 v[16:19], a[160:163], v[24:27], 0\n"
    "ds_read_b128 v[24:27], v65 offset:6144\n"
    "v_mfma_f32_16x16x32_bf16 v[20:23], a[160:163], v[28:31], 0\n"
    "ds_read_b128 v[28:31], v65 offset:7168\n"
    "s_waitcnt lgkmcnt(4)\n"
    "v_mfma_f32_16x16x32_bf16 v[0:3], a[4:7], v[32:35], v[0:3]\n"
    "v_mfma_f32_16x16x32_bf16 v[4:7], a[4:7], v[36:39], v[4:7]\n"
    "v_mfma_f32_16x16x32_bf16 v[8:11], a[84:87], v[32:35], v[8:11]\n"
    "buffer_store_dwordx4 v[52:55], v66, s[0:3], s46 offen offset:1024 sc1\n"
    "v_mfma_f32_16x16x32_bf16 v[12:15], a[84:87], v[36:39], v[12:15]\n"
    "v_mfma_f32_16x16x32_bf16 v[16:19], a[164:167], v[32:35], v[16:19]\n"
    "ds_read_b128 v[32:35], v65 offset:8192\n"
    "v_mfma_f32_16x16x32_bf16 v[20:23], a[164:167], v[36:39], v[20:23]\n"
    "ds_read_b128 v[36:39], v65 offset:9216\n"
    "s_waitcnt lgkmcnt(4)\n"
    "v_mfma_f32_16x16x32_bf16 v[0:3], a[8:11], v[40:43], v[0:3]\n"
    "v_mfma_f32_16x16x32_bf16 v[4:7], a[8:11], v[44:47], v[4:7]\n"
    "v_mfma_f32_16x16x32_bf16 v[8:11], a[88:91], v[40:43], v[8:11]\n"
    "v_mfma_f32_16x16x32_bf16 v[12:15], a[88:91], v[44:47], v[12:15]\n"
    "v_mfma_f32_16x16x32_bf16 v[16:19], a[168:171], v[40:43], v[16:19]\n"
    "ds_read_b128 v[40:43], v65 offset:10240\n"
    "buffer_store_dwordx4 v[56:59], v67, s[0:3], s46 offen sc1\n"
    "v_mfma_f32_16x16x32_bf16 v[20:23], a[168:171], v[44:47], v[20:23]\n"
    "ds_read_b128 v[44:47], v65 offset:11264\n"
    "s_waitcnt lgkmcnt(4)\n"
    "v_mfma_f32_16x16x32_bf16 v[0:3], a[12:15], v[24:27], v[0:3]\n"
    "s_add_u32 m0, s37, 0\n"
    "v_mfma_f32_16x16x32_bf16 v[4:7], a[12:15], v[28:31], v[4:7]\n"
    "v_mfma_f32_16x16x32_bf16 v[8:11], a[92:95], v[24:27], v[8:11]\n"
    "v_mfma_f32_16x16x32_bf16 v[12:15], a[92:95], v[28:31], v[12:15]\n"
    "v_mfma_f32_16x16x32_bf16 v[16:19], a[172:175], v[24:27], v[16:19]\n"
    "ds_read_b128 v[24:27], v65 offset:12288\n"
    "v_mfma_f32_16x16x32_bf16 v[20:23], a[172:175], v[28:31], v[20:23]\n"
    "ds_read_b128 v[28:31], v65 offset:13312\n"
    "s_waitcnt lgkmcnt(4)\n"
    "v_mfma_f32_16x16x32_bf16 v[0:3], a[16:19], v[32:35], v[0:3]\n"
    "buffer_load_dwordx4 v64, s[0:3], s32 offen sc1 lds\n"
    "v_mfma_f32_16x16x32_bf16 v[4:7], a[16:19], v[36:39], v[4:7]\n"
    "v_mfma_f32_16x16x32_bf16 v[8:11], a[96:99], v[32:35], v[8:11]\n"
    "v_mfma_f32_16x16x32_bf16 v[12:15], a[96:99], v[36:39], v[12:15]\n"
    "v_mfma_f32_16x16x32_bf16 v[16:19], a[176:179], v[32:35], v[16:19]\n"
    "ds_read_b128 v[32:35], v65 offset:14336\n"
    "buffer_load_dwordx4 v64, s[0:3], s32 offen offset:1024 sc1 lds\n"
    "v_mfma_f32_16x16x32_bf16 v[20:23], a[176:179], v[36:39], v[20:23]\n"
    "ds_read_b128 v[36:39], v65 offset:15360\n"
    "s_waitcnt lgkmcnt(4)\n"
    "v_mfma_f32_16x16x32_bf16 v[0:3], a[20:23], v[40:43], v[0:3]\n"
    "s_add_u32 m0, s37, 8192\n"
    "s_add_u32 s32, s32, 0x4000\n"
    "v_mfma_f32_16x16x32_bf16 v[4:7], a[20:23], v[44:47], v[4:7]\n"
    "v_mfma_f32_16x16x32_bf16 v[8:11], a[100:103], v[40:43], v[8:11]\n"
    "buffer_load_dwordx4 v64, s[0:3], s32 offen sc1 lds\n"
    "v_mfma_f32_16x16x32_bf16 v[12:15], a[100:103], v[44:47], v[12:15]\n"
    "v_mfma_f32_16x16x32_bf16 v[16:19], a[180:183], v[40:43], v[16:19]\n"
    "ds_read_b128 v[40:43], v65 offset:16384\n"
    "v_mfma_f32_16x16x32_bf16 v[20:23], a[180:183], v[44:47], v[20:23]\n"
    "ds_read_b128 v[44:47], v65 offset:17408\n"
    "s_waitcnt lgkmcnt(4)\n"
    "v_mfma_f32_16x16x32_bf16 v[0:3], a[24:27], v[24:27], v[0:3]\n"
    "buffer_load_dwordx4 v64, s[0:3], s32 offen offset:1024 sc1 lds\n"
    "s_add_u32 m0, s37, 16384\n"
    "v_mfma_f32_16x16x32_bf16 v[4:7], a[24:27], v[28:31], v[4:7]\n"
    "s_add_u32 s32, s32, 0x4000\n"
    "v_mfma_f32_16x16x32_bf16 v[8:11], a[104:107], v[24:27], v[8:11]\n"
    "v_mfma_f32_16x16x32_bf16 v[12:15], a[104:107], v[28:31], v[12:15]\n"
    "v_mfma_f32_16x16x32_bf16 v[16:19], a[184:187], v[24:27], v[16:19]\n"
    "ds_read_b128 v[24:27], v65 offset:18432\n"
    "buffer_load_dwordx4 v64, s[0:3], s32 offen sc1 lds\n"
    "v_mfma_f32_16x16x32_bf16 v[20:23], a[184:187], v[28:31], v[20:23]\n"
    "ds_read_b128 v[28:31], v65 offset:19456\n"
    "s_waitcnt lgkmcnt(4)\n"
    "v_mfma_f32_16x16x32_bf16 v[0:3], a[28:31], v[32:35], v[0:3]\n"
    "v_mfma_f32_16x16x32_bf16 v[4:7], a[28:31], v[36:39], v[4:7]\n"
    "v_mfma_f32_16x16x32_bf16 v[8:11], a[108:111], v[32:35], v[8:11]\n"
    "buffer_load_dwordx4 v64, s[0:3], s32 offen offset:1024 sc1 lds\n"
    "s_add_u32 m0, s37, 24576\n"
    "v_mfma_f32_16x16x32_bf16 v[12:15], a[108:111], v[36:39], v[12:15]\n"
    "s_add_u32 s32, s32, 0x4000\n"
    "v_mfma_f32_16x16x32_bf16 v[16:19], a[188:191], v[32:35], v[16:19]\n"
    "ds_read_b128 v[32:35], v65 offset:20480\n"
    "v_mfma_f32_16x16x32_bf16 v[20:23], a[188:191], v[36:39], v[20:23]\n"
    "ds_read_b128 v[36:39], v65 offset:21504\n"
    "s_waitcnt lgkmcnt(4)\n"
    "v_mfma_f32_16x16x32_bf16 v[0:3], a[32:35], v[40:43], v[0:3]\n"
    "buffer_load_dwordx4 v64, s[0:3], s32 offen sc1 lds\n"
    "v_mfma_f32_16x16x32_bf16 v[4:7], a[32:35], v[44:47], v[4:7]\n"
    "v_mfma_f32_16x16x32_bf16 v[8:11], a[112:115], v[40:43], v[8:11]\n"
    "v_mfma_f32_16x16x32_bf16 v[12:15], a[112:115], v[44:47], v[12:15]\n"
    "v_mfma_f32_16x16x32_bf16 v[16:19], a[192:195], v[40:43], v[16:19]\n"
    "ds_read_b128 v[40:43], v65 offset:22528\n"
    "buffer_load_dwordx4 v64, s[0:3], s32 offen offset:1024 sc1 lds\n"
    "v_mfma_f32_16x16x32_bf16 v[20:23], a[192:195], v[44:47], v[20:23]\n"
    "ds_read_b128 v[44:47], v65 offset:23552\n"
    "s_waitcnt lgkmcnt(4)\n"
    "v_mfma_f32_16x16x32_bf16 v[0:3], a[36:39], v[24:27], v[0:3]\n"
    "s_add_u32 m0, s37, 32768\n"
    "s_add_u32 s32, s32, 0x4000\n"
    "v_mfma_f32_16x16x32_bf16 v[4:7], a[36:39], v[28:31], v[4:7]\n"
    "v_mfma_f32_16x16x32_bf16 v[8:11], a[116:119], v[24:27], v[8:11]\n"
    "buffer_load_dwordx4 v64, s[0:3], s32 offen sc1 lds\n"
    "v_mfma_f32_16x16x32_bf16 v[12:15], a[116:119], v[28:31], v[12:15]\n"
    "v_mfma_f32_16x16x32_bf16 v[16:19], a[196:199], v[24:27], v[16:19]\n"
    "ds_read_b128 v[24:27], v65 offset:24576\n"
    "v_mfma_f32_16x16x32_bf16 v[20:23], a[196:199], v[28:31], v[20:23]\n"
    "ds_read_b128 v[28:31], v65 offset:25600\n"
    "s_waitcnt lgkmcnt(4)\n"
    "v_mfma_f32_16x16x32_bf16 v[0:3], a[40:43], v[32:35], v[0:3]\n"
    "buffer_load_dwordx4 v64, s[0:3], s32 offen offset:1024 sc1 lds\n"
    "s_waitcnt vmcnt(10)\n"
    "v_mfma_f32_16x16x32_bf16 v[4:7], a[40:43], v[36:39], v[4:7]\n"
    "v_mov_b32 v61, s44\n"
    "v_mfma_f32_16x16x32_bf16 v[8:11], a[120:123], v[32:35], v[8:11]\n"
    "v_mfma_f32_16x16x32_bf16 v[12:15], a[120:123], v[36:39], v[12:15]\n"
    "v_mfma_f32_16x16x32_bf16 v[16:19], a[200:203], v[32:35], v[16:19]\n"
    "ds_read_b128 v[32:35], v65 offset:26624\n"
    "buffer_store_dword v61, v69, s[0:3], s43 offen sc1\n"
    "v_mfma_f32_16x16x32_bf16 v[20:23], a[200:203], v[36:39], v[20:23]\n"
    "ds_read_b128 v[36:39], v65 offset:27648\n"
    "s_waitcnt lgkmcnt(4)\n"
    "v_mfma_f32_16x16x32_bf16 v[0:3], a[44:47], v[40:43], v[0:3]\n"
    "s_mov_b32 s46, s34\n"
    "s_mov_b32 s43, s41\n"
    "s_mov_b32 s44, s42\n"
    "v_mfma_f32_16x16x32_bf16 v[4:7], a[44:47], v[44:47], v[4:7]\n"
    "s_mov_b32 s41, s39\n"
    "s_mov_b32 s42, s40\n"
    "s_mov_b32 s34, s33\n"
    "v_mfma_f32_16x16x32_bf16 v[8:11], a[124:127], v[40:43], v[8:11]\n"
    "s_mov_b32 s35, s36\n"
    "s_xor_b32 s37, s37, 40960\n"
    "s_add_u32 s26, s26, 1\n"
    "v_mfma_f32_16x16x32_bf16 v[12:15], a[124:127], v[44:47], v[12:15]\n"
    "s_sub_u32 s27, s26, 10\n"
    "s_ashr_i32 s28, s27, 1\n"
    "s_and_b32 s29, s27, 1\n"
    "v_mfma_f32_16x16x32_bf16 v[16:19], a[204:207], v[40:43], v[16:19]\n"
    "ds_read_b128 v[40:43], v65 offset:28672\n"
    "s_add_u32 s30, s28, 10\n"
    "v_mfma_f32_16x16x32_bf16 v[20:23], a[204:207], v[44:47], v[20:23]\n"
    "ds_read_b128 v[44:47], v65 offset:29696\n"
    "s_waitcnt lgkmcnt(4)\n"
    "v_mfma_f32_16x16x32_bf16 v[0:3], a[48:51], v[24:27], v[0:3]\n"
    "s_cmp_lt_i32 s27, 0\n"
    "s_cselect_b32 s29, 0, s29\n"
    "s_cselect_b32 s30, s26, s30\n"
    "v_mfma_f32_16x16x32_bf16 v[4:7], a[48:51], v[28:31], v[4:7]\n"
    "s_cmp_eq_u32 s29, 1\n"
    "s_cselect_b32 s30, s28, s30\n"
    "s_cselect_b32 s27, s15, 64\n"
    "v_mfma_f32_16x16x32_bf16 v[8:11], a[128:131], v[24:27], v[8:11]\n"
    "s_lshl_b32 s31, s29, 13\n"
    "s_add_u32 s40, s30, 1\n"
    "s_lshl_b32 s32, s30, 19\n"
    "v_mfma_f32_16x16x32_bf16 v[12:15], a[128:131], v[28:31], v[12:15]\n"
    "s_lshl_b32 s25, s30, 11\n"
    "s_xor_b32 s25, s25, s31\n"
    "s_and_b32 s25, s25, 0x2000\n"
    "v_mfma_f32_16x16x32_bf16 v[16:19], a[208:211], v[24:27], v[16:19]\n"
    "ds_read_b128 v[24:27], v65 offset:30720\n"
    "s_add_u32 s32, s32, s25\n"
    "v_mfma_f32_16x16x32_bf16 v[20:23], a[208:211], v[28:31], v[20:23]\n"
    "ds_read_b128 v[28:31], v65 offset:31744\n"
    "s_waitcnt lgkmcnt(4)\n"
    "v_mfma_f32_16x16x32_bf16 v[0:3], a[52:55], v[32:35], v[0:3]\n"
    "s_add_u32 s32, s32, s13\n"
    "s_lshl_b32 s33, s30, 20\n"
    "s_lshl_b32 s25, s30, 12\n"
    "v_mfma_f32_16x16x32_bf16 v[4:7], a[52:55], v[36:39], v[4:7]\n"
    "s_xor_b32 s25, s25, s31\n"
    "s_and_b32 s25, s25, 0x2000\n"
    "s_add_u32 s33, s33, s25\n"
    "v_mfma_f32_16x16x32_bf16 v[8:11], a[132:135], v[32:35], v[8:11]\n"
    "s_add_u32 s33, s33, s14\n"
    "s_mul_i32 s47, s40, s29\n"
    "v_mfma_f32_16x16x32_bf16 v[12:15], a[132:135], v[36:39], v[12:15]\n"
    "s_cmp_lt_u32 s30, s27\n"
    "s_cselect_b32 s36, 1, 0\n"
    "s_cselect_b32 s32, s32, 2130706432\n"
    "v_mfma_f32_16x16x32_bf16 v[16:19], a[212:215], v[32:35], v[16:19]\n"
    "ds_read_b128 v[32:35], v65 offset:32768\n"
    "v_mfma_f32_16x16x32_bf16 v[20:23], a[212:215], v[36:39], v[20:23]\n"
    "ds_read_b128 v[36:39], v65 offset:33792\n"
    "s_waitcnt lgkmcnt(4)\n"
    "v_mfma_f32_16x16x32_bf16 v[0:3], a[56:59], v[40:43], v[0:3]\n"
    "s_cmp_lt_u32 s30, s27\n"
    "s_cselect_b32 s39, s31, 2130706432\n"
    "s_cselect_b32 s47, s47, 0\n"
    "v_mfma_f32_16x16x32_bf16 v[4:7], a[56:59], v[44:47], v[4:7]\n"
    "s_cmp_gt_u32 s47, 0\n"
    "s_cselect_b32 s38, s17, 2130706432\n"
    "v_mfma_f32_16x16x32_bf16 v[8:11], a[136:139], v[40:43], v[8:11]\n"
    "buffer_load_dword v60, v68, s[0:3], s38 offen sc1\n"
    "v_mfma_f32_16x16x32_bf16 v[12:15], a[136:139], v[44:47], v[12:15]\n"
    "v_mfma_f32_16x16x32_bf16 v[16:19], a[216:219], v[40:43], v[16:19]\n"
    "ds_read_b128 v[40:43], v65 offset:34816\n"
    "v_mfma_f32_16x16x32_bf16 v[20:23], a[216:219], v[44:47], v[20:23]\n"
    "ds_read_b128 v[44:47], v65 offset:35840\n"
    "s_waitcnt lgkmcnt(4)\n"
    "v_mfma_f32_16x16x32_bf16 v[0:3], a[60:63], v[24:27], v[0:3]\n"
    "v_mfma_f32_16x16x32_bf16 v[4:7], a[60:63], v[28:31], v[4:7]\n"
    "v_mfma_f32_16x16x32_bf16 v[8:11], a[140:143], v[24:27], v[8:11]\n"
    "v_mfma_f32_16x16x32_bf16 v[12:15], a[140:143], v[28:31], v[12:15]\n"
    "v_mfma_f32_16x16x32_bf16 v[16:19], a[220:223], v[24:27], v[16:19]\n"
    "ds_read_b128 v[24:27], v65 offset:36864\n"
    "v_mfma_f32_16x16x32_bf16 v[20:23], a[220:223], v[28:31], v[20:23]\n"
    "ds_read_b128 v[28:31], v65 offset:37888\n"
    "s_waitcnt lgkmcnt(4)\n"
    "v_mfma_f32_16x16x32_bf16 v[0:3], a[64:67], v[32:35], v[0:3]\n"
    "v_mfma_f32_16x16x32_bf16 v[4:7], a[64:67], v[36:39], v[4:7]\n"
    "v_mfma_f32_16x16x32_bf16 v[8:11], a[144:147], v[32:35], v[8:11]\n"
    "v_mfma_f32_16x16x32_bf16 v[12:15], a[144:147], v[36:39], v[12:15]\n"
    "v_mfma_f32_16x16x32_bf16 v[16:19], a[224:227], v[32:35], v[16:19]\n"
    "ds_read_b128 v[32:35], v65 offset:38912\n"
    "v_mfma_f32_16x16x32_bf16 v[20:23], a[224:227], v[36:39], v[20:23]\n"
    "ds_read_b128 v[36:39], v65 offset:39936\n"
    "s_waitcnt lgkmcnt(4)\n"
    "v_mfma_f32_16x16x32_bf16 v[0:3], a[68:71], v[40:43], v[0:3]\n"
    "v_mfma_f32_16x16x32_bf16 v[4:7], a[68:71], v[44:47], v[4:7]\n"
    "v_mfma_f32_16x16x32_bf16 v[8:11], a[148:151], v[40:43], v[8:11]\n"
    "v_mfma_f32_16x16x32_bf16 v[12:15], a[148:151], v[44:47], v[12:15]\n"
    "v_mfma_f32_16x16x32_bf16 v[16:19], a[228:231], v[40:43], v[16:19]\n"
    "v_mfma_f32_16x16x32_bf16 v[20:23], a[228:231], v[44:47], v[20:23]\n"
    "s_waitcnt lgkmcnt(2)\n"
    "v_mfma_f32_16x16x32_bf16 v[0:3], a[72:75], v[24:27], v[0:3]\n"
    "v_mfma_f32_16x16x32_bf16 v[4:7], a[72:75], v[28:31], v[4:7]\n"
    "v_mfma_f32_16x16x32_bf16 v[8:11], a[152:155], v[24:27], v[8:11]\n"
    "v_mfma_f32_16x16x32_bf16 v[12:15], a[152:155], v[28:31], v[12:15]\n"
    "v_mfma_f32_16x16x32_bf16 v[16:19], a[232:235], v[24:27], v[16:19]\n"
    "v_mfma_f32_16x16x32_bf16 v[20:23], a[232:235], v[28:31], v[20:23]\n"
    "s_waitcnt lgkmcnt(0)\n"
    "v_mfma_f32_16x16x32_bf16 v[0:3], a[76:79], v[32:35], v[0:3]\n"
    "v_mfma_f32_16x16x32_bf16 v[4:7], a[76:79], v[36:39], v[4:7]\n"
    "v_mfma_f32_16x16x32_bf16 v[8:11], a[156:159], v[32:35], v[8:11]\n"
    "v_mfma_f32_16x16x32_bf16 v[12:15], a[156:159], v[36:39], v[12:15]\n"
    "v_mfma_f32_16x16x32_bf16 v[16:19], a[236:239], v[32:35], v[16:19]\n"
    "v_mfma_f32_16x16x32_bf16 v[20:23], a[236:239], v[36:39], v[20:23]\n"
    "s_nop 2\n"
    "v_cvt_pk_bf16_f32 v48, v0, v1\n"
    "v_cvt_pk_bf16_f32 v49, v2, v3\n"
    "v_cvt_pk_bf16_f32 v50, v8, v9\n"
    "v_cvt_pk_bf16_f32 v51, v10, v11\n"
    "v_cvt_pk_bf16_f32 v52, v4, v5\n"
    "v_cvt_pk_bf16_f32 v53, v6, v7\n"
    "v_cvt_pk_bf16_f32 v54, v12, v13\n"
    "v_cvt_pk_bf16_f32 v55, v14, v15\n"
    "v_cvt_pk_bf16_f32 v56, v16, v17\n"
    "v_cvt_pk_bf16_f32 v57, v18, v19\n"
    "v_cvt_pk_bf16_f32 v58, v20, v21\n"
    "v_cvt_pk_bf16_f32 v59, v22, v23\n"
    ".Lqw_join_%=:\n"
    "v_xor_b32 v65, 40960, v65\n"
    "s_waitcnt vmcnt(0) lgkmcnt(0)\n"
    "s_barrier\n"
    "s_cmp_le_u32 s26, 138\n"
    "s_cbranch_scc1 .Lqw_top_%=\n"
    "buffer_store_dwordx4 v[48:51], v66, s[0:3], s46 offen offset:0 sc1\n"
    "buffer_store_dwordx4 v[52:55], v66, s[0:3], s46 offen offset:1024 sc1\n"
    "buffer_store_dwordx4 v[56:59], v67, s[0:3], s46 offen sc1\n"
    "s_waitcnt vmcnt(0)\n"
    "v_mov_b32 v61, s44\n"
    "buffer_store_dword v61, v69, s[0:3], s43 offen sc1\n"
    "s_waitcnt vmcnt(0) lgkmcnt(0)\n"
    "s_endpgm\n"
    ".Lqw_wx_%=:\n"
    "buffer_store_dwordx4 v[48:51], v66, s[0:3], s46 offen offset:0 sc1\n"
    "buffer_store_dwordx4 v[52:55], v66, s[0:3], s46 offen offset:1024 sc1\n"
    "buffer_store_dwordx4 v[56:59], v67, s[0:3], s46 offen sc1\n"
    "s_waitcnt vmcnt(0)\n"
    "v_mov_b32 v61, s44\n"
    "buffer_store_dword v61, v69, s[0:3], s43 offen sc1\n"
    "s_mov_b32 s43, 2130706432\n"
    "s_mov_b32 s46, 2130706432\n"
    ".Lqw_rt4_%=:\n"
    "s_memrealtime s[22:23]\n"
    "s_waitcnt lgkmcnt(0)\n"
    "s_sub_u32 s24, s45, s22\n"
    "s_cmp_lt_u32 s24, 600\n"
    "s_cbranch_scc1 .Lqw_rt4_%=\n"
    ".Lqw_po5_%=:\n"
    "buffer_load_dword v60, v68, s[0:3], s17 offen sc1\n"
    "s_waitcnt vmcnt(0)\n"
    "s_memrealtime s[22:23]\n"
    "s_waitcnt lgkmcnt(0)\n"
    "s_add_u32 s45, s22, 256\n"
    "v_cmp_gt_i32 vcc, s47, v60\n"
    "s_nop 0\n"
    "s_cbranch_vccz .Lqw_pd6_%=\n"
    "s_add_u32 s20, s20, 1\n"
    "s_cmp_lt_u32 s20, 0x40000\n"
    "s_cbranch_scc1 .Lqw_sp7_%=\n"
    "v_mov_b32 v60, 51\n"
    "s_mov_b32 s24, 33728\n"
    "buffer_store_dword v60, off, s[0:3], s24 sc1\n"
    "s_waitcnt vmcnt(0)\n"
    "s_branch .Lqw_pd6_%=\n"
    ".Lqw_sp7_%=:\n"
    "s_sleep 10\n"
    "s_branch .Lqw_po5_%=\n"
    ".Lqw_pd6_%=:\n"
    "s_branch .Lqw_wxb_%=\n"
    ".Lqw_skip_%=:\n"
    "buffer_store_dwordx4 v[48:51], v66, s[0:3], s46 offen offset:0 sc1\n"
    "buffer_store_dwordx4 v[52:55], v66, s[0:3], s46 offen offset:1024 sc1\n"
    "buffer_store_dwordx4 v[56:59], v67, s[0:3], s46 offen sc1\n"
    "s_add_u32 m0, s37, 0\n"
    "s_nop 0\n"
    "buffer_load_dwordx4 v64, s[0:3], s32 offen sc1 lds\n"
    "buffer_load_dwordx4 v64, s[0:3], s32 offen offset:1024 sc1 lds\n"
    "s_add_u32 m0, s37, 8192\n"
    "s_add_u32 s32, s32, 0x4000\n"
    "buffer_load_dwordx4 v64, s[0:3], s32 offen sc1 lds\n"
    "buffer_load_dwordx4 v64, s[0:3], s32 offen offset:1024 sc1 lds\n"
    "s_add_u32 m0, s37, 16384\n"
    "s_add_u32 s32, s32, 0x4000\n"
    "buffer_load_dwordx4 v64, s[0:3], s32 offen sc1 lds\n"
    "buffer_load_dwordx4 v64, s[0:3], s32 offen offset:1024 sc1 lds\n"
    "s_add_u32 m0, s37, 24576\n"
    "s_add_u32 s32, s32, 0x4000\n"
    "buffer_load_dwordx4 v64, s[0:3], s32 offen sc1 lds\n"
    "buffer_load_dwordx4 v64, s[0:3], s32 offen offset:1024 sc1 lds\n"
    "s_add_u32 m0, s37, 32768\n"
    "s_add_u32 s32, s32, 0x4000\n"
    "buffer_load_dwordx4 v64, s[0:3], s32 offen sc1 lds\n"
    "buffer_load_dwordx4 v64, s[0:3], s32 offen offset:1024 sc1 lds\n"
    "s_waitcnt vmcnt(0)\n"
    "v_mov_b32 v61, s44\n"
    "buffer_store_dword v61, v69, s[0:3], s43 offen sc1\n"
    "s_mov_b32 s34, 2130706432\n"
    "s_mov_b32 s46, s34\n"
    "s_mov_b32 s43, s41\n"
    "s_mov_b32 s44, s42\n"
    "s_mov_b32 s41, s39\n"
    "s_mov_b32 s42, s40\n"
    "s_mov_b32 s34, s33\n"
    "s_mov_b32 s35, s36\n"
    "s_xor_b32 s37, s37, 40960\n"
    "s_add_u32 s26, s26, 1\n"
    "s_sub_u32 s27, s26, 10\n"
    "s_ashr_i32 s28, s27, 1\n"
    "s_and_b32 s29, s27, 1\n"
    "s_add_u32 s30, s28, 10\n"
    "s_cmp_lt_i32 s27, 0\n"
    "s_cselect_b32 s29, 0, s29\n"
    "s_cselect_b32 s30, s26, s30\n"
    "s_cmp_eq_u32 s29, 1\n"
    "s_cselect_b32 s30, s28, s30\n"
    "s_cselect_b32 s27, s15, 64\n"
    "s_lshl_b32 s31, s29, 13\n"
    "s_add_u32 s40, s30, 1\n"
    "s_lshl_b32 s32, s30, 19\n"
    "s_lshl_b32 s25, s30, 11\n"
    "s_xor_b32 s25, s25, s31\n"
    "s_and_b32 s25, s25, 0x2000\n"
    "s_add_u32 s32, s32, s25\n"
    "s_add_u32 s32, s32, s13\n"
    "s_lshl_b32 s33, s30, 20\n"
    "s_lshl_b32 s25, s30, 12\n"
    "s_xor_b32 s25, s25, s31\n"
    "s_and_b32 s25, s25, 0x2000\n"
    "s_add_u32 s33, s33, s25\n"
    "s_add_u32 s33, s33, s14\n"
    "s_mul_i32 s47, s40, s29\n"
    "s_cmp_lt_u32 s30, s27\n"
    "s_cselect_b32 s36, 1, 0\n"
    "s_cselect_b32 s32, s32, 2130706432\n"
    "s_cmp_lt_u32 s30, s27\n"
    "s_cselect_b32 s39, s31, 2130706432\n"
    "s_cselect_b32 s47, s47, 0\n"
    "s_cmp_gt_u32 s47, 0\n"
    "s_cselect_b32 s38, s17, 2130706432\n"
    "buffer_load_dword v60, v68, s[0:3], s38 offen sc1\n"
    "s_branch .Lqw_join_%=\n"
    :: "{s[0:3]}"(s0_), "{s[4:7]}"(s4_), "{s[8:11]}"(s8_), "{s[12:15]}"(s12_), "{s[16:19]}"(s16_), "{v[0:3]}"(v0_), "{v[64:67]}"(v64_), "{v[68:71]}"(v68_) : "memory", "v255", "a255", "s95");
    __builtin_unreachable();
}
DEV void b1_role(const MegaArgs& A, const int L, const int i, const int rep) {
    ROLE_HEAD;
    const int qd = 4 * w + lq, sw = (l16 & 6) << 3, hw = w < 3 ? w : 2, hd = 3 * i + w;
    const bool live = w < 3 && hd < 32;
    const unsigned om = live ? RC_O - RC_X1 - w * 2048 + (hd >> 3) * 16384 + ((hd >> 1) & 3) * 2048 + (lq & 1) * 1024 + (2 * (hd & 1) + (lq >> 1)) * 256 + l16 * 16 : OOB;
    const unsigned ox = live && l < 16 ? RC_O - RC_X1 - w * 2048 + 65536 + hd * 256 + l16 * 16 : OOB;
    u64 wm[4];
#pragma unroll
    for (int e = 0; e < 4; ++e) { const int d = l16 + 16 - 16 * (e >> 1) - 4 * lq - 2 * (e & 1) - (l16 & 1); wm[e] = __ballot(d >= 0 && d <= 14); }
    const u32x4 s0_ = {RFL(rx[0]), RFL(rx[1]), RFL(rx[2]), RFL(rx[3])};
    const u32x4 s4_ = {RFL(lo(A.wqkv + 60 * i + 16 * w)), RFL(hi(A.wqkv + 60 * i + 16 * w)), RFL((640 * 1920 - 60 * i - 16 * w) * 2), RFL(0x20000u)};
    const u32x4 s8_ = {RFL((unsigned)wm[0]), RFL((unsigned)(wm[0] >> 32)), RFL((unsigned)wm[1]), RFL((unsigned)(wm[1] >> 32))};
    const u32x4 s12_ = {RFL((unsigned)wm[2]), RFL((unsigned)(wm[2] >> 32)), RFL((unsigned)wm[3]), RFL((unsigned)(wm[3] >> 32))};
    const u32x4 s16_ = {RFL(w == 3 ? 0xf000f000u : 0u), RFL(w == 3 ? 0xf000f000u : 0u), RFL(w), RFL(L << 13)};
    const u32x4 s20_ = {RFL(REC0 + RC_X1 + w * 2048), RFL(xl(Z_X1 + rep * 4096, L)), RFL(DBG0 + ((L * 128 + 96 + i) * 4 + w) * 2048), RFL(0u)};
    const u32x4 v0_ = {(unsigned)((l >> 1) * 3840 + (l & 1) * 16 + 0), (unsigned)((l >> 1) * 3840 + (l & 1) * 16 + 256), (unsigned)((l >> 1) * 3840 + (l & 1) * 16 + 512), (unsigned)(w * 40960 + (8 * lq + (l16 >> 2)) * 32 + (l & 3) * 8)};
    const u32x4 v224_ = {(unsigned)(l * 16 + ((w + 0) & 3) * 2048), (unsigned)(l * 16 + ((w + 1) & 3) * 2048), (unsigned)(l * 16 + ((w + 2) & 3) * 2048), (unsigned)(l * 16 + ((w + 3) & 3) * 2048)};
    const u32x4 v228_ = {(unsigned)(l * 16), (unsigned)((l & 31) * 128), (unsigned)(l < 4 && w == 3 ? xl(Z_O + l * 2048 + i * 128, L) : OOB), (unsigned)(qd == 15 ? 86016 : 40960 + (qd / 5) * 2048 + l16 * 64 + (((qd % 5) * 8) ^ sw))};
    const u32x4 v232_ = {(unsigned)(qd == 15 ? 86016 : 49152 + (qd / 5) * 4096 + l16 * 64 + (((qd % 5) * 8) ^ sw)), (unsigned)(qd == 15 ? 86016 : 61440 + (qd / 5) * 4096 + l16 * 64 + (((qd % 5) * 8) ^ sw)), (unsigned)(40960 + hw * 2048 + l16 * 64 + ((lq * 16) ^ sw)), (unsigned)(49152 + hw * 4096 + l16 * 64 + ((lq * 16) ^ sw))};
    const u32x4 v236_ = {(unsigned)(51200 + hw * 4096 + l16 * 64 + ((lq * 16) ^ sw)), (unsigned)(61440 + hw * 4096 + (l >> 2) * 64 + ((0 + (l & 3) * 8) ^ (((l >> 2) & 6) << 3))), (unsigned)(63488 + hw * 4096 + (l >> 2) * 64 + ((0 + (l & 3) * 8) ^ (((l >> 2) & 6) << 3))), (unsigned)(61440 + hw * 4096 + (l >> 2) * 64 + ((32 + (l & 3) * 8) ^ (((l >> 2) & 6) << 3)))};
    const u32x4 v240_ = {(unsigned)(63488 + hw * 4096 + (l >> 2) * 64 + ((32 + (l & 3) * 8) ^ (((l >> 2) & 6) << 3))), (unsigned)(73728 + w * 2048 + l * 16), (unsigned)(73728 + (l >> 5) * 1024 + ((l16 >> 2) * 16 + l16) * 16 + (l & 3) * 4), (unsigned)(81920 + w * 512 + l * 4)};
    const u32x4 v244_ = {(unsigned)(81920 + 3 * 512 + (l >> 5) * 256 + (48 + l16) * 4), (unsigned)(om), (unsigned)(ox), (unsigned)((l & 1) ? 0x01000c0cu : 0x0c0c0100u)};
    const u32x4 v248_ = {(unsigned)((l & 1) ? 0x03020c0cu : 0x0c0c0302u), (unsigned)(0xff800000u), (unsigned)(0u), (unsigned)(0u)};
    asm volatile(
    "s_mov_b32 s24, 0\n"
    "s_sleep 35\n"
    "s_sleep 35\n"
    "s_sleep 35\n"
    "s_sleep 35\n"
    "s_sleep 35\n"
    "s_sleep 35\n"
    "s_sleep 35\n"
    "s_sleep 35\n"
    "s_sleep 35\n"
    "s_sleep 35\n"
    "s_sleep 35\n"
    "s_sleep 35\n"
    "s_sleep 35\n"
    "s_sleep 35\n"
    ".Lb1_ho1_%=:\n"
    "s_mul_i32 s32, s18, 40960\n"
    "s_add_u32 s35, s18, 0\n"
    "s_and_b32 s35, s35, 3\n"
    "s_add_u32 s35, s35, 0\n"
    "s_mul_i32 s35, s35, 122880\n"
    "s_add_u32 m0, s32, 0\n"
    "s_nop 0\n"
    "buffer_load_dwordx4 v0, s[4:7], s35 offen offset:0 lds\n"
    "buffer_load_dwordx4 v1, s[4:7], s35 offen offset:1024 lds\n"
    "buffer_load_dwordx4 v2, s[4:7], s35 offen offset:2048 lds\n"
    "s_add_u32 s35, s18, 1\n"
    "s_and_b32 s35, s35, 3\n"
    "s_add_u32 s35, s35, 0\n"
    "s_mul_i32 s35, s35, 122880\n"
    "s_add_u32 m0, s32, 3072\n"
    "s_nop 0\n"
    "buffer_load_dwordx4 v0, s[4:7], s35 offen offset:0 lds\n"
    "buffer_load_dwordx4 v1, s[4:7], s35 offen offset:1024 lds\n"
    "buffer_load_dwordx4 v2, s[4:7], s35 offen offset:2048 lds\n"
    "s_add_u32 s35, s18, 2\n"
    "s_and_b32 s35, s35, 3\n"
    "s_add_u32 s35, s35, 0\n"
    "s_mul_i32 s35, s35, 122880\n"
    "s_add_u32 m0, s32, 6144\n"
    "s_nop 0\n"
    "buffer_load_dwordx4 v0, s[4:7], s35 offen offset:0 lds\n"
    "buffer_load_dwordx4 v1, s[4:7], s35 offen offset:1024 lds\n"
    "buffer_load_dwordx4 v2, s[4:7], s35 offen offset:2048 lds\n"
    "s_add_u32 s35, s18, 3\n"
    "s_and_b32 s35, s35, 3\n"
    "s_add_u32 s35, s35, 0\n"
    "s_mul_i32 s35, s35, 122880\n"
    "s_add_u32 m0, s32, 9216\n"
    "s_nop 0\n"
    "buffer_load_dwordx4 v0, s[4:7], s35 offen offset:0 lds\n"
    "buffer_load_dwordx4 v1, s[4:7], s35 offen offset:1024 lds\n"
    "buffer_load_dwordx4 v2, s[4:7], s35 offen offset:2048 lds\n"
    "s_add_u32 s35, s18, 0\n"
    "s_and_b32 s35, s35, 3\n"
    "s_add_u32 s35, s35, 4\n"
    "s_mul_i32 s35, s35, 122880\n"
    "s_add_u32 m0, s32, 12288\n"
    "s_nop 0\n"
    "buffer_load_dwordx4 v0, s[4:7], s35 offen offset:0 lds\n"
    "buffer_load_dwordx4 v1, s[4:7], s35 offen offset:1024 lds\n"
    "buffer_load_dwordx4 v2, s[4:7], s35 offen offset:2048 lds\n"
    "s_add_u32 s35, s18, 1\n"
    "s_and_b32 s35, s35, 3\n"
    "s_add_u32 s35, s35, 4\n"
    "s_mul_i32 s35, s35, 122880\n"
    "s_add_u32 m0, s32, 15360\n"
    "s_nop 0\n"
    "buffer_load_dwordx4 v0, s[4:7], s35 offen offset:0 lds\n"
    "buffer_load_dwordx4 v1, s[4:7], s35 offen offset:1024 lds\n"
    "buffer_load_dwordx4 v2, s[4:7], s35 offen offset:2048 lds\n"
    "s_add_u32 s35, s18, 2\n"
    "s_and_b32 s35, s35, 3\n"
    "s_add_u32 s35, s35, 4\n"
    "s_mul_i32 s35, s35, 122880\n"
    "s_add_u32 m0, s32, 18432\n"
    "s_nop 0\n"
    "buffer_load_dwordx4 v0, s[4:7], s35 offen offset:0 lds\n"
    "buffer_load_dwordx4 v1, s[4:7], s35 offen offset:1024 lds\n"
    "buffer_load_dwordx4 v2, s[4:7], s35 offen offset:2048 lds\n"
    "s_add_u32 s35, s18, 3\n"
    "s_and_b32 s35, s35, 3\n"
    "s_add_u32 s35, s35, 4\n"
    "s_mul_i32 s35, s35, 122880\n"
    "s_add_u32 m0, s32, 21504\n"
    "s_nop 0\n"
    "buffer_load_dwordx4 v0, s[4:7], s35 offen offset:0 lds\n"
    "buffer_load_dwordx4 v1, s[4:7], s35 offen offset:1024 lds\n"
    "buffer_load_dwordx4 v2, s[4:7], s35 offen offset:2048 lds\n"
    "s_add_u32 s35, s18, 0\n"
    "s_and_b32 s35, s35, 3\n"
    "s_add_u32 s35, s35, 8\n"
    "s_mul_i32 s35, s35, 122880\n"
    "s_add_u32 m0, s32, 24576\n"
    "s_nop 0\n"
    "buffer_load_dwordx4 v0, s[4:7], s35 offen offset:0 lds\n"
    "buffer_load_dwordx4 v1, s[4:7], s35 offen offset:1024 lds\n"
    "buffer_load_dwordx4 v2, s[4:7], s35 offen offset:2048 lds\n"
    "s_add_u32 s35, s18, 1\n"
    "s_and_b32 s35, s35, 3\n"
    "s_add_u32 s35, s35, 8\n"
    "s_mul_i32 s35, s35, 122880\n"
    "s_add_u32 m0, s32, 27648\n"
    "s_nop 0\n"
    "buffer_load_dwordx4 v0, s[4:7], s35 offen offset:0 lds\n"
    "buffer_load_dwordx4 v1, s[4:7], s35 offen offset:1024 lds\n"
    "buffer_load_dwordx4 v2, s[4:7], s35 offen offset:2048 lds\n"
    "s_add_u32 s35, s18, 2\n"
    "s_and_b32 s35, s35, 3\n"
    "s_add_u32 s35, s35, 8\n"
    "s_mul_i32 s35, s35, 122880\n"
    "s_add_u32 m0, s32, 30720\n"
    "s_nop 0\n"
    "buffer_load_dwordx4 v0, s[4:7], s35 offen offset:0 lds\n"
    "buffer_load_dwordx4 v1, s[4:7], s35 offen offset:1024 lds\n"
    "buffer_load_dwordx4 v2, s[4:7], s35 offen offset:2048 lds\n"
    "s_add_u32 s35, s18, 3\n"
    "s_and_b32 s35, s35, 3\n"
    "s_add_u32 s35, s35, 8\n"
    "s_mul_i32 s35, s35, 122880\n"
    "s_add_u32 m0, s32, 33792\n"
    "s_nop 0\n"
    "buffer_load_dwordx4 v0, s[4:7], s35 offen offset:0 lds\n"
    "buffer_load_dwordx4 v1, s[4:7], s35 offen offset:1024 lds\n"
    "buffer_load_dwordx4 v2, s[4:7], s35 offen offset:2048 lds\n"
    "s_add_u32 s35, s18, 0\n"
    "s_and_b32 s35, s35, 3\n"
    "s_add_u32 s35, s35, 12\n"
    "s_mul_i32 s35, s35, 122880\n"
    "s_add_u32 m0, s32, 36864\n"
    "s_nop 0\n"
    "buffer_load_dwordx4 v0, s[4:7], s35 offen offset:0 lds\n"
    "buffer_load_dwordx4 v1, s[4:7], s35 offen offset:1024 lds\n"
    "buffer_load_dwordx4 v2, s[4:7], s35 offen offset:2048 lds\n"
    "s_waitcnt vmcnt(36)\n"
    "ds_read_b64_tr_b16 a[0:1], v3 offset:0\n"
    "ds_read_b64_tr_b16 a[2:3], v3 offset:128\n"
    "ds_read_b64_tr_b16 a[80:81], v3 offset:1024\n"
    "ds_read_b64_tr_b16 a[82:83], v3 offset:1152\n"
    "ds_read_b64_tr_b16 a[160:161], v3 offset:2048\n"
    "ds_read_b64_tr_b16 a[162:163], v3 offset:2176\n"
    "s_waitcnt lgkmcnt(0)\n"
    "s_add_u32 s35, s18, 1\n"
    "s_and_b32 s35, s35, 3\n"
    "s_add_u32 s35, s35, 12\n"
    "s_mul_i32 s35, s35, 122880\n"
    "s_add_u32 m0, s32, 0\n"
    "s_nop 0\n"
    "buffer_load_dwordx4 v0, s[4:7], s35 offen offset:0 lds\n"
    "buffer_load_dwordx4 v1, s[4:7], s35 offen offset:1024 lds\n"
    "buffer_load_dwordx4 v2, s[4:7], s35 offen offset:2048 lds\n"
    "s_waitcnt vmcnt(36)\n"
    "ds_read_b64_tr_b16 a[4:5], v3 offset:3072\n"
    "ds_read_b64_tr_b16 a[6:7], v3 offset:3200\n"
    "ds_read_b64_tr_b16 a[84:85], v3 offset:4096\n"
    "ds_read_b64_tr_b16 a[86:87], v3 offset:4224\n"
    "ds_read_b64_tr_b16 a[164:165], v3 offset:5120\n"
    "ds_read_b64_tr_b16 a[166:167], v3 offset:5248\n"
    "s_waitcnt lgkmcnt(0)\n"
    "s_add_u32 s35, s18, 2\n"
    "s_and_b32 s35, s35, 3\n"
    "s_add_u32 s35, s35, 12\n"
    "s_mul_i32 s35, s35, 122880\n"
    "s_add_u32 m0, s32, 3072\n"
    "s_nop 0\n"
    "buffer_load_dwordx4 v0, s[4:7], s35 offen offset:0 lds\n"
    "buffer_load_dwordx4 v1, s[4:7], s35 offen offset:1024 lds\n"
    "buffer_load_dwordx4 v2, s[4:7], s35 offen offset:2048 lds\n"
    "s_waitcnt vmcnt(36)\n"
    "ds_read_b64_tr_b16 a[8:9], v3 offset:6144\n"
    "ds_read_b64_tr_b16 a[10:11], v3 offset:6272\n"
    "ds_read_b64_tr_b16 a[88:89], v3 offset:7168\n"
    "ds_read_b64_tr_b16 a[90:91], v3 offset:7296\n"
    "ds_read_b64_tr_b16 a[168:169], v3 offset:8192\n"
    "ds_read_b64_tr_b16 a[170:171], v3 offset:8320\n"
    "s_waitcnt lgkmcnt(0)\n"
    "s_add_u32 s35, s18, 3\n"
    "s_and_b32 s35, s35, 3\n"
    "s_add_u32 s35, s35, 12\n"
    "s_mul_i32 s35, s35, 122880\n"
    "s_add_u32 m0, s32, 6144\n"
    "s_nop 0\n"
    "buffer_load_dwordx4 v0, s[4:7], s35 offen offset:0 lds\n"
    "buffer_load_dwordx4 v1, s[4:7], s35 offen offset:1024 lds\n"
    "buffer_load_dwordx4 v2, s[4:7], s35 offen offset:2048 lds\n"
    "s_waitcnt vmcnt(36)\n"
    "ds_read_b64_tr_b16 a[12:13], v3 offset:9216\n"
    "ds_read_b64_tr_b16 a[14:15], v3 offset:9344\n"
    "ds_read_b64_tr_b16 a[92:93], v3 offset:10240\n"
    "ds_read_b64_tr_b16 a[94:95], v3 offset:10368\n"
    "ds_read_b64_tr_b16 a[172:173], v3 offset:11264\n"
    "ds_read_b64_tr_b16 a[174:175], v3 offset:11392\n"
    "s_waitcnt lgkmcnt(0)\n"
    "s_add_u32 s35, s18, 0\n"
    "s_and_b32 s35, s35, 3\n"
    "s_add_u32 s35, s35, 16\n"
    "s_mul_i32 s35, s35, 122880\n"
    "s_add_u32 m0, s32, 9216\n"
    "s_nop 0\n"
    "buffer_load_dwordx4 v0, s[4:7], s35 offen offset:0 lds\n"
    "buffer_load_dwordx4 v1, s[4:7], s35 offen offset:1024 lds\n"
    "buffer_load_dwordx4 v2, s[4:7], s35 offen offset:2048 lds\n"
    "s_waitcnt vmcnt(36)\n"
    "ds_read_b64_tr_b16 a[16:17], v3 offset:12288\n"
    "ds_read_b64_tr_b16 a[18:19], v3 offset:12416\n"
    "ds_read_b64_tr_b16 a[96:97], v3 offset:13312\n"
    "ds_read_b64_tr_b16 a[98:99], v3 offset:13440\n"
    "ds_read_b64_tr_b16 a[176:177], v3 offset:14336\n"
    "ds_read_b64_tr_b16 a[178:179], v3 offset:14464\n"
    "s_waitcnt lgkmcnt(0)\n"
    "s_add_u32 s35, s18, 1\n"
    "s_and_b32 s35, s35, 3\n"
    "s_add_u32 s35, s35, 16\n"
    "s_mul_i32 s35, s35, 122880\n"
    "s_add_u32 m0, s32, 12288\n"
    "s_nop 0\n"
    "buffer_load_dwordx4 v0, s[4:7], s35 offen offset:0 lds\n"
    "buffer_load_dwordx4 v1, s[4:7], s35 offen offset:1024 lds\n"
    "buffer_load_dwordx4 v2, s[4:7], s35 offen offset:2048 lds\n"
    "s_waitcnt vmcnt(36)\n"
    "ds_read_b64_tr_b16 a[20:21], v3 offset:15360\n"
    "ds_read_b64_tr_b16 a[22:23], v3 offset:15488\n"
    "ds_read_b64_tr_b16 a[100:101], v3 offset:16384\n"
    "ds_read_b64_tr_b16 a[102:103], v3 offset:16512\n"
    "ds_read_b64_tr_b16 a[180:181], v3 offset:17408\n"
    "ds_read_b64_tr_b16 a[182:183], v3 offset:17536\n"
    "s_waitcnt lgkmcnt(0)\n"
    "s_add_u32 s35, s18, 2\n"
    "s_and_b32 s35, s35, 3\n"
    "s_add_u32 s35, s35, 16\n"
    "s_mul_i32 s35, s35, 122880\n"
    "s_add_u32 m0, s32, 15360\n"
    "s_nop 0\n"
    "buffer_load_dwordx4 v0, s[4:7], s35 offen offset:0 lds\n"
    "buffer_load_dwordx4 v1, s[4:7], s35 offen offset:1024 lds\n"
    "buffer_load_dwordx4 v2, s[4:7], s35 offen offset:2048 lds\n"
    "s_waitcnt vmcnt(36)\n"
    "ds_read_b64_tr_b16 a[24:25], v3 offset:18432\n"
    "ds_read_b64_tr_b16 a[26:27], v3 offset:18560\n"
    "ds_read_b64_tr_b16 a[104:105], v3 offset:19456\n"
    "ds_read_b64_tr_b16 a[106:107], v3 offset:19584\n"
    "ds_read_b64_tr_b16 a[184:185], v3 offset:20480\n"
    "ds_read_b64_tr_b16 a[186:187], v3 offset:20608\n"
    "s_waitcnt lgkmcnt(0)\n"
    "s_add_u32 s35, s18, 3\n"
    "s_and_b32 s35, s35, 3\n"
    "s_add_u32 s35, s35, 16\n"
    "s_mul_i32 s35, s35, 122880\n"
    "s_add_u32 m0, s32, 18432\n"
    "s_nop 0\n"
    "buffer_load_dwordx4 v0, s[4:7], s35 offen offset:0 lds\n"
    "buffer_load_dwordx4 v1, s[4:7], s35 offen offset:1024 lds\n"
    "buffer_load_dwordx4 v2, s[4:7], s35 offen offset:2048 lds\n"
    "s_waitcnt vmcnt(36)\n"
    "ds_read_b64_tr_b16 a[28:29], v3 offset:21504\n"
    "ds_read_b64_tr_b16 a[30:31], v3 offset:21632\n"
    "ds_read_b64_tr_b16 a[108:109], v3 offset:22528\n"
    "ds_read_b64_tr_b16 a[110:111], v3 offset:22656\n"
    "ds_read_b64_tr_b16 a[188:189], v3 offset:23552\n"
    "ds_read_b64_tr_b16 a[190:191], v3 offset:23680\n"
    "s_waitcnt lgkmcnt(0)\n"
    "s_waitcnt vmcnt(33)\n"
    "ds_read_b64_tr_b16 a[32:33], v3 offset:24576\n"
    "ds_read_b64_tr_b16 a[34:35], v3 offset:24704\n"
    "ds_read_b64_tr_b16 a[112:113], v3 offset:25600\n"
    "ds_read_b64_tr_b16 a[114:115], v3 offset:25728\n"
    "ds_read_b64_tr_b16 a[192:193], v3 offset:26624\n"
    "ds_read_b64_tr_b16 a[194:195], v3 offset:26752\n"
    "s_waitcnt lgkmcnt(0)\n"
    "s_waitcnt vmcnt(30)\n"
    "ds_read_b64_tr_b16 a[36:37], v3 offset:27648\n"
    "ds_read_b64_tr_b16 a[38:39], v3 offset:27776\n"
    "ds_read_b64_tr_b16 a[116:117], v3 offset:28672\n"
    "ds_read_b64_tr_b16 a[118:119], v3 offset:28800\n"
    "ds_read_b64_tr_b16 a[196:197], v3 offset:29696\n"
    "ds_read_b64_tr_b16 a[198:199], v3 offset:29824\n"
    "s_waitcnt lgkmcnt(0)\n"
    "s_waitcnt vmcnt(27)\n"
    "ds_read_b64_tr_b16 a[40:41], v3 offset:30720\n"
    "ds_read_b64_tr_b16 a[42:43], v3 offset:30848\n"
    "ds_read_b64_tr_b16 a[120:121], v3 offset:31744\n"
    "ds_read_b64_tr_b16 a[122:123], v3 offset:31872\n"
    "ds_read_b64_tr_b16 a[200:201], v3 offset:32768\n"
    "ds_read_b64_tr_b16 a[202:203], v3 offset:32896\n"
    "s_waitcnt lgkmcnt(0)\n"
    "s_waitcnt vmcnt(24)\n"
    "ds_read_b64_tr_b16 a[44:45], v3 offset:33792\n"
    "ds_read_b64_tr_b16 a[46:47], v3 offset:33920\n"
    "ds_read_b64_tr_b16 a[124:125], v3 offset:34816\n"
    "ds_read_b64_tr_b16 a[126:127], v3 offset:34944\n"
    "ds_read_b64_tr_b16 a[204:205], v3 offset:35840\n"
    "ds_read_b64_tr_b16 a[206:207], v3 offset:35968\n"
    "s_waitcnt lgkmcnt(0)\n"
    "s_waitcnt vmcnt(21)\n"
    "ds_read_b64_tr_b16 a[48:49], v3 offset:36864\n"
    "ds_read_b64_tr_b16 a[50:51], v3 offset:36992\n"
    "ds_read_b64_tr_b16 a[128:129], v3 offset:37888\n"
    "ds_read_b64_tr_b16 a[130:131], v3 offset:38016\n"
    "ds_read_b64_tr_b16 a[208:209], v3 offset:38912\n"
    "ds_read_b64_tr_b16 a[210:211], v3 offset:39040\n"
    "s_waitcnt lgkmcnt(0)\n"
    "s_waitcnt vmcnt(18)\n"
    "ds_read_b64_tr_b16 a[52:53], v3 offset:0\n"
    "ds_read_b64_tr_b16 a[54:55], v3 offset:128\n"
    "ds_read_b64_tr_b16 a[132:133], v3 offset:1024\n"
    "ds_read_b64_tr_b16 a[134:135], v3 offset:1152\n"
    "ds_read_b64_tr_b16 a[212:213], v3 offset:2048\n"
    "ds_read_b64_tr_b16 a[214:215], v3 offset:2176\n"
    "s_waitcnt lgkmcnt(0)\n"
    "s_waitcnt vmcnt(15)\n"
    "ds_read_b64_tr_b16 a[56:57], v3 offset:3072\n"
    "ds_read_b64_tr_b16 a[58:59], v3 offset:3200\n"
    "ds_read_b64_tr_b16 a[136:137], v3 offset:4096\n"
    "ds_read_b64_tr_b16 a[138:139], v3 offset:4224\n"
    "ds_read_b64_tr_b16 a[216:217], v3 offset:5120\n"
    "ds_read_b64_tr_b16 a[218:219], v3 offset:5248\n"
    "s_waitcnt lgkmcnt(0)\n"
    "s_waitcnt vmcnt(12)\n"
    "ds_read_b64_tr_b16 a[60:61], v3 offset:6144\n"
    "ds_read_b64_tr_b16 a[62:63], v3 offset:6272\n"
    "ds_read_b64_tr_b16 a[140:141], v3 offset:7168\n"
    "ds_read_b64_tr_b16 a[142:143], v3 offset:7296\n"
    "ds_read_b64_tr_b16 a[220:221], v3 offset:8192\n"
    "ds_read_b64_tr_b16 a[222:223], v3 offset:8320\n"
    "s_waitcnt lgkmcnt(0)\n"
    "s_waitcnt vmcnt(9)\n"
    "ds_read_b64_tr_b16 a[64:65], v3 offset:9216\n"
    "ds_read_b64_tr_b16 a[66:67], v3 offset:9344\n"
    "ds_read_b64_tr_b16 a[144:145], v3 offset:10240\n"
    "ds_read_b64_tr_b16 a[146:147], v3 offset:10368\n"
    "ds_read_b64_tr_b16 a[224:225], v3 offset:11264\n"
    "ds_read_b64_tr_b16 a[226:227], v3 offset:11392\n"
    "s_waitcnt lgkmcnt(0)\n"
    "s_waitcnt vmcnt(6)\n"
    "ds_read_b64_tr_b16 a[68:69], v3 offset:12288\n"
    "ds_read_b64_tr_b16 a[70:71], v3 offset:12416\n"
    "ds_read_b64_tr_b16 a[148:149], v3 offset:13312\n"
    "ds_read_b64_tr_b16 a[150:151], v3 offset:13440\n"
    "ds_read_b64_tr_b16 a[228:229], v3 offset:14336\n"
    "ds_read_b64_tr_b16 a[230:231], v3 offset:14464\n"
    "s_waitcnt lgkmcnt(0)\n"
    "s_waitcnt vmcnt(3)\n"
    "ds_read_b64_tr_b16 a[72:73], v3 offset:15360\n"
    "ds_read_b64_tr_b16 a[74:75], v3 offset:15488\n"
    "ds_read_b64_tr_b16 a[152:153], v3 offset:16384\n"
    "ds_read_b64_tr_b16 a[154:155], v3 offset:16512\n"
    "ds_read_b64_tr_b16 a[232:233], v3 offset:17408\n"
    "ds_read_b64_tr_b16 a[234:235], v3 offset:17536\n"
    "s_waitcnt lgkmcnt(0)\n"
    "s_waitcnt vmcnt(0)\n"
    "ds_read_b64_tr_b16 a[76:77], v3 offset:18432\n"
    "ds_read_b64_tr_b16 a[78:79], v3 offset:18560\n"
    "ds_read_b64_tr_b16 a[156:157], v3 offset:19456\n"
    "ds_read_b64_tr_b16 a[158:159], v3 offset:19584\n"
    "ds_read_b64_tr_b16 a[236:237], v3 offset:20480\n"
    "ds_read_b64_tr_b16 a[238:239], v3 offset:20608\n"
    "s_waitcnt lgkmcnt(0)\n"
    "s_waitcnt lgkmcnt(0)\n"
    "s_barrier\n"
    "v_mov_b32 v32, 0x3f803f80\n"
    "v_mov_b32 v36, 0\n"
    "v_mov_b32 v33, 0x3f803f80\n"
    "v_mov_b32 v37, 0\n"
    "v_mov_b32 v34, 0x3f803f80\n"
    "v_mov_b32 v38, 0\n"
    "v_mov_b32 v35, 0x3f803f80\n"
    "v_mov_b32 v39, 0\n"
    "s_nop 0\n"
    "v_mfma_f32_16x16x32_bf16 v[96:99], a[0:3], v[32:35], 0\n"
    "v_mfma_f32_16x16x32_bf16 v[100:103], a[80:83], v[32:35], 0\n"
    "v_mfma_f32_16x16x32_bf16 v[104:107], a[160:163], v[32:35], 0\n"
    "v_mfma_f32_16x16x32_bf16 v[96:99], a[4:7], v[32:35], v[96:99]\n"
    "v_mfma_f32_16x16x32_bf16 v[100:103], a[84:87], v[32:35], v[100:103]\n"
    "v_mfma_f32_16x16x32_bf16 v[104:107], a[164:167], v[32:35], v[104:107]\n"
    "v_mfma_f32_16x16x32_bf16 v[96:99], a[8:11], v[32:35], v[96:99]\n"
    "v_mfma_f32_16x16x32_bf16 v[100:103], a[88:91], v[32:35], v[100:103]\n"
    "v_mfma_f32_16x16x32_bf16 v[104:107], a[168:171], v[32:35], v[104:107]\n"
    "v_mfma_f32_16x16x32_bf16 v[96:99], a[12:15], v[32:35], v[96:99]\n"
    "v_mfma_f32_16x16x32_bf16 v[100:103], a[92:95], v[32:35], v[100:103]\n"
    "v_mfma_f32_16x16x32_bf16 v[104:107], a[172:175], v[32:35], v[104:107]\n"
    "v_mfma_f32_16x16x32_bf16 v[96:99], a[16:19], v[32:35], v[96:99]\n"
    "v_mfma_f32_16x16x32_bf16 v[100:103], a[96:99], v[32:35], v[100:103]\n"
    "v_mfma_f32_16x16x32_bf16 v[104:107], a[176:179], v[32:35], v[104:107]\n"
    "v_mfma_f32_16x16x32_bf16 v[96:99], a[20:23], v[32:35], v[96:99]\n"
    "v_mfma_f32_16x16x32_bf16 v[100:103], a[100:103], v[32:35], v[100:103]\n"
    "v_mfma_f32_16x16x32_bf16 v[104:107], a[180:183], v[32:35], v[104:107]\n"
    "v_mfma_f32_16x16x32_bf16 v[96:99], a[24:27], v[32:35], v[96:99]\n"
    "v_mfma_f32_16x16x32_bf16 v[100:103], a[104:107], v[32:35], v[100:103]\n"
    "v_mfma_f32_16x16x32_bf16 v[104:107], a[184:187], v[32:35], v[104:107]\n"
    "v_mfma_f32_16x16x32_bf16 v[96:99], a[28:31], v[32:35], v[96:99]\n"
    "v_mfma_f32_16x16x32_bf16 v[100:103], a[108:111], v[32:35], v[100:103]\n"
    "v_mfma_f32_16x16x32_bf16 v[104:107], a[188:191], v[32:35], v[104:107]\n"
    "v_mfma_f32_16x16x32_bf16 v[96:99], a[32:35], v[32:35], v[96:99]\n"
    "v_mfma_f32_16x16x32_bf16 v[100:103], a[112:115], v[32:35], v[100:103]\n"
    "v_mfma_f32_16x16x32_bf16 v[104:107], a[192:195], v[32:35], v[104:107]\n"
    "v_mfma_f32_16x16x32_bf16 v[96:99], a[36:39], v[32:35], v[96:99]\n"
    "v_mfma_f32_16x16x32_bf16 v[100:103], a[116:119], v[32:35], v[100:103]\n"
    "v_mfma_f32_16x16x32_bf16 v[104:107], a[196:199], v[32:35], v[104:107]\n"
    "v_mfma_f32_16x16x32_bf16 v[96:99], a[40:43], v[32:35], v[96:99]\n"
    "v_mfma_f32_16x16x32_bf16 v[100:103], a[120:123], v[32:35], v[100:103]\n"
    "v_mfma_f32_16x16x32_bf16 v[104:107], a[200:203], v[32:35], v[104:107]\n"
    "v_mfma_f32_16x16x32_bf16 v[96:99], a[44:47], v[32:35], v[96:99]\n"
    "v_mfma_f32_16x16x32_bf16 v[100:103], a[124:127], v[32:35], v[100:103]\n"
    "v_mfma_f32_16x16x32_bf16 v[104:107], a[204:207], v[32:35], v[104:107]\n"
    "v_mfma_f32_16x16x32_bf16 v[96:99], a[48:51], v[32:35], v[96:99]\n"
    "v_mfma_f32_16x16x32_bf16 v[100:103], a[128:131], v[32:35], v[100:103]\n"
    "v_mfma_f32_16x16x32_bf16 v[104:107], a[208:211], v[32:35], v[104:107]\n"
    "v_mfma_f32_16x16x32_bf16 v[96:99], a[52:55], v[32:35], v[96:99]\n"
    "v_mfma_f32_16x16x32_bf16 v[100:103], a[132:135], v[32:35], v[100:103]\n"
    "v_mfma_f32_16x16x32_bf16 v[104:107], a[212:215], v[32:35], v[104:107]\n"
    "v_mfma_f32_16x16x32_bf16 v[96:99], a[56:59], v[32:35], v[96:99]\n"
    "v_mfma_f32_16x16x32_bf16 v[100:103], a[136:139], v[32:35], v[100:103]\n"
    "v_mfma_f32_16x16x32_bf16 v[104:107], a[216:219], v[32:35], v[104:107]\n"
    "v_mfma_f32_16x16x32_bf16 v[96:99], a[60:63], v[32:35], v[96:99]\n"
    "v_mfma_f32_16x16x32_bf16 v[100:103], a[140:143], v[32:35], v[100:103]\n"
    "v_mfma_f32_16x16x32_bf16 v[104:107], a[220:223], v[32:35], v[104:107]\n"
    "v_mfma_f32_16x16x32_bf16 v[96:99], a[64:67], v[32:35], v[96:99]\n"
    "v_mfma_f32_16x16x32_bf16 v[100:103], a[144:147], v[32:35], v[100:103]\n"
    "v_mfma_f32_16x16x32_bf16 v[104:107], a[224:227], v[32:35], v[104:107]\n"
    "v_mfma_f32_16x16x32_bf16 v[96:99], a[68:71], v[32:35], v[96:99]\n"
    "v_mfma_f32_16x16x32_bf16 v[100:103], a[148:151], v[32:35], v[100:103]\n"
    "v_mfma_f32_16x16x32_bf16 v[104:107], a[228:231], v[32:35], v[104:107]\n"
    "v_mfma_f32_16x16x32_bf16 v[96:99], a[72:75], v[32:35], v[96:99]\n"
    "v_mfma_f32_16x16x32_bf16 v[100:103], a[152:155], v[32:35], v[100:103]\n"
    "v_mfma_f32_16x16x32_bf16 v[104:107], a[232:235], v[32:35], v[104:107]\n"
    "v_mfma_f32_16x16x32_bf16 v[96:99], a[76:79], v[32:35], v[96:99]\n"
    "v_mfma_f32_16x16x32_bf16 v[100:103], a[156:159], v[32:35], v[100:103]\n"
    "v_mfma_f32_16x16x32_bf16 v[104:107], a[236:239], v[32:35], v[104:107]\n"
    "s_lshl_b32 s35, s18, 13\n"
    "v_add_u32 v8, s35, v228\n"
    "ds_write_b128 v8, v[36:39] offset:40960\n"
    "ds_write_b128 v8, v[36:39] offset:41984\n"
    "ds_write_b128 v8, v[36:39] offset:43008\n"
    "ds_write_b128 v8, v[36:39] offset:44032\n"
    "ds_write_b128 v8, v[36:39] offset:45056\n"
    "ds_write_b128 v8, v[36:39] offset:46080\n"
    "ds_write_b128 v8, v[36:39] offset:47104\n"
    "ds_write_b128 v8, v[36:39] offset:48128\n"
    "s_nop 7\n"
    "s_nop 7\n"
    "s_mov_b64 exec, s[16:17]\n"
    "v_accvgpr_write_b32 a0, v32\n"
    "v_accvgpr_write_b32 a1, v32\n"
    "v_accvgpr_write_b32 a2, v32\n"
    "v_accvgpr_write_b32 a3, v32\n"
    "v_accvgpr_write_b32 a4, v32\n"
    "v_accvgpr_write_b32 a5, v32\n"
    "v_accvgpr_write_b32 a6, v32\n"
    "v_accvgpr_write_b32 a7, v32\n"
    "v_accvgpr_write_b32 a8, v32\n"
    "v_accvgpr_write_b32 a9, v32\n"
    "v_accvgpr_write_b32 a10, v32\n"
    "v_accvgpr_write_b32 a11, v32\n"
    "v_accvgpr_write_b32 a12, v32\n"
    "v_accvgpr_write_b32 a13, v32\n"
    "v_accvgpr_write_b32 a14, v32\n"
    "v_accvgpr_write_b32 a15, v32\n"
    "v_accvgpr_write_b32 a16, v32\n"
    "v_accvgpr_write_b32 a17, v32\n"
    "v_accvgpr_write_b32 a18, v32\n"
    "v_accvgpr_write_b32 a19, v32\n"
    "v_accvgpr_write_b32 a20, v32\n"
    "v_accvgpr_write_b32 a21, v32\n"
    "v_accvgpr_write_b32 a22, v32\n"
    "v_accvgpr_write_b32 a23, v32\n"
    "v_accvgpr_write_b32 a24, v32\n"
    "v_accvgpr_write_b32 a25, v32\n"
    "v_accvgpr_write_b32 a26, v32\n"
    "v_accvgpr_write_b32 a27, v32\n"
    "v_accvgpr_write_b32 a28, v32\n"
    "v_accvgpr_write_b32 a29, v32\n"
    "v_accvgpr_write_b32 a30, v32\n"
    "v_accvgpr_write_b32 a31, v32\n"
    "v_accvgpr_write_b32 a32, v32\n"
    "v_accvgpr_write_b32 a33, v32\n"
    "v_accvgpr_write_b32 a34, v32\n"
    "v_accvgpr_write_b32 a35, v32\n"
    "v_accvgpr_write_b32 a36, v32\n"
    "v_accvgpr_write_b32 a37, v32\n"
    "v_accvgpr_write_b32 a38, v32\n"
    "v_accvgpr_write_b32 a39, v32\n"
    "v_accvgpr_write_b32 a40, v32\n"
    "v_accvgpr_write_b32 a41, v32\n"
    "v_accvgpr_write_b32 a42, v32\n"
    "v_accvgpr_write_b32 a43, v32\n"
    "v_accvgpr_write_b32 a44, v32\n"
    "v_accvgpr_write_b32 a45, v32\n"
    "v_accvgpr_write_b32 a46, v32\n"
    "v_accvgpr_write_b32 a47, v32\n"
    "v_accvgpr_write_b32 a48, v32\n"
    "v_accvgpr_write_b32 a49, v32\n"
    "v_accvgpr_write_b32 a50, v32\n"
    "v_accvgpr_write_b32 a51, v32\n"
    "v_accvgpr_write_b32 a52, v32\n"
    "v_accvgpr_write_b32 a53, v32\n"
    "v_accvgpr_write_b32 a54, v32\n"
    "v_accvgpr_write_b32 a55, v32\n"
    "v_accvgpr_write_b32 a56, v32\n"
    "v_accvgpr_write_b32 a57, v32\n"
    "v_accvgpr_write_b32 a58, v32\n"
    "v_accvgpr_write_b32 a59, v32\n"
    "v_accvgpr_write_b32 a60, v32\n"
    "v_accvgpr_write_b32 a61, v32\n"
    "v_accvgpr_write_b32 a62, v32\n"
    "v_accvgpr_write_b32 a63, v32\n"
    "v_accvgpr_write_b32 a64, v32\n"
    "v_accvgpr_write_b32 a65, v32\n"
    "v_accvgpr_write_b32 a66, v32\n"
    "v_accvgpr_write_b32 a67, v32\n"
    "v_accvgpr_write_b32 a68, v32\n"
    "v_accvgpr_write_b32 a69, v32\n"
    "v_accvgpr_write_b32 a70, v32\n"
    "v_accvgpr_write_b32 a71, v32\n"
    "v_accvgpr_write_b32 a72, v32\n"
    "v_accvgpr_write_b32 a73, v32\n"
    "v_accvgpr_write_b32 a74, v32\n"
    "v_accvgpr_write_b32 a75, v32\n"
    "v_accvgpr_write_b32 a76, v32\n"
    "v_accvgpr_write_b32 a77, v32\n"
    "v_accvgpr_write_b32 a78, v32\n"
    "v_accvgpr_write_b32 a79, v32\n"
    "s_mov_b64 exec, -1\n"
    "s_mov_b32 s30, -2\n"
    "s_mov_b32 s30, 0\n"
    "s_mov_b32 s31, 1\n"
    "s_lshl_b32 s34, s18, 11\n"
    "s_add_u32 s32, s20, s19\n"
    "s_mov_b32 s37, 0x3ea52b5c\n"
    "s_mov_b64 s[38:39], 0\n"
    "s_mov_b64 s[40:41], 0\n"
    "s_mov_b32 s36, 0\n"
    "v_mov_b32 v108, 0\n"
    "s_mov_b32 s42, 0xaaaaaaaa\n"
    "s_mov_b32 s43, 0xaaaaaaaa\n"
    "s_waitcnt lgkmcnt(0)\n"
    ".Lb1_top_%=:\n"
    "s_cmp_lg_u32 s18, 0\n"
    "s_cbranch_scc1 .Lb1_pd3_%=\n"
    "s_add_u32 s29, s26, 280\n"
    "v_min3_i32 v108, v108, v109, v110\n"
    "v_min_i32 v108, v108, v111\n"
    "v_cmp_gt_i32 vcc, s31, v108\n"
    "s_nop 0\n"
    "s_cbranch_vccz .Lb1_pd3_%=\n"
    ".Lb1_rt4_%=:\n"
    "s_memrealtime s[26:27]\n"
    "s_waitcnt lgkmcnt(0)\n"
    "s_sub_u32 s28, s36, s26\n"
    "s_cmp_lt_u32 s28, 600\n"
    "s_cbranch_scc1 .Lb1_rt4_%=\n"
    ".Lb1_po2_%=:\n"
    "buffer_load_dwordx4 v[108:111], v229, s[0:3], s21 offen sc1\n"
    "s_memrealtime s[26:27]\n"
    "s_waitcnt vmcnt(0) lgkmcnt(0)\n"
    "s_add_u32 s29, s26, 280\n"
    "v_min3_i32 v108, v108, v109, v110\n"
    "v_min_i32 v108, v108, v111\n"
    "v_cmp_gt_i32 vcc, s31, v108\n"
    "s_nop 0\n"
    "s_cbranch_vccz .Lb1_pd3_%=\n"
    "s_add_u32 s24, s24, 1\n"
    "s_cmp_lt_u32 s24, 0x40000\n"
    "s_cbranch_scc1 .Lb1_sp5_%=\n"
    "v_mov_b32 v108, 60\n"
    "s_mov_b32 s28, 33728\n"
    "buffer_store_dword v108, off, s[0:3], s28 sc1\n"
    "s_waitcnt vmcnt(0)\n"
    "s_branch .Lb1_pd3_%=\n"
    ".Lb1_sp5_%=:\n"
    "s_sleep 6\n"
    "s_branch .Lb1_po2_%=\n"
    ".Lb1_pd3_%=:\n"
    "s_barrier\n"
    "s_mov_b32 s36, s29\n"
    "s_mov_b32 s33, s32\n"
    "s_add_u32 m0, s34, 0\n"
    "s_nop 0\n"
    "buffer_load_dwordx4 v228, s[0:3], s32 offen sc1 lds\n"
    "buffer_load_dwordx4 v228, s[0:3], s32 offen offset:1024 sc1 lds\n"
    "s_add_u32 m0, s34, 8192\n"
    "s_add_u32 s32, s32, 0x4000\n"
    "buffer_load_dwordx4 v228, s[0:3], s32 offen sc1 lds\n"
    "buffer_load_dwordx4 v228, s[0:3], s32 offen offset:1024 sc1 lds\n"
    "s_add_u32 m0, s34, 16384\n"
    "s_add_u32 s32, s32, 0x4000\n"
    "buffer_load_dwordx4 v228, s[0:3], s32 offen sc1 lds\n"
    "buffer_load_dwordx4 v228, s[0:3], s32 offen offset:1024 sc1 lds\n"
    "s_add_u32 m0, s34, 24576\n"
    "s_add_u32 s32, s32, 0x4000\n"
    "buffer_load_dwordx4 v228, s[0:3], s32 offen sc1 lds\n"
    "buffer_load_dwordx4 v228, s[0:3], s32 offen offset:1024 sc1 lds\n"
    "s_add_u32 m0, s34, 32768\n"
    "s_add_u32 s32, s32, 0x4000\n"
    "buffer_load_dwordx4 v228, s[0:3], s32 offen sc1 lds\n"
    "buffer_load_dwordx4 v228, s[0:3], s32 offen offset:1024 sc1 lds\n"
    "s_waitcnt vmcnt(8)\n"
    "s_barrier\n"
    "ds_read_b128 v[32:35], v224 offset:0\n"
    "ds_read_b128 v[36:39], v224 offset:1024\n"
    "ds_read_b128 v[40:43], v225 offset:0\n"
    "ds_read_b128 v[44:47], v225 offset:1024\n"
    "s_waitcnt lgkmcnt(2)\n"
    "v_mfma_f32_16x16x32_bf16 v[24:27], v[32:35], v[32:35], 0\n"
    "ds_read_b128 v[48:51], v226 offset:0\n"
    "v_mfma_f32_16x16x32_bf16 v[28:31], v[36:39], v[36:39], 0\n"
    "ds_read_b128 v[52:55], v226 offset:1024\n"
    "v_mfma_f32_16x16x32_bf16 v[0:3], a[0:3], v[32:35], 0\n"
    "v_mfma_f32_16x16x32_bf16 v[4:7], a[0:3], v[36:39], 0\n"
    "v_mfma_f32_16x16x32_bf16 v[8:11], a[80:83], v[32:35], 0\n"
    "s_add_u32 s35, s30, 1\n"
    "s_lshl_b32 s32, s35, 19\n"
    "s_lshl_b32 s29, s35, 11\n"
    "v_mfma_f32_16x16x32_bf16 v[12:15], a[80:83], v[36:39], 0\n"
    "s_xor_b32 s29, s29, s19\n"
    "s_and_b32 s29, s29, 0x2000\n"
    "s_add_u32 s32, s32, s29\n"
    "v_mfma_f32_16x16x32_bf16 v[16:19], a[160:163], v[32:35], 0\n"
    "s_add_u32 s32, s32, s20\n"
    "v_xor_b32 v232, 0x800, v232\n"
    "v_mfma_f32_16x16x32_bf16 v[20:23], a[160:163], v[36:39], 0\n"
    "s_waitcnt lgkmcnt(2)\n"
    "v_xor_b32 v233, 0x800, v233\n"
    "v_mfma_f32_16x16x32_bf16 v[0:3], a[4:7], v[40:43], v[0:3]\n"
    "ds_read_b128 v[56:59], v227 offset:0\n"
    "v_xor_b32 v235, 0x800, v235\n"
    "v_mfma_f32_16x16x32_bf16 v[4:7], a[4:7], v[44:47], v[4:7]\n"
    "ds_read_b128 v[60:63], v227 offset:1024\n"
    "v_xor_b32 v236, 0x800, v236\n"
    "v_mfma_f32_16x16x32_bf16 v[8:11], a[84:87], v[40:43], v[8:11]\n"
    "v_xor_b32 v237, 0x800, v237\n"
    "v_xor_b32 v238, 0x800, v238\n"
    "v_mfma_f32_16x16x32_bf16 v[12:15], a[84:87], v[44:47], v[12:15]\n"
    "v_xor_b32 v239, 0x800, v239\n"
    "v_xor_b32 v240, 0x800, v240\n"
    "v_mfma_f32_16x16x32_bf16 v[16:19], a[164:167], v[40:43], v[16:19]\n"
    "v_mov_b32 v116, s31\n"
    "v_mov_b32 v117, s31\n"
    "v_mfma_f32_16x16x32_bf16 v[20:23], a[164:167], v[44:47], v[20:23]\n"
    "s_waitcnt lgkmcnt(2)\n"
    "v_mov_b32 v118, s31\n"
    "v_mfma_f32_16x16x32_bf16 v[0:3], a[8:11], v[48:51], v[0:3]\n"
    "s_waitcnt vmcnt(6)\n"
    "s_barrier\n"
    "v_mfma_f32_16x16x32_bf16 v[4:7], a[8:11], v[52:55], v[4:7]\n"
    "ds_read_b128 v[64:67], v224 offset:8192\n"
    "v_mov_b32 v119, s31\n"
    "v_mfma_f32_16x16x32_bf16 v[8:11], a[88:91], v[48:51], v[8:11]\n"
    "ds_read_b128 v[68:71], v224 offset:9216\n"
    "v_mfma_f32_16x16x32_bf16 v[12:15], a[88:91], v[52:55], v[12:15]\n"
    "v_mfma_f32_16x16x32_bf16 v[16:19], a[168:171], v[48:51], v[16:19]\n"
    "v_mfma_f32_16x16x32_bf16 v[20:23], a[168:171], v[52:55], v[20:23]\n"
    "s_waitcnt lgkmcnt(2)\n"
    "v_mfma_f32_16x16x32_bf16 v[0:3], a[12:15], v[56:59], v[0:3]\n"
    "ds_read_b128 v[72:75], v225 offset:8192\n"
    "v_mfma_f32_16x16x32_bf16 v[4:7], a[12:15], v[60:63], v[4:7]\n"
    "ds_read_b128 v[76:79], v225 offset:9216\n"
    "v_mfma_f32_16x16x32_bf16 v[8:11], a[92:95], v[56:59], v[8:11]\n"
    "v_mfma_f32_16x16x32_bf16 v[12:15], a[92:95], v[60:63], v[12:15]\n"
    "v_mfma_f32_16x16x32_bf16 v[16:19], a[172:175], v[56:59], v[16:19]\n"
    "v_mfma_f32_16x16x32_bf16 v[20:23], a[172:175], v[60:63], v[20:23]\n"
    "s_waitcnt lgkmcnt(2)\n"
    "v_mfma_f32_16x16x32_bf16 v[24:27], v[64:67], v[64:67], v[24:27]\n"
    "ds_read_b128 v[80:83], v226 offset:8192\n"
    "v_mfma_f32_16x16x32_bf16 v[28:31], v[68:71], v[68:71], v[28:31]\n"
    "ds_read_b128 v[84:87], v226 offset:9216\n"
    "v_mfma_f32_16x16x32_bf16 v[0:3], a[16:19], v[64:67], v[0:3]\n"
    "v_mfma_f32_16x16x32_bf16 v[4:7], a[16:19], v[68:71], v[4:7]\n"
    "v_mfma_f32_16x16x32_bf16 v[8:11], a[96:99], v[64:67], v[8:11]\n"
    "v_mfma_f32_16x16x32_bf16 v[12:15], a[96:99], v[68:71], v[12:15]\n"
    "v_mfma_f32_16x16x32_bf16 v[16:19], a[176:179], v[64:67], v[16:19]\n"
    "v_mfma_f32_16x16x32_bf16 v[20:23], a[176:179], v[68:71], v[20:23]\n"
    "s_waitcnt lgkmcnt(2)\n"
    "v_mfma_f32_16x16x32_bf16 v[0:3], a[20:23], v[72:75], v[0:3]\n"
    "ds_read_b128 v[88:91], v227 offset:8192\n"
    "v_mfma_f32_16x16x32_bf16 v[4:7], a[20:23], v[76:79], v[4:7]\n"
    "ds_read_b128 v[92:95], v227 offset:9216\n"
    "v_mfma_f32_16x16x32_bf16 v[8:11], a[100:103], v[72:75], v[8:11]\n"
    "v_mfma_f32_16x16x32_bf16 v[12:15], a[100:103], v[76:79], v[12:15]\n"
    "v_mfma_f32_16x16x32_bf16 v[16:19], a[180:183], v[72:75], v[16:19]\n"
    "v_mfma_f32_16x16x32_bf16 v[20:23], a[180:183], v[76:79], v[20:23]\n"
    "s_waitcnt lgkmcnt(2)\n"
    "v_mfma_f32_16x16x32_bf16 v[0:3], a[24:27], v[80:83], v[0:3]\n"
    "s_waitcnt vmcnt(4)\n"
    "s_barrier\n"
    "v_mfma_f32_16x16x32_bf16 v[4:7], a[24:27], v[84:87], v[4:7]\n"
    "ds_read_b128 v[32:35], v224 offset:16384\n"
    "v_mfma_f32_16x16x32_bf16 v[8:11], a[104:107], v[80:83], v[8:11]\n"
    "ds_read_b128 v[36:39], v224 offset:17408\n"
    "v_mfma_f32_16x16x32_bf16 v[12:15], a[104:107], v[84:87], v[12:15]\n"
    "v_mfma_f32_16x16x32_bf16 v[16:19], a[184:187], v[80:83], v[16:19]\n"
    "v_mfma_f32_16x16x32_bf16 v[20:23], a[184:187], v[84:87], v[20:23]\n"
    "s_waitcnt lgkmcnt(2)\n"
    "v_mfma_f32_16x16x32_bf16 v[0:3], a[28:31], v[88:91], v[0:3]\n"
    "ds_read_b128 v[40:43], v225 offset:16384\n"
    "v_mfma_f32_16x16x32_bf16 v[4:7], a[28:31], v[92:95], v[4:7]\n"
    "ds_read_b128 v[44:47], v225 offset:17408\n"
    "v_mfma_f32_16x16x32_bf16 v[8:11], a[108:111], v[88:91], v[8:11]\n"
    "v_mfma_f32_16x16x32_bf16 v[12:15], a[108:111], v[92:95], v[12:15]\n"
    "v_mfma_f32_16x16x32_bf16 v[16:19], a[188:191], v[88:91], v[16:19]\n"
    "v_mfma_f32_16x16x32_bf16 v[20:23], a[188:191], v[92:95], v[20:23]\n"
    "s_waitcnt lgkmcnt(2)\n"
    "v_mfma_f32_16x16x32_bf16 v[24:27], v[32:35], v[32:35], v[24:27]\n"
    "ds_read_b128 v[48:51], v226 offset:16384\n"
    "v_mfma_f32_16x16x32_bf16 v[28:31], v[36:39], v[36:39], v[28:31]\n"
    "ds_read_b128 v[52:55], v226 offset:17408\n"
    "v_mfma_f32_16x16x32_bf16 v[0:3], a[32:35], v[32:35], v[0:3]\n"
    "v_mfma_f32_16x16x32_bf16 v[4:7], a[32:35], v[36:39], v[4:7]\n"
    "v_mfma_f32_16x16x32_bf16 v[8:11], a[112:115], v[32:35], v[8:11]\n"
    "v_mfma_f32_16x16x32_bf16 v[12:15], a[112:115], v[36:39], v[12:15]\n"
    "v_mfma_f32_16x16x32_bf16 v[16:19], a[192:195], v[32:35], v[16:19]\n"
    "v_mfma_f32_16x16x32_bf16 v[20:23], a[192:195], v[36:39], v[20:23]\n"
    "s_waitcnt lgkmcnt(2)\n"
    "v_mfma_f32_16x16x32_bf16 v[0:3], a[36:39], v[40:43], v[0:3]\n"
    "ds_read_b128 v[56:59], v227 offset:16384\n"
    "v_mfma_f32_16x16x32_bf16 v[4:7], a[36:39], v[44:47], v[4:7]\n"
    "ds_read_b128 v[60:63], v227 offset:17408\n"
    "v_mfma_f32_16x16x32_bf16 v[8:11], a[116:119], v[40:43], v[8:11]\n"
    "v_mfma_f32_16x16x32_bf16 v[12:15], a[116:119], v[44:47], v[12:15]\n"
    "v_mfma_f32_16x16x32_bf16 v[16:19], a[196:199], v[40:43], v[16:19]\n"
    "v_mfma_f32_16x16x32_bf16 v[20:23], a[196:199], v[44:47], v[20:23]\n"
    "s_waitcnt lgkmcnt(2)\n"
    "v_mfma_f32_16x16x32_bf16 v[0:3], a[40:43], v[48:51], v[0:3]\n"
    "s_waitcnt vmcnt(2)\n"
    "s_barrier\n"
    "v_mfma_f32_16x16x32_bf16 v[4:7], a[40:43], v[52:55], v[4:7]\n"
    "ds_read_b128 v[64:67], v224 offset:24576\n"
    "v_mfma_f32_16x16x32_bf16 v[8:11], a[120:123], v[48:51], v[8:11]\n"
    "ds_read_b128 v[68:71], v224 offset:25600\n"
    "v_mfma_f32_16x16x32_bf16 v[12:15], a[120:123], v[52:55], v[12:15]\n"
    "ds_read_b128 v[72:75], v225 offset:24576\n"
    "v_mfma_f32_16x16x32_bf16 v[16:19], a[200:203], v[48:51], v[16:19]\n"
    "ds_read_b128 v[76:79], v225 offset:25600\n"
    "v_mfma_f32_16x16x32_bf16 v[20:23], a[200:203], v[52:55], v[20:23]\n"
    "s_waitcnt lgkmcnt(4)\n"
    "ds_read_b128 v[80:83], v226 offset:24576\n"
    "v_mfma_f32_16x16x32_bf16 v[0:3], a[44:47], v[56:59], v[0:3]\n"
    "ds_read_b128 v[84:87], v226 offset:25600\n"
    "v_mfma_f32_16x16x32_bf16 v[4:7], a[44:47], v[60:63], v[4:7]\n"
    "ds_read_b128 v[88:91], v227 offset:24576\n"
    "v_mfma_f32_16x16x32_bf16 v[8:11], a[124:127], v[56:59], v[8:11]\n"
    "ds_read_b128 v[92:95], v227 offset:25600\n"
    "s_waitcnt vmcnt(0)\n"
    "v_mfma_f32_16x16x32_bf16 v[12:15], a[124:127], v[60:63], v[12:15]\n"
    "s_barrier\n"
    "ds_read_b128 v[32:35], v224 offset:32768\n"
    "v_mfma_f32_16x16x32_bf16 v[16:19], a[204:207], v[56:59], v[16:19]\n"
    "ds_read_b128 v[36:39], v224 offset:33792\n"
    "v_mfma_f32_16x16x32_bf16 v[20:23], a[204:207], v[60:63], v[20:23]\n"
    "s_waitcnt lgkmcnt(8)\n"
    "ds_read_b128 v[40:43], v225 offset:32768\n"
    "v_mfma_f32_16x16x32_bf16 v[24:27], v[64:67], v[64:67], v[24:27]\n"
    "ds_read_b128 v[44:47], v225 offset:33792\n"
    "v_mfma_f32_16x16x32_bf16 v[28:31], v[68:71], v[68:71], v[28:31]\n"
    "ds_read_b128 v[48:51], v226 offset:32768\n"
    "v_mfma_f32_16x16x32_bf16 v[0:3], a[48:51], v[64:67], v[0:3]\n"
    "ds_read_b128 v[52:55], v226 offset:33792\n"
    "v_mfma_f32_16x16x32_bf16 v[4:7], a[48:51], v[68:71], v[4:7]\n"
    "s_waitcnt lgkmcnt(10)\n"
    "ds_read_b128 v[56:59], v227 offset:32768\n"
    "v_mfma_f32_16x16x32_bf16 v[0:3], a[52:55], v[72:75], v[0:3]\n"
    "ds_read_b128 v[60:63], v227 offset:33792\n"
    "v_mfma_f32_16x16x32_bf16 v[4:7], a[52:55], v[76:79], v[4:7]\n"
    "s_waitcnt lgkmcnt(10)\n"
    "v_mfma_f32_16x16x32_bf16 v[0:3], a[56:59], v[80:83], v[0:3]\n"
    "v_mfma_f32_16x16x32_bf16 v[4:7], a[56:59], v[84:87], v[4:7]\n"
    "s_waitcnt lgkmcnt(8)\n"
    "v_mfma_f32_16x16x32_bf16 v[0:3], a[60:63], v[88:91], v[0:3]\n"
    "v_mfma_f32_16x16x32_bf16 v[4:7], a[60:63], v[92:95], v[4:7]\n"
    "s_waitcnt lgkmcnt(6)\n"
    "v_mfma_f32_16x16x32_bf16 v[24:27], v[32:35], v[32:35], v[24:27]\n"
    "v_mfma_f32_16x16x32_bf16 v[28:31], v[36:39], v[36:39], v[28:31]\n"
    "v_mfma_f32_16x16x32_bf16 v[0:3], a[64:67], v[32:35], v[0:3]\n"
    "v_mfma_f32_16x16x32_bf16 v[4:7], a[64:67], v[36:39], v[4:7]\n"
    "s_waitcnt lgkmcnt(4)\n"
    "v_mfma_f32_16x16x32_bf16 v[0:3], a[68:71], v[40:43], v[0:3]\n"
    "v_mfma_f32_16x16x32_bf16 v[4:7], a[68:71], v[44:47], v[4:7]\n"
    "s_waitcnt lgkmcnt(2)\n"
    "v_mfma_f32_16x16x32_bf16 v[0:3], a[72:75], v[48:51], v[0:3]\n"
    "v_mfma_f32_16x16x32_bf16 v[4:7], a[72:75], v[52:55], v[4:7]\n"
    "s_waitcnt lgkmcnt(0)\n"
    "v_mfma_f32_16x16x32_bf16 v[0:3], a[76:79], v[56:59], v[0:3]\n"
    "v_mfma_f32_16x16x32_bf16 v[4:7], a[76:79], v[60:63], v[4:7]\n"
    "ds_write_b128 v241, v[24:27]\n"
    "ds_write_b128 v241, v[28:31] offset:1024\n"
    "v_mfma_f32_16x16x32_bf16 v[8:11], a[128:131], v[64:67], v[8:11]\n"
    "v_mfma_f32_16x16x32_bf16 v[12:15], a[128:131], v[68:71], v[12:15]\n"
    "v_mfma_f32_16x16x32_bf16 v[8:11], a[132:135], v[72:75], v[8:11]\n"
    "v_mfma_f32_16x16x32_bf16 v[12:15], a[132:135], v[76:79], v[12:15]\n"
    "v_mfma_f32_16x16x32_bf16 v[8:11], a[136:139], v[80:83], v[8:11]\n"
    "v_mfma_f32_16x16x32_bf16 v[12:15], a[136:139], v[84:87], v[12:15]\n"
    "ds_write2st64_b32 v243, v0, v4 offset1:1\n"
    "s_waitcnt lgkmcnt(0)\n"
    "v_mfma_f32_16x16x32_bf16 v[8:11], a[140:143], v[88:91], v[8:11]\n"
    "s_barrier\n"
    "ds_read2st64_b32 v[24:25], v242 offset1:8\n"
    "v_mfma_f32_16x16x32_bf16 v[12:15], a[140:143], v[92:95], v[12:15]\n"
    "ds_read2st64_b32 v[26:27], v242 offset0:16 offset1:24\n"
    "ds_read_b32 v108, v244\n"
    "v_mfma_f32_16x16x32_bf16 v[8:11], a[144:147], v[32:35], v[8:11]\n"
    "v_mfma_f32_16x16x32_bf16 v[12:15], a[144:147], v[36:39], v[12:15]\n"
    "v_mfma_f32_16x16x32_bf16 v[8:11], a[148:151], v[40:43], v[8:11]\n"
    "v_mfma_f32_16x16x32_bf16 v[12:15], a[148:151], v[44:47], v[12:15]\n"
    "v_mfma_f32_16x16x32_bf16 v[8:11], a[152:155], v[48:51], v[8:11]\n"
    "s_waitcnt lgkmcnt(0)\n"
    "v_add_f32 v24, v24, v26\n"
    "v_mfma_f32_16x16x32_bf16 v[12:15], a[152:155], v[52:55], v[12:15]\n"
    "v_add_f32 v25, v25, v27\n"
    "v_add_f32 v112, v24, v25\n"
    "v_mfma_f32_16x16x32_bf16 v[8:11], a[156:159], v[56:59], v[8:11]\n"
    "v_mul_f32 v108, 0x3acccccd, v108\n"
    "v_mul_f32 v112, 0x3acccccd, v112\n"
    "v_mfma_f32_16x16x32_bf16 v[12:15], a[156:159], v[60:63], v[12:15]\n"
    "v_fma_f32 v112, -v108, v108, v112\n"
    "v_max_f32 v112, 0, v112\n"
    "v_mfma_f32_16x16x32_bf16 v[16:19], a[208:211], v[64:67], v[16:19]\n"
    "v_cvt_pk_bf16_f32 v108, v108, v112\n"
    "v_and_b32 v112, 0xffff0000, v108\n"
    "v_mfma_f32_16x16x32_bf16 v[20:23], a[208:211], v[68:71], v[20:23]\n"
    "v_lshlrev_b32 v108, 16, v108\n"
    "v_add_f32 v112, 0x3727c5ac, v112\n"
    "v_mfma_f32_16x16x32_bf16 v[16:19], a[212:215], v[72:75], v[16:19]\n"
    "v_mov_b32 v110, v108\n"
    "v_cvt_pk_bf16_f32 v112, v112, v112\n"
    "v_mfma_f32_16x16x32_bf16 v[20:23], a[212:215], v[76:79], v[20:23]\n"
    "v_lshlrev_b32 v112, 16, v112\n"
    "v_mfma_f32_16x16x32_bf16 v[16:19], a[216:219], v[80:83], v[16:19]\n"
    "v_sqrt_f32 v112, v112\n"
    "v_mfma_f32_16x16x32_bf16 v[20:23], a[216:219], v[84:87], v[20:23]\n"
    "v_permlane32_swap_b32 v108, v110\n"
    "v_mfma_f32_16x16x32_bf16 v[16:19], a[220:223], v[88:91], v[16:19]\n"
    "v_cvt_pk_bf16_f32 v112, v112, v112\n"
    "v_lshlrev_b32 v112, 16, v112\n"
    "v_mfma_f32_16x16x32_bf16 v[20:23], a[220:223], v[92:95], v[20:23]\n"
    "v_rcp_f32 v112, v112\n"
    "v_mfma_f32_16x16x32_bf16 v[16:19], a[224:227], v[32:35], v[16:19]\n"
    "v_mov_b32 v114, v112\n"
    "v_mfma_f32_16x16x32_bf16 v[20:23], a[224:227], v[36:39], v[20:23]\n"
    "v_mfma_f32_16x16x32_bf16 v[16:19], a[228:231], v[40:43], v[16:19]\n"
    "v_permlane32_swap_b32 v112, v114\n"
    "v_mfma_f32_16x16x32_bf16 v[20:23], a[228:231], v[44:47], v[20:23]\n"
    "v_fma_f32 v0, v96, -v108, v0\n"
    "v_fma_f32 v1, v97, -v108, v1\n"
    "v_mfma_f32_16x16x32_bf16 v[16:19], a[232:235], v[48:51], v[16:19]\n"
    "v_fma_f32 v2, v98, -v108, v2\n"
    "v_fma_f32 v3, v99, -v108, v3\n"
    "v_mfma_f32_16x16x32_bf16 v[20:23], a[232:235], v[52:55], v[20:23]\n"
    "v_fma_f32 v4, v96, -v110, v4\n"
    "v_fma_f32 v5, v97, -v110, v5\n"
    "v_mfma_f32_16x16x32_bf16 v[16:19], a[236:239], v[56:59], v[16:19]\n"
    "v_fma_f32 v6, v98, -v110, v6\n"
    "v_fma_f32 v7, v99, -v110, v7\n"
    "v_mfma_f32_16x16x32_bf16 v[20:23], a[236:239], v[60:63], v[20:23]\n"
    "v_mul_f32 v0, v0, v112\n"
    "v_mul_f32 v1, v1, v112\n"
    "v_mul_f32 v2, v2, v112\n"
    "v_mul_f32 v3, v3, v112\n"
    "v_mul_f32 v4, v4, v114\n"
    "v_mul_f32 v5, v5, v114\n"
    "v_mul_f32 v6, v6, v114\n"
    "v_mul_f32 v7, v7, v114\n"
    "v_cvt_pk_bf16_f32 v0, v0, v1\n"
    "v_cvt_pk_bf16_f32 v1, v2, v3\n"
    "ds_write_b64 v231, v[0:1] offset:0\n"
    "v_cvt_pk_bf16_f32 v4, v4, v5\n"
    "v_cvt_pk_bf16_f32 v5, v6, v7\n"
    "ds_write_b64 v231, v[4:5] offset:1024\n"
    "v_pk_fma_f32 v[8:9], v[100:101], v[108:109], v[8:9] op_sel_hi:[1,0,1] neg_lo:[0,1,0] neg_hi:[0,1,0]\n"
    "v_pk_fma_f32 v[10:11], v[102:103], v[108:109], v[10:11] op_sel_hi:[1,0,1] neg_lo:[0,1,0] neg_hi:[0,1,0]\n"
    "v_pk_fma_f32 v[12:13], v[100:101], v[110:111], v[12:13] op_sel_hi:[1,0,1] neg_lo:[0,1,0] neg_hi:[0,1,0]\n"
    "v_pk_fma_f32 v[14:15], v[102:103], v[110:111], v[14:15] op_sel_hi:[1,0,1] neg_lo:[0,1,0] neg_hi:[0,1,0]\n"
    "v_pk_mul_f32 v[8:9], v[8:9], v[112:113] op_sel_hi:[1,0]\n"
    "v_pk_mul_f32 v[10:11], v[10:11], v[112:113] op_sel_hi:[1,0]\n"
    "v_pk_mul_f32 v[12:13], v[12:13], v[114:115] op_sel_hi:[1,0]\n"
    "v_pk_mul_f32 v[14:15], v[14:15], v[114:115] op_sel_hi:[1,0]\n"
    "v_cvt_pk_bf16_f32 v8, v8, v9\n"
    "v_cvt_pk_bf16_f32 v9, v10, v11\n"
    "ds_write_b64 v232, v[8:9] offset:0\n"
    "v_cvt_pk_bf16_f32 v12, v12, v13\n"
    "v_cvt_pk_bf16_f32 v13, v14, v15\n"
    "ds_write_b64 v232, v[12:13] offset:1024\n"
    "v_pk_fma_f32 v[16:17], v[104:105], v[108:109], v[16:17] op_sel_hi:[1,0,1] neg_lo:[0,1,0] neg_hi:[0,1,0]\n"
    "v_pk_fma_f32 v[18:19], v[106:107], v[108:109], v[18:19] op_sel_hi:[1,0,1] neg_lo:[0,1,0] neg_hi:[0,1,0]\n"
    "v_pk_fma_f32 v[20:21], v[104:105], v[110:111], v[20:21] op_sel_hi:[1,0,1] neg_lo:[0,1,0] neg_hi:[0,1,0]\n"
    "v_pk_fma_f32 v[22:23], v[106:107], v[110:111], v[22:23] op_sel_hi:[1,0,1] neg_lo:[0,1,0] neg_hi:[0,1,0]\n"
    "v_pk_mul_f32 v[16:17], v[16:17], v[112:113] op_sel_hi:[1,0]\n"
    "v_pk_mul_f32 v[18:19], v[18:19], v[112:113] op_sel_hi:[1,0]\n"
    "v_pk_mul_f32 v[20:21], v[20:21], v[114:115] op_sel_hi:[1,0]\n"
    "v_pk_mul_f32 v[22:23], v[22:23], v[114:115] op_sel_hi:[1,0]\n"
    "v_cvt_pk_bf16_f32 v16, v16, v17\n"
    "v_cvt_pk_bf16_f32 v17, v18, v19\n"
    "ds_write_b64 v233, v[16:17] offset:0\n"
    "v_cvt_pk_bf16_f32 v20, v20, v21\n"
    "v_cvt_pk_bf16_f32 v21, v22, v23\n"
    "ds_write_b64 v233, v[20:21] offset:1024\n"
    "s_waitcnt lgkmcnt(0)\n"
    "s_barrier\n"
    "ds_read_b128 v[32:35], v236 offset:1024\n"
    "ds_read_b128 v[44:47], v234 offset:0\n"
    "ds_read_b128 v[36:39], v235 offset:0\n"
    "ds_read_b128 v[48:51], v234 offset:1024\n"
    "ds_read_b128 v[40:43], v235 offset:1024\n"
    "ds_read_b64_tr_b16 v[52:53], v238 offset:1024\n"
    "ds_read_b64_tr_b16 v[54:55], v237 offset:0\n"
    "ds_read_b64_tr_b16 v[56:57], v237 offset:1024\n"
    "ds_read_b64_tr_b16 v[58:59], v240 offset:1024\n"
    "ds_read_b64_tr_b16 v[60:61], v239 offset:0\n"
    "ds_read_b64_tr_b16 v[62:63], v239 offset:1024\n"
    "s_waitcnt lgkmcnt(9)\n"
    "v_mfma_f32_16x16x32_bf16 v[64:67], v[32:35], v[44:47], 0\n"
    "s_waitcnt lgkmcnt(8)\n"
    "v_mfma_f32_16x16x32_bf16 v[68:71], v[36:39], v[44:47], 0\n"
    "s_waitcnt lgkmcnt(7)\n"
    "v_mfma_f32_16x16x32_bf16 v[72:75], v[36:39], v[48:51], 0\n"
    "s_waitcnt lgkmcnt(6)\n"
    "v_mfma_f32_16x16x32_bf16 v[76:79], v[40:43], v[48:51], 0\n"
    "s_nop 1\n"
    "v_cndmask_b32 v64, v64, v65, s[42:43]\n"
    "v_cndmask_b32 v65, v66, v67, s[42:43]\n"
    "v_cndmask_b32 v66, v68, v69, s[42:43]\n"
    "v_cndmask_b32 v67, v70, v71, s[42:43]\n"
    "v_cndmask_b32 v68, v72, v73, s[42:43]\n"
    "v_cndmask_b32 v69, v74, v75, s[42:43]\n"
    "v_cndmask_b32 v70, v76, v77, s[42:43]\n"
    "v_cndmask_b32 v71, v78, v79, s[42:43]\n"
    "v_cndmask_b32 v64, v249, v64, s[38:39]\n"
    "v_cndmask_b32 v65, v249, v65, s[40:41]\n"
    "v_cndmask_b32 v66, v249, v66, s[12:13]\n"
    "v_cndmask_b32 v67, v249, v67, s[14:15]\n"
    "v_cndmask_b32 v68, v249, v68, s[8:9]\n"
    "v_cndmask_b32 v69, v249, v69, s[10:11]\n"
    "v_cndmask_b32 v70, v249, v70, s[12:13]\n"
    "v_cndmask_b32 v71, v249, v71, s[14:15]\n"
    "v_max3_f32 v72, v64, v65, v66\n"
    "v_max3_f32 v74, v68, v69, v70\n"
    "v_max_f32 v72, v72, v67\n"
    "v_max_f32 v74, v74, v71\n"
    "s_nop 1\n"
    "v_permlane16_swap_b32 v72, v74\n"
    "v_max_f32 v72, v72, v74\n"
    "v_mov_b32 v74, v72\n"
    "s_nop 1\n"
    "v_permlane32_swap_b32 v72, v74\n"
    "v_max_f32 v72, v72, v74\n"
    "v_mul_f32 v72, s37, v72\n"
    "v_mov_b32 v74, v72\n"
    "s_nop 1\n"
    "v_permlane16_swap_b32 v72, v74\n"
    "v_fma_f32 v64, v64, s37, -v72\n"
    "v_fma_f32 v65, v65, s37, -v72\n"
    "v_fma_f32 v66, v66, s37, -v72\n"
    "v_fma_f32 v67, v67, s37, -v72\n"
    "v_fma_f32 v68, v68, s37, -v74\n"
    "v_fma_f32 v69, v69, s37, -v74\n"
    "v_fma_f32 v70, v70, s37, -v74\n"
    "v_fma_f32 v71, v71, s37, -v74\n"
    "v_exp_f32 v64, v64\n"
    "v_exp_f32 v65, v65\n"
    "v_exp_f32 v66, v66\n"
    "v_exp_f32 v67, v67\n"
    "v_exp_f32 v68, v68\n"
    "v_exp_f32 v69, v69\n"
    "v_exp_f32 v70, v70\n"
    "v_exp_f32 v71, v71\n"
    "v_pk_add_f32 v[72:73], v[64:65], v[66:67]\n"
    "v_pk_add_f32 v[74:75], v[68:69], v[70:71]\n"
    "v_add_f32 v72, v72, v73\n"
    "v_add_f32 v74, v74, v75\n"
    "s_nop 1\n"
    "v_permlane16_swap_b32 v72, v74\n"
    "v_add_f32 v72, v72, v74\n"
    "v_mov_b32 v74, v72\n"
    "s_nop 1\n"
    "v_permlane32_swap_b32 v72, v74\n"
    "v_add_f32 v72, v72, v74\n"
    "v_rcp_f32 v72, v72\n"
    "s_nop 0\n"
    "v_mov_b32 v74, v72\n"
    "s_nop 1\n"
    "v_permlane16_swap_b32 v72, v74\n"
    "v_pk_mul_f32 v[64:65], v[64:65], v[72:73] op_sel_hi:[1,0]\n"
    "v_pk_mul_f32 v[66:67], v[66:67], v[72:73] op_sel_hi:[1,0]\n"
    "v_pk_mul_f32 v[68:69], v[68:69], v[74:75] op_sel_hi:[1,0]\n"
    "v_pk_mul_f32 v[70:71], v[70:71], v[74:75] op_sel_hi:[1,0]\n"
    "v_cvt_pk_bf16_f32 v64, v64, v65\n"
    "v_cvt_pk_bf16_f32 v66, v66, v67\n"
    "v_cvt_pk_bf16_f32 v68, v68, v69\n"
    "v_cvt_pk_bf16_f32 v70, v70, v71\n"
    "v_perm_b32 v80, v64, v64, v247\n"
    "v_perm_b32 v81, v64, v64, v248\n"
    "v_perm_b32 v82, v66, v66, v247\n"
    "v_perm_b32 v83, v66, v66, v248\n"
    "v_perm_b32 v84, v68, v68, v247\n"
    "v_perm_b32 v85, v68, v68, v248\n"
    "v_perm_b32 v86, v70, v70, v247\n"
    "v_perm_b32 v87, v70, v70, v248\n"
    "s_waitcnt lgkmcnt(0)\n"
    "v_mfma_f32_16x16x32_bf16 v[0:3], v[52:55], v[80:83], 0\n"
    "v_mfma_f32_16x16x32_bf16 v[4:7], v[54:57], v[84:87], 0\n"
    "v_mfma_f32_16x16x32_bf16 v[8:11], v[58:61], v[80:83], 0\n"
    "v_mfma_f32_16x16x32_bf16 v[12:15], v[60:63], v[84:87], 0\n"
    "s_mov_b64 s[38:39], s[8:9]\n"
    "s_mov_b64 s[40:41], s[10:11]\n"
    "s_nop 2\n"
    "v_cvt_pk_bf16_f32 v16, v0, v1\n"
    "v_cvt_pk_bf16_f32 v17, v2, v3\n"
    "v_cvt_pk_bf16_f32 v18, v4, v5\n"
    "v_cvt_pk_bf16_f32 v19, v6, v7\n"
    "s_nop 0\n"
    "v_permlane16_swap_b32 v16, v18\n"
    "v_permlane16_swap_b32 v17, v19\n"
    "buffer_store_dwordx4 v[16:19], v245, s[0:3], s33 offen sc1\n"
    "v_cvt_pk_bf16_f32 v20, v8, v9\n"
    "v_cvt_pk_bf16_f32 v21, v10, v11\n"
    "v_cvt_pk_bf16_f32 v22, v12, v13\n"
    "v_cvt_pk_bf16_f32 v23, v14, v15\n"
    "buffer_store_dwordx4 v[20:23], v246, s[0:3], s33 offen sc1\n"
    "v_mov_b32 v108, 0\n"
    "s_memrealtime s[26:27]\n"
    "s_cmp_lg_u32 s18, 0\n"
    "s_waitcnt lgkmcnt(0)\n"
    "s_cbranch_scc1 .Lb1_pl6_%=\n"
    "s_sub_u32 s28, s36, s26\n"
    "s_cmp_gt_i32 s28, 0\n"
    "s_cbranch_scc1 .Lb1_pl6_%=\n"
    "buffer_load_dwordx4 v[108:111], v229, s[0:3], s21 offen sc1\n"
    ".Lb1_pl6_%=:\n"
    "s_waitcnt vmcnt(0)\n"
    "s_barrier\n"
    "buffer_store_dwordx4 v[116:119], v230, s[0:3], 0 offen sc1\n"
    "s_mov_b32 s30, s31\n"
    "s_add_u32 s31, s31, 1\n"
    "s_cmp_lt_u32 s30, 64\n"
    "s_cbranch_scc1 .Lb1_top_%=\n"
    "s_waitcnt vmcnt(0) lgkmcnt(0)\n"
    "s_endpgm\n"
    :: "{s[0:3]}"(s0_), "{s[4:7]}"(s4_), "{s[8:11]}"(s8_), "{s[12:15]}"(s12_), "{s[16:19]}"(s16_), "{s[20:23]}"(s20_), "{v[0:3]}"(v0_), "{v[224:227]}"(v224_), "{v[228:231]}"(v228_), "{v[232:235]}"(v232_), "{v[236:239]}"(v236_), "{v[240:243]}"(v240_), "{v[244:247]}"(v244_), "{v[248:251]}"(v248_) : "memory", "v255", "a255", "s95");
    __builtin_unreachable();
}
DEV void b2_role(const MegaArgs& A, const int L, const int c, const int rep) {
    ROLE_HEAD;
    const int b = 4 * c + w;
    const u32x4 ro = L ? u32x4{lo(A.out), hi(A.out), 2048u * 1280u, 0x20000u} : rx;
    if (L == 0) {
#pragma unroll
        for (int k = 0; k < 16; ++k) bst16(rr, ST0 + b * 32768 + (k >> 3) * 16384 + (k & 7) * 1024 + l * 16, ones);
        DRAIN_BARRIER();
        bst4(rr, threadIdx.x == 0 ? xl(Z_ST + c * 128, 0) : OOB, 1u);
    }
    const u32x4 s0_ = {RFL(rx[0]), RFL(rx[1]), RFL(rx[2]), RFL(rx[3])};
    const u32x4 s4_ = {RFL(lo(A.wo + 32 * b)), RFL(hi(A.wo + 32 * b)), RFL((640 * 640 - 32 * b) * 2), RFL(0x20000u)};
    const u32x4 s8_ = {RFL(ro[0]), RFL(ro[1]), RFL(ro[2]), RFL(ro[3])};
    const u32x4 s12_ = {RFL(w), RFL(L << 13), RFL(REC0 + RC_O + w * 2048), RFL(xl(Z_O + rep * 2048, L))};
    const u32x4 s16_ = {RFL(xl(Z_ST, 0)), RFL(ST0), RFL(DBG0 + ((L * 128 + 107 + c) * 4 + w) * 2048), RFL(0u)};
    const u32x4 v0_ = {(unsigned)((0 + (l >> 2)) * 1280 + (l & 3) * 16 - 0), (unsigned)((16 + (l >> 2)) * 1280 + (l & 3) * 16 - 1024), (unsigned)((32 + (l >> 2)) * 1280 + (l & 3) * 16 - 2048), (unsigned)((48 + (l >> 2)) * 1280 + (l & 3) * 16 - 3072)};
    const u32x4 v4_ = {(unsigned)(w * 40960 + (8 * (lq & 1) + 20 * (lq >> 1) + (l16 >> 2)) * 64 + (l & 3) * 16), (unsigned)(w * 40960 + (20 * lq + 16 + (l16 >> 2)) * 64 + (l & 3) * 16), (unsigned)((l < 5 ? l : 0) * 128), (unsigned)(0u)};
    const u32x4 v224_ = {(unsigned)(l * 16), (unsigned)((l < 11 ? l : 0) * 128), (unsigned)(l < 4 && w == 1 ? xl(Z_XN + l * 2048 + c * 128, 0) : OOB), (unsigned)(c * 16384 + l * 16)};
    const u32x4 v228_ = {(unsigned)(L ? (l16 + 0) * 1280 + b * 64 + lq * 16 : RC_X2 + c * 16384 + 0 + l * 16), (unsigned)(L ? (l16 + 16) * 1280 + b * 64 + lq * 16 : RC_X2 + c * 16384 + 1024 + l * 16), (unsigned)(l < 16 ? b * 32768 + l * 16 : OOB), (unsigned)((0 + lq) * 32768 + l16 * 16)};
    const u32x4 v232_ = {(unsigned)((4 + lq) * 32768 + l16 * 16), (unsigned)((8 + lq) * 32768 + l16 * 16), (unsigned)((12 + lq) * 32768 + l16 * 16), (unsigned)((16 + lq) * 32768 + l16 * 16)};
    asm volatile(
    "s_mov_b32 s20, 0\n"
    "s_sleep 35\n"
    "s_sleep 35\n"
    "s_sleep 35\n"
    "s_sleep 35\n"
    "s_sleep 35\n"
    "s_sleep 35\n"
    "s_sleep 35\n"
    "s_sleep 35\n"
    "s_sleep 35\n"
    "s_sleep 35\n"
    "s_sleep 35\n"
    "s_sleep 35\n"
    "s_sleep 35\n"
    "s_sleep 35\n"
    ".Lb2_ho1_%=:\n"
    "s_mul_i32 s28, s12, 40960\n"
    "s_mov_b32 s34, 0\n"
    "s_add_u32 m0, s28, 0\n"
    "s_nop 0\n"
    "buffer_load_dwordx4 v0, s[4:7], s34 offen offset:0 lds\n"
    "buffer_load_dwordx4 v1, s[4:7], s34 offen offset:1024 lds\n"
    "buffer_load_dwordx4 v2, s[4:7], s34 offen offset:2048 lds\n"
    "buffer_load_dwordx4 v3, s[4:7], s34 offen offset:3072 lds\n"
    "s_mov_b32 s34, 81920\n"
    "s_add_u32 m0, s28, 4096\n"
    "s_nop 0\n"
    "buffer_load_dwordx4 v0, s[4:7], s34 offen offset:0 lds\n"
    "buffer_load_dwordx4 v1, s[4:7], s34 offen offset:1024 lds\n"
    "buffer_load_dwordx4 v2, s[4:7], s34 offen offset:2048 lds\n"
    "buffer_load_dwordx4 v3, s[4:7], s34 offen offset:3072 lds\n"
    "s_mov_b32 s34, 163840\n"
    "s_add_u32 m0, s28, 8192\n"
    "s_nop 0\n"
    "buffer_load_dwordx4 v0, s[4:7], s34 offen offset:0 lds\n"
    "buffer_load_dwordx4 v1, s[4:7], s34 offen offset:1024 lds\n"
    "buffer_load_dwordx4 v2, s[4:7], s34 offen offset:2048 lds\n"
    "buffer_load_dwordx4 v3, s[4:7], s34 offen offset:3072 lds\n"
    "s_mov_b32 s34, 245760\n"
    "s_add_u32 m0, s28, 12288\n"
    "s_nop 0\n"
    "buffer_load_dwordx4 v0, s[4:7], s34 offen offset:0 lds\n"
    "buffer_load_dwordx4 v1, s[4:7], s34 offen offset:1024 lds\n"
    "buffer_load_dwordx4 v2, s[4:7], s34 offen offset:2048 lds\n"
    "buffer_load_dwordx4 v3, s[4:7], s34 offen offset:3072 lds\n"
    "s_mov_b32 s34, 327680\n"
    "s_add_u32 m0, s28, 16384\n"
    "s_nop 0\n"
    "buffer_load_dwordx4 v0, s[4:7], s34 offen offset:0 lds\n"
    "buffer_load_dwordx4 v1, s[4:7], s34 offen offset:1024 lds\n"
    "buffer_load_dwordx4 v2, s[4:7], s34 offen offset:2048 lds\n"
    "buffer_load_dwordx4 v3, s[4:7], s34 offen offset:3072 lds\n"
    "s_mov_b32 s34, 409600\n"
    "s_add_u32 m0, s28, 20480\n"
    "s_nop 0\n"
    "buffer_load_dwordx4 v0, s[4:7], s34 offen offset:0 lds\n"
    "buffer_load_dwordx4 v1, s[4:7], s34 offen offset:1024 lds\n"
    "buffer_load_dwordx4 v2, s[4:7], s34 offen offset:2048 lds\n"
    "buffer_load_dwordx4 v3, s[4:7], s34 offen offset:3072 lds\n"
    "s_mov_b32 s34, 491520\n"
    "s_add_u32 m0, s28, 24576\n"
    "s_nop 0\n"
    "buffer_load_dwordx4 v0, s[4:7], s34 offen offset:0 lds\n"
    "buffer_load_dwordx4 v1, s[4:7], s34 offen offset:1024 lds\n"
    "buffer_load_dwordx4 v2, s[4:7], s34 offen offset:2048 lds\n"
    "buffer_load_dwordx4 v3, s[4:7], s34 offen offset:3072 lds\n"
    "s_mov_b32 s34, 573440\n"
    "s_add_u32 m0, s28, 28672\n"
    "s_nop 0\n"
    "buffer_load_dwordx4 v0, s[4:7], s34 offen offset:0 lds\n"
    "buffer_load_dwordx4 v1, s[4:7], s34 offen offset:1024 lds\n"
    "buffer_load_dwordx4 v2, s[4:7], s34 offen offset:2048 lds\n"
    "buffer_load_dwordx4 v3, s[4:7], s34 offen offset:3072 lds\n"
    "s_mov_b32 s34, 655360\n"
    "s_add_u32 m0, s28, 32768\n"
    "s_nop 0\n"
    "buffer_load_dwordx4 v0, s[4:7], s34 offen offset:0 lds\n"
    "buffer_load_dwordx4 v1, s[4:7], s34 offen offset:1024 lds\n"
    "buffer_load_dwordx4 v2, s[4:7], s34 offen offset:2048 lds\n"
    "buffer_load_dwordx4 v3, s[4:7], s34 offen offset:3072 lds\n"
    "s_mov_b32 s34, 737280\n"
    "s_add_u32 m0, s28, 36864\n"
    "s_nop 0\n"
    "buffer_load_dwordx4 v0, s[4:7], s34 offen offset:0 lds\n"
    "buffer_load_dwordx4 v1, s[4:7], s34 offen offset:1024 lds\n"
    "buffer_load_dwordx4 v2, s[4:7], s34 offen offset:2048 lds\n"
    "buffer_load_dwordx4 v3, s[4:7], s34 offen offset:3072 lds\n"
    "s_waitcnt vmcnt(0)\n"
    "ds_read_b64_tr_b16 a[0:1], v4 offset:0\n"
    "ds_read_b64_tr_b16 a[2:3], v4 offset:256\n"
    "ds_read_b64_tr_b16 a[80:81], v4 offset:8\n"
    "ds_read_b64_tr_b16 a[82:83], v4 offset:264\n"
    "s_waitcnt lgkmcnt(0)\n"
    "ds_read_b64_tr_b16 a[4:5], v4 offset:2560\n"
    "ds_read_b64_tr_b16 a[6:7], v4 offset:2816\n"
    "ds_read_b64_tr_b16 a[84:85], v4 offset:2568\n"
    "ds_read_b64_tr_b16 a[86:87], v4 offset:2824\n"
    "s_waitcnt lgkmcnt(0)\n"
    "ds_read_b64_tr_b16 a[8:9], v4 offset:5120\n"
    "ds_read_b64_tr_b16 a[10:11], v4 offset:5376\n"
    "ds_read_b64_tr_b16 a[88:89], v4 offset:5128\n"
    "ds_read_b64_tr_b16 a[90:91], v4 offset:5384\n"
    "s_waitcnt lgkmcnt(0)\n"
    "ds_read_b64_tr_b16 a[12:13], v4 offset:7680\n"
    "ds_read_b64_tr_b16 a[14:15], v4 offset:7936\n"
    "ds_read_b64_tr_b16 a[92:93], v4 offset:7688\n"
    "ds_read_b64_tr_b16 a[94:95], v4 offset:7944\n"
    "s_waitcnt lgkmcnt(0)\n"
    "ds_read_b64_tr_b16 a[16:17], v4 offset:10240\n"
    "ds_read_b64_tr_b16 a[18:19], v4 offset:10496\n"
    "ds_read_b64_tr_b16 a[96:97], v4 offset:10248\n"
    "ds_read_b64_tr_b16 a[98:99], v4 offset:10504\n"
    "s_waitcnt lgkmcnt(0)\n"
    "ds_read_b64_tr_b16 a[20:21], v4 offset:12800\n"
    "ds_read_b64_tr_b16 a[22:23], v4 offset:13056\n"
    "ds_read_b64_tr_b16 a[100:101], v4 offset:12808\n"
    "ds_read_b64_tr_b16 a[102:103], v4 offset:13064\n"
    "s_waitcnt lgkmcnt(0)\n"
    "ds_read_b64_tr_b16 a[24:25], v4 offset:15360\n"
    "ds_read_b64_tr_b16 a[26:27], v4 offset:15616\n"
    "ds_read_b64_tr_b16 a[104:105], v4 offset:15368\n"
    "ds_read_b64_tr_b16 a[106:107], v4 offset:15624\n"
    "s_waitcnt lgkmcnt(0)\n"
    "ds_read_b64_tr_b16 a[28:29], v4 offset:17920\n"
    "ds_read_b64_tr_b16 a[30:31], v4 offset:18176\n"
    "ds_read_b64_tr_b16 a[108:109], v4 offset:17928\n"
    "ds_read_b64_tr_b16 a[110:111], v4 offset:18184\n"
    "s_waitcnt lgkmcnt(0)\n"
    "ds_read_b64_tr_b16 a[32:33], v4 offset:20480\n"
    "ds_read_b64_tr_b16 a[34:35], v4 offset:20736\n"
    "ds_read_b64_tr_b16 a[112:113], v4 offset:20488\n"
    "ds_read_b64_tr_b16 a[114:115], v4 offset:20744\n"
    "s_waitcnt lgkmcnt(0)\n"
    "ds_read_b64_tr_b16 a[36:37], v4 offset:23040\n"
    "ds_read_b64_tr_b16 a[38:39], v4 offset:23296\n"
    "ds_read_b64_tr_b16 a[116:117], v4 offset:23048\n"
    "ds_read_b64_tr_b16 a[118:119], v4 offset:23304\n"
    "s_waitcnt lgkmcnt(0)\n"
    "ds_read_b64_tr_b16 a[40:41], v4 offset:25600\n"
    "ds_read_b64_tr_b16 a[42:43], v4 offset:25856\n"
    "ds_read_b64_tr_b16 a[120:121], v4 offset:25608\n"
    "ds_read_b64_tr_b16 a[122:123], v4 offset:25864\n"
    "s_waitcnt lgkmcnt(0)\n"
    "ds_read_b64_tr_b16 a[44:45], v4 offset:28160\n"
    "ds_read_b64_tr_b16 a[46:47], v4 offset:28416\n"
    "ds_read_b64_tr_b16 a[124:125], v4 offset:28168\n"
    "ds_read_b64_tr_b16 a[126:127], v4 offset:28424\n"
    "s_waitcnt lgkmcnt(0)\n"
    "ds_read_b64_tr_b16 a[48:49], v4 offset:30720\n"
    "ds_read_b64_tr_b16 a[50:51], v4 offset:30976\n"
    "ds_read_b64_tr_b16 a[128:129], v4 offset:30728\n"
    "ds_read_b64_tr_b16 a[130:131], v4 offset:30984\n"
    "s_waitcnt lgkmcnt(0)\n"
    "ds_read_b64_tr_b16 a[52:53], v4 offset:33280\n"
    "ds_read_b64_tr_b16 a[54:55], v4 offset:33536\n"
    "ds_read_b64_tr_b16 a[132:133], v4 offset:33288\n"
    "ds_read_b64_tr_b16 a[134:135], v4 offset:33544\n"
    "s_waitcnt lgkmcnt(0)\n"
    "ds_read_b64_tr_b16 a[56:57], v4 offset:35840\n"
    "ds_read_b64_tr_b16 a[58:59], v4 offset:36096\n"
    "ds_read_b64_tr_b16 a[136:137], v4 offset:35848\n"
    "ds_read_b64_tr_b16 a[138:139], v4 offset:36104\n"
    "s_waitcnt lgkmcnt(0)\n"
    "ds_read_b64_tr_b16 a[60:61], v4 offset:38400\n"
    "ds_read_b64_tr_b16 a[62:63], v4 offset:38656\n"
    "ds_read_b64_tr_b16 a[140:141], v4 offset:38408\n"
    "ds_read_b64_tr_b16 a[142:143], v4 offset:38664\n"
    "s_waitcnt lgkmcnt(0)\n"
    "ds_read_b64_tr_b16 a[64:65], v5 offset:0\n"
    "ds_read_b64_tr_b16 a[66:67], v5 offset:5120\n"
    "ds_read_b64_tr_b16 a[144:145], v5 offset:8\n"
    "ds_read_b64_tr_b16 a[146:147], v5 offset:5128\n"
    "s_waitcnt lgkmcnt(0)\n"
    "ds_read_b64_tr_b16 a[68:69], v5 offset:10240\n"
    "ds_read_b64_tr_b16 a[70:71], v5 offset:15360\n"
    "ds_read_b64_tr_b16 a[148:149], v5 offset:10248\n"
    "ds_read_b64_tr_b16 a[150:151], v5 offset:15368\n"
    "s_waitcnt lgkmcnt(0)\n"
    "ds_read_b64_tr_b16 a[72:73], v5 offset:20480\n"
    "ds_read_b64_tr_b16 a[74:75], v5 offset:25600\n"
    "ds_read_b64_tr_b16 a[152:153], v5 offset:20488\n"
    "ds_read_b64_tr_b16 a[154:155], v5 offset:25608\n"
    "s_waitcnt lgkmcnt(0)\n"
    "ds_read_b64_tr_b16 a[76:77], v5 offset:30720\n"
    "ds_read_b64_tr_b16 a[78:79], v5 offset:35840\n"
    "ds_read_b64_tr_b16 a[156:157], v5 offset:30728\n"
    "ds_read_b64_tr_b16 a[158:159], v5 offset:35848\n"
    "s_waitcnt lgkmcnt(0)\n"
    "s_mov_b32 s26, -2\n"
    ".Lb2_po2_%=:\n"
    "buffer_load_dword v8, v6, s[0:3], s16 offen sc1\n"
    "s_waitcnt vmcnt(0)\n"
    "v_cmp_gt_i32 vcc, 1, v8\n"
    "s_nop 0\n"
    "s_cbranch_vccz .Lb2_pd3_%=\n"
    "s_add_u32 s20, s20, 1\n"
    "s_cmp_lt_u32 s20, 0x40000\n"
    "s_cbranch_scc1 .Lb2_sp4_%=\n"
    "v_mov_b32 v8, 70\n"
    "s_mov_b32 s24, 33728\n"
    "buffer_store_dword v8, off, s[0:3], s24 sc1\n"
    "s_waitcnt vmcnt(0)\n"
    "s_branch .Lb2_pd3_%=\n"
    ".Lb2_sp4_%=:\n"
    "s_sleep 4\n"
    "s_branch .Lb2_po2_%=\n"
    ".Lb2_pd3_%=:\n"
    "s_mov_b32 s26, 0\n"
    "s_lshl_b32 s33, s12, 11\n"
    "s_mov_b32 s35, 0\n"
    "v_mov_b32 v84, 0\n"
    ".Lb2_top_%=:\n"
    "s_lshl_b32 s28, s26, 19\n"
    "s_lshl_b32 s25, s26, 11\n"
    "s_xor_b32 s25, s25, s13\n"
    "s_and_b32 s25, s25, 0x2000\n"
    "s_add_u32 s28, s28, s25\n"
    "s_add_u32 s28, s28, s14\n"
    "s_add_u32 s27, s26, 1\n"
    "s_sub_u32 s29, s28, 81920\n"
    "s_sub_u32 s31, s28, 163840\n"
    "s_xor_b32 s31, s31, 0x2000\n"
    "s_mul_i32 s30, s26, 40960\n"
    "s_cmp_eq_u32 s13, 0\n"
    "s_cselect_b32 s30, s31, s30\n"
    "s_and_b32 s32, s26, 32\n"
    "s_add_u32 s32, s32, s26\n"
    "s_lshl_b32 s32, s32, 8\n"
    "s_add_u32 s32, s32, s17\n"
    "s_cmp_lg_u32 s12, 0\n"
    "s_cbranch_scc1 .Lb2_pd6_%=\n"
    "s_add_u32 s25, s22, 280\n"
    "v_min3_i32 v84, v84, v85, v86\n"
    "v_min_i32 v84, v84, v87\n"
    "v_cmp_gt_i32 vcc, s27, v84\n"
    "s_nop 0\n"
    "s_cbranch_vccz .Lb2_pd6_%=\n"
    ".Lb2_rt7_%=:\n"
    "s_memrealtime s[22:23]\n"
    "s_waitcnt lgkmcnt(0)\n"
    "s_sub_u32 s24, s35, s22\n"
    "s_cmp_lt_u32 s24, 600\n"
    "s_cbranch_scc1 .Lb2_rt7_%=\n"
    ".Lb2_po5_%=:\n"
    "buffer_load_dwordx4 v[84:87], v225, s[0:3], s15 offen sc1\n"
    "s_memrealtime s[22:23]\n"
    "s_waitcnt vmcnt(0) lgkmcnt(0)\n"
    "s_add_u32 s25, s22, 280\n"
    "v_min3_i32 v84, v84, v85, v86\n"
    "v_min_i32 v84, v84, v87\n"
    "v_cmp_gt_i32 vcc, s27, v84\n"
    "s_nop 0\n"
    "s_cbranch_vccz .Lb2_pd6_%=\n"
    "s_add_u32 s20, s20, 1\n"
    "s_cmp_lt_u32 s20, 0x40000\n"
    "s_cbranch_scc1 .Lb2_sp8_%=\n"
    "v_mov_b32 v84, 71\n"
    "s_mov_b32 s24, 33728\n"
    "buffer_store_dword v84, off, s[0:3], s24 sc1\n"
    "s_waitcnt vmcnt(0)\n"
    "s_branch .Lb2_pd6_%=\n"
    ".Lb2_sp8_%=:\n"
    "s_sleep 6\n"
    "s_branch .Lb2_po5_%=\n"
    ".Lb2_pd6_%=:\n"
    "s_barrier\n"
    "s_mov_b32 s35, s25\n"
    "s_add_u32 m0, s33, 0\n"
    "s_nop 0\n"
    "buffer_load_dwordx4 v224, s[0:3], s28 offen sc1 lds\n"
    "buffer_load_dwordx4 v224, s[0:3], s28 offen offset:1024 sc1 lds\n"
    "s_add_u32 m0, s33, 8192\n"
    "s_add_u32 s28, s28, 0x4000\n"
    "buffer_load_dwordx4 v224, s[0:3], s28 offen sc1 lds\n"
    "buffer_load_dwordx4 v224, s[0:3], s28 offen offset:1024 sc1 lds\n"
    "s_add_u32 m0, s33, 16384\n"
    "s_add_u32 s28, s28, 0x4000\n"
    "buffer_load_dwordx4 v224, s[0:3], s28 offen sc1 lds\n"
    "buffer_load_dwordx4 v224, s[0:3], s28 offen offset:1024 sc1 lds\n"
    "s_add_u32 m0, s33, 24576\n"
    "s_add_u32 s28, s28, 0x4000\n"
    "buffer_load_dwordx4 v224, s[0:3], s28 offen sc1 lds\n"
    "buffer_load_dwordx4 v224, s[0:3], s28 offen offset:1024 sc1 lds\n"
    "s_add_u32 m0, s33, 32768\n"
    "s_add_u32 s28, s28, 0x4000\n"
    "buffer_load_dwordx4 v224, s[0:3], s28 offen sc1 lds\n"
    "buffer_load_dwordx4 v224, s[0:3], s28 offen offset:1024 sc1 lds\n"
    "buffer_load_dwordx4 v[48:51], v227, s[0:3], s29 offen offset:0 sc1\n"
    "buffer_load_dwordx4 v[52:55], v227, s[0:3], s29 offen offset:1024 sc1\n"
    "s_waitcnt vmcnt(10)\n"
    "s_barrier\n"
    "ds_read_b128 v[16:19], v224 offset:0\n"
    "ds_read_b128 v[20:23], v224 offset:1024\n"
    "ds_read_b128 v[24:27], v224 offset:2048\n"
    "ds_read_b128 v[28:31], v224 offset:3072\n"
    "ds_read_b128 v[32:35], v224 offset:4096\n"
    "ds_read_b128 v[36:39], v224 offset:5120\n"
    "s_waitcnt lgkmcnt(4)\n"
    "v_mfma_f32_16x16x32_bf16 v[0:3], a[0:3], v[16:19], 0\n"
    "ds_read_b128 v[40:43], v224 offset:6144\n"
    "v_mfma_f32_16x16x32_bf16 v[8:11], a[0:3], v[20:23], 0\n"
    "ds_read_b128 v[44:47], v224 offset:7168\n"
    "v_mfma_f32_16x16x32_bf16 v[4:7], a[80:83], v[16:19], 0\n"
    "v_mfma_f32_16x16x32_bf16 v[12:15], a[80:83], v[20:23], 0\n"
    "s_waitcnt lgkmcnt(4)\n"
    "v_mfma_f32_16x16x32_bf16 v[0:3], a[4:7], v[24:27], v[0:3]\n"
    "s_waitcnt vmcnt(8)\n"
    "s_barrier\n"
    "v_mfma_f32_16x16x32_bf16 v[8:11], a[4:7], v[28:31], v[8:11]\n"
    "ds_read_b128 v[16:19], v224 offset:8192\n"
    "v_mfma_f32_16x16x32_bf16 v[4:7], a[84:87], v[24:27], v[4:7]\n"
    "ds_read_b128 v[20:23], v224 offset:9216\n"
    "v_mfma_f32_16x16x32_bf16 v[12:15], a[84:87], v[28:31], v[12:15]\n"
    "s_waitcnt lgkmcnt(4)\n"
    "v_mfma_f32_16x16x32_bf16 v[0:3], a[8:11], v[32:35], v[0:3]\n"
    "ds_read_b128 v[24:27], v224 offset:10240\n"
    "v_mfma_f32_16x16x32_bf16 v[8:11], a[8:11], v[36:39], v[8:11]\n"
    "ds_read_b128 v[28:31], v224 offset:11264\n"
    "v_mfma_f32_16x16x32_bf16 v[4:7], a[88:91], v[32:35], v[4:7]\n"
    "v_mfma_f32_16x16x32_bf16 v[12:15], a[88:91], v[36:39], v[12:15]\n"
    "s_waitcnt lgkmcnt(4)\n"
    "v_mfma_f32_16x16x32_bf16 v[0:3], a[12:15], v[40:43], v[0:3]\n"
    "ds_read_b128 v[32:35], v224 offset:12288\n"
    "v_mfma_f32_16x16x32_bf16 v[8:11], a[12:15], v[44:47], v[8:11]\n"
    "ds_read_b128 v[36:39], v224 offset:13312\n"
    "v_mfma_f32_16x16x32_bf16 v[4:7], a[92:95], v[40:43], v[4:7]\n"
    "v_mfma_f32_16x16x32_bf16 v[12:15], a[92:95], v[44:47], v[12:15]\n"
    "s_waitcnt lgkmcnt(4)\n"
    "v_mfma_f32_16x16x32_bf16 v[0:3], a[16:19], v[16:19], v[0:3]\n"
    "ds_read_b128 v[40:43], v224 offset:14336\n"
    "v_mfma_f32_16x16x32_bf16 v[8:11], a[16:19], v[20:23], v[8:11]\n"
    "ds_read_b128 v[44:47], v224 offset:15360\n"
    "v_mfma_f32_16x16x32_bf16 v[4:7], a[96:99], v[16:19], v[4:7]\n"
    "v_mfma_f32_16x16x32_bf16 v[12:15], a[96:99], v[20:23], v[12:15]\n"
    "s_waitcnt lgkmcnt(4)\n"
    "v_mfma_f32_16x16x32_bf16 v[0:3], a[20:23], v[24:27], v[0:3]\n"
    "s_waitcnt vmcnt(6)\n"
    "s_barrier\n"
    "v_mfma_f32_16x16x32_bf16 v[8:11], a[20:23], v[28:31], v[8:11]\n"
    "ds_read_b128 v[16:19], v224 offset:16384\n"
    "v_mfma_f32_16x16x32_bf16 v[4:7], a[100:103], v[24:27], v[4:7]\n"
    "ds_read_b128 v[20:23], v224 offset:17408\n"
    "v_mfma_f32_16x16x32_bf16 v[12:15], a[100:103], v[28:31], v[12:15]\n"
    "s_waitcnt lgkmcnt(4)\n"
    "v_mfma_f32_16x16x32_bf16 v[0:3], a[24:27], v[32:35], v[0:3]\n"
    "ds_read_b128 v[24:27], v224 offset:18432\n"
    "v_mfma_f32_16x16x32_bf16 v[8:11], a[24:27], v[36:39], v[8:11]\n"
    "ds_read_b128 v[28:31], v224 offset:19456\n"
    "v_mfma_f32_16x16x32_bf16 v[4:7], a[104:107], v[32:35], v[4:7]\n"
    "v_mfma_f32_16x16x32_bf16 v[12:15], a[104:107], v[36:39], v[12:15]\n"
    "s_waitcnt lgkmcnt(4)\n"
    "v_mfma_f32_16x16x32_bf16 v[0:3], a[28:31], v[40:43], v[0:3]\n"
    "ds_read_b128 v[32:35], v224 offset:20480\n"
    "v_mfma_f32_16x16x32_bf16 v[8:11], a[28:31], v[44:47], v[8:11]\n"
    "ds_read_b128 v[36:39], v224 offset:21504\n"
    "v_mfma_f32_16x16x32_bf16 v[4:7], a[108:111], v[40:43], v[4:7]\n"
    "v_mfma_f32_16x16x32_bf16 v[12:15], a[108:111], v[44:47], v[12:15]\n"
    "s_waitcnt lgkmcnt(4)\n"
    "v_mfma_f32_16x16x32_bf16 v[0:3], a[32:35], v[16:19], v[0:3]\n"
    "ds_read_b128 v[40:43], v224 offset:22528\n"
    "v_mfma_f32_16x16x32_bf16 v[8:11], a[32:35], v[20:23], v[8:11]\n"
    "ds_read_b128 v[44:47], v224 offset:23552\n"
    "v_mfma_f32_16x16x32_bf16 v[4:7], a[112:115], v[16:19], v[4:7]\n"
    "v_mfma_f32_16x16x32_bf16 v[12:15], a[112:115], v[20:23], v[12:15]\n"
    "s_waitcnt lgkmcnt(4)\n"
    "v_mfma_f32_16x16x32_bf16 v[0:3], a[36:39], v[24:27], v[0:3]\n"
    "s_waitcnt vmcnt(4)\n"
    "s_barrier\n"
    "v_mfma_f32_16x16x32_bf16 v[8:11], a[36:39], v[28:31], v[8:11]\n"
    "ds_read_b128 v[16:19], v224 offset:24576\n"
    "v_mfma_f32_16x16x32_bf16 v[4:7], a[116:119], v[24:27], v[4:7]\n"
    "ds_read_b128 v[20:23], v224 offset:25600\n"
    "v_mfma_f32_16x16x32_bf16 v[12:15], a[116:119], v[28:31], v[12:15]\n"
    "s_waitcnt lgkmcnt(4)\n"
    "v_mfma_f32_16x16x32_bf16 v[0:3], a[40:43], v[32:35], v[0:3]\n"
    "ds_read_b128 v[24:27], v224 offset:26624\n"
    "v_mfma_f32_16x16x32_bf16 v[8:11], a[40:43], v[36:39], v[8:11]\n"
    "ds_read_b128 v[28:31], v224 offset:27648\n"
    "v_mfma_f32_16x16x32_bf16 v[4:7], a[120:123], v[32:35], v[4:7]\n"
    "v_mfma_f32_16x16x32_bf16 v[12:15], a[120:123], v[36:39], v[12:15]\n"
    "s_waitcnt lgkmcnt(4)\n"
    "v_mfma_f32_16x16x32_bf16 v[0:3], a[44:47], v[40:43], v[0:3]\n"
    "ds_read_b128 v[32:35], v224 offset:28672\n"
    "v_mfma_f32_16x16x32_bf16 v[8:11], a[44:47], v[44:47], v[8:11]\n"
    "ds_read_b128 v[36:39], v224 offset:29696\n"
    "v_mfma_f32_16x16x32_bf16 v[4:7], a[124:127], v[40:43], v[4:7]\n"
    "v_mfma_f32_16x16x32_bf16 v[12:15], a[124:127], v[44:47], v[12:15]\n"
    "s_waitcnt lgkmcnt(4)\n"
    "v_mfma_f32_16x16x32_bf16 v[0:3], a[48:51], v[16:19], v[0:3]\n"
    "ds_read_b128 v[40:43], v224 offset:30720\n"
    "v_mfma_f32_16x16x32_bf16 v[8:11], a[48:51], v[20:23], v[8:11]\n"
    "ds_read_b128 v[44:47], v224 offset:31744\n"
    "v_mfma_f32_16x16x32_bf16 v[4:7], a[128:131], v[16:19], v[4:7]\n"
    "v_mfma_f32_16x16x32_bf16 v[12:15], a[128:131], v[20:23], v[12:15]\n"
    "s_waitcnt lgkmcnt(4)\n"
    "v_mfma_f32_16x16x32_bf16 v[0:3], a[52:55], v[24:27], v[0:3]\n"
    "s_waitcnt vmcnt(2)\n"
    "s_barrier\n"
    "v_mfma_f32_16x16x32_bf16 v[8:11], a[52:55], v[28:31], v[8:11]\n"
    "ds_read_b128 v[16:19], v224 offset:32768\n"
    "v_mfma_f32_16x16x32_bf16 v[4:7], a[132:135], v[24:27], v[4:7]\n"
    "ds_read_b128 v[20:23], v224 offset:33792\n"
    "v_mfma_f32_16x16x32_bf16 v[12:15], a[132:135], v[28:31], v[12:15]\n"
    "s_waitcnt lgkmcnt(4)\n"
    "v_mfma_f32_16x16x32_bf16 v[0:3], a[56:59], v[32:35], v[0:3]\n"
    "ds_read_b128 v[24:27], v224 offset:34816\n"
    "v_mfma_f32_16x16x32_bf16 v[8:11], a[56:59], v[36:39], v[8:11]\n"
    "ds_read_b128 v[28:31], v224 offset:35840\n"
    "v_mfma_f32_16x16x32_bf16 v[4:7], a[136:139], v[32:35], v[4:7]\n"
    "s_waitcnt vmcnt(0)\n"
    "v_lshlrev_b32 v96, 16, v48\n"
    "v_mfma_f32_16x16x32_bf16 v[12:15], a[136:139], v[36:39], v[12:15]\n"
    "s_waitcnt lgkmcnt(4)\n"
    "v_and_b32 v97, 0xffff0000, v48\n"
    "v_mfma_f32_16x16x32_bf16 v[0:3], a[60:63], v[40:43], v[0:3]\n"
    "s_waitcnt lgkmcnt(2)\n"
    "ds_read_b128 v[32:35], v224 offset:36864\n"
    "v_mfma_f32_16x16x32_bf16 v[8:11], a[60:63], v[44:47], v[8:11]\n"
    "v_swap_b32 v18, v20\n"
    "v_mfma_f32_16x16x32_bf16 v[4:7], a[140:143], v[40:43], v[4:7]\n"
    "v_swap_b32 v19, v21\n"
    "v_mfma_f32_16x16x32_bf16 v[12:15], a[140:143], v[44:47], v[12:15]\n"
    "ds_read_b128 v[36:39], v224 offset:37888\n"
    "v_lshlrev_b32 v98, 16, v49\n"
    "v_mfma_f32_16x16x32_bf16 v[0:3], a[64:67], v[16:19], v[0:3]\n"
    "s_waitcnt lgkmcnt(2)\n"
    "ds_read_b128 v[40:43], v224 offset:38912\n"
    "v_mfma_f32_16x16x32_bf16 v[8:11], a[64:67], v[20:23], v[8:11]\n"
    "v_swap_b32 v26, v28\n"
    "v_mfma_f32_16x16x32_bf16 v[4:7], a[144:147], v[16:19], v[4:7]\n"
    "v_swap_b32 v27, v29\n"
    "v_mfma_f32_16x16x32_bf16 v[12:15], a[144:147], v[20:23], v[12:15]\n"
    "ds_read_b128 v[44:47], v224 offset:39936\n"
    "v_and_b32 v99, 0xffff0000, v49\n"
    "v_mfma_f32_16x16x32_bf16 v[0:3], a[68:71], v[24:27], v[0:3]\n"
    "s_waitcnt lgkmcnt(2)\n"
    "v_lshlrev_b32 v100, 16, v50\n"
    "v_mfma_f32_16x16x32_bf16 v[8:11], a[68:71], v[28:31], v[8:11]\n"
    "v_swap_b32 v34, v36\n"
    "v_mfma_f32_16x16x32_bf16 v[4:7], a[148:151], v[24:27], v[4:7]\n"
    "v_swap_b32 v35, v37\n"
    "v_mfma_f32_16x16x32_bf16 v[12:15], a[148:151], v[28:31], v[12:15]\n"
    "v_and_b32 v101, 0xffff0000, v50\n"
    "v_lshlrev_b32 v102, 16, v51\n"
    "v_mfma_f32_16x16x32_bf16 v[0:3], a[72:75], v[32:35], v[0:3]\n"
    "s_waitcnt lgkmcnt(0)\n"
    "v_and_b32 v103, 0xffff0000, v51\n"
    "v_mfma_f32_16x16x32_bf16 v[8:11], a[72:75], v[36:39], v[8:11]\n"
    "v_swap_b32 v42, v44\n"
    "v_mfma_f32_16x16x32_bf16 v[4:7], a[152:155], v[32:35], v[4:7]\n"
    "v_swap_b32 v43, v45\n"
    "v_mfma_f32_16x16x32_bf16 v[12:15], a[152:155], v[36:39], v[12:15]\n"
    "v_lshlrev_b32 v104, 16, v52\n"
    "v_and_b32 v105, 0xffff0000, v52\n"
    "v_mfma_f32_16x16x32_bf16 v[0:3], a[76:79], v[40:43], v[0:3]\n"
    "v_lshlrev_b32 v106, 16, v53\n"
    "v_and_b32 v107, 0xffff0000, v53\n"
    "v_mfma_f32_16x16x32_bf16 v[8:11], a[76:79], v[44:47], v[8:11]\n"
    "v_lshlrev_b32 v108, 16, v54\n"
    "v_and_b32 v109, 0xffff0000, v54\n"
    "v_mfma_f32_16x16x32_bf16 v[4:7], a[156:159], v[40:43], v[4:7]\n"
    "v_lshlrev_b32 v110, 16, v55\n"
    "v_and_b32 v111, 0xffff0000, v55\n"
    "v_mfma_f32_16x16x32_bf16 v[12:15], a[156:159], v[44:47], v[12:15]\n"
    "v_pk_add_f32 v[0:1], v[0:1], v[96:97]\n"
    "v_pk_add_f32 v[2:3], v[2:3], v[98:99]\n"
    "v_pk_add_f32 v[8:9], v[8:9], v[104:105]\n"
    "v_pk_add_f32 v[10:11], v[10:11], v[106:107]\n"
    "s_nop 0\n"
    "v_pk_add_f32 v[4:5], v[4:5], v[100:101]\n"
    "v_pk_add_f32 v[6:7], v[6:7], v[102:103]\n"
    "s_nop 0\n"
    "v_pk_add_f32 v[12:13], v[12:13], v[108:109]\n"
    "v_pk_add_f32 v[14:15], v[14:15], v[110:111]\n"
    "s_cmp_lg_u32 s13, 0\n"
    "s_cbranch_scc1 .Lb2_out_%=\n"
    "v_pk_add_f32 v[64:65], v[0:1], v[2:3]\n"
    "v_pk_add_f32 v[68:69], v[8:9], v[10:11]\n"
    "v_pk_mul_f32 v[66:67], v[0:1], v[0:1]\n"
    "v_pk_mul_f32 v[70:71], v[8:9], v[8:9]\n"
    "v_pk_add_f32 v[96:97], v[4:5], v[6:7]\n"
    "v_pk_add_f32 v[98:99], v[12:13], v[14:15]\n"
    "v_pk_fma_f32 v[66:67], v[2:3], v[2:3], v[66:67]\n"
    "v_pk_fma_f32 v[70:71], v[10:11], v[10:11], v[70:71]\n"
    "v_pk_add_f32 v[64:65], v[64:65], v[96:97]\n"
    "v_pk_add_f32 v[68:69], v[68:69], v[98:99]\n"
    "v_pk_fma_f32 v[66:67], v[4:5], v[4:5], v[66:67]\n"
    "v_pk_fma_f32 v[70:71], v[12:13], v[12:13], v[70:71]\n"
    "v_add_f32 v64, v64, v65\n"
    "v_add_f32 v68, v68, v69\n"
    "v_pk_fma_f32 v[66:67], v[6:7], v[6:7], v[66:67]\n"
    "v_pk_fma_f32 v[70:71], v[14:15], v[14:15], v[70:71]\n"
    "v_add_f32 v66, v66, v67\n"
    "v_add_f32 v70, v70, v71\n"
    "s_nop 0\n"
    "v_permlane16_swap_b32 v64, v66\n"
    "v_permlane16_swap_b32 v68, v70\n"
    "v_add_f32 v64, v64, v66\n"
    "v_add_f32 v68, v68, v70\n"
    "s_nop 1\n"
    "v_permlane32_swap_b32 v64, v68\n"
    "v_add_f32 v64, v64, v68\n"
    "v_min_u32 v64, -2, v64\n"
    "v_mov_b32 v65, v64\n"
    "v_cvt_pk_bf16_f32 v56, v0, v1\n"
    "v_cvt_pk_bf16_f32 v57, v2, v3\n"
    "v_permlane16_swap_b32 v64, v65\n"
    "v_mov_b32 v66, v64\n"
    "v_mov_b32 v67, v65\n"
    "v_cvt_pk_bf16_f32 v58, v4, v5\n"
    "v_cvt_pk_bf16_f32 v59, v6, v7\n"
    "v_permlane32_swap_b32 v64, v66\n"
    "v_permlane32_swap_b32 v65, v67\n"
    "buffer_store_dwordx4 v[64:67], v230, s[0:3], s32 offen sc1\n"
    "v_cvt_pk_bf16_f32 v60, v8, v9\n"
    "v_cvt_pk_bf16_f32 v61, v10, v11\n"
    "v_cvt_pk_bf16_f32 v62, v12, v13\n"
    "v_cvt_pk_bf16_f32 v63, v14, v15\n"
    "v_lshlrev_b32 v0, 16, v56\n"
    "v_and_b32 v1, 0xffff0000, v56\n"
    "v_lshlrev_b32 v2, 16, v57\n"
    "v_and_b32 v3, 0xffff0000, v57\n"
    "v_lshlrev_b32 v4, 16, v58\n"
    "v_and_b32 v5, 0xffff0000, v58\n"
    "v_lshlrev_b32 v6, 16, v59\n"
    "v_and_b32 v7, 0xffff0000, v59\n"
    "v_lshlrev_b32 v8, 16, v60\n"
    "v_and_b32 v9, 0xffff0000, v60\n"
    "v_lshlrev_b32 v10, 16, v61\n"
    "v_and_b32 v11, 0xffff0000, v61\n"
    "v_lshlrev_b32 v12, 16, v62\n"
    "v_and_b32 v13, 0xffff0000, v62\n"
    "v_lshlrev_b32 v14, 16, v63\n"
    "v_and_b32 v15, 0xffff0000, v63\n"
    "s_sleep 6\n"
    "buffer_load_dwordx4 v[64:67], v231, s[0:3], s32 offen sc1\n"
    "buffer_load_dwordx4 v[68:71], v232, s[0:3], s32 offen sc1\n"
    "buffer_load_dwordx4 v[72:75], v233, s[0:3], s32 offen sc1\n"
    "buffer_load_dwordx4 v[76:79], v234, s[0:3], s32 offen sc1\n"
    "buffer_load_dwordx4 v[80:83], v235, s[0:3], s32 offen sc1\n"
    "buffer_store_dwordx4 v[56:59], v228, s[8:11], s30 offen sc1\n"
    "buffer_store_dwordx4 v[60:63], v229, s[8:11], s30 offen sc1\n"
    ".Lb2_sl_%=:\n"
    "s_waitcnt vmcnt(6)\n"
    "v_max_u32 v84, v64, v64\n"
    "s_waitcnt vmcnt(5)\n"
    "v_max_u32 v84, v68, v84\n"
    "v_pk_add_f32 v[64:65], v[64:65], v[68:69]\n"
    "v_pk_add_f32 v[66:67], v[66:67], v[70:71]\n"
    "s_waitcnt vmcnt(4)\n"
    "v_max_u32 v84, v72, v84\n"
    "v_pk_add_f32 v[64:65], v[64:65], v[72:73]\n"
    "v_pk_add_f32 v[66:67], v[66:67], v[74:75]\n"
    "s_waitcnt vmcnt(3)\n"
    "v_max_u32 v84, v76, v84\n"
    "v_pk_add_f32 v[64:65], v[64:65], v[76:77]\n"
    "v_pk_add_f32 v[66:67], v[66:67], v[78:79]\n"
    "s_waitcnt vmcnt(2)\n"
    "v_max_u32 v84, v80, v84\n"
    "v_pk_add_f32 v[64:65], v[64:65], v[80:81]\n"
    "v_pk_add_f32 v[66:67], v[66:67], v[82:83]\n"
    "v_cmp_eq_u32 vcc, -1, v84\n"
    "s_nop 0\n"
    "s_cbranch_vccnz .Lb2_sr_%=\n"
    ".Lb2_sg_%=:\n"
    "v_permlane16_swap_b32 v64, v65\n"
    "v_permlane16_swap_b32 v66, v67\n"
    "v_add_f32 v64, v64, v65\n"
    "v_add_f32 v66, v66, v67\n"
    "s_nop 1\n"
    "v_permlane32_swap_b32 v64, v66\n"
    "v_add_f32 v64, v64, v66\n"
    "v_mov_b32 v66, v64\n"
    "s_nop 1\n"
    "v_permlane16_swap_b32 v64, v66\n"
    "v_mul_f32 v64, 0x3acccccd, v64\n"
    "v_mul_f32 v66, 0x3acccccd, v66\n"
    "v_fma_f32 v66, -v64, v64, v66\n"
    "v_max_f32 v66, 0, v66\n"
    "v_cvt_pk_bf16_f32 v64, v64, v66\n"
    "v_and_b32 v66, 0xffff0000, v64\n"
    "v_lshlrev_b32 v64, 16, v64\n"
    "v_add_f32 v66, 0x3727c5ac, v66\n"
    "v_mov_b32 v68, v64\n"
    "v_cvt_pk_bf16_f32 v66, v66, v66\n"
    "v_lshlrev_b32 v66, 16, v66\n"
    "v_sqrt_f32 v66, v66\n"
    "v_permlane32_swap_b32 v64, v68\n"
    "v_cvt_pk_bf16_f32 v66, v66, v66\n"
    "v_lshlrev_b32 v66, 16, v66\n"
    "v_rcp_f32 v66, v66\n"
    "s_nop 0\n"
    "v_mov_b32 v70, v66\n"
    "s_nop 1\n"
    "v_permlane32_swap_b32 v66, v70\n"
    "v_mul_f32 v64, v64, v66\n"
    "v_mul_f32 v68, v68, v70\n"
    "v_pk_fma_f32 v[0:1], v[0:1], v[66:67], v[64:65] op_sel_hi:[1,0,0] neg_lo:[0,0,1] neg_hi:[0,0,1]\n"
    "v_pk_fma_f32 v[2:3], v[2:3], v[66:67], v[64:65] op_sel_hi:[1,0,0] neg_lo:[0,0,1] neg_hi:[0,0,1]\n"
    "v_pk_fma_f32 v[4:5], v[4:5], v[66:67], v[64:65] op_sel_hi:[1,0,0] neg_lo:[0,0,1] neg_hi:[0,0,1]\n"
    "v_pk_fma_f32 v[6:7], v[6:7], v[66:67], v[64:65] op_sel_hi:[1,0,0] neg_lo:[0,0,1] neg_hi:[0,0,1]\n"
    "v_cvt_pk_bf16_f32 v96, v0, v1\n"
    "v_cvt_pk_bf16_f32 v98, v2, v3\n"
    "v_cvt_pk_bf16_f32 v97, v4, v5\n"
    "v_cvt_pk_bf16_f32 v99, v6, v7\n"
    "buffer_store_dwordx4 v[96:99], v227, s[0:3], s31 offen offset:0 sc1\n"
    "v_pk_fma_f32 v[8:9], v[8:9], v[70:71], v[68:69] op_sel_hi:[1,0,0] neg_lo:[0,0,1] neg_hi:[0,0,1]\n"
    "v_pk_fma_f32 v[10:11], v[10:11], v[70:71], v[68:69] op_sel_hi:[1,0,0] neg_lo:[0,0,1] neg_hi:[0,0,1]\n"
    "v_pk_fma_f32 v[12:13], v[12:13], v[70:71], v[68:69] op_sel_hi:[1,0,0] neg_lo:[0,0,1] neg_hi:[0,0,1]\n"
    "v_pk_fma_f32 v[14:15], v[14:15], v[70:71], v[68:69] op_sel_hi:[1,0,0] neg_lo:[0,0,1] neg_hi:[0,0,1]\n"
    "v_cvt_pk_bf16_f32 v100, v8, v9\n"
    "v_cvt_pk_bf16_f32 v102, v10, v11\n"
    "v_cvt_pk_bf16_f32 v101, v12, v13\n"
    "v_cvt_pk_bf16_f32 v103, v14, v15\n"
    "buffer_store_dwordx4 v[100:103], v227, s[0:3], s31 offen offset:1024 sc1\n"
    "v_mov_b32 v88, s27\n"
    "v_mov_b32 v89, s27\n"
    "v_mov_b32 v90, s27\n"
    "v_mov_b32 v91, s27\n"
    "v_mov_b32 v84, 0\n"
    "s_memrealtime s[22:23]\n"
    "s_cmp_lg_u32 s12, 0\n"
    "s_waitcnt lgkmcnt(0)\n"
    "s_cbranch_scc1 .Lb2_pl9_%=\n"
    "s_sub_u32 s24, s35, s22\n"
    "s_cmp_gt_i32 s24, 0\n"
    "s_cbranch_scc1 .Lb2_pl9_%=\n"
    "buffer_load_dwordx4 v[84:87], v225, s[0:3], s15 offen sc1\n"
    ".Lb2_pl9_%=:\n"
    "s_waitcnt vmcnt(0)\n"
    "s_barrier\n"
    "buffer_store_dwordx4 v[88:91], v226, s[0:3], 0 offen sc1\n"
    ".Lb2_nx_%=:\n"
    "s_mov_b32 s26, s27\n"
    "s_cmp_lt_u32 s26, 64\n"
    "s_cbranch_scc1 .Lb2_top_%=\n"
    "s_waitcnt vmcnt(0) lgkmcnt(0)\n"
    "s_endpgm\n"
    ".Lb2_out_%=:\n"
    "v_cvt_pk_bf16_f32 v56, v0, v1\n"
    "v_cvt_pk_bf16_f32 v57, v2, v3\n"
    "v_cvt_pk_bf16_f32 v58, v4, v5\n"
    "v_cvt_pk_bf16_f32 v59, v6, v7\n"
    "v_cvt_pk_bf16_f32 v60, v8, v9\n"
    "v_cvt_pk_bf16_f32 v61, v10, v11\n"
    "v_cvt_pk_bf16_f32 v62, v12, v13\n"
    "v_cvt_pk_bf16_f32 v63, v14, v15\n"
    "buffer_store_dwordx4 v[56:59], v228, s[8:11], s30 offen\n"
    "buffer_store_dwordx4 v[60:63], v229, s[8:11], s30 offen\n"
    "v_mov_b32 v84, 0\n"
    "s_branch .Lb2_nx_%=\n"
    ".Lb2_sr_%=:\n"
    "s_add_u32 s20, s20, 1\n"
    "s_cmp_lt_u32 s20, 0x40000\n"
    "s_cbranch_scc1 .Lb2_sp10_%=\n"
    "v_mov_b32 v84, 72\n"
    "s_mov_b32 s24, 33728\n"
    "buffer_store_dword v84, off, s[0:3], s24 sc1\n"
    "s_waitcnt vmcnt(0)\n"
    "s_branch .Lb2_sg_%=\n"
    ".Lb2_sp10_%=:\n"
    "s_sleep 2\n"
    "buffer_load_dwordx4 v[64:67], v231, s[0:3], s32 offen sc1\n"
    "buffer_load_dwordx4 v[68:71], v232, s[0:3], s32 offen sc1\n"
    "buffer_load_dwordx4 v[72:75], v233, s[0:3], s32 offen sc1\n"
    "buffer_load_dwordx4 v[76:79], v234, s[0:3], s32 offen sc1\n"
    "buffer_load_dwordx4 v[80:83], v235, s[0:3], s32 offen sc1\n"
    "s_waitcnt vmcnt(0)\n"
    "s_branch .Lb2_sl_%=\n"
    :: "{s[0:3]}"(s0_), "{s[4:7]}"(s4_), "{s[8:11]}"(s8_), "{s[12:15]}"(s12_), "{s[16:19]}"(s16_), "{v[0:3]}"(v0_), "{v[4:7]}"(v4_), "{v[224:227]}"(v224_), "{v[228:231]}"(v228_), "{v[232:235]}"(v232_) : "memory", "v255", "a255", "s95");
    __builtin_unreachable();
}
constexpr int MEGA_LDS = 163840;
__global__ void __launch_bounds__(256, 1) mega_kernel(MegaArgs A) {
    extern __shared__ __attribute__((aligned(16))) char smem[];
    int* sRole = (int*)smem;
    const int x4 = (int)(__builtin_amdgcn_s_getreg((3 << 11) | 20) & 3u);
    if (threadIdx.x < 64) {
        const rsrc_t rf = mk_rsrc(arena_base(A.ar), ARENA);
        unsigned tm[2] = {~0u, ~0u};
#pragma unroll
        for (int k = 0; k < 4; ++k) {
            const unsigned long long t0 = __builtin_amdgcn_s_memtime();
            if (threadIdx.x == 0) bst4(rf, (blockIdx.x >> 7) * 16384 + (blockIdx.x & 127) * 64 + (k & 1) * 8192, 0u);
            DRAIN();
            const unsigned d = (unsigned)(__builtin_amdgcn_s_memtime() - t0);
            tm[k & 1] = d < tm[k & 1] ? d : tm[k & 1];
        }
        if (threadIdx.x == 0) {
            unsigned* cnt = (unsigned*)(arena_base(A.ar) + xl(Z_TK, 0));
            int role = -1;
            auto take = [&](int pool) {
                if (role >= 0) return;
                const int k = (int)__hip_atomic_fetch_add(cnt + 16 * pool, 1u, RLX_AGENT), x = pool & 3;
                if (k < 32) role = (pool >> 2) * 128 + (k < 24 ? (k >> 3) * 32 + 8 * x + (k & 7) : 96 + ((k - 24) >> 2) * 16 + 4 * x + (k & 3));
            };
            take((tm[1] < tm[0] ? 4 : 0) + x4);
            for (int p = 0; p < 8; ++p) take(p);
            *sRole = role;
        }
    }
    __syncthreads();
    const int role = RFL(*sRole), L = role >> 7, r = role & 127;
    __syncthreads();
    const bool on = (A.layers >> L) & 1;
    if (r < 32) { if (on) qk_role(A, L, r, x4); }
    else if (r < 64) { if (on) vo_role(A, L, r - 32, x4); }
    else if (r < 96) ct_role(A, L, r - 64);
    else if (r < 112) { if (on) { if (r < 107) b1_role(A, L, r - 96, x4); else b2_role(A, L, r - 107, x4); } }
    else qw_role(A, L, 8 * ((r - 112) >> 2) + 4 * L + (r & 3), x4);
}

#define LAYERS 3
extern "C" void kernel_launch(void* const* d_in, const int* in_sizes, int n_in, void* d_out, int out_size, void* d_ws, size_t ws_size, hipStream_t stream) {
    MegaArgs A;
    A.x0 = (const bf16_t*)d_in[0]; A.mem0 = (const bf16_t*)d_in[1]; A.Wq = (const bf16_t*)d_in[2]; A.Wk = (const bf16_t*)d_in[3]; A.Wv = (const bf16_t*)d_in[4];
    A.Wo = (const bf16_t*)d_in[5]; A.wqkv = (const bf16_t*)d_in[8]; A.wo = (const bf16_t*)d_in[9];
    A.ar = (char*)d_ws + 266240;
    A.out = (bf16_t*)d_out; A.layers = LAYERS; A.pad = 0;
    (void)hipFuncSetAttribute((const void*)mega_kernel, hipFuncAttributeMaxDynamicSharedMemorySize, MEGA_LDS);
    k_zero<<<2 * (Z_END - Z_TK) / 4096, 256, 0, stream>>>(A.ar);
    mega_kernel<<<256, 256, MEGA_LDS, stream>>>(A);
}
```

```cpp
#include <hip/hip_runtime.h>
#include <hip/hip_bf16.h>
#include <stdint.h>

typedef unsigned short bf16_t;
typedef short bf16x8 __attribute__((ext_vector_type(8)));
typedef short bf16x4 __attribute__((ext_vector_type(4)));
typedef float f32x4 __attribute__((ext_vector_type(4)));
typedef unsigned u32x4 __attribute__((ext_vector_type(4)));
typedef unsigned u32x2 __attribute__((ext_vector_type(2)));
typedef unsigned long long u64;
typedef __bf16 bf16v2 __attribute__((ext_vector_type(2)));
typedef float f32v2 __attribute__((ext_vector_type(2)));
#define DEV __device__ __forceinline__
DEV unsigned pk2(float a, float b) { f32v2 f = {a, b}; bf16v2 r = __builtin_convertvector(f, bf16v2); return __builtin_bit_cast(unsigned, r); }
DEV float rb(float a) { return __uint_as_float((pk2(a, 0.f) & 0xffffu) << 16); }
DEV float lo16(unsigned u) { return __uint_as_float(u << 16); }
DEV float hi16(unsigned u) { return __uint_as_float(u & 0xffff0000u); }
constexpr int D_ = 640;
#define RLX_AGENT __ATOMIC_RELAXED, __HIP_MEMORY_SCOPE_AGENT
#define AUX_SC1 16
typedef __amdgpu_buffer_rsrc_t rsrc_t;
DEV rsrc_t mk_rsrc(const void* p, unsigned bytes) { return __builtin_amdgcn_make_buffer_rsrc((void*)p, 0, (int)bytes, 0x00020000); }
DEV void bst16(rsrc_t r, unsigned off, u32x4 v) { __builtin_amdgcn_raw_buffer_store_b128(v, r, off, 0, AUX_SC1); }
DEV void bst8(rsrc_t r, unsigned off, u32x2 v) { __builtin_amdgcn_raw_buffer_store_b64(v, r, off, 0, AUX_SC1); }
DEV void bst4(rsrc_t r, unsigned off, unsigned v) { __builtin_amdgcn_raw_buffer_store_b32(v, r, off, 0, AUX_SC1); }
#define DRAIN() asm volatile("s_waitcnt vmcnt(0)" ::: "memory")
#define DRAIN_BARRIER() asm volatile("s_waitcnt vmcnt(0) lgkmcnt(0)\n\ts_barrier" ::: "memory")
#define RFL(x) ((unsigned)__builtin_amdgcn_readfirstlane((int)(x)))
DEV unsigned lo(const void* p) { return (unsigned)(size_t)p; }
DEV unsigned hi(const void* p) { return (unsigned)((size_t)p >> 32) & 0xffffu; }
DEV unsigned nosent(unsigned x) { return min(x, 0xfffffffeu); }
DEV int sw2(int x) { return ((x & 1) << 1) | (x >> 1); }

DEV unsigned xl(unsigned o, int side) { const unsigned ph = ((o >> 13) << 14) | (o & 8191u); return ph | ((((ph >> 21) ^ side) & 1u) << 13); }
constexpr unsigned Z_TK = 16384, Z_QV = Z_TK + 1024, Z_CT = Z_QV + 1024, Z_QW = Z_CT + 2048, Z_X = 3 * 8192, Z_X1 = 7 * 8192, Z_XN = 9 * 8192, Z_O = 10 * 8192, Z_ST = 11 * 8192, Z_END = 12 * 8192;
constexpr unsigned RG_UT = 1u << 21, RG_UTX = RG_UT + (1u << 20), RG_UM = RG_UTX + 262144, RG_UMX = RG_UM + 262144, RG_PT = RG_UMX + 65536, RG_PM = RG_PT + 65536, RG_MEM = RG_PM + 32768;
constexpr unsigned SLOT = 1u << 22;
constexpr unsigned REC0 = 20u << 20, RC_X1 = 5 * 16384, RC_O = 10 * 16384, RC_X2 = 15 * 16384, ST0 = 1u << 20, QT0 = 52u << 20, DBG0 = 116u << 20, ARENA = 120u << 20;
DEV unsigned rec(int s, int L) { return REC0 + (s << 19) + ((((s >> 2) ^ L) & 1) << 13); }
constexpr unsigned OOB = 0x7f000000u, WBYTES = 640u * 6144u * 2u;
DEV char* arena_base(char* p) { return (char*)(((size_t)p + 0x3fffff) & ~(size_t)0x3fffff); }

struct MegaArgs {
    const bf16_t* x0; const bf16_t* mem0; const bf16_t* Wq; const bf16_t* Wk; const bf16_t* Wv; const bf16_t* Wo; const bf16_t* wqkv; const bf16_t* wo;
    char* ar; bf16_t* out; int layers, pad;
};
__global__ void __launch_bounds__(256) k_zero(char* ar) {
    *(u32x4*)(arena_base(ar) + 2 * Z_TK + (blockIdx.x * 256 + threadIdx.x) * 16) = u32x4{0u, 0u, 0u, 0u};
}
#define ROLE_HEAD const int l = threadIdx.x & 63, l16 = l & 15, lq = l >> 4, w = RFL(threadIdx.x >> 6); char* const ab = arena_base(A.ar); \
    const rsrc_t rr = mk_rsrc(ab, ARENA); const u32x4 rx = {lo(ab), hi(ab), ARENA, 0x20000u}; const u32x4 ones = {~0u, ~0u, ~0u, ~0u}; const u32x2 ones2 = {~0u, ~0u}
DEV void qk_role(const MegaArgs& A, const int L, const int h, const int rep) {
    ROLE_HEAD;
#define XP 10
    const int i = 4 * (w & 1) + lq, p = l16 < 8 ? l16 >> 2 : l16 & 1, rho = 4 * (i >> 1) + 2 * p + (i & 1), tp = 4 * w + lq;
    const unsigned pm = (w >= 2 || l16 >= XP) ? OOB : xl(RG_PM + (h >> 3) * 8192 +
        (l16 < 8 ? ((h & 7) >> 1) * 1024 + ((2 * (h & 1) + ((l16 >> 1) & 1)) * 16 + rho) * 16 + (l16 & 1) * 8 : 4096 + (((h & 7) >> 1) * 16 + rho) * 16 + (h & 1) * 8), L);
    const unsigned pt = xl(RG_PT + h * 2048 + tp * 16 + (l16 < 8 ? (l16 & 3) * 256 + (l16 >> 2) * 8 : (l16 & 1) * 8 + (l16 < 10 ? 1792 : (l16 < 14 ? 1024 + ((l16 - 10) >> 1) * 256 : 1536))), L);
    u64 cm[4];
    const int k0 = (l16 >= 4 && l16 < 8) ? 4 * (l16 - 4) : ((l16 >= 10 && l16 < 14) ? 4 * (l16 - 10) : -4);
#pragma unroll
    for (int r = 0; r < 4; ++r) cm[r] = __ballot(k0 + r <= tp);
    bst8(rr, pm, ones2); bst8(rr, pt, ones2);
    DRAIN_BARRIER();
    bst4(rr, threadIdx.x < 4 ? xl(Z_QV + l * 256 + h * 4, L) : OOB, 1u);
    const u32x4 s0_ = {RFL(rx[0]), RFL(rx[1]), RFL(rx[2]), RFL(rx[3])};
    const u32x4 s4_ = {RFL(lo(A.Wk + h * 192)), RFL(hi(A.Wk + h * 192)), RFL(WBYTES - h * 384), RFL(0x20000u)};
    const u32x4 s8_ = {RFL(lo(A.Wq + h * 192)), RFL(hi(A.Wq + h * 192)), RFL(WBYTES - h * 384), RFL(0x20000u)};
    const u32x4 s12_ = {RFL((unsigned)cm[0]), RFL((unsigned)(cm[0] >> 32)), RFL((unsigned)cm[1]), RFL((unsigned)(cm[1] >> 32))};
    const u32x4 s16_ = {RFL((unsigned)cm[2]), RFL((unsigned)(cm[2] >> 32)), RFL((unsigned)cm[3]), RFL((unsigned)(cm[3] >> 32))};
    const u32x4 s20_ = {RFL(w < 2 ? ~0u : 1u), RFL(w < 2 ? ~0u : 0u), RFL(w), RFL(L << 13)};
    const u32x4 s24_ = {RFL(xl(RG_MEM + rep * 16384, L) + w * 2048), RFL(w < 2 ? xl(RG_MEM + rep * 16384 + 8192, L) + w * 1024 - 2048 : OOB), RFL(w * 11264 + 8192), RFL(REC0 + w * 2048)};
    const u32x4 s28_ = {RFL(QT0 + h * 32768 + w * 2048), RFL(h == 0 && w == 0 ? xl(Z_X, L) : OOB), RFL(xl(Z_X, L)), RFL(xl(Z_CT + (h & 7) * 256, L))};
    const u32x4 s32_ = {RFL(xl(Z_XN + rep * 2048, 0)), RFL(xl(Z_QW + h * 128, L)), RFL(DBG0 + ((L * 128 + h) * 4 + w) * 2048), RFL(0u)};
    const u32x4 v0_ = {(unsigned)(((3 * w + 0) * 64 + l) / 24 * 12288 + ((((3 * w + 0) * 64 + l) % 24) ^ ((((3 * w + 0) * 64 + l) / 48) & 6)) * 16 - 0), (unsigned)(((3 * w + 1) * 64 + l) / 24 * 12288 + ((((3 * w + 1) * 64 + l) % 24) ^ ((((3 * w + 1) * 64 + l) / 48) & 6)) * 16 - 1024), (unsigned)(((3 * w + 2) * 64 + l) / 24 * 12288 + ((((3 * w + 2) * 64 + l) % 24) ^ ((((3 * w + 2) * 64 + l) / 48) & 6)) * 16 - 2048), (unsigned)((8 * lq + ((l >> 2) & 1) + ((l >> 1) & 4)) * 384 + (w ^ (((8 * lq + ((l >> 2) & 1) + ((l >> 1) & 4)) >> 2) & 3)) * 32 + (l & 3) * 8 + ((w + 0) & 3) * 12288)};
    const u32x4 v4_ = {(unsigned)((8 * lq + ((l >> 2) & 1) + ((l >> 1) & 4)) * 384 + (w ^ (((8 * lq + ((l >> 2) & 1) + ((l >> 1) & 4)) >> 2) & 3)) * 32 + (l & 3) * 8 + ((w + 1) & 3) * 12288), (unsigned)((8 * lq + ((l >> 2) & 1) + ((l >> 1) & 4)) * 384 + (w ^ (((8 * lq + ((l >> 2) & 1) + ((l >> 1) & 4)) >> 2) & 3)) * 32 + (l & 3) * 8 + ((w + 2) & 3) * 12288), (unsigned)((8 * lq + ((l >> 2) & 1) + ((l >> 1) & 4)) * 384 + (w ^ (((8 * lq + ((l >> 2) & 1) + ((l >> 1) & 4)) >> 2) & 3)) * 32 + (l & 3) * 8 + ((w + 3) & 3) * 12288), (unsigned)((8 * lq + ((l >> 2) & 1) + ((l >> 1) & 4)) * 384 + (w ^ (((8 * lq + ((l >> 2) & 1) + ((l >> 1) & 4)) >> 2) & 3)) * 32 + (l & 3) * 8)};
    const u32x4 v92_ = {(unsigned)(l * 16 + ((w + 0) & 3) * 11264), (unsigned)(l * 16 + ((w + 1) & 3) * 11264), (unsigned)(l * 16 + ((w + 2) & 3) * 11264), (unsigned)(l * 16 + ((w + 3) & 3) * 11264)};
    const u32x4 v96_ = {(unsigned)(l * 16), (unsigned)(l * 16 + 53248 + w * 20480), (unsigned)((l & 2) ? 0xeeeeu : 0x4444u), (unsigned)(0xff800000u)};
    const u32x4 v100_ = {(unsigned)(l16 == 0 + 4 * lq ? 0x3f80u : (l16 == 0 + 4 * lq + 1 ? 0x3f800000u : 0u)), (unsigned)(l16 == 2 + 4 * lq ? 0x3f80u : (l16 == 2 + 4 * lq + 1 ? 0x3f800000u : 0u)), (unsigned)(135168 + w * 1936 + (2 * (l16 >> 2) + (l & 1)) * 240 + (l & 2) * 32 + lq * 16), (unsigned)(135168 + (l16 < 10 ? (4 * (w & 1) + lq) * 240 + l16 * 16 : 1920))};
    const u32x4 v104_ = {(unsigned)(142912 + w * 4352 + l16 * 272 + lq * 16), (unsigned)(142912 + (4 * w + lq) * 272 + l16 * 16), (unsigned)(pm), (unsigned)(pt)};
    asm volatile(
    "s_mov_b32 s36, 0\n"
    "s_cmp_eq_u32 s23, 0\n"
    "s_cbranch_scc1 .Lqk_ho1_%=\n"
    "s_sleep 35\n"
    "s_sleep 35\n"
    "s_sleep 35\n"
    "s_sleep 35\n"
    "s_sleep 35\n"
    "s_sleep 35\n"
    "s_sleep 35\n"
    "s_sleep 35\n"
    "s_sleep 35\n"
    "s_sleep 35\n"
    "s_sleep 35\n"
    "s_sleep 35\n"
    "s_sleep 35\n"
    "s_sleep 35\n"
    ".Lqk_ho1_%=:\n"
    "s_mul_i32 s47, s22, 3072\n"
    "s_mov_b32 s56, 0\n"
    "s_add_u32 m0, s47, 0\n"
    "s_nop 0\n"
    "buffer_load_dwordx4 v0, s[4:7], s56 offen offset:0 nt lds\n"
    "buffer_load_dwordx4 v1, s[4:7], s56 offen offset:1024 nt lds\n"
    "buffer_load_dwordx4 v2, s[4:7], s56 offen offset:2048 nt lds\n"
    "s_mov_b32 s56, 1572864\n"
    "s_add_u32 m0, s47, 12288\n"
    "s_nop 0\n"
    "buffer_load_dwordx4 v0, s[4:7], s56 offen offset:0 nt lds\n"
    "buffer_load_dwordx4 v1, s[4:7], s56 offen offset:1024 nt lds\n"
    "buffer_load_dwordx4 v2, s[4:7], s56 offen offset:2048 nt lds\n"
    "s_mov_b32 s56, 3145728\n"
    "s_add_u32 m0, s47, 24576\n"
    "s_nop 0\n"
    "buffer_load_dwordx4 v0, s[4:7], s56 offen offset:0 nt lds\n"
    "buffer_load_dwordx4 v1, s[4:7], s56 offen offset:1024 nt lds\n"
    "buffer_load_dwordx4 v2, s[4:7], s56 offen offset:2048 nt lds\n"
    "s_mov_b32 s56, 4718592\n"
    "s_add_u32 m0, s47, 36864\n"
    "s_nop 0\n"
    "buffer_load_dwordx4 v0, s[4:7], s56 offen offset:0 nt lds\n"
    "buffer_load_dwordx4 v1, s[4:7], s56 offen offset:1024 nt lds\n"
    "buffer_load_dwordx4 v2, s[4:7], s56 offen offset:2048 nt lds\n"
    "s_mov_b32 s56, 393216\n"
    "s_add_u32 m0, s47, 49152\n"
    "s_nop 0\n"
    "buffer_load_dwordx4 v0, s[4:7], s56 offen offset:0 nt lds\n"
    "buffer_load_dwordx4 v1, s[4:7], s56 offen offset:1024 nt lds\n"
    "buffer_load_dwordx4 v2, s[4:7], s56 offen offset:2048 nt lds\n"
    "s_mov_b32 s56, 1966080\n"
    "s_add_u32 m0, s47, 61440\n"
    "s_nop 0\n"
    "buffer_load_dwordx4 v0, s[4:7], s56 offen offset:0 nt lds\n"
    "buffer_load_dwordx4 v1, s[4:7], s56 offen offset:1024 nt lds\n"
    "buffer_load_dwordx4 v2, s[4:7], s56 offen offset:2048 nt lds\n"
    "s_mov_b32 s56, 3538944\n"
    "s_add_u32 m0, s47, 73728\n"
    "s_nop 0\n"
    "buffer_load_dwordx4 v0, s[4:7], s56 offen offset:0 nt lds\n"
    "buffer_load_dwordx4 v1, s[4:7], s56 offen offset:1024 nt lds\n"
    "buffer_load_dwordx4 v2, s[4:7], s56 offen offset:2048 nt lds\n"
    "s_mov_b32 s56, 5111808\n"
    "s_add_u32 m0, s47, 86016\n"
    "s_nop 0\n"
    "buffer_load_dwordx4 v0, s[4:7], s56 offen offset:0 nt lds\n"
    "buffer_load_dwordx4 v1, s[4:7], s56 offen offset:1024 nt lds\n"
    "buffer_load_dwordx4 v2, s[4:7], s56 offen offset:2048 nt lds\n"
    "s_mov_b32 s56, 786432\n"
    "s_add_u32 m0, s47, 98304\n"
    "s_nop 0\n"
    "buffer_load_dwordx4 v0, s[4:7], s56 offen offset:0 nt lds\n"
    "buffer_load_dwordx4 v1, s[4:7], s56 offen offset:1024 nt lds\n"
    "buffer_load_dwordx4 v2, s[4:7], s56 offen offset:2048 nt lds\n"
    "s_mov_b32 s56, 2359296\n"
    "s_add_u32 m0, s47, 110592\n"
    "s_nop 0\n"
    "buffer_load_dwordx4 v0, s[4:7], s56 offen offset:0 nt lds\n"
    "buffer_load_dwordx4 v1, s[4:7], s56 offen offset:1024 nt lds\n"
    "buffer_load_dwordx4 v2, s[4:7], s56 offen offset:2048 nt lds\n"
    "s_mov_b32 s56, 3932160\n"
    "s_add_u32 m0, s47, 122880\n"
    "s_nop 0\n"
    "buffer_load_dwordx4 v0, s[4:7], s56 offen offset:0 nt lds\n"
    "buffer_load_dwordx4 v1, s[4:7], s56 offen offset:1024 nt lds\n"
    "buffer_load_dwordx4 v2, s[4:7], s56 offen offset:2048 nt lds\n"
    "s_mov_b32 s56, 5505024\n"
    "s_add_u32 m0, s47, 135168\n"
    "s_nop 0\n"
    "buffer_load_dwordx4 v0, s[4:7], s56 offen offset:0 nt lds\n"
    "buffer_load_dwordx4 v1, s[4:7], s56 offen offset:1024 nt lds\n"
    "buffer_load_dwordx4 v2, s[4:7], s56 offen offset:2048 nt lds\n"
    "s_waitcnt vmcnt(24)\n"
    "s_barrier\n"
    "v_add_u32 v8, 0, v3\n"
    "ds_read_b64_tr_b16 a[0:1], v8 offset:0\n"
    "ds_read_b64_tr_b16 a[2:3], v8 offset:768\n"
    "ds_read_b64_tr_b16 a[80:81], v8 offset:128\n"
    "ds_read_b64_tr_b16 a[82:83], v8 offset:896\n"
    "ds_read_b64_tr_b16 a[160:161], v8 offset:256\n"
    "ds_read_b64_tr_b16 a[162:163], v8 offset:1024\n"
    "v_add_u32 v9, 0, v4\n"
    "ds_read_b64_tr_b16 a[16:17], v9 offset:0\n"
    "ds_read_b64_tr_b16 a[18:19], v9 offset:768\n"
    "ds_read_b64_tr_b16 a[96:97], v9 offset:128\n"
    "ds_read_b64_tr_b16 a[98:99], v9 offset:896\n"
    "ds_read_b64_tr_b16 a[176:177], v9 offset:256\n"
    "ds_read_b64_tr_b16 a[178:179], v9 offset:1024\n"
    "v_add_u32 v10, 0, v5\n"
    "ds_read_b64_tr_b16 a[32:33], v10 offset:0\n"
    "ds_read_b64_tr_b16 a[34:35], v10 offset:768\n"
    "ds_read_b64_tr_b16 a[112:113], v10 offset:128\n"
    "ds_read_b64_tr_b16 a[114:115], v10 offset:896\n"
    "ds_read_b64_tr_b16 a[192:193], v10 offset:256\n"
    "ds_read_b64_tr_b16 a[194:195], v10 offset:1024\n"
    "v_add_u32 v11, 0, v6\n"
    "ds_read_b64_tr_b16 a[48:49], v11 offset:0\n"
    "ds_read_b64_tr_b16 a[50:51], v11 offset:768\n"
    "ds_read_b64_tr_b16 a[128:129], v11 offset:128\n"
    "ds_read_b64_tr_b16 a[130:131], v11 offset:896\n"
    "ds_read_b64_tr_b16 a[208:209], v11 offset:256\n"
    "ds_read_b64_tr_b16 a[210:211], v11 offset:1024\n"
    "s_waitcnt lgkmcnt(0)\n"
    "s_barrier\n"
    "s_mov_b32 s56, 1179648\n"
    "s_add_u32 m0, s47, 0\n"
    "s_nop 0\n"
    "buffer_load_dwordx4 v0, s[4:7], s56 offen offset:0 nt lds\n"
    "buffer_load_dwordx4 v1, s[4:7], s56 offen offset:1024 nt lds\n"
    "buffer_load_dwordx4 v2, s[4:7], s56 offen offset:2048 nt lds\n"
    "s_mov_b32 s56, 2752512\n"
    "s_add_u32 m0, s47, 12288\n"
    "s_nop 0\n"
    "buffer_load_dwordx4 v0, s[4:7], s56 offen offset:0 nt lds\n"
    "buffer_load_dwordx4 v1, s[4:7], s56 offen offset:1024 nt lds\n"
    "buffer_load_dwordx4 v2, s[4:7], s56 offen offset:2048 nt lds\n"
    "s_mov_b32 s56, 4325376\n"
    "s_add_u32 m0, s47, 24576\n"
    "s_nop 0\n"
    "buffer_load_dwordx4 v0, s[4:7], s56 offen offset:0 nt lds\n"
    "buffer_load_dwordx4 v1, s[4:7], s56 offen offset:1024 nt lds\n"
    "buffer_load_dwordx4 v2, s[4:7], s56 offen offset:2048 nt lds\n"
    "s_mov_b32 s56, 5898240\n"
    "s_add_u32 m0, s47, 36864\n"
    "s_nop 0\n"
    "buffer_load_dwordx4 v0, s[4:7], s56 offen offset:0 nt lds\n"
    "buffer_load_dwordx4 v1, s[4:7], s56 offen offset:1024 nt lds\n"
    "buffer_load_dwordx4 v2, s[4:7], s56 offen offset:2048 nt lds\n"
    "s_waitcnt vmcnt(24)\n"
    "s_barrier\n"
    "v_add_u32 v8, 49152, v3\n"
    "ds_read_b64_tr_b16 a[4:5], v8 offset:0\n"
    "ds_read_b64_tr_b16 a[6:7], v8 offset:768\n"
    "ds_read_b64_tr_b16 a[84:85], v8 offset:128\n"
    "ds_read_b64_tr_b16 a[86:87], v8 offset:896\n"
    "ds_read_b64_tr_b16 a[164:165], v8 offset:256\n"
    "ds_read_b64_tr_b16 a[166:167], v8 offset:1024\n"
    "v_add_u32 v9, 49152, v4\n"
    "ds_read_b64_tr_b16 a[20:21], v9 offset:0\n"
    "ds_read_b64_tr_b16 a[22:23], v9 offset:768\n"
    "ds_read_b64_tr_b16 a[100:101], v9 offset:128\n"
    "ds_read_b64_tr_b16 a[102:103], v9 offset:896\n"
    "ds_read_b64_tr_b16 a[180:181], v9 offset:256\n"
    "ds_read_b64_tr_b16 a[182:183], v9 offset:1024\n"
    "v_add_u32 v10, 49152, v5\n"
    "ds_read_b64_tr_b16 a[36:37], v10 offset:0\n"
    "ds_read_b64_tr_b16 a[38:39], v10 offset:768\n"
    "ds_read_b64_tr_b16 a[116:117], v10 offset:128\n"
    "ds_read_b64_tr_b16 a[118:119], v10 offset:896\n"
    "ds_read_b64_tr_b16 a[196:197], v10 offset:256\n"
    "ds_read_b64_tr_b16 a[198:199], v10 offset:1024\n"
    "v_add_u32 v11, 49152, v6\n"
    "ds_read_b64_tr_b16 a[52:53], v11 offset:0\n"
    "ds_read_b64_tr_b16 a[54:55], v11 offset:768\n"
    "ds_read_b64_tr_b16 a[132:133], v11 offset:128\n"
    "ds_read_b64_tr_b16 a[134:135], v11 offset:896\n"
    "ds_read_b64_tr_b16 a[212:213], v11 offset:256\n"
    "ds_read_b64_tr_b16 a[214:215], v11 offset:1024\n"
    "s_waitcnt lgkmcnt(0)\n"
    "s_barrier\n"
    "s_mov_b32 s56, 6291456\n"
    "s_add_u32 m0, s47, 49152\n"
    "s_nop 0\n"
    "buffer_load_dwordx4 v0, s[4:7], s56 offen offset:0 nt lds\n"
    "buffer_load_dwordx4 v1, s[4:7], s56 offen offset:1024 nt lds\n"
    "buffer_load_dwordx4 v2, s[4:7], s56 offen offset:2048 nt lds\n"
    "s_mov_b32 s56, 6684672\n"
    "s_add_u32 m0, s47, 61440\n"
    "s_nop 0\n"
    "buffer_load_dwordx4 v0, s[4:7], s56 offen offset:0 nt lds\n"
    "buffer_load_dwordx4 v1, s[4:7], s56 offen offset:1024 nt lds\n"
    "buffer_load_dwordx4 v2, s[4:7], s56 offen offset:2048 nt lds\n"
    "s_mov_b32 s56, 7077888\n"
    "s_add_u32 m0, s47, 73728\n"
    "s_nop 0\n"
    "buffer_load_dwordx4 v0, s[4:7], s56 offen offset:0 nt lds\n"
    "buffer_load_dwordx4 v1, s[4:7], s56 offen offset:1024 nt lds\n"
    "buffer_load_dwordx4 v2, s[4:7], s56 offen offset:2048 nt lds\n"
    "s_mov_b32 s56, 7471104\n"
    "s_add_u32 m0, s47, 86016\n"
    "s_nop 0\n"
    "buffer_load_dwordx4 v0, s[4:7], s56 offen offset:0 nt lds\n"
    "buffer_load_dwordx4 v1, s[4:7], s56 offen offset:1024 nt lds\n"
    "buffer_load_dwordx4 v2, s[4:7], s56 offen offset:2048 nt lds\n"
    "s_waitcnt vmcnt(24)\n"
    "s_barrier\n"
    "v_add_u32 v8, 98304, v3\n"
    "ds_read_b64_tr_b16 a[8:9], v8 offset:0\n"
    "ds_read_b64_tr_b16 a[10:11], v8 offset:768\n"
    "ds_read_b64_tr_b16 a[88:89], v8 offset:128\n"
    "ds_read_b64_tr_b16 a[90:91], v8 offset:896\n"
    "ds_read_b64_tr_b16 a[168:169], v8 offset:256\n"
    "ds_read_b64_tr_b16 a[170:171], v8 offset:1024\n"
    "v_add_u32 v9, 98304, v4\n"
    "ds_read_b64_tr_b16 a[24:25], v9 offset:0\n"
    "ds_read_b64_tr_b16 a[26:27], v9 offset:768\n"
    "ds_read_b64_tr_b16 a[104:105], v9 offset:128\n"
    "ds_read_b64_tr_b16 a[106:107], v9 offset:896\n"
    "ds_read_b64_tr_b16 a[184:185], v9 offset:256\n"
    "ds_read_b64_tr_b16 a[186:187], v9 offset:1024\n"
    "v_add_u32 v10, 98304, v5\n"
    "ds_read_b64_tr_b16 a[40:41], v10 offset:0\n"
    "ds_read_b64_tr_b16 a[42:43], v10 offset:768\n"
    "ds_read_b64_tr_b16 a[120:121], v10 offset:128\n"
    "ds_read_b64_tr_b16 a[122:123], v10 offset:896\n"
    "ds_read_b64_tr_b16 a[200:201], v10 offset:256\n"
    "ds_read_b64_tr_b16 a[202:203], v10 offset:1024\n"
    "v_add_u32 v11, 98304, v6\n"
    "ds_read_b64_tr_b16 a[56:57], v11 offset:0\n"
    "ds_read_b64_tr_b16 a[58:59], v11 offset:768\n"
    "ds_read_b64_tr_b16 a[136:137], v11 offset:128\n"
    "ds_read_b64_tr_b16 a[138:139], v11 offset:896\n"
    "ds_read_b64_tr_b16 a[216:217], v11 offset:256\n"
    "ds_read_b64_tr_b16 a[218:219], v11 offset:1024\n"
    "s_waitcnt lgkmcnt(0)\n"
    "s_barrier\n"
    "s_mov_b32 s56, 0\n"
    "s_add_u32 m0, s47, 98304\n"
    "s_nop 0\n"
    "buffer_load_dwordx4 v0, s[8:11], s56 offen offset:0 nt lds\n"
    "buffer_load_dwordx4 v1, s[8:11], s56 offen offset:1024 nt lds\n"
    "buffer_load_dwordx4 v2, s[8:11], s56 offen offset:2048 nt lds\n"
    "s_mov_b32 s56, 1572864\n"
    "s_add_u32 m0, s47, 110592\n"
    "s_nop 0\n"
    "buffer_load_dwordx4 v0, s[8:11], s56 offen offset:0 nt lds\n"
    "buffer_load_dwordx4 v1, s[8:11], s56 offen offset:1024 nt lds\n"
    "buffer_load_dwordx4 v2, s[8:11], s56 offen offset:2048 nt lds\n"
    "s_mov_b32 s56, 3145728\n"
    "s_add_u32 m0, s47, 122880\n"
    "s_nop 0\n"
    "buffer_load_dwordx4 v0, s[8:11], s56 offen offset:0 nt lds\n"
    "buffer_load_dwordx4 v1, s[8:11], s56 offen offset:1024 nt lds\n"
    "buffer_load_dwordx4 v2, s[8:11], s56 offen offset:2048 nt lds\n"
    "s_mov_b32 s56, 4718592\n"
    "s_add_u32 m0, s47, 135168\n"
    "s_nop 0\n"
    "buffer_load_dwordx4 v0, s[8:11], s56 offen offset:0 nt lds\n"
    "buffer_load_dwordx4 v1, s[8:11], s56 offen offset:1024 nt lds\n"
    "buffer_load_dwordx4 v2, s[8:11], s56 offen offset:2048 nt lds\n"
    "s_waitcnt vmcnt(24)\n"
    "s_barrier\n"
    "v_add_u32 v8, 0, v3\n"
    "ds_read_b64_tr_b16 a[12:13], v8 offset:0\n"
    "ds_read_b64_tr_b16 a[14:15], v8 offset:768\n"
    "ds_read_b64_tr_b16 a[92:93], v8 offset:128\n"
    "ds_read_b64_tr_b16 a[94:95], v8 offset:896\n"
    "ds_read_b64_tr_b16 a[172:173], v8 offset:256\n"
    "ds_read_b64_tr_b16 a[174:175], v8 offset:1024\n"
    "v_add_u32 v9, 0, v4\n"
    "ds_read_b64_tr_b16 a[28:29], v9 offset:0\n"
    "ds_read_b64_tr_b16 a[30:31], v9 offset:768\n"
    "ds_read_b64_tr_b16 a[108:109], v9 offset:128\n"
    "ds_read_b64_tr_b16 a[110:111], v9 offset:896\n"
    "ds_read_b64_tr_b16 a[188:189], v9 offset:256\n"
    "ds_read_b64_tr_b16 a[190:191], v9 offset:1024\n"
    "v_add_u32 v10, 0, v5\n"
    "ds_read_b64_tr_b16 a[44:45], v10 offset:0\n"
    "ds_read_b64_tr_b16 a[46:47], v10 offset:768\n"
    "ds_read_b64_tr_b16 a[124:125], v10 offset:128\n"
    "ds_read_b64_tr_b16 a[126:127], v10 offset:896\n"
    "ds_read_b64_tr_b16 a[204:205], v10 offset:256\n"
    "ds_read_b64_tr_b16 a[206:207], v10 offset:1024\n"
    "v_add_u32 v11, 0, v6\n"
    "ds_read_b64_tr_b16 a[60:61], v11 offset:0\n"
    "ds_read_b64_tr_b16 a[62:63], v11 offset:768\n"
    "ds_read_b64_tr_b16 a[140:141], v11 offset:128\n"
    "ds_read_b64_tr_b16 a[142:143], v11 offset:896\n"
    "ds_read_b64_tr_b16 a[220:221], v11 offset:256\n"
    "ds_read_b64_tr_b16 a[222:223], v11 offset:1024\n"
    "s_waitcnt lgkmcnt(0)\n"
    "s_barrier\n"
    "s_mov_b32 s56, 393216\n"
    "s_add_u32 m0, s47, 0\n"
    "s_nop 0\n"
    "buffer_load_dwordx4 v0, s[8:11], s56 offen offset:0 nt lds\n"
    "buffer_load_dwordx4 v1, s[8:11], s56 offen offset:1024 nt lds\n"
    "buffer_load_dwordx4 v2, s[8:11], s56 offen offset:2048 nt lds\n"
    "s_mov_b32 s56, 1966080\n"
    "s_add_u32 m0, s47, 12288\n"
    "s_nop 0\n"
    "buffer_load_dwordx4 v0, s[8:11], s56 offen offset:0 nt lds\n"
    "buffer_load_dwordx4 v1, s[8:11], s56 offen offset:1024 nt lds\n"
    "buffer_load_dwordx4 v2, s[8:11], s56 offen offset:2048 nt lds\n"
    "s_mov_b32 s56, 3538944\n"
    "s_add_u32 m0, s47, 24576\n"
    "s_nop 0\n"
    "buffer_load_dwordx4 v0, s[8:11], s56 offen offset:0 nt lds\n"
    "buffer_load_dwordx4 v1, s[8:11], s56 offen offset:1024 nt lds\n"
    "buffer_load_dwordx4 v2, s[8:11], s56 offen offset:2048 nt lds\n"
    "s_mov_b32 s56, 5111808\n"
    "s_add_u32 m0, s47, 36864\n"
    "s_nop 0\n"
    "buffer_load_dwordx4 v0, s[8:11], s56 offen offset:0 nt lds\n"
    "buffer_load_dwordx4 v1, s[8:11], s56 offen offset:1024 nt lds\n"
    "buffer_load_dwordx4 v2, s[8:11], s56 offen offset:2048 nt lds\n"
    "s_waitcnt vmcnt(24)\n"
    "s_barrier\n"
    "v_add_u32 v8, 49152, v7\n"
    "ds_read_b64_tr_b16 a[64:65], v8 offset:0\n"
    "ds_read_b64_tr_b16 a[66:67], v8 offset:768\n"
    "ds_read_b64_tr_b16 a[144:145], v8 offset:128\n"
    "ds_read_b64_tr_b16 a[146:147], v8 offset:896\n"
    "ds_read_b64_tr_b16 a[224:225], v8 offset:256\n"
    "ds_read_b64_tr_b16 a[226:227], v8 offset:1024\n"
    "v_add_u32 v9, 61440, v7\n"
    "ds_read_b64_tr_b16 a[68:69], v9 offset:0\n"
    "ds_read_b64_tr_b16 a[70:71], v9 offset:768\n"
    "ds_read_b64_tr_b16 a[148:149], v9 offset:128\n"
    "ds_read_b64_tr_b16 a[150:151], v9 offset:896\n"
    "ds_read_b64_tr_b16 a[228:229], v9 offset:256\n"
    "ds_read_b64_tr_b16 a[230:231], v9 offset:1024\n"
    "v_add_u32 v10, 73728, v7\n"
    "ds_read_b64_tr_b16 a[72:73], v10 offset:0\n"
    "ds_read_b64_tr_b16 a[74:75], v10 offset:768\n"
    "ds_read_b64_tr_b16 a[152:153], v10 offset:128\n"
    "ds_read_b64_tr_b16 a[154:155], v10 offset:896\n"
    "ds_read_b64_tr_b16 a[232:233], v10 offset:256\n"
    "ds_read_b64_tr_b16 a[234:235], v10 offset:1024\n"
    "v_add_u32 v11, 86016, v7\n"
    "ds_read_b64_tr_b16 a[76:77], v11 offset:0\n"
    "ds_read_b64_tr_b16 a[78:79], v11 offset:768\n"
    "ds_read_b64_tr_b16 a[156:157], v11 offset:128\n"
    "ds_read_b64_tr_b16 a[158:159], v11 offset:896\n"
    "ds_read_b64_tr_b16 a[236:237], v11 offset:256\n"
    "ds_read_b64_tr_b16 a[238:239], v11 offset:1024\n"
    "s_waitcnt lgkmcnt(0)\n"
    "s_barrier\n"
    "s_mov_b32 s56, 786432\n"
    "s_add_u32 m0, s47, 49152\n"
    "s_nop 0\n"
    "buffer_load_dwordx4 v0, s[8:11], s56 offen offset:0 nt lds\n"
    "buffer_load_dwordx4 v1, s[8:11], s56 offen offset:1024 nt lds\n"
    "buffer_load_dwordx4 v2, s[8:11], s56 offen offset:2048 nt lds\n"
    "s_mov_b32 s56, 2359296\n"
    "s_add_u32 m0, s47, 61440\n"
    "s_nop 0\n"
    "buffer_load_dwordx4 v0, s[8:11], s56 offen offset:0 nt lds\n"
    "buffer_load_dwordx4 v1, s[8:11], s56 offen offset:1024 nt lds\n"
    "buffer_load_dwordx4 v2, s[8:11], s56 offen offset:2048 nt lds\n"
    "s_mov_b32 s56, 3932160\n"
    "s_add_u32 m0, s47, 73728\n"
    "s_nop 0\n"
    "buffer_load_dwordx4 v0, s[8:11], s56 offen offset:0 nt lds\n"
    "buffer_load_dwordx4 v1, s[8:11], s56 offen offset:1024 nt lds\n"
    "buffer_load_dwordx4 v2, s[8:11], s56 offen offset:2048 nt lds\n"
    "s_mov_b32 s56, 5505024\n"
    "s_add_u32 m0, s47, 86016\n"
    "s_nop 0\n"
    "buffer_load_dwordx4 v0, s[8:11], s56 offen offset:0 nt lds\n"
    "buffer_load_dwordx4 v1, s[8:11], s56 offen offset:1024 nt lds\n"
    "buffer_load_dwordx4 v2, s[8:11], s56 offen offset:2048 nt lds\n"
    "s_waitcnt vmcnt(24)\n"
    "s_barrier\n"
    "v_add_u32 v8, 98304, v3\n"
    "ds_read_b64_tr_b16 a[240:241], v8 offset:0\n"
    "ds_read_b64_tr_b16 a[242:243], v8 offset:768\n"
    "ds_read_b64_tr_b16 v[176:177], v8 offset:128\n"
    "ds_read_b64_tr_b16 v[178:179], v8 offset:896\n"
    "ds_read_b64_tr_b16 v[12:13], v8 offset:256\n"
    "ds_read_b64_tr_b16 v[14:15], v8 offset:1024\n"
    "v_add_u32 v9, 98304, v4\n"
    "ds_read_b64_tr_b16 v[112:113], v9 offset:0\n"
    "ds_read_b64_tr_b16 v[114:115], v9 offset:768\n"
    "ds_read_b64_tr_b16 v[192:193], v9 offset:128\n"
    "ds_read_b64_tr_b16 v[194:195], v9 offset:896\n"
    "ds_read_b64_tr_b16 v[28:29], v9 offset:256\n"
    "ds_read_b64_tr_b16 v[30:31], v9 offset:1024\n"
    "v_add_u32 v10, 98304, v5\n"
    "ds_read_b64_tr_b16 v[128:129], v10 offset:0\n"
    "ds_read_b64_tr_b16 v[130:131], v10 offset:768\n"
    "ds_read_b64_tr_b16 v[208:209], v10 offset:128\n"
    "ds_read_b64_tr_b16 v[210:211], v10 offset:896\n"
    "ds_read_b64_tr_b16 v[44:45], v10 offset:256\n"
    "ds_read_b64_tr_b16 v[46:47], v10 offset:1024\n"
    "v_add_u32 v11, 98304, v6\n"
    "ds_read_b64_tr_b16 v[144:145], v11 offset:0\n"
    "ds_read_b64_tr_b16 v[146:147], v11 offset:768\n"
    "ds_read_b64_tr_b16 v[224:225], v11 offset:128\n"
    "ds_read_b64_tr_b16 v[226:227], v11 offset:896\n"
    "ds_read_b64_tr_b16 v[60:61], v11 offset:256\n"
    "ds_read_b64_tr_b16 v[62:63], v11 offset:1024\n"
    "s_waitcnt lgkmcnt(0)\n"
    "s_barrier\n"
    "s_mov_b32 s56, 1179648\n"
    "s_add_u32 m0, s47, 98304\n"
    "s_nop 0\n"
    "buffer_load_dwordx4 v0, s[8:11], s56 offen offset:0 nt lds\n"
    "buffer_load_dwordx4 v1, s[8:11], s56 offen offset:1024 nt lds\n"
    "buffer_load_dwordx4 v2, s[8:11], s56 offen offset:2048 nt lds\n"
    "s_mov_b32 s56, 2752512\n"
    "s_add_u32 m0, s47, 110592\n"
    "s_nop 0\n"
    "buffer_load_dwordx4 v0, s[8:11], s56 offen offset:0 nt lds\n"
    "buffer_load_dwordx4 v1, s[8:11], s56 offen offset:1024 nt lds\n"
    "buffer_load_dwordx4 v2, s[8:11], s56 offen offset:2048 nt lds\n"
    "s_mov_b32 s56, 4325376\n"
    "s_add_u32 m0, s47, 122880\n"
    "s_nop 0\n"
    "buffer_load_dwordx4 v0, s[8:11], s56 offen offset:0 nt lds\n"
    "buffer_load_dwordx4 v1, s[8:11], s56 offen offset:1024 nt lds\n"
    "buffer_load_dwordx4 v2, s[8:11], s56 offen offset:2048 nt lds\n"
    "s_mov_b32 s56, 5898240\n"
    "s_add_u32 m0, s47, 135168\n"
    "s_nop 0\n"
    "buffer_load_dwordx4 v0, s[8:11], s56 offen offset:0 nt lds\n"
    "buffer_load_dwordx4 v1, s[8:11], s56 offen offset:1024 nt lds\n"
    "buffer_load_dwordx4 v2, s[8:11], s56 offen offset:2048 nt lds\n"
    "s_waitcnt vmcnt(24)\n"
    "s_barrier\n"
    "v_add_u32 v8, 0, v3\n"
    "ds_read_b64_tr_b16 a[244:245], v8 offset:0\n"
    "ds_read_b64_tr_b16 a[246:247], v8 offset:768\n"
    "ds_read_b64_tr_b16 v[180:181], v8 offset:128\n"
    "ds_read_b64_tr_b16 v[182:183], v8 offset:896\n"
    "ds_read_b64_tr_b16 v[16:17], v8 offset:256\n"
    "ds_read_b64_tr_b16 v[18:19], v8 offset:1024\n"
    "v_add_u32 v9, 0, v4\n"
    "ds_read_b64_tr_b16 v[116:117], v9 offset:0\n"
    "ds_read_b64_tr_b16 v[118:119], v9 offset:768\n"
    "ds_read_b64_tr_b16 v[196:197], v9 offset:128\n"
    "ds_read_b64_tr_b16 v[198:199], v9 offset:896\n"
    "ds_read_b64_tr_b16 v[32:33], v9 offset:256\n"
    "ds_read_b64_tr_b16 v[34:35], v9 offset:1024\n"
    "v_add_u32 v10, 0, v5\n"
    "ds_read_b64_tr_b16 v[132:133], v10 offset:0\n"
    "ds_read_b64_tr_b16 v[134:135], v10 offset:768\n"
    "ds_read_b64_tr_b16 v[212:213], v10 offset:128\n"
    "ds_read_b64_tr_b16 v[214:215], v10 offset:896\n"
    "ds_read_b64_tr_b16 v[48:49], v10 offset:256\n"
    "ds_read_b64_tr_b16 v[50:51], v10 offset:1024\n"
    "v_add_u32 v11, 0, v6\n"
    "ds_read_b64_tr_b16 v[148:149], v11 offset:0\n"
    "ds_read_b64_tr_b16 v[150:151], v11 offset:768\n"
    "ds_read_b64_tr_b16 v[228:229], v11 offset:128\n"
    "ds_read_b64_tr_b16 v[230:231], v11 offset:896\n"
    "ds_read_b64_tr_b16 v[64:65], v11 offset:256\n"
    "ds_read_b64_tr_b16 v[66:67], v11 offset:1024\n"
    "s_waitcnt lgkmcnt(0)\n"
    "s_barrier\n"
    "s_mov_b32 s56, 6291456\n"
    "s_add_u32 m0, s47, 0\n"
    "s_nop 0\n"
    "buffer_load_dwordx4 v0, s[8:11], s56 offen offset:0 nt lds\n"
    "buffer_load_dwordx4 v1, s[8:11], s56 offen offset:1024 nt lds\n"
    "buffer_load_dwordx4 v2, s[8:11], s56 offen offset:2048 nt lds\n"
    "s_mov_b32 s56, 6684672\n"
    "s_add_u32 m0, s47, 12288\n"
    "s_nop 0\n"
    "buffer_load_dwordx4 v0, s[8:11], s56 offen offset:0 nt lds\n"
    "buffer_load_dwordx4 v1, s[8:11], s56 offen offset:1024 nt lds\n"
    "buffer_load_dwordx4 v2, s[8:11], s56 offen offset:2048 nt lds\n"
    "s_mov_b32 s56, 7077888\n"
    "s_add_u32 m0, s47, 24576\n"
    "s_nop 0\n"
    "buffer_load_dwordx4 v0, s[8:11], s56 offen offset:0 nt lds\n"
    "buffer_load_dwordx4 v1, s[8:11], s56 offen offset:1024 nt lds\n"
    "buffer_load_dwordx4 v2, s[8:11], s56 offen offset:2048 nt lds\n"
    "s_mov_b32 s56, 7471104\n"
    "s_add_u32 m0, s47, 36864\n"
    "s_nop 0\n"
    "buffer_load_dwordx4 v0, s[8:11], s56 offen offset:0 nt lds\n"
    "buffer_load_dwordx4 v1, s[8:11], s56 offen offset:1024 nt lds\n"
    "buffer_load_dwordx4 v2, s[8:11], s56 offen offset:2048 nt lds\n"
    "s_waitcnt vmcnt(24)\n"
    "s_barrier\n"
    "v_add_u32 v8, 49152, v3\n"
    "ds_read_b64_tr_b16 a[248:249], v8 offset:0\n"
    "ds_read_b64_tr_b16 a[250:251], v8 offset:768\n"
    "ds_read_b64_tr_b16 v[184:185], v8 offset:128\n"
    "ds_read_b64_tr_b16 v[186:187], v8 offset:896\n"
    "ds_read_b64_tr_b16 v[20:21], v8 offset:256\n"
    "ds_read_b64_tr_b16 v[22:23], v8 offset:1024\n"
    "v_add_u32 v9, 49152, v4\n"
    "ds_read_b64_tr_b16 v[120:121], v9 offset:0\n"
    "ds_read_b64_tr_b16 v[122:123], v9 offset:768\n"
    "ds_read_b64_tr_b16 v[200:201], v9 offset:128\n"
    "ds_read_b64_tr_b16 v[202:203], v9 offset:896\n"
    "ds_read_b64_tr_b16 v[36:37], v9 offset:256\n"
    "ds_read_b64_tr_b16 v[38:39], v9 offset:1024\n"
    "v_add_u32 v10, 49152, v5\n"
    "ds_read_b64_tr_b16 v[136:137], v10 offset:0\n"
    "ds_read_b64_tr_b16 v[138:139], v10 offset:768\n"
    "ds_read_b64_tr_b16 v[216:217], v10 offset:128\n"
    "ds_read_b64_tr_b16 v[218:219], v10 offset:896\n"
    "ds_read_b64_tr_b16 v[52:53], v10 offset:256\n"
    "ds_read_b64_tr_b16 v[54:55], v10 offset:1024\n"
    "v_add_u32 v11, 49152, v6\n"
    "ds_read_b64_tr_b16 v[152:153], v11 offset:0\n"
    "ds_read_b64_tr_b16 v[154:155], v11 offset:768\n"
    "ds_read_b64_tr_b16 v[232:233], v11 offset:128\n"
    "ds_read_b64_tr_b16 v[234:235], v11 offset:896\n"
    "ds_read_b64_tr_b16 v[68:69], v11 offset:256\n"
    "ds_read_b64_tr_b16 v[70:71], v11 offset:1024\n"
    "s_waitcnt lgkmcnt(0)\n"
    "s_barrier\n"
    "s_waitcnt vmcnt(12)\n"
    "s_barrier\n"
    "v_add_u32 v8, 98304, v3\n"
    "ds_read_b64_tr_b16 a[252:253], v8 offset:0\n"
    "ds_read_b64_tr_b16 a[254:255], v8 offset:768\n"
    "ds_read_b64_tr_b16 v[188:189], v8 offset:128\n"
    "ds_read_b64_tr_b16 v[190:191], v8 offset:896\n"
    "ds_read_b64_tr_b16 v[24:25], v8 offset:256\n"
    "ds_read_b64_tr_b16 v[26:27], v8 offset:1024\n"
    "v_add_u32 v9, 98304, v4\n"
    "ds_read_b64_tr_b16 v[124:125], v9 offset:0\n"
    "ds_read_b64_tr_b16 v[126:127], v9 offset:768\n"
    "ds_read_b64_tr_b16 v[204:205], v9 offset:128\n"
    "ds_read_b64_tr_b16 v[206:207], v9 offset:896\n"
    "ds_read_b64_tr_b16 v[40:41], v9 offset:256\n"
    "ds_read_b64_tr_b16 v[42:43], v9 offset:1024\n"
    "v_add_u32 v10, 98304, v5\n"
    "ds_read_b64_tr_b16 v[140:141], v10 offset:0\n"
    "ds_read_b64_tr_b16 v[142:143], v10 offset:768\n"
    "ds_read_b64_tr_b16 v[220:221], v10 offset:128\n"
    "ds_read_b64_tr_b16 v[222:223], v10 offset:896\n"
    "ds_read_b64_tr_b16 v[56:57], v10 offset:256\n"
    "ds_read_b64_tr_b16 v[58:59], v10 offset:1024\n"
    "v_add_u32 v11, 98304, v6\n"
    "ds_read_b64_tr_b16 v[156:157], v11 offset:0\n"
    "ds_read_b64_tr_b16 v[158:159], v11 offset:768\n"
    "ds_read_b64_tr_b16 v[236:237], v11 offset:128\n"
    "ds_read_b64_tr_b16 v[238:239], v11 offset:896\n"
    "ds_read_b64_tr_b16 v[72:73], v11 offset:256\n"
    "ds_read_b64_tr_b16 v[74:75], v11 offset:1024\n"
    "s_waitcnt lgkmcnt(0)\n"
    "s_barrier\n"
    "s_waitcnt vmcnt(0)\n"
    "s_barrier\n"
    "v_add_u32 v8, 0, v7\n"
    "ds_read_b64_tr_b16 v[160:161], v8 offset:0\n"
    "ds_read_b64_tr_b16 v[162:163], v8 offset:768\n"
    "ds_read_b64_tr_b16 v[240:241], v8 offset:128\n"
    "ds_read_b64_tr_b16 v[242:243], v8 offset:896\n"
    "ds_read_b64_tr_b16 v[76:77], v8 offset:256\n"
    "ds_read_b64_tr_b16 v[78:79], v8 offset:1024\n"
    "v_add_u32 v9, 12288, v7\n"
    "ds_read_b64_tr_b16 v[164:165], v9 offset:0\n"
    "ds_read_b64_tr_b16 v[166:167], v9 offset:768\n"
    "ds_read_b64_tr_b16 v[244:245], v9 offset:128\n"
    "ds_read_b64_tr_b16 v[246:247], v9 offset:896\n"
    "ds_read_b64_tr_b16 v[80:81], v9 offset:256\n"
    "ds_read_b64_tr_b16 v[82:83], v9 offset:1024\n"
    "v_add_u32 v10, 24576, v7\n"
    "ds_read_b64_tr_b16 v[168:169], v10 offset:0\n"
    "ds_read_b64_tr_b16 v[170:171], v10 offset:768\n"
    "ds_read_b64_tr_b16 v[248:249], v10 offset:128\n"
    "ds_read_b64_tr_b16 v[250:251], v10 offset:896\n"
    "ds_read_b64_tr_b16 v[84:85], v10 offset:256\n"
    "ds_read_b64_tr_b16 v[86:87], v10 offset:1024\n"
    "v_add_u32 v11, 36864, v7\n"
    "ds_read_b64_tr_b16 v[172:173], v11 offset:0\n"
    "ds_read_b64_tr_b16 v[174:175], v11 offset:768\n"
    "ds_read_b64_tr_b16 v[252:253], v11 offset:128\n"
    "ds_read_b64_tr_b16 v[254:255], v11 offset:896\n"
    "ds_read_b64_tr_b16 v[88:89], v11 offset:256\n"
    "ds_read_b64_tr_b16 v[90:91], v11 offset:1024\n"
    "s_waitcnt lgkmcnt(0)\n"
    "s_barrier\n"
    "ds_write_b128 v97, v[12:15] offset:0\n"
    "ds_write_b128 v97, v[16:19] offset:1024\n"
    "ds_write_b128 v97, v[20:23] offset:2048\n"
    "ds_write_b128 v97, v[24:27] offset:3072\n"
    "ds_write_b128 v97, v[28:31] offset:4096\n"
    "ds_write_b128 v97, v[32:35] offset:5120\n"
    "ds_write_b128 v97, v[36:39] offset:6144\n"
    "ds_write_b128 v97, v[40:43] offset:7168\n"
    "ds_write_b128 v97, v[44:47] offset:8192\n"
    "ds_write_b128 v97, v[48:51] offset:9216\n"
    "ds_write_b128 v97, v[52:55] offset:10240\n"
    "ds_write_b128 v97, v[56:59] offset:11264\n"
    "ds_write_b128 v97, v[60:63] offset:12288\n"
    "ds_write_b128 v97, v[64:67] offset:13312\n"
    "ds_write_b128 v97, v[68:71] offset:14336\n"
    "ds_write_b128 v97, v[72:75] offset:15360\n"
    "ds_write_b128 v97, v[76:79] offset:16384\n"
    "ds_write_b128 v97, v[80:83] offset:17408\n"
    "ds_write_b128 v97, v[84:87] offset:18432\n"
    "ds_write_b128 v97, v[88:91] offset:19456\n"
    "s_mov_b32 s42, -2\n"
    "s_mov_b32 s58, 1037384597\n"
    "s_mov_b32 s59, 1037384597\n"
    "s_mov_b32 s60, 858993459\n"
    "s_mov_b32 s61, 858993459\n"
    "s_mov_b32 s62, 67044351\n"
    "s_mov_b32 s63, 67044351\n"
    "v_mov_b32 v8, v99\n"
    "v_mov_b32 v9, v99\n"
    "v_mov_b32 v10, 137088\n"
    "ds_write_b64 v10, v[8:9] offset:0\n"
    "ds_write_b64 v10, v[8:9] offset:8\n"
    "ds_write_b64 v10, v[8:9] offset:1936\n"
    "ds_write_b64 v10, v[8:9] offset:1944\n"
    "ds_write_b64 v10, v[8:9] offset:3872\n"
    "ds_write_b64 v10, v[8:9] offset:3880\n"
    "ds_write_b64 v10, v[8:9] offset:5808\n"
    "ds_write_b64 v10, v[8:9] offset:5816\n"
    "s_lshl_b32 s55, s22, 11\n"
    "v_lshrrev_b32 v4, 2, v96\n"
    "v_and_b32 v6, 12, v4\n"
    "v_lshlrev_b32 v5, 1, v96\n"
    "v_and_b32 v5, 0x380, v5\n"
    "v_min_u32 v5, 0x200, v5\n"
    "v_add_u32 v5, v5, v6\n"
    "s_lshr_b32 s56, s23, 13\n"
    "s_mul_i32 s56, s56, 3\n"
    ".Lqk_po2_%=:\n"
    "buffer_load_dword v8, v4, s[0:3], s31 offen sc1\n"
    "s_waitcnt vmcnt(0)\n"
    "v_cmp_gt_i32 vcc, 1, v8\n"
    "s_nop 0\n"
    "s_cbranch_vccz .Lqk_pd3_%=\n"
    "s_add_u32 s36, s36, 1\n"
    "s_cmp_lt_u32 s36, 0x40000\n"
    "s_cbranch_scc1 .Lqk_sp4_%=\n"
    "v_mov_b32 v8, 20\n"
    "s_mov_b32 s40, 33728\n"
    "buffer_store_dword v8, off, s[0:3], s40 sc1\n"
    "s_waitcnt vmcnt(0)\n"
    "s_branch .Lqk_pd3_%=\n"
    ".Lqk_sp4_%=:\n"
    "s_sleep 4\n"
    "s_branch .Lqk_po2_%=\n"
    ".Lqk_pd3_%=:\n"
    ".Lqk_po5_%=:\n"
    "buffer_load_dword v8, v5, s[0:3], s32 offen sc1\n"
    "s_waitcnt vmcnt(0)\n"
    "v_cmp_gt_i32 vcc, s56, v8\n"
    "s_nop 0\n"
    "s_cbranch_vccz .Lqk_pd6_%=\n"
    "s_add_u32 s36, s36, 1\n"
    "s_cmp_lt_u32 s36, 0x40000\n"
    "s_cbranch_scc1 .Lqk_sp7_%=\n"
    "v_mov_b32 v8, 21\n"
    "s_mov_b32 s40, 33728\n"
    "buffer_store_dword v8, off, s[0:3], s40 sc1\n"
    "s_waitcnt vmcnt(0)\n"
    "s_branch .Lqk_pd6_%=\n"
    ".Lqk_sp7_%=:\n"
    "s_sleep 4\n"
    "s_branch .Lqk_po5_%=\n"
    ".Lqk_pd6_%=:\n"
    "s_add_u32 s49, s27, s23\n"
    "s_add_u32 m0, s55, 0\n"
    "s_nop 0\n"
    "buffer_load_dwordx4 v96, s[0:3], s49 offen sc1 lds\n"
    "buffer_load_dwordx4 v96, s[0:3], s49 offen offset:1024 sc1 lds\n"
    "s_add_u32 m0, s55, 11264\n"
    "s_add_u32 s49, s49, 0x4000\n"
    "buffer_load_dwordx4 v96, s[0:3], s49 offen sc1 lds\n"
    "buffer_load_dwordx4 v96, s[0:3], s49 offen offset:1024 sc1 lds\n"
    "s_add_u32 m0, s55, 22528\n"
    "s_add_u32 s49, s49, 0x4000\n"
    "buffer_load_dwordx4 v96, s[0:3], s49 offen sc1 lds\n"
    "buffer_load_dwordx4 v96, s[0:3], s49 offen offset:1024 sc1 lds\n"
    "s_add_u32 m0, s55, 33792\n"
    "s_add_u32 s49, s49, 0x4000\n"
    "buffer_load_dwordx4 v96, s[0:3], s49 offen sc1 lds\n"
    "buffer_load_dwordx4 v96, s[0:3], s49 offen offset:1024 sc1 lds\n"
    ".Lqk_po8_%=:\n"
    "buffer_load_dword v8, v6, s[0:3], s33 offen sc1\n"
    "s_waitcnt vmcnt(0)\n"
    "v_cmp_gt_i32 vcc, 2, v8\n"
    "s_nop 0\n"
    "s_cbranch_vccz .Lqk_pd9_%=\n"
    "s_add_u32 s36, s36, 1\n"
    "s_cmp_lt_u32 s36, 0x40000\n"
    "s_cbranch_scc1 .Lqk_sp10_%=\n"
    "v_mov_b32 v8, 22\n"
    "s_mov_b32 s40, 33728\n"
    "buffer_store_dword v8, off, s[0:3], s40 sc1\n"
    "s_waitcnt vmcnt(0)\n"
    "s_branch .Lqk_pd9_%=\n"
    ".Lqk_sp10_%=:\n"
    "s_sleep 4\n"
    "s_branch .Lqk_po8_%=\n"
    ".Lqk_pd9_%=:\n"
    "s_memrealtime s[38:39]\n"
    "s_waitcnt lgkmcnt(0)\n"
    "s_sub_u32 s53, s38, 816\n"
    "s_mov_b32 s42, -1\n"
    "s_mov_b32 s43, -5\n"
    "s_mov_b32 s46, 2130706432\n"
    "s_mov_b32 s44, 2130706432\n"
    "s_mov_b32 s45, 4194304\n"
    "s_mov_b32 s51, s30\n"
    "s_add_u32 s50, s28, s23\n"
    "s_barrier\n"
    "v_mov_b32 v110, v104\n"
    "ds_read_b128 v[32:35], v92 offset:0\n"
    "ds_read_b128 v[36:39], v92 offset:1024\n"
    "ds_read_b128 v[40:43], v92 offset:2048\n"
    "ds_read_b128 v[44:47], v92 offset:3072\n"
    "ds_read_b128 v[48:51], v92 offset:4096\n"
    "ds_read_b128 v[52:55], v92 offset:5120\n"
    "s_waitcnt lgkmcnt(0)\n"
    "s_branch .Lqk_bulk_%=\n"
    ".Lqk_take_%=:\n"
    "s_waitcnt vmcnt(2)\n"
    ".Lqk_mk0x10_%=:\n"
    "ds_read_b128 v[56:59], v92 offset:8192\n"
    "s_waitcnt vmcnt(1)\n"
    "ds_read_b128 v[60:63], v92 offset:9216\n"
    "s_waitcnt lgkmcnt(1)\n"
    "v_smfmac_f32_16x16x64_bf16 v[8:11], v[56:59], v[32:39], v98\n"
    "v_max_u32 v108, v56, v58\n"
    "v_smfmac_f32_16x16x64_bf16 v[12:15], v[56:59], a[160:167], v98\n"
    "s_waitcnt lgkmcnt(0)\n"
    "ds_read_b128 v[32:35], v97 offset:6144\n"
    "v_max3_u32 v108, v108, v60, v62\n"
    "v_smfmac_f32_16x16x64_bf16 v[0:3], v[56:59], v[176:183], v98\n"
    "ds_read_b128 v[36:39], v97 offset:7168\n"
    "v_cmp_eq_u32 vcc, -1, v108\n"
    "v_smfmac_f32_16x16x64_bf16 v[4:7], v[56:59], a[80:87], v98\n"
    "s_cbranch_vccnz .Lqk_retry_%=\n"
    ".Lqk_go_%=:\n"
    "s_barrier\n"
    ".Lqk_mk1x11_%=:\n"
    "v_smfmac_f32_16x16x64_bf16 v[16:19], v[56:59], a[240:247], v98\n"
    "ds_read_b128 v[24:27], v93 offset:8192\n"
    "ds_read_b128 v[28:31], v93 offset:9216\n"
    "v_smfmac_f32_16x16x64_bf16 v[20:23], v[56:59], a[0:7], v98\n"
    "s_waitcnt vmcnt(0)\n"
    "ds_read_b128 v[64:67], v92 offset:10240\n"
    "v_smfmac_f32_16x16x64_bf16 v[8:11], v[60:63], v[40:47], v98\n"
    "ds_read_b128 v[56:59], v94 offset:9216\n"
    "v_smfmac_f32_16x16x64_bf16 v[12:15], v[60:63], a[168:175], v98\n"
    "ds_read_b128 v[40:43], v97 offset:8192\n"
    "ds_read_b128 v[44:47], v97 offset:9216\n"
    "v_smfmac_f32_16x16x64_bf16 v[0:3], v[60:63], v[184:191], v98\n"
    "v_smfmac_f32_16x16x64_bf16 v[4:7], v[60:63], a[88:95], v98\n"
    "v_smfmac_f32_16x16x64_bf16 v[16:19], v[60:63], a[248:255], v98\n"
    "s_waitcnt lgkmcnt(3)\n"
    "v_max_u32 v108, v64, v66\n"
    "v_smfmac_f32_16x16x64_bf16 v[20:23], v[60:63], a[8:15], v98\n"
    "v_cmp_eq_u32 vcc, -1, v108\n"
    "v_smfmac_f32_16x16x64_bf16 v[8:11], v[24:27], v[48:55], v98\n"
    "s_cbranch_vccnz .Lqk_r2_%=\n"
    ".Lqk_go2_%=:\n"
    "s_barrier\n"
    "v_smfmac_f32_16x16x64_bf16 v[12:15], v[24:27], a[176:183], v98\n"
    "s_memrealtime s[64:65]\n"
    "ds_read_b128 v[64:67], v94 offset:8192\n"
    "ds_read_b128 v[60:63], v95 offset:8192\n"
    "v_smfmac_f32_16x16x64_bf16 v[0:3], v[24:27], v[192:199], v98\n"
    "ds_read_b128 v[48:51], v97 offset:10240\n"
    "ds_read_b128 v[52:55], v97 offset:11264\n"
    "v_smfmac_f32_16x16x64_bf16 v[4:7], v[24:27], a[96:103], v98\n"
    "v_mov_b32 v109, s43\n"
    "v_smfmac_f32_16x16x64_bf16 v[16:19], v[24:27], v[112:119], v98\n"
    "v_lshlrev_b32 v110, 10, v96\n"
    "v_smfmac_f32_16x16x64_bf16 v[20:23], v[24:27], a[16:23], v98\n"
    "ds_read_b128 v[24:27], v95 offset:9216\n"
    "s_mov_b64 vcc, s[60:61]\n"
    "v_smfmac_f32_16x16x64_bf16 v[8:11], v[28:31], v[32:39], v98\n"
    "ds_read_b128 v[32:35], v97 offset:12288\n"
    "ds_read_b128 v[36:39], v97 offset:13312\n"
    "v_smfmac_f32_16x16x64_bf16 v[12:15], v[28:31], a[184:191], v98\n"
    "v_smfmac_f32_16x16x64_bf16 v[0:3], v[28:31], v[200:207], v98\n"
    "v_smfmac_f32_16x16x64_bf16 v[4:7], v[28:31], a[104:111], v98\n"
    "v_smfmac_f32_16x16x64_bf16 v[16:19], v[28:31], v[120:127], v98\n"
    "v_smfmac_f32_16x16x64_bf16 v[20:23], v[28:31], a[24:31], v98\n"
    "s_waitcnt lgkmcnt(0)\n"
    "v_smfmac_f32_16x16x64_bf16 v[8:11], v[64:67], v[40:47], v98\n"
    "s_sub_u32 s40, s64, s53\n"
    "ds_read_b128 v[40:43], v97 offset:14336\n"
    "ds_read_b128 v[44:47], v97 offset:15360\n"
    "v_smfmac_f32_16x16x64_bf16 v[12:15], v[64:67], a[192:199], v98\n"
    "ds_read_b128 v[28:31], v96 offset:10240\n"
    "s_add_u32 s41, s64, 18\n"
    "v_smfmac_f32_16x16x64_bf16 v[0:3], v[64:67], v[208:215], v98\n"
    "s_cmp_gt_i32 s40, 6\n"
    "s_cselect_b32 s53, s41, s53\n"
    "v_mov_b32 v108, s53\n"
    "v_smfmac_f32_16x16x64_bf16 v[4:7], v[64:67], a[112:119], v98\n"
    "s_mov_b64 exec, 15\n"
    "buffer_store_dwordx2 v[108:109], v110, s[0:3], s52 offen sc1\n"
    "s_mov_b64 exec, -1\n"
    "v_smfmac_f32_16x16x64_bf16 v[16:19], v[64:67], v[128:135], v98\n"
    "v_smfmac_f32_16x16x64_bf16 v[20:23], v[64:67], a[32:39], v98\n"
    "v_bfe_u32 v111, v96, 8, 2\n"
    "v_smfmac_f32_16x16x64_bf16 v[8:11], v[56:59], v[48:55], v98\n"
    "ds_read_b128 v[48:51], v97 offset:16384\n"
    "ds_read_b128 v[52:55], v97 offset:17408\n"
    "v_smfmac_f32_16x16x64_bf16 v[12:15], v[56:59], a[200:207], v98\n"
    "ds_read_b128 v[64:67], v96 offset:21504\n"
    "v_mad_i32_i24 v110, v111, -8, v104\n"
    "v_smfmac_f32_16x16x64_bf16 v[8:11], v[60:63], v[32:39], v98\n"
    "ds_read_b128 v[32:35], v97 offset:18432\n"
    "ds_read_b128 v[36:39], v97 offset:19456\n"
    "v_smfmac_f32_16x16x64_bf16 v[12:15], v[60:63], a[208:215], v98\n"
    "s_waitcnt lgkmcnt(5)\n"
    "v_mad_i32_i24 v111, v111, -8, v102\n"
    "v_smfmac_f32_16x16x64_bf16 v[8:11], v[24:27], v[40:47], v98\n"
    "v_smfmac_f32_16x16x64_bf16 v[12:15], v[24:27], a[216:223], v98\n"
    "v_smfmac_f32_16x16x64_bf16 v[0:3], v[56:59], v[216:223], v98\n"
    "v_smfmac_f32_16x16x64_bf16 v[4:7], v[56:59], a[120:127], v98\n"
    "s_waitcnt lgkmcnt(3)\n"
    "v_smfmac_f32_16x16x64_bf16 v[8:11], v[28:31], v[48:55], v98\n"
    "v_smfmac_f32_16x16x64_bf16 v[12:15], v[28:31], a[224:231], v98\n"
    "s_waitcnt lgkmcnt(0)\n"
    "v_smfmac_f32_16x16x64_bf16 v[8:11], v[64:67], v[32:39], v98\n"
    "v_smfmac_f32_16x16x64_bf16 v[12:15], v[64:67], a[232:239], v98\n"
    "ds_read_b128 v[32:35], v92 offset:0\n"
    "ds_read_b128 v[36:39], v92 offset:1024\n"
    "v_smfmac_f32_16x16x64_bf16 v[0:3], v[60:63], v[224:231], v98\n"
    "s_nop 1\n"
    "v_add_f32 v8, v8, v10\n"
    "v_smfmac_f32_16x16x64_bf16 v[4:7], v[60:63], a[128:135], v98\n"
    "ds_read_b128 v[40:43], v92 offset:2048\n"
    "v_add_f32 v9, v9, v11\n"
    "v_smfmac_f32_16x16x64_bf16 v[0:3], v[24:27], v[232:239], v98\n"
    "ds_read_b128 v[44:47], v92 offset:3072\n"
    "v_cvt_pk_bf16_f32 v8, v8, v9\n"
    "v_smfmac_f32_16x16x64_bf16 v[0:3], v[28:31], v[240:247], v98\n"
    "ds_read_b128 v[48:51], v92 offset:4096\n"
    "v_add_f32 v12, v12, v14\n"
    "v_smfmac_f32_16x16x64_bf16 v[0:3], v[64:67], v[248:255], v98\n"
    "ds_read_b128 v[52:55], v92 offset:5120\n"
    "v_add_f32 v13, v13, v15\n"
    "v_smfmac_f32_16x16x64_bf16 v[4:7], v[24:27], a[136:143], v98\n"
    "v_cvt_pk_bf16_f32 v9, v12, v13\n"
    "v_smfmac_f32_16x16x64_bf16 v[4:7], v[28:31], a[144:151], v98\n"
    "s_nop 0\n"
    "v_mfma_f32_16x16x16_bf16 v[12:15], v[8:9], v[100:101], 0\n"
    "v_add_f32 v0, v0, v2\n"
    "v_add_f32 v1, v1, v3\n"
    "v_smfmac_f32_16x16x64_bf16 v[4:7], v[64:67], a[152:159], v98\n"
    "v_cvt_pk_bf16_f32 v0, v0, v1\n"
    "v_smfmac_f32_16x16x64_bf16 v[16:19], v[56:59], v[136:143], v98\n"
    "s_nop 1\n"
    "v_cvt_pk_bf16_f32 v15, v14, v15\n"
    "v_smfmac_f32_16x16x64_bf16 v[20:23], v[56:59], a[40:47], v98\n"
    "s_nop 0\n"
    "v_add_f32 v4, v4, v6\n"
    "v_smfmac_f32_16x16x64_bf16 v[16:19], v[60:63], v[144:151], v98\n"
    "v_add_f32 v5, v5, v7\n"
    "v_smfmac_f32_16x16x64_bf16 v[20:23], v[60:63], a[48:55], v98\n"
    "v_cvt_pk_bf16_f32 v1, v4, v5\n"
    "v_smfmac_f32_16x16x64_bf16 v[16:19], v[24:27], v[152:159], v98\n"
    "s_nop 0\n"
    "v_mfma_f32_16x16x16_bf16 v[4:7], v[0:1], v[100:101], 0\n"
    "v_cvt_pk_bf16_f32 v14, v12, v13\n"
    "v_smfmac_f32_16x16x64_bf16 v[20:23], v[24:27], a[56:63], v98\n"
    "v_smfmac_f32_16x16x64_bf16 v[16:19], v[28:31], v[160:167], v98\n"
    "s_nop 1\n"
    "v_cvt_pk_bf16_f32 v13, v6, v7\n"
    "v_smfmac_f32_16x16x64_bf16 v[20:23], v[28:31], a[64:71], v98\n"
    "v_cvt_pk_bf16_f32 v12, v4, v5\n"
    "v_smfmac_f32_16x16x64_bf16 v[16:19], v[64:67], v[168:175], v98\n"
    "v_smfmac_f32_16x16x64_bf16 v[20:23], v[64:67], a[72:79], v98\n"
    ".Lqk_mk2x12_%=:\n"
    "v_mfma_f32_16x16x32_bf16 v[24:27], v[12:15], v[12:15], 0\n"
    "s_nop 1\n"
    "v_add_f32 v16, v16, v18\n"
    "v_add_f32 v17, v17, v19\n"
    "v_cvt_pk_bf16_f32 v16, v16, v17\n"
    "v_add_f32 v20, v20, v22\n"
    "v_add_f32 v21, v21, v23\n"
    "v_cvt_pk_bf16_f32 v17, v20, v21\n"
    "s_nop 1\n"
    "v_mfma_f32_16x16x16_bf16 v[20:23], v[16:17], v[100:101], 0\n"
    "v_mfma_f32_16x16x32_bf16 v[0:3], v[72:75], v[12:15], 0\n"
    "v_mfma_f32_16x16x32_bf16 v[8:11], v[68:71], v[12:15], 0\n"
    "s_nop 1\n"
    "v_cvt_pk_bf16_f32 v17, v22, v23\n"
    "v_cvt_pk_bf16_f32 v16, v20, v21\n"
    "s_nop 1\n"
    "v_mfma_f32_16x16x16_bf16 v[24:27], v[16:17], v[16:17], v[24:27]\n"
    "v_mfma_f32_16x16x16_bf16 v[0:3], v[78:79], v[16:17], v[0:3]\n"
    "v_mfma_f32_16x16x16_bf16 v[8:11], v[76:77], v[16:17], v[8:11]\n"
    "s_nop 1\n"
    "ds_write_b64 v111, v[26:27] offset:128\n"
    "v_mfma_f32_16x16x32_bf16 v[56:59], v[12:15], v[80:83], 0\n"
    "s_nop 3\n"
    "v_cndmask_b32_dpp v8, v0, v8, vcc quad_perm:[0,1,0,1] row_mask:0xf bank_mask:0xf\n"
    "v_cndmask_b32_dpp v9, v1, v9, vcc quad_perm:[0,1,0,1] row_mask:0xf bank_mask:0xf\n"
    "v_cndmask_b32_dpp v10, v2, v10, vcc quad_perm:[0,1,0,1] row_mask:0xf bank_mask:0xf\n"
    "v_cndmask_b32_dpp v11, v3, v11, vcc quad_perm:[0,1,0,1] row_mask:0xf bank_mask:0xf\n"
    "ds_write_b128 v102, v[8:11]\n"
    "v_mfma_f32_16x16x32_bf16 v[60:63], v[12:15], v[84:87], 0\n"
    "s_mov_b64 exec, s[20:21]\n"
    "s_waitcnt lgkmcnt(0)\n"
    "s_barrier\n"
    ".Lqk_mk3x13_%=:\n"
    "ds_read_b128 v[72:75], v103 offset:0\n"
    "ds_read_b128 v[76:79], v103 offset:1936\n"
    "ds_read_b128 v[80:83], v103 offset:3872\n"
    "ds_read_b128 v[84:87], v103 offset:5808\n"
    "s_mov_b64 exec, -1\n"
    "v_mfma_f32_16x16x16_bf16 v[56:59], v[16:17], v[88:89], v[56:59]\n"
    "v_mfma_f32_16x16x16_bf16 v[60:63], v[16:17], v[90:91], v[60:63]\n"
    "s_waitcnt lgkmcnt(2)\n"
    "v_pk_add_f32 v[72:73], v[72:73], v[76:77]\n"
    "v_pk_add_f32 v[74:75], v[74:75], v[78:79]\n"
    "s_waitcnt lgkmcnt(1)\n"
    "v_pk_add_f32 v[72:73], v[72:73], v[80:81]\n"
    "v_pk_add_f32 v[74:75], v[74:75], v[82:83]\n"
    "s_waitcnt lgkmcnt(0)\n"
    "v_pk_add_f32 v[72:73], v[72:73], v[84:85]\n"
    "v_pk_add_f32 v[74:75], v[74:75], v[86:87]\n"
    "v_max3_f32 v76, v72, v73, v74\n"
    "v_max_f32 v76, v76, v75\n"
    "v_pk_mul_f32 v[72:73], v[72:73], s[58:59]\n"
    "v_pk_mul_f32 v[74:75], v[74:75], s[58:59]\n"
    "v_max_f32_dpp v76, v76, v76 row_ror:8 row_mask:0xf bank_mask:0xf\n"
    "s_nop 1\n"
    "v_max_f32_dpp v76, v76, v76 row_ror:4 row_mask:0xf bank_mask:0xf\n"
    "s_nop 1\n"
    "v_max_f32_dpp v76, v76, v76 row_ror:2 row_mask:0xf bank_mask:0xf\n"
    "s_nop 1\n"
    "v_max_f32_dpp v76, v76, v76 row_ror:1 row_mask:0xf bank_mask:0xf\n"
    "v_pk_fma_f32 v[72:73], v[76:77], s[58:59], v[72:73] op_sel_hi:[0,1,1] neg_lo:[1,0,0] neg_hi:[1,0,0]\n"
    "v_pk_fma_f32 v[74:75], v[76:77], s[58:59], v[74:75] op_sel_hi:[0,1,1] neg_lo:[1,0,0] neg_hi:[1,0,0]\n"
    "v_exp_f32 v72, v72\n"
    "v_exp_f32 v74, v74\n"
    "v_exp_f32 v73, v73\n"
    "v_exp_f32 v75, v75\n"
    "v_add_f32 v76, v72, v74\n"
    "v_add_f32 v77, v73, v75\n"
    "v_add_f32 v76, v76, v77\n"
    "s_nop 1\n"
    "v_add_f32_dpp v76, v76, v76 row_ror:8 row_mask:0xf bank_mask:0xf\n"
    "s_nop 1\n"
    "v_add_f32_dpp v76, v76, v76 row_ror:4 row_mask:0xf bank_mask:0xf\n"
    "s_nop 1\n"
    "v_add_f32_dpp v76, v76, v76 row_ror:2 row_mask:0xf bank_mask:0xf\n"
    "s_nop 1\n"
    "v_add_f32_dpp v76, v76, v76 row_ror:1 row_mask:0xf bank_mask:0xf\n"
    "v_rcp_f32 v76, v76\n"
    "s_nop 0\n"
    "v_pk_mul_f32 v[72:73], v[72:73], v[76:77] op_sel_hi:[1,0] clamp\n"
    "v_pk_mul_f32 v[74:75], v[74:75], v[76:77] op_sel_hi:[1,0] clamp\n"
    "v_cvt_pk_bf16_f32 v72, v72, v73\n"
    "v_cvt_pk_bf16_f32 v73, v74, v75\n"
    "buffer_store_dwordx2 v[72:73], v106, s[0:3], s44 offen sc1\n"
    ".Lqk_mk4x14_%=:\n"
    ".Lqk_bulk_%=:\n"
    "v_mfma_f32_16x16x32_bf16 v[0:3], a[80:83], v[32:35], 0\n"
    "ds_write_b64 v110, v[58:59] offset:224\n"
    "v_mfma_f32_16x16x32_bf16 v[4:7], a[80:83], v[36:39], 0\n"
    "ds_write_b64 v110, v[62:63] offset:128\n"
    "v_mfma_f32_16x16x32_bf16 v[8:11], a[160:163], v[32:35], 0\n"
    "v_mfma_f32_16x16x32_bf16 v[12:15], a[160:163], v[36:39], 0\n"
    "v_mfma_f32_16x16x32_bf16 v[16:19], a[0:3], v[32:35], 0\n"
    "v_mfma_f32_16x16x32_bf16 v[20:23], a[0:3], v[36:39], 0\n"
    "s_waitcnt lgkmcnt(0)\n"
    "s_barrier\n"
    "ds_read_b128 v[56:59], v105 offset:0\n"
    "v_mfma_f32_16x16x32_bf16 v[24:27], a[240:243], v[32:35], 0\n"
    "ds_read_b128 v[60:63], v105 offset:4352\n"
    "v_mfma_f32_16x16x32_bf16 v[28:31], a[240:243], v[36:39], 0\n"
    "v_mfma_f32_16x16x32_bf16 v[0:3], a[84:87], v[40:43], v[0:3]\n"
    "ds_read_b128 v[32:35], v92 offset:6144\n"
    "ds_read_b128 v[36:39], v92 offset:7168\n"
    "v_mfma_f32_16x16x32_bf16 v[4:7], a[84:87], v[44:47], v[4:7]\n"
    "ds_read_b128 v[64:67], v105 offset:8704\n"
    "v_mfma_f32_16x16x32_bf16 v[8:11], a[164:167], v[40:43], v[8:11]\n"
    "v_mfma_f32_16x16x32_bf16 v[12:15], a[164:167], v[44:47], v[12:15]\n"
    "ds_read_b128 v[68:71], v105 offset:13056\n"
    "v_mfma_f32_16x16x32_bf16 v[16:19], a[4:7], v[40:43], v[16:19]\n"
    "s_add_u32 m0, s55, 45056\n"
    "s_add_u32 s49, s49, 0x4000\n"
    "v_mfma_f32_16x16x32_bf16 v[20:23], a[4:7], v[44:47], v[20:23]\n"
    "buffer_load_dwordx4 v96, s[0:3], s49 offen sc1 lds\n"
    "v_mfma_f32_16x16x32_bf16 v[24:27], a[244:247], v[40:43], v[24:27]\n"
    "v_mfma_f32_16x16x32_bf16 v[28:31], a[244:247], v[44:47], v[28:31]\n"
    "s_waitcnt lgkmcnt(4)\n"
    "v_add_f32 v56, v56, v60\n"
    "v_add_f32 v57, v57, v61\n"
    "v_mfma_f32_16x16x32_bf16 v[0:3], a[88:91], v[48:51], v[0:3]\n"
    "ds_read_b128 v[40:43], v93 offset:0\n"
    "ds_read_b128 v[44:47], v93 offset:1024\n"
    "v_mfma_f32_16x16x32_bf16 v[4:7], a[88:91], v[52:55], v[4:7]\n"
    "buffer_load_dwordx4 v96, s[0:3], s49 offen offset:1024 sc1 lds\n"
    "v_mfma_f32_16x16x32_bf16 v[8:11], a[168:171], v[48:51], v[8:11]\n"
    "v_mov_b64 v[110:111], -1\n"
    "v_add_f32 v58, v58, v62\n"
    "v_mfma_f32_16x16x32_bf16 v[12:15], a[168:171], v[52:55], v[12:15]\n"
    "v_add_f32 v59, v59, v63\n"
    "s_waitcnt lgkmcnt(3)\n"
    "v_mfma_f32_16x16x32_bf16 v[16:19], a[8:11], v[48:51], v[16:19]\n"
    "v_add_f32 v56, v56, v64\n"
    "v_mfma_f32_16x16x32_bf16 v[20:23], a[8:11], v[52:55], v[20:23]\n"
    "buffer_load_dwordx4 v[80:83], v96, s[0:3], s50 offen sc1\n"
    "v_mfma_f32_16x16x32_bf16 v[24:27], a[248:251], v[48:51], v[24:27]\n"
    "v_add_f32 v57, v57, v65\n"
    "v_add_f32 v58, v58, v66\n"
    "v_mfma_f32_16x16x32_bf16 v[28:31], a[248:251], v[52:55], v[28:31]\n"
    "s_waitcnt lgkmcnt(4)\n"
    "v_add_f32 v59, v59, v67\n"
    "s_waitcnt lgkmcnt(2)\n"
    "v_mfma_f32_16x16x32_bf16 v[0:3], a[92:95], v[32:35], v[0:3]\n"
    "ds_read_b128 v[48:51], v93 offset:2048\n"
    "ds_read_b128 v[52:55], v93 offset:3072\n"
    "v_mfma_f32_16x16x32_bf16 v[4:7], a[92:95], v[36:39], v[4:7]\n"
    "buffer_load_dwordx4 v[84:87], v96, s[0:3], s50 offen offset:1024 sc1\n"
    "v_mfma_f32_16x16x32_bf16 v[8:11], a[172:175], v[32:35], v[8:11]\n"
    "v_add_f32 v56, v56, v68\n"
    "v_add_f32 v57, v57, v69\n"
    "v_mfma_f32_16x16x32_bf16 v[12:15], a[172:175], v[36:39], v[12:15]\n"
    "v_add_f32 v58, v58, v70\n"
    "v_add_f32 v59, v59, v71\n"
    "v_mfma_f32_16x16x32_bf16 v[16:19], a[12:15], v[32:35], v[16:19]\n"
    "s_barrier\n"
    "v_cndmask_b32 v56, v99, v56, s[12:13]\n"
    "v_cndmask_b32 v57, v99, v57, s[14:15]\n"
    "v_mfma_f32_16x16x32_bf16 v[20:23], a[12:15], v[36:39], v[20:23]\n"
    "buffer_store_dwordx2 v[110:111], v106, s[0:3], s45 offen sc1\n"
    "v_cndmask_b32 v58, v99, v58, s[16:17]\n"
    "v_mfma_f32_16x16x32_bf16 v[24:27], a[252:255], v[32:35], v[24:27]\n"
    "v_cndmask_b32 v59, v99, v59, s[18:19]\n"
    "v_max3_f32 v72, v56, v57, v58\n"
    "v_mfma_f32_16x16x32_bf16 v[28:31], a[252:255], v[36:39], v[28:31]\n"
    "s_waitcnt lgkmcnt(2)\n"
    "v_max_f32 v72, v72, v59\n"
    "v_cndmask_b32 v74, v99, v72, s[62:63]\n"
    "v_mfma_f32_16x16x32_bf16 v[0:3], a[96:99], v[40:43], v[0:3]\n"
    "ds_read_b128 v[32:35], v93 offset:4096\n"
    "ds_read_b128 v[36:39], v93 offset:5120\n"
    "v_mfma_f32_16x16x32_bf16 v[4:7], a[96:99], v[44:47], v[4:7]\n"
    "v_cndmask_b32 v75, v72, v99, s[62:63]\n"
    "v_max_f32_dpp v74, v74, v74 row_ror:8 row_mask:0xf bank_mask:0xf\n"
    "v_mfma_f32_16x16x32_bf16 v[8:11], a[176:179], v[40:43], v[8:11]\n"
    "v_max_f32_dpp v75, v75, v75 row_ror:8 row_mask:0xf bank_mask:0xf\n"
    "v_max_f32_dpp v74, v74, v74 row_ror:4 row_mask:0xf bank_mask:0xf\n"
    "v_mfma_f32_16x16x32_bf16 v[12:15], a[176:179], v[44:47], v[12:15]\n"
    "v_max_f32_dpp v75, v75, v75 row_ror:4 row_mask:0xf bank_mask:0xf\n"
    "v_max_f32_dpp v74, v74, v74 row_ror:2 row_mask:0xf bank_mask:0xf\n"
    "v_mfma_f32_16x16x32_bf16 v[16:19], a[16:19], v[40:43], v[16:19]\n"
    "v_max_f32_dpp v75, v75, v75 row_ror:2 row_mask:0xf bank_mask:0xf\n"
    "v_max_f32_dpp v74, v74, v74 row_ror:1 row_mask:0xf bank_mask:0xf\n"
    "v_mfma_f32_16x16x32_bf16 v[20:23], a[16:19], v[44:47], v[20:23]\n"
    "buffer_store_dwordx2 v[110:111], v107, s[0:3], s45 offen sc1\n"
    "v_max_f32_dpp v75, v75, v75 row_ror:1 row_mask:0xf bank_mask:0xf\n"
    "v_mfma_f32_16x16x32_bf16 v[24:27], v[112:115], v[40:43], v[24:27]\n"
    "v_cndmask_b32 v72, v75, v74, s[62:63]\n"
    "v_mul_f32 v72, s58, v72\n"
    "v_mfma_f32_16x16x32_bf16 v[28:31], v[112:115], v[44:47], v[28:31]\n"
    "s_waitcnt lgkmcnt(2)\n"
    "v_fma_f32 v56, v56, s58, -v72\n"
    "v_fma_f32 v57, v57, s58, -v72\n"
    "v_mfma_f32_16x16x32_bf16 v[0:3], a[100:103], v[48:51], v[0:3]\n"
    "ds_read_b128 v[40:43], v93 offset:6144\n"
    "ds_read_b128 v[44:47], v93 offset:7168\n"
    "v_mfma_f32_16x16x32_bf16 v[4:7], a[100:103], v[52:55], v[4:7]\n"
    "v_fma_f32 v58, v58, s58, -v72\n"
    "v_fma_f32 v59, v59, s58, -v72\n"
    "v_mfma_f32_16x16x32_bf16 v[8:11], a[180:183], v[48:51], v[8:11]\n"
    "v_exp_f32 v56, v56\n"
    "v_mfma_f32_16x16x32_bf16 v[12:15], a[180:183], v[52:55], v[12:15]\n"
    "v_exp_f32 v58, v58\n"
    "v_mfma_f32_16x16x32_bf16 v[16:19], a[20:23], v[48:51], v[16:19]\n"
    "v_mfma_f32_16x16x32_bf16 v[20:23], a[20:23], v[52:55], v[20:23]\n"
    "buffer_load_dwordx2 v[108:109], off, s[0:3], s51 sc1\n"
    "v_mfma_f32_16x16x32_bf16 v[24:27], v[116:119], v[48:51], v[24:27]\n"
    "v_exp_f32 v57, v57\n"
    "v_mfma_f32_16x16x32_bf16 v[28:31], v[116:119], v[52:55], v[28:31]\n"
    "s_waitcnt lgkmcnt(2)\n"
    "v_exp_f32 v59, v59\n"
    "v_mfma_f32_16x16x32_bf16 v[0:3], a[104:107], v[32:35], v[0:3]\n"
    "ds_read_b128 v[48:51], v94 offset:0\n"
    "ds_read_b128 v[52:55], v94 offset:1024\n"
    "v_mfma_f32_16x16x32_bf16 v[4:7], a[104:107], v[36:39], v[4:7]\n"
    "v_add_f32 v72, v56, v58\n"
    "v_add_f32 v73, v57, v59\n"
    "v_mfma_f32_16x16x32_bf16 v[8:11], a[184:187], v[32:35], v[8:11]\n"
    "v_add_f32 v72, v72, v73\n"
    "v_cndmask_b32 v74, 0, v72, s[62:63]\n"
    "v_mfma_f32_16x16x32_bf16 v[12:15], a[184:187], v[36:39], v[12:15]\n"
    "v_cndmask_b32 v75, v72, 0, s[62:63]\n"
    "v_add_f32_dpp v74, v74, v74 row_ror:8 row_mask:0xf bank_mask:0xf\n"
    "v_mfma_f32_16x16x32_bf16 v[16:19], a[24:27], v[32:35], v[16:19]\n"
    "v_add_f32_dpp v75, v75, v75 row_ror:8 row_mask:0xf bank_mask:0xf\n"
    "v_add_f32_dpp v74, v74, v74 row_ror:4 row_mask:0xf bank_mask:0xf\n"
    "v_mfma_f32_16x16x32_bf16 v[20:23], a[24:27], v[36:39], v[20:23]\n"
    "v_add_f32_dpp v75, v75, v75 row_ror:4 row_mask:0xf bank_mask:0xf\n"
    "v_add_f32_dpp v74, v74, v74 row_ror:2 row_mask:0xf bank_mask:0xf\n"
    "v_mfma_f32_16x16x32_bf16 v[24:27], v[120:123], v[32:35], v[24:27]\n"
    "v_add_f32_dpp v75, v75, v75 row_ror:2 row_mask:0xf bank_mask:0xf\n"
    "v_add_f32_dpp v74, v74, v74 row_ror:1 row_mask:0xf bank_mask:0xf\n"
    "v_mfma_f32_16x16x32_bf16 v[28:31], v[120:123], v[36:39], v[28:31]\n"
    "s_waitcnt lgkmcnt(2)\n"
    "v_add_f32_dpp v75, v75, v75 row_ror:1 row_mask:0xf bank_mask:0xf\n"
    "v_cndmask_b32 v72, v75, v74, s[62:63]\n"
    "v_mfma_f32_16x16x32_bf16 v[0:3], a[108:111], v[40:43], v[0:3]\n"
    "ds_read_b128 v[32:35], v94 offset:2048\n"
    "ds_read_b128 v[36:39], v94 offset:3072\n"
    "v_mfma_f32_16x16x32_bf16 v[4:7], a[108:111], v[44:47], v[4:7]\n"
    "v_rcp_f32 v72, v72\n"
    "v_mfma_f32_16x16x32_bf16 v[8:11], a[188:191], v[40:43], v[8:11]\n"
    "v_mul_f32 v56, v56, v72 clamp\n"
    "v_mul_f32 v57, v57, v72 clamp\n"
    "v_mfma_f32_16x16x32_bf16 v[12:15], a[188:191], v[44:47], v[12:15]\n"
    "v_mul_f32 v58, v58, v72 clamp\n"
    "v_mul_f32 v59, v59, v72 clamp\n"
    "v_mfma_f32_16x16x32_bf16 v[16:19], a[28:31], v[40:43], v[16:19]\n"
    "v_cvt_pk_bf16_f32 v56, v56, v57\n"
    "v_cvt_pk_bf16_f32 v57, v58, v59\n"
    "v_mfma_f32_16x16x32_bf16 v[20:23], a[28:31], v[44:47], v[20:23]\n"
    "buffer_store_dwordx2 v[56:57], v107, s[0:3], s46 offen sc1\n"
    "v_mfma_f32_16x16x32_bf16 v[24:27], v[124:127], v[40:43], v[24:27]\n"
    "s_cmp_eq_u32 s42, 63\n"
    "s_cbranch_scc1 .Lqk_end_%=\n"
    "v_mfma_f32_16x16x32_bf16 v[28:31], v[124:127], v[44:47], v[28:31]\n"
    "s_waitcnt lgkmcnt(2)\n"
    "s_add_u32 s42, s42, 1\n"
    "s_add_u32 s56, s42, 1\n"
    "v_mfma_f32_16x16x32_bf16 v[0:3], a[112:115], v[48:51], v[0:3]\n"
    "s_add_u32 s44, s42, 0\n"
    "ds_read_b128 v[40:43], v94 offset:4096\n"
    "ds_read_b128 v[44:47], v94 offset:5120\n"
    "v_mfma_f32_16x16x32_bf16 v[4:7], a[112:115], v[52:55], v[4:7]\n"
    "s_and_b32 s44, s44, 3\n"
    "s_lshl_b32 s44, s44, 22\n"
    "s_add_u32 s45, s42, 2\n"
    "v_mfma_f32_16x16x32_bf16 v[8:11], a[192:195], v[48:51], v[8:11]\n"
    "s_and_b32 s45, s45, 3\n"
    "s_lshl_b32 s45, s45, 22\n"
    "s_and_b32 s51, s42, 63\n"
    "v_mfma_f32_16x16x32_bf16 v[12:15], a[192:195], v[52:55], v[12:15]\n"
    "s_lshl_b32 s51, s51, 7\n"
    "s_mov_b32 s46, s44\n"
    "s_add_u32 s47, s24, s44\n"
    "v_mfma_f32_16x16x32_bf16 v[16:19], a[32:35], v[48:51], v[16:19]\n"
    "s_add_u32 s48, s25, s44\n"
    "s_add_u32 s52, s29, s51\n"
    "s_add_u32 s51, s30, s51\n"
    "v_mfma_f32_16x16x32_bf16 v[20:23], a[32:35], v[52:55], v[20:23]\n"
    "s_lshl_b32 s49, s56, 19\n"
    "s_lshl_b32 s41, s56, 11\n"
    "s_xor_b32 s41, s41, s23\n"
    "v_mfma_f32_16x16x32_bf16 v[24:27], v[128:131], v[48:51], v[24:27]\n"
    "s_and_b32 s41, s41, 0x2000\n"
    "s_add_u32 s49, s49, s41\n"
    "s_add_u32 s49, s49, s27\n"
    "v_mfma_f32_16x16x32_bf16 v[28:31], v[128:131], v[52:55], v[28:31]\n"
    "s_waitcnt lgkmcnt(2)\n"
    "s_lshl_b32 s50, s56, 20\n"
    "s_lshl_b32 s41, s56, 12\n"
    "v_mfma_f32_16x16x32_bf16 v[0:3], a[116:119], v[32:35], v[0:3]\n"
    "s_xor_b32 s41, s41, s23\n"
    "ds_read_b128 v[48:51], v94 offset:6144\n"
    "ds_read_b128 v[52:55], v94 offset:7168\n"
    "v_mfma_f32_16x16x32_bf16 v[4:7], a[116:119], v[36:39], v[4:7]\n"
    "s_and_b32 s41, s41, 0x2000\n"
    "s_add_u32 s50, s50, s41\n"
    "s_add_u32 s50, s50, s28\n"
    "v_mfma_f32_16x16x32_bf16 v[8:11], a[196:199], v[32:35], v[8:11]\n"
    "v_mfma_f32_16x16x32_bf16 v[12:15], a[196:199], v[36:39], v[12:15]\n"
    "v_mfma_f32_16x16x32_bf16 v[16:19], a[36:39], v[32:35], v[16:19]\n"
    "v_mfma_f32_16x16x32_bf16 v[20:23], a[36:39], v[36:39], v[20:23]\n"
    "v_mfma_f32_16x16x32_bf16 v[24:27], v[132:135], v[32:35], v[24:27]\n"
    "v_mfma_f32_16x16x32_bf16 v[28:31], v[132:135], v[36:39], v[28:31]\n"
    "s_waitcnt lgkmcnt(2)\n"
    "v_mfma_f32_16x16x32_bf16 v[0:3], a[120:123], v[40:43], v[0:3]\n"
    "ds_read_b128 v[32:35], v95 offset:0\n"
    "ds_read_b128 v[36:39], v95 offset:1024\n"
    "v_mfma_f32_16x16x32_bf16 v[4:7], a[120:123], v[44:47], v[4:7]\n"
    "v_mfma_f32_16x16x32_bf16 v[8:11], a[200:203], v[40:43], v[8:11]\n"
    "v_mfma_f32_16x16x32_bf16 v[12:15], a[200:203], v[44:47], v[12:15]\n"
    "v_mfma_f32_16x16x32_bf16 v[16:19], a[40:43], v[40:43], v[16:19]\n"
    "v_mfma_f32_16x16x32_bf16 v[20:23], a[40:43], v[44:47], v[20:23]\n"
    "v_mfma_f32_16x16x32_bf16 v[24:27], v[136:139], v[40:43], v[24:27]\n"
    "v_mfma_f32_16x16x32_bf16 v[28:31], v[136:139], v[44:47], v[28:31]\n"
    "s_waitcnt lgkmcnt(2)\n"
    "v_mfma_f32_16x16x32_bf16 v[0:3], a[124:127], v[48:51], v[0:3]\n"
    "ds_read_b128 v[40:43], v95 offset:2048\n"
    "ds_read_b128 v[44:47], v95 offset:3072\n"
    "v_mfma_f32_16x16x32_bf16 v[4:7], a[124:127], v[52:55], v[4:7]\n"
    "v_mfma_f32_16x16x32_bf16 v[8:11], a[204:207], v[48:51], v[8:11]\n"
    "v_mfma_f32_16x16x32_bf16 v[12:15], a[204:207], v[52:55], v[12:15]\n"
    "v_mfma_f32_16x16x32_bf16 v[16:19], a[44:47], v[48:51], v[16:19]\n"
    "v_mfma_f32_16x16x32_bf16 v[20:23], a[44:47], v[52:55], v[20:23]\n"
    "v_mfma_f32_16x16x32_bf16 v[24:27], v[140:143], v[48:51], v[24:27]\n"
    "v_mfma_f32_16x16x32_bf16 v[28:31], v[140:143], v[52:55], v[28:31]\n"
    "s_waitcnt lgkmcnt(2)\n"
    "v_mfma_f32_16x16x32_bf16 v[0:3], a[128:131], v[32:35], v[0:3]\n"
    "s_waitcnt vmcnt(6)\n"
    "ds_read_b128 v[48:51], v95 offset:4096\n"
    "ds_read_b128 v[52:55], v95 offset:5120\n"
    "v_mfma_f32_16x16x32_bf16 v[4:7], a[128:131], v[36:39], v[4:7]\n"
    "s_barrier\n"
    "v_mfma_f32_16x16x32_bf16 v[8:11], a[208:211], v[32:35], v[8:11]\n"
    "v_mfma_f32_16x16x32_bf16 v[12:15], a[208:211], v[36:39], v[12:15]\n"
    "v_mfma_f32_16x16x32_bf16 v[16:19], a[48:51], v[32:35], v[16:19]\n"
    "v_mfma_f32_16x16x32_bf16 v[20:23], a[48:51], v[36:39], v[20:23]\n"
    "v_mfma_f32_16x16x32_bf16 v[24:27], v[144:147], v[32:35], v[24:27]\n"
    "v_mfma_f32_16x16x32_bf16 v[28:31], v[144:147], v[36:39], v[28:31]\n"
    "s_waitcnt lgkmcnt(2)\n"
    "v_mfma_f32_16x16x32_bf16 v[0:3], a[132:135], v[40:43], v[0:3]\n"
    "ds_read_b128 v[32:35], v95 offset:6144\n"
    "ds_read_b128 v[36:39], v95 offset:7168\n"
    "v_mfma_f32_16x16x32_bf16 v[4:7], a[132:135], v[44:47], v[4:7]\n"
    "v_mfma_f32_16x16x32_bf16 v[8:11], a[212:215], v[40:43], v[8:11]\n"
    "v_mfma_f32_16x16x32_bf16 v[12:15], a[212:215], v[44:47], v[12:15]\n"
    "v_mfma_f32_16x16x32_bf16 v[16:19], a[52:55], v[40:43], v[16:19]\n"
    "v_mfma_f32_16x16x32_bf16 v[20:23], a[52:55], v[44:47], v[20:23]\n"
    "s_waitcnt lgkmcnt(0)\n"
    "s_barrier\n"
    "v_mfma_f32_16x16x32_bf16 v[24:27], v[148:151], v[40:43], v[24:27]\n"
    "s_add_u32 m0, s55, 0\n"
    "v_mfma_f32_16x16x32_bf16 v[28:31], v[148:151], v[44:47], v[28:31]\n"
    "s_waitcnt lgkmcnt(2)\n"
    "v_mfma_f32_16x16x32_bf16 v[0:3], a[136:139], v[48:51], v[0:3]\n"
    "ds_read_b128 v[40:43], v96 offset:45056\n"
    "ds_read_b128 v[44:47], v96 offset:46080\n"
    "v_mfma_f32_16x16x32_bf16 v[4:7], a[136:139], v[52:55], v[4:7]\n"
    "buffer_load_dwordx4 v96, s[0:3], s49 offen sc1 lds\n"
    "v_mfma_f32_16x16x32_bf16 v[8:11], a[216:219], v[48:51], v[8:11]\n"
    "v_mfma_f32_16x16x32_bf16 v[12:15], a[216:219], v[52:55], v[12:15]\n"
    "v_mfma_f32_16x16x32_bf16 v[16:19], a[56:59], v[48:51], v[16:19]\n"
    "v_mfma_f32_16x16x32_bf16 v[20:23], a[56:59], v[52:55], v[20:23]\n"
    "buffer_load_dwordx4 v96, s[0:3], s49 offen offset:1024 sc1 lds\n"
    "v_mfma_f32_16x16x32_bf16 v[24:27], v[152:155], v[48:51], v[24:27]\n"
    "s_add_u32 m0, s55, 11264\n"
    "s_add_u32 s49, s49, 0x4000\n"
    "v_mfma_f32_16x16x32_bf16 v[28:31], v[152:155], v[52:55], v[28:31]\n"
    "s_waitcnt lgkmcnt(2)\n"
    "v_mfma_f32_16x16x32_bf16 v[0:3], a[140:143], v[32:35], v[0:3]\n"
    "ds_read_b128 v[48:51], v96 offset:47104\n"
    "ds_read_b128 v[52:55], v96 offset:48128\n"
    "v_mfma_f32_16x16x32_bf16 v[4:7], a[140:143], v[36:39], v[4:7]\n"
    "buffer_load_dwordx4 v96, s[0:3], s49 offen sc1 lds\n"
    "v_mfma_f32_16x16x32_bf16 v[8:11], a[220:223], v[32:35], v[8:11]\n"
    "s_waitcnt vmcnt(4)\n"
    "v_readfirstlane_b32 s40, v108\n"
    "v_readfirstlane_b32 s41, v109\n"
    "v_mfma_f32_16x16x32_bf16 v[12:15], a[220:223], v[36:39], v[12:15]\n"
    "v_mfma_f32_16x16x32_bf16 v[16:19], a[60:63], v[32:35], v[16:19]\n"
    "s_cmp_eq_u32 s41, s43\n"
    "s_cselect_b32 s53, s40, s53\n"
    "s_add_u32 s53, s53, 272\n"
    "v_mfma_f32_16x16x32_bf16 v[20:23], a[60:63], v[36:39], v[20:23]\n"
    "s_sub_u32 s54, s53, 63\n"
    "s_mov_b32 s43, s56\n"
    "v_mfma_f32_16x16x32_bf16 v[24:27], v[156:159], v[32:35], v[24:27]\n"
    "buffer_load_dwordx4 v96, s[0:3], s49 offen offset:1024 sc1 lds\n"
    "v_mfma_f32_16x16x32_bf16 v[28:31], v[156:159], v[36:39], v[28:31]\n"
    "s_waitcnt lgkmcnt(2)\n"
    "s_add_u32 m0, s55, 22528\n"
    "s_add_u32 s49, s49, 0x4000\n"
    "v_mfma_f32_16x16x32_bf16 v[0:3], a[144:147], v[40:43], v[0:3]\n"
    "ds_read_b128 v[32:35], v96 offset:49152\n"
    "ds_read_b128 v[36:39], v96 offset:50176\n"
    "v_mfma_f32_16x16x32_bf16 v[4:7], a[144:147], v[44:47], v[4:7]\n"
    "buffer_load_dwordx4 v96, s[0:3], s49 offen sc1 lds\n"
    "buffer_load_dwordx4 v96, s[0:3], s49 offen offset:1024 sc1 lds\n"
    "s_add_u32 m0, s55, 33792\n"
    "s_add_u32 s49, s49, 0x4000\n"
    "buffer_load_dwordx4 v96, s[0:3], s49 offen sc1 lds\n"
    "buffer_load_dwordx4 v96, s[0:3], s49 offen offset:1024 sc1 lds\n"
    "s_mov_b32 m0, s26\n"
    ".Lqk_mk6x15_%=:\n"
    ".Lqk_rt17_%=:\n"
    "s_memrealtime s[38:39]\n"
    "s_waitcnt lgkmcnt(0)\n"
    "s_sub_u32 s40, s54, s38\n"
    "s_cmp_lt_u32 s40, 600\n"
    "s_cbranch_scc1 .Lqk_rt17_%=\n"
    "buffer_load_dwordx4 v96, s[0:3], s47 offen sc1 lds\n"
    "buffer_load_dwordx4 v96, s[0:3], s47 offen offset:1024 sc1 lds\n"
    ".Lqk_mk7x17_%=:\n"
    "v_mfma_f32_16x16x32_bf16 v[8:11], a[224:227], v[40:43], v[8:11]\n"
    "v_mfma_f32_16x16x32_bf16 v[12:15], a[224:227], v[44:47], v[12:15]\n"
    "v_mfma_f32_16x16x32_bf16 v[16:19], a[64:67], v[40:43], v[16:19]\n"
    "v_mfma_f32_16x16x32_bf16 v[20:23], a[64:67], v[44:47], v[20:23]\n"
    "v_mfma_f32_16x16x32_bf16 v[24:27], v[160:163], v[40:43], v[24:27]\n"
    "v_mfma_f32_16x16x32_bf16 v[28:31], v[160:163], v[44:47], v[28:31]\n"
    "s_waitcnt lgkmcnt(2)\n"
    "v_mfma_f32_16x16x32_bf16 v[0:3], a[148:151], v[48:51], v[0:3]\n"
    "ds_read_b128 v[40:43], v96 offset:51200\n"
    "ds_read_b128 v[44:47], v96 offset:52224\n"
    "v_mfma_f32_16x16x32_bf16 v[4:7], a[148:151], v[52:55], v[4:7]\n"
    "buffer_load_dwordx4 v96, s[0:3], s48 offen offset:2048 sc1 lds\n"
    "v_mfma_f32_16x16x32_bf16 v[8:11], a[228:231], v[48:51], v[8:11]\n"
    "v_mfma_f32_16x16x32_bf16 v[12:15], a[228:231], v[52:55], v[12:15]\n"
    "v_mfma_f32_16x16x32_bf16 v[16:19], a[68:71], v[48:51], v[16:19]\n"
    "v_mfma_f32_16x16x32_bf16 v[20:23], a[68:71], v[52:55], v[20:23]\n"
    "v_mfma_f32_16x16x32_bf16 v[24:27], v[164:167], v[48:51], v[24:27]\n"
    "v_mfma_f32_16x16x32_bf16 v[28:31], v[164:167], v[52:55], v[28:31]\n"
    "s_waitcnt lgkmcnt(2)\n"
    "v_mfma_f32_16x16x32_bf16 v[0:3], a[152:155], v[32:35], v[0:3]\n"
    "v_mfma_f32_16x16x32_bf16 v[4:7], a[152:155], v[36:39], v[4:7]\n"
    "v_mfma_f32_16x16x32_bf16 v[8:11], a[232:235], v[32:35], v[8:11]\n"
    "v_mfma_f32_16x16x32_bf16 v[12:15], a[232:235], v[36:39], v[12:15]\n"
    "v_mfma_f32_16x16x32_bf16 v[16:19], a[72:75], v[32:35], v[16:19]\n"
    "v_mfma_f32_16x16x32_bf16 v[20:23], a[72:75], v[36:39], v[20:23]\n"
    "v_mfma_f32_16x16x32_bf16 v[24:27], v[168:171], v[32:35], v[24:27]\n"
    "v_mfma_f32_16x16x32_bf16 v[28:31], v[168:171], v[36:39], v[28:31]\n"
    "s_waitcnt lgkmcnt(0)\n"
    "v_mfma_f32_16x16x32_bf16 v[0:3], a[156:159], v[40:43], v[0:3]\n"
    "v_mfma_f32_16x16x32_bf16 v[4:7], a[156:159], v[44:47], v[4:7]\n"
    "v_mfma_f32_16x16x32_bf16 v[8:11], a[236:239], v[40:43], v[8:11]\n"
    "v_mfma_f32_16x16x32_bf16 v[12:15], a[236:239], v[44:47], v[12:15]\n"
    "v_mfma_f32_16x16x32_bf16 v[16:19], a[76:79], v[40:43], v[16:19]\n"
    "v_mfma_f32_16x16x32_bf16 v[20:23], a[76:79], v[44:47], v[20:23]\n"
    "v_mfma_f32_16x16x32_bf16 v[24:27], v[172:175], v[40:43], v[24:27]\n"
    "v_mfma_f32_16x16x32_bf16 v[28:31], v[172:175], v[44:47], v[28:31]\n"
    "ds_read_b128 v[32:35], v97 offset:0\n"
    "ds_read_b128 v[36:39], v97 offset:1024\n"
    "ds_read_b128 v[40:43], v97 offset:2048\n"
    "ds_read_b128 v[44:47], v97 offset:3072\n"
    "ds_read_b128 v[48:51], v97 offset:4096\n"
    "ds_read_b128 v[52:55], v97 offset:5120\n"
    "v_cvt_pk_bf16_f32 v68, v0, v1\n"
    "v_cvt_pk_bf16_f32 v69, v2, v3\n"
    "v_cvt_pk_bf16_f32 v70, v8, v9\n"
    "v_cvt_pk_bf16_f32 v71, v10, v11\n"
    "v_cvt_pk_bf16_f32 v72, v4, v5\n"
    "v_cvt_pk_bf16_f32 v73, v6, v7\n"
    "v_cvt_pk_bf16_f32 v74, v12, v13\n"
    "v_cvt_pk_bf16_f32 v75, v14, v15\n"
    "v_mfma_f32_16x16x32_bf16 v[108:111], v[68:71], v[84:87], 0\n"
    "v_cvt_pk_bf16_f32 v88, v24, v25\n"
    "v_cvt_pk_bf16_f32 v89, v26, v27\n"
    "v_cvt_pk_bf16_f32 v90, v28, v29\n"
    "v_cvt_pk_bf16_f32 v91, v30, v31\n"
    "v_mfma_f32_16x16x32_bf16 v[24:27], v[72:75], v[84:87], 0\n"
    "v_cvt_pk_bf16_f32 v76, v16, v17\n"
    "v_cvt_pk_bf16_f32 v77, v18, v19\n"
    "v_cvt_pk_bf16_f32 v78, v20, v21\n"
    "v_cvt_pk_bf16_f32 v79, v22, v23\n"
    "v_mfma_f32_16x16x32_bf16 v[28:31], v[68:71], v[80:83], 0\n"
    "v_mov_b64 v[0:1], 0\n"
    "v_mov_b64 v[2:3], 0\n"
    "v_mfma_f32_16x16x16_bf16 v[108:111], v[76:77], v[90:91], v[108:111]\n"
    "v_mov_b64 v[4:5], 0\n"
    "v_mov_b64 v[6:7], 0\n"
    "v_mfma_f32_16x16x16_bf16 v[24:27], v[78:79], v[90:91], v[24:27]\n"
    "v_mov_b64 v[8:9], 0\n"
    "v_mov_b64 v[10:11], 0\n"
    "v_mfma_f32_16x16x16_bf16 v[28:31], v[76:77], v[88:89], v[28:31]\n"
    "v_mov_b64 v[12:13], 0\n"
    "v_mov_b64 v[14:15], 0\n"
    "v_mov_b64 v[16:17], 0\n"
    "v_mov_b64 v[18:19], 0\n"
    "v_mov_b64 v[20:21], 0\n"
    "v_mov_b64 v[22:23], 0\n"
    "ds_write_b128 v104, v[108:111]\n"
    "ds_write_b128 v104, v[24:27] offset:64\n"
    "ds_write_b128 v104, v[28:31] offset:160\n"
    ".Lqk_mk8x18_%=:\n"
    "s_branch .Lqk_take_%=\n"
    ".Lqk_retry_%=:\n"
    "s_add_u32 s36, s36, 1\n"
    "s_cmp_lt_u32 s36, 0x40000\n"
    "s_cbranch_scc1 .Lqk_sp20_%=\n"
    "v_mov_b32 v108, 1\n"
    "s_mov_b32 s40, 33728\n"
    "buffer_store_dword v108, off, s[0:3], s40 sc1\n"
    "s_waitcnt vmcnt(0)\n"
    "s_branch .Lqk_go_%=\n"
    ".Lqk_sp20_%=:\n"
    "s_waitcnt lgkmcnt(0)\n"
    "s_nop 7\n"
    "ds_read_b128 v[32:35], v97 offset:0\n"
    "ds_read_b128 v[36:39], v97 offset:1024\n"
    "v_mov_b64 v[0:1], 0\n"
    "v_mov_b64 v[2:3], 0\n"
    "v_mov_b64 v[4:5], 0\n"
    "v_mov_b64 v[6:7], 0\n"
    "v_mov_b64 v[8:9], 0\n"
    "v_mov_b64 v[10:11], 0\n"
    "v_mov_b64 v[12:13], 0\n"
    "v_mov_b64 v[14:15], 0\n"
    "v_mov_b64 v[16:17], 0\n"
    "v_mov_b64 v[18:19], 0\n"
    "v_mov_b64 v[20:21], 0\n"
    "v_mov_b64 v[22:23], 0\n"
    "buffer_load_dwordx4 v96, s[0:3], s47 offen sc1 lds\n"
    "buffer_load_dwordx4 v96, s[0:3], s47 offen offset:1024 sc1 lds\n"
    "buffer_load_dwordx4 v96, s[0:3], s48 offen offset:2048 sc1 lds\n"
    "s_waitcnt vmcnt(0)\n"
    "s_branch .Lqk_take_%=\n"
    ".Lqk_r2_%=:\n"
    "s_add_u32 s36, s36, 1\n"
    "s_cmp_lt_u32 s36, 0x40000\n"
    "s_cbranch_scc1 .Lqk_sp21_%=\n"
    "v_mov_b32 v108, 5\n"
    "s_mov_b32 s40, 33728\n"
    "buffer_store_dword v108, off, s[0:3], s40 sc1\n"
    "s_waitcnt vmcnt(0)\n"
    "s_branch .Lqk_go2_%=\n"
    ".Lqk_sp21_%=:\n"
    "buffer_load_dwordx4 v96, s[0:3], s48 offen offset:2048 sc1 lds\n"
    "s_waitcnt vmcnt(0)\n"
    "ds_read_b128 v[64:67], v92 offset:10240\n"
    "s_waitcnt lgkmcnt(0)\n"
    "v_max_u32 v108, v64, v66\n"
    "v_cmp_eq_u32 vcc, -1, v108\n"
    "s_nop 0\n"
    "s_cbranch_vccnz .Lqk_r2_%=\n"
    "s_branch .Lqk_go2_%=\n"
    ".Lqk_end_%=:\n"
    "s_waitcnt vmcnt(0) lgkmcnt(0)\n"
    "s_endpgm\n"
    :: "{s[0:3]}"(s0_), "{s[4:7]}"(s4_), "{s[8:11]}"(s8_), "{s[12:15]}"(s12_), "{s[16:19]}"(s16_), "{s[20:23]}"(s20_), "{s[24:27]}"(s24_), "{s[28:31]}"(s28_), "{s[32:35]}"(s32_), "{v[0:3]}"(v0_), "{v[4:7]}"(v4_), "{v[92:95]}"(v92_), "{v[96:99]}"(v96_), "{v[100:103]}"(v100_), "{v[104:107]}"(v104_) : "memory", "v255", "a255", "s95");
    __builtin_unreachable();
}
DEV void vo_role(const MegaArgs& A, const int L, const int h, const int rep) {
    ROLE_HEAD;
    const unsigned um = xl(RG_UM + ((8 * w + lq) * 4 + (h >> 3)) * 2048 + (h & 7) * 256 + l16 * 16, L);
#define XU 0
    const unsigned umx = XU ? OOB : xl(RG_UMX + ((4 * (2 * w + (lq & 1)) + (l16 >> 2)) * 4 + (h >> 3)) * 512 + (h & 7) * 64 + (l & 3) * 16 + (lq >> 1) * 8, L);
    bst16(rr, um, ones); bst16(rr, um + 65536, ones); bst8(rr, umx, ones2);
    DRAIN_BARRIER();
    bst4(rr, threadIdx.x < 4 ? xl(Z_QV + l * 256 + (32 + h) * 4, L) : OOB, 1u);
    const u32x4 s0_ = {RFL(rx[0]), RFL(rx[1]), RFL(rx[2]), RFL(rx[3])};
    const u32x4 s4_ = {RFL(lo(A.Wo + h * 192 * 640)), RFL(hi(A.Wo + h * 192 * 640)), RFL(192u * 1280u), RFL(0x20000u)};
    const u32x4 s8_ = {RFL(lo(A.Wv + h * 192)), RFL(hi(A.Wv + h * 192)), RFL(WBYTES - h * 384), RFL(0x20000u)};
    const u32x4 s12_ = {RFL(w), RFL(L << 13), RFL(L), RFL(xl(RG_MEM + rep * 16384, L) + w * 2048)};
    const u32x4 s16_ = {RFL(w < 2 ? xl(RG_MEM + rep * 16384 + 8192, L) + w * 1024 - 2048 : OOB), RFL(w * 13312 + 8192), RFL(REC0 + w * 2048), RFL(QT0 + h * 32768 + 16384 + w * 1024)};
    const u32x4 s20_ = {RFL(146432 + w * 1024), RFL(xl(RG_UT + (32 * w + (h >> 3)) * 8192 + (h & 7) * 1024, L)), RFL(xl(Z_X + 8192, L)), RFL(xl(Z_CT + (h & 7) * 256, L))};
    const u32x4 s24_ = {RFL(DBG0 + ((L * 128 + 32 + h) * 4 + w) * 2048), RFL(0u), RFL(0u), RFL(0u)};
    const u32x4 v0_ = {(unsigned)(((3 * w + 0) * 64 + l) / 24 * 12288 + ((((3 * w + 0) * 64 + l) % 24) ^ ((((3 * w + 0) * 64 + l) / 48) & 6)) * 16 - 0), (unsigned)(((3 * w + 1) * 64 + l) / 24 * 12288 + ((((3 * w + 1) * 64 + l) % 24) ^ ((((3 * w + 1) * 64 + l) / 48) & 6)) * 16 - 1024), (unsigned)(((3 * w + 2) * 64 + l) / 24 * 12288 + ((((3 * w + 2) * 64 + l) % 24) ^ ((((3 * w + 2) * 64 + l) / 48) & 6)) * 16 - 2048), (unsigned)((8 * lq + ((l >> 2) & 1) + ((l >> 1) & 4)) * 384 + (w ^ (((8 * lq + ((l >> 2) & 1) + ((l >> 1) & 4)) >> 2) & 3)) * 32 + (l & 3) * 8 + ((w + 0) & 3) * 12288)};
    const u32x4 v4_ = {(unsigned)((8 * lq + ((l >> 2) & 1) + ((l >> 1) & 4)) * 384 + (w ^ (((8 * lq + ((l >> 2) & 1) + ((l >> 1) & 4)) >> 2) & 3)) * 32 + (l & 3) * 8 + ((w + 1) & 3) * 12288), (unsigned)((8 * lq + ((l >> 2) & 1) + ((l >> 1) & 4)) * 384 + (w ^ (((8 * lq + ((l >> 2) & 1) + ((l >> 1) & 4)) >> 2) & 3)) * 32 + (l & 3) * 8 + ((w + 2) & 3) * 12288), (unsigned)((8 * lq + ((l >> 2) & 1) + ((l >> 1) & 4)) * 384 + (w ^ (((8 * lq + ((l >> 2) & 1) + ((l >> 1) & 4)) >> 2) & 3)) * 32 + (l & 3) * 8 + ((w + 3) & 3) * 12288), (unsigned)((8 * lq + ((l >> 2) & 1) + ((l >> 1) & 4)) * 384 + (w ^ (((8 * lq + ((l >> 2) & 1) + ((l >> 1) & 4)) >> 2) & 3)) * 32 + (l & 3) * 8)};
    const u32x4 v8_ = {(unsigned)(0), (unsigned)(0), (unsigned)(0), (unsigned)(0)};
    const u32x4 v12_ = {(unsigned)((4 * 0 + lq) * 1280 + 256 * w + ((l16 ^ (2 * ((4 * 0 + lq) & 7))) * 16) - 0), (unsigned)((4 * 1 + lq) * 1280 + 256 * w + ((l16 ^ (2 * ((4 * 1 + lq) & 7))) * 16) - 1024), (unsigned)((4 * 2 + lq) * 1280 + 256 * w + ((l16 ^ (2 * ((4 * 2 + lq) & 7))) * 16) - 2048), (unsigned)((4 * 3 + lq) * 1280 + 256 * w + ((l16 ^ (2 * ((4 * 3 + lq) & 7))) * 16) - 3072)};
    const u32x4 v16_ = {(unsigned)((4 * w + lq) * 1280 + 1024 + ((l16 ^ (2 * ((4 * w + lq) & 7))) * 16)), (unsigned)(w * 8192 + (l >> 2) * 256 + ((l >> 2) & 7) * 32 + (l & 3) * 8), (unsigned)(32768 + (l >> 2) * 256 + (((l >> 2) & 7) ^ (2 * w)) * 32 + (l & 3) * 8), (unsigned)(0u)};
    const u32x4 v92_ = {(unsigned)(l * 16 + ((w + 0) & 3) * 13312), (unsigned)(l * 16 + ((w + 1) & 3) * 13312), (unsigned)(l * 16 + ((w + 2) & 3) * 13312), (unsigned)(l * 16 + ((w + 3) & 3) * 13312)};
    const u32x4 v96_ = {(unsigned)(l * 16), (unsigned)(l * 16 + 64512 + w * 20480), (unsigned)((l & 2) ? 0xeeeeu : 0x4444u), (unsigned)(61440 + 512 * w + (l16 >> 2) * 64 + (l & 1) * 32 + sw2(lq) * 8 + (l & 2) * 2)};
    const u32x4 v100_ = {(unsigned)(61440 + 2048 + 256 * w + (l16 >> 2) * 64 + (l & 1) * 32 + sw2(lq) * 8 + (l & 2) * 2), (unsigned)(61440 + 512 * (lq >> 1) + 128 * (lq & 1) + (l & 4) * 8 + (l & 8) * 32 + (l & 3) * 8), (unsigned)(um), (unsigned)(umx)};
    const u32x4 v104_ = {(unsigned)(xl(RG_UTX + ((8 * w + (l16 >> 2)) * 4 + (h >> 3)) * 2048 + (h & 1) * 1024 + ((h & 7) >> 1) * 256 + lq * 64 + (l & 3) * 16, L)), (unsigned)(l < 4 || l > 8 ? xl(Z_QW + h * 128 + (l & 3) * 4, L) : (L ? xl(Z_XN + rep * 2048 + (l - 4) * 128, 0) : OOB)), (unsigned)(146432 + l * 16), (unsigned)(148480 + l * 16)};
    asm volatile(
    "s_mov_b32 s28, 0\n"
    "s_cmp_eq_u32 s13, 0\n"
    "s_cbranch_scc1 .Lvo_ho1_%=\n"
    "s_sleep 35\n"
    "s_sleep 35\n"
    "s_sleep 35\n"
    "s_sleep 35\n"
    "s_sleep 35\n"
    "s_sleep 35\n"
    "s_sleep 35\n"
    "s_sleep 35\n"
    "s_sleep 35\n"
    "s_sleep 35\n"
    "s_sleep 35\n"
    "s_sleep 35\n"
    "s_sleep 35\n"
    "s_sleep 35\n"
    ".Lvo_ho1_%=:\n"
    "s_mul_i32 s41, s12, 3072\n"
    "s_lshl_b32 s42, s12, 13\n"
    "s_lshl_b32 s43, s12, 10\n"
    "s_mov_b32 s49, 0\n"
    "s_add_u32 m0, s42, 0\n"
    "s_nop 0\n"
    "buffer_load_dwordx4 v12, s[4:7], s49 offen offset:0 nt lds\n"
    "buffer_load_dwordx4 v13, s[4:7], s49 offen offset:1024 nt lds\n"
    "buffer_load_dwordx4 v14, s[4:7], s49 offen offset:2048 nt lds\n"
    "buffer_load_dwordx4 v15, s[4:7], s49 offen offset:3072 nt lds\n"
    "s_add_u32 m0, s43, 32768\n"
    "s_nop 0\n"
    "buffer_load_dwordx4 v16, s[4:7], s49 offen nt lds\n"
    "s_mov_b32 s49, 81920\n"
    "s_add_u32 m0, s42, 4096\n"
    "s_nop 0\n"
    "buffer_load_dwordx4 v12, s[4:7], s49 offen offset:0 nt lds\n"
    "buffer_load_dwordx4 v13, s[4:7], s49 offen offset:1024 nt lds\n"
    "buffer_load_dwordx4 v14, s[4:7], s49 offen offset:2048 nt lds\n"
    "buffer_load_dwordx4 v15, s[4:7], s49 offen offset:3072 nt lds\n"
    "s_add_u32 m0, s43, 36864\n"
    "s_nop 0\n"
    "buffer_load_dwordx4 v16, s[4:7], s49 offen nt lds\n"
    "s_mov_b32 s49, 20480\n"
    "s_add_u32 m0, s42, 49152\n"
    "s_nop 0\n"
    "buffer_load_dwordx4 v12, s[4:7], s49 offen offset:0 nt lds\n"
    "buffer_load_dwordx4 v13, s[4:7], s49 offen offset:1024 nt lds\n"
    "buffer_load_dwordx4 v14, s[4:7], s49 offen offset:2048 nt lds\n"
    "buffer_load_dwordx4 v15, s[4:7], s49 offen offset:3072 nt lds\n"
    "s_add_u32 m0, s43, 81920\n"
    "s_nop 0\n"
    "buffer_load_dwordx4 v16, s[4:7], s49 offen nt lds\n"
    "s_mov_b32 s49, 102400\n"
    "s_add_u32 m0, s42, 53248\n"
    "s_nop 0\n"
    "buffer_load_dwordx4 v12, s[4:7], s49 offen offset:0 nt lds\n"
    "buffer_load_dwordx4 v13, s[4:7], s49 offen offset:1024 nt lds\n"
    "buffer_load_dwordx4 v14, s[4:7], s49 offen offset:2048 nt lds\n"
    "buffer_load_dwordx4 v15, s[4:7], s49 offen offset:3072 nt lds\n"
    "s_add_u32 m0, s43, 86016\n"
    "s_nop 0\n"
    "buffer_load_dwordx4 v16, s[4:7], s49 offen nt lds\n"
    "s_mov_b32 s49, 40960\n"
    "s_add_u32 m0, s42, 98304\n"
    "s_nop 0\n"
    "buffer_load_dwordx4 v12, s[4:7], s49 offen offset:0 nt lds\n"
    "buffer_load_dwordx4 v13, s[4:7], s49 offen offset:1024 nt lds\n"
    "buffer_load_dwordx4 v14, s[4:7], s49 offen offset:2048 nt lds\n"
    "buffer_load_dwordx4 v15, s[4:7], s49 offen offset:3072 nt lds\n"
    "s_add_u32 m0, s43, 131072\n"
    "s_nop 0\n"
    "buffer_load_dwordx4 v16, s[4:7], s49 offen nt lds\n"
    "s_mov_b32 s49, 122880\n"
    "s_add_u32 m0, s42, 102400\n"
    "s_nop 0\n"
    "buffer_load_dwordx4 v12, s[4:7], s49 offen offset:0 nt lds\n"
    "buffer_load_dwordx4 v13, s[4:7], s49 offen offset:1024 nt lds\n"
    "buffer_load_dwordx4 v14, s[4:7], s49 offen offset:2048 nt lds\n"
    "buffer_load_dwordx4 v15, s[4:7], s49 offen offset:3072 nt lds\n"
    "s_add_u32 m0, s43, 135168\n"
    "s_nop 0\n"
    "buffer_load_dwordx4 v16, s[4:7], s49 offen nt lds\n"
    "s_waitcnt vmcnt(20)\n"
    "s_barrier\n"
    "v_add_u32 v24, 0, v17\n"
    "v_add_u32 v32, 0, v18\n"
    "v_xor_b32 v33, 32, v32\n"
    "v_xor_b32 v25, 32, v24\n"
    "v_xor_b32 v26, 64, v24\n"
    "v_xor_b32 v27, 96, v24\n"
    "v_xor_b32 v28, 128, v24\n"
    "v_xor_b32 v29, 160, v24\n"
    "v_xor_b32 v30, 192, v24\n"
    "v_xor_b32 v31, 224, v24\n"
    "ds_read_b64_tr_b16 a[0:1], v24 offset:0\n"
    "ds_read_b64_tr_b16 a[2:3], v24 offset:4096\n"
    "ds_read_b64_tr_b16 a[24:25], v25 offset:0\n"
    "ds_read_b64_tr_b16 a[26:27], v25 offset:4096\n"
    "ds_read_b64_tr_b16 a[48:49], v26 offset:0\n"
    "ds_read_b64_tr_b16 a[50:51], v26 offset:4096\n"
    "ds_read_b64_tr_b16 a[72:73], v27 offset:0\n"
    "ds_read_b64_tr_b16 a[74:75], v27 offset:4096\n"
    "ds_read_b64_tr_b16 a[96:97], v28 offset:0\n"
    "ds_read_b64_tr_b16 a[98:99], v28 offset:4096\n"
    "ds_read_b64_tr_b16 a[120:121], v29 offset:0\n"
    "ds_read_b64_tr_b16 a[122:123], v29 offset:4096\n"
    "ds_read_b64_tr_b16 a[144:145], v30 offset:0\n"
    "ds_read_b64_tr_b16 a[146:147], v30 offset:4096\n"
    "ds_read_b64_tr_b16 a[168:169], v31 offset:0\n"
    "ds_read_b64_tr_b16 a[170:171], v31 offset:4096\n"
    "ds_read_b64_tr_b16 a[192:193], v32 offset:0\n"
    "ds_read_b64_tr_b16 a[194:195], v32 offset:4096\n"
    "ds_read_b64_tr_b16 a[216:217], v33 offset:0\n"
    "ds_read_b64_tr_b16 a[218:219], v33 offset:4096\n"
    "s_waitcnt lgkmcnt(0)\n"
    "s_barrier\n"
    "s_mov_b32 s49, 61440\n"
    "s_add_u32 m0, s42, 0\n"
    "s_nop 0\n"
    "buffer_load_dwordx4 v12, s[4:7], s49 offen offset:0 nt lds\n"
    "buffer_load_dwordx4 v13, s[4:7], s49 offen offset:1024 nt lds\n"
    "buffer_load_dwordx4 v14, s[4:7], s49 offen offset:2048 nt lds\n"
    "buffer_load_dwordx4 v15, s[4:7], s49 offen offset:3072 nt lds\n"
    "s_add_u32 m0, s43, 32768\n"
    "s_nop 0\n"
    "buffer_load_dwordx4 v16, s[4:7], s49 offen nt lds\n"
    "s_mov_b32 s49, 143360\n"
    "s_add_u32 m0, s42, 4096\n"
    "s_nop 0\n"
    "buffer_load_dwordx4 v12, s[4:7], s49 offen offset:0 nt lds\n"
    "buffer_load_dwordx4 v13, s[4:7], s49 offen offset:1024 nt lds\n"
    "buffer_load_dwordx4 v14, s[4:7], s49 offen offset:2048 nt lds\n"
    "buffer_load_dwordx4 v15, s[4:7], s49 offen offset:3072 nt lds\n"
    "s_add_u32 m0, s43, 36864\n"
    "s_nop 0\n"
    "buffer_load_dwordx4 v16, s[4:7], s49 offen nt lds\n"
    "s_waitcnt vmcnt(20)\n"
    "s_barrier\n"
    "v_add_u32 v24, 49152, v17\n"
    "v_add_u32 v32, 49152, v18\n"
    "v_xor_b32 v33, 32, v32\n"
    "v_xor_b32 v25, 32, v24\n"
    "v_xor_b32 v26, 64, v24\n"
    "v_xor_b32 v27, 96, v24\n"
    "v_xor_b32 v28, 128, v24\n"
    "v_xor_b32 v29, 160, v24\n"
    "v_xor_b32 v30, 192, v24\n"
    "v_xor_b32 v31, 224, v24\n"
    "ds_read_b64_tr_b16 a[4:5], v24 offset:0\n"
    "ds_read_b64_tr_b16 a[6:7], v24 offset:4096\n"
    "ds_read_b64_tr_b16 a[28:29], v25 offset:0\n"
    "ds_read_b64_tr_b16 a[30:31], v25 offset:4096\n"
    "ds_read_b64_tr_b16 a[52:53], v26 offset:0\n"
    "ds_read_b64_tr_b16 a[54:55], v26 offset:4096\n"
    "ds_read_b64_tr_b16 a[76:77], v27 offset:0\n"
    "ds_read_b64_tr_b16 a[78:79], v27 offset:4096\n"
    "ds_read_b64_tr_b16 a[100:101], v28 offset:0\n"
    "ds_read_b64_tr_b16 a[102:103], v28 offset:4096\n"
    "ds_read_b64_tr_b16 a[124:125], v29 offset:0\n"
    "ds_read_b64_tr_b16 a[126:127], v29 offset:4096\n"
    "ds_read_b64_tr_b16 a[148:149], v30 offset:0\n"
    "ds_read_b64_tr_b16 a[150:151], v30 offset:4096\n"
    "ds_read_b64_tr_b16 a[172:173], v31 offset:0\n"
    "ds_read_b64_tr_b16 a[174:175], v31 offset:4096\n"
    "ds_read_b64_tr_b16 a[196:197], v32 offset:0\n"
    "ds_read_b64_tr_b16 a[198:199], v32 offset:4096\n"
    "ds_read_b64_tr_b16 a[220:221], v33 offset:0\n"
    "ds_read_b64_tr_b16 a[222:223], v33 offset:4096\n"
    "s_waitcnt lgkmcnt(0)\n"
    "s_barrier\n"
    "s_mov_b32 s49, 163840\n"
    "s_add_u32 m0, s42, 49152\n"
    "s_nop 0\n"
    "buffer_load_dwordx4 v12, s[4:7], s49 offen offset:0 nt lds\n"
    "buffer_load_dwordx4 v13, s[4:7], s49 offen offset:1024 nt lds\n"
    "buffer_load_dwordx4 v14, s[4:7], s49 offen offset:2048 nt lds\n"
    "buffer_load_dwordx4 v15, s[4:7], s49 offen offset:3072 nt lds\n"
    "s_add_u32 m0, s43, 81920\n"
    "s_nop 0\n"
    "buffer_load_dwordx4 v16, s[4:7], s49 offen nt lds\n"
    "s_mov_b32 s49, 184320\n"
    "s_add_u32 m0, s42, 53248\n"
    "s_nop 0\n"
    "buffer_load_dwordx4 v12, s[4:7], s49 offen offset:0 nt lds\n"
    "buffer_load_dwordx4 v13, s[4:7], s49 offen offset:1024 nt lds\n"
    "buffer_load_dwordx4 v14, s[4:7], s49 offen offset:2048 nt lds\n"
    "buffer_load_dwordx4 v15, s[4:7], s49 offen offset:3072 nt lds\n"
    "s_add_u32 m0, s43, 86016\n"
    "s_nop 0\n"
    "buffer_load_dwordx4 v16, s[4:7], s49 offen nt lds\n"
    "s_waitcnt vmcnt(20)\n"
    "s_barrier\n"
    "v_add_u32 v24, 98304, v17\n"
    "v_add_u32 v32, 98304, v18\n"
    "v_xor_b32 v33, 32, v32\n"
    "v_xor_b32 v25, 32, v24\n"
    "v_xor_b32 v26, 64, v24\n"
    "v_xor_b32 v27, 96, v24\n"
    "v_xor_b32 v28, 128, v24\n"
    "v_xor_b32 v29, 160, v24\n"
    "v_xor_b32 v30, 192, v24\n"
    "v_xor_b32 v31, 224, v24\n"
    "ds_read_b64_tr_b16 a[8:9], v24 offset:0\n"
    "ds_read_b64_tr_b16 a[10:11], v24 offset:4096\n"
    "ds_read_b64_tr_b16 a[32:33], v25 offset:0\n"
    "ds_read_b64_tr_b16 a[34:35], v25 offset:4096\n"
    "ds_read_b64_tr_b16 a[56:57], v26 offset:0\n"
    "ds_read_b64_tr_b16 a[58:59], v26 offset:4096\n"
    "ds_read_b64_tr_b16 a[80:81], v27 offset:0\n"
    "ds_read_b64_tr_b16 a[82:83], v27 offset:4096\n"
    "ds_read_b64_tr_b16 a[104:105], v28 offset:0\n"
    "ds_read_b64_tr_b16 a[106:107], v28 offset:4096\n"
    "ds_read_b64_tr_b16 a[128:129], v29 offset:0\n"
    "ds_read_b64_tr_b16 a[130:131], v29 offset:4096\n"
    "ds_read_b64_tr_b16 a[152:153], v30 offset:0\n"
    "ds_read_b64_tr_b16 a[154:155], v30 offset:4096\n"
    "ds_read_b64_tr_b16 a[176:177], v31 offset:0\n"
    "ds_read_b64_tr_b16 a[178:179], v31 offset:4096\n"
    "ds_read_b64_tr_b16 a[200:201], v32 offset:0\n"
    "ds_read_b64_tr_b16 a[202:203], v32 offset:4096\n"
    "ds_read_b64_tr_b16 a[224:225], v33 offset:0\n"
    "ds_read_b64_tr_b16 a[226:227], v33 offset:4096\n"
    "s_waitcnt lgkmcnt(0)\n"
    "s_barrier\n"
    "s_mov_b32 s49, 204800\n"
    "s_add_u32 m0, s42, 98304\n"
    "s_nop 0\n"
    "buffer_load_dwordx4 v12, s[4:7], s49 offen offset:0 nt lds\n"
    "buffer_load_dwordx4 v13, s[4:7], s49 offen offset:1024 nt lds\n"
    "buffer_load_dwordx4 v14, s[4:7], s49 offen offset:2048 nt lds\n"
    "buffer_load_dwordx4 v15, s[4:7], s49 offen offset:3072 nt lds\n"
    "s_add_u32 m0, s43, 131072\n"
    "s_nop 0\n"
    "buffer_load_dwordx4 v16, s[4:7], s49 offen nt lds\n"
    "s_mov_b32 s49, 225280\n"
    "s_add_u32 m0, s42, 102400\n"
    "s_nop 0\n"
    "buffer_load_dwordx4 v12, s[4:7], s49 offen offset:0 nt lds\n"
    "buffer_load_dwordx4 v13, s[4:7], s49 offen offset:1024 nt lds\n"
    "buffer_load_dwordx4 v14, s[4:7], s49 offen offset:2048 nt lds\n"
    "buffer_load_dwordx4 v15, s[4:7], s49 offen offset:3072 nt lds\n"
    "s_add_u32 m0, s43, 135168\n"
    "s_nop 0\n"
    "buffer_load_dwordx4 v16, s[4:7], s49 offen nt lds\n"
    "s_waitcnt vmcnt(20)\n"
    "s_barrier\n"
    "v_add_u32 v24, 0, v17\n"
    "v_add_u32 v32, 0, v18\n"
    "v_xor_b32 v33, 32, v32\n"
    "v_xor_b32 v25, 32, v24\n"
    "v_xor_b32 v26, 64, v24\n"
    "v_xor_b32 v27, 96, v24\n"
    "v_xor_b32 v28, 128, v24\n"
    "v_xor_b32 v29, 160, v24\n"
    "v_xor_b32 v30, 192, v24\n"
    "v_xor_b32 v31, 224, v24\n"
    "ds_read_b64_tr_b16 a[12:13], v24 offset:0\n"
    "ds_read_b64_tr_b16 a[14:15], v24 offset:4096\n"
    "ds_read_b64_tr_b16 a[36:37], v25 offset:0\n"
    "ds_read_b64_tr_b16 a[38:39], v25 offset:4096\n"
    "ds_read_b64_tr_b16 a[60:61], v26 offset:0\n"
    "ds_read_b64_tr_b16 a[62:63], v26 offset:4096\n"
    "ds_read_b64_tr_b16 a[84:85], v27 offset:0\n"
    "ds_read_b64_tr_b16 a[86:87], v27 offset:4096\n"
    "ds_read_b64_tr_b16 a[108:109], v28 offset:0\n"
    "ds_read_b64_tr_b16 a[110:111], v28 offset:4096\n"
    "ds_read_b64_tr_b16 a[132:133], v29 offset:0\n"
    "ds_read_b64_tr_b16 a[134:135], v29 offset:4096\n"
    "ds_read_b64_tr_b16 a[156:157], v30 offset:0\n"
    "ds_read_b64_tr_b16 a[158:159], v30 offset:4096\n"
    "ds_read_b64_tr_b16 a[180:181], v31 offset:0\n"
    "ds_read_b64_tr_b16 a[182:183], v31 offset:4096\n"
    "ds_read_b64_tr_b16 a[204:205], v32 offset:0\n"
    "ds_read_b64_tr_b16 a[206:207], v32 offset:4096\n"
    "ds_read_b64_tr_b16 a[228:229], v33 offset:0\n"
    "ds_read_b64_tr_b16 a[230:231], v33 offset:4096\n"
    "s_waitcnt lgkmcnt(0)\n"
    "s_barrier\n"
    "s_mov_b32 s49, 0\n"
    "s_add_u32 m0, s41, 0\n"
    "s_nop 0\n"
    "buffer_load_dwordx4 v0, s[8:11], s49 offen offset:0 nt lds\n"
    "buffer_load_dwordx4 v1, s[8:11], s49 offen offset:1024 nt lds\n"
    "buffer_load_dwordx4 v2, s[8:11], s49 offen offset:2048 nt lds\n"
    "s_mov_b32 s49, 1572864\n"
    "s_add_u32 m0, s41, 12288\n"
    "s_nop 0\n"
    "buffer_load_dwordx4 v0, s[8:11], s49 offen offset:0 nt lds\n"
    "buffer_load_dwordx4 v1, s[8:11], s49 offen offset:1024 nt lds\n"
    "buffer_load_dwordx4 v2, s[8:11], s49 offen offset:2048 nt lds\n"
    "s_mov_b32 s49, 3145728\n"
    "s_add_u32 m0, s41, 24576\n"
    "s_nop 0\n"
    "buffer_load_dwordx4 v0, s[8:11], s49 offen offset:0 nt lds\n"
    "buffer_load_dwordx4 v1, s[8:11], s49 offen offset:1024 nt lds\n"
    "buffer_load_dwordx4 v2, s[8:11], s49 offen offset:2048 nt lds\n"
    "s_mov_b32 s49, 4718592\n"
    "s_add_u32 m0, s41, 36864\n"
    "s_nop 0\n"
    "buffer_load_dwordx4 v0, s[8:11], s49 offen offset:0 nt lds\n"
    "buffer_load_dwordx4 v1, s[8:11], s49 offen offset:1024 nt lds\n"
    "buffer_load_dwordx4 v2, s[8:11], s49 offen offset:2048 nt lds\n"
    "s_waitcnt vmcnt(22)\n"
    "s_barrier\n"
    "v_add_u32 v24, 49152, v17\n"
    "v_add_u32 v32, 49152, v18\n"
    "v_xor_b32 v33, 32, v32\n"
    "v_xor_b32 v25, 32, v24\n"
    "v_xor_b32 v26, 64, v24\n"
    "v_xor_b32 v27, 96, v24\n"
    "v_xor_b32 v28, 128, v24\n"
    "v_xor_b32 v29, 160, v24\n"
    "v_xor_b32 v30, 192, v24\n"
    "v_xor_b32 v31, 224, v24\n"
    "ds_read_b64_tr_b16 a[16:17], v24 offset:0\n"
    "ds_read_b64_tr_b16 a[18:19], v24 offset:4096\n"
    "ds_read_b64_tr_b16 a[40:41], v25 offset:0\n"
    "ds_read_b64_tr_b16 a[42:43], v25 offset:4096\n"
    "ds_read_b64_tr_b16 a[64:65], v26 offset:0\n"
    "ds_read_b64_tr_b16 a[66:67], v26 offset:4096\n"
    "ds_read_b64_tr_b16 a[88:89], v27 offset:0\n"
    "ds_read_b64_tr_b16 a[90:91], v27 offset:4096\n"
    "ds_read_b64_tr_b16 a[112:113], v28 offset:0\n"
    "ds_read_b64_tr_b16 a[114:115], v28 offset:4096\n"
    "ds_read_b64_tr_b16 a[136:137], v29 offset:0\n"
    "ds_read_b64_tr_b16 a[138:139], v29 offset:4096\n"
    "ds_read_b64_tr_b16 a[160:161], v30 offset:0\n"
    "ds_read_b64_tr_b16 a[162:163], v30 offset:4096\n"
    "ds_read_b64_tr_b16 a[184:185], v31 offset:0\n"
    "ds_read_b64_tr_b16 a[186:187], v31 offset:4096\n"
    "ds_read_b64_tr_b16 a[208:209], v32 offset:0\n"
    "ds_read_b64_tr_b16 a[210:211], v32 offset:4096\n"
    "ds_read_b64_tr_b16 a[232:233], v33 offset:0\n"
    "ds_read_b64_tr_b16 a[234:235], v33 offset:4096\n"
    "s_waitcnt lgkmcnt(0)\n"
    "s_barrier\n"
    "s_mov_b32 s49, 393216\n"
    "s_add_u32 m0, s41, 49152\n"
    "s_nop 0\n"
    "buffer_load_dwordx4 v0, s[8:11], s49 offen offset:0 nt lds\n"
    "buffer_load_dwordx4 v1, s[8:11], s49 offen offset:1024 nt lds\n"
    "buffer_load_dwordx4 v2, s[8:11], s49 offen offset:2048 nt lds\n"
    "s_mov_b32 s49, 1966080\n"
    "s_add_u32 m0, s41, 61440\n"
    "s_nop 0\n"
    "buffer_load_dwordx4 v0, s[8:11], s49 offen offset:0 nt lds\n"
    "buffer_load_dwordx4 v1, s[8:11], s49 offen offset:1024 nt lds\n"
    "buffer_load_dwordx4 v2, s[8:11], s49 offen offset:2048 nt lds\n"
    "s_mov_b32 s49, 3538944\n"
    "s_add_u32 m0, s41, 73728\n"
    "s_nop 0\n"
    "buffer_load_dwordx4 v0, s[8:11], s49 offen offset:0 nt lds\n"
    "buffer_load_dwordx4 v1, s[8:11], s49 offen offset:1024 nt lds\n"
    "buffer_load_dwordx4 v2, s[8:11], s49 offen offset:2048 nt lds\n"
    "s_mov_b32 s49, 5111808\n"
    "s_add_u32 m0, s41, 86016\n"
    "s_nop 0\n"
    "buffer_load_dwordx4 v0, s[8:11], s49 offen offset:0 nt lds\n"
    "buffer_load_dwordx4 v1, s[8:11], s49 offen offset:1024 nt lds\n"
    "buffer_load_dwordx4 v2, s[8:11], s49 offen offset:2048 nt lds\n"
    "s_waitcnt vmcnt(24)\n"
    "s_barrier\n"
    "v_add_u32 v24, 98304, v17\n"
    "v_add_u32 v32, 98304, v18\n"
    "v_xor_b32 v33, 32, v32\n"
    "v_xor_b32 v25, 32, v24\n"
    "v_xor_b32 v26, 64, v24\n"
    "v_xor_b32 v27, 96, v24\n"
    "v_xor_b32 v28, 128, v24\n"
    "v_xor_b32 v29, 160, v24\n"
    "v_xor_b32 v30, 192, v24\n"
    "v_xor_b32 v31, 224, v24\n"
    "ds_read_b64_tr_b16 a[20:21], v24 offset:0\n"
    "ds_read_b64_tr_b16 a[22:23], v24 offset:4096\n"
    "ds_read_b64_tr_b16 a[44:45], v25 offset:0\n"
    "ds_read_b64_tr_b16 a[46:47], v25 offset:4096\n"
    "ds_read_b64_tr_b16 a[68:69], v26 offset:0\n"
    "ds_read_b64_tr_b16 a[70:71], v26 offset:4096\n"
    "ds_read_b64_tr_b16 a[92:93], v27 offset:0\n"
    "ds_read_b64_tr_b16 a[94:95], v27 offset:4096\n"
    "ds_read_b64_tr_b16 a[116:117], v28 offset:0\n"
    "ds_read_b64_tr_b16 a[118:119], v28 offset:4096\n"
    "ds_read_b64_tr_b16 a[140:141], v29 offset:0\n"
    "ds_read_b64_tr_b16 a[142:143], v29 offset:4096\n"
    "ds_read_b64_tr_b16 a[164:165], v30 offset:0\n"
    "ds_read_b64_tr_b16 a[166:167], v30 offset:4096\n"
    "ds_read_b64_tr_b16 a[188:189], v31 offset:0\n"
    "ds_read_b64_tr_b16 a[190:191], v31 offset:4096\n"
    "ds_read_b64_tr_b16 a[212:213], v32 offset:0\n"
    "ds_read_b64_tr_b16 a[214:215], v32 offset:4096\n"
    "ds_read_b64_tr_b16 a[236:237], v33 offset:0\n"
    "ds_read_b64_tr_b16 a[238:239], v33 offset:4096\n"
    "s_waitcnt lgkmcnt(0)\n"
    "s_barrier\n"
    "s_mov_b32 s49, 786432\n"
    "s_add_u32 m0, s41, 98304\n"
    "s_nop 0\n"
    "buffer_load_dwordx4 v0, s[8:11], s49 offen offset:0 nt lds\n"
    "buffer_load_dwordx4 v1, s[8:11], s49 offen offset:1024 nt lds\n"
    "buffer_load_dwordx4 v2, s[8:11], s49 offen offset:2048 nt lds\n"
    "s_mov_b32 s49, 2359296\n"
    "s_add_u32 m0, s41, 110592\n"
    "s_nop 0\n"
    "buffer_load_dwordx4 v0, s[8:11], s49 offen offset:0 nt lds\n"
    "buffer_load_dwordx4 v1, s[8:11], s49 offen offset:1024 nt lds\n"
    "buffer_load_dwordx4 v2, s[8:11], s49 offen offset:2048 nt lds\n"
    "s_mov_b32 s49, 3932160\n"
    "s_add_u32 m0, s41, 122880\n"
    "s_nop 0\n"
    "buffer_load_dwordx4 v0, s[8:11], s49 offen offset:0 nt lds\n"
    "buffer_load_dwordx4 v1, s[8:11], s49 offen offset:1024 nt lds\n"
    "buffer_load_dwordx4 v2, s[8:11], s49 offen offset:2048 nt lds\n"
    "s_mov_b32 s49, 5505024\n"
    "s_add_u32 m0, s41, 135168\n"
    "s_nop 0\n"
    "buffer_load_dwordx4 v0, s[8:11], s49 offen offset:0 nt lds\n"
    "buffer_load_dwordx4 v1, s[8:11], s49 offen offset:1024 nt lds\n"
    "buffer_load_dwordx4 v2, s[8:11], s49 offen offset:2048 nt lds\n"
    "s_waitcnt vmcnt(24)\n"
    "s_barrier\n"
    "v_add_u32 v8, 0, v3\n"
    "ds_read_b64_tr_b16 a[240:241], v8 offset:0\n"
    "ds_read_b64_tr_b16 a[242:243], v8 offset:768\n"
    "ds_read_b64_tr_b16 v[176:177], v8 offset:128\n"
    "ds_read_b64_tr_b16 v[178:179], v8 offset:896\n"
    "ds_read_b64_tr_b16 v[12:13], v8 offset:256\n"
    "ds_read_b64_tr_b16 v[14:15], v8 offset:1024\n"
    "v_add_u32 v9, 0, v4\n"
    "ds_read_b64_tr_b16 v[112:113], v9 offset:0\n"
    "ds_read_b64_tr_b16 v[114:115], v9 offset:768\n"
    "ds_read_b64_tr_b16 v[192:193], v9 offset:128\n"
    "ds_read_b64_tr_b16 v[194:195], v9 offset:896\n"
    "ds_read_b64_tr_b16 v[28:29], v9 offset:256\n"
    "ds_read_b64_tr_b16 v[30:31], v9 offset:1024\n"
    "v_add_u32 v10, 0, v5\n"
    "ds_read_b64_tr_b16 v[128:129], v10 offset:0\n"
    "ds_read_b64_tr_b16 v[130:131], v10 offset:768\n"
    "ds_read_b64_tr_b16 v[208:209], v10 offset:128\n"
    "ds_read_b64_tr_b16 v[210:211], v10 offset:896\n"
    "ds_read_b64_tr_b16 v[44:45], v10 offset:256\n"
    "ds_read_b64_tr_b16 v[46:47], v10 offset:1024\n"
    "v_add_u32 v11, 0, v6\n"
    "ds_read_b64_tr_b16 v[144:145], v11 offset:0\n"
    "ds_read_b64_tr_b16 v[146:147], v11 offset:768\n"
    "ds_read_b64_tr_b16 v[224:225], v11 offset:128\n"
    "ds_read_b64_tr_b16 v[226:227], v11 offset:896\n"
    "ds_read_b64_tr_b16 v[60:61], v11 offset:256\n"
    "ds_read_b64_tr_b16 v[62:63], v11 offset:1024\n"
    "s_waitcnt lgkmcnt(0)\n"
    "s_barrier\n"
    "s_mov_b32 s49, 1179648\n"
    "s_add_u32 m0, s41, 0\n"
    "s_nop 0\n"
    "buffer_load_dwordx4 v0, s[8:11], s49 offen offset:0 nt lds\n"
    "buffer_load_dwordx4 v1, s[8:11], s49 offen offset:1024 nt lds\n"
    "buffer_load_dwordx4 v2, s[8:11], s49 offen offset:2048 nt lds\n"
    "s_mov_b32 s49, 2752512\n"
    "s_add_u32 m0, s41, 12288\n"
    "s_nop 0\n"
    "buffer_load_dwordx4 v0, s[8:11], s49 offen offset:0 nt lds\n"
    "buffer_load_dwordx4 v1, s[8:11], s49 offen offset:1024 nt lds\n"
    "buffer_load_dwordx4 v2, s[8:11], s49 offen offset:2048 nt lds\n"
    "s_mov_b32 s49, 4325376\n"
    "s_add_u32 m0, s41, 24576\n"
    "s_nop 0\n"
    "buffer_load_dwordx4 v0, s[8:11], s49 offen offset:0 nt lds\n"
    "buffer_load_dwordx4 v1, s[8:11], s49 offen offset:1024 nt lds\n"
    "buffer_load_dwordx4 v2, s[8:11], s49 offen offset:2048 nt lds\n"
    "s_mov_b32 s49, 5898240\n"
    "s_add_u32 m0, s41, 36864\n"
    "s_nop 0\n"
    "buffer_load_dwordx4 v0, s[8:11], s49 offen offset:0 nt lds\n"
    "buffer_load_dwordx4 v1, s[8:11], s49 offen offset:1024 nt lds\n"
    "buffer_load_dwordx4 v2, s[8:11], s49 offen offset:2048 nt lds\n"
    "s_waitcnt vmcnt(24)\n"
    "s_barrier\n"
    "v_add_u32 v8, 49152, v3\n"
    "ds_read_b64_tr_b16 a[244:245], v8 offset:0\n"
    "ds_read_b64_tr_b16 a[246:247], v8 offset:768\n"
    "ds_read_b64_tr_b16 v[180:181], v8 offset:128\n"
    "ds_read_b64_tr_b16 v[182:183], v8 offset:896\n"
    "ds_read_b64_tr_b16 v[16:17], v8 offset:256\n"
    "ds_read_b64_tr_b16 v[18:19], v8 offset:1024\n"
    "v_add_u32 v9, 49152, v4\n"
    "ds_read_b64_tr_b16 v[116:117], v9 offset:0\n"
    "ds_read_b64_tr_b16 v[118:119], v9 offset:768\n"
    "ds_read_b64_tr_b16 v[196:197], v9 offset:128\n"
    "ds_read_b64_tr_b16 v[198:199], v9 offset:896\n"
    "ds_read_b64_tr_b16 v[32:33], v9 offset:256\n"
    "ds_read_b64_tr_b16 v[34:35], v9 offset:1024\n"
    "v_add_u32 v10, 49152, v5\n"
    "ds_read_b64_tr_b16 v[132:133], v10 offset:0\n"
    "ds_read_b64_tr_b16 v[134:135], v10 offset:768\n"
    "ds_read_b64_tr_b16 v[212:213], v10 offset:128\n"
    "ds_read_b64_tr_b16 v[214:215], v10 offset:896\n"
    "ds_read_b64_tr_b16 v[48:49], v10 offset:256\n"
    "ds_read_b64_tr_b16 v[50:51], v10 offset:1024\n"
    "v_add_u32 v11, 49152, v6\n"
    "ds_read_b64_tr_b16 v[148:149], v11 offset:0\n"
    "ds_read_b64_tr_b16 v[150:151], v11 offset:768\n"
    "ds_read_b64_tr_b16 v[228:229], v11 offset:128\n"
    "ds_read_b64_tr_b16 v[230:231], v11 offset:896\n"
    "ds_read_b64_tr_b16 v[64:65], v11 offset:256\n"
    "ds_read_b64_tr_b16 v[66:67], v11 offset:1024\n"
    "s_waitcnt lgkmcnt(0)\n"
    "s_barrier\n"
    "s_mov_b32 s49, 6291456\n"
    "s_add_u32 m0, s41, 49152\n"
    "s_nop 0\n"
    "buffer_load_dwordx4 v0, s[8:11], s49 offen offset:0 nt lds\n"
    "buffer_load_dwordx4 v1, s[8:11], s49 offen offset:1024 nt lds\n"
    "buffer_load_dwordx4 v2, s[8:11], s49 offen offset:2048 nt lds\n"
    "s_mov_b32 s49, 6684672\n"
    "s_add_u32 m0, s41, 61440\n"
    "s_nop 0\n"
    "buffer_load_dwordx4 v0, s[8:11], s49 offen offset:0 nt lds\n"
    "buffer_load_dwordx4 v1, s[8:11], s49 offen offset:1024 nt lds\n"
    "buffer_load_dwordx4 v2, s[8:11], s49 offen offset:2048 nt lds\n"
    "s_mov_b32 s49, 7077888\n"
    "s_add_u32 m0, s41, 73728\n"
    "s_nop 0\n"
    "buffer_load_dwordx4 v0, s[8:11], s49 offen offset:0 nt lds\n"
    "buffer_load_dwordx4 v1, s[8:11], s49 offen offset:1024 nt lds\n"
    "buffer_load_dwordx4 v2, s[8:11], s49 offen offset:2048 nt lds\n"
    "s_mov_b32 s49, 7471104\n"
    "s_add_u32 m0, s41, 86016\n"
    "s_nop 0\n"
    "buffer_load_dwordx4 v0, s[8:11], s49 offen offset:0 nt lds\n"
    "buffer_load_dwordx4 v1, s[8:11], s49 offen offset:1024 nt lds\n"
    "buffer_load_dwordx4 v2, s[8:11], s49 offen offset:2048 nt lds\n"
    "s_waitcnt vmcnt(24)\n"
    "s_barrier\n"
    "v_add_u32 v8, 98304, v3\n"
    "ds_read_b64_tr_b16 a[248:249], v8 offset:0\n"
    "ds_read_b64_tr_b16 a[250:251], v8 offset:768\n"
    "ds_read_b64_tr_b16 v[184:185], v8 offset:128\n"
    "ds_read_b64_tr_b16 v[186:187], v8 offset:896\n"
    "ds_read_b64_tr_b16 v[20:21], v8 offset:256\n"
    "ds_read_b64_tr_b16 v[22:23], v8 offset:1024\n"
    "v_add_u32 v9, 98304, v4\n"
    "ds_read_b64_tr_b16 v[120:121], v9 offset:0\n"
    "ds_read_b64_tr_b16 v[122:123], v9 offset:768\n"
    "ds_read_b64_tr_b16 v[200:201], v9 offset:128\n"
    "ds_read_b64_tr_b16 v[202:203], v9 offset:896\n"
    "ds_read_b64_tr_b16 v[36:37], v9 offset:256\n"
    "ds_read_b64_tr_b16 v[38:39], v9 offset:1024\n"
    "v_add_u32 v10, 98304, v5\n"
    "ds_read_b64_tr_b16 v[136:137], v10 offset:0\n"
    "ds_read_b64_tr_b16 v[138:139], v10 offset:768\n"
    "ds_read_b64_tr_b16 v[216:217], v10 offset:128\n"
    "ds_read_b64_tr_b16 v[218:219], v10 offset:896\n"
    "ds_read_b64_tr_b16 v[52:53], v10 offset:256\n"
    "ds_read_b64_tr_b16 v[54:55], v10 offset:1024\n"
    "v_add_u32 v11, 98304, v6\n"
    "ds_read_b64_tr_b16 v[152:153], v11 offset:0\n"
    "ds_read_b64_tr_b16 v[154:155], v11 offset:768\n"
    "ds_read_b64_tr_b16 v[232:233], v11 offset:128\n"
    "ds_read_b64_tr_b16 v[234:235], v11 offset:896\n"
    "ds_read_b64_tr_b16 v[68:69], v11 offset:256\n"
    "ds_read_b64_tr_b16 v[70:71], v11 offset:1024\n"
    "s_waitcnt lgkmcnt(0)\n"
    "s_barrier\n"
    "s_waitcnt vmcnt(12)\n"
    "s_barrier\n"
    "v_add_u32 v8, 0, v3\n"
    "ds_read_b64_tr_b16 a[252:253], v8 offset:0\n"
    "ds_read_b64_tr_b16 a[254:255], v8 offset:768\n"
    "ds_read_b64_tr_b16 v[188:189], v8 offset:128\n"
    "ds_read_b64_tr_b16 v[190:191], v8 offset:896\n"
    "ds_read_b64_tr_b16 v[24:25], v8 offset:256\n"
    "ds_read_b64_tr_b16 v[26:27], v8 offset:1024\n"
    "v_add_u32 v9, 0, v4\n"
    "ds_read_b64_tr_b16 v[124:125], v9 offset:0\n"
    "ds_read_b64_tr_b16 v[126:127], v9 offset:768\n"
    "ds_read_b64_tr_b16 v[204:205], v9 offset:128\n"
    "ds_read_b64_tr_b16 v[206:207], v9 offset:896\n"
    "ds_read_b64_tr_b16 v[40:41], v9 offset:256\n"
    "ds_read_b64_tr_b16 v[42:43], v9 offset:1024\n"
    "v_add_u32 v10, 0, v5\n"
    "ds_read_b64_tr_b16 v[140:141], v10 offset:0\n"
    "ds_read_b64_tr_b16 v[142:143], v10 offset:768\n"
    "ds_read_b64_tr_b16 v[220:221], v10 offset:128\n"
    "ds_read_b64_tr_b16 v[222:223], v10 offset:896\n"
    "ds_read_b64_tr_b16 v[56:57], v10 offset:256\n"
    "ds_read_b64_tr_b16 v[58:59], v10 offset:1024\n"
    "v_add_u32 v11, 0, v6\n"
    "ds_read_b64_tr_b16 v[156:157], v11 offset:0\n"
    "ds_read_b64_tr_b16 v[158:159], v11 offset:768\n"
    "ds_read_b64_tr_b16 v[236:237], v11 offset:128\n"
    "ds_read_b64_tr_b16 v[238:239], v11 offset:896\n"
    "ds_read_b64_tr_b16 v[72:73], v11 offset:256\n"
    "ds_read_b64_tr_b16 v[74:75], v11 offset:1024\n"
    "s_waitcnt lgkmcnt(0)\n"
    "s_barrier\n"
    "s_waitcnt vmcnt(0)\n"
    "s_barrier\n"
    "v_add_u32 v8, 49152, v7\n"
    "ds_read_b64_tr_b16 v[160:161], v8 offset:0\n"
    "ds_read_b64_tr_b16 v[162:163], v8 offset:768\n"
    "ds_read_b64_tr_b16 v[240:241], v8 offset:128\n"
    "ds_read_b64_tr_b16 v[242:243], v8 offset:896\n"
    "ds_read_b64_tr_b16 v[76:77], v8 offset:256\n"
    "ds_read_b64_tr_b16 v[78:79], v8 offset:1024\n"
    "v_add_u32 v9, 61440, v7\n"
    "ds_read_b64_tr_b16 v[164:165], v9 offset:0\n"
    "ds_read_b64_tr_b16 v[166:167], v9 offset:768\n"
    "ds_read_b64_tr_b16 v[244:245], v9 offset:128\n"
    "ds_read_b64_tr_b16 v[246:247], v9 offset:896\n"
    "ds_read_b64_tr_b16 v[80:81], v9 offset:256\n"
    "ds_read_b64_tr_b16 v[82:83], v9 offset:1024\n"
    "v_add_u32 v10, 73728, v7\n"
    "ds_read_b64_tr_b16 v[168:169], v10 offset:0\n"
    "ds_read_b64_tr_b16 v[170:171], v10 offset:768\n"
    "ds_read_b64_tr_b16 v[248:249], v10 offset:128\n"
    "ds_read_b64_tr_b16 v[250:251], v10 offset:896\n"
    "ds_read_b64_tr_b16 v[84:85], v10 offset:256\n"
    "ds_read_b64_tr_b16 v[86:87], v10 offset:1024\n"
    "v_add_u32 v11, 86016, v7\n"
    "ds_read_b64_tr_b16 v[172:173], v11 offset:0\n"
    "ds_read_b64_tr_b16 v[174:175], v11 offset:768\n"
    "ds_read_b64_tr_b16 v[252:253], v11 offset:128\n"
    "ds_read_b64_tr_b16 v[254:255], v11 offset:896\n"
    "ds_read_b64_tr_b16 v[88:89], v11 offset:256\n"
    "ds_read_b64_tr_b16 v[90:91], v11 offset:1024\n"
    "s_waitcnt lgkmcnt(0)\n"
    "s_barrier\n"
    "ds_write_b128 v97, v[12:15] offset:0\n"
    "ds_write_b128 v97, v[16:19] offset:1024\n"
    "ds_write_b128 v97, v[20:23] offset:2048\n"
    "ds_write_b128 v97, v[24:27] offset:3072\n"
    "ds_write_b128 v97, v[28:31] offset:4096\n"
    "ds_write_b128 v97, v[32:35] offset:5120\n"
    "ds_write_b128 v97, v[36:39] offset:6144\n"
    "ds_write_b128 v97, v[40:43] offset:7168\n"
    "ds_write_b128 v97, v[44:47] offset:8192\n"
    "ds_write_b128 v97, v[48:51] offset:9216\n"
    "ds_write_b128 v97, v[52:55] offset:10240\n"
    "ds_write_b128 v97, v[56:59] offset:11264\n"
    "ds_write_b128 v97, v[60:63] offset:12288\n"
    "ds_write_b128 v97, v[64:67] offset:13312\n"
    "ds_write_b128 v97, v[68:71] offset:14336\n"
    "ds_write_b128 v97, v[72:75] offset:15360\n"
    "ds_write_b128 v97, v[76:79] offset:16384\n"
    "ds_write_b128 v97, v[80:83] offset:17408\n"
    "ds_write_b128 v97, v[84:87] offset:18432\n"
    "ds_write_b128 v97, v[88:91] offset:19456\n"
    "s_mov_b32 s34, -4\n"
    "s_lshl_b32 s48, s12, 11\n"
    "v_lshrrev_b32 v4, 2, v96\n"
    ".Lvo_po2_%=:\n"
    "buffer_load_dword v8, v4, s[0:3], s23 offen sc1\n"
    "s_waitcnt vmcnt(0)\n"
    "v_cmp_gt_i32 vcc, 1, v8\n"
    "s_nop 0\n"
    "s_cbranch_vccz .Lvo_pd3_%=\n"
    "s_add_u32 s28, s28, 1\n"
    "s_cmp_lt_u32 s28, 0x40000\n"
    "s_cbranch_scc1 .Lvo_sp4_%=\n"
    "v_mov_b32 v8, 30\n"
    "s_mov_b32 s32, 33728\n"
    "buffer_store_dword v8, off, s[0:3], s32 sc1\n"
    "s_waitcnt vmcnt(0)\n"
    "s_branch .Lvo_pd3_%=\n"
    ".Lvo_sp4_%=:\n"
    "s_sleep 4\n"
    "s_branch .Lvo_po2_%=\n"
    ".Lvo_pd3_%=:\n"
    "s_mov_b32 s50, 0\n"
    "s_mov_b32 s51, s14\n"
    "v_mov_b32 v111, s50\n"
    "s_mov_b64 exec, 0x1f0\n"
    "v_mov_b32 v111, s51\n"
    "s_mov_b64 exec, -1\n"
    ".Lvo_po5_%=:\n"
    "buffer_load_dword v110, v105, s[0:3], 0 offen sc1\n"
    "s_waitcnt vmcnt(0)\n"
    "v_cmp_gt_i32 vcc, v111, v110\n"
    "s_nop 0\n"
    "s_cbranch_vccz .Lvo_pd6_%=\n"
    "s_add_u32 s28, s28, 1\n"
    "s_cmp_lt_u32 s28, 0x40000\n"
    "s_cbranch_scc1 .Lvo_sp7_%=\n"
    "v_mov_b32 v110, 31\n"
    "s_mov_b32 s32, 33728\n"
    "buffer_store_dword v110, off, s[0:3], s32 sc1\n"
    "s_waitcnt vmcnt(0)\n"
    "s_branch .Lvo_pd6_%=\n"
    ".Lvo_sp7_%=:\n"
    "s_sleep 4\n"
    "s_branch .Lvo_po5_%=\n"
    ".Lvo_pd6_%=:\n"
    "s_add_u32 s43, s18, s13\n"
    "s_add_u32 m0, s48, 0\n"
    "s_nop 0\n"
    "buffer_load_dwordx4 v96, s[0:3], s43 offen sc1 lds\n"
    "buffer_load_dwordx4 v96, s[0:3], s43 offen offset:1024 sc1 lds\n"
    "s_add_u32 m0, s48, 13312\n"
    "s_add_u32 s43, s43, 0x4000\n"
    "buffer_load_dwordx4 v96, s[0:3], s43 offen sc1 lds\n"
    "buffer_load_dwordx4 v96, s[0:3], s43 offen offset:1024 sc1 lds\n"
    "s_add_u32 m0, s48, 26624\n"
    "s_add_u32 s43, s43, 0x4000\n"
    "buffer_load_dwordx4 v96, s[0:3], s43 offen sc1 lds\n"
    "buffer_load_dwordx4 v96, s[0:3], s43 offen offset:1024 sc1 lds\n"
    "s_add_u32 m0, s48, 39936\n"
    "s_add_u32 s43, s43, 0x4000\n"
    "buffer_load_dwordx4 v96, s[0:3], s43 offen sc1 lds\n"
    "buffer_load_dwordx4 v96, s[0:3], s43 offen offset:1024 sc1 lds\n"
    "s_add_u32 m0, s48, 53248\n"
    "s_add_u32 s43, s43, 0x4000\n"
    "buffer_load_dwordx4 v96, s[0:3], s43 offen sc1 lds\n"
    "buffer_load_dwordx4 v96, s[0:3], s43 offen offset:1024 sc1 lds\n"
    "s_memrealtime s[30:31]\n"
    "s_waitcnt vmcnt(0) lgkmcnt(0)\n"
    "s_barrier\n"
    "ds_read_b128 v[24:27], v92 offset:0\n"
    "ds_read_b128 v[28:31], v92 offset:1024\n"
    "ds_read_b128 v[32:35], v92 offset:2048\n"
    "ds_read_b128 v[36:39], v92 offset:3072\n"
    "ds_read_b128 v[40:43], v92 offset:4096\n"
    "ds_read_b128 v[44:47], v92 offset:5120\n"
    "s_sub_u32 s46, s30, 1360\n"
    "s_mov_b32 s34, -3\n"
    "s_mov_b32 s35, -5\n"
    "s_mov_b32 s45, s22\n"
    "s_mov_b32 s38, 2130706432\n"
    "s_mov_b32 s39, 2130706432\n"
    "s_waitcnt lgkmcnt(0)\n"
    "s_branch .Lvo_vph_%=\n"
    ".Lvo_take_%=:\n"
    "s_waitcnt vmcnt(3)\n"
    ".Lvo_mk0x7_%=:\n"
    "ds_read_b128 v[72:75], v92 offset:8192\n"
    "s_waitcnt vmcnt(2)\n"
    "ds_read_b128 v[76:79], v92 offset:9216\n"
    "s_waitcnt lgkmcnt(1)\n"
    "v_smfmac_f32_16x16x64_bf16 v[84:87], v[72:75], v[0:7], v98\n"
    "v_max_u32 v108, v72, v74\n"
    "s_waitcnt lgkmcnt(0)\n"
    "v_max3_u32 v108, v108, v76, v78\n"
    "v_cmp_eq_u32 vcc, -1, v108\n"
    "v_smfmac_f32_16x16x64_bf16 v[64:67], v[72:75], a[240:247], v98\n"
    "s_cbranch_vccnz .Lvo_retry_%=\n"
    ".Lvo_go_%=:\n"
    "s_barrier\n"
    ".Lvo_mk1x8_%=:\n"
    "ds_read_b128 v[88:91], v93 offset:8192\n"
    "v_smfmac_f32_16x16x64_bf16 v[68:71], v[72:75], v[176:183], v98\n"
    "ds_read_b128 v[32:35], v93 offset:9216\n"
    "ds_read_b128 v[36:39], v94 offset:8192\n"
    "v_smfmac_f32_16x16x64_bf16 v[84:87], v[76:79], v[8:15], v98\n"
    "s_waitcnt vmcnt(1)\n"
    "ds_read_b128 v[80:83], v92 offset:10240\n"
    "ds_read_b128 v[40:43], v94 offset:9216\n"
    "v_smfmac_f32_16x16x64_bf16 v[64:67], v[76:79], a[248:255], v98\n"
    "ds_read_b128 v[44:47], v95 offset:8192\n"
    "v_smfmac_f32_16x16x64_bf16 v[68:71], v[76:79], v[184:191], v98\n"
    "s_waitcnt lgkmcnt(5)\n"
    "v_smfmac_f32_16x16x64_bf16 v[84:87], v[88:91], v[16:23], v98\n"
    "ds_read_b128 v[0:3], v97 offset:12288\n"
    "ds_read_b128 v[4:7], v97 offset:13312\n"
    "v_smfmac_f32_16x16x64_bf16 v[64:67], v[88:91], v[112:119], v98\n"
    "ds_read_b128 v[8:11], v97 offset:14336\n"
    "v_smfmac_f32_16x16x64_bf16 v[68:71], v[88:91], v[192:199], v98\n"
    "s_waitcnt lgkmcnt(5)\n"
    "ds_read_b128 v[12:15], v97 offset:15360\n"
    "v_max_u32 v108, v80, v82\n"
    "v_smfmac_f32_16x16x64_bf16 v[84:87], v[32:35], v[24:31], v98\n"
    "ds_read_b128 v[16:19], v97 offset:16384\n"
    "v_cmp_eq_u32 vcc, -1, v108\n"
    "v_smfmac_f32_16x16x64_bf16 v[64:67], v[32:35], v[120:127], v98\n"
    "s_cbranch_vccnz .Lvo_r2_%=\n"
    ".Lvo_go2_%=:\n"
    "s_barrier\n"
    "v_smfmac_f32_16x16x64_bf16 v[68:71], v[32:35], v[200:207], v98\n"
    "s_waitcnt lgkmcnt(5)\n"
    "ds_read_b128 v[80:83], v95 offset:9216\n"
    "ds_read_b128 v[72:75], v96 offset:10240\n"
    "v_smfmac_f32_16x16x64_bf16 v[84:87], v[36:39], v[48:55], v98\n"
    "s_mov_b32 m0, s20\n"
    "ds_read_b128 v[76:79], v96 offset:23552\n"
    "ds_read_b128 v[20:23], v97 offset:17408\n"
    "v_smfmac_f32_16x16x64_bf16 v[64:67], v[36:39], v[128:135], v98\n"
    "s_waitcnt vmcnt(0)\n"
    "ds_read_b128 v[24:27], v97 offset:18432\n"
    "v_cmp_gt_i32 vcc, v111, v110\n"
    "v_smfmac_f32_16x16x64_bf16 v[68:71], v[36:39], v[208:215], v98\n"
    "s_cbranch_vccnz .Lvo_wf_%=\n"
    ".Lvo_wfb_%=:\n"
    "ds_read_b128 v[28:31], v97 offset:19456\n"
    "v_smfmac_f32_16x16x64_bf16 v[64:67], v[40:43], v[136:143], v98\n"
    "buffer_load_dwordx4 v96, s[0:3], s44 offen sc1 lds\n"
    "v_smfmac_f32_16x16x64_bf16 v[64:67], v[44:47], v[144:151], v98\n"
    "s_waitcnt lgkmcnt(5)\n"
    "s_add_u32 m0, s48, 0\n"
    "v_mov_b64 v[32:33], 0\n"
    "v_smfmac_f32_16x16x64_bf16 v[64:67], v[80:83], v[152:159], v98\n"
    "s_waitcnt lgkmcnt(4)\n"
    "v_mov_b64 v[34:35], 0\n"
    "v_smfmac_f32_16x16x64_bf16 v[64:67], v[72:75], v[160:167], v98\n"
    "s_waitcnt lgkmcnt(3)\n"
    "v_mov_b64 v[36:37], 0\n"
    "v_smfmac_f32_16x16x64_bf16 v[64:67], v[76:79], v[168:175], v98\n"
    "v_mov_b64 v[38:39], 0\n"
    "v_smfmac_f32_16x16x64_bf16 v[84:87], v[40:43], v[56:63], v98\n"
    "v_mov_b64 v[48:49], 0\n"
    "v_smfmac_f32_16x16x64_bf16 v[84:87], v[44:47], v[0:7], v98\n"
    "s_nop 1\n"
    "v_add_f32 v64, v64, v66\n"
    "v_smfmac_f32_16x16x64_bf16 v[84:87], v[80:83], v[8:15], v98\n"
    "s_waitcnt lgkmcnt(0)\n"
    "v_add_f32 v65, v65, v67\n"
    "v_smfmac_f32_16x16x64_bf16 v[84:87], v[72:75], v[16:23], v98\n"
    "v_cvt_pk_bf16_f32 v64, v64, v65\n"
    "v_smfmac_f32_16x16x64_bf16 v[84:87], v[76:79], v[24:31], v98\n"
    "ds_write_b32 v99, v64 offset:0\n"
    "v_smfmac_f32_16x16x64_bf16 v[68:71], v[40:43], v[216:223], v98\n"
    "v_mov_b64 v[50:51], 0\n"
    "v_smfmac_f32_16x16x64_bf16 v[68:71], v[44:47], v[224:231], v98\n"
    "s_nop 1\n"
    "v_add_f32 v84, v84, v86\n"
    "v_smfmac_f32_16x16x64_bf16 v[68:71], v[80:83], v[232:239], v98\n"
    "v_add_f32 v85, v85, v87\n"
    "v_smfmac_f32_16x16x64_bf16 v[68:71], v[72:75], v[240:247], v98\n"
    "v_cvt_pk_bf16_f32 v84, v84, v85\n"
    "v_smfmac_f32_16x16x64_bf16 v[68:71], v[76:79], v[248:255], v98\n"
    "ds_write_b32 v100, v84 offset:0\n"
    "v_mov_b64 v[52:53], 0\n"
    "v_mov_b64 v[54:55], 0\n"
    "v_mov_b64 v[88:89], 0\n"
    "v_mov_b64 v[90:91], 0\n"
    "v_mov_b64 v[56:57], 0\n"
    "v_mov_b64 v[58:59], 0\n"
    "v_mov_b64 v[60:61], 0\n"
    "v_add_f32 v68, v68, v70\n"
    "v_add_f32 v69, v69, v71\n"
    "v_cvt_pk_bf16_f32 v68, v68, v69\n"
    "ds_write_b32 v99, v68 offset:256\n"
    "v_mov_b64 v[62:63], 0\n"
    "v_mov_b64 v[64:65], 0\n"
    "v_mov_b64 v[66:67], 0\n"
    "v_mov_b64 v[84:85], 0\n"
    "v_mov_b64 v[86:87], 0\n"
    "s_waitcnt lgkmcnt(0)\n"
    "s_barrier\n"
    ".Lvo_mk2x9_%=:\n"
    "ds_read_b64_tr_b16 v[72:73], v101 offset:0\n"
    "ds_read_b64_tr_b16 v[74:75], v101 offset:64\n"
    "ds_read_b64_tr_b16 v[76:77], v101 offset:1024\n"
    "ds_read_b64_tr_b16 v[78:79], v101 offset:1088\n"
    "ds_read_b64_tr_b16 v[80:81], v101 offset:2048\n"
    "ds_read_b64_tr_b16 v[82:83], v101 offset:2112\n"
    "buffer_load_dwordx4 v96, s[0:3], s43 offen sc1 lds\n"
    "buffer_load_dwordx4 v96, s[0:3], s43 offen offset:1024 sc1 lds\n"
    "v_mov_b64 v[68:69], 0\n"
    "v_mov_b64 v[70:71], 0\n"
    "s_waitcnt lgkmcnt(4)\n"
    "v_smfmac_f32_16x16x64_bf16 v[32:35], v[72:75], a[192:199], v98\n"
    "s_waitcnt lgkmcnt(2)\n"
    "v_smfmac_f32_16x16x64_bf16 v[32:35], v[76:79], a[200:207], v98\n"
    "s_waitcnt lgkmcnt(0)\n"
    "v_smfmac_f32_16x16x64_bf16 v[32:35], v[80:83], a[208:215], v98\n"
    "ds_read_b128 v[0:3], v96 offset:11264\n"
    "ds_read_b128 v[4:7], v96 offset:12288\n"
    "v_smfmac_f32_16x16x64_bf16 v[36:39], v[72:75], a[216:223], v98\n"
    "ds_read_b128 v[8:11], v96 offset:24576\n"
    "v_smfmac_f32_16x16x64_bf16 v[36:39], v[76:79], a[224:231], v98\n"
    "s_nop 1\n"
    "v_add_f32 v32, v32, v34\n"
    "v_smfmac_f32_16x16x64_bf16 v[36:39], v[80:83], a[232:239], v98\n"
    "ds_read_b128 v[12:15], v96 offset:25600\n"
    "v_add_f32 v33, v33, v35\n"
    "v_smfmac_f32_16x16x64_bf16 v[48:51], v[72:75], a[0:7], v98\n"
    "ds_read_b128 v[16:19], v96 offset:37888\n"
    "v_cvt_pk_bf16_f32 v108, v32, v33\n"
    "v_smfmac_f32_16x16x64_bf16 v[48:51], v[76:79], a[8:15], v98\n"
    "s_nop 1\n"
    "v_add_f32 v36, v36, v38\n"
    "v_smfmac_f32_16x16x64_bf16 v[48:51], v[80:83], a[16:23], v98\n"
    "ds_read_b128 v[20:23], v96 offset:38912\n"
    "v_add_f32 v37, v37, v39\n"
    "v_smfmac_f32_16x16x64_bf16 v[52:55], v[72:75], a[24:31], v98\n"
    "ds_read_b128 v[24:27], v96 offset:51200\n"
    "v_cvt_pk_bf16_f32 v109, v36, v37\n"
    "v_smfmac_f32_16x16x64_bf16 v[52:55], v[76:79], a[32:39], v98\n"
    "s_nop 1\n"
    "v_add_f32 v48, v48, v50\n"
    "v_permlane16_swap_b32 v108, v109\n"
    "v_min_u32 v108, -2, v108\n"
    "v_smfmac_f32_16x16x64_bf16 v[52:55], v[80:83], a[40:47], v98\n"
    "buffer_store_dwordx2 v[108:109], v103, s[0:3], s36 offen sc1\n"
    "v_add_f32 v49, v49, v51\n"
    "v_smfmac_f32_16x16x64_bf16 v[56:59], v[72:75], a[48:55], v98\n"
    "ds_read_b128 v[28:31], v96 offset:52224\n"
    "v_cvt_pk_bf16_f32 v48, v48, v49\n"
    "v_smfmac_f32_16x16x64_bf16 v[56:59], v[76:79], a[56:63], v98\n"
    "s_nop 1\n"
    "v_add_f32 v52, v52, v54\n"
    "v_smfmac_f32_16x16x64_bf16 v[56:59], v[80:83], a[64:71], v98\n"
    "v_add_f32 v53, v53, v55\n"
    "v_smfmac_f32_16x16x64_bf16 v[60:63], v[72:75], a[72:79], v98\n"
    "v_cvt_pk_bf16_f32 v49, v52, v53\n"
    "v_smfmac_f32_16x16x64_bf16 v[60:63], v[76:79], a[80:87], v98\n"
    "s_waitcnt vmcnt(3)\n"
    "s_barrier\n"
    "v_add_f32 v56, v56, v58\n"
    "v_smfmac_f32_16x16x64_bf16 v[60:63], v[80:83], a[88:95], v98\n"
    "ds_read2_b64 v[32:35], v106 offset0:0 offset1:128\n"
    "v_add_f32 v57, v57, v59\n"
    "v_smfmac_f32_16x16x64_bf16 v[64:67], v[72:75], a[96:103], v98\n"
    "ds_read2_b64 v[36:39], v106 offset0:1 offset1:129\n"
    "v_cvt_pk_bf16_f32 v50, v56, v57\n"
    "v_smfmac_f32_16x16x64_bf16 v[64:67], v[76:79], a[104:111], v98\n"
    "s_nop 1\n"
    "v_add_f32 v60, v60, v62\n"
    "v_permlane32_swap_b32 v48, v50\n"
    "v_smfmac_f32_16x16x64_bf16 v[64:67], v[80:83], a[112:119], v98\n"
    "ds_read2_b64 v[40:43], v107 offset0:0 offset1:128\n"
    "v_add_f32 v61, v61, v63\n"
    "v_smfmac_f32_16x16x64_bf16 v[68:71], v[72:75], a[120:127], v98\n"
    "ds_read2_b64 v[44:47], v107 offset0:1 offset1:129\n"
    "v_cvt_pk_bf16_f32 v51, v60, v61\n"
    "v_smfmac_f32_16x16x64_bf16 v[68:71], v[76:79], a[128:135], v98\n"
    "s_nop 0\n"
    "v_permlane32_swap_b32 v49, v51\n"
    "v_smfmac_f32_16x16x64_bf16 v[68:71], v[80:83], a[136:143], v98\n"
    "s_nop 0\n"
    "v_permlane16_swap_b32 v50, v51\n"
    "v_permlane16_swap_b32 v48, v49\n"
    "v_min_u32 v48, -2, v48\n"
    "v_smfmac_f32_16x16x64_bf16 v[84:87], v[72:75], a[144:151], v98\n"
    "buffer_store_dwordx4 v[48:51], v102, s[0:3], s36 offen sc1\n"
    "v_add_f32 v64, v64, v66\n"
    "v_add_f32 v65, v65, v67\n"
    "v_cvt_pk_bf16_f32 v64, v64, v65\n"
    "v_smfmac_f32_16x16x64_bf16 v[84:87], v[76:79], a[152:159], v98\n"
    "v_add_f32 v68, v68, v70\n"
    "v_smfmac_f32_16x16x64_bf16 v[84:87], v[80:83], a[160:167], v98\n"
    "v_add_f32 v69, v69, v71\n"
    "v_smfmac_f32_16x16x64_bf16 v[88:91], v[72:75], a[168:175], v98\n"
    "v_cvt_pk_bf16_f32 v65, v68, v69\n"
    "v_smfmac_f32_16x16x64_bf16 v[88:91], v[76:79], a[176:183], v98\n"
    "s_nop 1\n"
    "v_add_f32 v84, v84, v86\n"
    "v_smfmac_f32_16x16x64_bf16 v[88:91], v[80:83], a[184:191], v98\n"
    ".Lvo_mk3x10_%=:\n"
    ".Lvo_uph_%=:\n"
    "s_waitcnt lgkmcnt(0)\n"
    "v_add_f32 v85, v85, v87\n"
    "v_mfma_f32_16x16x32_bf16 v[56:59], v[0:3], a[0:3], 0\n"
    "v_cvt_pk_bf16_f32 v66, v84, v85\n"
    "v_mfma_f32_16x16x32_bf16 v[60:63], v[4:7], a[0:3], 0\n"
    "s_nop 0\n"
    "v_permlane32_swap_b32 v64, v66\n"
    "v_add_f32 v88, v88, v90\n"
    "v_add_f32 v89, v89, v91\n"
    "v_cvt_pk_bf16_f32 v67, v88, v89\n"
    "s_nop 1\n"
    "v_permlane32_swap_b32 v65, v67\n"
    "s_nop 1\n"
    "v_permlane16_swap_b32 v66, v67\n"
    "v_permlane16_swap_b32 v64, v65\n"
    "v_min_u32 v64, -2, v64\n"
    "buffer_store_dwordx4 v[64:67], v102, s[0:3], s37 offen sc1\n"
    ".Lvo_mkum_%=:\n"
    "s_cmp_eq_u32 s34, 63\n"
    "s_cbranch_scc1 .Lvo_end_%=\n"
    "v_mfma_f32_16x16x32_bf16 v[56:59], v[8:11], a[4:7], v[56:59]\n"
    "v_mfma_f32_16x16x32_bf16 v[60:63], v[12:15], a[4:7], v[60:63]\n"
    "v_mfma_f32_16x16x32_bf16 v[56:59], v[16:19], a[8:11], v[56:59]\n"
    "v_mfma_f32_16x16x32_bf16 v[60:63], v[20:23], a[8:11], v[60:63]\n"
    "v_mfma_f32_16x16x32_bf16 v[56:59], v[24:27], a[12:15], v[56:59]\n"
    "v_mov_b64 v[88:89], -1\n"
    "v_mov_b64 v[90:91], -1\n"
    "v_mfma_f32_16x16x32_bf16 v[60:63], v[28:31], a[12:15], v[60:63]\n"
    "s_add_u32 m0, s48, 13312\n"
    "s_add_u32 s43, s43, 0x4000\n"
    "v_mfma_f32_16x16x32_bf16 v[56:59], v[32:35], a[16:19], v[56:59]\n"
    "buffer_load_dwordx4 v96, s[0:3], s43 offen sc1 lds\n"
    "v_mfma_f32_16x16x32_bf16 v[60:63], v[36:39], a[16:19], v[60:63]\n"
    "v_mfma_f32_16x16x32_bf16 v[56:59], v[40:43], a[20:23], v[56:59]\n"
    "v_mfma_f32_16x16x32_bf16 v[60:63], v[44:47], a[20:23], v[60:63]\n"
    "v_mfma_f32_16x16x32_bf16 v[48:51], v[0:3], a[24:27], 0\n"
    "s_nop 1\n"
    "v_cvt_pk_bf16_f32 v72, v56, v57\n"
    "v_mfma_f32_16x16x32_bf16 v[52:55], v[4:7], a[24:27], 0\n"
    "v_cvt_pk_bf16_f32 v74, v58, v59\n"
    "v_cvt_pk_bf16_f32 v73, v60, v61\n"
    "v_mfma_f32_16x16x32_bf16 v[48:51], v[8:11], a[28:31], v[48:51]\n"
    "v_cvt_pk_bf16_f32 v75, v62, v63\n"
    "v_mfma_f32_16x16x32_bf16 v[52:55], v[12:15], a[28:31], v[52:55]\n"
    "buffer_load_dwordx4 v96, s[0:3], s43 offen offset:1024 sc1 lds\n"
    "v_mfma_f32_16x16x32_bf16 v[48:51], v[16:19], a[32:35], v[48:51]\n"
    "s_add_u32 m0, s48, 26624\n"
    "s_add_u32 s43, s43, 0x4000\n"
    "v_mfma_f32_16x16x32_bf16 v[52:55], v[20:23], a[32:35], v[52:55]\n"
    "v_mfma_f32_16x16x32_bf16 v[48:51], v[24:27], a[36:39], v[48:51]\n"
    "v_mfma_f32_16x16x32_bf16 v[52:55], v[28:31], a[36:39], v[52:55]\n"
    "buffer_load_dwordx4 v96, s[0:3], s43 offen sc1 lds\n"
    "v_mfma_f32_16x16x32_bf16 v[48:51], v[32:35], a[40:43], v[48:51]\n"
    "v_mfma_f32_16x16x32_bf16 v[52:55], v[36:39], a[40:43], v[52:55]\n"
    "v_mfma_f32_16x16x32_bf16 v[48:51], v[40:43], a[44:47], v[48:51]\n"
    "v_mfma_f32_16x16x32_bf16 v[52:55], v[44:47], a[44:47], v[52:55]\n"
    "buffer_load_dwordx4 v96, s[0:3], s43 offen offset:1024 sc1 lds\n"
    "v_mfma_f32_16x16x32_bf16 v[56:59], v[0:3], a[48:51], 0\n"
    "s_add_u32 m0, s48, 39936\n"
    "s_add_u32 s43, s43, 0x4000\n"
    "v_cvt_pk_bf16_f32 v76, v48, v49\n"
    "v_mfma_f32_16x16x32_bf16 v[60:63], v[4:7], a[48:51], 0\n"
    "v_cvt_pk_bf16_f32 v78, v50, v51\n"
    "v_cvt_pk_bf16_f32 v77, v52, v53\n"
    "v_mfma_f32_16x16x32_bf16 v[56:59], v[8:11], a[52:55], v[56:59]\n"
    "v_cvt_pk_bf16_f32 v79, v54, v55\n"
    "v_mfma_f32_16x16x32_bf16 v[60:63], v[12:15], a[52:55], v[60:63]\n"
    "buffer_load_dwordx4 v96, s[0:3], s43 offen sc1 lds\n"
    "v_mfma_f32_16x16x32_bf16 v[56:59], v[16:19], a[56:59], v[56:59]\n"
    "v_mfma_f32_16x16x32_bf16 v[60:63], v[20:23], a[56:59], v[60:63]\n"
    "v_mfma_f32_16x16x32_bf16 v[56:59], v[24:27], a[60:63], v[56:59]\n"
    "v_mfma_f32_16x16x32_bf16 v[60:63], v[28:31], a[60:63], v[60:63]\n"
    "buffer_load_dwordx4 v96, s[0:3], s43 offen offset:1024 sc1 lds\n"
    "v_mfma_f32_16x16x32_bf16 v[56:59], v[32:35], a[64:67], v[56:59]\n"
    "v_mfma_f32_16x16x32_bf16 v[60:63], v[36:39], a[64:67], v[60:63]\n"
    "v_mfma_f32_16x16x32_bf16 v[56:59], v[40:43], a[68:71], v[56:59]\n"
    "v_mfma_f32_16x16x32_bf16 v[60:63], v[44:47], a[68:71], v[60:63]\n"
    "buffer_store_dwordx4 v[72:75], v96, s[0:3], s40 offen sc1\n"
    "s_add_u32 s40, s40, 0x10000\n"
    "s_add_u32 m0, s48, 53248\n"
    "s_add_u32 s43, s43, 0x4000\n"
    "v_mfma_f32_16x16x32_bf16 v[48:51], v[0:3], a[72:75], 0\n"
    "v_cvt_pk_bf16_f32 v80, v56, v57\n"
    "v_mfma_f32_16x16x32_bf16 v[52:55], v[4:7], a[72:75], 0\n"
    "v_cvt_pk_bf16_f32 v82, v58, v59\n"
    "v_cvt_pk_bf16_f32 v81, v60, v61\n"
    "v_mfma_f32_16x16x32_bf16 v[48:51], v[8:11], a[76:79], v[48:51]\n"
    "v_cvt_pk_bf16_f32 v83, v62, v63\n"
    "v_mfma_f32_16x16x32_bf16 v[52:55], v[12:15], a[76:79], v[52:55]\n"
    "v_mfma_f32_16x16x32_bf16 v[48:51], v[16:19], a[80:83], v[48:51]\n"
    "v_mfma_f32_16x16x32_bf16 v[52:55], v[20:23], a[80:83], v[52:55]\n"
    "v_mfma_f32_16x16x32_bf16 v[48:51], v[24:27], a[84:87], v[48:51]\n"
    "buffer_load_dwordx4 v96, s[0:3], s43 offen sc1 lds\n"
    "v_mfma_f32_16x16x32_bf16 v[52:55], v[28:31], a[84:87], v[52:55]\n"
    "v_mfma_f32_16x16x32_bf16 v[48:51], v[32:35], a[88:91], v[48:51]\n"
    "v_mfma_f32_16x16x32_bf16 v[52:55], v[36:39], a[88:91], v[52:55]\n"
    "v_mfma_f32_16x16x32_bf16 v[48:51], v[40:43], a[92:95], v[48:51]\n"
    "buffer_store_dwordx4 v[76:79], v96, s[0:3], s40 offen sc1\n"
    "s_add_u32 s40, s40, 0x10000\n"
    "v_mfma_f32_16x16x32_bf16 v[52:55], v[44:47], a[92:95], v[52:55]\n"
    "v_mfma_f32_16x16x32_bf16 v[56:59], v[0:3], a[96:99], 0\n"
    "s_nop 1\n"
    "v_cvt_pk_bf16_f32 v72, v48, v49\n"
    "v_mfma_f32_16x16x32_bf16 v[60:63], v[4:7], a[96:99], 0\n"
    "v_cvt_pk_bf16_f32 v74, v50, v51\n"
    "v_cvt_pk_bf16_f32 v73, v52, v53\n"
    "v_mfma_f32_16x16x32_bf16 v[56:59], v[8:11], a[100:103], v[56:59]\n"
    "v_cvt_pk_bf16_f32 v75, v54, v55\n"
    "v_mfma_f32_16x16x32_bf16 v[60:63], v[12:15], a[100:103], v[60:63]\n"
    "v_mfma_f32_16x16x32_bf16 v[56:59], v[16:19], a[104:107], v[56:59]\n"
    "v_mfma_f32_16x16x32_bf16 v[60:63], v[20:23], a[104:107], v[60:63]\n"
    "buffer_load_dwordx4 v96, s[0:3], s43 offen offset:1024 sc1 lds\n"
    "v_mfma_f32_16x16x32_bf16 v[56:59], v[24:27], a[108:111], v[56:59]\n"
    "v_mfma_f32_16x16x32_bf16 v[60:63], v[28:31], a[108:111], v[60:63]\n"
    "v_mfma_f32_16x16x32_bf16 v[56:59], v[32:35], a[112:115], v[56:59]\n"
    "v_mfma_f32_16x16x32_bf16 v[60:63], v[36:39], a[112:115], v[60:63]\n"
    "buffer_store_dwordx4 v[80:83], v96, s[0:3], s40 offen sc1\n"
    "s_add_u32 s40, s40, 0x10000\n"
    "v_mfma_f32_16x16x32_bf16 v[56:59], v[40:43], a[116:119], v[56:59]\n"
    "v_mfma_f32_16x16x32_bf16 v[60:63], v[44:47], a[116:119], v[60:63]\n"
    "v_mfma_f32_16x16x32_bf16 v[48:51], v[0:3], a[120:123], 0\n"
    "s_nop 1\n"
    "v_cvt_pk_bf16_f32 v76, v56, v57\n"
    "v_mfma_f32_16x16x32_bf16 v[52:55], v[4:7], a[120:123], 0\n"
    "v_cvt_pk_bf16_f32 v78, v58, v59\n"
    "v_cvt_pk_bf16_f32 v77, v60, v61\n"
    "v_mfma_f32_16x16x32_bf16 v[48:51], v[8:11], a[124:127], v[48:51]\n"
    "v_cvt_pk_bf16_f32 v79, v62, v63\n"
    "v_mfma_f32_16x16x32_bf16 v[52:55], v[12:15], a[124:127], v[52:55]\n"
    "v_mfma_f32_16x16x32_bf16 v[48:51], v[16:19], a[128:131], v[48:51]\n"
    "buffer_load_dwordx2 v[108:109], off, s[0:3], s45 sc1\n"
    "v_mfma_f32_16x16x32_bf16 v[52:55], v[20:23], a[128:131], v[52:55]\n"
    "v_mfma_f32_16x16x32_bf16 v[48:51], v[24:27], a[132:135], v[48:51]\n"
    "v_mfma_f32_16x16x32_bf16 v[52:55], v[28:31], a[132:135], v[52:55]\n"
    "v_mfma_f32_16x16x32_bf16 v[48:51], v[32:35], a[136:139], v[48:51]\n"
    "buffer_store_dwordx4 v[72:75], v96, s[0:3], s40 offen sc1\n"
    "s_add_u32 s40, s40, 0x10000\n"
    "v_mfma_f32_16x16x32_bf16 v[52:55], v[36:39], a[136:139], v[52:55]\n"
    "v_mfma_f32_16x16x32_bf16 v[48:51], v[40:43], a[140:143], v[48:51]\n"
    "v_mfma_f32_16x16x32_bf16 v[52:55], v[44:47], a[140:143], v[52:55]\n"
    "v_mfma_f32_16x16x32_bf16 v[56:59], v[0:3], a[144:147], 0\n"
    "s_nop 1\n"
    "v_cvt_pk_bf16_f32 v80, v48, v49\n"
    "v_mfma_f32_16x16x32_bf16 v[60:63], v[4:7], a[144:147], 0\n"
    "v_cvt_pk_bf16_f32 v82, v50, v51\n"
    "v_cvt_pk_bf16_f32 v81, v52, v53\n"
    "v_mfma_f32_16x16x32_bf16 v[56:59], v[8:11], a[148:151], v[56:59]\n"
    "v_cvt_pk_bf16_f32 v83, v54, v55\n"
    "v_mfma_f32_16x16x32_bf16 v[60:63], v[12:15], a[148:151], v[60:63]\n"
    "buffer_store_dwordx4 v[88:91], v102, s[0:3], s38 offen sc1\n"
    "v_mfma_f32_16x16x32_bf16 v[56:59], v[16:19], a[152:155], v[56:59]\n"
    "v_mfma_f32_16x16x32_bf16 v[60:63], v[20:23], a[152:155], v[60:63]\n"
    "v_mfma_f32_16x16x32_bf16 v[56:59], v[24:27], a[156:159], v[56:59]\n"
    "v_mfma_f32_16x16x32_bf16 v[60:63], v[28:31], a[156:159], v[60:63]\n"
    "v_mfma_f32_16x16x32_bf16 v[56:59], v[32:35], a[160:163], v[56:59]\n"
    "v_mfma_f32_16x16x32_bf16 v[60:63], v[36:39], a[160:163], v[60:63]\n"
    "v_mfma_f32_16x16x32_bf16 v[56:59], v[40:43], a[164:167], v[56:59]\n"
    "buffer_store_dwordx4 v[76:79], v96, s[0:3], s40 offen sc1\n"
    "s_add_u32 s40, s40, 0x10000\n"
    "v_mfma_f32_16x16x32_bf16 v[60:63], v[44:47], a[164:167], v[60:63]\n"
    "v_mfma_f32_16x16x32_bf16 v[48:51], v[0:3], a[168:171], 0\n"
    "s_nop 1\n"
    "v_cvt_pk_bf16_f32 v72, v56, v57\n"
    "v_mfma_f32_16x16x32_bf16 v[52:55], v[4:7], a[168:171], 0\n"
    "v_cvt_pk_bf16_f32 v74, v58, v59\n"
    "v_cvt_pk_bf16_f32 v73, v60, v61\n"
    "v_mfma_f32_16x16x32_bf16 v[48:51], v[8:11], a[172:175], v[48:51]\n"
    "v_cvt_pk_bf16_f32 v75, v62, v63\n"
    "v_mfma_f32_16x16x32_bf16 v[52:55], v[12:15], a[172:175], v[52:55]\n"
    "v_mfma_f32_16x16x32_bf16 v[48:51], v[16:19], a[176:179], v[48:51]\n"
    "v_mfma_f32_16x16x32_bf16 v[52:55], v[20:23], a[176:179], v[52:55]\n"
    "buffer_store_dwordx4 v[80:83], v96, s[0:3], s40 offen sc1\n"
    "s_add_u32 s40, s40, 0x10000\n"
    "v_mfma_f32_16x16x32_bf16 v[48:51], v[24:27], a[180:183], v[48:51]\n"
    "v_mfma_f32_16x16x32_bf16 v[52:55], v[28:31], a[180:183], v[52:55]\n"
    "v_mfma_f32_16x16x32_bf16 v[48:51], v[32:35], a[184:187], v[48:51]\n"
    "v_mfma_f32_16x16x32_bf16 v[52:55], v[36:39], a[184:187], v[52:55]\n"
    "v_mfma_f32_16x16x32_bf16 v[48:51], v[40:43], a[188:191], v[48:51]\n"
    "v_mfma_f32_16x16x32_bf16 v[52:55], v[44:47], a[188:191], v[52:55]\n"
    "v_mfma_f32_16x16x32_bf16 v[56:59], v[0:3], a[192:195], 0\n"
    "buffer_store_dwordx4 v[88:91], v102, s[0:3], s39 offen sc1\n"
    "s_nop 0\n"
    "v_cvt_pk_bf16_f32 v76, v48, v49\n"
    "v_mfma_f32_16x16x32_bf16 v[60:63], v[4:7], a[192:195], 0\n"
    "v_cvt_pk_bf16_f32 v78, v50, v51\n"
    "v_cvt_pk_bf16_f32 v77, v52, v53\n"
    "v_mfma_f32_16x16x32_bf16 v[56:59], v[8:11], a[196:199], v[56:59]\n"
    "v_cvt_pk_bf16_f32 v79, v54, v55\n"
    "v_mfma_f32_16x16x32_bf16 v[60:63], v[12:15], a[196:199], v[60:63]\n"
    "v_mfma_f32_16x16x32_bf16 v[56:59], v[16:19], a[200:203], v[56:59]\n"
    "v_mfma_f32_16x16x32_bf16 v[60:63], v[20:23], a[200:203], v[60:63]\n"
    "v_mfma_f32_16x16x32_bf16 v[56:59], v[24:27], a[204:207], v[56:59]\n"
    "v_mfma_f32_16x16x32_bf16 v[60:63], v[28:31], a[204:207], v[60:63]\n"
    "buffer_store_dwordx4 v[72:75], v96, s[0:3], s40 offen sc1\n"
    "s_add_u32 s40, s40, 0x10000\n"
    "v_mfma_f32_16x16x32_bf16 v[56:59], v[32:35], a[208:211], v[56:59]\n"
    "s_waitcnt vmcnt(8)\n"
    "s_barrier\n"
    "v_mfma_f32_16x16x32_bf16 v[60:63], v[36:39], a[208:211], v[60:63]\n"
    "v_mfma_f32_16x16x32_bf16 v[56:59], v[40:43], a[212:215], v[56:59]\n"
    "v_mfma_f32_16x16x32_bf16 v[60:63], v[44:47], a[212:215], v[60:63]\n"
    "v_mfma_f32_16x16x32_bf16 v[48:51], v[24:27], a[228:231], 0\n"
    "s_nop 1\n"
    "v_cvt_pk_bf16_f32 v80, v56, v57\n"
    "v_mfma_f32_16x16x32_bf16 v[52:55], v[28:31], a[228:231], 0\n"
    "v_cvt_pk_bf16_f32 v82, v58, v59\n"
    "v_cvt_pk_bf16_f32 v81, v60, v61\n"
    "v_mfma_f32_16x16x32_bf16 v[48:51], v[32:35], a[232:235], v[48:51]\n"
    "buffer_store_dwordx4 v[76:79], v96, s[0:3], s40 offen sc1\n"
    "ds_read_b128 v[24:27], v92 offset:0\n"
    "v_cvt_pk_bf16_f32 v83, v62, v63\n"
    "s_add_u32 s40, s40, 0x10000\n"
    "v_mfma_f32_16x16x32_bf16 v[52:55], v[36:39], a[232:235], v[52:55]\n"
    "ds_read_b128 v[28:31], v92 offset:1024\n"
    "v_mfma_f32_16x16x32_bf16 v[48:51], v[40:43], a[236:239], v[48:51]\n"
    "ds_read_b128 v[32:35], v92 offset:2048\n"
    "v_mfma_f32_16x16x32_bf16 v[52:55], v[44:47], a[236:239], v[52:55]\n"
    "ds_read_b128 v[36:39], v92 offset:3072\n"
    "v_mfma_f32_16x16x32_bf16 v[48:51], v[0:3], a[216:219], v[48:51]\n"
    "ds_read_b128 v[40:43], v92 offset:4096\n"
    "v_mfma_f32_16x16x32_bf16 v[52:55], v[4:7], a[216:219], v[52:55]\n"
    "ds_read_b128 v[44:47], v92 offset:5120\n"
    "v_mfma_f32_16x16x32_bf16 v[48:51], v[8:11], a[220:223], v[48:51]\n"
    "v_mfma_f32_16x16x32_bf16 v[52:55], v[12:15], a[220:223], v[52:55]\n"
    "buffer_store_dwordx2 v[88:89], v103, s[0:3], s38 offen sc1\n"
    "v_mfma_f32_16x16x32_bf16 v[48:51], v[16:19], a[224:227], v[48:51]\n"
    "v_mfma_f32_16x16x32_bf16 v[52:55], v[20:23], a[224:227], v[52:55]\n"
    ".Lvo_mk4x11_%=:\n"
    ".Lvo_vph_%=:\n"
    "s_waitcnt lgkmcnt(4)\n"
    "v_mfma_f32_16x16x32_bf16 v[64:67], a[240:243], v[24:27], 0\n"
    "s_nop 1\n"
    "v_cvt_pk_bf16_f32 v72, v48, v49\n"
    "v_mfma_f32_16x16x32_bf16 v[84:87], v[176:179], v[24:27], 0\n"
    "s_add_u32 s34, s34, 1\n"
    "v_cvt_pk_bf16_f32 v74, v50, v51\n"
    "v_cvt_pk_bf16_f32 v73, v52, v53\n"
    "v_mfma_f32_16x16x32_bf16 v[68:71], a[240:243], v[28:31], 0\n"
    "s_add_u32 s36, s34, 0\n"
    "ds_read_b128 v[24:27], v92 offset:6144\n"
    "v_cvt_pk_bf16_f32 v75, v54, v55\n"
    "v_mfma_f32_16x16x32_bf16 v[88:91], v[176:179], v[28:31], 0\n"
    "s_waitcnt lgkmcnt(3)\n"
    "ds_read_b128 v[28:31], v92 offset:7168\n"
    "s_and_b32 s36, s36, 3\n"
    "v_mfma_f32_16x16x32_bf16 v[64:67], a[244:247], v[32:35], v[64:67]\n"
    "s_lshl_b32 s36, s36, 22\n"
    "s_add_u32 s41, s15, s36\n"
    "s_add_u32 s42, s16, s36\n"
    "v_mfma_f32_16x16x32_bf16 v[84:87], v[180:183], v[32:35], v[84:87]\n"
    "buffer_store_dwordx4 v[80:83], v104, s[0:3], s38 offen sc1\n"
    "s_add_u32 s37, s36, 0x10000\n"
    "s_add_u32 s50, s34, 3\n"
    "s_cmp_gt_i32 s50, 64\n"
    "s_cselect_b32 s50, 0, s50\n"
    "v_mfma_f32_16x16x32_bf16 v[68:71], a[244:247], v[36:39], v[68:71]\n"
    "ds_read_b128 v[32:35], v93 offset:0\n"
    "s_add_u32 s51, s34, 4\n"
    "v_mfma_f32_16x16x32_bf16 v[88:91], v[180:183], v[36:39], v[88:91]\n"
    "s_waitcnt lgkmcnt(3)\n"
    "ds_read_b128 v[36:39], v93 offset:1024\n"
    "ds_read_b128 v[0:3], v97 offset:0\n"
    "v_mfma_f32_16x16x32_bf16 v[64:67], a[248:251], v[40:43], v[64:67]\n"
    "s_cmp_gt_i32 s51, 64\n"
    "s_cselect_b32 s51, 0, s51\n"
    "s_mul_i32 s51, s51, s14\n"
    "v_mfma_f32_16x16x32_bf16 v[84:87], v[184:187], v[40:43], v[84:87]\n"
    "ds_read_b128 v[4:7], v97 offset:1024\n"
    "v_mov_b32 v111, s50\n"
    "v_mfma_f32_16x16x32_bf16 v[68:71], a[248:251], v[44:47], v[68:71]\n"
    "ds_read_b128 v[40:43], v93 offset:2048\n"
    "v_mfma_f32_16x16x32_bf16 v[88:91], v[184:187], v[44:47], v[88:91]\n"
    "s_waitcnt lgkmcnt(5)\n"
    "ds_read_b128 v[44:47], v93 offset:3072\n"
    "v_mfma_f32_16x16x32_bf16 v[64:67], a[252:255], v[24:27], v[64:67]\n"
    "buffer_store_dwordx4 v[72:75], v104, s[0:3], s39 offen sc1\n"
    "ds_read_b128 v[8:11], v97 offset:2048\n"
    "s_mov_b64 exec, 0x1f0\n"
    "v_mov_b32 v111, s51\n"
    "s_mov_b64 exec, -1\n"
    "v_mfma_f32_16x16x32_bf16 v[84:87], v[188:191], v[24:27], v[84:87]\n"
    "ds_read_b128 v[12:15], v97 offset:3072\n"
    "v_mfma_f32_16x16x32_bf16 v[68:71], a[252:255], v[28:31], v[68:71]\n"
    "ds_read_b128 v[24:27], v93 offset:4096\n"
    "v_mfma_f32_16x16x32_bf16 v[88:91], v[188:191], v[28:31], v[88:91]\n"
    "s_waitcnt lgkmcnt(7)\n"
    "ds_read_b128 v[28:31], v93 offset:5120\n"
    "v_mfma_f32_16x16x32_bf16 v[64:67], v[112:115], v[32:35], v[64:67]\n"
    "ds_read_b128 v[16:19], v97 offset:4096\n"
    "v_mfma_f32_16x16x32_bf16 v[84:87], v[192:195], v[32:35], v[84:87]\n"
    "ds_read_b128 v[20:23], v97 offset:5120\n"
    "v_mfma_f32_16x16x32_bf16 v[68:71], v[112:115], v[36:39], v[68:71]\n"
    "ds_read_b128 v[32:35], v93 offset:6144\n"
    "v_mfma_f32_16x16x32_bf16 v[88:91], v[192:195], v[36:39], v[88:91]\n"
    "s_waitcnt lgkmcnt(7)\n"
    "ds_read_b128 v[36:39], v93 offset:7168\n"
    "v_mfma_f32_16x16x32_bf16 v[64:67], v[116:119], v[40:43], v[64:67]\n"
    "ds_read_b128 v[48:51], v97 offset:8192\n"
    "v_mfma_f32_16x16x32_bf16 v[84:87], v[196:199], v[40:43], v[84:87]\n"
    "ds_read_b128 v[52:55], v97 offset:9216\n"
    "v_mfma_f32_16x16x32_bf16 v[68:71], v[116:119], v[44:47], v[68:71]\n"
    "ds_read_b128 v[40:43], v94 offset:0\n"
    "v_mfma_f32_16x16x32_bf16 v[88:91], v[196:199], v[44:47], v[88:91]\n"
    "s_waitcnt lgkmcnt(7)\n"
    "ds_read_b128 v[44:47], v94 offset:1024\n"
    "v_mfma_f32_16x16x32_bf16 v[64:67], v[120:123], v[24:27], v[64:67]\n"
    "ds_read_b128 v[56:59], v97 offset:10240\n"
    "v_mfma_f32_16x16x32_bf16 v[84:87], v[200:203], v[24:27], v[84:87]\n"
    "ds_read_b128 v[60:63], v97 offset:11264\n"
    "v_mfma_f32_16x16x32_bf16 v[68:71], v[120:123], v[28:31], v[68:71]\n"
    "ds_read_b128 v[24:27], v94 offset:2048\n"
    "v_mfma_f32_16x16x32_bf16 v[88:91], v[200:203], v[28:31], v[88:91]\n"
    "s_waitcnt lgkmcnt(7)\n"
    "ds_read_b128 v[28:31], v94 offset:3072\n"
    "v_mfma_f32_16x16x32_bf16 v[64:67], v[124:127], v[32:35], v[64:67]\n"
    "v_mfma_f32_16x16x32_bf16 v[84:87], v[204:207], v[32:35], v[84:87]\n"
    "v_mfma_f32_16x16x32_bf16 v[68:71], v[124:127], v[36:39], v[68:71]\n"
    "ds_read_b128 v[32:35], v94 offset:4096\n"
    "v_mfma_f32_16x16x32_bf16 v[88:91], v[204:207], v[36:39], v[88:91]\n"
    "s_waitcnt lgkmcnt(5)\n"
    "ds_read_b128 v[36:39], v94 offset:5120\n"
    "s_waitcnt vmcnt(10)\n"
    "v_mfma_f32_16x16x32_bf16 v[64:67], v[128:131], v[40:43], v[64:67]\n"
    "v_readfirstlane_b32 s32, v108\n"
    "v_readfirstlane_b32 s33, v109\n"
    "v_mfma_f32_16x16x32_bf16 v[84:87], v[208:211], v[40:43], v[84:87]\n"
    "v_mfma_f32_16x16x32_bf16 v[68:71], v[128:131], v[44:47], v[68:71]\n"
    "s_cmp_eq_u32 s33, s35\n"
    "s_cselect_b32 s46, s32, s46\n"
    "ds_read_b128 v[40:43], v94 offset:6144\n"
    "v_mfma_f32_16x16x32_bf16 v[88:91], v[208:211], v[44:47], v[88:91]\n"
    "s_waitcnt lgkmcnt(3)\n"
    "ds_read_b128 v[44:47], v94 offset:7168\n"
    "s_add_u32 s46, s46, 272\n"
    "v_mfma_f32_16x16x32_bf16 v[64:67], v[132:135], v[24:27], v[64:67]\n"
    "s_sub_u32 s47, s46, 63\n"
    "s_mov_b32 m0, s17\n"
    "v_mfma_f32_16x16x32_bf16 v[84:87], v[212:215], v[24:27], v[84:87]\n"
    "v_mfma_f32_16x16x32_bf16 v[68:71], v[132:135], v[28:31], v[68:71]\n"
    "ds_read_b128 v[24:27], v95 offset:0\n"
    "v_mfma_f32_16x16x32_bf16 v[88:91], v[212:215], v[28:31], v[88:91]\n"
    "s_waitcnt lgkmcnt(3)\n"
    "ds_read_b128 v[28:31], v95 offset:1024\n"
    ".Lvo_mk6x12_%=:\n"
    ".Lvo_rt14_%=:\n"
    "s_memrealtime s[30:31]\n"
    "s_waitcnt lgkmcnt(0)\n"
    "s_sub_u32 s32, s47, s30\n"
    "s_cmp_lt_u32 s32, 600\n"
    "s_cbranch_scc1 .Lvo_rt14_%=\n"
    "buffer_load_dwordx4 v96, s[0:3], s41 offen sc1 lds\n"
    "buffer_load_dwordx4 v96, s[0:3], s41 offen offset:1024 sc1 lds\n"
    ".Lvo_mk7x14_%=:\n"
    "v_mfma_f32_16x16x32_bf16 v[64:67], v[136:139], v[32:35], v[64:67]\n"
    "s_add_u32 s49, s34, 3\n"
    "s_lshl_b32 s43, s49, 19\n"
    "s_lshl_b32 s33, s49, 11\n"
    "v_mfma_f32_16x16x32_bf16 v[84:87], v[216:219], v[32:35], v[84:87]\n"
    "s_xor_b32 s33, s33, s13\n"
    "s_and_b32 s33, s33, 0x2000\n"
    "s_add_u32 s43, s43, s33\n"
    "v_mfma_f32_16x16x32_bf16 v[68:71], v[136:139], v[36:39], v[68:71]\n"
    "ds_read_b128 v[32:35], v95 offset:2048\n"
    "s_add_u32 s43, s43, s18\n"
    "v_mfma_f32_16x16x32_bf16 v[88:91], v[216:219], v[36:39], v[88:91]\n"
    "s_waitcnt lgkmcnt(3)\n"
    "ds_read_b128 v[36:39], v95 offset:3072\n"
    "s_add_u32 s49, s34, 2\n"
    "v_mfma_f32_16x16x32_bf16 v[64:67], v[140:143], v[40:43], v[64:67]\n"
    "s_lshl_b32 s44, s49, 20\n"
    "s_lshl_b32 s33, s49, 12\n"
    "s_xor_b32 s33, s33, s13\n"
    "v_mfma_f32_16x16x32_bf16 v[84:87], v[220:223], v[40:43], v[84:87]\n"
    "s_and_b32 s33, s33, 0x2000\n"
    "s_add_u32 s44, s44, s33\n"
    "s_add_u32 s44, s44, s19\n"
    "v_mfma_f32_16x16x32_bf16 v[68:71], v[140:143], v[44:47], v[68:71]\n"
    "ds_read_b128 v[40:43], v95 offset:4096\n"
    "s_add_u32 s38, s34, 2\n"
    "v_mfma_f32_16x16x32_bf16 v[88:91], v[220:223], v[44:47], v[88:91]\n"
    "s_waitcnt lgkmcnt(3)\n"
    "ds_read_b128 v[44:47], v95 offset:5120\n"
    "s_and_b32 s38, s38, 3\n"
    "v_mfma_f32_16x16x32_bf16 v[64:67], v[144:147], v[24:27], v[64:67]\n"
    "buffer_load_dwordx4 v96, s[0:3], s42 offen offset:2048 sc1 lds\n"
    "v_mfma_f32_16x16x32_bf16 v[84:87], v[224:227], v[24:27], v[84:87]\n"
    "s_lshl_b32 s38, s38, 22\n"
    "s_add_u32 s39, s38, 0x10000\n"
    "s_add_u32 s40, s21, s38\n"
    "v_mfma_f32_16x16x32_bf16 v[68:71], v[144:147], v[28:31], v[68:71]\n"
    "ds_read_b128 v[24:27], v95 offset:6144\n"
    "s_and_b32 s45, s34, 63\n"
    "v_mfma_f32_16x16x32_bf16 v[88:91], v[224:227], v[28:31], v[88:91]\n"
    "s_waitcnt lgkmcnt(3)\n"
    "ds_read_b128 v[28:31], v95 offset:7168\n"
    "s_lshl_b32 s45, s45, 7\n"
    "v_mfma_f32_16x16x32_bf16 v[64:67], v[148:151], v[32:35], v[64:67]\n"
    "s_add_u32 s45, s22, s45\n"
    "s_add_u32 s35, s34, 1\n"
    "v_mfma_f32_16x16x32_bf16 v[84:87], v[228:231], v[32:35], v[84:87]\n"
    "s_cmp_lt_i32 s34, 0\n"
    "s_cselect_b32 s35, -5, s35\n"
    "v_mfma_f32_16x16x32_bf16 v[68:71], v[148:151], v[36:39], v[68:71]\n"
    "ds_read_b128 v[32:35], v96 offset:53248\n"
    "v_mfma_f32_16x16x32_bf16 v[88:91], v[228:231], v[36:39], v[88:91]\n"
    "s_waitcnt lgkmcnt(3)\n"
    "ds_read_b128 v[36:39], v96 offset:54272\n"
    "v_mfma_f32_16x16x32_bf16 v[64:67], v[152:155], v[40:43], v[64:67]\n"
    "buffer_load_dword v110, v105, s[0:3], 0 offen sc1\n"
    "v_mfma_f32_16x16x32_bf16 v[84:87], v[232:235], v[40:43], v[84:87]\n"
    "v_mfma_f32_16x16x32_bf16 v[68:71], v[152:155], v[44:47], v[68:71]\n"
    "ds_read_b128 v[40:43], v96 offset:55296\n"
    "v_mfma_f32_16x16x32_bf16 v[88:91], v[232:235], v[44:47], v[88:91]\n"
    "s_waitcnt lgkmcnt(3)\n"
    "ds_read_b128 v[44:47], v96 offset:56320\n"
    "v_mfma_f32_16x16x32_bf16 v[64:67], v[156:159], v[24:27], v[64:67]\n"
    "v_mfma_f32_16x16x32_bf16 v[84:87], v[236:239], v[24:27], v[84:87]\n"
    "v_mfma_f32_16x16x32_bf16 v[68:71], v[156:159], v[28:31], v[68:71]\n"
    "ds_read_b128 v[24:27], v96 offset:57344\n"
    "v_mfma_f32_16x16x32_bf16 v[88:91], v[236:239], v[28:31], v[88:91]\n"
    "s_waitcnt lgkmcnt(3)\n"
    "ds_read_b128 v[28:31], v96 offset:58368\n"
    "v_mfma_f32_16x16x32_bf16 v[64:67], v[160:163], v[32:35], v[64:67]\n"
    "v_mfma_f32_16x16x32_bf16 v[84:87], v[240:243], v[32:35], v[84:87]\n"
    "v_mfma_f32_16x16x32_bf16 v[68:71], v[160:163], v[36:39], v[68:71]\n"
    "ds_read_b128 v[32:35], v96 offset:59392\n"
    "v_mfma_f32_16x16x32_bf16 v[88:91], v[240:243], v[36:39], v[88:91]\n"
    "s_waitcnt lgkmcnt(3)\n"
    "ds_read_b128 v[36:39], v96 offset:60416\n"
    "v_mfma_f32_16x16x32_bf16 v[64:67], v[164:167], v[40:43], v[64:67]\n"
    "v_mfma_f32_16x16x32_bf16 v[84:87], v[244:247], v[40:43], v[84:87]\n"
    "v_mfma_f32_16x16x32_bf16 v[68:71], v[164:167], v[44:47], v[68:71]\n"
    "v_mfma_f32_16x16x32_bf16 v[88:91], v[244:247], v[44:47], v[88:91]\n"
    "s_waitcnt lgkmcnt(2)\n"
    "v_mfma_f32_16x16x32_bf16 v[64:67], v[168:171], v[24:27], v[64:67]\n"
    "v_mfma_f32_16x16x32_bf16 v[84:87], v[248:251], v[24:27], v[84:87]\n"
    "v_mfma_f32_16x16x32_bf16 v[68:71], v[168:171], v[28:31], v[68:71]\n"
    "v_mfma_f32_16x16x32_bf16 v[88:91], v[248:251], v[28:31], v[88:91]\n"
    "s_waitcnt lgkmcnt(0)\n"
    "v_mfma_f32_16x16x32_bf16 v[64:67], v[172:175], v[32:35], v[64:67]\n"
    "ds_read_b128 v[24:27], v97 offset:6144\n"
    "ds_read_b128 v[28:31], v97 offset:7168\n"
    "v_mfma_f32_16x16x32_bf16 v[84:87], v[252:255], v[32:35], v[84:87]\n"
    "v_mfma_f32_16x16x32_bf16 v[68:71], v[172:175], v[36:39], v[68:71]\n"
    "v_mfma_f32_16x16x32_bf16 v[88:91], v[252:255], v[36:39], v[88:91]\n"
    "v_cvt_pk_bf16_f32 v40, v64, v65\n"
    "v_cvt_pk_bf16_f32 v41, v66, v67\n"
    "v_cvt_pk_bf16_f32 v42, v84, v85\n"
    "v_cvt_pk_bf16_f32 v43, v86, v87\n"
    "ds_write_b128 v92, v[40:43] offset:11264\n"
    "v_cvt_pk_bf16_f32 v44, v68, v69\n"
    "v_cvt_pk_bf16_f32 v45, v70, v71\n"
    "v_cvt_pk_bf16_f32 v46, v88, v89\n"
    "v_cvt_pk_bf16_f32 v47, v90, v91\n"
    "ds_write_b128 v92, v[44:47] offset:12288\n"
    "v_mov_b64 v[84:85], 0\n"
    "v_mov_b64 v[86:87], 0\n"
    "v_mov_b64 v[64:65], 0\n"
    "v_mov_b64 v[66:67], 0\n"
    "v_mov_b64 v[68:69], 0\n"
    "v_mov_b64 v[70:71], 0\n"
    ".Lvo_mk8x15_%=:\n"
    "s_cmp_lt_i32 s34, 0\n"
    "s_cbranch_scc0 .Lvo_take_%=\n"
    "s_waitcnt vmcnt(0) lgkmcnt(0)\n"
    "s_barrier\n"
    ".Lvo_po17_%=:\n"
    "buffer_load_dword v110, v105, s[0:3], 0 offen sc1\n"
    "s_waitcnt vmcnt(0)\n"
    "v_cmp_gt_i32 vcc, v111, v110\n"
    "s_nop 0\n"
    "s_cbranch_vccz .Lvo_pd18_%=\n"
    "s_add_u32 s28, s28, 1\n"
    "s_cmp_lt_u32 s28, 0x40000\n"
    "s_cbranch_scc1 .Lvo_sp19_%=\n"
    "v_mov_b32 v110, 32\n"
    "s_mov_b32 s32, 33728\n"
    "buffer_store_dword v110, off, s[0:3], s32 sc1\n"
    "s_waitcnt vmcnt(0)\n"
    "s_branch .Lvo_pd18_%=\n"
    ".Lvo_sp19_%=:\n"
    "s_sleep 4\n"
    "s_branch .Lvo_po17_%=\n"
    ".Lvo_pd18_%=:\n"
    "s_mov_b32 m0, s20\n"
    "s_nop 0\n"
    "buffer_load_dwordx4 v96, s[0:3], s44 offen sc1 lds\n"
    "s_add_u32 m0, s48, 0\n"
    "s_nop 0\n"
    "buffer_load_dwordx4 v96, s[0:3], s43 offen sc1 lds\n"
    "buffer_load_dwordx4 v96, s[0:3], s43 offen offset:1024 sc1 lds\n"
    "ds_read_b128 v[0:3], v96 offset:11264\n"
    "ds_read_b128 v[4:7], v96 offset:12288\n"
    "ds_read_b128 v[8:11], v96 offset:24576\n"
    "ds_read_b128 v[12:15], v96 offset:25600\n"
    "ds_read_b128 v[16:19], v96 offset:37888\n"
    "ds_read_b128 v[20:23], v96 offset:38912\n"
    "ds_read_b128 v[24:27], v96 offset:51200\n"
    "ds_read_b128 v[28:31], v96 offset:52224\n"
    "s_waitcnt vmcnt(0) lgkmcnt(0)\n"
    "s_barrier\n"
    "ds_read2_b64 v[32:35], v106 offset0:0 offset1:128\n"
    "ds_read2_b64 v[36:39], v106 offset0:1 offset1:129\n"
    "ds_read2_b64 v[40:43], v107 offset0:0 offset1:128\n"
    "ds_read2_b64 v[44:47], v107 offset0:1 offset1:129\n"
    "s_waitcnt lgkmcnt(0)\n"
    "s_mov_b32 s37, 2130706432\n"
    "s_mov_b32 s36, 2130706432\n"
    "s_branch .Lvo_uph_%=\n"
    ".Lvo_retry_%=:\n"
    "s_add_u32 s28, s28, 1\n"
    "s_cmp_lt_u32 s28, 0x40000\n"
    "s_cbranch_scc1 .Lvo_sp20_%=\n"
    "v_mov_b32 v108, 2\n"
    "s_mov_b32 s32, 33728\n"
    "buffer_store_dword v108, off, s[0:3], s32 sc1\n"
    "s_waitcnt vmcnt(0)\n"
    "s_branch .Lvo_go_%=\n"
    ".Lvo_sp20_%=:\n"
    "s_nop 7\n"
    "v_mov_b64 v[64:65], 0\n"
    "v_mov_b64 v[66:67], 0\n"
    "v_mov_b64 v[68:69], 0\n"
    "v_mov_b64 v[70:71], 0\n"
    "v_mov_b64 v[84:85], 0\n"
    "v_mov_b64 v[86:87], 0\n"
    "buffer_load_dwordx4 v96, s[0:3], s41 offen sc1 lds\n"
    "buffer_load_dwordx4 v96, s[0:3], s41 offen offset:1024 sc1 lds\n"
    "buffer_load_dwordx4 v96, s[0:3], s42 offen offset:2048 sc1 lds\n"
    "s_waitcnt vmcnt(0)\n"
    "s_branch .Lvo_take_%=\n"
    ".Lvo_r2_%=:\n"
    "s_add_u32 s28, s28, 1\n"
    "s_cmp_lt_u32 s28, 0x40000\n"
    "s_cbranch_scc1 .Lvo_sp21_%=\n"
    "v_mov_b32 v108, 6\n"
    "s_mov_b32 s32, 33728\n"
    "buffer_store_dword v108, off, s[0:3], s32 sc1\n"
    "s_waitcnt vmcnt(0)\n"
    "s_branch .Lvo_go2_%=\n"
    ".Lvo_sp21_%=:\n"
    "buffer_load_dwordx4 v96, s[0:3], s42 offen offset:2048 sc1 lds\n"
    "s_waitcnt vmcnt(0)\n"
    "ds_read_b128 v[80:83], v92 offset:10240\n"
    "s_waitcnt lgkmcnt(0)\n"
    "v_max_u32 v108, v80, v82\n"
    "v_cmp_eq_u32 vcc, -1, v108\n"
    "s_nop 0\n"
    "s_cbranch_vccnz .Lvo_r2_%=\n"
    "s_branch .Lvo_go2_%=\n"
    ".Lvo_wf_%=:\n"
    ".Lvo_po22_%=:\n"
    "buffer_load_dword v110, v105, s[0:3], 0 offen sc1\n"
    "s_waitcnt vmcnt(0)\n"
    "v_cmp_gt_i32 vcc, v111, v110\n"
    "s_nop 0\n"
    "s_cbranch_vccz .Lvo_pd23_%=\n"
    "s_add_u32 s28, s28, 1\n"
    "s_cmp_lt_u32 s28, 0x40000\n"
    "s_cbranch_scc1 .Lvo_sp24_%=\n"
    "v_mov_b32 v110, 33\n"
    "s_mov_b32 s32, 33728\n"
    "buffer_store_dword v110, off, s[0:3], s32 sc1\n"
    "s_waitcnt vmcnt(0)\n"
    "s_branch .Lvo_pd23_%=\n"
    ".Lvo_sp24_%=:\n"
    "s_sleep 4\n"
    "s_branch .Lvo_po22_%=\n"
    ".Lvo_pd23_%=:\n"
    "s_branch .Lvo_wfb_%=\n"
    ".Lvo_end_%=:\n"
    "s_waitcnt vmcnt(0) lgkmcnt(0)\n"
    "s_endpgm\n"
    :: "{s[0:3]}"(s0_), "{s[4:7]}"(s4_), "{s[8:11]}"(s8_), "{s[12:15]}"(s12_), "{s[16:19]}"(s16_), "{s[20:23]}"(s20_), "{s[24:27]}"(s24_), "{v[0:3]}"(v0_), "{v[4:7]}"(v4_), "{v[8:11]}"(v8_), "{v[12:15]}"(v12_), "{v[16:19]}"(v16_), "{v[92:95]}"(v92_), "{v[96:99]}"(v96_), "{v[100:103]}"(v100_), "{v[104:107]}"(v104_) : "memory", "v255", "a255", "s95");
    __builtin_unreachable();
}
DEV void ct_role(const MegaArgs& A, const int L, const int u) {
    ROLE_HEAD;
    {
        const int sg = 2 * u + L;
        u32x4 xa[8], xb[8];
#pragma unroll
        for (int r = 0; r < 8; ++r) {
            const bf16_t* xr = A.x0 + (size_t)(32 * sg + 8 * w + r) * D_;
            xa[r] = *(const u32x4*)(xr + 8 * l);
            xb[r] = *(const u32x4*)(xr + 512 + 8 * l16);
        }
        const float mk = l < 16 ? 1.f : 0.f;
#pragma unroll
        for (int r = 0; r < 8; ++r) {
            float v[16], s = 0.f, s2 = 0.f, q = 0.f, q2 = 0.f;
#pragma unroll
            for (int e = 0; e < 4; ++e) { v[2 * e] = lo16(xa[r][e]); v[2 * e + 1] = hi16(xa[r][e]); v[8 + 2 * e] = lo16(xb[r][e]); v[9 + 2 * e] = hi16(xb[r][e]); }
#pragma unroll
            for (int e = 0; e < 8; ++e) { s += v[e]; s2 += v[8 + e]; }
            s += l < 16 ? s2 : 0.f;
            for (int o = 32; o > 0; o >>= 1) s += __shfl_xor(s, o);
            const float mean = s * (1.0f / D_);
#pragma unroll
            for (int e = 0; e < 8; ++e) { const float d = v[e] - mean, d2 = v[8 + e] - mean; q += d * d; q2 += d2 * d2; }
            q += l < 16 ? q2 : 0.f;
            for (int o = 32; o > 0; o >>= 1) q += __shfl_xor(q, o);
            const float mb = rb(mean), rd = __builtin_amdgcn_rcpf(rb(sqrtf(rb(rb(q * (1.0f / D_)) + 1e-5f))));
            u32x4 oa, ob;
#pragma unroll
            for (int e = 0; e < 4; ++e) {
                oa[sw2(e)] = pk2(rb(v[2 * e] - mb) * rd, rb(v[2 * e + 1] - mb) * rd);
                ob[sw2(e)] = pk2(rb(v[8 + 2 * e] - mb) * rd, rb(v[9 + 2 * e] - mb) * rd);
            }
            const int row = 8 * w + r;
            const unsigned o = rec(sg, 0) + (row >> 4) * 1024 + (row & 15) * 16 + (l16 >> 2) * 2048 + (l & 3) * 256;
            bst16(rr, o + lq * 16384, oa);
            bst16(rr, l < 16 ? o + 65536 : OOB, ob);
        }
    }
    const int r5 = l / 5, q5 = l - 5 * r5, c0 = q5 < 4 ? 16 * u + 4 * q5 : 512 + 4 * u, tk = 8 * w + r5;
#define XM 5
    const unsigned mpos = l < 40 && q5 < XM ? xl(RG_MEM + w * 16384 + (c0 >> 6) * 1024 + (((c0 >> 4) & 3) * 16 + 4 * (r5 >> 1) + ((c0 >> 1) & 2) + (r5 & 1)) * 16 + (c0 & 8), L) : OOB;
    const unsigned xpos = l < 40 ? (c0 >> 7) * 16384 + ((c0 >> 5) & 3) * 2048 + (tk >> 4) * 1024 + (((c0 >> 3) & 3) * 16 + (tk & 15)) * 16 + (c0 & 4) * 2 : OOB;
    const unsigned rpos = (L || l >= 40) ? xpos + RC_X2 : tk * 1280 + c0 * 2;
    const u32x4 rs = L ? rx : u32x4{lo(A.x0), hi(A.x0), 2048u * 1280u, 0x20000u};
#pragma unroll
    for (int k = 0; k < 4; ++k) bst16(rr, rec(2 * u + (k >> 1), L) + RC_X1 + 65536 + w * 2048 + (k & 1) * 1024 + l * 16, ones);
    const u32x2 m0 = *(const u32x2*)(A.mem0 + (r5 & 7) * D_ + c0);
    bst8(rr, mpos, u32x2{nosent(m0[0]), m0[1]}); bst8(rr, mpos + SLOT, ones2); bst8(rr, mpos + 2 * SLOT, ones2);
    DRAIN_BARRIER();
    bst4(rr, threadIdx.x < 16 ? xl(Z_CT + (l & 7) * 256 + (32 * L + u) * 4, l >> 3) : OOB, 1u);
    if (!((A.layers >> L) & 1)) return;
    const u32x4 s0_ = {RFL(rx[0]), RFL(rx[1]), RFL(rx[2]), RFL(rx[3])};
    const u32x4 s4_ = {RFL(rs[0]), RFL(rs[1]), RFL(rs[2]), RFL(rs[3])};
    const u32x4 s8_ = {RFL(L << 13), RFL(REC0), RFL(xl(RG_UT + (u * 4 + w) * 8192, L)), RFL(xl(RG_UTX + (u * 4 + w) * 2048, L))};
    const u32x4 s12_ = {RFL(xl(RG_UM + (u * 4 + w) * 2048, L)), RFL(xl(RG_UMX + (u * 4 + w) * 512, L)), RFL(xl(RG_PT + w * 16384, L)), RFL(xl(RG_PM + w * 8192, L))};
    const u32x4 s16_ = {RFL(xl(Z_X + 16384 + (u & 1) * 8192, L)), RFL(xl(Z_QV + (u & 3) * 256, L)), RFL(DBG0 + ((L * 128 + 64 + u) * 4 + w) * 2048), RFL(DBG0 + (2 << 20) + ((L * 32 + u) * 4 + w) * 8192)};
    const u32x4 v192_ = {(unsigned)(l * 16), (unsigned)(l * 16 + 4096), (unsigned)((l & 2) ? 0xeeeeu : 0x4444u), (unsigned)(0x4444u)};
    const u32x4 v196_ = {(unsigned)((l16 >> 2) * 256 + lq * 64 + (l & 3) * 16), (unsigned)(lq * 64 + (l & 3) * 16), (unsigned)((lq >> 1) * 2048 + 1024 + (lq & 1) * 256 + l16 * 16), (unsigned)(lq * 2048 + 1536 + l16 * 16)};
    const u32x4 v200_ = {(unsigned)(0 + w * 640 + lq * 160 + l16 * 4), (unsigned)(l16 < 4 ? 0 + w * 640 + lq * 160 + 64 + l16 * 4 : 12800), (unsigned)(0 + (l < 40 ? l : 0) * 16), (unsigned)(mpos)};
    const u32x4 v204_ = {(unsigned)(2560 + w * 2560 + lq * 320 + l16 * 4), (unsigned)(l16 < 4 ? 2560 + w * 2560 + lq * 320 + 64 + l16 * 4 : 12800), (unsigned)(3840 + w * 2560 + lq * 320 + l16 * 4), (unsigned)(l16 < 4 ? 3840 + w * 2560 + lq * 320 + 64 + l16 * 4 : 12800)};
    const u32x4 v208_ = {(unsigned)(2560 + w * 640 + (l < 40 ? l : 0) * 16), (unsigned)(xpos + RC_X1), (unsigned)(rpos), (unsigned)(l < 4 && w == 1 ? xl(Z_X1 + l * 4096 + u * 128, L) : OOB)};
    const u32x4 v212_ = {(unsigned)(l < 16 ? ((u >> 2) & 1) * 16384 + (u & 3) * 2048 + l * 128 : OOB), (unsigned)(0u), (unsigned)(0u), (unsigned)(0u)};
    asm volatile(
    "s_mov_b32 s20, 0\n"
    "v_lshrrev_b32 v4, 2, v192\n"
    ".Lct_po1_%=:\n"
    "buffer_load_dword v8, v4, s[0:3], s17 offen sc1\n"
    "s_waitcnt vmcnt(0)\n"
    "v_cmp_gt_i32 vcc, 1, v8\n"
    "s_nop 0\n"
    "s_cbranch_vccz .Lct_pd2_%=\n"
    "s_add_u32 s20, s20, 1\n"
    "s_cmp_lt_u32 s20, 0x40000\n"
    "s_cbranch_scc1 .Lct_sp3_%=\n"
    "v_mov_b32 v8, 40\n"
    "s_mov_b32 s24, 33728\n"
    "buffer_store_dword v8, off, s[0:3], s24 sc1\n"
    "s_waitcnt vmcnt(0)\n"
    "s_branch .Lct_pd2_%=\n"
    ".Lct_sp3_%=:\n"
    "s_sleep 4\n"
    "s_branch .Lct_po1_%=\n"
    ".Lct_pd2_%=:\n"
    "s_memrealtime s[22:23]\n"
    "s_waitcnt lgkmcnt(0)\n"
    "s_sub_u32 s41, s22, 816\n"
    "s_mov_b32 s26, -1\n"
    "s_add_u32 s26, s26, 1\n"
    "s_add_u32 s27, s26, 1\n"
    "s_add_u32 s28, s26, 0\n"
    "s_and_b32 s28, s28, 3\n"
    "s_lshl_b32 s28, s28, 22\n"
    "s_add_u32 s29, s26, 1\n"
    "s_and_b32 s29, s29, 3\n"
    "s_lshl_b32 s29, s29, 22\n"
    "s_add_u32 s30, s26, 3\n"
    "s_and_b32 s30, s30, 3\n"
    "s_lshl_b32 s30, s30, 22\n"
    "s_and_b32 s40, s26, 63\n"
    "s_lshl_b32 s40, s40, 7\n"
    "s_add_u32 s31, s12, s28\n"
    "s_add_u32 s32, s13, s28\n"
    "s_add_u32 s33, s15, s28\n"
    "s_add_u32 s34, s14, s28\n"
    "s_add_u32 s38, s10, s29\n"
    "s_add_u32 s39, s11, s29\n"
    "s_add_u32 s40, s16, s40\n"
    "s_add_u32 s35, s34, 4096\n"
    "s_add_u32 s36, s34, 16384\n"
    "s_add_u32 s37, s34, 20480\n"
    "s_add_u32 s43, s41, 316\n"
    "s_lshl_b32 s45, s26, 19\n"
    "s_lshl_b32 s25, s26, 11\n"
    "s_xor_b32 s25, s25, s8\n"
    "s_and_b32 s25, s25, 0x2000\n"
    "s_add_u32 s45, s45, s25\n"
    "s_add_u32 s45, s45, s9\n"
    "s_mul_i32 s46, s26, 40960\n"
    "s_cmp_eq_u32 s8, 0\n"
    "s_cselect_b32 s46, s46, s45\n"
    "v_mov_b64 v[76:77], 0\n"
    "v_mov_b64 v[78:79], 0\n"
    "v_mov_b64 v[80:81], 0\n"
    "v_mov_b64 v[82:83], 0\n"
    ".Lct_p0_%=:\n"
    "buffer_load_dwordx4 v[40:43], v192, s[0:3], s31 offen offset:0 sc1\n"
    "buffer_load_dwordx4 v[44:47], v192, s[0:3], s31 offen offset:1024 sc1\n"
    "buffer_load_dwordx4 v[48:51], v197, s[0:3], s32 offen offset:0 sc1\n"
    "buffer_load_dwordx4 v[52:55], v197, s[0:3], s32 offen offset:256 sc1\n"
    "s_waitcnt vmcnt(0)\n"
    "v_max_u32 v184, v40, v44\n"
    "v_max3_u32 v184, v48, v50, v184\n"
    "v_max3_u32 v184, v52, v54, v184\n"
    "v_cmp_eq_u32 vcc, -1, v184\n"
    "s_nop 0\n"
    "s_cbranch_vccz .Lct_p1_%=\n"
    "s_add_u32 s20, s20, 1\n"
    "s_cmp_lt_u32 s20, 0x40000\n"
    "s_cbranch_scc1 .Lct_sp4_%=\n"
    "v_mov_b32 v184, 41\n"
    "s_mov_b32 s24, 33728\n"
    "buffer_store_dword v184, off, s[0:3], s24 sc1\n"
    "s_waitcnt vmcnt(0)\n"
    "s_branch .Lct_p1_%=\n"
    ".Lct_sp4_%=:\n"
    "s_sleep 8\n"
    "s_branch .Lct_p0_%=\n"
    ".Lct_p1_%=:\n"
    "s_mov_b32 s38, s10\n"
    "s_mov_b32 s39, s11\n"
    "buffer_load_dwordx4 a[0:3], v192, s[0:3], s38 offen offset:0 sc1\n"
    "buffer_load_dwordx4 a[4:7], v192, s[0:3], s38 offen offset:1024 sc1\n"
    "buffer_load_dwordx4 a[8:11], v192, s[0:3], s38 offen offset:2048 sc1\n"
    "buffer_load_dwordx4 a[12:15], v192, s[0:3], s38 offen offset:3072 sc1\n"
    "buffer_load_dwordx4 a[16:19], v193, s[0:3], s38 offen offset:0 sc1\n"
    "buffer_load_dwordx4 a[20:23], v193, s[0:3], s38 offen offset:1024 sc1\n"
    "buffer_load_dwordx4 a[24:27], v193, s[0:3], s38 offen offset:2048 sc1\n"
    "buffer_load_dwordx4 a[28:31], v193, s[0:3], s38 offen offset:3072 sc1\n"
    "buffer_load_dwordx4 v[0:3], v196, s[0:3], s39 offen offset:0 sc1\n"
    "buffer_load_dwordx4 v[4:7], v196, s[0:3], s39 offen offset:1024 sc1\n"
    "s_add_u32 s38, s10, s29\n"
    "s_add_u32 s39, s11, s29\n"
    "s_waitcnt vmcnt(0)\n"
    ".Lct_top_%=:\n"
    ".Lct_rt5_%=:\n"
    "s_memrealtime s[22:23]\n"
    "s_waitcnt lgkmcnt(0)\n"
    "s_sub_u32 s24, s43, s22\n"
    "s_cmp_lt_u32 s24, 600\n"
    "s_cbranch_scc1 .Lct_rt5_%=\n"
    "buffer_load_dwordx2 v[190:191], off, s[0:3], s40 sc1\n"
    "s_waitcnt vmcnt(2)\n"
    "v_mov_b64 v[8:9], v[0:1]\n"
    "v_mov_b64 v[10:11], v[2:3]\n"
    "v_mov_b64 v[12:13], v[4:5]\n"
    "v_mov_b64 v[14:15], v[6:7]\n"
    "v_mov_b32_dpp v16, v0 row_shl:4 row_mask:0xf bank_mask:0xf bound_ctrl:1\n"
    "v_mov_b32_dpp v17, v1 row_shl:4 row_mask:0xf bank_mask:0xf bound_ctrl:1\n"
    "v_mov_b32_dpp v18, v2 row_shl:4 row_mask:0xf bank_mask:0xf bound_ctrl:1\n"
    "v_mov_b32_dpp v19, v3 row_shl:4 row_mask:0xf bank_mask:0xf bound_ctrl:1\n"
    "v_mov_b32_dpp v20, v4 row_shl:4 row_mask:0xf bank_mask:0xf bound_ctrl:1\n"
    "v_mov_b32_dpp v21, v5 row_shl:4 row_mask:0xf bank_mask:0xf bound_ctrl:1\n"
    "v_mov_b32_dpp v22, v6 row_shl:4 row_mask:0xf bank_mask:0xf bound_ctrl:1\n"
    "v_mov_b32_dpp v23, v7 row_shl:4 row_mask:0xf bank_mask:0xf bound_ctrl:1\n"
    "v_mov_b32_dpp v24, v0 row_shl:8 row_mask:0xf bank_mask:0xf bound_ctrl:1\n"
    "v_mov_b32_dpp v25, v1 row_shl:8 row_mask:0xf bank_mask:0xf bound_ctrl:1\n"
    "v_mov_b32_dpp v26, v2 row_shl:8 row_mask:0xf bank_mask:0xf bound_ctrl:1\n"
    "v_mov_b32_dpp v27, v3 row_shl:8 row_mask:0xf bank_mask:0xf bound_ctrl:1\n"
    "v_mov_b32_dpp v28, v4 row_shl:8 row_mask:0xf bank_mask:0xf bound_ctrl:1\n"
    "v_mov_b32_dpp v29, v5 row_shl:8 row_mask:0xf bank_mask:0xf bound_ctrl:1\n"
    "v_mov_b32_dpp v30, v6 row_shl:8 row_mask:0xf bank_mask:0xf bound_ctrl:1\n"
    "v_mov_b32_dpp v31, v7 row_shl:8 row_mask:0xf bank_mask:0xf bound_ctrl:1\n"
    "v_mov_b32_dpp v32, v0 row_shl:12 row_mask:0xf bank_mask:0xf bound_ctrl:1\n"
    "v_mov_b32_dpp v33, v1 row_shl:12 row_mask:0xf bank_mask:0xf bound_ctrl:1\n"
    "v_mov_b32_dpp v34, v2 row_shl:12 row_mask:0xf bank_mask:0xf bound_ctrl:1\n"
    "v_mov_b32_dpp v35, v3 row_shl:12 row_mask:0xf bank_mask:0xf bound_ctrl:1\n"
    "v_mov_b32_dpp v36, v4 row_shl:12 row_mask:0xf bank_mask:0xf bound_ctrl:1\n"
    "v_mov_b32_dpp v37, v5 row_shl:12 row_mask:0xf bank_mask:0xf bound_ctrl:1\n"
    "v_mov_b32_dpp v38, v6 row_shl:12 row_mask:0xf bank_mask:0xf bound_ctrl:1\n"
    "v_mov_b32_dpp v39, v7 row_shl:12 row_mask:0xf bank_mask:0xf bound_ctrl:1\n"
    "v_mov_b32 v148, s26\n"
    "v_mov_b32 v149, s26\n"
    "v_mov_b32 v150, s26\n"
    "v_mov_b32 v151, s26\n"
    "s_waitcnt vmcnt(0)\n"
    "s_barrier\n"
    "buffer_store_dwordx4 v[148:151], v211, s[0:3], 0 offen sc1\n"
    ".Lct_x0_%=:\n"
    "v_readfirstlane_b32 s25, v191\n"
    "v_readfirstlane_b32 s41, v190\n"
    "s_cmp_eq_u32 s25, s27\n"
    "s_cbranch_scc0 .Lct_xr0_%=\n"
    ".Lct_xg0_%=:\n"
    "s_add_u32 s43, s41, 93\n"
    "s_add_u32 s44, s41, 109\n"
    ".Lct_mk0x5_%=:\n"
    ".Lct_rt7_%=:\n"
    "s_memrealtime s[22:23]\n"
    "s_waitcnt lgkmcnt(0)\n"
    "s_sub_u32 s24, s43, s22\n"
    "s_cmp_lt_u32 s24, 600\n"
    "s_cbranch_scc1 .Lct_rt7_%=\n"
    "buffer_load_dwordx4 v[40:43], v192, s[0:3], s31 offen offset:0 sc1\n"
    "buffer_load_dwordx4 v[44:47], v192, s[0:3], s31 offen offset:1024 sc1\n"
    "buffer_load_dwordx4 v[48:51], v197, s[0:3], s32 offen offset:0 sc1\n"
    "buffer_load_dwordx4 v[52:55], v197, s[0:3], s32 offen offset:256 sc1\n"
    ".Lct_rt8_%=:\n"
    "s_memrealtime s[22:23]\n"
    "s_waitcnt lgkmcnt(0)\n"
    "s_sub_u32 s24, s44, s22\n"
    "s_cmp_lt_u32 s24, 600\n"
    "s_cbranch_scc1 .Lct_rt8_%=\n"
    "buffer_load_dwordx4 v[72:75], v193, s[0:3], s33 offen sc1\n"
    "buffer_load_dwordx4 v[56:59], v192, s[0:3], s33 offen offset:0 sc1\n"
    "buffer_load_dwordx4 v[60:63], v192, s[0:3], s33 offen offset:1024 sc1\n"
    "buffer_load_dwordx4 v[64:67], v192, s[0:3], s33 offen offset:2048 sc1\n"
    "buffer_load_dwordx4 v[68:71], v192, s[0:3], s33 offen offset:3072 sc1\n"
    ".Lct_mk1x8_%=:\n"
    ".Lct_take0_%=:\n"
    "s_waitcnt vmcnt(5)\n"
    "v_max_u32 v184, v40, v44\n"
    "v_max3_u32 v184, v48, v50, v184\n"
    "v_max3_u32 v184, v52, v54, v184\n"
    "s_waitcnt vmcnt(4)\n"
    "v_smfmac_f32_16x16x64_bf16 v[76:79], v[72:75], v[40:47], v194\n"
    "v_max3_u32 v184, v72, v74, v184\n"
    "v_smfmac_f32_16x16x64_bf16 v[80:83], v[72:75], v[48:55], v194\n"
    "s_waitcnt vmcnt(3)\n"
    "v_smfmac_f32_16x16x64_bf16 v[80:83], v[56:59], v[8:15], v194\n"
    "v_max3_u32 v184, v56, v58, v184\n"
    "v_smfmac_f32_16x16x64_bf16 v[76:79], v[56:59], a[0:7], v194\n"
    "s_waitcnt vmcnt(2)\n"
    "v_smfmac_f32_16x16x64_bf16 v[80:83], v[60:63], v[16:23], v194\n"
    "v_max3_u32 v184, v60, v62, v184\n"
    "v_smfmac_f32_16x16x64_bf16 v[76:79], v[60:63], a[8:15], v194\n"
    "s_waitcnt vmcnt(1)\n"
    "v_smfmac_f32_16x16x64_bf16 v[80:83], v[64:67], v[24:31], v194\n"
    "v_max3_u32 v184, v64, v66, v184\n"
    "v_smfmac_f32_16x16x64_bf16 v[76:79], v[64:67], a[16:23], v194\n"
    "s_waitcnt vmcnt(0)\n"
    "v_smfmac_f32_16x16x64_bf16 v[80:83], v[68:71], v[32:39], v194\n"
    "v_max3_u32 v184, v68, v70, v184\n"
    "v_smfmac_f32_16x16x64_bf16 v[76:79], v[68:71], a[24:31], v194\n"
    ".Lct_mk2x9_%=:\n"
    "s_nop 1\n"
    "v_cmp_eq_u32 vcc, -1, v184\n"
    "s_nop 1\n"
    "v_add_f32 v80, v80, v82\n"
    "v_add_f32 v81, v81, v83\n"
    "s_nop 0\n"
    "v_pk_add_f32 v[76:77], v[76:77], v[78:79]\n"
    "ds_write2_b32 v201, v80, v81 offset1:20\n"
    "ds_write2_b32 v200, v76, v77 offset1:20\n"
    "s_cbranch_vccnz .Lct_retry0_%=\n"
    ".Lct_go0_%=:\n"
    "s_waitcnt lgkmcnt(0)\n"
    "s_barrier\n"
    "ds_read_b128 v[164:167], v202 offset:0\n"
    "ds_read_b128 v[168:171], v202 offset:640\n"
    "ds_read_b128 v[172:175], v202 offset:1280\n"
    "ds_read_b128 v[176:179], v202 offset:1920\n"
    "s_waitcnt lgkmcnt(2)\n"
    "v_pk_add_f32 v[164:165], v[164:165], v[168:169]\n"
    "v_pk_add_f32 v[166:167], v[166:167], v[170:171]\n"
    "s_waitcnt lgkmcnt(1)\n"
    "v_pk_add_f32 v[164:165], v[164:165], v[172:173]\n"
    "v_pk_add_f32 v[166:167], v[166:167], v[174:175]\n"
    "s_waitcnt lgkmcnt(0)\n"
    "v_pk_add_f32 v[164:165], v[164:165], v[176:177]\n"
    "v_pk_add_f32 v[166:167], v[166:167], v[178:179]\n"
    "v_cvt_pk_bf16_f32 v164, v164, v165\n"
    "v_cvt_pk_bf16_f32 v165, v166, v167\n"
    "v_min_u32 v164, -2, v164\n"
    "buffer_store_dwordx2 v[164:165], v203, s[0:3], s29 offen sc1\n"
    ".Lct_mk3x10_%=:\n"
    "s_sleep 2\n"
    "v_mov_b64 v[166:167], -1\n"
    "buffer_load_dwordx4 v[132:135], v199, s[0:3], s34 offen offset:0 sc1\n"
    "buffer_load_dwordx4 v[136:139], v199, s[0:3], s36 offen offset:0 sc1\n"
    "buffer_load_dwordx4 v[140:143], v199, s[0:3], s34 offen offset:256 sc1\n"
    "buffer_load_dwordx4 v[144:147], v199, s[0:3], s36 offen offset:256 sc1\n"
    "buffer_load_dwordx4 v[116:119], v198, s[0:3], s34 offen sc1\n"
    "buffer_load_dwordx4 v[84:87], v192, s[0:3], s34 offen offset:0 sc1\n"
    "buffer_load_dwordx4 v[88:91], v192, s[0:3], s34 offen offset:2048 sc1\n"
    "buffer_load_dwordx4 v[120:123], v198, s[0:3], s35 offen sc1\n"
    "buffer_load_dwordx4 v[92:95], v192, s[0:3], s35 offen offset:0 sc1\n"
    "buffer_load_dwordx4 v[96:99], v192, s[0:3], s35 offen offset:2048 sc1\n"
    ".Lct_mk7x11_%=:\n"
    "buffer_load_dwordx4 v[124:127], v198, s[0:3], s36 offen sc1\n"
    "s_waitcnt vmcnt(10)\n"
    "v_max3_u32 v184, v132, v134, v132\n"
    "v_swap_b32 v133, v134\n"
    "s_nop 1\n"
    "v_mfma_f32_16x16x32_bf16 v[148:151], v[132:135], v[40:43], 0\n"
    "v_mfma_f32_16x16x32_bf16 v[152:155], v[132:135], v[48:51], 0\n"
    "buffer_load_dwordx4 v[100:103], v192, s[0:3], s36 offen offset:0 sc1\n"
    "s_waitcnt vmcnt(10)\n"
    "v_max3_u32 v184, v136, v138, v184\n"
    "v_swap_b32 v137, v138\n"
    "s_nop 1\n"
    "v_mfma_f32_16x16x32_bf16 v[148:151], v[136:139], v[44:47], v[148:151]\n"
    "v_mfma_f32_16x16x32_bf16 v[152:155], v[136:139], v[52:55], v[152:155]\n"
    "buffer_load_dwordx4 v[104:107], v192, s[0:3], s36 offen offset:2048 sc1\n"
    "s_waitcnt vmcnt(10)\n"
    "v_max3_u32 v184, v140, v142, v184\n"
    "v_swap_b32 v141, v142\n"
    "s_nop 1\n"
    "v_mfma_f32_16x16x32_bf16 v[156:159], v[140:143], v[40:43], 0\n"
    "v_mfma_f32_16x16x32_bf16 v[160:163], v[140:143], v[48:51], 0\n"
    "buffer_load_dwordx4 v[128:131], v198, s[0:3], s37 offen sc1\n"
    "s_waitcnt vmcnt(10)\n"
    "v_max3_u32 v184, v144, v146, v184\n"
    "v_swap_b32 v145, v146\n"
    "s_nop 1\n"
    "v_mfma_f32_16x16x32_bf16 v[156:159], v[144:147], v[44:47], v[156:159]\n"
    "v_mfma_f32_16x16x32_bf16 v[160:163], v[144:147], v[52:55], v[160:163]\n"
    "buffer_load_dwordx4 v[108:111], v192, s[0:3], s37 offen offset:0 sc1\n"
    "s_waitcnt vmcnt(10)\n"
    "v_max3_u32 v184, v116, v118, v184\n"
    "v_smfmac_f32_16x16x64_bf16 v[148:151], v[116:119], a[0:7], v195\n"
    "v_smfmac_f32_16x16x64_bf16 v[152:155], v[116:119], v[8:15], v195\n"
    "buffer_load_dwordx4 v[112:115], v192, s[0:3], s37 offen offset:2048 sc1\n"
    "s_waitcnt vmcnt(10)\n"
    "v_max3_u32 v184, v84, v86, v184\n"
    "v_swap_b32 v85, v86\n"
    "s_nop 1\n"
    "v_mfma_f32_16x16x32_bf16 v[156:159], v[84:87], a[0:3], v[156:159]\n"
    "v_mfma_f32_16x16x32_bf16 v[160:163], v[84:87], v[8:11], v[160:163]\n"
    "buffer_load_dwordx2 v[186:187], v210, s[4:7], s46 offen sc1\n"
    "s_waitcnt vmcnt(10)\n"
    "v_max3_u32 v184, v88, v90, v184\n"
    "v_swap_b32 v89, v90\n"
    "s_nop 1\n"
    "v_mfma_f32_16x16x32_bf16 v[156:159], v[88:91], a[4:7], v[156:159]\n"
    "v_mfma_f32_16x16x32_bf16 v[160:163], v[88:91], v[12:15], v[160:163]\n"
    "buffer_store_dwordx2 v[166:167], v203, s[0:3], s30 offen sc1\n"
    "s_waitcnt vmcnt(10)\n"
    "v_max3_u32 v184, v120, v122, v184\n"
    "v_smfmac_f32_16x16x64_bf16 v[148:151], v[120:123], a[8:15], v195\n"
    "v_smfmac_f32_16x16x64_bf16 v[152:155], v[120:123], v[16:23], v195\n"
    "buffer_load_dwordx4 a[32:35], v192, s[0:3], s38 offen offset:0 sc1\n"
    "s_waitcnt vmcnt(10)\n"
    "v_max3_u32 v184, v92, v94, v184\n"
    "v_swap_b32 v93, v94\n"
    "s_nop 1\n"
    "v_mfma_f32_16x16x32_bf16 v[156:159], v[92:95], a[8:11], v[156:159]\n"
    "v_mfma_f32_16x16x32_bf16 v[160:163], v[92:95], v[16:19], v[160:163]\n"
    "buffer_load_dwordx4 a[36:39], v192, s[0:3], s38 offen offset:1024 sc1\n"
    "s_waitcnt vmcnt(10)\n"
    "v_max3_u32 v184, v96, v98, v184\n"
    "v_swap_b32 v97, v98\n"
    "s_nop 1\n"
    "v_mfma_f32_16x16x32_bf16 v[156:159], v[96:99], a[12:15], v[156:159]\n"
    "v_mfma_f32_16x16x32_bf16 v[160:163], v[96:99], v[20:23], v[160:163]\n"
    "buffer_load_dwordx4 a[40:43], v192, s[0:3], s38 offen offset:2048 sc1\n"
    "s_waitcnt vmcnt(10)\n"
    "v_max3_u32 v184, v124, v126, v184\n"
    "v_smfmac_f32_16x16x64_bf16 v[148:151], v[124:127], a[16:23], v195\n"
    "v_smfmac_f32_16x16x64_bf16 v[152:155], v[124:127], v[24:31], v195\n"
    "buffer_load_dwordx4 a[44:47], v192, s[0:3], s38 offen offset:3072 sc1\n"
    "s_waitcnt vmcnt(10)\n"
    "v_max3_u32 v184, v100, v102, v184\n"
    "v_swap_b32 v101, v102\n"
    "s_nop 1\n"
    "v_mfma_f32_16x16x32_bf16 v[156:159], v[100:103], a[16:19], v[156:159]\n"
    "v_mfma_f32_16x16x32_bf16 v[160:163], v[100:103], v[24:27], v[160:163]\n"
    "buffer_load_dwordx4 a[48:51], v193, s[0:3], s38 offen offset:0 sc1\n"
    "s_waitcnt vmcnt(10)\n"
    "v_max3_u32 v184, v104, v106, v184\n"
    "v_swap_b32 v105, v106\n"
    "s_nop 1\n"
    "v_mfma_f32_16x16x32_bf16 v[156:159], v[104:107], a[20:23], v[156:159]\n"
    "v_mfma_f32_16x16x32_bf16 v[160:163], v[104:107], v[28:31], v[160:163]\n"
    "buffer_load_dwordx4 a[52:55], v193, s[0:3], s38 offen offset:1024 sc1\n"
    "s_waitcnt vmcnt(10)\n"
    "v_max3_u32 v184, v128, v130, v184\n"
    "v_smfmac_f32_16x16x64_bf16 v[148:151], v[128:131], a[24:31], v195\n"
    "v_smfmac_f32_16x16x64_bf16 v[152:155], v[128:131], v[32:39], v195\n"
    "buffer_load_dwordx4 a[56:59], v193, s[0:3], s38 offen offset:2048 sc1\n"
    "s_waitcnt vmcnt(10)\n"
    "v_max3_u32 v184, v108, v110, v184\n"
    "v_swap_b32 v109, v110\n"
    "s_nop 1\n"
    "v_mfma_f32_16x16x32_bf16 v[156:159], v[108:111], a[24:27], v[156:159]\n"
    "v_mfma_f32_16x16x32_bf16 v[160:163], v[108:111], v[32:35], v[160:163]\n"
    "buffer_load_dwordx4 a[60:63], v193, s[0:3], s38 offen offset:3072 sc1\n"
    "s_waitcnt vmcnt(10)\n"
    "v_max3_u32 v184, v112, v114, v184\n"
    "v_swap_b32 v113, v114\n"
    "s_nop 1\n"
    "v_mfma_f32_16x16x32_bf16 v[156:159], v[112:115], a[28:31], v[156:159]\n"
    "v_mfma_f32_16x16x32_bf16 v[160:163], v[112:115], v[36:39], v[160:163]\n"
    "v_cmp_eq_u32 vcc, -1, v184\n"
    "s_nop 0\n"
    "s_cbranch_vccnz .Lct_tr0_%=\n"
    ".Lct_tg0_%=:\n"
    ".Lct_mk4x12_%=:\n"
    "ds_write2_b32 v204, v148, v149 offset0:0 offset1:20\n"
    "ds_write2_b32 v204, v150, v151 offset0:40 offset1:60\n"
    "ds_write2_b32 v205, v152, v153 offset0:0 offset1:20\n"
    "ds_write2_b32 v205, v154, v155 offset0:40 offset1:60\n"
    "ds_write2_b32 v206, v156, v157 offset0:0 offset1:20\n"
    "ds_write2_b32 v206, v158, v159 offset0:40 offset1:60\n"
    "ds_write2_b32 v207, v160, v161 offset0:0 offset1:20\n"
    "ds_write2_b32 v207, v162, v163 offset0:40 offset1:60\n"
    "s_mov_b32 s42, s45\n"
    "buffer_load_dwordx4 v[0:3], v196, s[0:3], s39 offen offset:0 sc1\n"
    "buffer_load_dwordx4 v[4:7], v196, s[0:3], s39 offen offset:1024 sc1\n"
    "s_add_u32 s26, s26, 1\n"
    "s_add_u32 s27, s26, 1\n"
    "s_add_u32 s28, s26, 0\n"
    "s_and_b32 s28, s28, 3\n"
    "s_lshl_b32 s28, s28, 22\n"
    "s_add_u32 s29, s26, 1\n"
    "s_and_b32 s29, s29, 3\n"
    "s_lshl_b32 s29, s29, 22\n"
    "s_add_u32 s30, s26, 3\n"
    "s_and_b32 s30, s30, 3\n"
    "s_lshl_b32 s30, s30, 22\n"
    "s_and_b32 s40, s26, 63\n"
    "s_lshl_b32 s40, s40, 7\n"
    "s_add_u32 s31, s12, s28\n"
    "s_add_u32 s32, s13, s28\n"
    "s_add_u32 s33, s15, s28\n"
    "s_add_u32 s34, s14, s28\n"
    "s_add_u32 s38, s10, s29\n"
    "s_add_u32 s39, s11, s29\n"
    "s_add_u32 s40, s16, s40\n"
    "s_add_u32 s35, s34, 4096\n"
    "s_add_u32 s36, s34, 16384\n"
    "s_add_u32 s37, s34, 20480\n"
    "s_add_u32 s43, s41, 316\n"
    "s_lshl_b32 s45, s26, 19\n"
    "s_lshl_b32 s25, s26, 11\n"
    "s_xor_b32 s25, s25, s8\n"
    "s_and_b32 s25, s25, 0x2000\n"
    "s_add_u32 s45, s45, s25\n"
    "s_add_u32 s45, s45, s9\n"
    "s_mul_i32 s46, s26, 40960\n"
    "s_cmp_eq_u32 s8, 0\n"
    "s_cselect_b32 s46, s46, s45\n"
    "v_mov_b64 v[76:77], 0\n"
    "v_mov_b64 v[78:79], 0\n"
    "v_mov_b64 v[80:81], 0\n"
    "v_mov_b64 v[82:83], 0\n"
    "s_waitcnt vmcnt(11)\n"
    "v_lshlrev_b32 v148, 16, v186\n"
    "v_and_b32 v149, 0xffff0000, v186\n"
    "v_lshlrev_b32 v150, 16, v187\n"
    "v_and_b32 v151, 0xffff0000, v187\n"
    "s_waitcnt lgkmcnt(0)\n"
    "s_barrier\n"
    ".Lct_mk6x13_%=:\n"
    "ds_read_b128 v[164:167], v208 offset:0\n"
    "ds_read_b128 v[168:171], v208 offset:2560\n"
    "ds_read_b128 v[172:175], v208 offset:5120\n"
    "ds_read_b128 v[176:179], v208 offset:7680\n"
    "s_waitcnt lgkmcnt(2)\n"
    "v_pk_add_f32 v[164:165], v[164:165], v[168:169]\n"
    "v_pk_add_f32 v[166:167], v[166:167], v[170:171]\n"
    "s_waitcnt lgkmcnt(1)\n"
    "v_pk_add_f32 v[164:165], v[164:165], v[172:173]\n"
    "v_pk_add_f32 v[166:167], v[166:167], v[174:175]\n"
    "s_waitcnt lgkmcnt(0)\n"
    "v_pk_add_f32 v[164:165], v[164:165], v[176:177]\n"
    "v_pk_add_f32 v[166:167], v[166:167], v[178:179]\n"
    "v_cvt_pk_bf16_f32 v164, v164, v165\n"
    "v_cvt_pk_bf16_f32 v165, v166, v167\n"
    "v_lshlrev_b32 v168, 16, v164\n"
    "v_and_b32 v169, 0xffff0000, v164\n"
    "v_lshlrev_b32 v170, 16, v165\n"
    "v_and_b32 v171, 0xffff0000, v165\n"
    "v_pk_add_f32 v[168:169], v[168:169], v[148:149]\n"
    "v_pk_add_f32 v[170:171], v[170:171], v[150:151]\n"
    "v_cvt_pk_bf16_f32 v164, v168, v169\n"
    "v_cvt_pk_bf16_f32 v165, v170, v171\n"
    "buffer_store_dwordx2 v[164:165], v209, s[0:3], s42 offen sc1\n"
    ".Lct_mk5x14_%=:\n"
    ".Lct_rt19_%=:\n"
    "s_memrealtime s[22:23]\n"
    "s_waitcnt lgkmcnt(0)\n"
    "s_sub_u32 s24, s43, s22\n"
    "s_cmp_lt_u32 s24, 600\n"
    "s_cbranch_scc1 .Lct_rt19_%=\n"
    "buffer_load_dwordx2 v[190:191], off, s[0:3], s40 sc1\n"
    "s_waitcnt vmcnt(2)\n"
    "v_mov_b64 v[8:9], v[0:1]\n"
    "v_mov_b64 v[10:11], v[2:3]\n"
    "v_mov_b64 v[12:13], v[4:5]\n"
    "v_mov_b64 v[14:15], v[6:7]\n"
    "v_mov_b32_dpp v16, v0 row_shl:4 row_mask:0xf bank_mask:0xf bound_ctrl:1\n"
    "v_mov_b32_dpp v17, v1 row_shl:4 row_mask:0xf bank_mask:0xf bound_ctrl:1\n"
    "v_mov_b32_dpp v18, v2 row_shl:4 row_mask:0xf bank_mask:0xf bound_ctrl:1\n"
    "v_mov_b32_dpp v19, v3 row_shl:4 row_mask:0xf bank_mask:0xf bound_ctrl:1\n"
    "v_mov_b32_dpp v20, v4 row_shl:4 row_mask:0xf bank_mask:0xf bound_ctrl:1\n"
    "v_mov_b32_dpp v21, v5 row_shl:4 row_mask:0xf bank_mask:0xf bound_ctrl:1\n"
    "v_mov_b32_dpp v22, v6 row_shl:4 row_mask:0xf bank_mask:0xf bound_ctrl:1\n"
    "v_mov_b32_dpp v23, v7 row_shl:4 row_mask:0xf bank_mask:0xf bound_ctrl:1\n"
    "v_mov_b32_dpp v24, v0 row_shl:8 row_mask:0xf bank_mask:0xf bound_ctrl:1\n"
    "v_mov_b32_dpp v25, v1 row_shl:8 row_mask:0xf bank_mask:0xf bound_ctrl:1\n"
    "v_mov_b32_dpp v26, v2 row_shl:8 row_mask:0xf bank_mask:0xf bound_ctrl:1\n"
    "v_mov_b32_dpp v27, v3 row_shl:8 row_mask:0xf bank_mask:0xf bound_ctrl:1\n"
    "v_mov_b32_dpp v28, v4 row_shl:8 row_mask:0xf bank_mask:0xf bound_ctrl:1\n"
    "v_mov_b32_dpp v29, v5 row_shl:8 row_mask:0xf bank_mask:0xf bound_ctrl:1\n"
    "v_mov_b32_dpp v30, v6 row_shl:8 row_mask:0xf bank_mask:0xf bound_ctrl:1\n"
    "v_mov_b32_dpp v31, v7 row_shl:8 row_mask:0xf bank_mask:0xf bound_ctrl:1\n"
    "v_mov_b32_dpp v32, v0 row_shl:12 row_mask:0xf bank_mask:0xf bound_ctrl:1\n"
    "v_mov_b32_dpp v33, v1 row_shl:12 row_mask:0xf bank_mask:0xf bound_ctrl:1\n"
    "v_mov_b32_dpp v34, v2 row_shl:12 row_mask:0xf bank_mask:0xf bound_ctrl:1\n"
    "v_mov_b32_dpp v35, v3 row_shl:12 row_mask:0xf bank_mask:0xf bound_ctrl:1\n"
    "v_mov_b32_dpp v36, v4 row_shl:12 row_mask:0xf bank_mask:0xf bound_ctrl:1\n"
    "v_mov_b32_dpp v37, v5 row_shl:12 row_mask:0xf bank_mask:0xf bound_ctrl:1\n"
    "v_mov_b32_dpp v38, v6 row_shl:12 row_mask:0xf bank_mask:0xf bound_ctrl:1\n"
    "v_mov_b32_dpp v39, v7 row_shl:12 row_mask:0xf bank_mask:0xf bound_ctrl:1\n"
    "v_mov_b32 v148, s26\n"
    "v_mov_b32 v149, s26\n"
    "v_mov_b32 v150, s26\n"
    "v_mov_b32 v151, s26\n"
    "s_waitcnt vmcnt(0)\n"
    "s_barrier\n"
    "buffer_store_dwordx4 v[148:151], v211, s[0:3], 0 offen sc1\n"
    ".Lct_x1_%=:\n"
    "v_readfirstlane_b32 s25, v191\n"
    "v_readfirstlane_b32 s41, v190\n"
    "s_cmp_eq_u32 s25, s27\n"
    "s_cbranch_scc0 .Lct_xr1_%=\n"
    ".Lct_xg1_%=:\n"
    "s_add_u32 s43, s41, 93\n"
    "s_add_u32 s44, s41, 109\n"
    ".Lct_mk0x19_%=:\n"
    ".Lct_rt21_%=:\n"
    "s_memrealtime s[22:23]\n"
    "s_waitcnt lgkmcnt(0)\n"
    "s_sub_u32 s24, s43, s22\n"
    "s_cmp_lt_u32 s24, 600\n"
    "s_cbranch_scc1 .Lct_rt21_%=\n"
    "buffer_load_dwordx4 v[40:43], v192, s[0:3], s31 offen offset:0 sc1\n"
    "buffer_load_dwordx4 v[44:47], v192, s[0:3], s31 offen offset:1024 sc1\n"
    "buffer_load_dwordx4 v[48:51], v197, s[0:3], s32 offen offset:0 sc1\n"
    "buffer_load_dwordx4 v[52:55], v197, s[0:3], s32 offen offset:256 sc1\n"
    ".Lct_rt22_%=:\n"
    "s_memrealtime s[22:23]\n"
    "s_waitcnt lgkmcnt(0)\n"
    "s_sub_u32 s24, s44, s22\n"
    "s_cmp_lt_u32 s24, 600\n"
    "s_cbranch_scc1 .Lct_rt22_%=\n"
    "buffer_load_dwordx4 v[72:75], v193, s[0:3], s33 offen sc1\n"
    "buffer_load_dwordx4 v[56:59], v192, s[0:3], s33 offen offset:0 sc1\n"
    "buffer_load_dwordx4 v[60:63], v192, s[0:3], s33 offen offset:1024 sc1\n"
    "buffer_load_dwordx4 v[64:67], v192, s[0:3], s33 offen offset:2048 sc1\n"
    "buffer_load_dwordx4 v[68:71], v192, s[0:3], s33 offen offset:3072 sc1\n"
    ".Lct_mk1x22_%=:\n"
    ".Lct_take1_%=:\n"
    "s_waitcnt vmcnt(5)\n"
    "v_max_u32 v184, v40, v44\n"
    "v_max3_u32 v184, v48, v50, v184\n"
    "v_max3_u32 v184, v52, v54, v184\n"
    "s_waitcnt vmcnt(4)\n"
    "v_smfmac_f32_16x16x64_bf16 v[76:79], v[72:75], v[40:47], v194\n"
    "v_max3_u32 v184, v72, v74, v184\n"
    "v_smfmac_f32_16x16x64_bf16 v[80:83], v[72:75], v[48:55], v194\n"
    "s_waitcnt vmcnt(3)\n"
    "v_smfmac_f32_16x16x64_bf16 v[80:83], v[56:59], v[8:15], v194\n"
    "v_max3_u32 v184, v56, v58, v184\n"
    "v_smfmac_f32_16x16x64_bf16 v[76:79], v[56:59], a[32:39], v194\n"
    "s_waitcnt vmcnt(2)\n"
    "v_smfmac_f32_16x16x64_bf16 v[80:83], v[60:63], v[16:23], v194\n"
    "v_max3_u32 v184, v60, v62, v184\n"
    "v_smfmac_f32_16x16x64_bf16 v[76:79], v[60:63], a[40:47], v194\n"
    "s_waitcnt vmcnt(1)\n"
    "v_smfmac_f32_16x16x64_bf16 v[80:83], v[64:67], v[24:31], v194\n"
    "v_max3_u32 v184, v64, v66, v184\n"
    "v_smfmac_f32_16x16x64_bf16 v[76:79], v[64:67], a[48:55], v194\n"
    "s_waitcnt vmcnt(0)\n"
    "v_smfmac_f32_16x16x64_bf16 v[80:83], v[68:71], v[32:39], v194\n"
    "v_max3_u32 v184, v68, v70, v184\n"
    "v_smfmac_f32_16x16x64_bf16 v[76:79], v[68:71], a[56:63], v194\n"
    ".Lct_mk2x23_%=:\n"
    "s_nop 1\n"
    "v_cmp_eq_u32 vcc, -1, v184\n"
    "s_nop 1\n"
    "v_add_f32 v80, v80, v82\n"
    "v_add_f32 v81, v81, v83\n"
    "s_nop 0\n"
    "v_pk_add_f32 v[76:77], v[76:77], v[78:79]\n"
    "ds_write2_b32 v201, v80, v81 offset1:20\n"
    "ds_write2_b32 v200, v76, v77 offset1:20\n"
    "s_cbranch_vccnz .Lct_retry1_%=\n"
    ".Lct_go1_%=:\n"
    "s_waitcnt lgkmcnt(0)\n"
    "s_barrier\n"
    "ds_read_b128 v[164:167], v202 offset:0\n"
    "ds_read_b128 v[168:171], v202 offset:640\n"
    "ds_read_b128 v[172:175], v202 offset:1280\n"
    "ds_read_b128 v[176:179], v202 offset:1920\n"
    "s_waitcnt lgkmcnt(2)\n"
    "v_pk_add_f32 v[164:165], v[164:165], v[168:169]\n"
    "v_pk_add_f32 v[166:167], v[166:167], v[170:171]\n"
    "s_waitcnt lgkmcnt(1)\n"
    "v_pk_add_f32 v[164:165], v[164:165], v[172:173]\n"
    "v_pk_add_f32 v[166:167], v[166:167], v[174:175]\n"
    "s_waitcnt lgkmcnt(0)\n"
    "v_pk_add_f32 v[164:165], v[164:165], v[176:177]\n"
    "v_pk_add_f32 v[166:167], v[166:167], v[178:179]\n"
    "v_cvt_pk_bf16_f32 v164, v164, v165\n"
    "v_cvt_pk_bf16_f32 v165, v166, v167\n"
    "v_min_u32 v164, -2, v164\n"
    "buffer_store_dwordx2 v[164:165], v203, s[0:3], s29 offen sc1\n"
    ".Lct_mk3x24_%=:\n"
    "s_sleep 2\n"
    "v_mov_b64 v[166:167], -1\n"
    "buffer_load_dwordx4 v[132:135], v199, s[0:3], s34 offen offset:0 sc1\n"
    "buffer_load_dwordx4 v[136:139], v199, s[0:3], s36 offen offset:0 sc1\n"
    "buffer_load_dwordx4 v[140:143], v199, s[0:3], s34 offen offset:256 sc1\n"
    "buffer_load_dwordx4 v[144:147], v199, s[0:3], s36 offen offset:256 sc1\n"
    "buffer_load_dwordx4 v[116:119], v198, s[0:3], s34 offen sc1\n"
    "buffer_load_dwordx4 v[84:87], v192, s[0:3], s34 offen offset:0 sc1\n"
    "buffer_load_dwordx4 v[88:91], v192, s[0:3], s34 offen offset:2048 sc1\n"
    "buffer_load_dwordx4 v[120:123], v198, s[0:3], s35 offen sc1\n"
    "buffer_load_dwordx4 v[92:95], v192, s[0:3], s35 offen offset:0 sc1\n"
    "buffer_load_dwordx4 v[96:99], v192, s[0:3], s35 offen offset:2048 sc1\n"
    ".Lct_mk7x25_%=:\n"
    "buffer_load_dwordx4 v[124:127], v198, s[0:3], s36 offen sc1\n"
    "s_waitcnt vmcnt(10)\n"
    "v_max3_u32 v184, v132, v134, v132\n"
    "v_swap_b32 v133, v134\n"
    "s_nop 1\n"
    "v_mfma_f32_16x16x32_bf16 v[148:151], v[132:135], v[40:43], 0\n"
    "v_mfma_f32_16x16x32_bf16 v[152:155], v[132:135], v[48:51], 0\n"
    "buffer_load_dwordx4 v[100:103], v192, s[0:3], s36 offen offset:0 sc1\n"
    "s_waitcnt vmcnt(10)\n"
    "v_max3_u32 v184, v136, v138, v184\n"
    "v_swap_b32 v137, v138\n"
    "s_nop 1\n"
    "v_mfma_f32_16x16x32_bf16 v[148:151], v[136:139], v[44:47], v[148:151]\n"
    "v_mfma_f32_16x16x32_bf16 v[152:155], v[136:139], v[52:55], v[152:155]\n"
    "buffer_load_dwordx4 v[104:107], v192, s[0:3], s36 offen offset:2048 sc1\n"
    "s_waitcnt vmcnt(10)\n"
    "v_max3_u32 v184, v140, v142, v184\n"
    "v_swap_b32 v141, v142\n"
    "s_nop 1\n"
    "v_mfma_f32_16x16x32_bf16 v[156:159], v[140:143], v[40:43], 0\n"
    "v_mfma_f32_16x16x32_bf16 v[160:163], v[140:143], v[48:51], 0\n"
    "buffer_load_dwordx4 v[128:131], v198, s[0:3], s37 offen sc1\n"
    "s_waitcnt vmcnt(10)\n"
    "v_max3_u32 v184, v144, v146, v184\n"
    "v_swap_b32 v145, v146\n"
    "s_nop 1\n"
    "v_mfma_f32_16x16x32_bf16 v[156:159], v[144:147], v[44:47], v[156:159]\n"
    "v_mfma_f32_16x16x32_bf16 v[160:163], v[144:147], v[52:55], v[160:163]\n"
    "buffer_load_dwordx4 v[108:111], v192, s[0:3], s37 offen offset:0 sc1\n"
    "s_waitcnt vmcnt(10)\n"
    "v_max3_u32 v184, v116, v118, v184\n"
    "v_smfmac_f32_16x16x64_bf16 v[148:151], v[116:119], a[32:39], v195\n"
    "v_smfmac_f32_16x16x64_bf16 v[152:155], v[116:119], v[8:15], v195\n"
    "buffer_load_dwordx4 v[112:115], v192, s[0:3], s37 offen offset:2048 sc1\n"
    "s_waitcnt vmcnt(10)\n"
    "v_max3_u32 v184, v84, v86, v184\n"
    "v_swap_b32 v85, v86\n"
    "s_nop 1\n"
    "v_mfma_f32_16x16x32_bf16 v[156:159], v[84:87], a[32:35], v[156:159]\n"
    "v_mfma_f32_16x16x32_bf16 v[160:163], v[84:87], v[8:11], v[160:163]\n"
    "buffer_load_dwordx2 v[186:187], v210, s[4:7], s46 offen sc1\n"
    "s_waitcnt vmcnt(10)\n"
    "v_max3_u32 v184, v88, v90, v184\n"
    "v_swap_b32 v89, v90\n"
    "s_nop 1\n"
    "v_mfma_f32_16x16x32_bf16 v[156:159], v[88:91], a[36:39], v[156:159]\n"
    "v_mfma_f32_16x16x32_bf16 v[160:163], v[88:91], v[12:15], v[160:163]\n"
    "buffer_store_dwordx2 v[166:167], v203, s[0:3], s30 offen sc1\n"
    "s_waitcnt vmcnt(10)\n"
    "v_max3_u32 v184, v120, v122, v184\n"
    "v_smfmac_f32_16x16x64_bf16 v[148:151], v[120:123], a[40:47], v195\n"
    "v_smfmac_f32_16x16x64_bf16 v[152:155], v[120:123], v[16:23], v195\n"
    "buffer_load_dwordx4 a[0:3], v192, s[0:3], s38 offen offset:0 sc1\n"
    "s_waitcnt vmcnt(10)\n"
    "v_max3_u32 v184, v92, v94, v184\n"
    "v_swap_b32 v93, v94\n"
    "s_nop 1\n"
    "v_mfma_f32_16x16x32_bf16 v[156:159], v[92:95], a[40:43], v[156:159]\n"
    "v_mfma_f32_16x16x32_bf16 v[160:163], v[92:95], v[16:19], v[160:163]\n"
    "buffer_load_dwordx4 a[4:7], v192, s[0:3], s38 offen offset:1024 sc1\n"
    "s_waitcnt vmcnt(10)\n"
    "v_max3_u32 v184, v96, v98, v184\n"
    "v_swap_b32 v97, v98\n"
    "s_nop 1\n"
    "v_mfma_f32_16x16x32_bf16 v[156:159], v[96:99], a[44:47], v[156:159]\n"
    "v_mfma_f32_16x16x32_bf16 v[160:163], v[96:99], v[20:23], v[160:163]\n"
    "buffer_load_dwordx4 a[8:11], v192, s[0:3], s38 offen offset:2048 sc1\n"
    "s_waitcnt vmcnt(10)\n"
    "v_max3_u32 v184, v124, v126, v184\n"
    "v_smfmac_f32_16x16x64_bf16 v[148:151], v[124:127], a[48:55], v195\n"
    "v_smfmac_f32_16x16x64_bf16 v[152:155], v[124:127], v[24:31], v195\n"
    "buffer_load_dwordx4 a[12:15], v192, s[0:3], s38 offen offset:3072 sc1\n"
    "s_waitcnt vmcnt(10)\n"
    "v_max3_u32 v184, v100, v102, v184\n"
    "v_swap_b32 v101, v102\n"
    "s_nop 1\n"
    "v_mfma_f32_16x16x32_bf16 v[156:159], v[100:103], a[48:51], v[156:159]\n"
    "v_mfma_f32_16x16x32_bf16 v[160:163], v[100:103], v[24:27], v[160:163]\n"
    "buffer_load_dwordx4 a[16:19], v193, s[0:3], s38 offen offset:0 sc1\n"
    "s_waitcnt vmcnt(10)\n"
    "v_max3_u32 v184, v104, v106, v184\n"
    "v_swap_b32 v105, v106\n"
    "s_nop 1\n"
    "v_mfma_f32_16x16x32_bf16 v[156:159], v[104:107], a[52:55], v[156:159]\n"
    "v_mfma_f32_16x16x32_bf16 v[160:163], v[104:107], v[28:31], v[160:163]\n"
    "buffer_load_dwordx4 a[20:23], v193, s[0:3], s38 offen offset:1024 sc1\n"
    "s_waitcnt vmcnt(10)\n"
    "v_max3_u32 v184, v128, v130, v184\n"
    "v_smfmac_f32_16x16x64_bf16 v[148:151], v[128:131], a[56:63], v195\n"
    "v_smfmac_f32_16x16x64_bf16 v[152:155], v[128:131], v[32:39], v195\n"
    "buffer_load_dwordx4 a[24:27], v193, s[0:3], s38 offen offset:2048 sc1\n"
    "s_waitcnt vmcnt(10)\n"
    "v_max3_u32 v184, v108, v110, v184\n"
    "v_swap_b32 v109, v110\n"
    "s_nop 1\n"
    "v_mfma_f32_16x16x32_bf16 v[156:159], v[108:111], a[56:59], v[156:159]\n"
    "v_mfma_f32_16x16x32_bf16 v[160:163], v[108:111], v[32:35], v[160:163]\n"
    "buffer_load_dwordx4 a[28:31], v193, s[0:3], s38 offen offset:3072 sc1\n"
    "s_waitcnt vmcnt(10)\n"
    "v_max3_u32 v184, v112, v114, v184\n"
    "v_swap_b32 v113, v114\n"
    "s_nop 1\n"
    "v_mfma_f32_16x16x32_bf16 v[156:159], v[112:115], a[60:63], v[156:159]\n"
    "v_mfma_f32_16x16x32_bf16 v[160:163], v[112:115], v[36:39], v[160:163]\n"
    "v_cmp_eq_u32 vcc, -1, v184\n"
    "s_nop 0\n"
    "s_cbranch_vccnz .Lct_tr1_%=\n"
    ".Lct_tg1_%=:\n"
    ".Lct_mk4x26_%=:\n"
    "ds_write2_b32 v204, v148, v149 offset0:0 offset1:20\n"
    "ds_write2_b32 v204, v150, v151 offset0:40 offset1:60\n"
    "ds_write2_b32 v205, v152, v153 offset0:0 offset1:20\n"
    "ds_write2_b32 v205, v154, v155 offset0:40 offset1:60\n"
    "ds_write2_b32 v206, v156, v157 offset0:0 offset1:20\n"
    "ds_write2_b32 v206, v158, v159 offset0:40 offset1:60\n"
    "ds_write2_b32 v207, v160, v161 offset0:0 offset1:20\n"
    "ds_write2_b32 v207, v162, v163 offset0:40 offset1:60\n"
    "s_mov_b32 s42, s45\n"
    "buffer_load_dwordx4 v[0:3], v196, s[0:3], s39 offen offset:0 sc1\n"
    "buffer_load_dwordx4 v[4:7], v196, s[0:3], s39 offen offset:1024 sc1\n"
    "s_add_u32 s26, s26, 1\n"
    "s_add_u32 s27, s26, 1\n"
    "s_add_u32 s28, s26, 0\n"
    "s_and_b32 s28, s28, 3\n"
    "s_lshl_b32 s28, s28, 22\n"
    "s_add_u32 s29, s26, 1\n"
    "s_and_b32 s29, s29, 3\n"
    "s_lshl_b32 s29, s29, 22\n"
    "s_add_u32 s30, s26, 3\n"
    "s_and_b32 s30, s30, 3\n"
    "s_lshl_b32 s30, s30, 22\n"
    "s_and_b32 s40, s26, 63\n"
    "s_lshl_b32 s40, s40, 7\n"
    "s_add_u32 s31, s12, s28\n"
    "s_add_u32 s32, s13, s28\n"
    "s_add_u32 s33, s15, s28\n"
    "s_add_u32 s34, s14, s28\n"
    "s_add_u32 s38, s10, s29\n"
    "s_add_u32 s39, s11, s29\n"
    "s_add_u32 s40, s16, s40\n"
    "s_add_u32 s35, s34, 4096\n"
    "s_add_u32 s36, s34, 16384\n"
    "s_add_u32 s37, s34, 20480\n"
    "s_add_u32 s43, s41, 316\n"
    "s_lshl_b32 s45, s26, 19\n"
    "s_lshl_b32 s25, s26, 11\n"
    "s_xor_b32 s25, s25, s8\n"
    "s_and_b32 s25, s25, 0x2000\n"
    "s_add_u32 s45, s45, s25\n"
    "s_add_u32 s45, s45, s9\n"
    "s_mul_i32 s46, s26, 40960\n"
    "s_cmp_eq_u32 s8, 0\n"
    "s_cselect_b32 s46, s46, s45\n"
    "v_mov_b64 v[76:77], 0\n"
    "v_mov_b64 v[78:79], 0\n"
    "v_mov_b64 v[80:81], 0\n"
    "v_mov_b64 v[82:83], 0\n"
    "s_waitcnt vmcnt(11)\n"
    "v_lshlrev_b32 v148, 16, v186\n"
    "v_and_b32 v149, 0xffff0000, v186\n"
    "v_lshlrev_b32 v150, 16, v187\n"
    "v_and_b32 v151, 0xffff0000, v187\n"
    "s_waitcnt lgkmcnt(0)\n"
    "s_barrier\n"
    ".Lct_mk6x27_%=:\n"
    "ds_read_b128 v[164:167], v208 offset:0\n"
    "ds_read_b128 v[168:171], v208 offset:2560\n"
    "ds_read_b128 v[172:175], v208 offset:5120\n"
    "ds_read_b128 v[176:179], v208 offset:7680\n"
    "s_waitcnt lgkmcnt(2)\n"
    "v_pk_add_f32 v[164:165], v[164:165], v[168:169]\n"
    "v_pk_add_f32 v[166:167], v[166:167], v[170:171]\n"
    "s_waitcnt lgkmcnt(1)\n"
    "v_pk_add_f32 v[164:165], v[164:165], v[172:173]\n"
    "v_pk_add_f32 v[166:167], v[166:167], v[174:175]\n"
    "s_waitcnt lgkmcnt(0)\n"
    "v_pk_add_f32 v[164:165], v[164:165], v[176:177]\n"
    "v_pk_add_f32 v[166:167], v[166:167], v[178:179]\n"
    "v_cvt_pk_bf16_f32 v164, v164, v165\n"
    "v_cvt_pk_bf16_f32 v165, v166, v167\n"
    "v_lshlrev_b32 v168, 16, v164\n"
    "v_and_b32 v169, 0xffff0000, v164\n"
    "v_lshlrev_b32 v170, 16, v165\n"
    "v_and_b32 v171, 0xffff0000, v165\n"
    "v_pk_add_f32 v[168:169], v[168:169], v[148:149]\n"
    "v_pk_add_f32 v[170:171], v[170:171], v[150:151]\n"
    "v_cvt_pk_bf16_f32 v164, v168, v169\n"
    "v_cvt_pk_bf16_f32 v165, v170, v171\n"
    "buffer_store_dwordx2 v[164:165], v209, s[0:3], s42 offen sc1\n"
    ".Lct_mk5x28_%=:\n"
    "s_cmp_lt_u32 s26, 64\n"
    "s_cbranch_scc1 .Lct_top_%=\n"
    "v_mov_b32 v148, s26\n"
    "v_mov_b32 v149, s26\n"
    "v_mov_b32 v150, s26\n"
    "v_mov_b32 v151, s26\n"
    "s_waitcnt vmcnt(0)\n"
    "s_barrier\n"
    "buffer_store_dwordx4 v[148:151], v211, s[0:3], 0 offen sc1\n"
    "s_waitcnt vmcnt(0) lgkmcnt(0)\n"
    "s_endpgm\n"
    ".Lct_xr0_%=:\n"
    "s_add_u32 s20, s20, 1\n"
    "s_cmp_lt_u32 s20, 0x40000\n"
    "s_cbranch_scc1 .Lct_sp16_%=\n"
    "v_mov_b32 v184, 42\n"
    "s_mov_b32 s24, 33728\n"
    "buffer_store_dword v184, off, s[0:3], s24 sc1\n"
    "s_waitcnt vmcnt(0)\n"
    "s_branch .Lct_xg0_%=\n"
    ".Lct_sp16_%=:\n"
    "s_sleep 4\n"
    "buffer_load_dwordx2 v[190:191], off, s[0:3], s40 sc1\n"
    "s_waitcnt vmcnt(0)\n"
    "s_branch .Lct_x0_%=\n"
    ".Lct_retry0_%=:\n"
    "s_add_u32 s20, s20, 1\n"
    "s_cmp_lt_u32 s20, 0x40000\n"
    "s_cbranch_scc1 .Lct_sp17_%=\n"
    "v_mov_b32 v184, 3\n"
    "s_mov_b32 s24, 33728\n"
    "buffer_store_dword v184, off, s[0:3], s24 sc1\n"
    "s_waitcnt vmcnt(0)\n"
    "s_branch .Lct_go0_%=\n"
    ".Lct_sp17_%=:\n"
    "s_waitcnt vmcnt(0) lgkmcnt(0)\n"
    "v_mov_b64 v[76:77], 0\n"
    "v_mov_b64 v[78:79], 0\n"
    "v_mov_b64 v[80:81], 0\n"
    "v_mov_b64 v[82:83], 0\n"
    "buffer_load_dwordx4 v[40:43], v192, s[0:3], s31 offen offset:0 sc1\n"
    "buffer_load_dwordx4 v[44:47], v192, s[0:3], s31 offen offset:1024 sc1\n"
    "buffer_load_dwordx4 v[48:51], v197, s[0:3], s32 offen offset:0 sc1\n"
    "buffer_load_dwordx4 v[52:55], v197, s[0:3], s32 offen offset:256 sc1\n"
    "buffer_load_dwordx4 v[72:75], v193, s[0:3], s33 offen sc1\n"
    "buffer_load_dwordx4 v[56:59], v192, s[0:3], s33 offen offset:0 sc1\n"
    "buffer_load_dwordx4 v[60:63], v192, s[0:3], s33 offen offset:1024 sc1\n"
    "buffer_load_dwordx4 v[64:67], v192, s[0:3], s33 offen offset:2048 sc1\n"
    "buffer_load_dwordx4 v[68:71], v192, s[0:3], s33 offen offset:3072 sc1\n"
    "s_waitcnt vmcnt(0)\n"
    "s_branch .Lct_take0_%=\n"
    ".Lct_tr0_%=:\n"
    "s_add_u32 s20, s20, 1\n"
    "s_cmp_lt_u32 s20, 0x40000\n"
    "s_cbranch_scc1 .Lct_sp18_%=\n"
    "v_mov_b32 v184, 4\n"
    "s_mov_b32 s24, 33728\n"
    "buffer_store_dword v184, off, s[0:3], s24 sc1\n"
    "s_waitcnt vmcnt(0)\n"
    "s_branch .Lct_tg0_%=\n"
    ".Lct_sp18_%=:\n"
    "buffer_load_dwordx4 v[132:135], v199, s[0:3], s34 offen offset:0 sc1\n"
    "buffer_load_dwordx4 v[136:139], v199, s[0:3], s36 offen offset:0 sc1\n"
    "buffer_load_dwordx4 v[140:143], v199, s[0:3], s34 offen offset:256 sc1\n"
    "buffer_load_dwordx4 v[144:147], v199, s[0:3], s36 offen offset:256 sc1\n"
    "buffer_load_dwordx4 v[116:119], v198, s[0:3], s34 offen sc1\n"
    "buffer_load_dwordx4 v[84:87], v192, s[0:3], s34 offen offset:0 sc1\n"
    "buffer_load_dwordx4 v[88:91], v192, s[0:3], s34 offen offset:2048 sc1\n"
    "buffer_load_dwordx4 v[120:123], v198, s[0:3], s35 offen sc1\n"
    "buffer_load_dwordx4 v[92:95], v192, s[0:3], s35 offen offset:0 sc1\n"
    "buffer_load_dwordx4 v[96:99], v192, s[0:3], s35 offen offset:2048 sc1\n"
    "buffer_load_dwordx4 v[124:127], v198, s[0:3], s36 offen sc1\n"
    "buffer_load_dwordx4 v[100:103], v192, s[0:3], s36 offen offset:0 sc1\n"
    "buffer_load_dwordx4 v[104:107], v192, s[0:3], s36 offen offset:2048 sc1\n"
    "buffer_load_dwordx4 v[128:131], v198, s[0:3], s37 offen sc1\n"
    "buffer_load_dwordx4 v[108:111], v192, s[0:3], s37 offen offset:0 sc1\n"
    "buffer_load_dwordx4 v[112:115], v192, s[0:3], s37 offen offset:2048 sc1\n"
    "s_waitcnt vmcnt(0)\n"
    "v_max3_u32 v184, v132, v134, v132\n"
    "v_swap_b32 v133, v134\n"
    "s_nop 1\n"
    "v_mfma_f32_16x16x32_bf16 v[148:151], v[132:135], v[40:43], 0\n"
    "v_mfma_f32_16x16x32_bf16 v[152:155], v[132:135], v[48:51], 0\n"
    "v_max3_u32 v184, v136, v138, v184\n"
    "v_swap_b32 v137, v138\n"
    "s_nop 1\n"
    "v_mfma_f32_16x16x32_bf16 v[148:151], v[136:139], v[44:47], v[148:151]\n"
    "v_mfma_f32_16x16x32_bf16 v[152:155], v[136:139], v[52:55], v[152:155]\n"
    "v_max3_u32 v184, v140, v142, v184\n"
    "v_swap_b32 v141, v142\n"
    "s_nop 1\n"
    "v_mfma_f32_16x16x32_bf16 v[156:159], v[140:143], v[40:43], 0\n"
    "v_mfma_f32_16x16x32_bf16 v[160:163], v[140:143], v[48:51], 0\n"
    "v_max3_u32 v184, v144, v146, v184\n"
    "v_swap_b32 v145, v146\n"
    "s_nop 1\n"
    "v_mfma_f32_16x16x32_bf16 v[156:159], v[144:147], v[44:47], v[156:159]\n"
    "v_mfma_f32_16x16x32_bf16 v[160:163], v[144:147], v[52:55], v[160:163]\n"
    "v_max3_u32 v184, v116, v118, v184\n"
    "v_smfmac_f32_16x16x64_bf16 v[148:151], v[116:119], a[0:7], v195\n"
    "v_smfmac_f32_16x16x64_bf16 v[152:155], v[116:119], v[8:15], v195\n"
    "v_max3_u32 v184, v84, v86, v184\n"
    "v_swap_b32 v85, v86\n"
    "s_nop 1\n"
    "v_mfma_f32_16x16x32_bf16 v[156:159], v[84:87], a[0:3], v[156:159]\n"
    "v_mfma_f32_16x16x32_bf16 v[160:163], v[84:87], v[8:11], v[160:163]\n"
    "v_max3_u32 v184, v88, v90, v184\n"
    "v_swap_b32 v89, v90\n"
    "s_nop 1\n"
    "v_mfma_f32_16x16x32_bf16 v[156:159], v[88:91], a[4:7], v[156:159]\n"
    "v_mfma_f32_16x16x32_bf16 v[160:163], v[88:91], v[12:15], v[160:163]\n"
    "v_max3_u32 v184, v120, v122, v184\n"
    "v_smfmac_f32_16x16x64_bf16 v[148:151], v[120:123], a[8:15], v195\n"
    "v_smfmac_f32_16x16x64_bf16 v[152:155], v[120:123], v[16:23], v195\n"
    "v_max3_u32 v184, v92, v94, v184\n"
    "v_swap_b32 v93, v94\n"
    "s_nop 1\n"
    "v_mfma_f32_16x16x32_bf16 v[156:159], v[92:95], a[8:11], v[156:159]\n"
    "v_mfma_f32_16x16x32_bf16 v[160:163], v[92:95], v[16:19], v[160:163]\n"
    "v_max3_u32 v184, v96, v98, v184\n"
    "v_swap_b32 v97, v98\n"
    "s_nop 1\n"
    "v_mfma_f32_16x16x32_bf16 v[156:159], v[96:99], a[12:15], v[156:159]\n"
    "v_mfma_f32_16x16x32_bf16 v[160:163], v[96:99], v[20:23], v[160:163]\n"
    "v_max3_u32 v184, v124, v126, v184\n"
    "v_smfmac_f32_16x16x64_bf16 v[148:151], v[124:127], a[16:23], v195\n"
    "v_smfmac_f32_16x16x64_bf16 v[152:155], v[124:127], v[24:31], v195\n"
    "v_max3_u32 v184, v100, v102, v184\n"
    "v_swap_b32 v101, v102\n"
    "s_nop 1\n"
    "v_mfma_f32_16x16x32_bf16 v[156:159], v[100:103], a[16:19], v[156:159]\n"
    "v_mfma_f32_16x16x32_bf16 v[160:163], v[100:103], v[24:27], v[160:163]\n"
    "v_max3_u32 v184, v104, v106, v184\n"
    "v_swap_b32 v105, v106\n"
    "s_nop 1\n"
    "v_mfma_f32_16x16x32_bf16 v[156:159], v[104:107], a[20:23], v[156:159]\n"
    "v_mfma_f32_16x16x32_bf16 v[160:163], v[104:107], v[28:31], v[160:163]\n"
    "v_max3_u32 v184, v128, v130, v184\n"
    "v_smfmac_f32_16x16x64_bf16 v[148:151], v[128:131], a[24:31], v195\n"
    "v_smfmac_f32_16x16x64_bf16 v[152:155], v[128:131], v[32:39], v195\n"
    "v_max3_u32 v184, v108, v110, v184\n"
    "v_swap_b32 v109, v110\n"
    "s_nop 1\n"
    "v_mfma_f32_16x16x32_bf16 v[156:159], v[108:111], a[24:27], v[156:159]\n"
    "v_mfma_f32_16x16x32_bf16 v[160:163], v[108:111], v[32:35], v[160:163]\n"
    "v_max3_u32 v184, v112, v114, v184\n"
    "v_swap_b32 v113, v114\n"
    "s_nop 1\n"
    "v_mfma_f32_16x16x32_bf16 v[156:159], v[112:115], a[28:31], v[156:159]\n"
    "v_mfma_f32_16x16x32_bf16 v[160:163], v[112:115], v[36:39], v[160:163]\n"
    "v_cmp_eq_u32 vcc, -1, v184\n"
    "s_nop 0\n"
    "s_cbranch_vccnz .Lct_tr0_%=\n"
    "s_nop 7\n"
    "s_branch .Lct_tg0_%=\n"
    ".Lct_xr1_%=:\n"
    "s_add_u32 s20, s20, 1\n"
    "s_cmp_lt_u32 s20, 0x40000\n"
    "s_cbranch_scc1 .Lct_sp30_%=\n"
    "v_mov_b32 v184, 42\n"
    "s_mov_b32 s24, 33728\n"
    "buffer_store_dword v184, off, s[0:3], s24 sc1\n"
    "s_waitcnt vmcnt(0)\n"
    "s_branch .Lct_xg1_%=\n"
    ".Lct_sp30_%=:\n"
    "s_sleep 4\n"
    "buffer_load_dwordx2 v[190:191], off, s[0:3], s40 sc1\n"
    "s_waitcnt vmcnt(0)\n"
    "s_branch .Lct_x1_%=\n"
    ".Lct_retry1_%=:\n"
    "s_add_u32 s20, s20, 1\n"
    "s_cmp_lt_u32 s20, 0x40000\n"
    "s_cbranch_scc1 .Lct_sp31_%=\n"
    "v_mov_b32 v184, 3\n"
    "s_mov_b32 s24, 33728\n"
    "buffer_store_dword v184, off, s[0:3], s24 sc1\n"
    "s_waitcnt vmcnt(0)\n"
    "s_branch .Lct_go1_%=\n"
    ".Lct_sp31_%=:\n"
    "s_waitcnt vmcnt(0) lgkmcnt(0)\n"
    "v_mov_b64 v[76:77], 0\n"
    "v_mov_b64 v[78:79], 0\n"
    "v_mov_b64 v[80:81], 0\n"
    "v_mov_b64 v[82:83], 0\n"
    "buffer_load_dwordx4 v[40:43], v192, s[0:3], s31 offen offset:0 sc1\n"
    "buffer_load_dwordx4 v[44:47], v192, s[0:3], s31 offen offset:1024 sc1\n"
    "buffer_load_dwordx4 v[48:51], v197, s[0:3], s32 offen offset:0 sc1\n"
    "buffer_load_dwordx4 v[52:55], v197, s[0:3], s32 offen offset:256 sc1\n"
    "buffer_load_dwordx4 v[72:75], v193, s[0:3], s33 offen sc1\n"
    "buffer_load_dwordx4 v[56:59], v192, s[0:3], s33 offen offset:0 sc1\n"
    "buffer_load_dwordx4 v[60:63], v192, s[0:3], s33 offen offset:1024 sc1\n"
    "buffer_load_dwordx4 v[64:67], v192, s[0:3], s33 offen offset:2048 sc1\n"
    "buffer_load_dwordx4 v[68:71], v192, s[0:3], s33 offen offset:3072 sc1\n"
    "s_waitcnt vmcnt(0)\n"
    "s_branch .Lct_take1_%=\n"
    ".Lct_tr1_%=:\n"
    "s_add_u32 s20, s20, 1\n"
    "s_cmp_lt_u32 s20, 0x40000\n"
    "s_cbranch_scc1 .Lct_sp32_%=\n"
    "v_mov_b32 v184, 4\n"
    "s_mov_b32 s24, 33728\n"
    "buffer_store_dword v184, off, s[0:3], s24 sc1\n"
    "s_waitcnt vmcnt(0)\n"
    "s_branch .Lct_tg1_%=\n"
    ".Lct_sp32_%=:\n"
    "buffer_load_dwordx4 v[132:135], v199, s[0:3], s34 offen offset:0 sc1\n"
    "buffer_load_dwordx4 v[136:139], v199, s[0:3], s36 offen offset:0 sc1\n"
    "buffer_load_dwordx4 v[140:143], v199, s[0:3], s34 offen offset:256 sc1\n"
    "buffer_load_dwordx4 v[144:147], v199, s[0:3], s36 offen offset:256 sc1\n"
    "buffer_load_dwordx4 v[116:119], v198, s[0:3], s34 offen sc1\n"
    "buffer_load_dwordx4 v[84:87], v192, s[0:3], s34 offen offset:0 sc1\n"
    "buffer_load_dwordx4 v[88:91], v192, s[0:3], s34 offen offset:2048 sc1\n"
    "buffer_load_dwordx4 v[120:123], v198, s[0:3], s35 offen sc1\n"
    "buffer_load_dwordx4 v[92:95], v192, s[0:3], s35 offen offset:0 sc1\n"
    "buffer_load_dwordx4 v[96:99], v192, s[0:3], s35 offen offset:2048 sc1\n"
    "buffer_load_dwordx4 v[124:127], v198, s[0:3], s36 offen sc1\n"
    "buffer_load_dwordx4 v[100:103], v192, s[0:3], s36 offen offset:0 sc1\n"
    "buffer_load_dwordx4 v[104:107], v192, s[0:3], s36 offen offset:2048 sc1\n"
    "buffer_load_dwordx4 v[128:131], v198, s[0:3], s37 offen sc1\n"
    "buffer_load_dwordx4 v[108:111], v192, s[0:3], s37 offen offset:0 sc1\n"
    "buffer_load_dwordx4 v[112:115], v192, s[0:3], s37 offen offset:2048 sc1\n"
    "s_waitcnt vmcnt(0)\n"
    "v_max3_u32 v184, v132, v134, v132\n"
    "v_swap_b32 v133, v134\n"
    "s_nop 1\n"
    "v_mfma_f32_16x16x32_bf16 v[148:151], v[132:135], v[40:43], 0\n"
    "v_mfma_f32_16x16x32_bf16 v[152:155], v[132:135], v[48:51], 0\n"
    "v_max3_u32 v184, v136, v138, v184\n"
    "v_swap_b32 v137, v138\n"
    "s_nop 1\n"
    "v_mfma_f32_16x16x32_bf16 v[148:151], v[136:139], v[44:47], v[148:151]\n"
    "v_mfma_f32_16x16x32_bf16 v[152:155], v[136:139], v[52:55], v[152:155]\n"
    "v_max3_u32 v184, v140, v142, v184\n"
    "v_swap_b32 v141, v142\n"
    "s_nop 1\n"
    "v_mfma_f32_16x16x32_bf16 v[156:159], v[140:143], v[40:43], 0\n"
    "v_mfma_f32_16x16x32_bf16 v[160:163], v[140:143], v[48:51], 0\n"
    "v_max3_u32 v184, v144, v146, v184\n"
    "v_swap_b32 v145, v146\n"
    "s_nop 1\n"
    "v_mfma_f32_16x16x32_bf16 v[156:159], v[144:147], v[44:47], v[156:159]\n"
    "v_mfma_f32_16x16x32_bf16 v[160:163], v[144:147], v[52:55], v[160:163]\n"
    "v_max3_u32 v184, v116, v118, v184\n"
    "v_smfmac_f32_16x16x64_bf16 v[148:151], v[116:119], a[32:39], v195\n"
    "v_smfmac_f32_16x16x64_bf16 v[152:155], v[116:119], v[8:15], v195\n"
    "v_max3_u32 v184, v84, v86, v184\n"
    "v_swap_b32 v85, v86\n"
    "s_nop 1\n"
    "v_mfma_f32_16x16x32_bf16 v[156:159], v[84:87], a[32:35], v[156:159]\n"
    "v_mfma_f32_16x16x32_bf16 v[160:163], v[84:87], v[8:11], v[160:163]\n"
    "v_max3_u32 v184, v88, v90, v184\n"
    "v_swap_b32 v89, v90\n"
    "s_nop 1\n"
    "v_mfma_f32_16x16x32_bf16 v[156:159], v[88:91], a[36:39], v[156:159]\n"
    "v_mfma_f32_16x16x32_bf16 v[160:163], v[88:91], v[12:15], v[160:163]\n"
    "v_max3_u32 v184, v120, v122, v184\n"
    "v_smfmac_f32_16x16x64_bf16 v[148:151], v[120:123], a[40:47], v195\n"
    "v_smfmac_f32_16x16x64_bf16 v[152:155], v[120:123], v[16:23], v195\n"
    "v_max3_u32 v184, v92, v94, v184\n"
    "v_swap_b32 v93, v94\n"
    "s_nop 1\n"
    "v_mfma_f32_16x16x32_bf16 v[156:159], v[92:95], a[40:43], v[156:159]\n"
    "v_mfma_f32_16x16x32_bf16 v[160:163], v[92:95], v[16:19], v[160:163]\n"
    "v_max3_u32 v184, v96, v98, v184\n"
    "v_swap_b32 v97, v98\n"
    "s_nop 1\n"
    "v_mfma_f32_16x16x32_bf16 v[156:159], v[96:99], a[44:47], v[156:159]\n"
    "v_mfma_f32_16x16x32_bf16 v[160:163], v[96:99], v[20:23], v[160:163]\n"
    "v_max3_u32 v184, v124, v126, v184\n"
    "v_smfmac_f32_16x16x64_bf16 v[148:151], v[124:127], a[48:55], v195\n"
    "v_smfmac_f32_16x16x64_bf16 v[152:155], v[124:127], v[24:31], v195\n"
    "v_max3_u32 v184, v100, v102, v184\n"
    "v_swap_b32 v101, v102\n"
    "s_nop 1\n"
    "v_mfma_f32_16x16x32_bf16 v[156:159], v[100:103], a[48:51], v[156:159]\n"
    "v_mfma_f32_16x16x32_bf16 v[160:163], v[100:103], v[24:27], v[160:163]\n"
    "v_max3_u32 v184, v104, v106, v184\n"
    "v_swap_b32 v105, v106\n"
    "s_nop 1\n"
    "v_mfma_f32_16x16x32_bf16 v[156:159], v[104:107], a[52:55], v[156:159]\n"
    "v_mfma_f32_16x16x32_bf16 v[160:163], v[104:107], v[28:31], v[160:163]\n"
    "v_max3_u32 v184, v128, v130, v184\n"
    "v_smfmac_f32_16x16x64_bf16 v[148:151], v[128:131], a[56:63], v195\n"
    "v_smfmac_f32_16x16x64_bf16 v[152:155], v[128:131], v[32:39], v195\n"
    "v_max3_u32 v184, v108, v110, v184\n"
    "v_swap_b32 v109, v110\n"
    "s_nop 1\n"
    "v_mfma_f32_16x16x32_bf16 v[156:159], v[108:111], a[56:59], v[156:159]\n"
    "v_mfma_f32_16x16x32_bf16 v[160:163], v[108:111], v[32:35], v[160:163]\n"
    "v_max3_u32 v184, v112, v114, v184\n"
    "v_swap_b32 v113, v114\n"
    "s_nop 1\n"
    "v_mfma_f32_16x16x32_bf16 v[156:159], v[112:115], a[60:63], v[156:159]\n"
    "v_mfma_f32_16x16x32_bf16 v[160:163], v[112:115], v[36:39], v[160:163]\n"
    "v_cmp_eq_u32 vcc, -1, v184\n"
    "s_nop 0\n"
    "s_cbranch_vccnz .Lct_tr1_%=\n"
    "s_nop 7\n"
    "s_branch .Lct_tg1_%=\n"
    :: "{s[0:3]}"(s0_), "{s[4:7]}"(s4_), "{s[8:11]}"(s8_), "{s[12:15]}"(s12_), "{s[16:19]}"(s16_), "{v[192:195]}"(v192_), "{v[196:199]}"(v196_), "{v[200:203]}"(v200_), "{v[204:207]}"(v204_), "{v[208:211]}"(v208_), "{v[212:215]}"(v212_) : "memory", "v255", "a255", "s95");
    __builtin_unreachable();
}
DEV void qw_role(const MegaArgs& A, const int L, const int h, const int rep) {
    ROLE_HEAD;
    const u32x4 s0_ = {RFL(rx[0]), RFL(rx[1]), RFL(rx[2]), RFL(rx[3])};
    const u32x4 s4_ = {RFL(lo(A.Wq + h * 192 + 64)), RFL(hi(A.Wq + h * 192 + 64)), RFL(WBYTES - h * 384 - 128), RFL(0x20000u)};
    const u32x4 s8_ = {RFL(lo(A.Wv + h * 192 + 128)), RFL(hi(A.Wv + h * 192 + 128)), RFL(WBYTES - h * 384 - 256), RFL(0x20000u)};
    const u32x4 s12_ = {RFL(w), RFL(REC0 + w * 2048), RFL(QT0 + h * 32768), RFL((A.layers & 2) ? 64 : 0)};
    const u32x4 s16_ = {RFL(xl(Z_CT + (h & 7) * 256, L)), RFL(xl(Z_XN + rep * 2048, 0)), RFL(DBG0 + ((L * 128 + 112 + (h >> 3) * 4 + (h & 3)) * 4 + w) * 2048), RFL(0u)};
    const u32x4 v0_ = {(unsigned)((8 * w + (l >> 3)) * 12288 + ((l & 7) ^ (((8 * w + (l >> 3)) >> 1) & 6)) * 16), (unsigned)((8 * w + (l >> 3)) * 12288 + ((l & 7) ^ (((8 * w + (l >> 3)) >> 1) & 6)) * 16 + 128), (unsigned)((8 * lq + ((l >> 2) & 1) + ((l >> 1) & 4)) * 128 + (w ^ (((8 * lq + ((l >> 2) & 1) + ((l >> 1) & 4)) >> 2) & 3)) * 32 + (l & 3) * 8), (unsigned)(0u)};
    const u32x4 v64_ = {(unsigned)(l * 16), (unsigned)(l * 16 + 40960), (unsigned)(l * 16 + w * 2048), (unsigned)(l * 16 + w * 1024 + 16384)};
    const u32x4 v68_ = {(unsigned)(l < 20 ? (l >> 2) * 128 + (l & 3) * 4 : 0), (unsigned)(l == 0 ? xl(Z_QW + h * 128 + w * 4, 0) : OOB), (unsigned)(0u), (unsigned)(0u)};
    asm volatile(
    "s_mov_b32 s20, 0\n"
    "s_lshl_b32 s32, s12, 10\n"
    "s_mov_b32 s38, 0\n"
    "s_add_u32 m0, s32, 0\n"
    "s_nop 0\n"
    "buffer_load_dwordx4 v0, s[4:7], s38 offen nt lds\n"
    "s_add_u32 m0, s32, 4096\n"
    "s_nop 0\n"
    "buffer_load_dwordx4 v1, s[4:7], s38 offen nt lds\n"
    "s_add_u32 m0, s32, 8192\n"
    "s_nop 0\n"
    "buffer_load_dwordx4 v0, s[8:11], s38 offen nt lds\n"
    "s_mov_b32 s38, 393216\n"
    "s_add_u32 m0, s32, 12288\n"
    "s_nop 0\n"
    "buffer_load_dwordx4 v0, s[4:7], s38 offen nt lds\n"
    "s_add_u32 m0, s32, 16384\n"
    "s_nop 0\n"
    "buffer_load_dwordx4 v1, s[4:7], s38 offen nt lds\n"
    "s_add_u32 m0, s32, 20480\n"
    "s_nop 0\n"
    "buffer_load_dwordx4 v0, s[8:11], s38 offen nt lds\n"
    "s_mov_b32 s38, 786432\n"
    "s_add_u32 m0, s32, 24576\n"
    "s_nop 0\n"
    "buffer_load_dwordx4 v0, s[4:7], s38 offen nt lds\n"
    "s_add_u32 m0, s32, 28672\n"
    "s_nop 0\n"
    "buffer_load_dwordx4 v1, s[4:7], s38 offen nt lds\n"
    "s_add_u32 m0, s32, 32768\n"
    "s_nop 0\n"
    "buffer_load_dwordx4 v0, s[8:11], s38 offen nt lds\n"
    "s_mov_b32 s38, 1179648\n"
    "s_add_u32 m0, s32, 36864\n"
    "s_nop 0\n"
    "buffer_load_dwordx4 v0, s[4:7], s38 offen nt lds\n"
    "s_add_u32 m0, s32, 40960\n"
    "s_nop 0\n"
    "buffer_load_dwordx4 v1, s[4:7], s38 offen nt lds\n"
    "s_add_u32 m0, s32, 45056\n"
    "s_nop 0\n"
    "buffer_load_dwordx4 v0, s[8:11], s38 offen nt lds\n"
    "s_mov_b32 s38, 1572864\n"
    "s_add_u32 m0, s32, 49152\n"
    "s_nop 0\n"
    "buffer_load_dwordx4 v0, s[4:7], s38 offen nt lds\n"
    "s_add_u32 m0, s32, 53248\n"
    "s_nop 0\n"
    "buffer_load_dwordx4 v1, s[4:7], s38 offen nt lds\n"
    "s_add_u32 m0, s32, 57344\n"
    "s_nop 0\n"
    "buffer_load_dwordx4 v0, s[8:11], s38 offen nt lds\n"
    "s_mov_b32 s38, 1966080\n"
    "s_add_u32 m0, s32, 61440\n"
    "s_nop 0\n"
    "buffer_load_dwordx4 v0, s[4:7], s38 offen nt lds\n"
    "s_add_u32 m0, s32, 65536\n"
    "s_nop 0\n"
    "buffer_load_dwordx4 v1, s[4:7], s38 offen nt lds\n"
    "s_add_u32 m0, s32, 69632\n"
    "s_nop 0\n"
    "buffer_load_dwordx4 v0, s[8:11], s38 offen nt lds\n"
    "s_mov_b32 s38, 2359296\n"
    "s_add_u32 m0, s32, 73728\n"
    "s_nop 0\n"
    "buffer_load_dwordx4 v0, s[4:7], s38 offen nt lds\n"
    "s_add_u32 m0, s32, 77824\n"
    "s_nop 0\n"
    "buffer_load_dwordx4 v1, s[4:7], s38 offen nt lds\n"
    "s_add_u32 m0, s32, 81920\n"
    "s_nop 0\n"
    "buffer_load_dwordx4 v0, s[8:11], s38 offen nt lds\n"
    "s_mov_b32 s38, 2752512\n"
    "s_add_u32 m0, s32, 86016\n"
    "s_nop 0\n"
    "buffer_load_dwordx4 v0, s[4:7], s38 offen nt lds\n"
    "s_add_u32 m0, s32, 90112\n"
    "s_nop 0\n"
    "buffer_load_dwordx4 v1, s[4:7], s38 offen nt lds\n"
    "s_add_u32 m0, s32, 94208\n"
    "s_nop 0\n"
    "buffer_load_dwordx4 v0, s[8:11], s38 offen nt lds\n"
    "s_mov_b32 s38, 3145728\n"
    "s_add_u32 m0, s32, 98304\n"
    "s_nop 0\n"
    "buffer_load_dwordx4 v0, s[4:7], s38 offen nt lds\n"
    "s_add_u32 m0, s32, 102400\n"
    "s_nop 0\n"
    "buffer_load_dwordx4 v1, s[4:7], s38 offen nt lds\n"
    "s_add_u32 m0, s32, 106496\n"
    "s_nop 0\n"
    "buffer_load_dwordx4 v0, s[8:11], s38 offen nt lds\n"
    "s_mov_b32 s38, 3538944\n"
    "s_add_u32 m0, s32, 110592\n"
    "s_nop 0\n"
    "buffer_load_dwordx4 v0, s[4:7], s38 offen nt lds\n"
    "s_add_u32 m0, s32, 114688\n"
    "s_nop 0\n"
    "buffer_load_dwordx4 v1, s[4:7], s38 offen nt lds\n"
    "s_add_u32 m0, s32, 118784\n"
    "s_nop 0\n"
    "buffer_load_dwordx4 v0, s[8:11], s38 offen nt lds\n"
    "s_mov_b32 s38, 3932160\n"
    "s_add_u32 m0, s32, 122880\n"
    "s_nop 0\n"
    "buffer_load_dwordx4 v0, s[4:7], s38 offen nt lds\n"
    "s_add_u32 m0, s32, 126976\n"
    "s_nop 0\n"
    "buffer_load_dwordx4 v1, s[4:7], s38 offen nt lds\n"
    "s_add_u32 m0, s32, 131072\n"
    "s_nop 0\n"
    "buffer_load_dwordx4 v0, s[8:11], s38 offen nt lds\n"
    "s_mov_b32 s38, 4325376\n"
    "s_add_u32 m0, s32, 135168\n"
    "s_nop 0\n"
    "buffer_load_dwordx4 v0, s[4:7], s38 offen nt lds\n"
    "s_add_u32 m0, s32, 139264\n"
    "s_nop 0\n"
    "buffer_load_dwordx4 v1, s[4:7], s38 offen nt lds\n"
    "s_add_u32 m0, s32, 143360\n"
    "s_nop 0\n"
    "buffer_load_dwordx4 v0, s[8:11], s38 offen nt lds\n"
    "s_waitcnt vmcnt(24)\n"
    "s_barrier\n"
    "v_add_u32 v8, 0, v2\n"
    "ds_read_b64_tr_b16 a[0:1], v8 offset:0\n"
    "ds_read_b64_tr_b16 a[2:3], v8 offset:256\n"
    "ds_read_b64_tr_b16 a[80:81], v8 offset:4096\n"
    "ds_read_b64_tr_b16 a[82:83], v8 offset:4352\n"
    "ds_read_b64_tr_b16 a[160:161], v8 offset:8192\n"
    "ds_read_b64_tr_b16 a[162:163], v8 offset:8448\n"
    "ds_read_b64_tr_b16 a[4:5], v8 offset:12288\n"
    "ds_read_b64_tr_b16 a[6:7], v8 offset:12544\n"
    "ds_read_b64_tr_b16 a[84:85], v8 offset:16384\n"
    "ds_read_b64_tr_b16 a[86:87], v8 offset:16640\n"
    "ds_read_b64_tr_b16 a[164:165], v8 offset:20480\n"
    "ds_read_b64_tr_b16 a[166:167], v8 offset:20736\n"
    "ds_read_b64_tr_b16 a[8:9], v8 offset:24576\n"
    "ds_read_b64_tr_b16 a[10:11], v8 offset:24832\n"
    "ds_read_b64_tr_b16 a[88:89], v8 offset:28672\n"
    "ds_read_b64_tr_b16 a[90:91], v8 offset:28928\n"
    "ds_read_b64_tr_b16 a[168:169], v8 offset:32768\n"
    "ds_read_b64_tr_b16 a[170:171], v8 offset:33024\n"
    "ds_read_b64_tr_b16 a[12:13], v8 offset:36864\n"
    "ds_read_b64_tr_b16 a[14:15], v8 offset:37120\n"
    "ds_read_b64_tr_b16 a[92:93], v8 offset:40960\n"
    "ds_read_b64_tr_b16 a[94:95], v8 offset:41216\n"
    "ds_read_b64_tr_b16 a[172:173], v8 offset:45056\n"
    "ds_read_b64_tr_b16 a[174:175], v8 offset:45312\n"
    "s_waitcnt lgkmcnt(0)\n"
    "s_barrier\n"
    "s_mov_b32 s38, 4718592\n"
    "s_add_u32 m0, s32, 0\n"
    "s_nop 0\n"
    "buffer_load_dwordx4 v0, s[4:7], s38 offen nt lds\n"
    "s_add_u32 m0, s32, 4096\n"
    "s_nop 0\n"
    "buffer_load_dwordx4 v1, s[4:7], s38 offen nt lds\n"
    "s_add_u32 m0, s32, 8192\n"
    "s_nop 0\n"
    "buffer_load_dwordx4 v0, s[8:11], s38 offen nt lds\n"
    "s_mov_b32 s38, 5111808\n"
    "s_add_u32 m0, s32, 12288\n"
    "s_nop 0\n"
    "buffer_load_dwordx4 v0, s[4:7], s38 offen nt lds\n"
    "s_add_u32 m0, s32, 16384\n"
    "s_nop 0\n"
    "buffer_load_dwordx4 v1, s[4:7], s38 offen nt lds\n"
    "s_add_u32 m0, s32, 20480\n"
    "s_nop 0\n"
    "buffer_load_dwordx4 v0, s[8:11], s38 offen nt lds\n"
    "s_mov_b32 s38, 5505024\n"
    "s_add_u32 m0, s32, 24576\n"
    "s_nop 0\n"
    "buffer_load_dwordx4 v0, s[4:7], s38 offen nt lds\n"
    "s_add_u32 m0, s32, 28672\n"
    "s_nop 0\n"
    "buffer_load_dwordx4 v1, s[4:7], s38 offen nt lds\n"
    "s_add_u32 m0, s32, 32768\n"
    "s_nop 0\n"
    "buffer_load_dwordx4 v0, s[8:11], s38 offen nt lds\n"
    "s_mov_b32 s38, 5898240\n"
    "s_add_u32 m0, s32, 36864\n"
    "s_nop 0\n"
    "buffer_load_dwordx4 v0, s[4:7], s38 offen nt lds\n"
    "s_add_u32 m0, s32, 40960\n"
    "s_nop 0\n"
    "buffer_load_dwordx4 v1, s[4:7], s38 offen nt lds\n"
    "s_add_u32 m0, s32, 45056\n"
    "s_nop 0\n"
    "buffer_load_dwordx4 v0, s[8:11], s38 offen nt lds\n"
    "s_waitcnt vmcnt(24)\n"
    "s_barrier\n"
    "v_add_u32 v8, 49152, v2\n"
    "ds_read_b64_tr_b16 a[16:17], v8 offset:0\n"
    "ds_read_b64_tr_b16 a[18:19], v8 offset:256\n"
    "ds_read_b64_tr_b16 a[96:97], v8 offset:4096\n"
    "ds_read_b64_tr_b16 a[98:99], v8 offset:4352\n"
    "ds_read_b64_tr_b16 a[176:177], v8 offset:8192\n"
    "ds_read_b64_tr_b16 a[178:179], v8 offset:8448\n"
    "ds_read_b64_tr_b16 a[20:21], v8 offset:12288\n"
    "ds_read_b64_tr_b16 a[22:23], v8 offset:12544\n"
    "ds_read_b64_tr_b16 a[100:101], v8 offset:16384\n"
    "ds_read_b64_tr_b16 a[102:103], v8 offset:16640\n"
    "ds_read_b64_tr_b16 a[180:181], v8 offset:20480\n"
    "ds_read_b64_tr_b16 a[182:183], v8 offset:20736\n"
    "ds_read_b64_tr_b16 a[24:25], v8 offset:24576\n"
    "ds_read_b64_tr_b16 a[26:27], v8 offset:24832\n"
    "ds_read_b64_tr_b16 a[104:105], v8 offset:28672\n"
    "ds_read_b64_tr_b16 a[106:107], v8 offset:28928\n"
    "ds_read_b64_tr_b16 a[184:185], v8 offset:32768\n"
    "ds_read_b64_tr_b16 a[186:187], v8 offset:33024\n"
    "ds_read_b64_tr_b16 a[28:29], v8 offset:36864\n"
    "ds_read_b64_tr_b16 a[30:31], v8 offset:37120\n"
    "ds_read_b64_tr_b16 a[108:109], v8 offset:40960\n"
    "ds_read_b64_tr_b16 a[110:111], v8 offset:41216\n"
    "ds_read_b64_tr_b16 a[188:189], v8 offset:45056\n"
    "ds_read_b64_tr_b16 a[190:191], v8 offset:45312\n"
    "s_waitcnt lgkmcnt(0)\n"
    "s_barrier\n"
    "s_mov_b32 s38, 6291456\n"
    "s_add_u32 m0, s32, 49152\n"
    "s_nop 0\n"
    "buffer_load_dwordx4 v0, s[4:7], s38 offen nt lds\n"
    "s_add_u32 m0, s32, 53248\n"
    "s_nop 0\n"
    "buffer_load_dwordx4 v1, s[4:7], s38 offen nt lds\n"
    "s_add_u32 m0, s32, 57344\n"
    "s_nop 0\n"
    "buffer_load_dwordx4 v0, s[8:11], s38 offen nt lds\n"
    "s_mov_b32 s38, 6684672\n"
    "s_add_u32 m0, s32, 61440\n"
    "s_nop 0\n"
    "buffer_load_dwordx4 v0, s[4:7], s38 offen nt lds\n"
    "s_add_u32 m0, s32, 65536\n"
    "s_nop 0\n"
    "buffer_load_dwordx4 v1, s[4:7], s38 offen nt lds\n"
    "s_add_u32 m0, s32, 69632\n"
    "s_nop 0\n"
    "buffer_load_dwordx4 v0, s[8:11], s38 offen nt lds\n"
    "s_mov_b32 s38, 7077888\n"
    "s_add_u32 m0, s32, 73728\n"
    "s_nop 0\n"
    "buffer_load_dwordx4 v0, s[4:7], s38 offen nt lds\n"
    "s_add_u32 m0, s32, 77824\n"
    "s_nop 0\n"
    "buffer_load_dwordx4 v1, s[4:7], s38 offen nt lds\n"
    "s_add_u32 m0, s32, 81920\n"
    "s_nop 0\n"
    "buffer_load_dwordx4 v0, s[8:11], s38 offen nt lds\n"
    "s_mov_b32 s38, 7471104\n"
    "s_add_u32 m0, s32, 86016\n"
    "s_nop 0\n"
    "buffer_load_dwordx4 v0, s[4:7], s38 offen nt lds\n"
    "s_add_u32 m0, s32, 90112\n"
    "s_nop 0\n"
    "buffer_load_dwordx4 v1, s[4:7], s38 offen nt lds\n"
    "s_add_u32 m0, s32, 94208\n"
    "s_nop 0\n"
    "buffer_load_dwordx4 v0, s[8:11], s38 offen nt lds\n"
    "s_waitcnt vmcnt(24)\n"
    "s_barrier\n"
    "v_add_u32 v8, 98304, v2\n"
    "ds_read_b64_tr_b16 a[32:33], v8 offset:0\n"
    "ds_read_b64_tr_b16 a[34:35], v8 offset:256\n"
    "ds_read_b64_tr_b16 a[112:113], v8 offset:4096\n"
    "ds_read_b64_tr_b16 a[114:115], v8 offset:4352\n"
    "ds_read_b64_tr_b16 a[192:193], v8 offset:8192\n"
    "ds_read_b64_tr_b16 a[194:195], v8 offset:8448\n"
    "ds_read_b64_tr_b16 a[36:37], v8 offset:12288\n"
    "ds_read_b64_tr_b16 a[38:39], v8 offset:12544\n"
    "ds_read_b64_tr_b16 a[116:117], v8 offset:16384\n"
    "ds_read_b64_tr_b16 a[118:119], v8 offset:16640\n"
    "ds_read_b64_tr_b16 a[196:197], v8 offset:20480\n"
    "ds_read_b64_tr_b16 a[198:199], v8 offset:20736\n"
    "ds_read_b64_tr_b16 a[40:41], v8 offset:24576\n"
    "ds_read_b64_tr_b16 a[42:43], v8 offset:24832\n"
    "ds_read_b64_tr_b16 a[120:121], v8 offset:28672\n"
    "ds_read_b64_tr_b16 a[122:123], v8 offset:28928\n"
    "ds_read_b64_tr_b16 a[200:201], v8 offset:32768\n"
    "ds_read_b64_tr_b16 a[202:203], v8 offset:33024\n"
    "ds_read_b64_tr_b16 a[44:45], v8 offset:36864\n"
    "ds_read_b64_tr_b16 a[46:47], v8 offset:37120\n"
    "ds_read_b64_tr_b16 a[124:125], v8 offset:40960\n"
    "ds_read_b64_tr_b16 a[126:127], v8 offset:41216\n"
    "ds_read_b64_tr_b16 a[204:205], v8 offset:45056\n"
    "ds_read_b64_tr_b16 a[206:207], v8 offset:45312\n"
    "s_waitcnt lgkmcnt(0)\n"
    "s_barrier\n"
    "s_waitcnt vmcnt(12)\n"
    "s_barrier\n"
    "v_add_u32 v8, 0, v2\n"
    "ds_read_b64_tr_b16 a[48:49], v8 offset:0\n"
    "ds_read_b64_tr_b16 a[50:51], v8 offset:256\n"
    "ds_read_b64_tr_b16 a[128:129], v8 offset:4096\n"
    "ds_read_b64_tr_b16 a[130:131], v8 offset:4352\n"
    "ds_read_b64_tr_b16 a[208:209], v8 offset:8192\n"
    "ds_read_b64_tr_b16 a[210:211], v8 offset:8448\n"
    "ds_read_b64_tr_b16 a[52:53], v8 offset:12288\n"
    "ds_read_b64_tr_b16 a[54:55], v8 offset:12544\n"
    "ds_read_b64_tr_b16 a[132:133], v8 offset:16384\n"
    "ds_read_b64_tr_b16 a[134:135], v8 offset:16640\n"
    "ds_read_b64_tr_b16 a[212:213], v8 offset:20480\n"
    "ds_read_b64_tr_b16 a[214:215], v8 offset:20736\n"
    "ds_read_b64_tr_b16 a[56:57], v8 offset:24576\n"
    "ds_read_b64_tr_b16 a[58:59], v8 offset:24832\n"
    "ds_read_b64_tr_b16 a[136:137], v8 offset:28672\n"
    "ds_read_b64_tr_b16 a[138:139], v8 offset:28928\n"
    "ds_read_b64_tr_b16 a[216:217], v8 offset:32768\n"
    "ds_read_b64_tr_b16 a[218:219], v8 offset:33024\n"
    "ds_read_b64_tr_b16 a[60:61], v8 offset:36864\n"
    "ds_read_b64_tr_b16 a[62:63], v8 offset:37120\n"
    "ds_read_b64_tr_b16 a[140:141], v8 offset:40960\n"
    "ds_read_b64_tr_b16 a[142:143], v8 offset:41216\n"
    "ds_read_b64_tr_b16 a[220:221], v8 offset:45056\n"
    "ds_read_b64_tr_b16 a[222:223], v8 offset:45312\n"
    "s_waitcnt lgkmcnt(0)\n"
    "s_barrier\n"
    "s_waitcnt vmcnt(0)\n"
    "s_barrier\n"
    "v_add_u32 v8, 49152, v2\n"
    "ds_read_b64_tr_b16 a[64:65], v8 offset:0\n"
    "ds_read_b64_tr_b16 a[66:67], v8 offset:256\n"
    "ds_read_b64_tr_b16 a[144:145], v8 offset:4096\n"
    "ds_read_b64_tr_b16 a[146:147], v8 offset:4352\n"
    "ds_read_b64_tr_b16 a[224:225], v8 offset:8192\n"
    "ds_read_b64_tr_b16 a[226:227], v8 offset:8448\n"
    "ds_read_b64_tr_b16 a[68:69], v8 offset:12288\n"
    "ds_read_b64_tr_b16 a[70:71], v8 offset:12544\n"
    "ds_read_b64_tr_b16 a[148:149], v8 offset:16384\n"
    "ds_read_b64_tr_b16 a[150:151], v8 offset:16640\n"
    "ds_read_b64_tr_b16 a[228:229], v8 offset:20480\n"
    "ds_read_b64_tr_b16 a[230:231], v8 offset:20736\n"
    "ds_read_b64_tr_b16 a[72:73], v8 offset:24576\n"
    "ds_read_b64_tr_b16 a[74:75], v8 offset:24832\n"
    "ds_read_b64_tr_b16 a[152:153], v8 offset:28672\n"
    "ds_read_b64_tr_b16 a[154:155], v8 offset:28928\n"
    "ds_read_b64_tr_b16 a[232:233], v8 offset:32768\n"
    "ds_read_b64_tr_b16 a[234:235], v8 offset:33024\n"
    "ds_read_b64_tr_b16 a[76:77], v8 offset:36864\n"
    "ds_read_b64_tr_b16 a[78:79], v8 offset:37120\n"
    "ds_read_b64_tr_b16 a[156:157], v8 offset:40960\n"
    "ds_read_b64_tr_b16 a[158:159], v8 offset:41216\n"
    "ds_read_b64_tr_b16 a[236:237], v8 offset:45056\n"
    "ds_read_b64_tr_b16 a[238:239], v8 offset:45312\n"
    "s_waitcnt lgkmcnt(0)\n"
    "s_barrier\n"
    "s_mov_b32 s26, -2\n"
    "v_lshrrev_b32 v4, 2, v64\n"
    ".Lqw_po1_%=:\n"
    "buffer_load_dword v8, v4, s[0:3], s16 offen sc1\n"
    "s_waitcnt vmcnt(0)\n"
    "v_cmp_gt_i32 vcc, 1, v8\n"
    "s_nop 0\n"
    "s_cbranch_vccz .Lqw_pd2_%=\n"
    "s_add_u32 s20, s20, 1\n"
    "s_cmp_lt_u32 s20, 0x40000\n"
    "s_cbranch_scc1 .Lqw_sp3_%=\n"
    "v_mov_b32 v8, 50\n"
    "s_mov_b32 s24, 33728\n"
    "buffer_store_dword v8, off, s[0:3], s24 sc1\n"
    "s_waitcnt vmcnt(0)\n"
    "s_branch .Lqw_pd2_%=\n"
    ".Lqw_sp3_%=:\n"
    "s_sleep 4\n"
    "s_branch .Lqw_po1_%=\n"
    ".Lqw_pd2_%=:\n"
    "s_mov_b32 s26, -1\n"
    "s_mov_b32 s35, 0\n"
    "s_lshl_b32 s37, s12, 11\n"
    "s_mov_b32 s45, 0\n"
    "v_mov_b32 v60, 0\n"
    "s_mov_b32 s43, 2130706432\n"
    "s_mov_b32 s41, 2130706432\n"
    "s_mov_b32 s46, 2130706432\n"
    "s_mov_b32 s34, 2130706432\n"
    "s_add_u32 s26, s26, 1\n"
    "s_sub_u32 s27, s26, 14\n"
    "s_ashr_i32 s28, s27, 1\n"
    "s_and_b32 s29, s27, 1\n"
    "s_add_u32 s30, s28, 14\n"
    "s_cmp_lt_i32 s27, 0\n"
    "s_cselect_b32 s29, 0, s29\n"
    "s_cselect_b32 s30, s26, s30\n"
    "s_cmp_eq_u32 s29, 1\n"
    "s_cselect_b32 s30, s28, s30\n"
    "s_cselect_b32 s27, s15, 64\n"
    "s_lshl_b32 s31, s29, 13\n"
    "s_add_u32 s40, s30, 1\n"
    "s_lshl_b32 s32, s30, 19\n"
    "s_lshl_b32 s25, s30, 11\n"
    "s_xor_b32 s25, s25, s31\n"
    "s_and_b32 s25, s25, 0x2000\n"
    "s_add_u32 s32, s32, s25\n"
    "s_add_u32 s32, s32, s13\n"
    "s_lshl_b32 s33, s30, 20\n"
    "s_lshl_b32 s25, s30, 12\n"
    "s_xor_b32 s25, s25, s31\n"
    "s_and_b32 s25, s25, 0x2000\n"
    "s_add_u32 s33, s33, s25\n"
    "s_add_u32 s33, s33, s14\n"
    "s_mul_i32 s47, s40, s29\n"
    "s_cmp_lt_u32 s30, s27\n"
    "s_cselect_b32 s36, 1, 0\n"
    "s_cselect_b32 s32, s32, 2130706432\n"
    "s_cmp_lt_u32 s30, s27\n"
    "s_cselect_b32 s39, s31, 2130706432\n"
    "s_cselect_b32 s47, s47, 0\n"
    "s_cmp_gt_u32 s47, 0\n"
    "s_cselect_b32 s38, s17, 2130706432\n"
    ".Lqw_top_%=:\n"
    "ds_read_b128 v[24:27], v65 offset:0\n"
    "ds_read_b128 v[28:31], v65 offset:1024\n"
    "ds_read_b128 v[32:35], v65 offset:2048\n"
    "ds_read_b128 v[36:39], v65 offset:3072\n"
    "ds_read_b128 v[40:43], v65 offset:4096\n"
    "ds_read_b128 v[44:47], v65 offset:5120\n"
    "v_cmp_gt_i32 vcc, s47, v60\n"
    "s_nop 0\n"
    "s_cbranch_vccnz .Lqw_wx_%=\n"
    ".Lqw_wxb_%=:\n"
    "s_cmp_eq_u32 s35, 0\n"
    "s_cbranch_scc1 .Lqw_skip_%=\n"
    ".Lqw_mk0x3_%=:\n"
    "s_waitcnt lgkmcnt(4)\n"
    "v_mfma_f32_16x16x32_bf16 v[0:3], a[0:3], v[24:27], 0\n"
    "buffer_store_dwordx4 v[48:51], v66, s[0:3], s46 offen offset:0 sc1\n"
    "v_mfma_f32_16x16x32_bf16 v[4:7], a[0:3], v[28:31], 0\n"
    "v_mfma_f32_16x16x32_bf16 v[8:11], a[80:83], v[24:27], 0\n"
    "v_mfma_f32_16x16x32_bf16 v[12:15], a[80:83], v[28:31], 0\n"
    "v_mfma_f32_16x16x32_bf16 v[16:19], a[160:163], v[24:27], 0\n"
    "ds_read_b128 v[24:27], v65 offset:6144\n"
    "v_mfma_f32_16x16x32_bf16 v[20:23], a[160:163], v[28:31], 0\n"
    "ds_read_b128 v[28:31], v65 offset:7168\n"
    "s_waitcnt lgkmcnt(4)\n"
    "v_mfma_f32_16x16x32_bf16 v[0:3], a[4:7], v[32:35], v[0:3]\n"
    "v_mfma_f32_16x16x32_bf16 v[4:7], a[4:7], v[36:39], v[4:7]\n"
    "buffer_store_dwordx4 v[52:55], v66, s[0:3], s46 offen offset:1024 sc1\n"
    "v_mfma_f32_16x16x32_bf16 v[8:11], a[84:87], v[32:35], v[8:11]\n"
    "v_mfma_f32_16x16x32_bf16 v[12:15], a[84:87], v[36:39], v[12:15]\n"
    "v_mfma_f32_16x16x32_bf16 v[16:19], a[164:167], v[32:35], v[16:19]\n"
    "ds_read_b128 v[32:35], v65 offset:8192\n"
    "v_mfma_f32_16x16x32_bf16 v[20:23], a[164:167], v[36:39], v[20:23]\n"
    "ds_read_b128 v[36:39], v65 offset:9216\n"
    "s_waitcnt lgkmcnt(4)\n"
    "v_mfma_f32_16x16x32_bf16 v[0:3], a[8:11], v[40:43], v[0:3]\n"
    "v_mfma_f32_16x16x32_bf16 v[4:7], a[8:11], v[44:47], v[4:7]\n"
    "v_mfma_f32_16x16x32_bf16 v[8:11], a[88:91], v[40:43], v[8:11]\n"
    "buffer_store_dwordx4 v[56:59], v67, s[0:3], s46 offen sc1\n"
    "s_add_u32 m0, s37, 0\n"
    "v_mfma_f32_16x16x32_bf16 v[12:15], a[88:91], v[44:47], v[12:15]\n"
    "v_mfma_f32_16x16x32_bf16 v[16:19], a[168:171], v[40:43], v[16:19]\n"
    "ds_read_b128 v[40:43], v65 offset:10240\n"
    "v_mfma_f32_16x16x32_bf16 v[20:23], a[168:171], v[44:47], v[20:23]\n"
    "ds_read_b128 v[44:47], v65 offset:11264\n"
    "s_waitcnt lgkmcnt(4)\n"
    "v_mfma_f32_16x16x32_bf16 v[0:3], a[12:15], v[24:27], v[0:3]\n"
    "v_mfma_f32_16x16x32_bf16 v[4:7], a[12:15], v[28:31], v[4:7]\n"
    "v_mfma_f32_16x16x32_bf16 v[8:11], a[92:95], v[24:27], v[8:11]\n"
    "v_mfma_f32_16x16x32_bf16 v[12:15], a[92:95], v[28:31], v[12:15]\n"
    "buffer_load_dwordx4 v64, s[0:3], s32 offen sc1 lds\n"
    "v_mfma_f32_16x16x32_bf16 v[16:19], a[172:175], v[24:27], v[16:19]\n"
    "ds_read_b128 v[24:27], v65 offset:12288\n"
    "v_mfma_f32_16x16x32_bf16 v[20:23], a[172:175], v[28:31], v[20:23]\n"
    "ds_read_b128 v[28:31], v65 offset:13312\n"
    "s_waitcnt lgkmcnt(4)\n"
    "v_mfma_f32_16x16x32_bf16 v[0:3], a[16:19], v[32:35], v[0:3]\n"
    "v_mfma_f32_16x16x32_bf16 v[4:7], a[16:19], v[36:39], v[4:7]\n"
    "buffer_load_dwordx4 v64, s[0:3], s32 offen offset:1024 sc1 lds\n"
    "v_mfma_f32_16x16x32_bf16 v[8:11], a[96:99], v[32:35], v[8:11]\n"
    "s_add_u32 m0, s37, 8192\n"
    "s_add_u32 s32, s32, 0x4000\n"
    "v_mfma_f32_16x16x32_bf16 v[12:15], a[96:99], v[36:39], v[12:15]\n"
    "v_mfma_f32_16x16x32_bf16 v[16:19], a[176:179], v[32:35], v[16:19]\n"
    "ds_read_b128 v[32:35], v65 offset:14336\n"
    "v_mfma_f32_16x16x32_bf16 v[20:23], a[176:179], v[36:39], v[20:23]\n"
    "ds_read_b128 v[36:39], v65 offset:15360\n"
    "s_waitcnt lgkmcnt(4)\n"
    "v_mfma_f32_16x16x32_bf16 v[0:3], a[20:23], v[40:43], v[0:3]\n"
    "buffer_load_dwordx4 v64, s[0:3], s32 offen sc1 lds\n"
    "v_mfma_f32_16x16x32_bf16 v[4:7], a[20:23], v[44:47], v[4:7]\n"
    "v_mfma_f32_16x16x32_bf16 v[8:11], a[100:103], v[40:43], v[8:11]\n"
    "v_mfma_f32_16x16x32_bf16 v[12:15], a[100:103], v[44:47], v[12:15]\n"
    "v_mfma_f32_16x16x32_bf16 v[16:19], a[180:183], v[40:43], v[16:19]\n"
    "ds_read_b128 v[40:43], v65 offset:16384\n"
    "v_mfma_f32_16x16x32_bf16 v[20:23], a[180:183], v[44:47], v[20:23]\n"
    "ds_read_b128 v[44:47], v65 offset:17408\n"
    "s_waitcnt lgkmcnt(4)\n"
    "v_mfma_f32_16x16x32_bf16 v[0:3], a[24:27], v[24:27], v[0:3]\n"
    "buffer_load_dwordx4 v64, s[0:3], s32 offen offset:1024 sc1 lds\n"
    "v_mfma_f32_16x16x32_bf16 v[4:7], a[24:27], v[28:31], v[4:7]\n"
    "s_add_u32 m0, s37, 16384\n"
    "s_add_u32 s32, s32, 0x4000\n"
    "v_mfma_f32_16x16x32_bf16 v[8:11], a[104:107], v[24:27], v[8:11]\n"
    "v_mfma_f32_16x16x32_bf16 v[12:15], a[104:107], v[28:31], v[12:15]\n"
    "v_mfma_f32_16x16x32_bf16 v[16:19], a[184:187], v[24:27], v[16:19]\n"
    "ds_read_b128 v[24:27], v65 offset:18432\n"
    "v_mfma_f32_16x16x32_bf16 v[20:23], a[184:187], v[28:31], v[20:23]\n"
    "ds_read_b128 v[28:31], v65 offset:19456\n"
    "s_waitcnt lgkmcnt(4)\n"
    "v_mfma_f32_16x16x32_bf16 v[0:3], a[28:31], v[32:35], v[0:3]\n"
    "buffer_load_dwordx4 v64, s[0:3], s32 offen sc1 lds\n"
    "v_mfma_f32_16x16x32_bf16 v[4:7], a[28:31], v[36:39], v[4:7]\n"
    "v_mfma_f32_16x16x32_bf16 v[8:11], a[108:111], v[32:35], v[8:11]\n"
    "v_mfma_f32_16x16x32_bf16 v[12:15], a[108:111], v[36:39], v[12:15]\n"
    "v_mfma_f32_16x16x32_bf16 v[16:19], a[188:191], v[32:35], v[16:19]\n"
    "ds_read_b128 v[32:35], v65 offset:20480\n"
    "v_mfma_f32_16x16x32_bf16 v[20:23], a[188:191], v[36:39], v[20:23]\n"
    "ds_read_b128 v[36:39], v65 offset:21504\n"
    "s_waitcnt lgkmcnt(4)\n"
    "v_mfma_f32_16x16x32_bf16 v[0:3], a[32:35], v[40:43], v[0:3]\n"
    "buffer_load_dwordx4 v64, s[0:3], s32 offen offset:1024 sc1 lds\n"
    "v_mfma_f32_16x16x32_bf16 v[4:7], a[32:35], v[44:47], v[4:7]\n"
    "s_add_u32 m0, s37, 24576\n"
    "s_add_u32 s32, s32, 0x4000\n"
    "v_mfma_f32_16x16x32_bf16 v[8:11], a[112:115], v[40:43], v[8:11]\n"
    "v_mfma_f32_16x16x32_bf16 v[12:15], a[112:115], v[44:47], v[12:15]\n"
    "v_mfma_f32_16x16x32_bf16 v[16:19], a[192:195], v[40:43], v[16:19]\n"
    "ds_read_b128 v[40:43], v65 offset:22528\n"
    "v_mfma_f32_16x16x32_bf16 v[20:23], a[192:195], v[44:47], v[20:23]\n"
    "ds_read_b128 v[44:47], v65 offset:23552\n"
    "s_waitcnt lgkmcnt(4)\n"
    "v_mfma_f32_16x16x32_bf16 v[0:3], a[36:39], v[24:27], v[0:3]\n"
    "buffer_load_dwordx4 v64, s[0:3], s32 offen sc1 lds\n"
    "v_mfma_f32_16x16x32_bf16 v[4:7], a[36:39], v[28:31], v[4:7]\n"
    "v_mfma_f32_16x16x32_bf16 v[8:11], a[116:119], v[24:27], v[8:11]\n"
    "v_mfma_f32_16x16x32_bf16 v[12:15], a[116:119], v[28:31], v[12:15]\n"
    "v_mfma_f32_16x16x32_bf16 v[16:19], a[196:199], v[24:27], v[16:19]\n"
    "ds_read_b128 v[24:27], v65 offset:24576\n"
    "v_mfma_f32_16x16x32_bf16 v[20:23], a[196:199], v[28:31], v[20:23]\n"
    "ds_read_b128 v[28:31], v65 offset:25600\n"
    "s_waitcnt lgkmcnt(4)\n"
    "v_mfma_f32_16x16x32_bf16 v[0:3], a[40:43], v[32:35], v[0:3]\n"
    "buffer_load_dwordx4 v64, s[0:3], s32 offen offset:1024 sc1 lds\n"
    "v_mfma_f32_16x16x32_bf16 v[4:7], a[40:43], v[36:39], v[4:7]\n"
    "s_add_u32 m0, s37, 32768\n"
    "s_add_u32 s32, s32, 0x4000\n"
    "v_mfma_f32_16x16x32_bf16 v[8:11], a[120:123], v[32:35], v[8:11]\n"
    "v_mfma_f32_16x16x32_bf16 v[12:15], a[120:123], v[36:39], v[12:15]\n"
    "v_mfma_f32_16x16x32_bf16 v[16:19], a[200:203], v[32:35], v[16:19]\n"
    "ds_read_b128 v[32:35], v65 offset:26624\n"
    "v_mfma_f32_16x16x32_bf16 v[20:23], a[200:203], v[36:39], v[20:23]\n"
    "ds_read_b128 v[36:39], v65 offset:27648\n"
    "s_waitcnt lgkmcnt(4)\n"
    "v_mfma_f32_16x16x32_bf16 v[0:3], a[44:47], v[40:43], v[0:3]\n"
    "buffer_load_dwordx4 v64, s[0:3], s32 offen sc1 lds\n"
    "v_mfma_f32_16x16x32_bf16 v[4:7], a[44:47], v[44:47], v[4:7]\n"
    "v_mfma_f32_16x16x32_bf16 v[8:11], a[124:127], v[40:43], v[8:11]\n"
    "v_mfma_f32_16x16x32_bf16 v[12:15], a[124:127], v[44:47], v[12:15]\n"
    "v_mfma_f32_16x16x32_bf16 v[16:19], a[204:207], v[40:43], v[16:19]\n"
    "ds_read_b128 v[40:43], v65 offset:28672\n"
    "v_mfma_f32_16x16x32_bf16 v[20:23], a[204:207], v[44:47], v[20:23]\n"
    "ds_read_b128 v[44:47], v65 offset:29696\n"
    "s_waitcnt lgkmcnt(4)\n"
    "v_mfma_f32_16x16x32_bf16 v[0:3], a[48:51], v[24:27], v[0:3]\n"
    "buffer_load_dwordx4 v64, s[0:3], s32 offen offset:1024 sc1 lds\n"
    "v_mfma_f32_16x16x32_bf16 v[4:7], a[48:51], v[28:31], v[4:7]\n"
    "s_waitcnt vmcnt(10)\n"
    "v_mov_b32 v61, s44\n"
    "v_mfma_f32_16x16x32_bf16 v[8:11], a[128:131], v[24:27], v[8:11]\n"
    "v_mfma_f32_16x16x32_bf16 v[12:15], a[128:131], v[28:31], v[12:15]\n"
    "v_mfma_f32_16x16x32_bf16 v[16:19], a[208:211], v[24:27], v[16:19]\n"
    "buffer_store_dword v61, v69, s[0:3], s43 offen sc1\n"
    "ds_read_b128 v[24:27], v65 offset:30720\n"
    "v_mfma_f32_16x16x32_bf16 v[20:23], a[208:211], v[28:31], v[20:23]\n"
    "s_mov_b32 s46, s34\n"
    "ds_read_b128 v[28:31], v65 offset:31744\n"
    "s_waitcnt lgkmcnt(4)\n"
    "v_mfma_f32_16x16x32_bf16 v[0:3], a[52:55], v[32:35], v[0:3]\n"
    "s_mov_b32 s43, s41\n"
    "s_mov_b32 s44, s42\n"
    "s_mov_b32 s41, s39\n"
    "v_mfma_f32_16x16x32_bf16 v[4:7], a[52:55], v[36:39], v[4:7]\n"
    "s_mov_b32 s42, s40\n"
    "s_mov_b32 s34, s33\n"
    "s_mov_b32 s35, s36\n"
    "v_mfma_f32_16x16x32_bf16 v[8:11], a[132:135], v[32:35], v[8:11]\n"
    "s_xor_b32 s37, s37, 40960\n"
    "s_add_u32 s26, s26, 1\n"
    "s_sub_u32 s27, s26, 14\n"
    "v_mfma_f32_16x16x32_bf16 v[12:15], a[132:135], v[36:39], v[12:15]\n"
    "s_ashr_i32 s28, s27, 1\n"
    "s_and_b32 s29, s27, 1\n"
    "s_add_u32 s30, s28, 14\n"
    "v_mfma_f32_16x16x32_bf16 v[16:19], a[212:215], v[32:35], v[16:19]\n"
    "ds_read_b128 v[32:35], v65 offset:32768\n"
    "v_mfma_f32_16x16x32_bf16 v[20:23], a[212:215], v[36:39], v[20:23]\n"
    "ds_read_b128 v[36:39], v65 offset:33792\n"
    "s_waitcnt lgkmcnt(4)\n"
    "v_mfma_f32_16x16x32_bf16 v[0:3], a[56:59], v[40:43], v[0:3]\n"
    "s_cmp_lt_i32 s27, 0\n"
    "s_cselect_b32 s29, 0, s29\n"
    "s_cselect_b32 s30, s26, s30\n"
    "v_mfma_f32_16x16x32_bf16 v[4:7], a[56:59], v[44:47], v[4:7]\n"
    "s_cmp_eq_u32 s29, 1\n"
    "s_cselect_b32 s30, s28, s30\n"
    "s_cselect_b32 s27, s15, 64\n"
    "v_mfma_f32_16x16x32_bf16 v[8:11], a[136:139], v[40:43], v[8:11]\n"
    "s_lshl_b32 s31, s29, 13\n"
    "s_add_u32 s40, s30, 1\n"
    "s_lshl_b32 s32, s30, 19\n"
    "v_mfma_f32_16x16x32_bf16 v[12:15], a[136:139], v[44:47], v[12:15]\n"
    "s_lshl_b32 s25, s30, 11\n"
    "s_xor_b32 s25, s25, s31\n"
    "s_and_b32 s25, s25, 0x2000\n"
    "v_mfma_f32_16x16x32_bf16 v[16:19], a[216:219], v[40:43], v[16:19]\n"
    "ds_read_b128 v[40:43], v65 offset:34816\n"
    "s_add_u32 s32, s32, s25\n"
    "v_mfma_f32_16x16x32_bf16 v[20:23], a[216:219], v[44:47], v[20:23]\n"
    "s_add_u32 s32, s32, s13\n"
    "ds_read_b128 v[44:47], v65 offset:35840\n"
    "s_waitcnt lgkmcnt(4)\n"
    "v_mfma_f32_16x16x32_bf16 v[0:3], a[60:63], v[24:27], v[0:3]\n"
    "s_lshl_b32 s33, s30, 20\n"
    "s_lshl_b32 s25, s30, 12\n"
    "s_xor_b32 s25, s25, s31\n"
    "v_mfma_f32_16x16x32_bf16 v[4:7], a[60:63], v[28:31], v[4:7]\n"
    "s_and_b32 s25, s25, 0x2000\n"
    "s_add_u32 s33, s33, s25\n"
    "s_add_u32 s33, s33, s14\n"
    "v_mfma_f32_16x16x32_bf16 v[8:11], a[140:143], v[24:27], v[8:11]\n"
    "s_mul_i32 s47, s40, s29\n"
    "v_mfma_f32_16x16x32_bf16 v[12:15], a[140:143], v[28:31], v[12:15]\n"
    "s_cmp_lt_u32 s30, s27\n"
    "s_cselect_b32 s36, 1, 0\n"
    "s_cselect_b32 s32, s32, 2130706432\n"
    "v_mfma_f32_16x16x32_bf16 v[16:19], a[220:223], v[24:27], v[16:19]\n"
    "ds_read_b128 v[24:27], v65 offset:36864\n"
    "v_mfma_f32_16x16x32_bf16 v[20:23], a[220:223], v[28:31], v[20:23]\n"
    "ds_read_b128 v[28:31], v65 offset:37888\n"
    "s_waitcnt lgkmcnt(4)\n"
    "v_mfma_f32_16x16x32_bf16 v[0:3], a[64:67], v[32:35], v[0:3]\n"
    "s_cmp_lt_u32 s30, s27\n"
    "s_cselect_b32 s39, s31, 2130706432\n"
    "s_cselect_b32 s47, s47, 0\n"
    "v_mfma_f32_16x16x32_bf16 v[4:7], a[64:67], v[36:39], v[4:7]\n"
    "s_cmp_gt_u32 s47, 0\n"
    "s_cselect_b32 s38, s17, 2130706432\n"
    "v_mfma_f32_16x16x32_bf16 v[8:11], a[144:147], v[32:35], v[8:11]\n"
    "buffer_load_dword v60, v68, s[0:3], s38 offen sc1\n"
    "v_mfma_f32_16x16x32_bf16 v[12:15], a[144:147], v[36:39], v[12:15]\n"
    "v_mfma_f32_16x16x32_bf16 v[16:19], a[224:227], v[32:35], v[16:19]\n"
    "ds_read_b128 v[32:35], v65 offset:38912\n"
    "v_mfma_f32_16x16x32_bf16 v[20:23], a[224:227], v[36:39], v[20:23]\n"
    "ds_read_b128 v[36:39], v65 offset:39936\n"
    "s_waitcnt lgkmcnt(4)\n"
    "v_mfma_f32_16x16x32_bf16 v[0:3], a[68:71], v[40:43], v[0:3]\n"
    "v_mfma_f32_16x16x32_bf16 v[4:7], a[68:71], v[44:47], v[4:7]\n"
    "v_mfma_f32_16x16x32_bf16 v[8:11], a[148:151], v[40:43], v[8:11]\n"
    "v_mfma_f32_16x16x32_bf16 v[12:15], a[148:151], v[44:47], v[12:15]\n"
    "v_mfma_f32_16x16x32_bf16 v[16:19], a[228:231], v[40:43], v[16:19]\n"
    "v_mfma_f32_16x16x32_bf16 v[20:23], a[228:231], v[44:47], v[20:23]\n"
    "s_waitcnt lgkmcnt(2)\n"
    "v_mfma_f32_16x16x32_bf16 v[0:3], a[72:75], v[24:27], v[0:3]\n"
    "v_mfma_f32_16x16x32_bf16 v[4:7], a[72:75], v[28:31], v[4:7]\n"
    "v_mfma_f32_16x16x32_bf16 v[8:11], a[152:155], v[24:27], v[8:11]\n"
    "v_mfma_f32_16x16x32_bf16 v[12:15], a[152:155], v[28:31], v[12:15]\n"
    "v_mfma_f32_16x16x32_bf16 v[16:19], a[232:235], v[24:27], v[16:19]\n"
    "v_mfma_f32_16x16x32_bf16 v[20:23], a[232:235], v[28:31], v[20:23]\n"
    "s_waitcnt lgkmcnt(0)\n"
    "v_mfma_f32_16x16x32_bf16 v[0:3], a[76:79], v[32:35], v[0:3]\n"
    "v_mfma_f32_16x16x32_bf16 v[4:7], a[76:79], v[36:39], v[4:7]\n"
    "v_mfma_f32_16x16x32_bf16 v[8:11], a[156:159], v[32:35], v[8:11]\n"
    "v_mfma_f32_16x16x32_bf16 v[12:15], a[156:159], v[36:39], v[12:15]\n"
    "v_mfma_f32_16x16x32_bf16 v[16:19], a[236:239], v[32:35], v[16:19]\n"
    "v_mfma_f32_16x16x32_bf16 v[20:23], a[236:239], v[36:39], v[20:23]\n"
    "v_cvt_pk_bf16_f32 v48, v0, v1\n"
    "v_cvt_pk_bf16_f32 v49, v2, v3\n"
    "v_cvt_pk_bf16_f32 v50, v8, v9\n"
    "v_cvt_pk_bf16_f32 v51, v10, v11\n"
    "v_cvt_pk_bf16_f32 v52, v4, v5\n"
    "v_cvt_pk_bf16_f32 v53, v6, v7\n"
    "v_cvt_pk_bf16_f32 v54, v12, v13\n"
    "v_cvt_pk_bf16_f32 v55, v14, v15\n"
    "v_cvt_pk_bf16_f32 v56, v16, v17\n"
    "v_cvt_pk_bf16_f32 v57, v18, v19\n"
    "v_cvt_pk_bf16_f32 v58, v20, v21\n"
    "v_cvt_pk_bf16_f32 v59, v22, v23\n"
    ".Lqw_mk1x4_%=:\n"
    ".Lqw_join_%=:\n"
    "v_xor_b32 v65, 40960, v65\n"
    "s_waitcnt vmcnt(0) lgkmcnt(0)\n"
    "s_barrier\n"
    ".Lqw_mk2x5_%=:\n"
    "s_cmp_le_u32 s26, 142\n"
    "s_cbranch_scc1 .Lqw_top_%=\n"
    "buffer_store_dwordx4 v[48:51], v66, s[0:3], s46 offen offset:0 sc1\n"
    "buffer_store_dwordx4 v[52:55], v66, s[0:3], s46 offen offset:1024 sc1\n"
    "buffer_store_dwordx4 v[56:59], v67, s[0:3], s46 offen sc1\n"
    "s_waitcnt vmcnt(0)\n"
    "v_mov_b32 v61, s44\n"
    "buffer_store_dword v61, v69, s[0:3], s43 offen sc1\n"
    "s_waitcnt vmcnt(0) lgkmcnt(0)\n"
    "s_endpgm\n"
    ".Lqw_wx_%=:\n"
    "buffer_store_dwordx4 v[48:51], v66, s[0:3], s46 offen offset:0 sc1\n"
    "buffer_store_dwordx4 v[52:55], v66, s[0:3], s46 offen offset:1024 sc1\n"
    "buffer_store_dwordx4 v[56:59], v67, s[0:3], s46 offen sc1\n"
    "s_waitcnt vmcnt(0)\n"
    "v_mov_b32 v61, s44\n"
    "buffer_store_dword v61, v69, s[0:3], s43 offen sc1\n"
    "s_mov_b32 s43, 2130706432\n"
    "s_mov_b32 s46, 2130706432\n"
    ".Lqw_rt7_%=:\n"
    "s_memrealtime s[22:23]\n"
    "s_waitcnt lgkmcnt(0)\n"
    "s_sub_u32 s24, s45, s22\n"
    "s_cmp_lt_u32 s24, 600\n"
    "s_cbranch_scc1 .Lqw_rt7_%=\n"
    ".Lqw_po8_%=:\n"
    "buffer_load_dword v60, v68, s[0:3], s17 offen sc1\n"
    "s_waitcnt vmcnt(0)\n"
    "s_memrealtime s[22:23]\n"
    "s_waitcnt lgkmcnt(0)\n"
    "s_add_u32 s45, s22, 240\n"
    "v_cmp_gt_i32 vcc, s47, v60\n"
    "s_nop 0\n"
    "s_cbranch_vccz .Lqw_pd9_%=\n"
    "s_add_u32 s20, s20, 1\n"
    "s_cmp_lt_u32 s20, 0x40000\n"
    "s_cbranch_scc1 .Lqw_sp10_%=\n"
    "v_mov_b32 v60, 51\n"
    "s_mov_b32 s24, 33728\n"
    "buffer_store_dword v60, off, s[0:3], s24 sc1\n"
    "s_waitcnt vmcnt(0)\n"
    "s_branch .Lqw_pd9_%=\n"
    ".Lqw_sp10_%=:\n"
    "s_sleep 10\n"
    "s_branch .Lqw_po8_%=\n"
    ".Lqw_pd9_%=:\n"
    "s_branch .Lqw_wxb_%=\n"
    ".Lqw_skip_%=:\n"
    "buffer_store_dwordx4 v[48:51], v66, s[0:3], s46 offen offset:0 sc1\n"
    "buffer_store_dwordx4 v[52:55], v66, s[0:3], s46 offen offset:1024 sc1\n"
    "buffer_store_dwordx4 v[56:59], v67, s[0:3], s46 offen sc1\n"
    "s_add_u32 m0, s37, 0\n"
    "s_nop 0\n"
    "buffer_load_dwordx4 v64, s[0:3], s32 offen sc1 lds\n"
    "buffer_load_dwordx4 v64, s[0:3], s32 offen offset:1024 sc1 lds\n"
    "s_add_u32 m0, s37, 8192\n"
    "s_add_u32 s32, s32, 0x4000\n"
    "buffer_load_dwordx4 v64, s[0:3], s32 offen sc1 lds\n"
    "buffer_load_dwordx4 v64, s[0:3], s32 offen offset:1024 sc1 lds\n"
    "s_add_u32 m0, s37, 16384\n"
    "s_add_u32 s32, s32, 0x4000\n"
    "buffer_load_dwordx4 v64, s[0:3], s32 offen sc1 lds\n"
    "buffer_load_dwordx4 v64, s[0:3], s32 offen offset:1024 sc1 lds\n"
    "s_add_u32 m0, s37, 24576\n"
    "s_add_u32 s32, s32, 0x4000\n"
    "buffer_load_dwordx4 v64, s[0:3], s32 offen sc1 lds\n"
    "buffer_load_dwordx4 v64, s[0:3], s32 offen offset:1024 sc1 lds\n"
    "s_add_u32 m0, s37, 32768\n"
    "s_add_u32 s32, s32, 0x4000\n"
    "buffer_load_dwordx4 v64, s[0:3], s32 offen sc1 lds\n"
    "buffer_load_dwordx4 v64, s[0:3], s32 offen offset:1024 sc1 lds\n"
    "s_waitcnt vmcnt(0)\n"
    "v_mov_b32 v61, s44\n"
    "buffer_store_dword v61, v69, s[0:3], s43 offen sc1\n"
    "s_mov_b32 s34, 2130706432\n"
    "s_mov_b32 s46, s34\n"
    "s_mov_b32 s43, s41\n"
    "s_mov_b32 s44, s42\n"
    "s_mov_b32 s41, s39\n"
    "s_mov_b32 s42, s40\n"
    "s_mov_b32 s34, s33\n"
    "s_mov_b32 s35, s36\n"
    "s_xor_b32 s37, s37, 40960\n"
    "s_add_u32 s26, s26, 1\n"
    "s_sub_u32 s27, s26, 14\n"
    "s_ashr_i32 s28, s27, 1\n"
    "s_and_b32 s29, s27, 1\n"
    "s_add_u32 s30, s28, 14\n"
    "s_cmp_lt_i32 s27, 0\n"
    "s_cselect_b32 s29, 0, s29\n"
    "s_cselect_b32 s30, s26, s30\n"
    "s_cmp_eq_u32 s29, 1\n"
    "s_cselect_b32 s30, s28, s30\n"
    "s_cselect_b32 s27, s15, 64\n"
    "s_lshl_b32 s31, s29, 13\n"
    "s_add_u32 s40, s30, 1\n"
    "s_lshl_b32 s32, s30, 19\n"
    "s_lshl_b32 s25, s30, 11\n"
    "s_xor_b32 s25, s25, s31\n"
    "s_and_b32 s25, s25, 0x2000\n"
    "s_add_u32 s32, s32, s25\n"
    "s_add_u32 s32, s32, s13\n"
    "s_lshl_b32 s33, s30, 20\n"
    "s_lshl_b32 s25, s30, 12\n"
    "s_xor_b32 s25, s25, s31\n"
    "s_and_b32 s25, s25, 0x2000\n"
    "s_add_u32 s33, s33, s25\n"
    "s_add_u32 s33, s33, s14\n"
    "s_mul_i32 s47, s40, s29\n"
    "s_cmp_lt_u32 s30, s27\n"
    "s_cselect_b32 s36, 1, 0\n"
    "s_cselect_b32 s32, s32, 2130706432\n"
    "s_cmp_lt_u32 s30, s27\n"
    "s_cselect_b32 s39, s31, 2130706432\n"
    "s_cselect_b32 s47, s47, 0\n"
    "s_cmp_gt_u32 s47, 0\n"
    "s_cselect_b32 s38, s17, 2130706432\n"
    "buffer_load_dword v60, v68, s[0:3], s38 offen sc1\n"
    "s_branch .Lqw_join_%=\n"
    :: "{s[0:3]}"(s0_), "{s[4:7]}"(s4_), "{s[8:11]}"(s8_), "{s[12:15]}"(s12_), "{s[16:19]}"(s16_), "{v[0:3]}"(v0_), "{v[64:67]}"(v64_), "{v[68:71]}"(v68_) : "memory", "v255", "a255", "s95");
    __builtin_unreachable();
}
DEV void b1_role(const MegaArgs& A, const int L, const int i, const int rep) {
    ROLE_HEAD;
    const int qd = 4 * w + lq, sw = (l16 & 6) << 3, hw = w < 3 ? w : 2, hd = 3 * i + w;
    const bool live = w < 3 && hd < 32;
    const unsigned om = live ? RC_O - RC_X1 - w * 2048 + (hd >> 3) * 16384 + ((hd >> 1) & 3) * 2048 + (lq & 1) * 1024 + (2 * (hd & 1) + (lq >> 1)) * 256 + l16 * 16 : OOB;
    const unsigned ox = live && l < 16 ? RC_O - RC_X1 - w * 2048 + 65536 + hd * 256 + l16 * 16 : OOB;
    u64 wm[4];
#pragma unroll
    for (int e = 0; e < 4; ++e) { const int d = l16 + 16 - 16 * (e >> 1) - 4 * lq - 2 * (e & 1) - (l16 & 1); wm[e] = __ballot(d >= 0 && d <= 14); }
    const u32x4 s0_ = {RFL(rx[0]), RFL(rx[1]), RFL(rx[2]), RFL(rx[3])};
    const u32x4 s4_ = {RFL(lo(A.wqkv + 60 * i + 16 * w)), RFL(hi(A.wqkv + 60 * i + 16 * w)), RFL((640 * 1920 - 60 * i - 16 * w) * 2), RFL(0x20000u)};
    const u32x4 s8_ = {RFL((unsigned)wm[0]), RFL((unsigned)(wm[0] >> 32)), RFL((unsigned)wm[1]), RFL((unsigned)(wm[1] >> 32))};
    const u32x4 s12_ = {RFL((unsigned)wm[2]), RFL((unsigned)(wm[2] >> 32)), RFL((unsigned)wm[3]), RFL((unsigned)(wm[3] >> 32))};
    const u32x4 s16_ = {RFL(w == 3 ? 0xf000f000u : 0u), RFL(w == 3 ? 0xf000f000u : 0u), RFL(w), RFL(L << 13)};
    const u32x4 s20_ = {RFL(REC0 + RC_X1 + w * 2048), RFL(xl(Z_X1 + rep * 4096, L)), RFL(DBG0 + ((L * 128 + 96 + i) * 4 + w) * 2048), RFL(0u)};
    const u32x4 v0_ = {(unsigned)((l >> 1) * 3840 + (l & 1) * 16 + 0), (unsigned)((l >> 1) * 3840 + (l & 1) * 16 + 256), (unsigned)((l >> 1) * 3840 + (l & 1) * 16 + 512), (unsigned)(w * 40960 + (8 * lq + (l16 >> 2)) * 32 + (l & 3) * 8)};
    const u32x4 v224_ = {(unsigned)(l * 16 + ((w + 0) & 3) * 2048), (unsigned)(l * 16 + ((w + 1) & 3) * 2048), (unsigned)(l * 16 + ((w + 2) & 3) * 2048), (unsigned)(l * 16 + ((w + 3) & 3) * 2048)};
    const u32x4 v228_ = {(unsigned)(l * 16), (unsigned)((l & 31) * 128), (unsigned)(l < 4 && w == 3 ? xl(Z_O + l * 2048 + i * 128, L) : OOB), (unsigned)(qd == 15 ? 86016 : 40960 + (qd / 5) * 2048 + l16 * 64 + (((qd % 5) * 8) ^ sw))};
    const u32x4 v232_ = {(unsigned)(qd == 15 ? 86016 : 49152 + (qd / 5) * 4096 + l16 * 64 + (((qd % 5) * 8) ^ sw)), (unsigned)(qd == 15 ? 86016 : 61440 + (qd / 5) * 4096 + l16 * 64 + (((qd % 5) * 8) ^ sw)), (unsigned)(40960 + hw * 2048 + l16 * 64 + ((lq * 16) ^ sw)), (unsigned)(49152 + hw * 4096 + l16 * 64 + ((lq * 16) ^ sw))};
    const u32x4 v236_ = {(unsigned)(51200 + hw * 4096 + l16 * 64 + ((lq * 16) ^ sw)), (unsigned)(61440 + hw * 4096 + (l >> 2) * 64 + ((0 + (l & 3) * 8) ^ (((l >> 2) & 6) << 3))), (unsigned)(63488 + hw * 4096 + (l >> 2) * 64 + ((0 + (l & 3) * 8) ^ (((l >> 2) & 6) << 3))), (unsigned)(61440 + hw * 4096 + (l >> 2) * 64 + ((32 + (l & 3) * 8) ^ (((l >> 2) & 6) << 3)))};
    const u32x4 v240_ = {(unsigned)(63488 + hw * 4096 + (l >> 2) * 64 + ((32 + (l & 3) * 8) ^ (((l >> 2) & 6) << 3))), (unsigned)(73728 + w * 2048 + l * 16), (unsigned)(73728 + (l >> 5) * 1024 + ((l16 >> 2) * 16 + l16) * 16 + (l & 3) * 4), (unsigned)(81920 + w * 512 + l * 4)};
    const u32x4 v244_ = {(unsigned)(81920 + 3 * 512 + (l >> 5) * 256 + (48 + l16) * 4), (unsigned)(om), (unsigned)(ox), (unsigned)((l & 1) ? 0x01000c0cu : 0x0c0c0100u)};
    const u32x4 v248_ = {(unsigned)((l & 1) ? 0x03020c0cu : 0x0c0c0302u), (unsigned)(0xff800000u), (unsigned)(0u), (unsigned)(0u)};
    asm volatile(
    "s_mov_b32 s24, 0\n"
    "s_sleep 35\n"
    "s_sleep 35\n"
    "s_sleep 35\n"
    "s_sleep 35\n"
    "s_sleep 35\n"
    "s_sleep 35\n"
    "s_sleep 35\n"
    "s_sleep 35\n"
    "s_sleep 35\n"
    "s_sleep 35\n"
    "s_sleep 35\n"
    "s_sleep 35\n"
    "s_sleep 35\n"
    "s_sleep 35\n"
    ".Lb1_ho1_%=:\n"
    "s_mul_i32 s32, s18, 40960\n"
    "s_add_u32 s35, s18, 0\n"
    "s_and_b32 s35, s35, 3\n"
    "s_add_u32 s35, s35, 0\n"
    "s_mul_i32 s35, s35, 122880\n"
    "s_add_u32 m0, s32, 0\n"
    "s_nop 0\n"
    "buffer_load_dwordx4 v0, s[4:7], s35 offen offset:0 lds\n"
    "buffer_load_dwordx4 v1, s[4:7], s35 offen offset:1024 lds\n"
    "buffer_load_dwordx4 v2, s[4:7], s35 offen offset:2048 lds\n"
    "s_add_u32 s35, s18, 1\n"
    "s_and_b32 s35, s35, 3\n"
    "s_add_u32 s35, s35, 0\n"
    "s_mul_i32 s35, s35, 122880\n"
    "s_add_u32 m0, s32, 3072\n"
    "s_nop 0\n"
    "buffer_load_dwordx4 v0, s[4:7], s35 offen offset:0 lds\n"
    "buffer_load_dwordx4 v1, s[4:7], s35 offen offset:1024 lds\n"
    "buffer_load_dwordx4 v2, s[4:7], s35 offen offset:2048 lds\n"
    "s_add_u32 s35, s18, 2\n"
    "s_and_b32 s35, s35, 3\n"
    "s_add_u32 s35, s35, 0\n"
    "s_mul_i32 s35, s35, 122880\n"
    "s_add_u32 m0, s32, 6144\n"
    "s_nop 0\n"
    "buffer_load_dwordx4 v0, s[4:7], s35 offen offset:0 lds\n"
    "buffer_load_dwordx4 v1, s[4:7], s35 offen offset:1024 lds\n"
    "buffer_load_dwordx4 v2, s[4:7], s35 offen offset:2048 lds\n"
    "s_add_u32 s35, s18, 3\n"
    "s_and_b32 s35, s35, 3\n"
    "s_add_u32 s35, s35, 0\n"
    "s_mul_i32 s35, s35, 122880\n"
    "s_add_u32 m0, s32, 9216\n"
    "s_nop 0\n"
    "buffer_load_dwordx4 v0, s[4:7], s35 offen offset:0 lds\n"
    "buffer_load_dwordx4 v1, s[4:7], s35 offen offset:1024 lds\n"
    "buffer_load_dwordx4 v2, s[4:7], s35 offen offset:2048 lds\n"
    "s_add_u32 s35, s18, 0\n"
    "s_and_b32 s35, s35, 3\n"
    "s_add_u32 s35, s35, 4\n"
    "s_mul_i32 s35, s35, 122880\n"
    "s_add_u32 m0, s32, 12288\n"
    "s_nop 0\n"
    "buffer_load_dwordx4 v0, s[4:7], s35 offen offset:0 lds\n"
    "buffer_load_dwordx4 v1, s[4:7], s35 offen offset:1024 lds\n"
    "buffer_load_dwordx4 v2, s[4:7], s35 offen offset:2048 lds\n"
    "s_add_u32 s35, s18, 1\n"
    "s_and_b32 s35, s35, 3\n"
    "s_add_u32 s35, s35, 4\n"
    "s_mul_i32 s35, s35, 122880\n"
    "s_add_u32 m0, s32, 15360\n"
    "s_nop 0\n"
    "buffer_load_dwordx4 v0, s[4:7], s35 offen offset:0 lds\n"
    "buffer_load_dwordx4 v1, s[4:7], s35 offen offset:1024 lds\n"
    "buffer_load_dwordx4 v2, s[4:7], s35 offen offset:2048 lds\n"
    "s_add_u32 s35, s18, 2\n"
    "s_and_b32 s35, s35, 3\n"
    "s_add_u32 s35, s35, 4\n"
    "s_mul_i32 s35, s35, 122880\n"
    "s_add_u32 m0, s32, 18432\n"
    "s_nop 0\n"
    "buffer_load_dwordx4 v0, s[4:7], s35 offen offset:0 lds\n"
    "buffer_load_dwordx4 v1, s[4:7], s35 offen offset:1024 lds\n"
    "buffer_load_dwordx4 v2, s[4:7], s35 offen offset:2048 lds\n"
    "s_add_u32 s35, s18, 3\n"
    "s_and_b32 s35, s35, 3\n"
    "s_add_u32 s35, s35, 4\n"
    "s_mul_i32 s35, s35, 122880\n"
    "s_add_u32 m0, s32, 21504\n"
    "s_nop 0\n"
    "buffer_load_dwordx4 v0, s[4:7], s35 offen offset:0 lds\n"
    "buffer_load_dwordx4 v1, s[4:7], s35 offen offset:1024 lds\n"
    "buffer_load_dwordx4 v2, s[4:7], s35 offen offset:2048 lds\n"
    "s_add_u32 s35, s18, 0\n"
    "s_and_b32 s35, s35, 3\n"
    "s_add_u32 s35, s35, 8\n"
    "s_mul_i32 s35, s35, 122880\n"
    "s_add_u32 m0, s32, 24576\n"
    "s_nop 0\n"
    "buffer_load_dwordx4 v0, s[4:7], s35 offen offset:0 lds\n"
    "buffer_load_dwordx4 v1, s[4:7], s35 offen offset:1024 lds\n"
    "buffer_load_dwordx4 v2, s[4:7], s35 offen offset:2048 lds\n"
    "s_add_u32 s35, s18, 1\n"
    "s_and_b32 s35, s35, 3\n"
    "s_add_u32 s35, s35, 8\n"
    "s_mul_i32 s35, s35, 122880\n"
    "s_add_u32 m0, s32, 27648\n"
    "s_nop 0\n"
    "buffer_load_dwordx4 v0, s[4:7], s35 offen offset:0 lds\n"
    "buffer_load_dwordx4 v1, s[4:7], s35 offen offset:1024 lds\n"
    "buffer_load_dwordx4 v2, s[4:7], s35 offen offset:2048 lds\n"
    "s_add_u32 s35, s18, 2\n"
    "s_and_b32 s35, s35, 3\n"
    "s_add_u32 s35, s35, 8\n"
    "s_mul_i32 s35, s35, 122880\n"
    "s_add_u32 m0, s32, 30720\n"
    "s_nop 0\n"
    "buffer_load_dwordx4 v0, s[4:7], s35 offen offset:0 lds\n"
    "buffer_load_dwordx4 v1, s[4:7], s35 offen offset:1024 lds\n"
    "buffer_load_dwordx4 v2, s[4:7], s35 offen offset:2048 lds\n"
    "s_add_u32 s35, s18, 3\n"
    "s_and_b32 s35, s35, 3\n"
    "s_add_u32 s35, s35, 8\n"
    "s_mul_i32 s35, s35, 122880\n"
    "s_add_u32 m0, s32, 33792\n"
    "s_nop 0\n"
    "buffer_load_dwordx4 v0, s[4:7], s35 offen offset:0 lds\n"
    "buffer_load_dwordx4 v1, s[4:7], s35 offen offset:1024 lds\n"
    "buffer_load_dwordx4 v2, s[4:7], s35 offen offset:2048 lds\n"
    "s_add_u32 s35, s18, 0\n"
    "s_and_b32 s35, s35, 3\n"
    "s_add_u32 s35, s35, 12\n"
    "s_mul_i32 s35, s35, 122880\n"
    "s_add_u32 m0, s32, 36864\n"
    "s_nop 0\n"
    "buffer_load_dwordx4 v0, s[4:7], s35 offen offset:0 lds\n"
    "buffer_load_dwordx4 v1, s[4:7], s35 offen offset:1024 lds\n"
    "buffer_load_dwordx4 v2, s[4:7], s35 offen offset:2048 lds\n"
    "s_waitcnt vmcnt(36)\n"
    "ds_read_b64_tr_b16 a[0:1], v3 offset:0\n"
    "ds_read_b64_tr_b16 a[2:3], v3 offset:128\n"
    "ds_read_b64_tr_b16 a[80:81], v3 offset:1024\n"
    "ds_read_b64_tr_b16 a[82:83], v3 offset:1152\n"
    "ds_read_b64_tr_b16 a[160:161], v3 offset:2048\n"
    "ds_read_b64_tr_b16 a[162:163], v3 offset:2176\n"
    "s_waitcnt lgkmcnt(0)\n"
    "s_add_u32 s35, s18, 1\n"
    "s_and_b32 s35, s35, 3\n"
    "s_add_u32 s35, s35, 12\n"
    "s_mul_i32 s35, s35, 122880\n"
    "s_add_u32 m0, s32, 0\n"
    "s_nop 0\n"
    "buffer_load_dwordx4 v0, s[4:7], s35 offen offset:0 lds\n"
    "buffer_load_dwordx4 v1, s[4:7], s35 offen offset:1024 lds\n"
    "buffer_load_dwordx4 v2, s[4:7], s35 offen offset:2048 lds\n"
    "s_waitcnt vmcnt(36)\n"
    "ds_read_b64_tr_b16 a[4:5], v3 offset:3072\n"
    "ds_read_b64_tr_b16 a[6:7], v3 offset:3200\n"
    "ds_read_b64_tr_b16 a[84:85], v3 offset:4096\n"
    "ds_read_b64_tr_b16 a[86:87], v3 offset:4224\n"
    "ds_read_b64_tr_b16 a[164:165], v3 offset:5120\n"
    "ds_read_b64_tr_b16 a[166:167], v3 offset:5248\n"
    "s_waitcnt lgkmcnt(0)\n"
    "s_add_u32 s35, s18, 2\n"
    "s_and_b32 s35, s35, 3\n"
    "s_add_u32 s35, s35, 12\n"
    "s_mul_i32 s35, s35, 122880\n"
    "s_add_u32 m0, s32, 3072\n"
    "s_nop 0\n"
    "buffer_load_dwordx4 v0, s[4:7], s35 offen offset:0 lds\n"
    "buffer_load_dwordx4 v1, s[4:7], s35 offen offset:1024 lds\n"
    "buffer_load_dwordx4 v2, s[4:7], s35 offen offset:2048 lds\n"
    "s_waitcnt vmcnt(36)\n"
    "ds_read_b64_tr_b16 a[8:9], v3 offset:6144\n"
    "ds_read_b64_tr_b16 a[10:11], v3 offset:6272\n"
    "ds_read_b64_tr_b16 a[88:89], v3 offset:7168\n"
    "ds_read_b64_tr_b16 a[90:91], v3 offset:7296\n"
    "ds_read_b64_tr_b16 a[168:169], v3 offset:8192\n"
    "ds_read_b64_tr_b16 a[170:171], v3 offset:8320\n"
    "s_waitcnt lgkmcnt(0)\n"
    "s_add_u32 s35, s18, 3\n"
    "s_and_b32 s35, s35, 3\n"
    "s_add_u32 s35, s35, 12\n"
    "s_mul_i32 s35, s35, 122880\n"
    "s_add_u32 m0, s32, 6144\n"
    "s_nop 0\n"
    "buffer_load_dwordx4 v0, s[4:7], s35 offen offset:0 lds\n"
    "buffer_load_dwordx4 v1, s[4:7], s35 offen offset:1024 lds\n"
    "buffer_load_dwordx4 v2, s[4:7], s35 offen offset:2048 lds\n"
    "s_waitcnt vmcnt(36)\n"
    "ds_read_b64_tr_b16 a[12:13], v3 offset:9216\n"
    "ds_read_b64_tr_b16 a[14:15], v3 offset:9344\n"
    "ds_read_b64_tr_b16 a[92:93], v3 offset:10240\n"
    "ds_read_b64_tr_b16 a[94:95], v3 offset:10368\n"
    "ds_read_b64_tr_b16 a[172:173], v3 offset:11264\n"
    "ds_read_b64_tr_b16 a[174:175], v3 offset:11392\n"
    "s_waitcnt lgkmcnt(0)\n"
    "s_add_u32 s35, s18, 0\n"
    "s_and_b32 s35, s35, 3\n"
    "s_add_u32 s35, s35, 16\n"
    "s_mul_i32 s35, s35, 122880\n"
    "s_add_u32 m0, s32, 9216\n"
    "s_nop 0\n"
    "buffer_load_dwordx4 v0, s[4:7], s35 offen offset:0 lds\n"
    "buffer_load_dwordx4 v1, s[4:7], s35 offen offset:1024 lds\n"
    "buffer_load_dwordx4 v2, s[4:7], s35 offen offset:2048 lds\n"
    "s_waitcnt vmcnt(36)\n"
    "ds_read_b64_tr_b16 a[16:17], v3 offset:12288\n"
    "ds_read_b64_tr_b16 a[18:19], v3 offset:12416\n"
    "ds_read_b64_tr_b16 a[96:97], v3 offset:13312\n"
    "ds_read_b64_tr_b16 a[98:99], v3 offset:13440\n"
    "ds_read_b64_tr_b16 a[176:177], v3 offset:14336\n"
    "ds_read_b64_tr_b16 a[178:179], v3 offset:14464\n"
    "s_waitcnt lgkmcnt(0)\n"
    "s_add_u32 s35, s18, 1\n"
    "s_and_b32 s35, s35, 3\n"
    "s_add_u32 s35, s35, 16\n"
    "s_mul_i32 s35, s35, 122880\n"
    "s_add_u32 m0, s32, 12288\n"
    "s_nop 0\n"
    "buffer_load_dwordx4 v0, s[4:7], s35 offen offset:0 lds\n"
    "buffer_load_dwordx4 v1, s[4:7], s35 offen offset:1024 lds\n"
    "buffer_load_dwordx4 v2, s[4:7], s35 offen offset:2048 lds\n"
    "s_waitcnt vmcnt(36)\n"
    "ds_read_b64_tr_b16 a[20:21], v3 offset:15360\n"
    "ds_read_b64_tr_b16 a[22:23], v3 offset:15488\n"
    "ds_read_b64_tr_b16 a[100:101], v3 offset:16384\n"
    "ds_read_b64_tr_b16 a[102:103], v3 offset:16512\n"
    "ds_read_b64_tr_b16 a[180:181], v3 offset:17408\n"
    "ds_read_b64_tr_b16 a[182:183], v3 offset:17536\n"
    "s_waitcnt lgkmcnt(0)\n"
    "s_add_u32 s35, s18, 2\n"
    "s_and_b32 s35, s35, 3\n"
    "s_add_u32 s35, s35, 16\n"
    "s_mul_i32 s35, s35, 122880\n"
    "s_add_u32 m0, s32, 15360\n"
    "s_nop 0\n"
    "buffer_load_dwordx4 v0, s[4:7], s35 offen offset:0 lds\n"
    "buffer_load_dwordx4 v1, s[4:7], s35 offen offset:1024 lds\n"
    "buffer_load_dwordx4 v2, s[4:7], s35 offen offset:2048 lds\n"
    "s_waitcnt vmcnt(36)\n"
    "ds_read_b64_tr_b16 a[24:25], v3 offset:18432\n"
    "ds_read_b64_tr_b16 a[26:27], v3 offset:18560\n"
    "ds_read_b64_tr_b16 a[104:105], v3 offset:19456\n"
    "ds_read_b64_tr_b16 a[106:107], v3 offset:19584\n"
    "ds_read_b64_tr_b16 a[184:185], v3 offset:20480\n"
    "ds_read_b64_tr_b16 a[186:187], v3 offset:20608\n"
    "s_waitcnt lgkmcnt(0)\n"
    "s_add_u32 s35, s18, 3\n"
    "s_and_b32 s35, s35, 3\n"
    "s_add_u32 s35, s35, 16\n"
    "s_mul_i32 s35, s35, 122880\n"
    "s_add_u32 m0, s32, 18432\n"
    "s_nop 0\n"
    "buffer_load_dwordx4 v0, s[4:7], s35 offen offset:0 lds\n"
    "buffer_load_dwordx4 v1, s[4:7], s35 offen offset:1024 lds\n"
    "buffer_load_dwordx4 v2, s[4:7], s35 offen offset:2048 lds\n"
    "s_waitcnt vmcnt(36)\n"
    "ds_read_b64_tr_b16 a[28:29], v3 offset:21504\n"
    "ds_read_b64_tr_b16 a[30:31], v3 offset:21632\n"
    "ds_read_b64_tr_b16 a[108:109], v3 offset:22528\n"
    "ds_read_b64_tr_b16 a[110:111], v3 offset:22656\n"
    "ds_read_b64_tr_b16 a[188:189], v3 offset:23552\n"
    "ds_read_b64_tr_b16 a[190:191], v3 offset:23680\n"
    "s_waitcnt lgkmcnt(0)\n"
    "s_waitcnt vmcnt(33)\n"
    "ds_read_b64_tr_b16 a[32:33], v3 offset:24576\n"
    "ds_read_b64_tr_b16 a[34:35], v3 offset:24704\n"
    "ds_read_b64_tr_b16 a[112:113], v3 offset:25600\n"
    "ds_read_b64_tr_b16 a[114:115], v3 offset:25728\n"
    "ds_read_b64_tr_b16 a[192:193], v3 offset:26624\n"
    "ds_read_b64_tr_b16 a[194:195], v3 offset:26752\n"
    "s_waitcnt lgkmcnt(0)\n"
    "s_waitcnt vmcnt(30)\n"
    "ds_read_b64_tr_b16 a[36:37], v3 offset:27648\n"
    "ds_read_b64_tr_b16 a[38:39], v3 offset:27776\n"
    "ds_read_b64_tr_b16 a[116:117], v3 offset:28672\n"
    "ds_read_b64_tr_b16 a[118:119], v3 offset:28800\n"
    "ds_read_b64_tr_b16 a[196:197], v3 offset:29696\n"
    "ds_read_b64_tr_b16 a[198:199], v3 offset:29824\n"
    "s_waitcnt lgkmcnt(0)\n"
    "s_waitcnt vmcnt(27)\n"
    "ds_read_b64_tr_b16 a[40:41], v3 offset:30720\n"
    "ds_read_b64_tr_b16 a[42:43], v3 offset:30848\n"
    "ds_read_b64_tr_b16 a[120:121], v3 offset:31744\n"
    "ds_read_b64_tr_b16 a[122:123], v3 offset:31872\n"
    "ds_read_b64_tr_b16 a[200:201], v3 offset:32768\n"
    "ds_read_b64_tr_b16 a[202:203], v3 offset:32896\n"
    "s_waitcnt lgkmcnt(0)\n"
    "s_waitcnt vmcnt(24)\n"
    "ds_read_b64_tr_b16 a[44:45], v3 offset:33792\n"
    "ds_read_b64_tr_b16 a[46:47], v3 offset:33920\n"
    "ds_read_b64_tr_b16 a[124:125], v3 offset:34816\n"
    "ds_read_b64_tr_b16 a[126:127], v3 offset:34944\n"
    "ds_read_b64_tr_b16 a[204:205], v3 offset:35840\n"
    "ds_read_b64_tr_b16 a[206:207], v3 offset:35968\n"
    "s_waitcnt lgkmcnt(0)\n"
    "s_waitcnt vmcnt(21)\n"
    "ds_read_b64_tr_b16 a[48:49], v3 offset:36864\n"
    "ds_read_b64_tr_b16 a[50:51], v3 offset:36992\n"
    "ds_read_b64_tr_b16 a[128:129], v3 offset:37888\n"
    "ds_read_b64_tr_b16 a[130:131], v3 offset:38016\n"
    "ds_read_b64_tr_b16 a[208:209], v3 offset:38912\n"
    "ds_read_b64_tr_b16 a[210:211], v3 offset:39040\n"
    "s_waitcnt lgkmcnt(0)\n"
    "s_waitcnt vmcnt(18)\n"
    "ds_read_b64_tr_b16 a[52:53], v3 offset:0\n"
    "ds_read_b64_tr_b16 a[54:55], v3 offset:128\n"
    "ds_read_b64_tr_b16 a[132:133], v3 offset:1024\n"
    "ds_read_b64_tr_b16 a[134:135], v3 offset:1152\n"
    "ds_read_b64_tr_b16 a[212:213], v3 offset:2048\n"
    "ds_read_b64_tr_b16 a[214:215], v3 offset:2176\n"
    "s_waitcnt lgkmcnt(0)\n"
    "s_waitcnt vmcnt(15)\n"
    "ds_read_b64_tr_b16 a[56:57], v3 offset:3072\n"
    "ds_read_b64_tr_b16 a[58:59], v3 offset:3200\n"
    "ds_read_b64_tr_b16 a[136:137], v3 offset:4096\n"
    "ds_read_b64_tr_b16 a[138:139], v3 offset:4224\n"
    "ds_read_b64_tr_b16 a[216:217], v3 offset:5120\n"
    "ds_read_b64_tr_b16 a[218:219], v3 offset:5248\n"
    "s_waitcnt lgkmcnt(0)\n"
    "s_waitcnt vmcnt(12)\n"
    "ds_read_b64_tr_b16 a[60:61], v3 offset:6144\n"
    "ds_read_b64_tr_b16 a[62:63], v3 offset:6272\n"
    "ds_read_b64_tr_b16 a[140:141], v3 offset:7168\n"
    "ds_read_b64_tr_b16 a[142:143], v3 offset:7296\n"
    "ds_read_b64_tr_b16 a[220:221], v3 offset:8192\n"
    "ds_read_b64_tr_b16 a[222:223], v3 offset:8320\n"
    "s_waitcnt lgkmcnt(0)\n"
    "s_waitcnt vmcnt(9)\n"
    "ds_read_b64_tr_b16 a[64:65], v3 offset:9216\n"
    "ds_read_b64_tr_b16 a[66:67], v3 offset:9344\n"
    "ds_read_b64_tr_b16 a[144:145], v3 offset:10240\n"
    "ds_read_b64_tr_b16 a[146:147], v3 offset:10368\n"
    "ds_read_b64_tr_b16 a[224:225], v3 offset:11264\n"
    "ds_read_b64_tr_b16 a[226:227], v3 offset:11392\n"
    "s_waitcnt lgkmcnt(0)\n"
    "s_waitcnt vmcnt(6)\n"
    "ds_read_b64_tr_b16 a[68:69], v3 offset:12288\n"
    "ds_read_b64_tr_b16 a[70:71], v3 offset:12416\n"
    "ds_read_b64_tr_b16 a[148:149], v3 offset:13312\n"
    "ds_read_b64_tr_b16 a[150:151], v3 offset:13440\n"
    "ds_read_b64_tr_b16 a[228:229], v3 offset:14336\n"
    "ds_read_b64_tr_b16 a[230:231], v3 offset:14464\n"
    "s_waitcnt lgkmcnt(0)\n"
    "s_waitcnt vmcnt(3)\n"
    "ds_read_b64_tr_b16 a[72:73], v3 offset:15360\n"
    "ds_read_b64_tr_b16 a[74:75], v3 offset:15488\n"
    "ds_read_b64_tr_b16 a[152:153], v3 offset:16384\n"
    "ds_read_b64_tr_b16 a[154:155], v3 offset:16512\n"
    "ds_read_b64_tr_b16 a[232:233], v3 offset:17408\n"
    "ds_read_b64_tr_b16 a[234:235], v3 offset:17536\n"
    "s_waitcnt lgkmcnt(0)\n"
    "s_waitcnt vmcnt(0)\n"
    "ds_read_b64_tr_b16 a[76:77], v3 offset:18432\n"
    "ds_read_b64_tr_b16 a[78:79], v3 offset:18560\n"
    "ds_read_b64_tr_b16 a[156:157], v3 offset:19456\n"
    "ds_read_b64_tr_b16 a[158:159], v3 offset:19584\n"
    "ds_read_b64_tr_b16 a[236:237], v3 offset:20480\n"
    "ds_read_b64_tr_b16 a[238:239], v3 offset:20608\n"
    "s_waitcnt lgkmcnt(0)\n"
    "s_waitcnt lgkmcnt(0)\n"
    "s_barrier\n"
    "v_mov_b32 v32, 0x3f803f80\n"
    "v_mov_b32 v36, 0\n"
    "v_mov_b32 v33, 0x3f803f80\n"
    "v_mov_b32 v37, 0\n"
    "v_mov_b32 v34, 0x3f803f80\n"
    "v_mov_b32 v38, 0\n"
    "v_mov_b32 v35, 0x3f803f80\n"
    "v_mov_b32 v39, 0\n"
    "s_nop 0\n"
    "v_mfma_f32_16x16x32_bf16 v[96:99], a[0:3], v[32:35], 0\n"
    "v_mfma_f32_16x16x32_bf16 v[100:103], a[80:83], v[32:35], 0\n"
    "v_mfma_f32_16x16x32_bf16 v[104:107], a[160:163], v[32:35], 0\n"
    "v_mfma_f32_16x16x32_bf16 v[96:99], a[4:7], v[32:35], v[96:99]\n"
    "v_mfma_f32_16x16x32_bf16 v[100:103], a[84:87], v[32:35], v[100:103]\n"
    "v_mfma_f32_16x16x32_bf16 v[104:107], a[164:167], v[32:35], v[104:107]\n"
    "v_mfma_f32_16x16x32_bf16 v[96:99], a[8:11], v[32:35], v[96:99]\n"
    "v_mfma_f32_16x16x32_bf16 v[100:103], a[88:91], v[32:35], v[100:103]\n"
    "v_mfma_f32_16x16x32_bf16 v[104:107], a[168:171], v[32:35], v[104:107]\n"
    "v_mfma_f32_16x16x32_bf16 v[96:99], a[12:15], v[32:35], v[96:99]\n"
    "v_mfma_f32_16x16x32_bf16 v[100:103], a[92:95], v[32:35], v[100:103]\n"
    "v_mfma_f32_16x16x32_bf16 v[104:107], a[172:175], v[32:35], v[104:107]\n"
    "v_mfma_f32_16x16x32_bf16 v[96:99], a[16:19], v[32:35], v[96:99]\n"
    "v_mfma_f32_16x16x32_bf16 v[100:103], a[96:99], v[32:35], v[100:103]\n"
    "v_mfma_f32_16x16x32_bf16 v[104:107], a[176:179], v[32:35], v[104:107]\n"
    "v_mfma_f32_16x16x32_bf16 v[96:99], a[20:23], v[32:35], v[96:99]\n"
    "v_mfma_f32_16x16x32_bf16 v[100:103], a[100:103], v[32:35], v[100:103]\n"
    "v_mfma_f32_16x16x32_bf16 v[104:107], a[180:183], v[32:35], v[104:107]\n"
    "v_mfma_f32_16x16x32_bf16 v[96:99], a[24:27], v[32:35], v[96:99]\n"
    "v_mfma_f32_16x16x32_bf16 v[100:103], a[104:107], v[32:35], v[100:103]\n"
    "v_mfma_f32_16x16x32_bf16 v[104:107], a[184:187], v[32:35], v[104:107]\n"
    "v_mfma_f32_16x16x32_bf16 v[96:99], a[28:31], v[32:35], v[96:99]\n"
    "v_mfma_f32_16x16x32_bf16 v[100:103], a[108:111], v[32:35], v[100:103]\n"
    "v_mfma_f32_16x16x32_bf16 v[104:107], a[188:191], v[32:35], v[104:107]\n"
    "v_mfma_f32_16x16x32_bf16 v[96:99], a[32:35], v[32:35], v[96:99]\n"
    "v_mfma_f32_16x16x32_bf16 v[100:103], a[112:115], v[32:35], v[100:103]\n"
    "v_mfma_f32_16x16x32_bf16 v[104:107], a[192:195], v[32:35], v[104:107]\n"
    "v_mfma_f32_16x16x32_bf16 v[96:99], a[36:39], v[32:35], v[96:99]\n"
    "v_mfma_f32_16x16x32_bf16 v[100:103], a[116:119], v[32:35], v[100:103]\n"
    "v_mfma_f32_16x16x32_bf16 v[104:107], a[196:199], v[32:35], v[104:107]\n"
    "v_mfma_f32_16x16x32_bf16 v[96:99], a[40:43], v[32:35], v[96:99]\n"
    "v_mfma_f32_16x16x32_bf16 v[100:103], a[120:123], v[32:35], v[100:103]\n"
    "v_mfma_f32_16x16x32_bf16 v[104:107], a[200:203], v[32:35], v[104:107]\n"
    "v_mfma_f32_16x16x32_bf16 v[96:99], a[44:47], v[32:35], v[96:99]\n"
    "v_mfma_f32_16x16x32_bf16 v[100:103], a[124:127], v[32:35], v[100:103]\n"
    "v_mfma_f32_16x16x32_bf16 v[104:107], a[204:207], v[32:35], v[104:107]\n"
    "v_mfma_f32_16x16x32_bf16 v[96:99], a[48:51], v[32:35], v[96:99]\n"
    "v_mfma_f32_16x16x32_bf16 v[100:103], a[128:131], v[32:35], v[100:103]\n"
    "v_mfma_f32_16x16x32_bf16 v[104:107], a[208:211], v[32:35], v[104:107]\n"
    "v_mfma_f32_16x16x32_bf16 v[96:99], a[52:55], v[32:35], v[96:99]\n"
    "v_mfma_f32_16x16x32_bf16 v[100:103], a[132:135], v[32:35], v[100:103]\n"
    "v_mfma_f32_16x16x32_bf16 v[104:107], a[212:215], v[32:35], v[104:107]\n"
    "v_mfma_f32_16x16x32_bf16 v[96:99], a[56:59], v[32:35], v[96:99]\n"
    "v_mfma_f32_16x16x32_bf16 v[100:103], a[136:139], v[32:35], v[100:103]\n"
    "v_mfma_f32_16x16x32_bf16 v[104:107], a[216:219], v[32:35], v[104:107]\n"
    "v_mfma_f32_16x16x32_bf16 v[96:99], a[60:63], v[32:35], v[96:99]\n"
    "v_mfma_f32_16x16x32_bf16 v[100:103], a[140:143], v[32:35], v[100:103]\n"
    "v_mfma_f32_16x16x32_bf16 v[104:107], a[220:223], v[32:35], v[104:107]\n"
    "v_mfma_f32_16x16x32_bf16 v[96:99], a[64:67], v[32:35], v[96:99]\n"
    "v_mfma_f32_16x16x32_bf16 v[100:103], a[144:147], v[32:35], v[100:103]\n"
    "v_mfma_f32_16x16x32_bf16 v[104:107], a[224:227], v[32:35], v[104:107]\n"
    "v_mfma_f32_16x16x32_bf16 v[96:99], a[68:71], v[32:35], v[96:99]\n"
    "v_mfma_f32_16x16x32_bf16 v[100:103], a[148:151], v[32:35], v[100:103]\n"
    "v_mfma_f32_16x16x32_bf16 v[104:107], a[228:231], v[32:35], v[104:107]\n"
    "v_mfma_f32_16x16x32_bf16 v[96:99], a[72:75], v[32:35], v[96:99]\n"
    "v_mfma_f32_16x16x32_bf16 v[100:103], a[152:155], v[32:35], v[100:103]\n"
    "v_mfma_f32_16x16x32_bf16 v[104:107], a[232:235], v[32:35], v[104:107]\n"
    "v_mfma_f32_16x16x32_bf16 v[96:99], a[76:79], v[32:35], v[96:99]\n"
    "v_mfma_f32_16x16x32_bf16 v[100:103], a[156:159], v[32:35], v[100:103]\n"
    "v_mfma_f32_16x16x32_bf16 v[104:107], a[236:239], v[32:35], v[104:107]\n"
    "s_lshl_b32 s35, s18, 13\n"
    "v_add_u32 v8, s35, v228\n"
    "ds_write_b128 v8, v[36:39] offset:40960\n"
    "ds_write_b128 v8, v[36:39] offset:41984\n"
    "ds_write_b128 v8, v[36:39] offset:43008\n"
    "ds_write_b128 v8, v[36:39] offset:44032\n"
    "ds_write_b128 v8, v[36:39] offset:45056\n"
    "ds_write_b128 v8, v[36:39] offset:46080\n"
    "ds_write_b128 v8, v[36:39] offset:47104\n"
    "ds_write_b128 v8, v[36:39] offset:48128\n"
    "s_nop 7\n"
    "s_nop 7\n"
    "s_mov_b64 exec, s[16:17]\n"
    "v_accvgpr_write_b32 a0, v32\n"
    "v_accvgpr_write_b32 a1, v32\n"
    "v_accvgpr_write_b32 a2, v32\n"
    "v_accvgpr_write_b32 a3, v32\n"
    "v_accvgpr_write_b32 a4, v32\n"
    "v_accvgpr_write_b32 a5, v32\n"
    "v_accvgpr_write_b32 a6, v32\n"
    "v_accvgpr_write_b32 a7, v32\n"
    "v_accvgpr_write_b32 a8, v32\n"
    "v_accvgpr_write_b32 a9, v32\n"
    "v_accvgpr_write_b32 a10, v32\n"
    "v_accvgpr_write_b32 a11, v32\n"
    "v_accvgpr_write_b32 a12, v32\n"
    "v_accvgpr_write_b32 a13, v32\n"
    "v_accvgpr_write_b32 a14, v32\n"
    "v_accvgpr_write_b32 a15, v32\n"
    "v_accvgpr_write_b32 a16, v32\n"
    "v_accvgpr_write_b32 a17, v32\n"
    "v_accvgpr_write_b32 a18, v32\n"
    "v_accvgpr_write_b32 a19, v32\n"
    "v_accvgpr_write_b32 a20, v32\n"
    "v_accvgpr_write_b32 a21, v32\n"
    "v_accvgpr_write_b32 a22, v32\n"
    "v_accvgpr_write_b32 a23, v32\n"
    "v_accvgpr_write_b32 a24, v32\n"
    "v_accvgpr_write_b32 a25, v32\n"
    "v_accvgpr_write_b32 a26, v32\n"
    "v_accvgpr_write_b32 a27, v32\n"
    "v_accvgpr_write_b32 a28, v32\n"
    "v_accvgpr_write_b32 a29, v32\n"
    "v_accvgpr_write_b32 a30, v32\n"
    "v_accvgpr_write_b32 a31, v32\n"
    "v_accvgpr_write_b32 a32, v32\n"
    "v_accvgpr_write_b32 a33, v32\n"
    "v_accvgpr_write_b32 a34, v32\n"
    "v_accvgpr_write_b32 a35, v32\n"
    "v_accvgpr_write_b32 a36, v32\n"
    "v_accvgpr_write_b32 a37, v32\n"
    "v_accvgpr_write_b32 a38, v32\n"
    "v_accvgpr_write_b32 a39, v32\n"
    "v_accvgpr_write_b32 a40, v32\n"
    "v_accvgpr_write_b32 a41, v32\n"
    "v_accvgpr_write_b32 a42, v32\n"
    "v_accvgpr_write_b32 a43, v32\n"
    "v_accvgpr_write_b32 a44, v32\n"
    "v_accvgpr_write_b32 a45, v32\n"
    "v_accvgpr_write_b32 a46, v32\n"
    "v_accvgpr_write_b32 a47, v32\n"
    "v_accvgpr_write_b32 a48, v32\n"
    "v_accvgpr_write_b32 a49, v32\n"
    "v_accvgpr_write_b32 a50, v32\n"
    "v_accvgpr_write_b32 a51, v32\n"
    "v_accvgpr_write_b32 a52, v32\n"
    "v_accvgpr_write_b32 a53, v32\n"
    "v_accvgpr_write_b32 a54, v32\n"
    "v_accvgpr_write_b32 a55, v32\n"
    "v_accvgpr_write_b32 a56, v32\n"
    "v_accvgpr_write_b32 a57, v32\n"
    "v_accvgpr_write_b32 a58, v32\n"
    "v_accvgpr_write_b32 a59, v32\n"
    "v_accvgpr_write_b32 a60, v32\n"
    "v_accvgpr_write_b32 a61, v32\n"
    "v_accvgpr_write_b32 a62, v32\n"
    "v_accvgpr_write_b32 a63, v32\n"
    "v_accvgpr_write_b32 a64, v32\n"
    "v_accvgpr_write_b32 a65, v32\n"
    "v_accvgpr_write_b32 a66, v32\n"
    "v_accvgpr_write_b32 a67, v32\n"
    "v_accvgpr_write_b32 a68, v32\n"
    "v_accvgpr_write_b32 a69, v32\n"
    "v_accvgpr_write_b32 a70, v32\n"
    "v_accvgpr_write_b32 a71, v32\n"
    "v_accvgpr_write_b32 a72, v32\n"
    "v_accvgpr_write_b32 a73, v32\n"
    "v_accvgpr_write_b32 a74, v32\n"
    "v_accvgpr_write_b32 a75, v32\n"
    "v_accvgpr_write_b32 a76, v32\n"
    "v_accvgpr_write_b32 a77, v32\n"
    "v_accvgpr_write_b32 a78, v32\n"
    "v_accvgpr_write_b32 a79, v32\n"
    "s_mov_b64 exec, -1\n"
    "s_mov_b32 s30, -2\n"
    "s_mov_b32 s30, 0\n"
    "s_mov_b32 s31, 1\n"
    "s_lshl_b32 s34, s18, 11\n"
    "s_add_u32 s32, s20, s19\n"
    "s_mov_b32 s37, 0x3ea52b5c\n"
    "s_mov_b64 s[38:39], 0\n"
    "s_mov_b64 s[40:41], 0\n"
    "s_mov_b32 s36, 0\n"
    "v_mov_b32 v108, 0\n"
    "s_mov_b32 s42, 0xaaaaaaaa\n"
    "s_mov_b32 s43, 0xaaaaaaaa\n"
    "s_waitcnt lgkmcnt(0)\n"
    ".Lb1_top_%=:\n"
    "s_cmp_lg_u32 s18, 0\n"
    "s_cbranch_scc1 .Lb1_pd3_%=\n"
    "s_add_u32 s29, s26, 256\n"
    "v_min3_i32 v108, v108, v109, v110\n"
    "v_min_i32 v108, v108, v111\n"
    "v_cmp_gt_i32 vcc, s31, v108\n"
    "s_nop 0\n"
    "s_cbranch_vccz .Lb1_pd3_%=\n"
    ".Lb1_rt4_%=:\n"
    "s_memrealtime s[26:27]\n"
    "s_waitcnt lgkmcnt(0)\n"
    "s_sub_u32 s28, s36, s26\n"
    "s_cmp_lt_u32 s28, 600\n"
    "s_cbranch_scc1 .Lb1_rt4_%=\n"
    ".Lb1_po2_%=:\n"
    "buffer_load_dwordx4 v[108:111], v229, s[0:3], s21 offen sc1\n"
    "s_memrealtime s[26:27]\n"
    "s_waitcnt vmcnt(0) lgkmcnt(0)\n"
    "s_add_u32 s29, s26, 256\n"
    "v_min3_i32 v108, v108, v109, v110\n"
    "v_min_i32 v108, v108, v111\n"
    "v_cmp_gt_i32 vcc, s31, v108\n"
    "s_nop 0\n"
    "s_cbranch_vccz .Lb1_pd3_%=\n"
    "s_add_u32 s24, s24, 1\n"
    "s_cmp_lt_u32 s24, 0x40000\n"
    "s_cbranch_scc1 .Lb1_sp5_%=\n"
    "v_mov_b32 v108, 60\n"
    "s_mov_b32 s28, 33728\n"
    "buffer_store_dword v108, off, s[0:3], s28 sc1\n"
    "s_waitcnt vmcnt(0)\n"
    "s_branch .Lb1_pd3_%=\n"
    ".Lb1_sp5_%=:\n"
    "s_sleep 6\n"
    "s_branch .Lb1_po2_%=\n"
    ".Lb1_pd3_%=:\n"
    "s_barrier\n"
    "s_mov_b32 s36, s29\n"
    ".Lb1_mk0x5_%=:\n"
    "s_mov_b32 s33, s32\n"
    "s_add_u32 m0, s34, 0\n"
    "s_nop 0\n"
    "buffer_load_dwordx4 v228, s[0:3], s32 offen sc1 lds\n"
    "buffer_load_dwordx4 v228, s[0:3], s32 offen offset:1024 sc1 lds\n"
    "s_add_u32 m0, s34, 8192\n"
    "s_add_u32 s32, s32, 0x4000\n"
    "buffer_load_dwordx4 v228, s[0:3], s32 offen sc1 lds\n"
    "buffer_load_dwordx4 v228, s[0:3], s32 offen offset:1024 sc1 lds\n"
    "s_add_u32 m0, s34, 16384\n"
    "s_add_u32 s32, s32, 0x4000\n"
    "buffer_load_dwordx4 v228, s[0:3], s32 offen sc1 lds\n"
    "buffer_load_dwordx4 v228, s[0:3], s32 offen offset:1024 sc1 lds\n"
    "s_add_u32 m0, s34, 24576\n"
    "s_add_u32 s32, s32, 0x4000\n"
    "buffer_load_dwordx4 v228, s[0:3], s32 offen sc1 lds\n"
    "buffer_load_dwordx4 v228, s[0:3], s32 offen offset:1024 sc1 lds\n"
    "s_add_u32 m0, s34, 32768\n"
    "s_add_u32 s32, s32, 0x4000\n"
    "buffer_load_dwordx4 v228, s[0:3], s32 offen sc1 lds\n"
    "buffer_load_dwordx4 v228, s[0:3], s32 offen offset:1024 sc1 lds\n"
    "s_waitcnt vmcnt(8)\n"
    "s_barrier\n"
    ".Lb1_mk1x6_%=:\n"
    "ds_read_b128 v[32:35], v224 offset:0\n"
    "ds_read_b128 v[36:39], v224 offset:1024\n"
    "ds_read_b128 v[40:43], v225 offset:0\n"
    "ds_read_b128 v[44:47], v225 offset:1024\n"
    "s_waitcnt lgkmcnt(2)\n"
    "v_mfma_f32_16x16x32_bf16 v[24:27], v[32:35], v[32:35], 0\n"
    "ds_read_b128 v[48:51], v226 offset:0\n"
    "ds_read_b128 v[52:55], v226 offset:1024\n"
    "v_mfma_f32_16x16x32_bf16 v[28:31], v[36:39], v[36:39], 0\n"
    "v_mfma_f32_16x16x32_bf16 v[0:3], a[0:3], v[32:35], 0\n"
    "v_mfma_f32_16x16x32_bf16 v[4:7], a[0:3], v[36:39], 0\n"
    "v_mfma_f32_16x16x32_bf16 v[8:11], a[80:83], v[32:35], 0\n"
    "s_add_u32 s35, s30, 1\n"
    "s_lshl_b32 s32, s35, 19\n"
    "s_lshl_b32 s29, s35, 11\n"
    "v_mfma_f32_16x16x32_bf16 v[12:15], a[80:83], v[36:39], 0\n"
    "s_xor_b32 s29, s29, s19\n"
    "s_and_b32 s29, s29, 0x2000\n"
    "s_add_u32 s32, s32, s29\n"
    "v_mfma_f32_16x16x32_bf16 v[16:19], a[160:163], v[32:35], 0\n"
    "s_add_u32 s32, s32, s20\n"
    "v_xor_b32 v232, 0x800, v232\n"
    "v_xor_b32 v233, 0x800, v233\n"
    "v_mfma_f32_16x16x32_bf16 v[20:23], a[160:163], v[36:39], 0\n"
    "s_waitcnt lgkmcnt(2)\n"
    "v_xor_b32 v235, 0x800, v235\n"
    "v_xor_b32 v236, 0x800, v236\n"
    "v_mfma_f32_16x16x32_bf16 v[0:3], a[4:7], v[40:43], v[0:3]\n"
    "ds_read_b128 v[56:59], v227 offset:0\n"
    "ds_read_b128 v[60:63], v227 offset:1024\n"
    "v_mfma_f32_16x16x32_bf16 v[4:7], a[4:7], v[44:47], v[4:7]\n"
    "v_xor_b32 v237, 0x800, v237\n"
    "v_xor_b32 v238, 0x800, v238\n"
    "v_mfma_f32_16x16x32_bf16 v[8:11], a[84:87], v[40:43], v[8:11]\n"
    "v_xor_b32 v239, 0x800, v239\n"
    "v_xor_b32 v240, 0x800, v240\n"
    "v_mfma_f32_16x16x32_bf16 v[12:15], a[84:87], v[44:47], v[12:15]\n"
    "v_mov_b32 v116, s31\n"
    "v_mov_b32 v117, s31\n"
    "v_mfma_f32_16x16x32_bf16 v[16:19], a[164:167], v[40:43], v[16:19]\n"
    "v_mov_b32 v118, s31\n"
    "v_mov_b32 v119, s31\n"
    "v_mfma_f32_16x16x32_bf16 v[20:23], a[164:167], v[44:47], v[20:23]\n"
    "s_waitcnt lgkmcnt(2)\n"
    "v_mfma_f32_16x16x32_bf16 v[0:3], a[8:11], v[48:51], v[0:3]\n"
    "s_waitcnt vmcnt(6)\n"
    "s_barrier\n"
    "ds_read_b128 v[64:67], v224 offset:8192\n"
    "v_mfma_f32_16x16x32_bf16 v[4:7], a[8:11], v[52:55], v[4:7]\n"
    "ds_read_b128 v[68:71], v224 offset:9216\n"
    "v_mfma_f32_16x16x32_bf16 v[8:11], a[88:91], v[48:51], v[8:11]\n"
    "v_mfma_f32_16x16x32_bf16 v[12:15], a[88:91], v[52:55], v[12:15]\n"
    "v_mfma_f32_16x16x32_bf16 v[16:19], a[168:171], v[48:51], v[16:19]\n"
    "v_mfma_f32_16x16x32_bf16 v[20:23], a[168:171], v[52:55], v[20:23]\n"
    "s_waitcnt lgkmcnt(2)\n"
    "v_mfma_f32_16x16x32_bf16 v[0:3], a[12:15], v[56:59], v[0:3]\n"
    "ds_read_b128 v[72:75], v225 offset:8192\n"
    "ds_read_b128 v[76:79], v225 offset:9216\n"
    "v_mfma_f32_16x16x32_bf16 v[4:7], a[12:15], v[60:63], v[4:7]\n"
    "v_mfma_f32_16x16x32_bf16 v[8:11], a[92:95], v[56:59], v[8:11]\n"
    "v_mfma_f32_16x16x32_bf16 v[12:15], a[92:95], v[60:63], v[12:15]\n"
    "v_mfma_f32_16x16x32_bf16 v[16:19], a[172:175], v[56:59], v[16:19]\n"
    "v_mfma_f32_16x16x32_bf16 v[20:23], a[172:175], v[60:63], v[20:23]\n"
    "s_waitcnt lgkmcnt(2)\n"
    "v_mfma_f32_16x16x32_bf16 v[24:27], v[64:67], v[64:67], v[24:27]\n"
    "ds_read_b128 v[80:83], v226 offset:8192\n"
    "ds_read_b128 v[84:87], v226 offset:9216\n"
    "v_mfma_f32_16x16x32_bf16 v[28:31], v[68:71], v[68:71], v[28:31]\n"
    "v_mfma_f32_16x16x32_bf16 v[0:3], a[16:19], v[64:67], v[0:3]\n"
    "v_mfma_f32_16x16x32_bf16 v[4:7], a[16:19], v[68:71], v[4:7]\n"
    "v_mfma_f32_16x16x32_bf16 v[8:11], a[96:99], v[64:67], v[8:11]\n"
    "v_mfma_f32_16x16x32_bf16 v[12:15], a[96:99], v[68:71], v[12:15]\n"
    "v_mfma_f32_16x16x32_bf16 v[16:19], a[176:179], v[64:67], v[16:19]\n"
    "v_mfma_f32_16x16x32_bf16 v[20:23], a[176:179], v[68:71], v[20:23]\n"
    "s_waitcnt lgkmcnt(2)\n"
    "v_mfma_f32_16x16x32_bf16 v[0:3], a[20:23], v[72:75], v[0:3]\n"
    "ds_read_b128 v[88:91], v227 offset:8192\n"
    "ds_read_b128 v[92:95], v227 offset:9216\n"
    "v_mfma_f32_16x16x32_bf16 v[4:7], a[20:23], v[76:79], v[4:7]\n"
    "v_mfma_f32_16x16x32_bf16 v[8:11], a[100:103], v[72:75], v[8:11]\n"
    "v_mfma_f32_16x16x32_bf16 v[12:15], a[100:103], v[76:79], v[12:15]\n"
    "v_mfma_f32_16x16x32_bf16 v[16:19], a[180:183], v[72:75], v[16:19]\n"
    "v_mfma_f32_16x16x32_bf16 v[20:23], a[180:183], v[76:79], v[20:23]\n"
    "s_waitcnt lgkmcnt(2)\n"
    "v_mfma_f32_16x16x32_bf16 v[0:3], a[24:27], v[80:83], v[0:3]\n"
    "s_waitcnt vmcnt(4)\n"
    "s_barrier\n"
    "ds_read_b128 v[32:35], v224 offset:16384\n"
    "v_mfma_f32_16x16x32_bf16 v[4:7], a[24:27], v[84:87], v[4:7]\n"
    "ds_read_b128 v[36:39], v224 offset:17408\n"
    "v_mfma_f32_16x16x32_bf16 v[8:11], a[104:107], v[80:83], v[8:11]\n"
    "v_mfma_f32_16x16x32_bf16 v[12:15], a[104:107], v[84:87], v[12:15]\n"
    "v_mfma_f32_16x16x32_bf16 v[16:19], a[184:187], v[80:83], v[16:19]\n"
    "v_mfma_f32_16x16x32_bf16 v[20:23], a[184:187], v[84:87], v[20:23]\n"
    "s_waitcnt lgkmcnt(2)\n"
    "v_mfma_f32_16x16x32_bf16 v[0:3], a[28:31], v[88:91], v[0:3]\n"
    "ds_read_b128 v[40:43], v225 offset:16384\n"
    "ds_read_b128 v[44:47], v225 offset:17408\n"
    "v_mfma_f32_16x16x32_bf16 v[4:7], a[28:31], v[92:95], v[4:7]\n"
    "v_mfma_f32_16x16x32_bf16 v[8:11], a[108:111], v[88:91], v[8:11]\n"
    "v_mfma_f32_16x16x32_bf16 v[12:15], a[108:111], v[92:95], v[12:15]\n"
    "v_mfma_f32_16x16x32_bf16 v[16:19], a[188:191], v[88:91], v[16:19]\n"
    "v_mfma_f32_16x16x32_bf16 v[20:23], a[188:191], v[92:95], v[20:23]\n"
    "s_waitcnt lgkmcnt(2)\n"
    "v_mfma_f32_16x16x32_bf16 v[24:27], v[32:35], v[32:35], v[24:27]\n"
    "ds_read_b128 v[48:51], v226 offset:16384\n"
    "ds_read_b128 v[52:55], v226 offset:17408\n"
    "v_mfma_f32_16x16x32_bf16 v[28:31], v[36:39], v[36:39], v[28:31]\n"
    "v_mfma_f32_16x16x32_bf16 v[0:3], a[32:35], v[32:35], v[0:3]\n"
    "v_mfma_f32_16x16x32_bf16 v[4:7], a[32:35], v[36:39], v[4:7]\n"
    "v_mfma_f32_16x16x32_bf16 v[8:11], a[112:115], v[32:35], v[8:11]\n"
    "v_mfma_f32_16x16x32_bf16 v[12:15], a[112:115], v[36:39], v[12:15]\n"
    "v_mfma_f32_16x16x32_bf16 v[16:19], a[192:195], v[32:35], v[16:19]\n"
    "v_mfma_f32_16x16x32_bf16 v[20:23], a[192:195], v[36:39], v[20:23]\n"
    "s_waitcnt lgkmcnt(2)\n"
    "v_mfma_f32_16x16x32_bf16 v[0:3], a[36:39], v[40:43], v[0:3]\n"
    "ds_read_b128 v[56:59], v227 offset:16384\n"
    "ds_read_b128 v[60:63], v227 offset:17408\n"
    "v_mfma_f32_16x16x32_bf16 v[4:7], a[36:39], v[44:47], v[4:7]\n"
    "v_mfma_f32_16x16x32_bf16 v[8:11], a[116:119], v[40:43], v[8:11]\n"
    "v_mfma_f32_16x16x32_bf16 v[12:15], a[116:119], v[44:47], v[12:15]\n"
    "v_mfma_f32_16x16x32_bf16 v[16:19], a[196:199], v[40:43], v[16:19]\n"
    "v_mfma_f32_16x16x32_bf16 v[20:23], a[196:199], v[44:47], v[20:23]\n"
    "s_waitcnt lgkmcnt(2)\n"
    "v_mfma_f32_16x16x32_bf16 v[0:3], a[40:43], v[48:51], v[0:3]\n"
    "s_waitcnt vmcnt(2)\n"
    "s_barrier\n"
    "ds_read_b128 v[64:67], v224 offset:24576\n"
    "v_mfma_f32_16x16x32_bf16 v[4:7], a[40:43], v[52:55], v[4:7]\n"
    "ds_read_b128 v[68:71], v224 offset:25600\n"
    "ds_read_b128 v[72:75], v225 offset:24576\n"
    "v_mfma_f32_16x16x32_bf16 v[8:11], a[120:123], v[48:51], v[8:11]\n"
    "v_mfma_f32_16x16x32_bf16 v[12:15], a[120:123], v[52:55], v[12:15]\n"
    "ds_read_b128 v[76:79], v225 offset:25600\n"
    "v_mfma_f32_16x16x32_bf16 v[16:19], a[200:203], v[48:51], v[16:19]\n"
    "ds_read_b128 v[80:83], v226 offset:24576\n"
    "ds_read_b128 v[84:87], v226 offset:25600\n"
    "v_mfma_f32_16x16x32_bf16 v[20:23], a[200:203], v[52:55], v[20:23]\n"
    "s_waitcnt lgkmcnt(6)\n"
    "v_mfma_f32_16x16x32_bf16 v[0:3], a[44:47], v[56:59], v[0:3]\n"
    "ds_read_b128 v[88:91], v227 offset:24576\n"
    "v_mfma_f32_16x16x32_bf16 v[4:7], a[44:47], v[60:63], v[4:7]\n"
    "ds_read_b128 v[92:95], v227 offset:25600\n"
    "s_waitcnt vmcnt(0)\n"
    "v_mfma_f32_16x16x32_bf16 v[8:11], a[124:127], v[56:59], v[8:11]\n"
    "s_barrier\n"
    "ds_read_b128 v[32:35], v224 offset:32768\n"
    "v_mfma_f32_16x16x32_bf16 v[12:15], a[124:127], v[60:63], v[12:15]\n"
    "ds_read_b128 v[36:39], v224 offset:33792\n"
    "v_mfma_f32_16x16x32_bf16 v[16:19], a[204:207], v[56:59], v[16:19]\n"
    "ds_read_b128 v[40:43], v225 offset:32768\n"
    "v_mfma_f32_16x16x32_bf16 v[20:23], a[204:207], v[60:63], v[20:23]\n"
    "s_waitcnt lgkmcnt(9)\n"
    "ds_read_b128 v[44:47], v225 offset:33792\n"
    "v_mfma_f32_16x16x32_bf16 v[24:27], v[64:67], v[64:67], v[24:27]\n"
    "ds_read_b128 v[48:51], v226 offset:32768\n"
    "v_mfma_f32_16x16x32_bf16 v[28:31], v[68:71], v[68:71], v[28:31]\n"
    "ds_read_b128 v[52:55], v226 offset:33792\n"
    "v_mfma_f32_16x16x32_bf16 v[0:3], a[48:51], v[64:67], v[0:3]\n"
    "ds_read_b128 v[56:59], v227 offset:32768\n"
    "v_mfma_f32_16x16x32_bf16 v[4:7], a[48:51], v[68:71], v[4:7]\n"
    "s_waitcnt lgkmcnt(11)\n"
    "ds_read_b128 v[60:63], v227 offset:33792\n"
    "v_mfma_f32_16x16x32_bf16 v[0:3], a[52:55], v[72:75], v[0:3]\n"
    "v_mfma_f32_16x16x32_bf16 v[4:7], a[52:55], v[76:79], v[4:7]\n"
    "s_waitcnt lgkmcnt(10)\n"
    "v_mfma_f32_16x16x32_bf16 v[0:3], a[56:59], v[80:83], v[0:3]\n"
    "v_mfma_f32_16x16x32_bf16 v[4:7], a[56:59], v[84:87], v[4:7]\n"
    "s_waitcnt lgkmcnt(8)\n"
    "v_mfma_f32_16x16x32_bf16 v[0:3], a[60:63], v[88:91], v[0:3]\n"
    "v_mfma_f32_16x16x32_bf16 v[4:7], a[60:63], v[92:95], v[4:7]\n"
    "s_waitcnt lgkmcnt(6)\n"
    "v_mfma_f32_16x16x32_bf16 v[24:27], v[32:35], v[32:35], v[24:27]\n"
    "v_mfma_f32_16x16x32_bf16 v[28:31], v[36:39], v[36:39], v[28:31]\n"
    "v_mfma_f32_16x16x32_bf16 v[0:3], a[64:67], v[32:35], v[0:3]\n"
    "v_mfma_f32_16x16x32_bf16 v[4:7], a[64:67], v[36:39], v[4:7]\n"
    "s_waitcnt lgkmcnt(4)\n"
    "v_mfma_f32_16x16x32_bf16 v[0:3], a[68:71], v[40:43], v[0:3]\n"
    "v_mfma_f32_16x16x32_bf16 v[4:7], a[68:71], v[44:47], v[4:7]\n"
    "s_waitcnt lgkmcnt(2)\n"
    "v_mfma_f32_16x16x32_bf16 v[0:3], a[72:75], v[48:51], v[0:3]\n"
    "v_mfma_f32_16x16x32_bf16 v[4:7], a[72:75], v[52:55], v[4:7]\n"
    "s_waitcnt lgkmcnt(0)\n"
    "v_mfma_f32_16x16x32_bf16 v[0:3], a[76:79], v[56:59], v[0:3]\n"
    "v_mfma_f32_16x16x32_bf16 v[4:7], a[76:79], v[60:63], v[4:7]\n"
    "ds_write_b128 v241, v[24:27]\n"
    "v_mfma_f32_16x16x32_bf16 v[8:11], a[128:131], v[64:67], v[8:11]\n"
    "ds_write_b128 v241, v[28:31] offset:1024\n"
    "v_mfma_f32_16x16x32_bf16 v[12:15], a[128:131], v[68:71], v[12:15]\n"
    "ds_write2st64_b32 v243, v0, v4 offset1:1\n"
    "v_mfma_f32_16x16x32_bf16 v[8:11], a[132:135], v[72:75], v[8:11]\n"
    "s_waitcnt lgkmcnt(0)\n"
    "s_barrier\n"
    "ds_read2st64_b32 v[24:25], v242 offset1:8\n"
    "v_mfma_f32_16x16x32_bf16 v[12:15], a[132:135], v[76:79], v[12:15]\n"
    "ds_read2st64_b32 v[26:27], v242 offset0:16 offset1:24\n"
    "ds_read_b32 v108, v244\n"
    "v_mfma_f32_16x16x32_bf16 v[8:11], a[136:139], v[80:83], v[8:11]\n"
    "v_mfma_f32_16x16x32_bf16 v[12:15], a[136:139], v[84:87], v[12:15]\n"
    "v_mfma_f32_16x16x32_bf16 v[8:11], a[140:143], v[88:91], v[8:11]\n"
    "v_mfma_f32_16x16x32_bf16 v[12:15], a[140:143], v[92:95], v[12:15]\n"
    "v_mfma_f32_16x16x32_bf16 v[8:11], a[144:147], v[32:35], v[8:11]\n"
    "s_waitcnt lgkmcnt(0)\n"
    "v_add_f32 v24, v24, v26\n"
    "v_add_f32 v25, v25, v27\n"
    "v_mfma_f32_16x16x32_bf16 v[12:15], a[144:147], v[36:39], v[12:15]\n"
    "v_add_f32 v112, v24, v25\n"
    "v_mul_f32 v108, 0x3acccccd, v108\n"
    "v_mfma_f32_16x16x32_bf16 v[8:11], a[148:151], v[40:43], v[8:11]\n"
    "v_mul_f32 v112, 0x3acccccd, v112\n"
    "v_fma_f32 v112, -v108, v108, v112\n"
    "v_mfma_f32_16x16x32_bf16 v[12:15], a[148:151], v[44:47], v[12:15]\n"
    "v_max_f32 v112, 0, v112\n"
    "v_cvt_pk_bf16_f32 v108, v108, v112\n"
    "v_mfma_f32_16x16x32_bf16 v[8:11], a[152:155], v[48:51], v[8:11]\n"
    "v_and_b32 v112, 0xffff0000, v108\n"
    "v_lshlrev_b32 v108, 16, v108\n"
    "v_mfma_f32_16x16x32_bf16 v[12:15], a[152:155], v[52:55], v[12:15]\n"
    "v_add_f32 v112, 0x3727c5ac, v112\n"
    "v_mov_b32 v110, v108\n"
    "v_mfma_f32_16x16x32_bf16 v[8:11], a[156:159], v[56:59], v[8:11]\n"
    "v_cvt_pk_bf16_f32 v112, v112, v112\n"
    "v_lshlrev_b32 v112, 16, v112\n"
    "v_mfma_f32_16x16x32_bf16 v[12:15], a[156:159], v[60:63], v[12:15]\n"
    "v_sqrt_f32 v112, v112\n"
    "v_mfma_f32_16x16x32_bf16 v[16:19], a[208:211], v[64:67], v[16:19]\n"
    "v_permlane32_swap_b32 v108, v110\n"
    "v_mfma_f32_16x16x32_bf16 v[20:23], a[208:211], v[68:71], v[20:23]\n"
    "v_cvt_pk_bf16_f32 v112, v112, v112\n"
    "v_lshlrev_b32 v112, 16, v112\n"
    "v_mfma_f32_16x16x32_bf16 v[16:19], a[212:215], v[72:75], v[16:19]\n"
    "v_rcp_f32 v112, v112\n"
    "v_mfma_f32_16x16x32_bf16 v[20:23], a[212:215], v[76:79], v[20:23]\n"
    "v_mov_b32 v114, v112\n"
    "v_mfma_f32_16x16x32_bf16 v[16:19], a[216:219], v[80:83], v[16:19]\n"
    "s_nop 0\n"
    "v_permlane32_swap_b32 v112, v114\n"
    "v_mfma_f32_16x16x32_bf16 v[20:23], a[216:219], v[84:87], v[20:23]\n"
    "v_fma_f32 v0, v96, -v108, v0\n"
    "v_fma_f32 v1, v97, -v108, v1\n"
    "v_mfma_f32_16x16x32_bf16 v[16:19], a[220:223], v[88:91], v[16:19]\n"
    "v_fma_f32 v2, v98, -v108, v2\n"
    "v_fma_f32 v3, v99, -v108, v3\n"
    "v_mfma_f32_16x16x32_bf16 v[20:23], a[220:223], v[92:95], v[20:23]\n"
    "v_fma_f32 v4, v96, -v110, v4\n"
    "v_fma_f32 v5, v97, -v110, v5\n"
    "v_mfma_f32_16x16x32_bf16 v[16:19], a[224:227], v[32:35], v[16:19]\n"
    "v_fma_f32 v6, v98, -v110, v6\n"
    "v_fma_f32 v7, v99, -v110, v7\n"
    "v_mfma_f32_16x16x32_bf16 v[20:23], a[224:227], v[36:39], v[20:23]\n"
    "v_mul_f32 v0, v0, v112\n"
    "v_mul_f32 v1, v1, v112\n"
    "v_mfma_f32_16x16x32_bf16 v[16:19], a[228:231], v[40:43], v[16:19]\n"
    "v_mul_f32 v2, v2, v112\n"
    "v_mul_f32 v3, v3, v112\n"
    "v_mfma_f32_16x16x32_bf16 v[20:23], a[228:231], v[44:47], v[20:23]\n"
    "v_mul_f32 v4, v4, v114\n"
    "v_mul_f32 v5, v5, v114\n"
    "v_mfma_f32_16x16x32_bf16 v[16:19], a[232:235], v[48:51], v[16:19]\n"
    "v_mul_f32 v6, v6, v114\n"
    "v_mul_f32 v7, v7, v114\n"
    "v_mfma_f32_16x16x32_bf16 v[20:23], a[232:235], v[52:55], v[20:23]\n"
    "v_cvt_pk_bf16_f32 v0, v0, v1\n"
    "v_cvt_pk_bf16_f32 v1, v2, v3\n"
    "v_mfma_f32_16x16x32_bf16 v[16:19], a[236:239], v[56:59], v[16:19]\n"
    "ds_write_b64 v231, v[0:1] offset:0\n"
    "v_mfma_f32_16x16x32_bf16 v[20:23], a[236:239], v[60:63], v[20:23]\n"
    "v_cvt_pk_bf16_f32 v4, v4, v5\n"
    "v_cvt_pk_bf16_f32 v5, v6, v7\n"
    ".Lb1_mk2x7_%=:\n"
    "ds_write_b64 v231, v[4:5] offset:1024\n"
    "v_pk_fma_f32 v[8:9], v[100:101], v[108:109], v[8:9] op_sel_hi:[1,0,1] neg_lo:[0,1,0] neg_hi:[0,1,0]\n"
    "v_pk_fma_f32 v[10:11], v[102:103], v[108:109], v[10:11] op_sel_hi:[1,0,1] neg_lo:[0,1,0] neg_hi:[0,1,0]\n"
    "v_pk_fma_f32 v[12:13], v[100:101], v[110:111], v[12:13] op_sel_hi:[1,0,1] neg_lo:[0,1,0] neg_hi:[0,1,0]\n"
    "v_pk_fma_f32 v[14:15], v[102:103], v[110:111], v[14:15] op_sel_hi:[1,0,1] neg_lo:[0,1,0] neg_hi:[0,1,0]\n"
    "v_pk_mul_f32 v[8:9], v[8:9], v[112:113] op_sel_hi:[1,0]\n"
    "v_pk_mul_f32 v[10:11], v[10:11], v[112:113] op_sel_hi:[1,0]\n"
    "v_pk_mul_f32 v[12:13], v[12:13], v[114:115] op_sel_hi:[1,0]\n"
    "v_pk_mul_f32 v[14:15], v[14:15], v[114:115] op_sel_hi:[1,0]\n"
    "v_cvt_pk_bf16_f32 v8, v8, v9\n"
    "v_cvt_pk_bf16_f32 v9, v10, v11\n"
    "ds_write_b64 v232, v[8:9] offset:0\n"
    "v_cvt_pk_bf16_f32 v12, v12, v13\n"
    "v_cvt_pk_bf16_f32 v13, v14, v15\n"
    "ds_write_b64 v232, v[12:13] offset:1024\n"
    "v_pk_fma_f32 v[16:17], v[104:105], v[108:109], v[16:17] op_sel_hi:[1,0,1] neg_lo:[0,1,0] neg_hi:[0,1,0]\n"
    "v_pk_fma_f32 v[18:19], v[106:107], v[108:109], v[18:19] op_sel_hi:[1,0,1] neg_lo:[0,1,0] neg_hi:[0,1,0]\n"
    "v_pk_fma_f32 v[20:21], v[104:105], v[110:111], v[20:21] op_sel_hi:[1,0,1] neg_lo:[0,1,0] neg_hi:[0,1,0]\n"
    "v_pk_fma_f32 v[22:23], v[106:107], v[110:111], v[22:23] op_sel_hi:[1,0,1] neg_lo:[0,1,0] neg_hi:[0,1,0]\n"
    "v_pk_mul_f32 v[16:17], v[16:17], v[112:113] op_sel_hi:[1,0]\n"
    "v_pk_mul_f32 v[18:19], v[18:19], v[112:113] op_sel_hi:[1,0]\n"
    "v_pk_mul_f32 v[20:21], v[20:21], v[114:115] op_sel_hi:[1,0]\n"
    "v_pk_mul_f32 v[22:23], v[22:23], v[114:115] op_sel_hi:[1,0]\n"
    "v_cvt_pk_bf16_f32 v16, v16, v17\n"
    "v_cvt_pk_bf16_f32 v17, v18, v19\n"
    "ds_write_b64 v233, v[16:17] offset:0\n"
    "v_cvt_pk_bf16_f32 v20, v20, v21\n"
    "v_cvt_pk_bf16_f32 v21, v22, v23\n"
    "ds_write_b64 v233, v[20:21] offset:1024\n"
    "s_waitcnt lgkmcnt(0)\n"
    "s_barrier\n"
    ".Lb1_mk3x8_%=:\n"
    "ds_read_b128 v[32:35], v236 offset:1024\n"
    "ds_read_b128 v[44:47], v234 offset:0\n"
    "ds_read_b128 v[36:39], v235 offset:0\n"
    "ds_read_b128 v[48:51], v234 offset:1024\n"
    "ds_read_b128 v[40:43], v235 offset:1024\n"
    "ds_read_b64_tr_b16 v[52:53], v238 offset:1024\n"
    "ds_read_b64_tr_b16 v[54:55], v237 offset:0\n"
    "ds_read_b64_tr_b16 v[56:57], v237 offset:1024\n"
    "ds_read_b64_tr_b16 v[58:59], v240 offset:1024\n"
    "ds_read_b64_tr_b16 v[60:61], v239 offset:0\n"
    "ds_read_b64_tr_b16 v[62:63], v239 offset:1024\n"
    "s_waitcnt lgkmcnt(9)\n"
    "v_mfma_f32_16x16x32_bf16 v[64:67], v[32:35], v[44:47], 0\n"
    "s_waitcnt lgkmcnt(8)\n"
    "v_mfma_f32_16x16x32_bf16 v[68:71], v[36:39], v[44:47], 0\n"
    "s_waitcnt lgkmcnt(7)\n"
    "v_mfma_f32_16x16x32_bf16 v[72:75], v[36:39], v[48:51], 0\n"
    "s_waitcnt lgkmcnt(6)\n"
    "v_mfma_f32_16x16x32_bf16 v[76:79], v[40:43], v[48:51], 0\n"
    "v_cndmask_b32 v64, v64, v65, s[42:43]\n"
    "v_cndmask_b32 v65, v66, v67, s[42:43]\n"
    "v_cndmask_b32 v66, v68, v69, s[42:43]\n"
    "v_cndmask_b32 v67, v70, v71, s[42:43]\n"
    "s_nop 0\n"
    "v_cndmask_b32 v68, v72, v73, s[42:43]\n"
    "v_cndmask_b32 v69, v74, v75, s[42:43]\n"
    "s_nop 0\n"
    "v_cndmask_b32 v70, v76, v77, s[42:43]\n"
    "v_cndmask_b32 v71, v78, v79, s[42:43]\n"
    "v_cndmask_b32 v64, v249, v64, s[38:39]\n"
    "v_cndmask_b32 v65, v249, v65, s[40:41]\n"
    "v_cndmask_b32 v66, v249, v66, s[12:13]\n"
    "v_cndmask_b32 v67, v249, v67, s[14:15]\n"
    "v_cndmask_b32 v68, v249, v68, s[8:9]\n"
    "v_cndmask_b32 v69, v249, v69, s[10:11]\n"
    "v_cndmask_b32 v70, v249, v70, s[12:13]\n"
    "v_cndmask_b32 v71, v249, v71, s[14:15]\n"
    "v_max3_f32 v72, v64, v65, v66\n"
    "v_max3_f32 v74, v68, v69, v70\n"
    "v_max_f32 v72, v72, v67\n"
    "v_max_f32 v74, v74, v71\n"
    "s_nop 1\n"
    "v_permlane16_swap_b32 v72, v74\n"
    "v_max_f32 v72, v72, v74\n"
    "v_mov_b32 v74, v72\n"
    "s_nop 1\n"
    "v_permlane32_swap_b32 v72, v74\n"
    "v_max_f32 v72, v72, v74\n"
    "v_mul_f32 v72, s37, v72\n"
    "v_mov_b32 v74, v72\n"
    "s_nop 1\n"
    "v_permlane16_swap_b32 v72, v74\n"
    "v_fma_f32 v64, v64, s37, -v72\n"
    "v_fma_f32 v65, v65, s37, -v72\n"
    "v_fma_f32 v66, v66, s37, -v72\n"
    "v_fma_f32 v67, v67, s37, -v72\n"
    "v_fma_f32 v68, v68, s37, -v74\n"
    "v_fma_f32 v69, v69, s37, -v74\n"
    "v_fma_f32 v70, v70, s37, -v74\n"
    "v_fma_f32 v71, v71, s37, -v74\n"
    "v_exp_f32 v64, v64\n"
    "v_exp_f32 v65, v65\n"
    "v_exp_f32 v66, v66\n"
    "v_exp_f32 v67, v67\n"
    "v_exp_f32 v68, v68\n"
    "v_exp_f32 v69, v69\n"
    "v_exp_f32 v70, v70\n"
    "v_exp_f32 v71, v71\n"
    "v_pk_add_f32 v[72:73], v[64:65], v[66:67]\n"
    "v_pk_add_f32 v[74:75], v[68:69], v[70:71]\n"
    "v_add_f32 v72, v72, v73\n"
    "v_add_f32 v74, v74, v75\n"
    "s_nop 1\n"
    "v_permlane16_swap_b32 v72, v74\n"
    "v_add_f32 v72, v72, v74\n"
    "v_mov_b32 v74, v72\n"
    "s_nop 1\n"
    "v_permlane32_swap_b32 v72, v74\n"
    "v_add_f32 v72, v72, v74\n"
    "v_rcp_f32 v72, v72\n"
    "s_nop 0\n"
    "v_mov_b32 v74, v72\n"
    "s_nop 1\n"
    "v_permlane16_swap_b32 v72, v74\n"
    "v_pk_mul_f32 v[64:65], v[64:65], v[72:73] op_sel_hi:[1,0]\n"
    "v_pk_mul_f32 v[66:67], v[66:67], v[72:73] op_sel_hi:[1,0]\n"
    "v_pk_mul_f32 v[68:69], v[68:69], v[74:75] op_sel_hi:[1,0]\n"
    "v_pk_mul_f32 v[70:71], v[70:71], v[74:75] op_sel_hi:[1,0]\n"
    "v_cvt_pk_bf16_f32 v64, v64, v65\n"
    "v_cvt_pk_bf16_f32 v66, v66, v67\n"
    "v_cvt_pk_bf16_f32 v68, v68, v69\n"
    "v_cvt_pk_bf16_f32 v70, v70, v71\n"
    "v_perm_b32 v80, v64, v64, v247\n"
    "v_perm_b32 v81, v64, v64, v248\n"
    "v_perm_b32 v82, v66, v66, v247\n"
    "v_perm_b32 v83, v66, v66, v248\n"
    "v_perm_b32 v84, v68, v68, v247\n"
    "v_perm_b32 v85, v68, v68, v248\n"
    "v_perm_b32 v86, v70, v70, v247\n"
    "v_perm_b32 v87, v70, v70, v248\n"
    "s_waitcnt lgkmcnt(0)\n"
    "v_mfma_f32_16x16x32_bf16 v[0:3], v[52:55], v[80:83], 0\n"
    "v_mfma_f32_16x16x32_bf16 v[4:7], v[54:57], v[84:87], 0\n"
    "v_mfma_f32_16x16x32_bf16 v[8:11], v[58:61], v[80:83], 0\n"
    "v_mfma_f32_16x16x32_bf16 v[12:15], v[60:63], v[84:87], 0\n"
    "s_mov_b64 s[38:39], s[8:9]\n"
    "s_mov_b64 s[40:41], s[10:11]\n"
    "v_cvt_pk_bf16_f32 v16, v0, v1\n"
    "v_cvt_pk_bf16_f32 v17, v2, v3\n"
    "v_cvt_pk_bf16_f32 v18, v4, v5\n"
    "v_cvt_pk_bf16_f32 v19, v6, v7\n"
    "s_nop 0\n"
    "v_permlane16_swap_b32 v16, v18\n"
    "v_permlane16_swap_b32 v17, v19\n"
    "buffer_store_dwordx4 v[16:19], v245, s[0:3], s33 offen sc1\n"
    "v_cvt_pk_bf16_f32 v20, v8, v9\n"
    "v_cvt_pk_bf16_f32 v21, v10, v11\n"
    "v_cvt_pk_bf16_f32 v22, v12, v13\n"
    "v_cvt_pk_bf16_f32 v23, v14, v15\n"
    "buffer_store_dwordx4 v[20:23], v246, s[0:3], s33 offen sc1\n"
    ".Lb1_mk4x9_%=:\n"
    "v_mov_b32 v108, 0\n"
    "s_memrealtime s[26:27]\n"
    "s_cmp_lg_u32 s18, 0\n"
    "s_waitcnt lgkmcnt(0)\n"
    "s_cbranch_scc1 .Lb1_pl11_%=\n"
    "s_sub_u32 s28, s36, s26\n"
    "s_cmp_gt_i32 s28, 40\n"
    "s_cbranch_scc1 .Lb1_pl11_%=\n"
    "buffer_load_dwordx4 v[108:111], v229, s[0:3], s21 offen sc1\n"
    ".Lb1_pl11_%=:\n"
    "s_waitcnt vmcnt(0)\n"
    "s_barrier\n"
    "buffer_store_dwordx4 v[116:119], v230, s[0:3], 0 offen sc1\n"
    ".Lb1_mk5x11_%=:\n"
    "s_mov_b32 s30, s31\n"
    "s_add_u32 s31, s31, 1\n"
    "s_cmp_lt_u32 s30, 64\n"
    "s_cbranch_scc1 .Lb1_top_%=\n"
    "s_waitcnt vmcnt(0) lgkmcnt(0)\n"
    "s_endpgm\n"
    :: "{s[0:3]}"(s0_), "{s[4:7]}"(s4_), "{s[8:11]}"(s8_), "{s[12:15]}"(s12_), "{s[16:19]}"(s16_), "{s[20:23]}"(s20_), "{v[0:3]}"(v0_), "{v[224:227]}"(v224_), "{v[228:231]}"(v228_), "{v[232:235]}"(v232_), "{v[236:239]}"(v236_), "{v[240:243]}"(v240_), "{v[244:247]}"(v244_), "{v[248:251]}"(v248_) : "memory", "v255", "a255", "s95");
    __builtin_unreachable();
}
DEV void b2_role(const MegaArgs& A, const int L, const int c, const int rep) {
    ROLE_HEAD;
    const int b = 4 * c + w;
    const u32x4 ro = L ? u32x4{lo(A.out), hi(A.out), 2048u * 1280u, 0x20000u} : rx;
    if (L == 0) {
#pragma unroll
        for (int k = 0; k < 16; ++k) bst16(rr, ST0 + b * 32768 + (k >> 3) * 16384 + (k & 7) * 1024 + l * 16, ones);
        DRAIN_BARRIER();
        bst4(rr, threadIdx.x == 0 ? xl(Z_ST + c * 128, 0) : OOB, 1u);
    }
    const u32x4 s0_ = {RFL(rx[0]), RFL(rx[1]), RFL(rx[2]), RFL(rx[3])};
    const u32x4 s4_ = {RFL(lo(A.wo + 32 * b)), RFL(hi(A.wo + 32 * b)), RFL((640 * 640 - 32 * b) * 2), RFL(0x20000u)};
    const u32x4 s8_ = {RFL(ro[0]), RFL(ro[1]), RFL(ro[2]), RFL(ro[3])};
    const u32x4 s12_ = {RFL(w), RFL(L << 13), RFL(REC0 + RC_O + w * 2048), RFL(xl(Z_O + rep * 2048, L))};
    const u32x4 s16_ = {RFL(xl(Z_ST, 0)), RFL(ST0), RFL(DBG0 + ((L * 128 + 107 + c) * 4 + w) * 2048), RFL(0u)};
    const u32x4 v0_ = {(unsigned)((0 + (l >> 2)) * 1280 + (l & 3) * 16 - 0), (unsigned)((16 + (l >> 2)) * 1280 + (l & 3) * 16 - 1024), (unsigned)((32 + (l >> 2)) * 1280 + (l & 3) * 16 - 2048), (unsigned)((48 + (l >> 2)) * 1280 + (l & 3) * 16 - 3072)};
    const u32x4 v4_ = {(unsigned)(w * 40960 + (8 * (lq & 1) + 20 * (lq >> 1) + (l16 >> 2)) * 64 + (l & 3) * 16), (unsigned)(w * 40960 + (20 * lq + 16 + (l16 >> 2)) * 64 + (l & 3) * 16), (unsigned)((l < 5 ? l : 0) * 128), (unsigned)(0u)};
    const u32x4 v224_ = {(unsigned)(l * 16), (unsigned)((l < 11 ? l : 0) * 128), (unsigned)(l < 4 && w == 1 ? xl(Z_XN + l * 2048 + c * 128, 0) : OOB), (unsigned)(c * 16384 + l * 16)};
    const u32x4 v228_ = {(unsigned)(L ? (l16 + 0) * 1280 + b * 64 + lq * 16 : RC_X2 + c * 16384 + 0 + l * 16), (unsigned)(L ? (l16 + 16) * 1280 + b * 64 + lq * 16 : RC_X2 + c * 16384 + 1024 + l * 16), (unsigned)(l < 16 ? b * 32768 + l * 16 : OOB), (unsigned)((0 + lq) * 32768 + l16 * 16)};
    const u32x4 v232_ = {(unsigned)((4 + lq) * 32768 + l16 * 16), (unsigned)((8 + lq) * 32768 + l16 * 16), (unsigned)((12 + lq) * 32768 + l16 * 16), (unsigned)((16 + lq) * 32768 + l16 * 16)};
    asm volatile(
    "s_mov_b32 s20, 0\n"
    "s_sleep 35\n"
    "s_sleep 35\n"
    "s_sleep 35\n"
    "s_sleep 35\n"
    "s_sleep 35\n"
    "s_sleep 35\n"
    "s_sleep 35\n"
    "s_sleep 35\n"
    "s_sleep 35\n"
    "s_sleep 35\n"
    "s_sleep 35\n"
    "s_sleep 35\n"
    "s_sleep 35\n"
    "s_sleep 35\n"
    ".Lb2_ho1_%=:\n"
    "s_mul_i32 s28, s12, 40960\n"
    "s_mov_b32 s34, 0\n"
    "s_add_u32 m0, s28, 0\n"
    "s_nop 0\n"
    "buffer_load_dwordx4 v0, s[4:7], s34 offen offset:0 lds\n"
    "buffer_load_dwordx4 v1, s[4:7], s34 offen offset:1024 lds\n"
    "buffer_load_dwordx4 v2, s[4:7], s34 offen offset:2048 lds\n"
    "buffer_load_dwordx4 v3, s[4:7], s34 offen offset:3072 lds\n"
    "s_mov_b32 s34, 81920\n"
    "s_add_u32 m0, s28, 4096\n"
    "s_nop 0\n"
    "buffer_load_dwordx4 v0, s[4:7], s34 offen offset:0 lds\n"
    "buffer_load_dwordx4 v1, s[4:7], s34 offen offset:1024 lds\n"
    "buffer_load_dwordx4 v2, s[4:7], s34 offen offset:2048 lds\n"
    "buffer_load_dwordx4 v3, s[4:7], s34 offen offset:3072 lds\n"
    "s_mov_b32 s34, 163840\n"
    "s_add_u32 m0, s28, 8192\n"
    "s_nop 0\n"
    "buffer_load_dwordx4 v0, s[4:7], s34 offen offset:0 lds\n"
    "buffer_load_dwordx4 v1, s[4:7], s34 offen offset:1024 lds\n"
    "buffer_load_dwordx4 v2, s[4:7], s34 offen offset:2048 lds\n"
    "buffer_load_dwordx4 v3, s[4:7], s34 offen offset:3072 lds\n"
    "s_mov_b32 s34, 245760\n"
    "s_add_u32 m0, s28, 12288\n"
    "s_nop 0\n"
    "buffer_load_dwordx4 v0, s[4:7], s34 offen offset:0 lds\n"
    "buffer_load_dwordx4 v1, s[4:7], s34 offen offset:1024 lds\n"
    "buffer_load_dwordx4 v2, s[4:7], s34 offen offset:2048 lds\n"
    "buffer_load_dwordx4 v3, s[4:7], s34 offen offset:3072 lds\n"
    "s_mov_b32 s34, 327680\n"
    "s_add_u32 m0, s28, 16384\n"
    "s_nop 0\n"
    "buffer_load_dwordx4 v0, s[4:7], s34 offen offset:0 lds\n"
    "buffer_load_dwordx4 v1, s[4:7], s34 offen offset:1024 lds\n"
    "buffer_load_dwordx4 v2, s[4:7], s34 offen offset:2048 lds\n"
    "buffer_load_dwordx4 v3, s[4:7], s34 offen offset:3072 lds\n"
    "s_mov_b32 s34, 409600\n"
    "s_add_u32 m0, s28, 20480\n"
    "s_nop 0\n"
    "buffer_load_dwordx4 v0, s[4:7], s34 offen offset:0 lds\n"
    "buffer_load_dwordx4 v1, s[4:7], s34 offen offset:1024 lds\n"
    "buffer_load_dwordx4 v2, s[4:7], s34 offen offset:2048 lds\n"
    "buffer_load_dwordx4 v3, s[4:7], s34 offen offset:3072 lds\n"
    "s_mov_b32 s34, 491520\n"
    "s_add_u32 m0, s28, 24576\n"
    "s_nop 0\n"
    "buffer_load_dwordx4 v0, s[4:7], s34 offen offset:0 lds\n"
    "buffer_load_dwordx4 v1, s[4:7], s34 offen offset:1024 lds\n"
    "buffer_load_dwordx4 v2, s[4:7], s34 offen offset:2048 lds\n"
    "buffer_load_dwordx4 v3, s[4:7], s34 offen offset:3072 lds\n"
    "s_mov_b32 s34, 573440\n"
    "s_add_u32 m0, s28, 28672\n"
    "s_nop 0\n"
    "buffer_load_dwordx4 v0, s[4:7], s34 offen offset:0 lds\n"
    "buffer_load_dwordx4 v1, s[4:7], s34 offen offset:1024 lds\n"
    "buffer_load_dwordx4 v2, s[4:7], s34 offen offset:2048 lds\n"
    "buffer_load_dwordx4 v3, s[4:7], s34 offen offset:3072 lds\n"
    "s_mov_b32 s34, 655360\n"
    "s_add_u32 m0, s28, 32768\n"
    "s_nop 0\n"
    "buffer_load_dwordx4 v0, s[4:7], s34 offen offset:0 lds\n"
    "buffer_load_dwordx4 v1, s[4:7], s34 offen offset:1024 lds\n"
    "buffer_load_dwordx4 v2, s[4:7], s34 offen offset:2048 lds\n"
    "buffer_load_dwordx4 v3, s[4:7], s34 offen offset:3072 lds\n"
    "s_mov_b32 s34, 737280\n"
    "s_add_u32 m0, s28, 36864\n"
    "s_nop 0\n"
    "buffer_load_dwordx4 v0, s[4:7], s34 offen offset:0 lds\n"
    "buffer_load_dwordx4 v1, s[4:7], s34 offen offset:1024 lds\n"
    "buffer_load_dwordx4 v2, s[4:7], s34 offen offset:2048 lds\n"
    "buffer_load_dwordx4 v3, s[4:7], s34 offen offset:3072 lds\n"
    "s_waitcnt vmcnt(0)\n"
    "ds_read_b64_tr_b16 a[0:1], v4 offset:0\n"
    "ds_read_b64_tr_b16 a[2:3], v4 offset:256\n"
    "ds_read_b64_tr_b16 a[80:81], v4 offset:8\n"
    "ds_read_b64_tr_b16 a[82:83], v4 offset:264\n"
    "s_waitcnt lgkmcnt(0)\n"
    "ds_read_b64_tr_b16 a[4:5], v4 offset:2560\n"
    "ds_read_b64_tr_b16 a[6:7], v4 offset:2816\n"
    "ds_read_b64_tr_b16 a[84:85], v4 offset:2568\n"
    "ds_read_b64_tr_b16 a[86:87], v4 offset:2824\n"
    "s_waitcnt lgkmcnt(0)\n"
    "ds_read_b64_tr_b16 a[8:9], v4 offset:5120\n"
    "ds_read_b64_tr_b16 a[10:11], v4 offset:5376\n"
    "ds_read_b64_tr_b16 a[88:89], v4 offset:5128\n"
    "ds_read_b64_tr_b16 a[90:91], v4 offset:5384\n"
    "s_waitcnt lgkmcnt(0)\n"
    "ds_read_b64_tr_b16 a[12:13], v4 offset:7680\n"
    "ds_read_b64_tr_b16 a[14:15], v4 offset:7936\n"
    "ds_read_b64_tr_b16 a[92:93], v4 offset:7688\n"
    "ds_read_b64_tr_b16 a[94:95], v4 offset:7944\n"
    "s_waitcnt lgkmcnt(0)\n"
    "ds_read_b64_tr_b16 a[16:17], v4 offset:10240\n"
    "ds_read_b64_tr_b16 a[18:19], v4 offset:10496\n"
    "ds_read_b64_tr_b16 a[96:97], v4 offset:10248\n"
    "ds_read_b64_tr_b16 a[98:99], v4 offset:10504\n"
    "s_waitcnt lgkmcnt(0)\n"
    "ds_read_b64_tr_b16 a[20:21], v4 offset:12800\n"
    "ds_read_b64_tr_b16 a[22:23], v4 offset:13056\n"
    "ds_read_b64_tr_b16 a[100:101], v4 offset:12808\n"
    "ds_read_b64_tr_b16 a[102:103], v4 offset:13064\n"
    "s_waitcnt lgkmcnt(0)\n"
    "ds_read_b64_tr_b16 a[24:25], v4 offset:15360\n"
    "ds_read_b64_tr_b16 a[26:27], v4 offset:15616\n"
    "ds_read_b64_tr_b16 a[104:105], v4 offset:15368\n"
    "ds_read_b64_tr_b16 a[106:107], v4 offset:15624\n"
    "s_waitcnt lgkmcnt(0)\n"
    "ds_read_b64_tr_b16 a[28:29], v4 offset:17920\n"
    "ds_read_b64_tr_b16 a[30:31], v4 offset:18176\n"
    "ds_read_b64_tr_b16 a[108:109], v4 offset:17928\n"
    "ds_read_b64_tr_b16 a[110:111], v4 offset:18184\n"
    "s_waitcnt lgkmcnt(0)\n"
    "ds_read_b64_tr_b16 a[32:33], v4 offset:20480\n"
    "ds_read_b64_tr_b16 a[34:35], v4 offset:20736\n"
    "ds_read_b64_tr_b16 a[112:113], v4 offset:20488\n"
    "ds_read_b64_tr_b16 a[114:115], v4 offset:20744\n"
    "s_waitcnt lgkmcnt(0)\n"
    "ds_read_b64_tr_b16 a[36:37], v4 offset:23040\n"
    "ds_read_b64_tr_b16 a[38:39], v4 offset:23296\n"
    "ds_read_b64_tr_b16 a[116:117], v4 offset:23048\n"
    "ds_read_b64_tr_b16 a[118:119], v4 offset:23304\n"
    "s_waitcnt lgkmcnt(0)\n"
    "ds_read_b64_tr_b16 a[40:41], v4 offset:25600\n"
    "ds_read_b64_tr_b16 a[42:43], v4 offset:25856\n"
    "ds_read_b64_tr_b16 a[120:121], v4 offset:25608\n"
    "ds_read_b64_tr_b16 a[122:123], v4 offset:25864\n"
    "s_waitcnt lgkmcnt(0)\n"
    "ds_read_b64_tr_b16 a[44:45], v4 offset:28160\n"
    "ds_read_b64_tr_b16 a[46:47], v4 offset:28416\n"
    "ds_read_b64_tr_b16 a[124:125], v4 offset:28168\n"
    "ds_read_b64_tr_b16 a[126:127], v4 offset:28424\n"
    "s_waitcnt lgkmcnt(0)\n"
    "ds_read_b64_tr_b16 a[48:49], v4 offset:30720\n"
    "ds_read_b64_tr_b16 a[50:51], v4 offset:30976\n"
    "ds_read_b64_tr_b16 a[128:129], v4 offset:30728\n"
    "ds_read_b64_tr_b16 a[130:131], v4 offset:30984\n"
    "s_waitcnt lgkmcnt(0)\n"
    "ds_read_b64_tr_b16 a[52:53], v4 offset:33280\n"
    "ds_read_b64_tr_b16 a[54:55], v4 offset:33536\n"
    "ds_read_b64_tr_b16 a[132:133], v4 offset:33288\n"
    "ds_read_b64_tr_b16 a[134:135], v4 offset:33544\n"
    "s_waitcnt lgkmcnt(0)\n"
    "ds_read_b64_tr_b16 a[56:57], v4 offset:35840\n"
    "ds_read_b64_tr_b16 a[58:59], v4 offset:36096\n"
    "ds_read_b64_tr_b16 a[136:137], v4 offset:35848\n"
    "ds_read_b64_tr_b16 a[138:139], v4 offset:36104\n"
    "s_waitcnt lgkmcnt(0)\n"
    "ds_read_b64_tr_b16 a[60:61], v4 offset:38400\n"
    "ds_read_b64_tr_b16 a[62:63], v4 offset:38656\n"
    "ds_read_b64_tr_b16 a[140:141], v4 offset:38408\n"
    "ds_read_b64_tr_b16 a[142:143], v4 offset:38664\n"
    "s_waitcnt lgkmcnt(0)\n"
    "ds_read_b64_tr_b16 a[64:65], v5 offset:0\n"
    "ds_read_b64_tr_b16 a[66:67], v5 offset:5120\n"
    "ds_read_b64_tr_b16 a[144:145], v5 offset:8\n"
    "ds_read_b64_tr_b16 a[146:147], v5 offset:5128\n"
    "s_waitcnt lgkmcnt(0)\n"
    "ds_read_b64_tr_b16 a[68:69], v5 offset:10240\n"
    "ds_read_b64_tr_b16 a[70:71], v5 offset:15360\n"
    "ds_read_b64_tr_b16 a[148:149], v5 offset:10248\n"
    "ds_read_b64_tr_b16 a[150:151], v5 offset:15368\n"
    "s_waitcnt lgkmcnt(0)\n"
    "ds_read_b64_tr_b16 a[72:73], v5 offset:20480\n"
    "ds_read_b64_tr_b16 a[74:75], v5 offset:25600\n"
    "ds_read_b64_tr_b16 a[152:153], v5 offset:20488\n"
    "ds_read_b64_tr_b16 a[154:155], v5 offset:25608\n"
    "s_waitcnt lgkmcnt(0)\n"
    "ds_read_b64_tr_b16 a[76:77], v5 offset:30720\n"
    "ds_read_b64_tr_b16 a[78:79], v5 offset:35840\n"
    "ds_read_b64_tr_b16 a[156:157], v5 offset:30728\n"
    "ds_read_b64_tr_b16 a[158:159], v5 offset:35848\n"
    "s_waitcnt lgkmcnt(0)\n"
    "s_mov_b32 s26, -2\n"
    ".Lb2_po2_%=:\n"
    "buffer_load_dword v8, v6, s[0:3], s16 offen sc1\n"
    "s_waitcnt vmcnt(0)\n"
    "v_cmp_gt_i32 vcc, 1, v8\n"
    "s_nop 0\n"
    "s_cbranch_vccz .Lb2_pd3_%=\n"
    "s_add_u32 s20, s20, 1\n"
    "s_cmp_lt_u32 s20, 0x40000\n"
    "s_cbranch_scc1 .Lb2_sp4_%=\n"
    "v_mov_b32 v8, 70\n"
    "s_mov_b32 s24, 33728\n"
    "buffer_store_dword v8, off, s[0:3], s24 sc1\n"
    "s_waitcnt vmcnt(0)\n"
    "s_branch .Lb2_pd3_%=\n"
    ".Lb2_sp4_%=:\n"
    "s_sleep 4\n"
    "s_branch .Lb2_po2_%=\n"
    ".Lb2_pd3_%=:\n"
    "s_mov_b32 s26, 0\n"
    "s_lshl_b32 s33, s12, 11\n"
    "s_mov_b32 s35, 0\n"
    "v_mov_b32 v84, 0\n"
    ".Lb2_top_%=:\n"
    "s_lshl_b32 s28, s26, 19\n"
    "s_lshl_b32 s25, s26, 11\n"
    "s_xor_b32 s25, s25, s13\n"
    "s_and_b32 s25, s25, 0x2000\n"
    "s_add_u32 s28, s28, s25\n"
    "s_add_u32 s28, s28, s14\n"
    "s_add_u32 s27, s26, 1\n"
    "s_sub_u32 s29, s28, 81920\n"
    "s_sub_u32 s31, s28, 163840\n"
    "s_xor_b32 s31, s31, 0x2000\n"
    "s_mul_i32 s30, s26, 40960\n"
    "s_cmp_eq_u32 s13, 0\n"
    "s_cselect_b32 s30, s31, s30\n"
    "s_and_b32 s32, s26, 32\n"
    "s_add_u32 s32, s32, s26\n"
    "s_lshl_b32 s32, s32, 8\n"
    "s_add_u32 s32, s32, s17\n"
    "s_cmp_lg_u32 s12, 0\n"
    "s_cbranch_scc1 .Lb2_pd6_%=\n"
    "s_add_u32 s25, s22, 256\n"
    "v_min3_i32 v84, v84, v85, v86\n"
    "v_min_i32 v84, v84, v87\n"
    "v_cmp_gt_i32 vcc, s27, v84\n"
    "s_nop 0\n"
    "s_cbranch_vccz .Lb2_pd6_%=\n"
    ".Lb2_rt7_%=:\n"
    "s_memrealtime s[22:23]\n"
    "s_waitcnt lgkmcnt(0)\n"
    "s_sub_u32 s24, s35, s22\n"
    "s_cmp_lt_u32 s24, 600\n"
    "s_cbranch_scc1 .Lb2_rt7_%=\n"
    ".Lb2_po5_%=:\n"
    "buffer_load_dwordx4 v[84:87], v225, s[0:3], s15 offen sc1\n"
    "s_memrealtime s[22:23]\n"
    "s_waitcnt vmcnt(0) lgkmcnt(0)\n"
    "s_add_u32 s25, s22, 256\n"
    "v_min3_i32 v84, v84, v85, v86\n"
    "v_min_i32 v84, v84, v87\n"
    "v_cmp_gt_i32 vcc, s27, v84\n"
    "s_nop 0\n"
    "s_cbranch_vccz .Lb2_pd6_%=\n"
    "s_add_u32 s20, s20, 1\n"
    "s_cmp_lt_u32 s20, 0x40000\n"
    "s_cbranch_scc1 .Lb2_sp8_%=\n"
    "v_mov_b32 v84, 71\n"
    "s_mov_b32 s24, 33728\n"
    "buffer_store_dword v84, off, s[0:3], s24 sc1\n"
    "s_waitcnt vmcnt(0)\n"
    "s_branch .Lb2_pd6_%=\n"
    ".Lb2_sp8_%=:\n"
    "s_sleep 6\n"
    "s_branch .Lb2_po5_%=\n"
    ".Lb2_pd6_%=:\n"
    "s_barrier\n"
    "s_mov_b32 s35, s25\n"
    ".Lb2_mk0x8_%=:\n"
    "s_add_u32 m0, s33, 0\n"
    "s_nop 0\n"
    "buffer_load_dwordx4 v224, s[0:3], s28 offen sc1 lds\n"
    "buffer_load_dwordx4 v224, s[0:3], s28 offen offset:1024 sc1 lds\n"
    "s_add_u32 m0, s33, 8192\n"
    "s_add_u32 s28, s28, 0x4000\n"
    "buffer_load_dwordx4 v224, s[0:3], s28 offen sc1 lds\n"
    "buffer_load_dwordx4 v224, s[0:3], s28 offen offset:1024 sc1 lds\n"
    "s_add_u32 m0, s33, 16384\n"
    "s_add_u32 s28, s28, 0x4000\n"
    "buffer_load_dwordx4 v224, s[0:3], s28 offen sc1 lds\n"
    "buffer_load_dwordx4 v224, s[0:3], s28 offen offset:1024 sc1 lds\n"
    "s_add_u32 m0, s33, 24576\n"
    "s_add_u32 s28, s28, 0x4000\n"
    "buffer_load_dwordx4 v224, s[0:3], s28 offen sc1 lds\n"
    "buffer_load_dwordx4 v224, s[0:3], s28 offen offset:1024 sc1 lds\n"
    "s_add_u32 m0, s33, 32768\n"
    "s_add_u32 s28, s28, 0x4000\n"
    "buffer_load_dwordx4 v224, s[0:3], s28 offen sc1 lds\n"
    "buffer_load_dwordx4 v224, s[0:3], s28 offen offset:1024 sc1 lds\n"
    "buffer_load_dwordx4 v[48:51], v227, s[0:3], s29 offen offset:0 sc1\n"
    "buffer_load_dwordx4 v[52:55], v227, s[0:3], s29 offen offset:1024 sc1\n"
    "s_waitcnt vmcnt(10)\n"
    "s_barrier\n"
    ".Lb2_mk1x9_%=:\n"
    "ds_read_b128 v[16:19], v224 offset:0\n"
    "ds_read_b128 v[20:23], v224 offset:1024\n"
    "ds_read_b128 v[24:27], v224 offset:2048\n"
    "ds_read_b128 v[28:31], v224 offset:3072\n"
    "ds_read_b128 v[32:35], v224 offset:4096\n"
    "ds_read_b128 v[36:39], v224 offset:5120\n"
    "s_waitcnt lgkmcnt(4)\n"
    "v_mfma_f32_16x16x32_bf16 v[0:3], a[0:3], v[16:19], 0\n"
    "ds_read_b128 v[40:43], v224 offset:6144\n"
    "ds_read_b128 v[44:47], v224 offset:7168\n"
    "v_mfma_f32_16x16x32_bf16 v[8:11], a[0:3], v[20:23], 0\n"
    "v_mfma_f32_16x16x32_bf16 v[4:7], a[80:83], v[16:19], 0\n"
    "v_mfma_f32_16x16x32_bf16 v[12:15], a[80:83], v[20:23], 0\n"
    "s_waitcnt lgkmcnt(4)\n"
    "v_mfma_f32_16x16x32_bf16 v[0:3], a[4:7], v[24:27], v[0:3]\n"
    "s_waitcnt vmcnt(8)\n"
    "s_barrier\n"
    "ds_read_b128 v[16:19], v224 offset:8192\n"
    "v_mfma_f32_16x16x32_bf16 v[8:11], a[4:7], v[28:31], v[8:11]\n"
    "ds_read_b128 v[20:23], v224 offset:9216\n"
    "v_mfma_f32_16x16x32_bf16 v[4:7], a[84:87], v[24:27], v[4:7]\n"
    "v_mfma_f32_16x16x32_bf16 v[12:15], a[84:87], v[28:31], v[12:15]\n"
    "s_waitcnt lgkmcnt(4)\n"
    "v_mfma_f32_16x16x32_bf16 v[0:3], a[8:11], v[32:35], v[0:3]\n"
    "ds_read_b128 v[24:27], v224 offset:10240\n"
    "ds_read_b128 v[28:31], v224 offset:11264\n"
    "v_mfma_f32_16x16x32_bf16 v[8:11], a[8:11], v[36:39], v[8:11]\n"
    "v_mfma_f32_16x16x32_bf16 v[4:7], a[88:91], v[32:35], v[4:7]\n"
    "v_mfma_f32_16x16x32_bf16 v[12:15], a[88:91], v[36:39], v[12:15]\n"
    "s_waitcnt lgkmcnt(4)\n"
    "v_mfma_f32_16x16x32_bf16 v[0:3], a[12:15], v[40:43], v[0:3]\n"
    "ds_read_b128 v[32:35], v224 offset:12288\n"
    "ds_read_b128 v[36:39], v224 offset:13312\n"
    "v_mfma_f32_16x16x32_bf16 v[8:11], a[12:15], v[44:47], v[8:11]\n"
    "v_mfma_f32_16x16x32_bf16 v[4:7], a[92:95], v[40:43], v[4:7]\n"
    "v_mfma_f32_16x16x32_bf16 v[12:15], a[92:95], v[44:47], v[12:15]\n"
    "s_waitcnt lgkmcnt(4)\n"
    "v_mfma_f32_16x16x32_bf16 v[0:3], a[16:19], v[16:19], v[0:3]\n"
    "ds_read_b128 v[40:43], v224 offset:14336\n"
    "ds_read_b128 v[44:47], v224 offset:15360\n"
    "v_mfma_f32_16x16x32_bf16 v[8:11], a[16:19], v[20:23], v[8:11]\n"
    "v_mfma_f32_16x16x32_bf16 v[4:7], a[96:99], v[16:19], v[4:7]\n"
    "v_mfma_f32_16x16x32_bf16 v[12:15], a[96:99], v[20:23], v[12:15]\n"
    "s_waitcnt lgkmcnt(4)\n"
    "v_mfma_f32_16x16x32_bf16 v[0:3], a[20:23], v[24:27], v[0:3]\n"
    "s_waitcnt vmcnt(6)\n"
    "s_barrier\n"
    "ds_read_b128 v[16:19], v224 offset:16384\n"
    "v_mfma_f32_16x16x32_bf16 v[8:11], a[20:23], v[28:31], v[8:11]\n"
    "ds_read_b128 v[20:23], v224 offset:17408\n"
    "v_mfma_f32_16x16x32_bf16 v[4:7], a[100:103], v[24:27], v[4:7]\n"
    "v_mfma_f32_16x16x32_bf16 v[12:15], a[100:103], v[28:31], v[12:15]\n"
    "s_waitcnt lgkmcnt(4)\n"
    "v_mfma_f32_16x16x32_bf16 v[0:3], a[24:27], v[32:35], v[0:3]\n"
    "ds_read_b128 v[24:27], v224 offset:18432\n"
    "ds_read_b128 v[28:31], v224 offset:19456\n"
    "v_mfma_f32_16x16x32_bf16 v[8:11], a[24:27], v[36:39], v[8:11]\n"
    "v_mfma_f32_16x16x32_bf16 v[4:7], a[104:107], v[32:35], v[4:7]\n"
    "v_mfma_f32_16x16x32_bf16 v[12:15], a[104:107], v[36:39], v[12:15]\n"
    "s_waitcnt lgkmcnt(4)\n"
    "v_mfma_f32_16x16x32_bf16 v[0:3], a[28:31], v[40:43], v[0:3]\n"
    "ds_read_b128 v[32:35], v224 offset:20480\n"
    "ds_read_b128 v[36:39], v224 offset:21504\n"
    "v_mfma_f32_16x16x32_bf16 v[8:11], a[28:31], v[44:47], v[8:11]\n"
    "v_mfma_f32_16x16x32_bf16 v[4:7], a[108:111], v[40:43], v[4:7]\n"
    "v_mfma_f32_16x16x32_bf16 v[12:15], a[108:111], v[44:47], v[12:15]\n"
    "s_waitcnt lgkmcnt(4)\n"
    "v_mfma_f32_16x16x32_bf16 v[0:3], a[32:35], v[16:19], v[0:3]\n"
    "ds_read_b128 v[40:43], v224 offset:22528\n"
    "ds_read_b128 v[44:47], v224 offset:23552\n"
    "v_mfma_f32_16x16x32_bf16 v[8:11], a[32:35], v[20:23], v[8:11]\n"
    "v_mfma_f32_16x16x32_bf16 v[4:7], a[112:115], v[16:19], v[4:7]\n"
    "v_mfma_f32_16x16x32_bf16 v[12:15], a[112:115], v[20:23], v[12:15]\n"
    "s_waitcnt lgkmcnt(4)\n"
    "v_mfma_f32_16x16x32_bf16 v[0:3], a[36:39], v[24:27], v[0:3]\n"
    "s_waitcnt vmcnt(4)\n"
    "s_barrier\n"
    "ds_read_b128 v[16:19], v224 offset:24576\n"
    "v_mfma_f32_16x16x32_bf16 v[8:11], a[36:39], v[28:31], v[8:11]\n"
    "ds_read_b128 v[20:23], v224 offset:25600\n"
    "v_mfma_f32_16x16x32_bf16 v[4:7], a[116:119], v[24:27], v[4:7]\n"
    "v_mfma_f32_16x16x32_bf16 v[12:15], a[116:119], v[28:31], v[12:15]\n"
    "s_waitcnt lgkmcnt(4)\n"
    "v_mfma_f32_16x16x32_bf16 v[0:3], a[40:43], v[32:35], v[0:3]\n"
    "ds_read_b128 v[24:27], v224 offset:26624\n"
    "ds_read_b128 v[28:31], v224 offset:27648\n"
    "v_mfma_f32_16x16x32_bf16 v[8:11], a[40:43], v[36:39], v[8:11]\n"
    "v_mfma_f32_16x16x32_bf16 v[4:7], a[120:123], v[32:35], v[4:7]\n"
    "v_mfma_f32_16x16x32_bf16 v[12:15], a[120:123], v[36:39], v[12:15]\n"
    "s_waitcnt lgkmcnt(4)\n"
    "v_mfma_f32_16x16x32_bf16 v[0:3], a[44:47], v[40:43], v[0:3]\n"
    "ds_read_b128 v[32:35], v224 offset:28672\n"
    "ds_read_b128 v[36:39], v224 offset:29696\n"
    "v_mfma_f32_16x16x32_bf16 v[8:11], a[44:47], v[44:47], v[8:11]\n"
    "v_mfma_f32_16x16x32_bf16 v[4:7], a[124:127], v[40:43], v[4:7]\n"
    "v_mfma_f32_16x16x32_bf16 v[12:15], a[124:127], v[44:47], v[12:15]\n"
    "s_waitcnt lgkmcnt(4)\n"
    "v_mfma_f32_16x16x32_bf16 v[0:3], a[48:51], v[16:19], v[0:3]\n"
    "ds_read_b128 v[40:43], v224 offset:30720\n"
    "ds_read_b128 v[44:47], v224 offset:31744\n"
    "v_mfma_f32_16x16x32_bf16 v[8:11], a[48:51], v[20:23], v[8:11]\n"
    "v_mfma_f32_16x16x32_bf16 v[4:7], a[128:131], v[16:19], v[4:7]\n"
    "v_mfma_f32_16x16x32_bf16 v[12:15], a[128:131], v[20:23], v[12:15]\n"
    "s_waitcnt lgkmcnt(4)\n"
    "v_mfma_f32_16x16x32_bf16 v[0:3], a[52:55], v[24:27], v[0:3]\n"
    "s_waitcnt vmcnt(2)\n"
    "s_barrier\n"
    "ds_read_b128 v[16:19], v224 offset:32768\n"
    "v_mfma_f32_16x16x32_bf16 v[8:11], a[52:55], v[28:31], v[8:11]\n"
    "ds_read_b128 v[20:23], v224 offset:33792\n"
    "v_mfma_f32_16x16x32_bf16 v[4:7], a[132:135], v[24:27], v[4:7]\n"
    "v_mfma_f32_16x16x32_bf16 v[12:15], a[132:135], v[28:31], v[12:15]\n"
    "s_waitcnt lgkmcnt(4)\n"
    "v_mfma_f32_16x16x32_bf16 v[0:3], a[56:59], v[32:35], v[0:3]\n"
    "ds_read_b128 v[24:27], v224 offset:34816\n"
    "ds_read_b128 v[28:31], v224 offset:35840\n"
    "v_mfma_f32_16x16x32_bf16 v[8:11], a[56:59], v[36:39], v[8:11]\n"
    "v_mfma_f32_16x16x32_bf16 v[4:7], a[136:139], v[32:35], v[4:7]\n"
    "s_waitcnt vmcnt(0)\n"
    "v_lshlrev_b32 v96, 16, v48\n"
    "v_and_b32 v97, 0xffff0000, v48\n"
    "v_mfma_f32_16x16x32_bf16 v[12:15], a[136:139], v[36:39], v[12:15]\n"
    "s_waitcnt lgkmcnt(4)\n"
    "v_lshlrev_b32 v98, 16, v49\n"
    "v_and_b32 v99, 0xffff0000, v49\n"
    "v_mfma_f32_16x16x32_bf16 v[0:3], a[60:63], v[40:43], v[0:3]\n"
    "s_waitcnt lgkmcnt(2)\n"
    "ds_read_b128 v[32:35], v224 offset:36864\n"
    "ds_read_b128 v[36:39], v224 offset:37888\n"
    "v_mfma_f32_16x16x32_bf16 v[8:11], a[60:63], v[44:47], v[8:11]\n"
    "v_swap_b32 v18, v20\n"
    "v_mfma_f32_16x16x32_bf16 v[4:7], a[140:143], v[40:43], v[4:7]\n"
    "v_swap_b32 v19, v21\n"
    "v_mfma_f32_16x16x32_bf16 v[12:15], a[140:143], v[44:47], v[12:15]\n"
    "v_lshlrev_b32 v100, 16, v50\n"
    "v_and_b32 v101, 0xffff0000, v50\n"
    "v_mfma_f32_16x16x32_bf16 v[0:3], a[64:67], v[16:19], v[0:3]\n"
    "s_waitcnt lgkmcnt(2)\n"
    "ds_read_b128 v[40:43], v224 offset:38912\n"
    "ds_read_b128 v[44:47], v224 offset:39936\n"
    "v_mfma_f32_16x16x32_bf16 v[8:11], a[64:67], v[20:23], v[8:11]\n"
    "v_swap_b32 v26, v28\n"
    "v_mfma_f32_16x16x32_bf16 v[4:7], a[144:147], v[16:19], v[4:7]\n"
    "v_swap_b32 v27, v29\n"
    "v_mfma_f32_16x16x32_bf16 v[12:15], a[144:147], v[20:23], v[12:15]\n"
    "v_lshlrev_b32 v102, 16, v51\n"
    "v_and_b32 v103, 0xffff0000, v51\n"
    "v_mfma_f32_16x16x32_bf16 v[0:3], a[68:71], v[24:27], v[0:3]\n"
    "s_waitcnt lgkmcnt(2)\n"
    "v_lshlrev_b32 v104, 16, v52\n"
    "v_and_b32 v105, 0xffff0000, v52\n"
    "v_mfma_f32_16x16x32_bf16 v[8:11], a[68:71], v[28:31], v[8:11]\n"
    "v_swap_b32 v34, v36\n"
    "v_mfma_f32_16x16x32_bf16 v[4:7], a[148:151], v[24:27], v[4:7]\n"
    "v_swap_b32 v35, v37\n"
    "v_mfma_f32_16x16x32_bf16 v[12:15], a[148:151], v[28:31], v[12:15]\n"
    "v_lshlrev_b32 v106, 16, v53\n"
    "v_and_b32 v107, 0xffff0000, v53\n"
    "v_mfma_f32_16x16x32_bf16 v[0:3], a[72:75], v[32:35], v[0:3]\n"
    "s_waitcnt lgkmcnt(0)\n"
    "v_lshlrev_b32 v108, 16, v54\n"
    "v_and_b32 v109, 0xffff0000, v54\n"
    "v_mfma_f32_16x16x32_bf16 v[8:11], a[72:75], v[36:39], v[8:11]\n"
    "v_swap_b32 v42, v44\n"
    "v_mfma_f32_16x16x32_bf16 v[4:7], a[152:155], v[32:35], v[4:7]\n"
    "v_swap_b32 v43, v45\n"
    "v_mfma_f32_16x16x32_bf16 v[12:15], a[152:155], v[36:39], v[12:15]\n"
    "v_lshlrev_b32 v110, 16, v55\n"
    "v_and_b32 v111, 0xffff0000, v55\n"
    "v_mfma_f32_16x16x32_bf16 v[0:3], a[76:79], v[40:43], v[0:3]\n"
    "v_mfma_f32_16x16x32_bf16 v[8:11], a[76:79], v[44:47], v[8:11]\n"
    "v_mfma_f32_16x16x32_bf16 v[4:7], a[156:159], v[40:43], v[4:7]\n"
    "v_mfma_f32_16x16x32_bf16 v[12:15], a[156:159], v[44:47], v[12:15]\n"
    ".Lb2_mk2x10_%=:\n"
    "v_pk_add_f32 v[0:1], v[0:1], v[96:97]\n"
    "v_pk_add_f32 v[2:3], v[2:3], v[98:99]\n"
    "v_pk_add_f32 v[8:9], v[8:9], v[104:105]\n"
    "v_pk_add_f32 v[10:11], v[10:11], v[106:107]\n"
    "s_nop 0\n"
    "v_pk_add_f32 v[4:5], v[4:5], v[100:101]\n"
    "v_pk_add_f32 v[6:7], v[6:7], v[102:103]\n"
    "s_nop 0\n"
    "v_pk_add_f32 v[12:13], v[12:13], v[108:109]\n"
    "v_pk_add_f32 v[14:15], v[14:15], v[110:111]\n"
    "s_cmp_lg_u32 s13, 0\n"
    "s_cbranch_scc1 .Lb2_out_%=\n"
    "v_pk_add_f32 v[64:65], v[0:1], v[2:3]\n"
    "v_pk_add_f32 v[68:69], v[8:9], v[10:11]\n"
    "v_pk_mul_f32 v[66:67], v[0:1], v[0:1]\n"
    "v_pk_mul_f32 v[70:71], v[8:9], v[8:9]\n"
    "v_pk_add_f32 v[96:97], v[4:5], v[6:7]\n"
    "v_pk_add_f32 v[98:99], v[12:13], v[14:15]\n"
    "v_pk_fma_f32 v[66:67], v[2:3], v[2:3], v[66:67]\n"
    "v_pk_fma_f32 v[70:71], v[10:11], v[10:11], v[70:71]\n"
    "v_pk_add_f32 v[64:65], v[64:65], v[96:97]\n"
    "v_pk_add_f32 v[68:69], v[68:69], v[98:99]\n"
    "v_pk_fma_f32 v[66:67], v[4:5], v[4:5], v[66:67]\n"
    "v_pk_fma_f32 v[70:71], v[12:13], v[12:13], v[70:71]\n"
    "v_add_f32 v64, v64, v65\n"
    "v_add_f32 v68, v68, v69\n"
    "v_pk_fma_f32 v[66:67], v[6:7], v[6:7], v[66:67]\n"
    "v_pk_fma_f32 v[70:71], v[14:15], v[14:15], v[70:71]\n"
    "v_add_f32 v66, v66, v67\n"
    "v_add_f32 v70, v70, v71\n"
    "s_nop 0\n"
    "v_permlane16_swap_b32 v64, v66\n"
    "v_permlane16_swap_b32 v68, v70\n"
    "v_add_f32 v64, v64, v66\n"
    "v_add_f32 v68, v68, v70\n"
    "s_nop 1\n"
    "v_permlane32_swap_b32 v64, v68\n"
    "v_add_f32 v64, v64, v68\n"
    "v_min_u32 v64, -2, v64\n"
    "v_mov_b32 v65, v64\n"
    "v_cvt_pk_bf16_f32 v56, v0, v1\n"
    "v_cvt_pk_bf16_f32 v57, v2, v3\n"
    "v_permlane16_swap_b32 v64, v65\n"
    "v_mov_b32 v66, v64\n"
    "v_mov_b32 v67, v65\n"
    "v_cvt_pk_bf16_f32 v58, v4, v5\n"
    "v_cvt_pk_bf16_f32 v59, v6, v7\n"
    "v_permlane32_swap_b32 v64, v66\n"
    "v_permlane32_swap_b32 v65, v67\n"
    "buffer_store_dwordx4 v[64:67], v230, s[0:3], s32 offen sc1\n"
    "v_cvt_pk_bf16_f32 v60, v8, v9\n"
    "v_cvt_pk_bf16_f32 v61, v10, v11\n"
    "v_cvt_pk_bf16_f32 v62, v12, v13\n"
    "v_cvt_pk_bf16_f32 v63, v14, v15\n"
    ".Lb2_mk3x11_%=:\n"
    "v_lshlrev_b32 v0, 16, v56\n"
    "v_and_b32 v1, 0xffff0000, v56\n"
    "v_lshlrev_b32 v2, 16, v57\n"
    "v_and_b32 v3, 0xffff0000, v57\n"
    "v_lshlrev_b32 v4, 16, v58\n"
    "v_and_b32 v5, 0xffff0000, v58\n"
    "v_lshlrev_b32 v6, 16, v59\n"
    "v_and_b32 v7, 0xffff0000, v59\n"
    "v_lshlrev_b32 v8, 16, v60\n"
    "v_and_b32 v9, 0xffff0000, v60\n"
    "v_lshlrev_b32 v10, 16, v61\n"
    "v_and_b32 v11, 0xffff0000, v61\n"
    "v_lshlrev_b32 v12, 16, v62\n"
    "v_and_b32 v13, 0xffff0000, v62\n"
    "v_lshlrev_b32 v14, 16, v63\n"
    "v_and_b32 v15, 0xffff0000, v63\n"
    "s_sleep 6\n"
    "buffer_load_dwordx4 v[64:67], v231, s[0:3], s32 offen sc1\n"
    "buffer_load_dwordx4 v[68:71], v232, s[0:3], s32 offen sc1\n"
    "buffer_load_dwordx4 v[72:75], v233, s[0:3], s32 offen sc1\n"
    "buffer_load_dwordx4 v[76:79], v234, s[0:3], s32 offen sc1\n"
    "buffer_load_dwordx4 v[80:83], v235, s[0:3], s32 offen sc1\n"
    "buffer_store_dwordx4 v[56:59], v228, s[8:11], s30 offen sc1\n"
    "buffer_store_dwordx4 v[60:63], v229, s[8:11], s30 offen sc1\n"
    ".Lb2_sl_%=:\n"
    "s_waitcnt vmcnt(6)\n"
    "v_max_u32 v84, v64, v64\n"
    "s_waitcnt vmcnt(5)\n"
    "v_max_u32 v84, v68, v84\n"
    "v_pk_add_f32 v[64:65], v[64:65], v[68:69]\n"
    "v_pk_add_f32 v[66:67], v[66:67], v[70:71]\n"
    "s_waitcnt vmcnt(4)\n"
    "v_max_u32 v84, v72, v84\n"
    "v_pk_add_f32 v[64:65], v[64:65], v[72:73]\n"
    "v_pk_add_f32 v[66:67], v[66:67], v[74:75]\n"
    "s_waitcnt vmcnt(3)\n"
    "v_max_u32 v84, v76, v84\n"
    "v_pk_add_f32 v[64:65], v[64:65], v[76:77]\n"
    "v_pk_add_f32 v[66:67], v[66:67], v[78:79]\n"
    "s_waitcnt vmcnt(2)\n"
    "v_max_u32 v84, v80, v84\n"
    "v_pk_add_f32 v[64:65], v[64:65], v[80:81]\n"
    "v_pk_add_f32 v[66:67], v[66:67], v[82:83]\n"
    "v_cmp_eq_u32 vcc, -1, v84\n"
    "s_nop 0\n"
    "s_cbranch_vccnz .Lb2_sr_%=\n"
    ".Lb2_sg_%=:\n"
    ".Lb2_mk4x12_%=:\n"
    "v_permlane16_swap_b32 v64, v65\n"
    "v_permlane16_swap_b32 v66, v67\n"
    "v_add_f32 v64, v64, v65\n"
    "v_add_f32 v66, v66, v67\n"
    "s_nop 1\n"
    "v_permlane32_swap_b32 v64, v66\n"
    "v_add_f32 v64, v64, v66\n"
    "v_mov_b32 v66, v64\n"
    "s_nop 1\n"
    "v_permlane16_swap_b32 v64, v66\n"
    "v_mul_f32 v64, 0x3acccccd, v64\n"
    "v_mul_f32 v66, 0x3acccccd, v66\n"
    "v_fma_f32 v66, -v64, v64, v66\n"
    "v_max_f32 v66, 0, v66\n"
    "v_cvt_pk_bf16_f32 v64, v64, v66\n"
    "v_and_b32 v66, 0xffff0000, v64\n"
    "v_lshlrev_b32 v64, 16, v64\n"
    "v_add_f32 v66, 0x3727c5ac, v66\n"
    "v_mov_b32 v68, v64\n"
    "v_cvt_pk_bf16_f32 v66, v66, v66\n"
    "v_lshlrev_b32 v66, 16, v66\n"
    "v_sqrt_f32 v66, v66\n"
    "v_permlane32_swap_b32 v64, v68\n"
    "v_cvt_pk_bf16_f32 v66, v66, v66\n"
    "v_lshlrev_b32 v66, 16, v66\n"
    "v_rcp_f32 v66, v66\n"
    "s_nop 0\n"
    "v_mov_b32 v70, v66\n"
    "s_nop 1\n"
    "v_permlane32_swap_b32 v66, v70\n"
    "v_mul_f32 v64, v64, v66\n"
    "v_mul_f32 v68, v68, v70\n"
    "v_pk_fma_f32 v[0:1], v[0:1], v[66:67], v[64:65] op_sel_hi:[1,0,0] neg_lo:[0,0,1] neg_hi:[0,0,1]\n"
    "v_pk_fma_f32 v[2:3], v[2:3], v[66:67], v[64:65] op_sel_hi:[1,0,0] neg_lo:[0,0,1] neg_hi:[0,0,1]\n"
    "v_pk_fma_f32 v[4:5], v[4:5], v[66:67], v[64:65] op_sel_hi:[1,0,0] neg_lo:[0,0,1] neg_hi:[0,0,1]\n"
    "v_pk_fma_f32 v[6:7], v[6:7], v[66:67], v[64:65] op_sel_hi:[1,0,0] neg_lo:[0,0,1] neg_hi:[0,0,1]\n"
    "v_cvt_pk_bf16_f32 v96, v0, v1\n"
    "v_cvt_pk_bf16_f32 v98, v2, v3\n"
    "v_cvt_pk_bf16_f32 v97, v4, v5\n"
    "v_cvt_pk_bf16_f32 v99, v6, v7\n"
    "buffer_store_dwordx4 v[96:99], v227, s[0:3], s31 offen offset:0 sc1\n"
    "v_pk_fma_f32 v[8:9], v[8:9], v[70:71], v[68:69] op_sel_hi:[1,0,0] neg_lo:[0,0,1] neg_hi:[0,0,1]\n"
    "v_pk_fma_f32 v[10:11], v[10:11], v[70:71], v[68:69] op_sel_hi:[1,0,0] neg_lo:[0,0,1] neg_hi:[0,0,1]\n"
    "v_pk_fma_f32 v[12:13], v[12:13], v[70:71], v[68:69] op_sel_hi:[1,0,0] neg_lo:[0,0,1] neg_hi:[0,0,1]\n"
    "v_pk_fma_f32 v[14:15], v[14:15], v[70:71], v[68:69] op_sel_hi:[1,0,0] neg_lo:[0,0,1] neg_hi:[0,0,1]\n"
    "v_cvt_pk_bf16_f32 v100, v8, v9\n"
    "v_cvt_pk_bf16_f32 v102, v10, v11\n"
    "v_cvt_pk_bf16_f32 v101, v12, v13\n"
    "v_cvt_pk_bf16_f32 v103, v14, v15\n"
    "buffer_store_dwordx4 v[100:103], v227, s[0:3], s31 offen offset:1024 sc1\n"
    "v_mov_b32 v88, s27\n"
    "v_mov_b32 v89, s27\n"
    "v_mov_b32 v90, s27\n"
    "v_mov_b32 v91, s27\n"
    ".Lb2_mk5x13_%=:\n"
    "v_mov_b32 v84, 0\n"
    "s_memrealtime s[22:23]\n"
    "s_cmp_lg_u32 s12, 0\n"
    "s_waitcnt lgkmcnt(0)\n"
    "s_cbranch_scc1 .Lb2_pl15_%=\n"
    "s_sub_u32 s24, s35, s22\n"
    "s_cmp_gt_i32 s24, 40\n"
    "s_cbranch_scc1 .Lb2_pl15_%=\n"
    "buffer_load_dwordx4 v[84:87], v225, s[0:3], s15 offen sc1\n"
    ".Lb2_pl15_%=:\n"
    "s_waitcnt vmcnt(0)\n"
    "s_barrier\n"
    "buffer_store_dwordx4 v[88:91], v226, s[0:3], 0 offen sc1\n"
    ".Lb2_mk6x15_%=:\n"
    ".Lb2_nx_%=:\n"
    "s_mov_b32 s26, s27\n"
    "s_cmp_lt_u32 s26, 64\n"
    "s_cbranch_scc1 .Lb2_top_%=\n"
    "s_waitcnt vmcnt(0) lgkmcnt(0)\n"
    "s_endpgm\n"
    ".Lb2_out_%=:\n"
    "v_cvt_pk_bf16_f32 v56, v0, v1\n"
    "v_cvt_pk_bf16_f32 v57, v2, v3\n"
    "v_cvt_pk_bf16_f32 v58, v4, v5\n"
    "v_cvt_pk_bf16_f32 v59, v6, v7\n"
    "v_cvt_pk_bf16_f32 v60, v8, v9\n"
    "v_cvt_pk_bf16_f32 v61, v10, v11\n"
    "v_cvt_pk_bf16_f32 v62, v12, v13\n"
    "v_cvt_pk_bf16_f32 v63, v14, v15\n"
    "buffer_store_dwordx4 v[56:59], v228, s[8:11], s30 offen\n"
    "buffer_store_dwordx4 v[60:63], v229, s[8:11], s30 offen\n"
    "v_mov_b32 v84, 0\n"
    "s_branch .Lb2_nx_%=\n"
    ".Lb2_sr_%=:\n"
    "s_add_u32 s20, s20, 1\n"
    "s_cmp_lt_u32 s20, 0x40000\n"
    "s_cbranch_scc1 .Lb2_sp17_%=\n"
    "v_mov_b32 v84, 72\n"
    "s_mov_b32 s24, 33728\n"
    "buffer_store_dword v84, off, s[0:3], s24 sc1\n"
    "s_waitcnt vmcnt(0)\n"
    "s_branch .Lb2_sg_%=\n"
    ".Lb2_sp17_%=:\n"
    "s_sleep 2\n"
    "buffer_load_dwordx4 v[64:67], v231, s[0:3], s32 offen sc1\n"
    "buffer_load_dwordx4 v[68:71], v232, s[0:3], s32 offen sc1\n"
    "buffer_load_dwordx4 v[72:75], v233, s[0:3], s32 offen sc1\n"
    "buffer_load_dwordx4 v[76:79], v234, s[0:3], s32 offen sc1\n"
    "buffer_load_dwordx4 v[80:83], v235, s[0:3], s32 offen sc1\n"
    "s_waitcnt vmcnt(0)\n"
    "s_branch .Lb2_sl_%=\n"
    :: "{s[0:3]}"(s0_), "{s[4:7]}"(s4_), "{s[8:11]}"(s8_), "{s[12:15]}"(s12_), "{s[16:19]}"(s16_), "{v[0:3]}"(v0_), "{v[4:7]}"(v4_), "{v[224:227]}"(v224_), "{v[228:231]}"(v228_), "{v[232:235]}"(v232_) : "memory", "v255", "a255", "s95");
    __builtin_unreachable();
}
constexpr int MEGA_LDS = 163840;
__global__ void __launch_bounds__(256, 1) mega_kernel(MegaArgs A) {
    extern __shared__ __attribute__((aligned(16))) char smem[];
    int* sRole = (int*)smem;
    const int x4 = (int)(__builtin_amdgcn_s_getreg((3 << 11) | 20) & 3u);
    if (threadIdx.x < 64) {
        const rsrc_t rf = mk_rsrc(arena_base(A.ar), ARENA);
        unsigned tm[2] = {~0u, ~0u};
#pragma unroll
        for (int k = 0; k < 4; ++k) {
            const unsigned long long t0 = __builtin_amdgcn_s_memtime();
            if (threadIdx.x == 0) bst4(rf, (blockIdx.x >> 7) * 16384 + (blockIdx.x & 127) * 64 + (k & 1) * 8192, 0u);
            DRAIN();
            const unsigned d = (unsigned)(__builtin_amdgcn_s_memtime() - t0);
            tm[k & 1] = d < tm[k & 1] ? d : tm[k & 1];
        }
        if (threadIdx.x == 0) {
            unsigned* cnt = (unsigned*)(arena_base(A.ar) + xl(Z_TK, 0));
            int role = -1;
            auto take = [&](int pool) {
                if (role >= 0) return;
                const int k = (int)__hip_atomic_fetch_add(cnt + 16 * pool, 1u, RLX_AGENT), x = pool & 3;
                if (k < 32) role = (pool >> 2) * 128 + (k < 24 ? (k >> 3) * 32 + 8 * x + (k & 7) : 96 + ((k - 24) >> 2) * 16 + 4 * x + (k & 3));
            };
            take((tm[1] < tm[0] ? 4 : 0) + x4);
            for (int p = 0; p < 8; ++p) take(p);
            *sRole = role;
        }
    }
    __syncthreads();
    const int role = RFL(*sRole), L = role >> 7, r = role & 127;
    __syncthreads();
    const bool on = (A.layers >> L) & 1;
    if (r < 32) { if (on) qk_role(A, L, r, x4); }
    else if (r < 64) { if (on) vo_role(A, L, r - 32, x4); }
    else if (r < 96) ct_role(A, L, r - 64);
    else if (r < 112) { if (on) { if (r < 107) b1_role(A, L, r - 96, x4); else b2_role(A, L, r - 107, x4); } }
    else qw_role(A, L, 8 * ((r - 112) >> 2) + 4 * L + (r & 3), x4);
}

#define LAYERS 3
extern "C" void kernel_launch(void* const* d_in, const int* in_sizes, int n_in, void* d_out, int out_size, void* d_ws, size_t ws_size, hipStream_t stream) {
    MegaArgs A;
    A.x0 = (const bf16_t*)d_in[0]; A.mem0 = (const bf16_t*)d_in[1]; A.Wq = (const bf16_t*)d_in[2]; A.Wk = (const bf16_t*)d_in[3]; A.Wv = (const bf16_t*)d_in[4];
    A.Wo = (const bf16_t*)d_in[5]; A.wqkv = (const bf16_t*)d_in[8]; A.wo = (const bf16_t*)d_in[9];
    A.ar = (char*)d_ws + 266240;
    A.out = (bf16_t*)d_out; A.layers = LAYERS; A.pad = 0;
    (void)hipFuncSetAttribute((const void*)mega_kernel, hipFuncAttributeMaxDynamicSharedMemorySize, MEGA_LDS);
    k_zero<<<2 * (Z_END - Z_TK) / 4096, 256, 0, stream>>>(A.ar);
    mega_kernel<<<256, 256, MEGA_LDS, stream>>>(A);
}
```

```cpp
#include <hip/hip_runtime.h>
#include <hip/hip_bf16.h>
#include <stdint.h>

typedef unsigned short bf16_t;
typedef short bf16x8 __attribute__((ext_vector_type(8)));
typedef short bf16x4 __attribute__((ext_vector_type(4)));
typedef float f32x4 __attribute__((ext_vector_type(4)));
typedef unsigned u32x4 __attribute__((ext_vector_type(4)));
typedef unsigned u32x2 __attribute__((ext_vector_type(2)));
typedef unsigned long long u64;
typedef __bf16 bf16v2 __attribute__((ext_vector_type(2)));
typedef float f32v2 __attribute__((ext_vector_type(2)));
#define DEV __device__ __forceinline__
DEV unsigned pk2(float a, float b) { f32v2 f = {a, b}; bf16v2 r = __builtin_convertvector(f, bf16v2); return __builtin_bit_cast(unsigned, r); }
DEV float rb(float a) { return __uint_as_float((pk2(a, 0.f) & 0xffffu) << 16); }
DEV float lo16(unsigned u) { return __uint_as_float(u << 16); }
DEV float hi16(unsigned u) { return __uint_as_float(u & 0xffff0000u); }
constexpr int D_ = 640;
#define RLX_AGENT __ATOMIC_RELAXED, __HIP_MEMORY_SCOPE_AGENT
#define AUX_SC1 16
typedef __amdgpu_buffer_rsrc_t rsrc_t;
DEV rsrc_t mk_rsrc(const void* p, unsigned bytes) { return __builtin_amdgcn_make_buffer_rsrc((void*)p, 0, (int)bytes, 0x00020000); }
DEV void bst16(rsrc_t r, unsigned off, u32x4 v) { __builtin_amdgcn_raw_buffer_store_b128(v, r, off, 0, AUX_SC1); }
DEV void bst8(rsrc_t r, unsigned off, u32x2 v) { __builtin_amdgcn_raw_buffer_store_b64(v, r, off, 0, AUX_SC1); }
DEV void bst4(rsrc_t r, unsigned off, unsigned v) { __builtin_amdgcn_raw_buffer_store_b32(v, r, off, 0, AUX_SC1); }
#define DRAIN() asm volatile("s_waitcnt vmcnt(0)" ::: "memory")
#define DRAIN_BARRIER() asm volatile("s_waitcnt vmcnt(0) lgkmcnt(0)\n\ts_barrier" ::: "memory")
#define RFL(x) ((unsigned)__builtin_amdgcn_readfirstlane((int)(x)))
DEV unsigned lo(const void* p) { return (unsigned)(size_t)p; }
DEV unsigned hi(const void* p) { return (unsigned)((size_t)p >> 32) & 0xffffu; }
DEV unsigned nosent(unsigned x) { return min(x, 0xfffffffeu); }
DEV int sw2(int x) { return ((x & 1) << 1) | (x >> 1); }

DEV unsigned xl(unsigned o, int side) { const unsigned ph = ((o >> 13) << 14) | (o & 8191u); return ph | ((((ph >> 21) ^ side) & 1u) << 13); }
constexpr unsigned Z_TK = 16384, Z_QV = Z_TK + 1024, Z_CT = Z_QV + 1024, Z_QW = Z_CT + 2048, Z_X = 3 * 8192, Z_X1 = 7 * 8192, Z_XN = 9 * 8192, Z_O = 10 * 8192, Z_ST = 11 * 8192, Z_END = 12 * 8192;
constexpr unsigned RG_UT = 1u << 21, RG_UTX = RG_UT + (1u << 20), RG_UM = RG_UTX + 262144, RG_UMX = RG_UM + 262144, RG_PT = RG_UMX + 65536, RG_PM = RG_PT + 65536, RG_MEM = RG_PM + 32768;
constexpr unsigned SLOT = 1u << 22;
constexpr unsigned REC0 = 20u << 20, RC_X1 = 5 * 16384, RC_O = 10 * 16384, RC_X2 = 15 * 16384, ST0 = 1u << 20, QT0 = 52u << 20, DBG0 = 116u << 20, ARENA = 120u << 20;
DEV unsigned rec(int s, int L) { return REC0 + (s << 19) + ((((s >> 2) ^ L) & 1) << 13); }
constexpr unsigned OOB = 0x7f000000u, WBYTES = 640u * 6144u * 2u;
DEV char* arena_base(char* p) { return (char*)(((size_t)p + 0x3fffff) & ~(size_t)0x3fffff); }

struct MegaArgs {
    const bf16_t* x0; const bf16_t* mem0; const bf16_t* Wq; const bf16_t* Wk; const bf16_t* Wv; const bf16_t* Wo; const bf16_t* wqkv; const bf16_t* wo;
    char* ar; bf16_t* out; int layers, pad;
};
__global__ void __launch_bounds__(256) k_zero(char* ar) {
    *(u32x4*)(arena_base(ar) + 2 * Z_TK + (blockIdx.x * 256 + threadIdx.x) * 16) = u32x4{0u, 0u, 0u, 0u};
}
#define TS 128
template <int BY> struct Touch {
    static constexpr int PR = BY / TS, N = (640 * PR + 255) / 256;
    unsigned v[N], o[N];
    DEV void plan(bool quarters) {
#pragma unroll
        for (int k = 0; k < N; ++k) {
            const int i = min(k * 256 + (int)threadIdx.x, 640 * PR - 1), r = i / PR, s5 = r >> 5;
            o[k] = ((quarters && s5 < 16 ? 4 * (s5 & 3) + (s5 >> 2) : s5) * 32 + (r & 31)) * 12288 + (i % PR) * TS;
        }
    }
    DEV void go(const bf16_t* W) {
#pragma unroll
        for (int k = 0; k < N; ++k) asm volatile("global_load_dword %0, %1, %2 nt" : "=&v"(v[k]) : "v"(o[k]), "s"(W) : "memory");
    }
    DEV void done() {
        asm volatile("s_waitcnt vmcnt(0)" ::: "memory");
#pragma unroll
        for (int k = 0; k < N; ++k) asm volatile("" :: "v"(v[k]));
    }
};
#define ROLE_HEAD const int l = threadIdx.x & 63, l16 = l & 15, lq = l >> 4, w = RFL(threadIdx.x >> 6); char* const ab = arena_base(A.ar); \
    const rsrc_t rr = mk_rsrc(ab, ARENA); const u32x4 rx = {lo(ab), hi(ab), ARENA, 0x20000u}; const u32x4 ones = {~0u, ~0u, ~0u, ~0u}; const u32x2 ones2 = {~0u, ~0u}
DEV void qk_role(const MegaArgs& A, const int L, const int h, const int rep) {
    ROLE_HEAD;
#define XP 10
    const int i = 4 * (w & 1) + lq, p = l16 < 8 ? l16 >> 2 : l16 & 1, rho = 4 * (i >> 1) + 2 * p + (i & 1), tp = 4 * w + lq;
    const unsigned pm = (w >= 2 || l16 >= XP) ? OOB : xl(RG_PM + (h >> 3) * 8192 +
        (l16 < 8 ? ((h & 7) >> 1) * 1024 + ((2 * (h & 1) + ((l16 >> 1) & 1)) * 16 + rho) * 16 + (l16 & 1) * 8 : 4096 + (((h & 7) >> 1) * 16 + rho) * 16 + (h & 1) * 8), L);
    const unsigned pt = xl(RG_PT + h * 2048 + tp * 16 + (l16 < 8 ? (l16 & 3) * 256 + (l16 >> 2) * 8 : (l16 & 1) * 8 + (l16 < 10 ? 1792 : (l16 < 14 ? 1024 + ((l16 - 10) >> 1) * 256 : 1536))), L);
    u64 cm[4];
    const int k0 = (l16 >= 4 && l16 < 8) ? 4 * (l16 - 4) : ((l16 >= 10 && l16 < 14) ? 4 * (l16 - 10) : -4);
#pragma unroll
    for (int r = 0; r < 4; ++r) cm[r] = __ballot(k0 + r <= tp);
    const u32x4 s0_ = {RFL(rx[0]), RFL(rx[1]), RFL(rx[2]), RFL(rx[3])};
    const u32x4 s4_ = {RFL(lo(A.Wk + h * 192)), RFL(hi(A.Wk + h * 192)), RFL(WBYTES - h * 384), RFL(0x20000u)};
    const u32x4 s8_ = {RFL(lo(A.Wq + h * 192)), RFL(hi(A.Wq + h * 192)), RFL(WBYTES - h * 384), RFL(0x20000u)};
    const u32x4 s12_ = {RFL((unsigned)cm[0]), RFL((unsigned)(cm[0] >> 32)), RFL((unsigned)cm[1]), RFL((unsigned)(cm[1] >> 32))};
    const u32x4 s16_ = {RFL((unsigned)cm[2]), RFL((unsigned)(cm[2] >> 32)), RFL((unsigned)cm[3]), RFL((unsigned)(cm[3] >> 32))};
    const u32x4 s20_ = {RFL(w < 2 ? ~0u : 1u), RFL(w < 2 ? ~0u : 0u), RFL(w), RFL(L << 13)};
    const u32x4 s24_ = {RFL(xl(RG_MEM + rep * 16384, L) + w * 2048), RFL(w < 2 ? xl(RG_MEM + rep * 16384 + 8192, L) + w * 1024 - 2048 : OOB), RFL(w * 11264 + 8192), RFL(REC0 + w * 16384)};
    const u32x4 s28_ = {RFL(65536 - 4096 - w * 14336), RFL(QT0 + h * 32768 + w * 2048), RFL(h == 0 && w == 0 ? xl(Z_X, L) : OOB), RFL(xl(Z_X, L))};
    const u32x4 s32_ = {RFL(xl(Z_CT + (h & 7) * 256, L)), RFL(xl(Z_XN + rep * 2048, 0)), RFL(xl(Z_QW + h * 128, L)), RFL(w == 0 ? xl(Z_QV + h * 4, L) : OOB)};
    const u32x4 s36_ = {RFL(DBG0 + ((L * 128 + h) * 4 + w) * 2048), RFL(0u), RFL(0u), RFL(0u)};
    const u32x4 v0_ = {(unsigned)(((3 * w + 0) * 64 + l) / 24 * 12288 + ((((3 * w + 0) * 64 + l) % 24) ^ ((((3 * w + 0) * 64 + l) / 48) & 6)) * 16 - 0), (unsigned)(((3 * w + 1) * 64 + l) / 24 * 12288 + ((((3 * w + 1) * 64 + l) % 24) ^ ((((3 * w + 1) * 64 + l) / 48) & 6)) * 16 - 1024), (unsigned)(((3 * w + 2) * 64 + l) / 24 * 12288 + ((((3 * w + 2) * 64 + l) % 24) ^ ((((3 * w + 2) * 64 + l) / 48) & 6)) * 16 - 2048), (unsigned)((8 * lq + ((l >> 2) & 1) + ((l >> 1) & 4)) * 384 + (w ^ (((8 * lq + ((l >> 2) & 1) + ((l >> 1) & 4)) >> 2) & 3)) * 32 + (l & 3) * 8 + ((w + 0) & 3) * 12288)};
    const u32x4 v4_ = {(unsigned)((8 * lq + ((l >> 2) & 1) + ((l >> 1) & 4)) * 384 + (w ^ (((8 * lq + ((l >> 2) & 1) + ((l >> 1) & 4)) >> 2) & 3)) * 32 + (l & 3) * 8 + ((w + 1) & 3) * 12288), (unsigned)((8 * lq + ((l >> 2) & 1) + ((l >> 1) & 4)) * 384 + (w ^ (((8 * lq + ((l >> 2) & 1) + ((l >> 1) & 4)) >> 2) & 3)) * 32 + (l & 3) * 8 + ((w + 2) & 3) * 12288), (unsigned)((8 * lq + ((l >> 2) & 1) + ((l >> 1) & 4)) * 384 + (w ^ (((8 * lq + ((l >> 2) & 1) + ((l >> 1) & 4)) >> 2) & 3)) * 32 + (l & 3) * 8 + ((w + 3) & 3) * 12288), (unsigned)((8 * lq + ((l >> 2) & 1) + ((l >> 1) & 4)) * 384 + (w ^ (((8 * lq + ((l >> 2) & 1) + ((l >> 1) & 4)) >> 2) & 3)) * 32 + (l & 3) * 8)};
    const u32x4 v92_ = {(unsigned)(l * 16 + ((w + 0) & 3) * 11264), (unsigned)(l * 16 + ((w + 1) & 3) * 11264), (unsigned)(l * 16 + ((w + 2) & 3) * 11264), (unsigned)(l * 16 + ((w + 3) & 3) * 11264)};
    const u32x4 v96_ = {(unsigned)(l * 16), (unsigned)(l * 16 + 53248 + w * 20480), (unsigned)((l & 2) ? 0xeeeeu : 0x4444u), (unsigned)(0xff800000u)};
    const u32x4 v100_ = {(unsigned)(l16 == 0 + 4 * lq ? 0x3f80u : (l16 == 0 + 4 * lq + 1 ? 0x3f800000u : 0u)), (unsigned)(l16 == 2 + 4 * lq ? 0x3f80u : (l16 == 2 + 4 * lq + 1 ? 0x3f800000u : 0u)), (unsigned)(135168 + w * 1936 + (2 * (l16 >> 2) + (l & 1)) * 240 + (l & 2) * 32 + lq * 16), (unsigned)(135168 + (l16 < 10 ? (4 * (w & 1) + lq) * 240 + l16 * 16 : 1920))};
    const u32x4 v104_ = {(unsigned)(142912 + w * 4352 + l16 * 272 + lq * 16), (unsigned)(142912 + (4 * w + lq) * 272 + l16 * 16), (unsigned)(pm), (unsigned)(pt)};
    asm volatile(
    "s_mov_b32 s40, 0\n"
    "s_cmp_eq_u32 s23, 0\n"
    "s_cselect_b32 s44, 0, 24\n"
    ".Lqk_ho1_%=:\n"
    "s_sub_u32 s44, s44, 1\n"
    "s_cbranch_scc1 .Lqk_hd2_%=\n"
    "s_sleep 17\n"
    "s_branch .Lqk_ho1_%=\n"
    ".Lqk_hd2_%=:\n"
    "s_mul_i32 s51, s22, 3072\n"
    "s_mov_b32 s60, 0\n"
    "s_add_u32 m0, s51, 0\n"
    "s_nop 0\n"
    "buffer_load_dwordx4 v0, s[8:11], s60 offen offset:0 nt lds\n"
    "buffer_load_dwordx4 v1, s[8:11], s60 offen offset:1024 nt lds\n"
    "buffer_load_dwordx4 v2, s[8:11], s60 offen offset:2048 nt lds\n"
    "s_mov_b32 s60, 1572864\n"
    "s_add_u32 m0, s51, 12288\n"
    "s_nop 0\n"
    "buffer_load_dwordx4 v0, s[8:11], s60 offen offset:0 nt lds\n"
    "buffer_load_dwordx4 v1, s[8:11], s60 offen offset:1024 nt lds\n"
    "buffer_load_dwordx4 v2, s[8:11], s60 offen offset:2048 nt lds\n"
    "s_mov_b32 s60, 3145728\n"
    "s_add_u32 m0, s51, 24576\n"
    "s_nop 0\n"
    "buffer_load_dwordx4 v0, s[8:11], s60 offen offset:0 nt lds\n"
    "buffer_load_dwordx4 v1, s[8:11], s60 offen offset:1024 nt lds\n"
    "buffer_load_dwordx4 v2, s[8:11], s60 offen offset:2048 nt lds\n"
    "s_mov_b32 s60, 4718592\n"
    "s_add_u32 m0, s51, 36864\n"
    "s_nop 0\n"
    "buffer_load_dwordx4 v0, s[8:11], s60 offen offset:0 nt lds\n"
    "buffer_load_dwordx4 v1, s[8:11], s60 offen offset:1024 nt lds\n"
    "buffer_load_dwordx4 v2, s[8:11], s60 offen offset:2048 nt lds\n"
    "s_mov_b32 s60, 393216\n"
    "s_add_u32 m0, s51, 49152\n"
    "s_nop 0\n"
    "buffer_load_dwordx4 v0, s[8:11], s60 offen offset:0 nt lds\n"
    "buffer_load_dwordx4 v1, s[8:11], s60 offen offset:1024 nt lds\n"
    "buffer_load_dwordx4 v2, s[8:11], s60 offen offset:2048 nt lds\n"
    "s_mov_b32 s60, 1966080\n"
    "s_add_u32 m0, s51, 61440\n"
    "s_nop 0\n"
    "buffer_load_dwordx4 v0, s[8:11], s60 offen offset:0 nt lds\n"
    "buffer_load_dwordx4 v1, s[8:11], s60 offen offset:1024 nt lds\n"
    "buffer_load_dwordx4 v2, s[8:11], s60 offen offset:2048 nt lds\n"
    "s_mov_b32 s60, 3538944\n"
    "s_add_u32 m0, s51, 73728\n"
    "s_nop 0\n"
    "buffer_load_dwordx4 v0, s[8:11], s60 offen offset:0 nt lds\n"
    "buffer_load_dwordx4 v1, s[8:11], s60 offen offset:1024 nt lds\n"
    "buffer_load_dwordx4 v2, s[8:11], s60 offen offset:2048 nt lds\n"
    "s_mov_b32 s60, 5111808\n"
    "s_add_u32 m0, s51, 86016\n"
    "s_nop 0\n"
    "buffer_load_dwordx4 v0, s[8:11], s60 offen offset:0 nt lds\n"
    "buffer_load_dwordx4 v1, s[8:11], s60 offen offset:1024 nt lds\n"
    "buffer_load_dwordx4 v2, s[8:11], s60 offen offset:2048 nt lds\n"
    "s_mov_b32 s60, 786432\n"
    "s_add_u32 m0, s51, 98304\n"
    "s_nop 0\n"
    "buffer_load_dwordx4 v0, s[8:11], s60 offen offset:0 nt lds\n"
    "buffer_load_dwordx4 v1, s[8:11], s60 offen offset:1024 nt lds\n"
    "buffer_load_dwordx4 v2, s[8:11], s60 offen offset:2048 nt lds\n"
    "s_mov_b32 s60, 2359296\n"
    "s_add_u32 m0, s51, 110592\n"
    "s_nop 0\n"
    "buffer_load_dwordx4 v0, s[8:11], s60 offen offset:0 nt lds\n"
    "buffer_load_dwordx4 v1, s[8:11], s60 offen offset:1024 nt lds\n"
    "buffer_load_dwordx4 v2, s[8:11], s60 offen offset:2048 nt lds\n"
    "s_mov_b32 s60, 3932160\n"
    "s_add_u32 m0, s51, 122880\n"
    "s_nop 0\n"
    "buffer_load_dwordx4 v0, s[8:11], s60 offen offset:0 nt lds\n"
    "buffer_load_dwordx4 v1, s[8:11], s60 offen offset:1024 nt lds\n"
    "buffer_load_dwordx4 v2, s[8:11], s60 offen offset:2048 nt lds\n"
    "s_mov_b32 s60, 5505024\n"
    "s_add_u32 m0, s51, 135168\n"
    "s_nop 0\n"
    "buffer_load_dwordx4 v0, s[8:11], s60 offen offset:0 nt lds\n"
    "buffer_load_dwordx4 v1, s[8:11], s60 offen offset:1024 nt lds\n"
    "buffer_load_dwordx4 v2, s[8:11], s60 offen offset:2048 nt lds\n"
    "v_mov_b64 v[108:109], -1\n"
    "buffer_store_dwordx2 v[108:109], v106, s[0:3], 0 offen sc1\n"
    "buffer_store_dwordx2 v[108:109], v107, s[0:3], 0 offen sc1\n"
    "s_waitcnt vmcnt(26)\n"
    "s_barrier\n"
    "v_add_u32 v8, 0, v3\n"
    "ds_read_b64_tr_b16 a[240:241], v8 offset:0\n"
    "ds_read_b64_tr_b16 a[242:243], v8 offset:768\n"
    "ds_read_b64_tr_b16 v[176:177], v8 offset:128\n"
    "ds_read_b64_tr_b16 v[178:179], v8 offset:896\n"
    "ds_read_b64_tr_b16 v[12:13], v8 offset:256\n"
    "ds_read_b64_tr_b16 v[14:15], v8 offset:1024\n"
    "v_add_u32 v9, 0, v4\n"
    "ds_read_b64_tr_b16 v[112:113], v9 offset:0\n"
    "ds_read_b64_tr_b16 v[114:115], v9 offset:768\n"
    "ds_read_b64_tr_b16 v[192:193], v9 offset:128\n"
    "ds_read_b64_tr_b16 v[194:195], v9 offset:896\n"
    "ds_read_b64_tr_b16 v[28:29], v9 offset:256\n"
    "ds_read_b64_tr_b16 v[30:31], v9 offset:1024\n"
    "v_add_u32 v10, 0, v5\n"
    "ds_read_b64_tr_b16 v[128:129], v10 offset:0\n"
    "ds_read_b64_tr_b16 v[130:131], v10 offset:768\n"
    "ds_read_b64_tr_b16 v[208:209], v10 offset:128\n"
    "ds_read_b64_tr_b16 v[210:211], v10 offset:896\n"
    "ds_read_b64_tr_b16 v[44:45], v10 offset:256\n"
    "ds_read_b64_tr_b16 v[46:47], v10 offset:1024\n"
    "v_add_u32 v11, 0, v6\n"
    "ds_read_b64_tr_b16 v[144:145], v11 offset:0\n"
    "ds_read_b64_tr_b16 v[146:147], v11 offset:768\n"
    "ds_read_b64_tr_b16 v[224:225], v11 offset:128\n"
    "ds_read_b64_tr_b16 v[226:227], v11 offset:896\n"
    "ds_read_b64_tr_b16 v[60:61], v11 offset:256\n"
    "ds_read_b64_tr_b16 v[62:63], v11 offset:1024\n"
    "s_waitcnt lgkmcnt(0)\n"
    "s_barrier\n"
    "s_mov_b32 s60, 1179648\n"
    "s_add_u32 m0, s51, 0\n"
    "s_nop 0\n"
    "buffer_load_dwordx4 v0, s[8:11], s60 offen offset:0 nt lds\n"
    "buffer_load_dwordx4 v1, s[8:11], s60 offen offset:1024 nt lds\n"
    "buffer_load_dwordx4 v2, s[8:11], s60 offen offset:2048 nt lds\n"
    "s_mov_b32 s60, 2752512\n"
    "s_add_u32 m0, s51, 12288\n"
    "s_nop 0\n"
    "buffer_load_dwordx4 v0, s[8:11], s60 offen offset:0 nt lds\n"
    "buffer_load_dwordx4 v1, s[8:11], s60 offen offset:1024 nt lds\n"
    "buffer_load_dwordx4 v2, s[8:11], s60 offen offset:2048 nt lds\n"
    "s_mov_b32 s60, 4325376\n"
    "s_add_u32 m0, s51, 24576\n"
    "s_nop 0\n"
    "buffer_load_dwordx4 v0, s[8:11], s60 offen offset:0 nt lds\n"
    "buffer_load_dwordx4 v1, s[8:11], s60 offen offset:1024 nt lds\n"
    "buffer_load_dwordx4 v2, s[8:11], s60 offen offset:2048 nt lds\n"
    "s_mov_b32 s60, 5898240\n"
    "s_add_u32 m0, s51, 36864\n"
    "s_nop 0\n"
    "buffer_load_dwordx4 v0, s[8:11], s60 offen offset:0 nt lds\n"
    "buffer_load_dwordx4 v1, s[8:11], s60 offen offset:1024 nt lds\n"
    "buffer_load_dwordx4 v2, s[8:11], s60 offen offset:2048 nt lds\n"
    "s_waitcnt vmcnt(26)\n"
    "s_barrier\n"
    "v_add_u32 v8, 49152, v3\n"
    "ds_read_b64_tr_b16 a[244:245], v8 offset:0\n"
    "ds_read_b64_tr_b16 a[246:247], v8 offset:768\n"
    "ds_read_b64_tr_b16 v[180:181], v8 offset:128\n"
    "ds_read_b64_tr_b16 v[182:183], v8 offset:896\n"
    "ds_read_b64_tr_b16 v[16:17], v8 offset:256\n"
    "ds_read_b64_tr_b16 v[18:19], v8 offset:1024\n"
    "v_add_u32 v9, 49152, v4\n"
    "ds_read_b64_tr_b16 v[116:117], v9 offset:0\n"
    "ds_read_b64_tr_b16 v[118:119], v9 offset:768\n"
    "ds_read_b64_tr_b16 v[196:197], v9 offset:128\n"
    "ds_read_b64_tr_b16 v[198:199], v9 offset:896\n"
    "ds_read_b64_tr_b16 v[32:33], v9 offset:256\n"
    "ds_read_b64_tr_b16 v[34:35], v9 offset:1024\n"
    "v_add_u32 v10, 49152, v5\n"
    "ds_read_b64_tr_b16 v[132:133], v10 offset:0\n"
    "ds_read_b64_tr_b16 v[134:135], v10 offset:768\n"
    "ds_read_b64_tr_b16 v[212:213], v10 offset:128\n"
    "ds_read_b64_tr_b16 v[214:215], v10 offset:896\n"
    "ds_read_b64_tr_b16 v[48:49], v10 offset:256\n"
    "ds_read_b64_tr_b16 v[50:51], v10 offset:1024\n"
    "v_add_u32 v11, 49152, v6\n"
    "ds_read_b64_tr_b16 v[148:149], v11 offset:0\n"
    "ds_read_b64_tr_b16 v[150:151], v11 offset:768\n"
    "ds_read_b64_tr_b16 v[228:229], v11 offset:128\n"
    "ds_read_b64_tr_b16 v[230:231], v11 offset:896\n"
    "ds_read_b64_tr_b16 v[64:65], v11 offset:256\n"
    "ds_read_b64_tr_b16 v[66:67], v11 offset:1024\n"
    "s_waitcnt lgkmcnt(0)\n"
    "s_barrier\n"
    "s_mov_b32 s60, 6291456\n"
    "s_add_u32 m0, s51, 49152\n"
    "s_nop 0\n"
    "buffer_load_dwordx4 v0, s[8:11], s60 offen offset:0 nt lds\n"
    "buffer_load_dwordx4 v1, s[8:11], s60 offen offset:1024 nt lds\n"
    "buffer_load_dwordx4 v2, s[8:11], s60 offen offset:2048 nt lds\n"
    "s_mov_b32 s60, 6684672\n"
    "s_add_u32 m0, s51, 61440\n"
    "s_nop 0\n"
    "buffer_load_dwordx4 v0, s[8:11], s60 offen offset:0 nt lds\n"
    "buffer_load_dwordx4 v1, s[8:11], s60 offen offset:1024 nt lds\n"
    "buffer_load_dwordx4 v2, s[8:11], s60 offen offset:2048 nt lds\n"
    "s_mov_b32 s60, 7077888\n"
    "s_add_u32 m0, s51, 73728\n"
    "s_nop 0\n"
    "buffer_load_dwordx4 v0, s[8:11], s60 offen offset:0 nt lds\n"
    "buffer_load_dwordx4 v1, s[8:11], s60 offen offset:1024 nt lds\n"
    "buffer_load_dwordx4 v2, s[8:11], s60 offen offset:2048 nt lds\n"
    "s_mov_b32 s60, 7471104\n"
    "s_add_u32 m0, s51, 86016\n"
    "s_nop 0\n"
    "buffer_load_dwordx4 v0, s[8:11], s60 offen offset:0 nt lds\n"
    "buffer_load_dwordx4 v1, s[8:11], s60 offen offset:1024 nt lds\n"
    "buffer_load_dwordx4 v2, s[8:11], s60 offen offset:2048 nt lds\n"
    "s_waitcnt vmcnt(26)\n"
    "s_barrier\n"
    "v_add_u32 v8, 98304, v3\n"
    "ds_read_b64_tr_b16 a[248:249], v8 offset:0\n"
    "ds_read_b64_tr_b16 a[250:251], v8 offset:768\n"
    "ds_read_b64_tr_b16 v[184:185], v8 offset:128\n"
    "ds_read_b64_tr_b16 v[186:187], v8 offset:896\n"
    "ds_read_b64_tr_b16 v[20:21], v8 offset:256\n"
    "ds_read_b64_tr_b16 v[22:23], v8 offset:1024\n"
    "v_add_u32 v9, 98304, v4\n"
    "ds_read_b64_tr_b16 v[120:121], v9 offset:0\n"
    "ds_read_b64_tr_b16 v[122:123], v9 offset:768\n"
    "ds_read_b64_tr_b16 v[200:201], v9 offset:128\n"
    "ds_read_b64_tr_b16 v[202:203], v9 offset:896\n"
    "ds_read_b64_tr_b16 v[36:37], v9 offset:256\n"
    "ds_read_b64_tr_b16 v[38:39], v9 offset:1024\n"
    "v_add_u32 v10, 98304, v5\n"
    "ds_read_b64_tr_b16 v[136:137], v10 offset:0\n"
    "ds_read_b64_tr_b16 v[138:139], v10 offset:768\n"
    "ds_read_b64_tr_b16 v[216:217], v10 offset:128\n"
    "ds_read_b64_tr_b16 v[218:219], v10 offset:896\n"
    "ds_read_b64_tr_b16 v[52:53], v10 offset:256\n"
    "ds_read_b64_tr_b16 v[54:55], v10 offset:1024\n"
    "v_add_u32 v11, 98304, v6\n"
    "ds_read_b64_tr_b16 v[152:153], v11 offset:0\n"
    "ds_read_b64_tr_b16 v[154:155], v11 offset:768\n"
    "ds_read_b64_tr_b16 v[232:233], v11 offset:128\n"
    "ds_read_b64_tr_b16 v[234:235], v11 offset:896\n"
    "ds_read_b64_tr_b16 v[68:69], v11 offset:256\n"
    "ds_read_b64_tr_b16 v[70:71], v11 offset:1024\n"
    "s_waitcnt lgkmcnt(0)\n"
    "s_barrier\n"
    "s_mov_b32 s60, 0\n"
    "s_add_u32 m0, s51, 98304\n"
    "s_nop 0\n"
    "buffer_load_dwordx4 v0, s[4:7], s60 offen offset:0 nt lds\n"
    "buffer_load_dwordx4 v1, s[4:7], s60 offen offset:1024 nt lds\n"
    "buffer_load_dwordx4 v2, s[4:7], s60 offen offset:2048 nt lds\n"
    "s_mov_b32 s60, 1572864\n"
    "s_add_u32 m0, s51, 110592\n"
    "s_nop 0\n"
    "buffer_load_dwordx4 v0, s[4:7], s60 offen offset:0 nt lds\n"
    "buffer_load_dwordx4 v1, s[4:7], s60 offen offset:1024 nt lds\n"
    "buffer_load_dwordx4 v2, s[4:7], s60 offen offset:2048 nt lds\n"
    "s_mov_b32 s60, 3145728\n"
    "s_add_u32 m0, s51, 122880\n"
    "s_nop 0\n"
    "buffer_load_dwordx4 v0, s[4:7], s60 offen offset:0 nt lds\n"
    "buffer_load_dwordx4 v1, s[4:7], s60 offen offset:1024 nt lds\n"
    "buffer_load_dwordx4 v2, s[4:7], s60 offen offset:2048 nt lds\n"
    "s_mov_b32 s60, 4718592\n"
    "s_add_u32 m0, s51, 135168\n"
    "s_nop 0\n"
    "buffer_load_dwordx4 v0, s[4:7], s60 offen offset:0 nt lds\n"
    "buffer_load_dwordx4 v1, s[4:7], s60 offen offset:1024 nt lds\n"
    "buffer_load_dwordx4 v2, s[4:7], s60 offen offset:2048 nt lds\n"
    "s_waitcnt vmcnt(24)\n"
    "s_barrier\n"
    "v_add_u32 v8, 0, v3\n"
    "ds_read_b64_tr_b16 a[252:253], v8 offset:0\n"
    "ds_read_b64_tr_b16 a[254:255], v8 offset:768\n"
    "ds_read_b64_tr_b16 v[188:189], v8 offset:128\n"
    "ds_read_b64_tr_b16 v[190:191], v8 offset:896\n"
    "ds_read_b64_tr_b16 v[24:25], v8 offset:256\n"
    "ds_read_b64_tr_b16 v[26:27], v8 offset:1024\n"
    "v_add_u32 v9, 0, v4\n"
    "ds_read_b64_tr_b16 v[124:125], v9 offset:0\n"
    "ds_read_b64_tr_b16 v[126:127], v9 offset:768\n"
    "ds_read_b64_tr_b16 v[204:205], v9 offset:128\n"
    "ds_read_b64_tr_b16 v[206:207], v9 offset:896\n"
    "ds_read_b64_tr_b16 v[40:41], v9 offset:256\n"
    "ds_read_b64_tr_b16 v[42:43], v9 offset:1024\n"
    "v_add_u32 v10, 0, v5\n"
    "ds_read_b64_tr_b16 v[140:141], v10 offset:0\n"
    "ds_read_b64_tr_b16 v[142:143], v10 offset:768\n"
    "ds_read_b64_tr_b16 v[220:221], v10 offset:128\n"
    "ds_read_b64_tr_b16 v[222:223], v10 offset:896\n"
    "ds_read_b64_tr_b16 v[56:57], v10 offset:256\n"
    "ds_read_b64_tr_b16 v[58:59], v10 offset:1024\n"
    "v_add_u32 v11, 0, v6\n"
    "ds_read_b64_tr_b16 v[156:157], v11 offset:0\n"
    "ds_read_b64_tr_b16 v[158:159], v11 offset:768\n"
    "ds_read_b64_tr_b16 v[236:237], v11 offset:128\n"
    "ds_read_b64_tr_b16 v[238:239], v11 offset:896\n"
    "ds_read_b64_tr_b16 v[72:73], v11 offset:256\n"
    "ds_read_b64_tr_b16 v[74:75], v11 offset:1024\n"
    "s_waitcnt lgkmcnt(0)\n"
    "s_barrier\n"
    "s_mov_b32 s60, 393216\n"
    "s_add_u32 m0, s51, 0\n"
    "s_nop 0\n"
    "buffer_load_dwordx4 v0, s[4:7], s60 offen offset:0 nt lds\n"
    "buffer_load_dwordx4 v1, s[4:7], s60 offen offset:1024 nt lds\n"
    "buffer_load_dwordx4 v2, s[4:7], s60 offen offset:2048 nt lds\n"
    "s_mov_b32 s60, 1966080\n"
    "s_add_u32 m0, s51, 12288\n"
    "s_nop 0\n"
    "buffer_load_dwordx4 v0, s[4:7], s60 offen offset:0 nt lds\n"
    "buffer_load_dwordx4 v1, s[4:7], s60 offen offset:1024 nt lds\n"
    "buffer_load_dwordx4 v2, s[4:7], s60 offen offset:2048 nt lds\n"
    "s_mov_b32 s60, 3538944\n"
    "s_add_u32 m0, s51, 24576\n"
    "s_nop 0\n"
    "buffer_load_dwordx4 v0, s[4:7], s60 offen offset:0 nt lds\n"
    "buffer_load_dwordx4 v1, s[4:7], s60 offen offset:1024 nt lds\n"
    "buffer_load_dwordx4 v2, s[4:7], s60 offen offset:2048 nt lds\n"
    "s_mov_b32 s60, 5111808\n"
    "s_add_u32 m0, s51, 36864\n"
    "s_nop 0\n"
    "buffer_load_dwordx4 v0, s[4:7], s60 offen offset:0 nt lds\n"
    "buffer_load_dwordx4 v1, s[4:7], s60 offen offset:1024 nt lds\n"
    "buffer_load_dwordx4 v2, s[4:7], s60 offen offset:2048 nt lds\n"
    "v_lshlrev_b32 v108, 4, v96\n"
    "v_mov_b32 v109, 1\n"
    "s_mov_b64 exec, 15\n"
    "buffer_store_dword v109, v108, s[0:3], s35 offen sc1\n"
    "s_mov_b64 exec, -1\n"
    "s_waitcnt vmcnt(25)\n"
    "s_barrier\n"
    "v_add_u32 v8, 49152, v7\n"
    "ds_read_b64_tr_b16 v[160:161], v8 offset:0\n"
    "ds_read_b64_tr_b16 v[162:163], v8 offset:768\n"
    "ds_read_b64_tr_b16 v[240:241], v8 offset:128\n"
    "ds_read_b64_tr_b16 v[242:243], v8 offset:896\n"
    "ds_read_b64_tr_b16 v[76:77], v8 offset:256\n"
    "ds_read_b64_tr_b16 v[78:79], v8 offset:1024\n"
    "v_add_u32 v9, 61440, v7\n"
    "ds_read_b64_tr_b16 v[164:165], v9 offset:0\n"
    "ds_read_b64_tr_b16 v[166:167], v9 offset:768\n"
    "ds_read_b64_tr_b16 v[244:245], v9 offset:128\n"
    "ds_read_b64_tr_b16 v[246:247], v9 offset:896\n"
    "ds_read_b64_tr_b16 v[80:81], v9 offset:256\n"
    "ds_read_b64_tr_b16 v[82:83], v9 offset:1024\n"
    "v_add_u32 v10, 73728, v7\n"
    "ds_read_b64_tr_b16 v[168:169], v10 offset:0\n"
    "ds_read_b64_tr_b16 v[170:171], v10 offset:768\n"
    "ds_read_b64_tr_b16 v[248:249], v10 offset:128\n"
    "ds_read_b64_tr_b16 v[250:251], v10 offset:896\n"
    "ds_read_b64_tr_b16 v[84:85], v10 offset:256\n"
    "ds_read_b64_tr_b16 v[86:87], v10 offset:1024\n"
    "v_add_u32 v11, 86016, v7\n"
    "ds_read_b64_tr_b16 v[172:173], v11 offset:0\n"
    "ds_read_b64_tr_b16 v[174:175], v11 offset:768\n"
    "ds_read_b64_tr_b16 v[252:253], v11 offset:128\n"
    "ds_read_b64_tr_b16 v[254:255], v11 offset:896\n"
    "ds_read_b64_tr_b16 v[88:89], v11 offset:256\n"
    "ds_read_b64_tr_b16 v[90:91], v11 offset:1024\n"
    "s_waitcnt lgkmcnt(0)\n"
    "s_barrier\n"
    "s_mov_b32 s60, 786432\n"
    "s_add_u32 m0, s51, 49152\n"
    "s_nop 0\n"
    "buffer_load_dwordx4 v0, s[4:7], s60 offen offset:0 nt lds\n"
    "buffer_load_dwordx4 v1, s[4:7], s60 offen offset:1024 nt lds\n"
    "buffer_load_dwordx4 v2, s[4:7], s60 offen offset:2048 nt lds\n"
    "s_mov_b32 s60, 2359296\n"
    "s_add_u32 m0, s51, 61440\n"
    "s_nop 0\n"
    "buffer_load_dwordx4 v0, s[4:7], s60 offen offset:0 nt lds\n"
    "buffer_load_dwordx4 v1, s[4:7], s60 offen offset:1024 nt lds\n"
    "buffer_load_dwordx4 v2, s[4:7], s60 offen offset:2048 nt lds\n"
    "s_mov_b32 s60, 3932160\n"
    "s_add_u32 m0, s51, 73728\n"
    "s_nop 0\n"
    "buffer_load_dwordx4 v0, s[4:7], s60 offen offset:0 nt lds\n"
    "buffer_load_dwordx4 v1, s[4:7], s60 offen offset:1024 nt lds\n"
    "buffer_load_dwordx4 v2, s[4:7], s60 offen offset:2048 nt lds\n"
    "s_mov_b32 s60, 5505024\n"
    "s_add_u32 m0, s51, 86016\n"
    "s_nop 0\n"
    "buffer_load_dwordx4 v0, s[4:7], s60 offen offset:0 nt lds\n"
    "buffer_load_dwordx4 v1, s[4:7], s60 offen offset:1024 nt lds\n"
    "buffer_load_dwordx4 v2, s[4:7], s60 offen offset:2048 nt lds\n"
    "s_waitcnt vmcnt(25)\n"
    "s_barrier\n"
    "v_add_u32 v8, 98304, v3\n"
    "ds_read_b64_tr_b16 a[0:1], v8 offset:0\n"
    "ds_read_b64_tr_b16 a[2:3], v8 offset:768\n"
    "ds_read_b64_tr_b16 a[80:81], v8 offset:128\n"
    "ds_read_b64_tr_b16 a[82:83], v8 offset:896\n"
    "ds_read_b64_tr_b16 a[160:161], v8 offset:256\n"
    "ds_read_b64_tr_b16 a[162:163], v8 offset:1024\n"
    "v_add_u32 v9, 98304, v4\n"
    "ds_read_b64_tr_b16 a[16:17], v9 offset:0\n"
    "ds_read_b64_tr_b16 a[18:19], v9 offset:768\n"
    "ds_read_b64_tr_b16 a[96:97], v9 offset:128\n"
    "ds_read_b64_tr_b16 a[98:99], v9 offset:896\n"
    "ds_read_b64_tr_b16 a[176:177], v9 offset:256\n"
    "ds_read_b64_tr_b16 a[178:179], v9 offset:1024\n"
    "v_add_u32 v10, 98304, v5\n"
    "ds_read_b64_tr_b16 a[32:33], v10 offset:0\n"
    "ds_read_b64_tr_b16 a[34:35], v10 offset:768\n"
    "ds_read_b64_tr_b16 a[112:113], v10 offset:128\n"
    "ds_read_b64_tr_b16 a[114:115], v10 offset:896\n"
    "ds_read_b64_tr_b16 a[192:193], v10 offset:256\n"
    "ds_read_b64_tr_b16 a[194:195], v10 offset:1024\n"
    "v_add_u32 v11, 98304, v6\n"
    "ds_read_b64_tr_b16 a[48:49], v11 offset:0\n"
    "ds_read_b64_tr_b16 a[50:51], v11 offset:768\n"
    "ds_read_b64_tr_b16 a[128:129], v11 offset:128\n"
    "ds_read_b64_tr_b16 a[130:131], v11 offset:896\n"
    "ds_read_b64_tr_b16 a[208:209], v11 offset:256\n"
    "ds_read_b64_tr_b16 a[210:211], v11 offset:1024\n"
    "s_waitcnt lgkmcnt(0)\n"
    "s_barrier\n"
    "s_mov_b32 s60, 1179648\n"
    "s_add_u32 m0, s51, 98304\n"
    "s_nop 0\n"
    "buffer_load_dwordx4 v0, s[4:7], s60 offen offset:0 nt lds\n"
    "buffer_load_dwordx4 v1, s[4:7], s60 offen offset:1024 nt lds\n"
    "buffer_load_dwordx4 v2, s[4:7], s60 offen offset:2048 nt lds\n"
    "s_mov_b32 s60, 2752512\n"
    "s_add_u32 m0, s51, 110592\n"
    "s_nop 0\n"
    "buffer_load_dwordx4 v0, s[4:7], s60 offen offset:0 nt lds\n"
    "buffer_load_dwordx4 v1, s[4:7], s60 offen offset:1024 nt lds\n"
    "buffer_load_dwordx4 v2, s[4:7], s60 offen offset:2048 nt lds\n"
    "s_mov_b32 s60, 4325376\n"
    "s_add_u32 m0, s51, 122880\n"
    "s_nop 0\n"
    "buffer_load_dwordx4 v0, s[4:7], s60 offen offset:0 nt lds\n"
    "buffer_load_dwordx4 v1, s[4:7], s60 offen offset:1024 nt lds\n"
    "buffer_load_dwordx4 v2, s[4:7], s60 offen offset:2048 nt lds\n"
    "s_mov_b32 s60, 5898240\n"
    "s_add_u32 m0, s51, 135168\n"
    "s_nop 0\n"
    "buffer_load_dwordx4 v0, s[4:7], s60 offen offset:0 nt lds\n"
    "buffer_load_dwordx4 v1, s[4:7], s60 offen offset:1024 nt lds\n"
    "buffer_load_dwordx4 v2, s[4:7], s60 offen offset:2048 nt lds\n"
    "s_waitcnt vmcnt(25)\n"
    "s_barrier\n"
    "v_add_u32 v8, 0, v3\n"
    "ds_read_b64_tr_b16 a[4:5], v8 offset:0\n"
    "ds_read_b64_tr_b16 a[6:7], v8 offset:768\n"
    "ds_read_b64_tr_b16 a[84:85], v8 offset:128\n"
    "ds_read_b64_tr_b16 a[86:87], v8 offset:896\n"
    "ds_read_b64_tr_b16 a[164:165], v8 offset:256\n"
    "ds_read_b64_tr_b16 a[166:167], v8 offset:1024\n"
    "v_add_u32 v9, 0, v4\n"
    "ds_read_b64_tr_b16 a[20:21], v9 offset:0\n"
    "ds_read_b64_tr_b16 a[22:23], v9 offset:768\n"
    "ds_read_b64_tr_b16 a[100:101], v9 offset:128\n"
    "ds_read_b64_tr_b16 a[102:103], v9 offset:896\n"
    "ds_read_b64_tr_b16 a[180:181], v9 offset:256\n"
    "ds_read_b64_tr_b16 a[182:183], v9 offset:1024\n"
    "v_add_u32 v10, 0, v5\n"
    "ds_read_b64_tr_b16 a[36:37], v10 offset:0\n"
    "ds_read_b64_tr_b16 a[38:39], v10 offset:768\n"
    "ds_read_b64_tr_b16 a[116:117], v10 offset:128\n"
    "ds_read_b64_tr_b16 a[118:119], v10 offset:896\n"
    "ds_read_b64_tr_b16 a[196:197], v10 offset:256\n"
    "ds_read_b64_tr_b16 a[198:199], v10 offset:1024\n"
    "v_add_u32 v11, 0, v6\n"
    "ds_read_b64_tr_b16 a[52:53], v11 offset:0\n"
    "ds_read_b64_tr_b16 a[54:55], v11 offset:768\n"
    "ds_read_b64_tr_b16 a[132:133], v11 offset:128\n"
    "ds_read_b64_tr_b16 a[134:135], v11 offset:896\n"
    "ds_read_b64_tr_b16 a[212:213], v11 offset:256\n"
    "ds_read_b64_tr_b16 a[214:215], v11 offset:1024\n"
    "s_waitcnt lgkmcnt(0)\n"
    "s_barrier\n"
    "s_mov_b32 s60, 6291456\n"
    "s_add_u32 m0, s51, 0\n"
    "s_nop 0\n"
    "buffer_load_dwordx4 v0, s[4:7], s60 offen offset:0 nt lds\n"
    "buffer_load_dwordx4 v1, s[4:7], s60 offen offset:1024 nt lds\n"
    "buffer_load_dwordx4 v2, s[4:7], s60 offen offset:2048 nt lds\n"
    "s_mov_b32 s60, 6684672\n"
    "s_add_u32 m0, s51, 12288\n"
    "s_nop 0\n"
    "buffer_load_dwordx4 v0, s[4:7], s60 offen offset:0 nt lds\n"
    "buffer_load_dwordx4 v1, s[4:7], s60 offen offset:1024 nt lds\n"
    "buffer_load_dwordx4 v2, s[4:7], s60 offen offset:2048 nt lds\n"
    "s_mov_b32 s60, 7077888\n"
    "s_add_u32 m0, s51, 24576\n"
    "s_nop 0\n"
    "buffer_load_dwordx4 v0, s[4:7], s60 offen offset:0 nt lds\n"
    "buffer_load_dwordx4 v1, s[4:7], s60 offen offset:1024 nt lds\n"
    "buffer_load_dwordx4 v2, s[4:7], s60 offen offset:2048 nt lds\n"
    "s_mov_b32 s60, 7471104\n"
    "s_add_u32 m0, s51, 36864\n"
    "s_nop 0\n"
    "buffer_load_dwordx4 v0, s[4:7], s60 offen offset:0 nt lds\n"
    "buffer_load_dwordx4 v1, s[4:7], s60 offen offset:1024 nt lds\n"
    "buffer_load_dwordx4 v2, s[4:7], s60 offen offset:2048 nt lds\n"
    "s_waitcnt vmcnt(24)\n"
    "s_barrier\n"
    "v_add_u32 v8, 49152, v3\n"
    "ds_read_b64_tr_b16 a[8:9], v8 offset:0\n"
    "ds_read_b64_tr_b16 a[10:11], v8 offset:768\n"
    "ds_read_b64_tr_b16 a[88:89], v8 offset:128\n"
    "ds_read_b64_tr_b16 a[90:91], v8 offset:896\n"
    "ds_read_b64_tr_b16 a[168:169], v8 offset:256\n"
    "ds_read_b64_tr_b16 a[170:171], v8 offset:1024\n"
    "v_add_u32 v9, 49152, v4\n"
    "ds_read_b64_tr_b16 a[24:25], v9 offset:0\n"
    "ds_read_b64_tr_b16 a[26:27], v9 offset:768\n"
    "ds_read_b64_tr_b16 a[104:105], v9 offset:128\n"
    "ds_read_b64_tr_b16 a[106:107], v9 offset:896\n"
    "ds_read_b64_tr_b16 a[184:185], v9 offset:256\n"
    "ds_read_b64_tr_b16 a[186:187], v9 offset:1024\n"
    "v_add_u32 v10, 49152, v5\n"
    "ds_read_b64_tr_b16 a[40:41], v10 offset:0\n"
    "ds_read_b64_tr_b16 a[42:43], v10 offset:768\n"
    "ds_read_b64_tr_b16 a[120:121], v10 offset:128\n"
    "ds_read_b64_tr_b16 a[122:123], v10 offset:896\n"
    "ds_read_b64_tr_b16 a[200:201], v10 offset:256\n"
    "ds_read_b64_tr_b16 a[202:203], v10 offset:1024\n"
    "v_add_u32 v11, 49152, v6\n"
    "ds_read_b64_tr_b16 a[56:57], v11 offset:0\n"
    "ds_read_b64_tr_b16 a[58:59], v11 offset:768\n"
    "ds_read_b64_tr_b16 a[136:137], v11 offset:128\n"
    "ds_read_b64_tr_b16 a[138:139], v11 offset:896\n"
    "ds_read_b64_tr_b16 a[216:217], v11 offset:256\n"
    "ds_read_b64_tr_b16 a[218:219], v11 offset:1024\n"
    "s_waitcnt lgkmcnt(0)\n"
    "s_barrier\n"
    "s_waitcnt vmcnt(12)\n"
    "s_barrier\n"
    "v_add_u32 v8, 98304, v3\n"
    "ds_read_b64_tr_b16 a[12:13], v8 offset:0\n"
    "ds_read_b64_tr_b16 a[14:15], v8 offset:768\n"
    "ds_read_b64_tr_b16 a[92:93], v8 offset:128\n"
    "ds_read_b64_tr_b16 a[94:95], v8 offset:896\n"
    "ds_read_b64_tr_b16 a[172:173], v8 offset:256\n"
    "ds_read_b64_tr_b16 a[174:175], v8 offset:1024\n"
    "v_add_u32 v9, 98304, v4\n"
    "ds_read_b64_tr_b16 a[28:29], v9 offset:0\n"
    "ds_read_b64_tr_b16 a[30:31], v9 offset:768\n"
    "ds_read_b64_tr_b16 a[108:109], v9 offset:128\n"
    "ds_read_b64_tr_b16 a[110:111], v9 offset:896\n"
    "ds_read_b64_tr_b16 a[188:189], v9 offset:256\n"
    "ds_read_b64_tr_b16 a[190:191], v9 offset:1024\n"
    "v_add_u32 v10, 98304, v5\n"
    "ds_read_b64_tr_b16 a[44:45], v10 offset:0\n"
    "ds_read_b64_tr_b16 a[46:47], v10 offset:768\n"
    "ds_read_b64_tr_b16 a[124:125], v10 offset:128\n"
    "ds_read_b64_tr_b16 a[126:127], v10 offset:896\n"
    "ds_read_b64_tr_b16 a[204:205], v10 offset:256\n"
    "ds_read_b64_tr_b16 a[206:207], v10 offset:1024\n"
    "v_add_u32 v11, 98304, v6\n"
    "ds_read_b64_tr_b16 a[60:61], v11 offset:0\n"
    "ds_read_b64_tr_b16 a[62:63], v11 offset:768\n"
    "ds_read_b64_tr_b16 a[140:141], v11 offset:128\n"
    "ds_read_b64_tr_b16 a[142:143], v11 offset:896\n"
    "ds_read_b64_tr_b16 a[220:221], v11 offset:256\n"
    "ds_read_b64_tr_b16 a[222:223], v11 offset:1024\n"
    "s_waitcnt lgkmcnt(0)\n"
    "s_barrier\n"
    "s_waitcnt vmcnt(0)\n"
    "s_barrier\n"
    "v_add_u32 v8, 0, v7\n"
    "ds_read_b64_tr_b16 a[64:65], v8 offset:0\n"
    "ds_read_b64_tr_b16 a[66:67], v8 offset:768\n"
    "ds_read_b64_tr_b16 a[144:145], v8 offset:128\n"
    "ds_read_b64_tr_b16 a[146:147], v8 offset:896\n"
    "ds_read_b64_tr_b16 a[224:225], v8 offset:256\n"
    "ds_read_b64_tr_b16 a[226:227], v8 offset:1024\n"
    "v_add_u32 v9, 12288, v7\n"
    "ds_read_b64_tr_b16 a[68:69], v9 offset:0\n"
    "ds_read_b64_tr_b16 a[70:71], v9 offset:768\n"
    "ds_read_b64_tr_b16 a[148:149], v9 offset:128\n"
    "ds_read_b64_tr_b16 a[150:151], v9 offset:896\n"
    "ds_read_b64_tr_b16 a[228:229], v9 offset:256\n"
    "ds_read_b64_tr_b16 a[230:231], v9 offset:1024\n"
    "v_add_u32 v10, 24576, v7\n"
    "ds_read_b64_tr_b16 a[72:73], v10 offset:0\n"
    "ds_read_b64_tr_b16 a[74:75], v10 offset:768\n"
    "ds_read_b64_tr_b16 a[152:153], v10 offset:128\n"
    "ds_read_b64_tr_b16 a[154:155], v10 offset:896\n"
    "ds_read_b64_tr_b16 a[232:233], v10 offset:256\n"
    "ds_read_b64_tr_b16 a[234:235], v10 offset:1024\n"
    "v_add_u32 v11, 36864, v7\n"
    "ds_read_b64_tr_b16 a[76:77], v11 offset:0\n"
    "ds_read_b64_tr_b16 a[78:79], v11 offset:768\n"
    "ds_read_b64_tr_b16 a[156:157], v11 offset:128\n"
    "ds_read_b64_tr_b16 a[158:159], v11 offset:896\n"
    "ds_read_b64_tr_b16 a[236:237], v11 offset:256\n"
    "ds_read_b64_tr_b16 a[238:239], v11 offset:1024\n"
    "s_waitcnt lgkmcnt(0)\n"
    "s_barrier\n"
    "ds_write_b128 v97, v[12:15] offset:0\n"
    "ds_write_b128 v97, v[16:19] offset:1024\n"
    "ds_write_b128 v97, v[20:23] offset:2048\n"
    "ds_write_b128 v97, v[24:27] offset:3072\n"
    "ds_write_b128 v97, v[28:31] offset:4096\n"
    "ds_write_b128 v97, v[32:35] offset:5120\n"
    "ds_write_b128 v97, v[36:39] offset:6144\n"
    "ds_write_b128 v97, v[40:43] offset:7168\n"
    "ds_write_b128 v97, v[44:47] offset:8192\n"
    "ds_write_b128 v97, v[48:51] offset:9216\n"
    "ds_write_b128 v97, v[52:55] offset:10240\n"
    "ds_write_b128 v97, v[56:59] offset:11264\n"
    "ds_write_b128 v97, v[60:63] offset:12288\n"
    "ds_write_b128 v97, v[64:67] offset:13312\n"
    "ds_write_b128 v97, v[68:71] offset:14336\n"
    "ds_write_b128 v97, v[72:75] offset:15360\n"
    "ds_write_b128 v97, v[76:79] offset:16384\n"
    "ds_write_b128 v97, v[80:83] offset:17408\n"
    "ds_write_b128 v97, v[84:87] offset:18432\n"
    "ds_write_b128 v97, v[88:91] offset:19456\n"
    "s_mov_b32 s46, -2\n"
    "s_mov_b32 s62, 1037384597\n"
    "s_mov_b32 s63, 1037384597\n"
    "s_mov_b32 s64, 858993459\n"
    "s_mov_b32 s65, 858993459\n"
    "s_mov_b32 s66, 67044351\n"
    "s_mov_b32 s67, 67044351\n"
    "v_mov_b32 v8, v99\n"
    "v_mov_b32 v9, v99\n"
    "v_mov_b32 v10, 137088\n"
    "ds_write_b64 v10, v[8:9] offset:0\n"
    "ds_write_b64 v10, v[8:9] offset:8\n"
    "ds_write_b64 v10, v[8:9] offset:1936\n"
    "ds_write_b64 v10, v[8:9] offset:1944\n"
    "ds_write_b64 v10, v[8:9] offset:3872\n"
    "ds_write_b64 v10, v[8:9] offset:3880\n"
    "ds_write_b64 v10, v[8:9] offset:5808\n"
    "ds_write_b64 v10, v[8:9] offset:5816\n"
    "s_lshl_b32 s59, s22, 11\n"
    "s_add_u32 s59, s59, 45056\n"
    "v_lshrrev_b32 v4, 2, v96\n"
    "v_and_b32 v6, 12, v4\n"
    "v_lshlrev_b32 v5, 1, v96\n"
    "v_and_b32 v5, 0x380, v5\n"
    "v_min_u32 v5, 0x200, v5\n"
    "v_add_u32 v5, v5, v6\n"
    "s_lshr_b32 s60, s23, 13\n"
    "s_mul_i32 s60, s60, 3\n"
    ".Lqk_po3_%=:\n"
    "buffer_load_dword v8, v4, s[0:3], s32 offen sc1\n"
    "s_waitcnt vmcnt(0)\n"
    "v_cmp_gt_i32 vcc, 1, v8\n"
    "s_nop 0\n"
    "s_cbranch_vccz .Lqk_pd4_%=\n"
    "s_add_u32 s40, s40, 1\n"
    "s_cmp_lt_u32 s40, 0x40000\n"
    "s_cbranch_scc1 .Lqk_sp5_%=\n"
    "v_mov_b32 v8, 20\n"
    "s_mov_b32 s44, 33728\n"
    "buffer_store_dword v8, off, s[0:3], s44 sc1\n"
    "s_waitcnt vmcnt(0)\n"
    "s_branch .Lqk_pd4_%=\n"
    ".Lqk_sp5_%=:\n"
    "s_sleep 4\n"
    "s_branch .Lqk_po3_%=\n"
    ".Lqk_pd4_%=:\n"
    ".Lqk_po6_%=:\n"
    "buffer_load_dword v8, v5, s[0:3], s33 offen sc1\n"
    "s_waitcnt vmcnt(0)\n"
    "v_cmp_gt_i32 vcc, s60, v8\n"
    "s_nop 0\n"
    "s_cbranch_vccz .Lqk_pd7_%=\n"
    "s_add_u32 s40, s40, 1\n"
    "s_cmp_lt_u32 s40, 0x40000\n"
    "s_cbranch_scc1 .Lqk_sp8_%=\n"
    "v_mov_b32 v8, 21\n"
    "s_mov_b32 s44, 33728\n"
    "buffer_store_dword v8, off, s[0:3], s44 sc1\n"
    "s_waitcnt vmcnt(0)\n"
    "s_branch .Lqk_pd7_%=\n"
    ".Lqk_sp8_%=:\n"
    "s_sleep 4\n"
    "s_branch .Lqk_po6_%=\n"
    ".Lqk_pd7_%=:\n"
    "s_add_u32 s53, s27, s23\n"
    "s_sub_u32 m0, s26, 8192\n"
    "s_nop 0\n"
    "buffer_load_dwordx4 v96, s[0:3], s53 offen offset:0 sc1 lds\n"
    "buffer_load_dwordx4 v96, s[0:3], s53 offen offset:1024 sc1 lds\n"
    "buffer_load_dwordx4 v96, s[0:3], s53 offen offset:2048 sc1 lds\n"
    "buffer_load_dwordx4 v96, s[0:3], s53 offen offset:3072 sc1 lds\n"
    "s_sub_u32 m0, s26, 4096\n"
    "s_add_u32 s53, s53, 4096\n"
    "buffer_load_dwordx4 v96, s[0:3], s53 offen offset:0 sc1 lds\n"
    "buffer_load_dwordx4 v96, s[0:3], s53 offen offset:1024 sc1 lds\n"
    ".Lqk_po9_%=:\n"
    "buffer_load_dword v8, v6, s[0:3], s34 offen sc1\n"
    "s_waitcnt vmcnt(0)\n"
    "v_cmp_gt_i32 vcc, 2, v8\n"
    "s_nop 0\n"
    "s_cbranch_vccz .Lqk_pd10_%=\n"
    "s_add_u32 s40, s40, 1\n"
    "s_cmp_lt_u32 s40, 0x40000\n"
    "s_cbranch_scc1 .Lqk_sp11_%=\n"
    "v_mov_b32 v8, 22\n"
    "s_mov_b32 s44, 33728\n"
    "buffer_store_dword v8, off, s[0:3], s44 sc1\n"
    "s_waitcnt vmcnt(0)\n"
    "s_branch .Lqk_pd10_%=\n"
    ".Lqk_sp11_%=:\n"
    "s_sleep 4\n"
    "s_branch .Lqk_po9_%=\n"
    ".Lqk_pd10_%=:\n"
    "s_memrealtime s[42:43]\n"
    "s_waitcnt lgkmcnt(0)\n"
    "s_sub_u32 s57, s42, 804\n"
    "s_mov_b32 s46, -1\n"
    "s_mov_b32 s47, -5\n"
    "s_mov_b32 s50, 2130706432\n"
    "s_mov_b32 s48, 2130706432\n"
    "s_mov_b32 s49, 4194304\n"
    "s_mov_b32 s55, s31\n"
    "s_add_u32 s54, s29, s23\n"
    "s_barrier\n"
    "v_mov_b32 v110, v104\n"
    "ds_read_b128 v[32:35], v92 offset:0\n"
    "ds_read_b128 v[36:39], v92 offset:1024\n"
    "ds_read_b128 v[40:43], v93 offset:0\n"
    "ds_read_b128 v[44:47], v93 offset:1024\n"
    "ds_read_b128 v[48:51], v94 offset:0\n"
    "ds_read_b128 v[52:55], v94 offset:1024\n"
    "s_waitcnt lgkmcnt(0)\n"
    "s_branch .Lqk_bulk_%=\n"
    ".Lqk_take_%=:\n"
    "s_waitcnt vmcnt(2)\n"
    ".Lqk_mk0x11_%=:\n"
    "ds_read_b128 v[56:59], v92 offset:8192\n"
    "s_waitcnt vmcnt(1)\n"
    "ds_read_b128 v[60:63], v92 offset:9216\n"
    "s_waitcnt lgkmcnt(1)\n"
    "v_smfmac_f32_16x16x64_bf16 v[8:11], v[56:59], v[32:39], v98\n"
    "v_max_u32 v108, v56, v58\n"
    "v_smfmac_f32_16x16x64_bf16 v[12:15], v[56:59], a[160:167], v98\n"
    "s_waitcnt lgkmcnt(0)\n"
    "ds_read_b128 v[32:35], v97 offset:6144\n"
    "v_max3_u32 v108, v108, v60, v62\n"
    "v_smfmac_f32_16x16x64_bf16 v[0:3], v[56:59], v[176:183], v98\n"
    "ds_read_b128 v[36:39], v97 offset:7168\n"
    "v_cmp_eq_u32 vcc, -1, v108\n"
    "v_smfmac_f32_16x16x64_bf16 v[4:7], v[56:59], a[80:87], v98\n"
    "s_cbranch_vccnz .Lqk_retry_%=\n"
    ".Lqk_go_%=:\n"
    "s_barrier\n"
    ".Lqk_mk1x12_%=:\n"
    "v_smfmac_f32_16x16x64_bf16 v[16:19], v[56:59], a[240:247], v98\n"
    "ds_read_b128 v[24:27], v93 offset:8192\n"
    "ds_read_b128 v[28:31], v93 offset:9216\n"
    "v_smfmac_f32_16x16x64_bf16 v[20:23], v[56:59], a[0:7], v98\n"
    "s_waitcnt vmcnt(0)\n"
    "ds_read_b128 v[64:67], v92 offset:10240\n"
    "v_smfmac_f32_16x16x64_bf16 v[8:11], v[60:63], v[40:47], v98\n"
    "ds_read_b128 v[56:59], v94 offset:9216\n"
    "v_smfmac_f32_16x16x64_bf16 v[12:15], v[60:63], a[168:175], v98\n"
    "ds_read_b128 v[40:43], v97 offset:8192\n"
    "ds_read_b128 v[44:47], v97 offset:9216\n"
    "v_smfmac_f32_16x16x64_bf16 v[0:3], v[60:63], v[184:191], v98\n"
    "v_smfmac_f32_16x16x64_bf16 v[4:7], v[60:63], a[88:95], v98\n"
    "v_smfmac_f32_16x16x64_bf16 v[16:19], v[60:63], a[248:255], v98\n"
    "s_waitcnt lgkmcnt(3)\n"
    "v_max_u32 v108, v64, v66\n"
    "v_smfmac_f32_16x16x64_bf16 v[20:23], v[60:63], a[8:15], v98\n"
    "v_cmp_eq_u32 vcc, -1, v108\n"
    "v_smfmac_f32_16x16x64_bf16 v[8:11], v[24:27], v[48:55], v98\n"
    "s_cbranch_vccnz .Lqk_r2_%=\n"
    ".Lqk_go2_%=:\n"
    "s_barrier\n"
    "v_smfmac_f32_16x16x64_bf16 v[12:15], v[24:27], a[176:183], v98\n"
    "s_memrealtime s[68:69]\n"
    "ds_read_b128 v[64:67], v94 offset:8192\n"
    "ds_read_b128 v[60:63], v95 offset:8192\n"
    "v_smfmac_f32_16x16x64_bf16 v[0:3], v[24:27], v[192:199], v98\n"
    "ds_read_b128 v[48:51], v97 offset:10240\n"
    "ds_read_b128 v[52:55], v97 offset:11264\n"
    "v_smfmac_f32_16x16x64_bf16 v[4:7], v[24:27], a[96:103], v98\n"
    "v_mov_b32 v109, s47\n"
    "v_smfmac_f32_16x16x64_bf16 v[16:19], v[24:27], v[112:119], v98\n"
    "v_lshlrev_b32 v110, 10, v96\n"
    "v_smfmac_f32_16x16x64_bf16 v[20:23], v[24:27], a[16:23], v98\n"
    "ds_read_b128 v[24:27], v95 offset:9216\n"
    "s_mov_b64 vcc, s[64:65]\n"
    "v_smfmac_f32_16x16x64_bf16 v[8:11], v[28:31], v[32:39], v98\n"
    "ds_read_b128 v[32:35], v97 offset:12288\n"
    "ds_read_b128 v[36:39], v97 offset:13312\n"
    "v_smfmac_f32_16x16x64_bf16 v[12:15], v[28:31], a[184:191], v98\n"
    "v_smfmac_f32_16x16x64_bf16 v[0:3], v[28:31], v[200:207], v98\n"
    "v_smfmac_f32_16x16x64_bf16 v[4:7], v[28:31], a[104:111], v98\n"
    "v_smfmac_f32_16x16x64_bf16 v[16:19], v[28:31], v[120:127], v98\n"
    "v_smfmac_f32_16x16x64_bf16 v[20:23], v[28:31], a[24:31], v98\n"
    "s_waitcnt lgkmcnt(0)\n"
    "v_smfmac_f32_16x16x64_bf16 v[8:11], v[64:67], v[40:47], v98\n"
    "s_sub_u32 s44, s68, s57\n"
    "ds_read_b128 v[40:43], v97 offset:14336\n"
    "ds_read_b128 v[44:47], v97 offset:15360\n"
    "v_smfmac_f32_16x16x64_bf16 v[12:15], v[64:67], a[192:199], v98\n"
    "ds_read_b128 v[28:31], v96 offset:10240\n"
    "s_add_u32 s45, s68, 18\n"
    "v_smfmac_f32_16x16x64_bf16 v[0:3], v[64:67], v[208:215], v98\n"
    "s_cmp_gt_i32 s44, 6\n"
    "s_cselect_b32 s57, s45, s57\n"
    "v_mov_b32 v108, s57\n"
    "v_smfmac_f32_16x16x64_bf16 v[4:7], v[64:67], a[112:119], v98\n"
    "s_mov_b64 exec, 15\n"
    "buffer_store_dwordx2 v[108:109], v110, s[0:3], s56 offen sc1\n"
    "s_mov_b64 exec, -1\n"
    "v_smfmac_f32_16x16x64_bf16 v[16:19], v[64:67], v[128:135], v98\n"
    "v_smfmac_f32_16x16x64_bf16 v[20:23], v[64:67], a[32:39], v98\n"
    "v_bfe_u32 v111, v96, 8, 2\n"
    "v_smfmac_f32_16x16x64_bf16 v[8:11], v[56:59], v[48:55], v98\n"
    "ds_read_b128 v[48:51], v97 offset:16384\n"
    "ds_read_b128 v[52:55], v97 offset:17408\n"
    "v_smfmac_f32_16x16x64_bf16 v[12:15], v[56:59], a[200:207], v98\n"
    "ds_read_b128 v[64:67], v96 offset:21504\n"
    "v_mad_i32_i24 v110, v111, -8, v104\n"
    "v_smfmac_f32_16x16x64_bf16 v[8:11], v[60:63], v[32:39], v98\n"
    "ds_read_b128 v[32:35], v97 offset:18432\n"
    "ds_read_b128 v[36:39], v97 offset:19456\n"
    "v_smfmac_f32_16x16x64_bf16 v[12:15], v[60:63], a[208:215], v98\n"
    "s_waitcnt lgkmcnt(5)\n"
    "v_mad_i32_i24 v111, v111, -8, v102\n"
    "v_smfmac_f32_16x16x64_bf16 v[8:11], v[24:27], v[40:47], v98\n"
    "v_smfmac_f32_16x16x64_bf16 v[12:15], v[24:27], a[216:223], v98\n"
    "v_smfmac_f32_16x16x64_bf16 v[0:3], v[56:59], v[216:223], v98\n"
    "v_smfmac_f32_16x16x64_bf16 v[4:7], v[56:59], a[120:127], v98\n"
    "s_waitcnt lgkmcnt(3)\n"
    "v_smfmac_f32_16x16x64_bf16 v[8:11], v[28:31], v[48:55], v98\n"
    "v_smfmac_f32_16x16x64_bf16 v[12:15], v[28:31], a[224:231], v98\n"
    "s_waitcnt lgkmcnt(0)\n"
    "v_smfmac_f32_16x16x64_bf16 v[8:11], v[64:67], v[32:39], v98\n"
    "v_smfmac_f32_16x16x64_bf16 v[12:15], v[64:67], a[232:239], v98\n"
    "ds_read_b128 v[32:35], v92 offset:0\n"
    "ds_read_b128 v[36:39], v92 offset:1024\n"
    "v_smfmac_f32_16x16x64_bf16 v[0:3], v[60:63], v[224:231], v98\n"
    "s_nop 1\n"
    "v_add_f32 v8, v8, v10\n"
    "v_smfmac_f32_16x16x64_bf16 v[4:7], v[60:63], a[128:135], v98\n"
    "ds_read_b128 v[40:43], v93 offset:0\n"
    "v_add_f32 v9, v9, v11\n"
    "v_smfmac_f32_16x16x64_bf16 v[0:3], v[24:27], v[232:239], v98\n"
    "ds_read_b128 v[44:47], v93 offset:1024\n"
    "v_cvt_pk_bf16_f32 v8, v8, v9\n"
    "v_smfmac_f32_16x16x64_bf16 v[0:3], v[28:31], v[240:247], v98\n"
    "ds_read_b128 v[48:51], v94 offset:0\n"
    "v_add_f32 v12, v12, v14\n"
    "v_smfmac_f32_16x16x64_bf16 v[0:3], v[64:67], v[248:255], v98\n"
    "ds_read_b128 v[52:55], v94 offset:1024\n"
    "v_add_f32 v13, v13, v15\n"
    "v_smfmac_f32_16x16x64_bf16 v[4:7], v[24:27], a[136:143], v98\n"
    "v_cvt_pk_bf16_f32 v9, v12, v13\n"
    "v_smfmac_f32_16x16x64_bf16 v[4:7], v[28:31], a[144:151], v98\n"
    "s_nop 0\n"
    "v_mfma_f32_16x16x16_bf16 v[12:15], v[8:9], v[100:101], 0\n"
    "v_add_f32 v0, v0, v2\n"
    "v_add_f32 v1, v1, v3\n"
    "v_smfmac_f32_16x16x64_bf16 v[4:7], v[64:67], a[152:159], v98\n"
    "v_cvt_pk_bf16_f32 v0, v0, v1\n"
    "v_smfmac_f32_16x16x64_bf16 v[16:19], v[56:59], v[136:143], v98\n"
    "s_nop 1\n"
    "v_cvt_pk_bf16_f32 v15, v14, v15\n"
    "v_smfmac_f32_16x16x64_bf16 v[20:23], v[56:59], a[40:47], v98\n"
    "s_nop 0\n"
    "v_add_f32 v4, v4, v6\n"
    "v_smfmac_f32_16x16x64_bf16 v[16:19], v[60:63], v[144:151], v98\n"
    "v_add_f32 v5, v5, v7\n"
    "v_smfmac_f32_16x16x64_bf16 v[20:23], v[60:63], a[48:55], v98\n"
    "v_cvt_pk_bf16_f32 v1, v4, v5\n"
    "v_smfmac_f32_16x16x64_bf16 v[16:19], v[24:27], v[152:159], v98\n"
    "s_nop 0\n"
    "v_mfma_f32_16x16x16_bf16 v[4:7], v[0:1], v[100:101], 0\n"
    "v_cvt_pk_bf16_f32 v14, v12, v13\n"
    "v_smfmac_f32_16x16x64_bf16 v[20:23], v[24:27], a[56:63], v98\n"
    "v_smfmac_f32_16x16x64_bf16 v[16:19], v[28:31], v[160:167], v98\n"
    "s_nop 1\n"
    "v_cvt_pk_bf16_f32 v13, v6, v7\n"
    "v_smfmac_f32_16x16x64_bf16 v[20:23], v[28:31], a[64:71], v98\n"
    "v_cvt_pk_bf16_f32 v12, v4, v5\n"
    "v_smfmac_f32_16x16x64_bf16 v[16:19], v[64:67], v[168:175], v98\n"
    "v_smfmac_f32_16x16x64_bf16 v[20:23], v[64:67], a[72:79], v98\n"
    ".Lqk_mk2x13_%=:\n"
    "v_mfma_f32_16x16x32_bf16 v[24:27], v[12:15], v[12:15], 0\n"
    "s_nop 1\n"
    "v_add_f32 v16, v16, v18\n"
    "v_add_f32 v17, v17, v19\n"
    "v_cvt_pk_bf16_f32 v16, v16, v17\n"
    "v_add_f32 v20, v20, v22\n"
    "v_add_f32 v21, v21, v23\n"
    "v_cvt_pk_bf16_f32 v17, v20, v21\n"
    "s_nop 1\n"
    "v_mfma_f32_16x16x16_bf16 v[20:23], v[16:17], v[100:101], 0\n"
    "v_mfma_f32_16x16x32_bf16 v[0:3], v[72:75], v[12:15], 0\n"
    "v_mfma_f32_16x16x32_bf16 v[8:11], v[68:71], v[12:15], 0\n"
    "s_nop 1\n"
    "v_cvt_pk_bf16_f32 v17, v22, v23\n"
    "v_cvt_pk_bf16_f32 v16, v20, v21\n"
    "s_nop 1\n"
    "v_mfma_f32_16x16x16_bf16 v[24:27], v[16:17], v[16:17], v[24:27]\n"
    "v_mfma_f32_16x16x16_bf16 v[0:3], v[78:79], v[16:17], v[0:3]\n"
    "v_mfma_f32_16x16x16_bf16 v[8:11], v[76:77], v[16:17], v[8:11]\n"
    "s_nop 1\n"
    "ds_write_b64 v111, v[26:27] offset:128\n"
    "v_mfma_f32_16x16x32_bf16 v[56:59], v[12:15], v[80:83], 0\n"
    "s_nop 3\n"
    "v_cndmask_b32_dpp v8, v0, v8, vcc quad_perm:[0,1,0,1] row_mask:0xf bank_mask:0xf\n"
    "v_cndmask_b32_dpp v9, v1, v9, vcc quad_perm:[0,1,0,1] row_mask:0xf bank_mask:0xf\n"
    "v_cndmask_b32_dpp v10, v2, v10, vcc quad_perm:[0,1,0,1] row_mask:0xf bank_mask:0xf\n"
    "v_cndmask_b32_dpp v11, v3, v11, vcc quad_perm:[0,1,0,1] row_mask:0xf bank_mask:0xf\n"
    "ds_write_b128 v102, v[8:11]\n"
    "v_mfma_f32_16x16x32_bf16 v[60:63], v[12:15], v[84:87], 0\n"
    "s_mov_b64 exec, s[20:21]\n"
    "s_waitcnt lgkmcnt(0)\n"
    "s_barrier\n"
    ".Lqk_mk3x14_%=:\n"
    "ds_read_b128 v[72:75], v103 offset:0\n"
    "ds_read_b128 v[76:79], v103 offset:1936\n"
    "ds_read_b128 v[80:83], v103 offset:3872\n"
    "ds_read_b128 v[84:87], v103 offset:5808\n"
    "s_mov_b64 exec, -1\n"
    "v_mfma_f32_16x16x16_bf16 v[56:59], v[16:17], v[88:89], v[56:59]\n"
    "v_mfma_f32_16x16x16_bf16 v[60:63], v[16:17], v[90:91], v[60:63]\n"
    "s_waitcnt lgkmcnt(2)\n"
    "v_pk_add_f32 v[72:73], v[72:73], v[76:77]\n"
    "v_pk_add_f32 v[74:75], v[74:75], v[78:79]\n"
    "s_waitcnt lgkmcnt(1)\n"
    "v_pk_add_f32 v[72:73], v[72:73], v[80:81]\n"
    "v_pk_add_f32 v[74:75], v[74:75], v[82:83]\n"
    "s_waitcnt lgkmcnt(0)\n"
    "v_pk_add_f32 v[72:73], v[72:73], v[84:85]\n"
    "v_pk_add_f32 v[74:75], v[74:75], v[86:87]\n"
    "v_max3_f32 v76, v72, v73, v74\n"
    "v_max_f32 v76, v76, v75\n"
    "v_pk_mul_f32 v[72:73], v[72:73], s[62:63]\n"
    "v_pk_mul_f32 v[74:75], v[74:75], s[62:63]\n"
    "v_max_f32_dpp v76, v76, v76 row_ror:8 row_mask:0xf bank_mask:0xf\n"
    "s_nop 1\n"
    "v_max_f32_dpp v76, v76, v76 row_ror:4 row_mask:0xf bank_mask:0xf\n"
    "s_nop 1\n"
    "v_max_f32_dpp v76, v76, v76 row_ror:2 row_mask:0xf bank_mask:0xf\n"
    "s_nop 1\n"
    "v_max_f32_dpp v76, v76, v76 row_ror:1 row_mask:0xf bank_mask:0xf\n"
    "v_pk_fma_f32 v[72:73], v[76:77], s[62:63], v[72:73] op_sel_hi:[0,1,1] neg_lo:[1,0,0] neg_hi:[1,0,0]\n"
    "v_pk_fma_f32 v[74:75], v[76:77], s[62:63], v[74:75] op_sel_hi:[0,1,1] neg_lo:[1,0,0] neg_hi:[1,0,0]\n"
    "v_exp_f32 v72, v72\n"
    "v_exp_f32 v74, v74\n"
    "v_exp_f32 v73, v73\n"
    "v_exp_f32 v75, v75\n"
    "v_add_f32 v76, v72, v74\n"
    "v_add_f32 v77, v73, v75\n"
    "v_add_f32 v76, v76, v77\n"
    "s_nop 1\n"
    "v_add_f32_dpp v76, v76, v76 row_ror:8 row_mask:0xf bank_mask:0xf\n"
    "s_nop 1\n"
    "v_add_f32_dpp v76, v76, v76 row_ror:4 row_mask:0xf bank_mask:0xf\n"
    "s_nop 1\n"
    "v_add_f32_dpp v76, v76, v76 row_ror:2 row_mask:0xf bank_mask:0xf\n"
    "s_nop 1\n"
    "v_add_f32_dpp v76, v76, v76 row_ror:1 row_mask:0xf bank_mask:0xf\n"
    "v_rcp_f32 v76, v76\n"
    "s_nop 0\n"
    "v_pk_mul_f32 v[72:73], v[72:73], v[76:77] op_sel_hi:[1,0] clamp\n"
    "v_pk_mul_f32 v[74:75], v[74:75], v[76:77] op_sel_hi:[1,0] clamp\n"
    "v_cvt_pk_bf16_f32 v72, v72, v73\n"
    "v_cvt_pk_bf16_f32 v73, v74, v75\n"
    "buffer_store_dwordx2 v[72:73], v106, s[0:3], s48 offen sc1\n"
    ".Lqk_mk4x15_%=:\n"
    ".Lqk_bulk_%=:\n"
    "v_mfma_f32_16x16x32_bf16 v[0:3], a[80:83], v[32:35], 0\n"
    "ds_write_b64 v110, v[58:59] offset:224\n"
    "v_mfma_f32_16x16x32_bf16 v[4:7], a[80:83], v[36:39], 0\n"
    "ds_write_b64 v110, v[62:63] offset:128\n"
    ".Lqk_mkL0_%=:\n"
    "v_mfma_f32_16x16x32_bf16 v[8:11], a[160:163], v[32:35], 0\n"
    "v_mfma_f32_16x16x32_bf16 v[12:15], a[160:163], v[36:39], 0\n"
    "v_mfma_f32_16x16x32_bf16 v[16:19], a[0:3], v[32:35], 0\n"
    "v_mfma_f32_16x16x32_bf16 v[20:23], a[0:3], v[36:39], 0\n"
    "s_waitcnt lgkmcnt(0)\n"
    "s_barrier\n"
    "ds_read_b128 v[56:59], v105 offset:0\n"
    "v_mfma_f32_16x16x32_bf16 v[24:27], a[240:243], v[32:35], 0\n"
    "ds_read_b128 v[60:63], v105 offset:4352\n"
    "v_mfma_f32_16x16x32_bf16 v[28:31], a[240:243], v[36:39], 0\n"
    "v_mfma_f32_16x16x32_bf16 v[0:3], a[96:99], v[40:43], v[0:3]\n"
    "s_sub_u32 m0, s26, 4096\n"
    "ds_read_b128 v[32:35], v95 offset:0\n"
    "ds_read_b128 v[36:39], v95 offset:1024\n"
    "v_mfma_f32_16x16x32_bf16 v[4:7], a[96:99], v[44:47], v[4:7]\n"
    "ds_read_b128 v[64:67], v105 offset:8704\n"
    "v_mfma_f32_16x16x32_bf16 v[8:11], a[176:179], v[40:43], v[8:11]\n"
    "buffer_load_dwordx4 v96, s[0:3], s53 offen offset:2048 sc1 lds\n"
    "v_mfma_f32_16x16x32_bf16 v[12:15], a[176:179], v[44:47], v[12:15]\n"
    "ds_read_b128 v[68:71], v105 offset:13056\n"
    "v_mfma_f32_16x16x32_bf16 v[16:19], a[16:19], v[40:43], v[16:19]\n"
    "v_mfma_f32_16x16x32_bf16 v[20:23], a[16:19], v[44:47], v[20:23]\n"
    "v_mfma_f32_16x16x32_bf16 v[24:27], v[112:115], v[40:43], v[24:27]\n"
    "buffer_load_dwordx4 v96, s[0:3], s53 offen offset:3072 sc1 lds\n"
    "v_mfma_f32_16x16x32_bf16 v[28:31], v[112:115], v[44:47], v[28:31]\n"
    "s_mov_b32 m0, s59\n"
    "s_add_u32 s53, s53, s28\n"
    "s_waitcnt lgkmcnt(4)\n"
    "v_mfma_f32_16x16x32_bf16 v[0:3], a[112:115], v[48:51], v[0:3]\n"
    "ds_read_b128 v[40:43], v92 offset:2048\n"
    "ds_read_b128 v[44:47], v92 offset:3072\n"
    "v_mfma_f32_16x16x32_bf16 v[4:7], a[112:115], v[52:55], v[4:7]\n"
    "v_add_f32 v56, v56, v60\n"
    "v_mfma_f32_16x16x32_bf16 v[8:11], a[192:195], v[48:51], v[8:11]\n"
    "buffer_load_dwordx4 v96, s[0:3], s53 offen offset:0 sc1 lds\n"
    "v_mfma_f32_16x16x32_bf16 v[12:15], a[192:195], v[52:55], v[12:15]\n"
    "v_add_f32 v57, v57, v61\n"
    "v_add_f32 v58, v58, v62\n"
    "v_mfma_f32_16x16x32_bf16 v[16:19], a[32:35], v[48:51], v[16:19]\n"
    "v_add_f32 v59, v59, v63\n"
    "s_waitcnt lgkmcnt(3)\n"
    "v_mfma_f32_16x16x32_bf16 v[20:23], a[32:35], v[52:55], v[20:23]\n"
    "v_add_f32 v56, v56, v64\n"
    ".Lqk_mkL1_%=:\n"
    "v_mfma_f32_16x16x32_bf16 v[24:27], v[128:131], v[48:51], v[24:27]\n"
    "buffer_load_dwordx4 v96, s[0:3], s53 offen offset:1024 sc1 lds\n"
    "v_mfma_f32_16x16x32_bf16 v[28:31], v[128:131], v[52:55], v[28:31]\n"
    "s_waitcnt lgkmcnt(4)\n"
    "v_mov_b64 v[110:111], -1\n"
    "v_add_f32 v57, v57, v65\n"
    "v_mfma_f32_16x16x32_bf16 v[0:3], a[128:131], v[32:35], v[0:3]\n"
    "ds_read_b128 v[48:51], v93 offset:2048\n"
    "ds_read_b128 v[52:55], v93 offset:3072\n"
    "v_mfma_f32_16x16x32_bf16 v[4:7], a[128:131], v[36:39], v[4:7]\n"
    "v_add_f32 v58, v58, v66\n"
    "v_mfma_f32_16x16x32_bf16 v[8:11], a[208:211], v[32:35], v[8:11]\n"
    "buffer_load_dwordx4 v[80:83], v96, s[0:3], s54 offen sc1\n"
    "v_mfma_f32_16x16x32_bf16 v[12:15], a[208:211], v[36:39], v[12:15]\n"
    "v_add_f32 v59, v59, v67\n"
    "s_waitcnt lgkmcnt(4)\n"
    "v_mfma_f32_16x16x32_bf16 v[16:19], a[48:51], v[32:35], v[16:19]\n"
    "v_add_f32 v56, v56, v68\n"
    "v_add_f32 v57, v57, v69\n"
    "v_mfma_f32_16x16x32_bf16 v[20:23], a[48:51], v[36:39], v[20:23]\n"
    "v_add_f32 v58, v58, v70\n"
    "v_mfma_f32_16x16x32_bf16 v[24:27], v[144:147], v[32:35], v[24:27]\n"
    "buffer_load_dwordx4 v[84:87], v96, s[0:3], s54 offen offset:1024 sc1\n"
    "v_mfma_f32_16x16x32_bf16 v[28:31], v[144:147], v[36:39], v[28:31]\n"
    "s_waitcnt lgkmcnt(2)\n"
    "v_add_f32 v59, v59, v71\n"
    "s_barrier\n"
    "v_mfma_f32_16x16x32_bf16 v[0:3], a[84:87], v[40:43], v[0:3]\n"
    "ds_read_b128 v[32:35], v94 offset:2048\n"
    "ds_read_b128 v[36:39], v94 offset:3072\n"
    "v_mfma_f32_16x16x32_bf16 v[4:7], a[84:87], v[44:47], v[4:7]\n"
    "v_cndmask_b32 v56, v99, v56, s[12:13]\n"
    "v_cndmask_b32 v57, v99, v57, s[14:15]\n"
    "v_mfma_f32_16x16x32_bf16 v[8:11], a[164:167], v[40:43], v[8:11]\n"
    "buffer_store_dwordx2 v[110:111], v106, s[0:3], s49 offen sc1\n"
    "v_cndmask_b32 v58, v99, v58, s[16:17]\n"
    "v_mfma_f32_16x16x32_bf16 v[12:15], a[164:167], v[44:47], v[12:15]\n"
    "v_cndmask_b32 v59, v99, v59, s[18:19]\n"
    "v_max3_f32 v72, v56, v57, v58\n"
    "v_mfma_f32_16x16x32_bf16 v[16:19], a[4:7], v[40:43], v[16:19]\n"
    "v_max_f32 v72, v72, v59\n"
    "v_cndmask_b32 v74, v99, v72, s[66:67]\n"
    "v_mfma_f32_16x16x32_bf16 v[20:23], a[4:7], v[44:47], v[20:23]\n"
    "v_cndmask_b32 v75, v72, v99, s[66:67]\n"
    "v_max_f32_dpp v74, v74, v74 row_ror:8 row_mask:0xf bank_mask:0xf\n"
    "v_mfma_f32_16x16x32_bf16 v[24:27], a[244:247], v[40:43], v[24:27]\n"
    "v_max_f32_dpp v75, v75, v75 row_ror:8 row_mask:0xf bank_mask:0xf\n"
    "v_max_f32_dpp v74, v74, v74 row_ror:4 row_mask:0xf bank_mask:0xf\n"
    "v_mfma_f32_16x16x32_bf16 v[28:31], a[244:247], v[44:47], v[28:31]\n"
    "s_waitcnt lgkmcnt(2)\n"
    "v_max_f32_dpp v75, v75, v75 row_ror:4 row_mask:0xf bank_mask:0xf\n"
    "v_max_f32_dpp v74, v74, v74 row_ror:2 row_mask:0xf bank_mask:0xf\n"
    "v_mfma_f32_16x16x32_bf16 v[0:3], a[100:103], v[48:51], v[0:3]\n"
    "ds_read_b128 v[40:43], v95 offset:2048\n"
    "ds_read_b128 v[44:47], v95 offset:3072\n"
    "v_mfma_f32_16x16x32_bf16 v[4:7], a[100:103], v[52:55], v[4:7]\n"
    "v_max_f32_dpp v75, v75, v75 row_ror:2 row_mask:0xf bank_mask:0xf\n"
    "v_max_f32_dpp v74, v74, v74 row_ror:1 row_mask:0xf bank_mask:0xf\n"
    "v_mfma_f32_16x16x32_bf16 v[8:11], a[180:183], v[48:51], v[8:11]\n"
    "buffer_store_dwordx2 v[110:111], v107, s[0:3], s49 offen sc1\n"
    "v_max_f32_dpp v75, v75, v75 row_ror:1 row_mask:0xf bank_mask:0xf\n"
    ".Lqk_mkL2_%=:\n"
    "v_mfma_f32_16x16x32_bf16 v[12:15], a[180:183], v[52:55], v[12:15]\n"
    "v_cndmask_b32 v72, v75, v74, s[66:67]\n"
    "v_mul_f32 v72, s62, v72\n"
    "v_mfma_f32_16x16x32_bf16 v[16:19], a[20:23], v[48:51], v[16:19]\n"
    "v_fma_f32 v56, v56, s62, -v72\n"
    "v_fma_f32 v57, v57, s62, -v72\n"
    "v_mfma_f32_16x16x32_bf16 v[20:23], a[20:23], v[52:55], v[20:23]\n"
    "v_fma_f32 v58, v58, s62, -v72\n"
    "v_fma_f32 v59, v59, s62, -v72\n"
    "v_mfma_f32_16x16x32_bf16 v[24:27], v[116:119], v[48:51], v[24:27]\n"
    "v_exp_f32 v56, v56\n"
    "v_mfma_f32_16x16x32_bf16 v[28:31], v[116:119], v[52:55], v[28:31]\n"
    "s_waitcnt lgkmcnt(2)\n"
    "v_exp_f32 v58, v58\n"
    "v_mfma_f32_16x16x32_bf16 v[0:3], a[116:119], v[32:35], v[0:3]\n"
    "ds_read_b128 v[48:51], v92 offset:4096\n"
    "ds_read_b128 v[52:55], v92 offset:5120\n"
    "v_mfma_f32_16x16x32_bf16 v[4:7], a[116:119], v[36:39], v[4:7]\n"
    "v_mfma_f32_16x16x32_bf16 v[8:11], a[196:199], v[32:35], v[8:11]\n"
    "buffer_load_dwordx2 v[108:109], off, s[0:3], s55 sc1\n"
    "v_mfma_f32_16x16x32_bf16 v[12:15], a[196:199], v[36:39], v[12:15]\n"
    "v_exp_f32 v57, v57\n"
    "v_mfma_f32_16x16x32_bf16 v[16:19], a[36:39], v[32:35], v[16:19]\n"
    "v_exp_f32 v59, v59\n"
    "v_mfma_f32_16x16x32_bf16 v[20:23], a[36:39], v[36:39], v[20:23]\n"
    "v_add_f32 v72, v56, v58\n"
    "v_add_f32 v73, v57, v59\n"
    "v_mfma_f32_16x16x32_bf16 v[24:27], v[132:135], v[32:35], v[24:27]\n"
    "v_add_f32 v72, v72, v73\n"
    "v_cndmask_b32 v74, 0, v72, s[66:67]\n"
    "v_mfma_f32_16x16x32_bf16 v[28:31], v[132:135], v[36:39], v[28:31]\n"
    "s_waitcnt lgkmcnt(2)\n"
    "v_cndmask_b32 v75, v72, 0, s[66:67]\n"
    "v_add_f32_dpp v74, v74, v74 row_ror:8 row_mask:0xf bank_mask:0xf\n"
    "v_mfma_f32_16x16x32_bf16 v[0:3], a[132:135], v[40:43], v[0:3]\n"
    "ds_read_b128 v[32:35], v93 offset:4096\n"
    "ds_read_b128 v[36:39], v93 offset:5120\n"
    "v_mfma_f32_16x16x32_bf16 v[4:7], a[132:135], v[44:47], v[4:7]\n"
    "v_add_f32_dpp v75, v75, v75 row_ror:8 row_mask:0xf bank_mask:0xf\n"
    "v_add_f32_dpp v74, v74, v74 row_ror:4 row_mask:0xf bank_mask:0xf\n"
    "v_mfma_f32_16x16x32_bf16 v[8:11], a[212:215], v[40:43], v[8:11]\n"
    "v_add_f32_dpp v75, v75, v75 row_ror:4 row_mask:0xf bank_mask:0xf\n"
    "v_add_f32_dpp v74, v74, v74 row_ror:2 row_mask:0xf bank_mask:0xf\n"
    "v_mfma_f32_16x16x32_bf16 v[12:15], a[212:215], v[44:47], v[12:15]\n"
    "v_add_f32_dpp v75, v75, v75 row_ror:2 row_mask:0xf bank_mask:0xf\n"
    "v_add_f32_dpp v74, v74, v74 row_ror:1 row_mask:0xf bank_mask:0xf\n"
    "v_mfma_f32_16x16x32_bf16 v[16:19], a[52:55], v[40:43], v[16:19]\n"
    "v_add_f32_dpp v75, v75, v75 row_ror:1 row_mask:0xf bank_mask:0xf\n"
    "v_cndmask_b32 v72, v75, v74, s[66:67]\n"
    "v_mfma_f32_16x16x32_bf16 v[20:23], a[52:55], v[44:47], v[20:23]\n"
    "v_rcp_f32 v72, v72\n"
    "v_mfma_f32_16x16x32_bf16 v[24:27], v[148:151], v[40:43], v[24:27]\n"
    "v_mul_f32 v56, v56, v72 clamp\n"
    "v_mul_f32 v57, v57, v72 clamp\n"
    "v_mfma_f32_16x16x32_bf16 v[28:31], v[148:151], v[44:47], v[28:31]\n"
    "s_waitcnt lgkmcnt(2)\n"
    "v_mul_f32 v58, v58, v72 clamp\n"
    "v_mul_f32 v59, v59, v72 clamp\n"
    "v_mfma_f32_16x16x32_bf16 v[0:3], a[88:91], v[48:51], v[0:3]\n"
    "ds_read_b128 v[40:43], v94 offset:4096\n"
    "ds_read_b128 v[44:47], v94 offset:5120\n"
    "v_mfma_f32_16x16x32_bf16 v[4:7], a[88:91], v[52:55], v[4:7]\n"
    "v_cvt_pk_bf16_f32 v56, v56, v57\n"
    "v_cvt_pk_bf16_f32 v57, v58, v59\n"
    "v_mfma_f32_16x16x32_bf16 v[8:11], a[168:171], v[48:51], v[8:11]\n"
    "buffer_store_dwordx2 v[56:57], v107, s[0:3], s50 offen sc1\n"
    ".Lqk_mkL3_%=:\n"
    "v_mfma_f32_16x16x32_bf16 v[12:15], a[168:171], v[52:55], v[12:15]\n"
    "s_cmp_eq_u32 s46, 63\n"
    "s_cbranch_scc1 .Lqk_end_%=\n"
    "v_mfma_f32_16x16x32_bf16 v[16:19], a[8:11], v[48:51], v[16:19]\n"
    "s_add_u32 s46, s46, 1\n"
    "s_add_u32 s60, s46, 1\n"
    "s_lshl_b32 s53, s60, 19\n"
    "v_mfma_f32_16x16x32_bf16 v[20:23], a[8:11], v[52:55], v[20:23]\n"
    "s_lshl_b32 s45, s60, 11\n"
    "s_xor_b32 s45, s45, s23\n"
    "s_and_b32 s45, s45, 0x2000\n"
    "v_mfma_f32_16x16x32_bf16 v[24:27], a[248:251], v[48:51], v[24:27]\n"
    "s_add_u32 s53, s53, s45\n"
    "s_add_u32 s53, s53, s27\n"
    "s_add_u32 s48, s46, 0\n"
    "v_mfma_f32_16x16x32_bf16 v[28:31], a[248:251], v[52:55], v[28:31]\n"
    "s_waitcnt lgkmcnt(2)\n"
    "s_and_b32 s48, s48, 3\n"
    "s_lshl_b32 s48, s48, 22\n"
    "v_mfma_f32_16x16x32_bf16 v[0:3], a[104:107], v[32:35], v[0:3]\n"
    "s_add_u32 s49, s46, 2\n"
    "ds_read_b128 v[48:51], v95 offset:4096\n"
    "ds_read_b128 v[52:55], v95 offset:5120\n"
    "v_mfma_f32_16x16x32_bf16 v[4:7], a[104:107], v[36:39], v[4:7]\n"
    "s_and_b32 s49, s49, 3\n"
    "s_lshl_b32 s49, s49, 22\n"
    "s_and_b32 s55, s46, 63\n"
    "v_mfma_f32_16x16x32_bf16 v[8:11], a[184:187], v[32:35], v[8:11]\n"
    "s_lshl_b32 s55, s55, 7\n"
    "s_mov_b32 s50, s48\n"
    "s_add_u32 s51, s24, s48\n"
    "v_mfma_f32_16x16x32_bf16 v[12:15], a[184:187], v[36:39], v[12:15]\n"
    "s_add_u32 s52, s25, s48\n"
    "s_add_u32 s56, s30, s55\n"
    "s_add_u32 s55, s31, s55\n"
    "v_mfma_f32_16x16x32_bf16 v[16:19], a[24:27], v[32:35], v[16:19]\n"
    "s_lshl_b32 s54, s60, 20\n"
    "s_lshl_b32 s45, s60, 12\n"
    "s_xor_b32 s45, s45, s23\n"
    "v_mfma_f32_16x16x32_bf16 v[20:23], a[24:27], v[36:39], v[20:23]\n"
    "s_and_b32 s45, s45, 0x2000\n"
    "s_add_u32 s54, s54, s45\n"
    "s_add_u32 s54, s54, s29\n"
    "v_mfma_f32_16x16x32_bf16 v[24:27], v[120:123], v[32:35], v[24:27]\n"
    "v_mfma_f32_16x16x32_bf16 v[28:31], v[120:123], v[36:39], v[28:31]\n"
    "s_waitcnt lgkmcnt(2)\n"
    "s_waitcnt vmcnt(6) lgkmcnt(0)\n"
    "s_barrier\n"
    "v_mfma_f32_16x16x32_bf16 v[0:3], a[120:123], v[40:43], v[0:3]\n"
    "s_sub_u32 m0, s26, 8192\n"
    "ds_read_b128 v[32:35], v92 offset:6144\n"
    "ds_read_b128 v[36:39], v92 offset:7168\n"
    "v_mfma_f32_16x16x32_bf16 v[4:7], a[120:123], v[44:47], v[4:7]\n"
    "buffer_load_dwordx4 v96, s[0:3], s53 offen offset:0 sc1 lds\n"
    "v_mfma_f32_16x16x32_bf16 v[8:11], a[200:203], v[40:43], v[8:11]\n"
    ".Lqk_mkL4_%=:\n"
    "v_mfma_f32_16x16x32_bf16 v[12:15], a[200:203], v[44:47], v[12:15]\n"
    "v_mfma_f32_16x16x32_bf16 v[16:19], a[40:43], v[40:43], v[16:19]\n"
    "v_mfma_f32_16x16x32_bf16 v[20:23], a[40:43], v[44:47], v[20:23]\n"
    "buffer_load_dwordx4 v96, s[0:3], s53 offen offset:1024 sc1 lds\n"
    "v_mfma_f32_16x16x32_bf16 v[24:27], v[136:139], v[40:43], v[24:27]\n"
    "v_mfma_f32_16x16x32_bf16 v[28:31], v[136:139], v[44:47], v[28:31]\n"
    "s_waitcnt lgkmcnt(2)\n"
    "v_mfma_f32_16x16x32_bf16 v[0:3], a[136:139], v[48:51], v[0:3]\n"
    "ds_read_b128 v[40:43], v93 offset:6144\n"
    "ds_read_b128 v[44:47], v93 offset:7168\n"
    "v_mfma_f32_16x16x32_bf16 v[4:7], a[136:139], v[52:55], v[4:7]\n"
    "buffer_load_dwordx4 v96, s[0:3], s53 offen offset:2048 sc1 lds\n"
    "v_mfma_f32_16x16x32_bf16 v[8:11], a[216:219], v[48:51], v[8:11]\n"
    "v_mfma_f32_16x16x32_bf16 v[12:15], a[216:219], v[52:55], v[12:15]\n"
    "v_mfma_f32_16x16x32_bf16 v[16:19], a[56:59], v[48:51], v[16:19]\n"
    "v_mfma_f32_16x16x32_bf16 v[20:23], a[56:59], v[52:55], v[20:23]\n"
    "buffer_load_dwordx4 v96, s[0:3], s53 offen offset:3072 sc1 lds\n"
    "v_mfma_f32_16x16x32_bf16 v[24:27], v[152:155], v[48:51], v[24:27]\n"
    "s_sub_u32 m0, s26, 4096\n"
    "s_add_u32 s53, s53, 4096\n"
    "v_mfma_f32_16x16x32_bf16 v[28:31], v[152:155], v[52:55], v[28:31]\n"
    "s_waitcnt lgkmcnt(2)\n"
    "v_mfma_f32_16x16x32_bf16 v[0:3], a[92:95], v[32:35], v[0:3]\n"
    "ds_read_b128 v[48:51], v94 offset:6144\n"
    "ds_read_b128 v[52:55], v94 offset:7168\n"
    "v_mfma_f32_16x16x32_bf16 v[4:7], a[92:95], v[36:39], v[4:7]\n"
    "buffer_load_dwordx4 v96, s[0:3], s53 offen offset:0 sc1 lds\n"
    "v_mfma_f32_16x16x32_bf16 v[8:11], a[172:175], v[32:35], v[8:11]\n"
    "v_mfma_f32_16x16x32_bf16 v[12:15], a[172:175], v[36:39], v[12:15]\n"
    "v_mfma_f32_16x16x32_bf16 v[16:19], a[12:15], v[32:35], v[16:19]\n"
    "v_mfma_f32_16x16x32_bf16 v[20:23], a[12:15], v[36:39], v[20:23]\n"
    "buffer_load_dwordx4 v96, s[0:3], s53 offen offset:1024 sc1 lds\n"
    "v_mfma_f32_16x16x32_bf16 v[24:27], a[252:255], v[32:35], v[24:27]\n"
    "v_mfma_f32_16x16x32_bf16 v[28:31], a[252:255], v[36:39], v[28:31]\n"
    "s_waitcnt lgkmcnt(2)\n"
    "v_mfma_f32_16x16x32_bf16 v[0:3], a[108:111], v[40:43], v[0:3]\n"
    "ds_read_b128 v[32:35], v95 offset:6144\n"
    "ds_read_b128 v[36:39], v95 offset:7168\n"
    "v_mfma_f32_16x16x32_bf16 v[4:7], a[108:111], v[44:47], v[4:7]\n"
    "v_mfma_f32_16x16x32_bf16 v[8:11], a[188:191], v[40:43], v[8:11]\n"
    ".Lqk_mkL5_%=:\n"
    "v_mfma_f32_16x16x32_bf16 v[12:15], a[188:191], v[44:47], v[12:15]\n"
    "v_mfma_f32_16x16x32_bf16 v[16:19], a[28:31], v[40:43], v[16:19]\n"
    "v_mfma_f32_16x16x32_bf16 v[20:23], a[28:31], v[44:47], v[20:23]\n"
    "v_mfma_f32_16x16x32_bf16 v[24:27], v[124:127], v[40:43], v[24:27]\n"
    "v_mfma_f32_16x16x32_bf16 v[28:31], v[124:127], v[44:47], v[28:31]\n"
    "s_waitcnt lgkmcnt(2)\n"
    "v_mfma_f32_16x16x32_bf16 v[0:3], a[124:127], v[48:51], v[0:3]\n"
    "ds_read_b128 v[40:43], v96 offset:45056\n"
    "ds_read_b128 v[44:47], v96 offset:46080\n"
    "v_mfma_f32_16x16x32_bf16 v[4:7], a[124:127], v[52:55], v[4:7]\n"
    "v_mfma_f32_16x16x32_bf16 v[8:11], a[204:207], v[48:51], v[8:11]\n"
    "v_mfma_f32_16x16x32_bf16 v[12:15], a[204:207], v[52:55], v[12:15]\n"
    "v_mfma_f32_16x16x32_bf16 v[16:19], a[44:47], v[48:51], v[16:19]\n"
    "v_mfma_f32_16x16x32_bf16 v[20:23], a[44:47], v[52:55], v[20:23]\n"
    "v_mfma_f32_16x16x32_bf16 v[24:27], v[140:143], v[48:51], v[24:27]\n"
    "v_mfma_f32_16x16x32_bf16 v[28:31], v[140:143], v[52:55], v[28:31]\n"
    "s_waitcnt lgkmcnt(2)\n"
    "v_mfma_f32_16x16x32_bf16 v[0:3], a[140:143], v[32:35], v[0:3]\n"
    "ds_read_b128 v[48:51], v96 offset:47104\n"
    "ds_read_b128 v[52:55], v96 offset:48128\n"
    "v_mfma_f32_16x16x32_bf16 v[4:7], a[140:143], v[36:39], v[4:7]\n"
    "v_mfma_f32_16x16x32_bf16 v[8:11], a[220:223], v[32:35], v[8:11]\n"
    "v_mfma_f32_16x16x32_bf16 v[12:15], a[220:223], v[36:39], v[12:15]\n"
    "v_mfma_f32_16x16x32_bf16 v[16:19], a[60:63], v[32:35], v[16:19]\n"
    "v_mfma_f32_16x16x32_bf16 v[20:23], a[60:63], v[36:39], v[20:23]\n"
    "v_mfma_f32_16x16x32_bf16 v[24:27], v[156:159], v[32:35], v[24:27]\n"
    "v_mfma_f32_16x16x32_bf16 v[28:31], v[156:159], v[36:39], v[28:31]\n"
    "s_waitcnt lgkmcnt(2)\n"
    "v_mfma_f32_16x16x32_bf16 v[0:3], a[144:147], v[40:43], v[0:3]\n"
    "s_waitcnt vmcnt(7)\n"
    "ds_read_b128 v[32:35], v96 offset:49152\n"
    "ds_read_b128 v[36:39], v96 offset:50176\n"
    "s_memrealtime s[70:71]\n"
    "v_mfma_f32_16x16x32_bf16 v[4:7], a[144:147], v[44:47], v[4:7]\n"
    "v_readfirstlane_b32 s44, v108\n"
    "v_readfirstlane_b32 s45, v109\n"
    "v_mfma_f32_16x16x32_bf16 v[8:11], a[224:227], v[40:43], v[8:11]\n"
    "v_mfma_f32_16x16x32_bf16 v[12:15], a[224:227], v[44:47], v[12:15]\n"
    "s_cmp_eq_u32 s45, s47\n"
    "s_cselect_b32 s57, s44, s57\n"
    "s_add_u32 s57, s57, 268\n"
    "v_mfma_f32_16x16x32_bf16 v[16:19], a[64:67], v[40:43], v[16:19]\n"
    "s_sub_u32 s58, s57, 59\n"
    "s_mov_b32 s47, s60\n"
    "s_mov_b32 m0, s26\n"
    "v_mfma_f32_16x16x32_bf16 v[20:23], a[64:67], v[44:47], v[20:23]\n"
    ".Lqk_mkL6_%=:\n"
    "v_mfma_f32_16x16x32_bf16 v[24:27], v[160:163], v[40:43], v[24:27]\n"
    "s_sub_u32 s72, s58, s70\n"
    "v_mfma_f32_16x16x32_bf16 v[28:31], v[160:163], v[44:47], v[28:31]\n"
    "s_waitcnt lgkmcnt(2)\n"
    ".Lqk_mk6x16_%=:\n"
    "s_cmp_gt_i32 s72, 4\n"
    "s_cbranch_scc1 .Lqk_early_%=\n"
    "buffer_load_dwordx4 v96, s[0:3], s51 offen sc1 lds\n"
    "buffer_load_dwordx4 v96, s[0:3], s51 offen offset:1024 sc1 lds\n"
    ".Lqk_asked_%=:\n"
    ".Lqk_mk7x18_%=:\n"
    "v_mfma_f32_16x16x32_bf16 v[0:3], a[148:151], v[48:51], v[0:3]\n"
    "ds_read_b128 v[40:43], v96 offset:51200\n"
    "ds_read_b128 v[44:47], v96 offset:52224\n"
    "v_mfma_f32_16x16x32_bf16 v[4:7], a[148:151], v[52:55], v[4:7]\n"
    ".Lqk_mkM0_%=:\n"
    "v_mfma_f32_16x16x32_bf16 v[8:11], a[228:231], v[48:51], v[8:11]\n"
    "v_mfma_f32_16x16x32_bf16 v[12:15], a[228:231], v[52:55], v[12:15]\n"
    "v_mfma_f32_16x16x32_bf16 v[16:19], a[68:71], v[48:51], v[16:19]\n"
    "v_mfma_f32_16x16x32_bf16 v[20:23], a[68:71], v[52:55], v[20:23]\n"
    ".Lqk_mkM1_%=:\n"
    "v_mfma_f32_16x16x32_bf16 v[24:27], v[164:167], v[48:51], v[24:27]\n"
    "v_mfma_f32_16x16x32_bf16 v[28:31], v[164:167], v[52:55], v[28:31]\n"
    "s_waitcnt lgkmcnt(2)\n"
    "v_mfma_f32_16x16x32_bf16 v[0:3], a[152:155], v[32:35], v[0:3]\n"
    "buffer_load_dwordx4 v96, s[0:3], s52 offen offset:2048 sc1 lds\n"
    "v_mfma_f32_16x16x32_bf16 v[4:7], a[152:155], v[36:39], v[4:7]\n"
    "v_mfma_f32_16x16x32_bf16 v[8:11], a[232:235], v[32:35], v[8:11]\n"
    ".Lqk_mkM2_%=:\n"
    "v_mfma_f32_16x16x32_bf16 v[12:15], a[232:235], v[36:39], v[12:15]\n"
    "v_mfma_f32_16x16x32_bf16 v[16:19], a[72:75], v[32:35], v[16:19]\n"
    "v_mfma_f32_16x16x32_bf16 v[20:23], a[72:75], v[36:39], v[20:23]\n"
    "v_mfma_f32_16x16x32_bf16 v[24:27], v[168:171], v[32:35], v[24:27]\n"
    ".Lqk_mkM3_%=:\n"
    "v_mfma_f32_16x16x32_bf16 v[28:31], v[168:171], v[36:39], v[28:31]\n"
    "s_waitcnt lgkmcnt(0)\n"
    "v_mfma_f32_16x16x32_bf16 v[0:3], a[156:159], v[40:43], v[0:3]\n"
    "v_mfma_f32_16x16x32_bf16 v[4:7], a[156:159], v[44:47], v[4:7]\n"
    "v_mfma_f32_16x16x32_bf16 v[8:11], a[236:239], v[40:43], v[8:11]\n"
    "v_mfma_f32_16x16x32_bf16 v[12:15], a[236:239], v[44:47], v[12:15]\n"
    "v_mfma_f32_16x16x32_bf16 v[16:19], a[76:79], v[40:43], v[16:19]\n"
    "v_mfma_f32_16x16x32_bf16 v[20:23], a[76:79], v[44:47], v[20:23]\n"
    "v_mfma_f32_16x16x32_bf16 v[24:27], v[172:175], v[40:43], v[24:27]\n"
    ".Lqk_mkM4_%=:\n"
    "v_mfma_f32_16x16x32_bf16 v[28:31], v[172:175], v[44:47], v[28:31]\n"
    "ds_read_b128 v[32:35], v97 offset:0\n"
    "ds_read_b128 v[36:39], v97 offset:1024\n"
    "ds_read_b128 v[40:43], v97 offset:2048\n"
    "ds_read_b128 v[44:47], v97 offset:3072\n"
    "ds_read_b128 v[48:51], v97 offset:4096\n"
    "ds_read_b128 v[52:55], v97 offset:5120\n"
    "v_cvt_pk_bf16_f32 v68, v0, v1\n"
    "v_cvt_pk_bf16_f32 v69, v2, v3\n"
    "v_cvt_pk_bf16_f32 v70, v8, v9\n"
    "v_cvt_pk_bf16_f32 v71, v10, v11\n"
    "v_cvt_pk_bf16_f32 v72, v4, v5\n"
    "v_cvt_pk_bf16_f32 v73, v6, v7\n"
    "v_cvt_pk_bf16_f32 v74, v12, v13\n"
    "v_cvt_pk_bf16_f32 v75, v14, v15\n"
    "v_mfma_f32_16x16x32_bf16 v[108:111], v[68:71], v[84:87], 0\n"
    "v_cvt_pk_bf16_f32 v88, v24, v25\n"
    "v_cvt_pk_bf16_f32 v89, v26, v27\n"
    "v_cvt_pk_bf16_f32 v90, v28, v29\n"
    "v_cvt_pk_bf16_f32 v91, v30, v31\n"
    "v_mfma_f32_16x16x32_bf16 v[24:27], v[72:75], v[84:87], 0\n"
    "v_cvt_pk_bf16_f32 v76, v16, v17\n"
    "v_cvt_pk_bf16_f32 v77, v18, v19\n"
    "v_cvt_pk_bf16_f32 v78, v20, v21\n"
    "v_cvt_pk_bf16_f32 v79, v22, v23\n"
    "v_mfma_f32_16x16x32_bf16 v[28:31], v[68:71], v[80:83], 0\n"
    "v_mov_b64 v[0:1], 0\n"
    "v_mov_b64 v[2:3], 0\n"
    "v_mfma_f32_16x16x16_bf16 v[108:111], v[76:77], v[90:91], v[108:111]\n"
    "v_mov_b64 v[4:5], 0\n"
    "v_mov_b64 v[6:7], 0\n"
    "v_mfma_f32_16x16x16_bf16 v[24:27], v[78:79], v[90:91], v[24:27]\n"
    "v_mov_b64 v[8:9], 0\n"
    "v_mov_b64 v[10:11], 0\n"
    "v_mfma_f32_16x16x16_bf16 v[28:31], v[76:77], v[88:89], v[28:31]\n"
    "v_mov_b64 v[12:13], 0\n"
    "v_mov_b64 v[14:15], 0\n"
    "v_mov_b64 v[16:17], 0\n"
    "v_mov_b64 v[18:19], 0\n"
    "v_mov_b64 v[20:21], 0\n"
    "v_mov_b64 v[22:23], 0\n"
    "ds_write_b128 v104, v[108:111]\n"
    "ds_write_b128 v104, v[24:27] offset:64\n"
    "ds_write_b128 v104, v[28:31] offset:160\n"
    ".Lqk_mk8x19_%=:\n"
    "s_branch .Lqk_take_%=\n"
    ".Lqk_early_%=:\n"
    ".Lqk_rt18_%=:\n"
    "s_memrealtime s[42:43]\n"
    "s_waitcnt lgkmcnt(0)\n"
    "s_sub_u32 s44, s58, s42\n"
    "s_cmp_lt_u32 s44, 600\n"
    "s_cbranch_scc1 .Lqk_rt18_%=\n"
    "buffer_load_dwordx4 v96, s[0:3], s51 offen sc1 lds\n"
    "buffer_load_dwordx4 v96, s[0:3], s51 offen offset:1024 sc1 lds\n"
    "s_branch .Lqk_asked_%=\n"
    ".Lqk_retry_%=:\n"
    "s_add_u32 s40, s40, 1\n"
    "s_cmp_lt_u32 s40, 0x40000\n"
    "s_cbranch_scc1 .Lqk_sp21_%=\n"
    "v_mov_b32 v108, 1\n"
    "s_mov_b32 s44, 33728\n"
    "buffer_store_dword v108, off, s[0:3], s44 sc1\n"
    "s_waitcnt vmcnt(0)\n"
    "s_branch .Lqk_go_%=\n"
    ".Lqk_sp21_%=:\n"
    "s_waitcnt lgkmcnt(0)\n"
    "s_nop 7\n"
    "ds_read_b128 v[32:35], v97 offset:0\n"
    "ds_read_b128 v[36:39], v97 offset:1024\n"
    "v_mov_b64 v[0:1], 0\n"
    "v_mov_b64 v[2:3], 0\n"
    "v_mov_b64 v[4:5], 0\n"
    "v_mov_b64 v[6:7], 0\n"
    "v_mov_b64 v[8:9], 0\n"
    "v_mov_b64 v[10:11], 0\n"
    "v_mov_b64 v[12:13], 0\n"
    "v_mov_b64 v[14:15], 0\n"
    "v_mov_b64 v[16:17], 0\n"
    "v_mov_b64 v[18:19], 0\n"
    "v_mov_b64 v[20:21], 0\n"
    "v_mov_b64 v[22:23], 0\n"
    "buffer_load_dwordx4 v96, s[0:3], s51 offen sc1 lds\n"
    "buffer_load_dwordx4 v96, s[0:3], s51 offen offset:1024 sc1 lds\n"
    "buffer_load_dwordx4 v96, s[0:3], s52 offen offset:2048 sc1 lds\n"
    "s_waitcnt vmcnt(0)\n"
    "s_branch .Lqk_take_%=\n"
    ".Lqk_r2_%=:\n"
    "s_add_u32 s40, s40, 1\n"
    "s_cmp_lt_u32 s40, 0x40000\n"
    "s_cbranch_scc1 .Lqk_sp22_%=\n"
    "v_mov_b32 v108, 5\n"
    "s_mov_b32 s44, 33728\n"
    "buffer_store_dword v108, off, s[0:3], s44 sc1\n"
    "s_waitcnt vmcnt(0)\n"
    "s_branch .Lqk_go2_%=\n"
    ".Lqk_sp22_%=:\n"
    "buffer_load_dwordx4 v96, s[0:3], s52 offen offset:2048 sc1 lds\n"
    "s_waitcnt vmcnt(0)\n"
    "ds_read_b128 v[64:67], v92 offset:10240\n"
    "s_waitcnt lgkmcnt(0)\n"
    "v_max_u32 v108, v64, v66\n"
    "v_cmp_eq_u32 vcc, -1, v108\n"
    "s_nop 0\n"
    "s_cbranch_vccnz .Lqk_r2_%=\n"
    "s_branch .Lqk_go2_%=\n"
    ".Lqk_end_%=:\n"
    "s_waitcnt vmcnt(0) lgkmcnt(0)\n"
    "s_endpgm\n"
    :: "{s[0:3]}"(s0_), "{s[4:7]}"(s4_), "{s[8:11]}"(s8_), "{s[12:15]}"(s12_), "{s[16:19]}"(s16_), "{s[20:23]}"(s20_), "{s[24:27]}"(s24_), "{s[28:31]}"(s28_), "{s[32:35]}"(s32_), "{s[36:39]}"(s36_), "{v[0:3]}"(v0_), "{v[4:7]}"(v4_), "{v[92:95]}"(v92_), "{v[96:99]}"(v96_), "{v[100:103]}"(v100_), "{v[104:107]}"(v104_) : "memory", "v255", "a255", "s95");
    __builtin_unreachable();
}
DEV void vo_role(const MegaArgs& A, const int L, const int h, const int rep) {
    ROLE_HEAD;
    const unsigned um = xl(RG_UM + ((8 * w + lq) * 4 + (h >> 3)) * 2048 + (h & 7) * 256 + l16 * 16, L);
#define XU 0
    const unsigned umx = XU ? OOB : xl(RG_UMX + ((4 * (2 * w + (lq & 1)) + (l16 >> 2)) * 4 + (h >> 3)) * 512 + (h & 7) * 64 + (l & 3) * 16 + (lq >> 1) * 8, L);
    const u32x4 s0_ = {RFL(rx[0]), RFL(rx[1]), RFL(rx[2]), RFL(rx[3])};
    const u32x4 s4_ = {RFL(lo(A.Wo + h * 192 * 640)), RFL(hi(A.Wo + h * 192 * 640)), RFL(192u * 1280u), RFL(0x20000u)};
    const u32x4 s8_ = {RFL(lo(A.Wv + h * 192)), RFL(hi(A.Wv + h * 192)), RFL(WBYTES - h * 384), RFL(0x20000u)};
    const u32x4 s12_ = {RFL(w), RFL(L << 13), RFL(L), RFL(xl(RG_MEM + rep * 16384, L) + w * 2048)};
    const u32x4 s16_ = {RFL(w < 2 ? xl(RG_MEM + rep * 16384 + 8192, L) + w * 1024 - 2048 : OOB), RFL(w * 13312 + 8192), RFL(REC0 + w * 16384), RFL(65536 - 4096 - w * 14336)};
    const u32x4 s20_ = {RFL(QT0 + h * 32768 + 16384 + w * 1024), RFL(146432 + w * 1024), RFL(xl(RG_UT + (32 * w + (h >> 3)) * 8192 + (h & 7) * 1024, L)), RFL(xl(Z_X + 8192, L))};
    const u32x4 s24_ = {RFL(xl(Z_CT + (h & 7) * 256, L)), RFL(w == 0 ? xl(Z_QV + (32 + h) * 4, L) : OOB), RFL(DBG0 + ((L * 128 + 32 + h) * 4 + w) * 2048), RFL(0u)};
    const u32x4 v0_ = {(unsigned)(((3 * w + 0) * 64 + l) / 24 * 12288 + ((((3 * w + 0) * 64 + l) % 24) ^ ((((3 * w + 0) * 64 + l) / 48) & 6)) * 16 - 0), (unsigned)(((3 * w + 1) * 64 + l) / 24 * 12288 + ((((3 * w + 1) * 64 + l) % 24) ^ ((((3 * w + 1) * 64 + l) / 48) & 6)) * 16 - 1024), (unsigned)(((3 * w + 2) * 64 + l) / 24 * 12288 + ((((3 * w + 2) * 64 + l) % 24) ^ ((((3 * w + 2) * 64 + l) / 48) & 6)) * 16 - 2048), (unsigned)((8 * lq + ((l >> 2) & 1) + ((l >> 1) & 4)) * 384 + (w ^ (((8 * lq + ((l >> 2) & 1) + ((l >> 1) & 4)) >> 2) & 3)) * 32 + (l & 3) * 8 + ((w + 0) & 3) * 12288)};
    const u32x4 v4_ = {(unsigned)((8 * lq + ((l >> 2) & 1) + ((l >> 1) & 4)) * 384 + (w ^ (((8 * lq + ((l >> 2) & 1) + ((l >> 1) & 4)) >> 2) & 3)) * 32 + (l & 3) * 8 + ((w + 1) & 3) * 12288), (unsigned)((8 * lq + ((l >> 2) & 1) + ((l >> 1) & 4)) * 384 + (w ^ (((8 * lq + ((l >> 2) & 1) + ((l >> 1) & 4)) >> 2) & 3)) * 32 + (l & 3) * 8 + ((w + 2) & 3) * 12288), (unsigned)((8 * lq + ((l >> 2) & 1) + ((l >> 1) & 4)) * 384 + (w ^ (((8 * lq + ((l >> 2) & 1) + ((l >> 1) & 4)) >> 2) & 3)) * 32 + (l & 3) * 8 + ((w + 3) & 3) * 12288), (unsigned)((8 * lq + ((l >> 2) & 1) + ((l >> 1) & 4)) * 384 + (w ^ (((8 * lq + ((l >> 2) & 1) + ((l >> 1) & 4)) >> 2) & 3)) * 32 + (l & 3) * 8)};
    const u32x4 v8_ = {(unsigned)(0), (unsigned)(0), (unsigned)(0), (unsigned)(0)};
    const u32x4 v12_ = {(unsigned)((4 * 0 + lq) * 1280 + 256 * w + ((l16 ^ (2 * ((4 * 0 + lq) & 7))) * 16) - 0), (unsigned)((4 * 1 + lq) * 1280 + 256 * w + ((l16 ^ (2 * ((4 * 1 + lq) & 7))) * 16) - 1024), (unsigned)((4 * 2 + lq) * 1280 + 256 * w + ((l16 ^ (2 * ((4 * 2 + lq) & 7))) * 16) - 2048), (unsigned)((4 * 3 + lq) * 1280 + 256 * w + ((l16 ^ (2 * ((4 * 3 + lq) & 7))) * 16) - 3072)};
    const u32x4 v16_ = {(unsigned)((4 * w + lq) * 1280 + 1024 + ((l16 ^ (2 * ((4 * w + lq) & 7))) * 16)), (unsigned)(w * 8192 + (l >> 2) * 256 + ((l >> 2) & 7) * 32 + (l & 3) * 8), (unsigned)(32768 + (l >> 2) * 256 + (((l >> 2) & 7) ^ (2 * w)) * 32 + (l & 3) * 8), (unsigned)(0u)};
    const u32x4 v92_ = {(unsigned)(l * 16 + ((w + 0) & 3) * 13312), (unsigned)(l * 16 + ((w + 1) & 3) * 13312), (unsigned)(l * 16 + ((w + 2) & 3) * 13312), (unsigned)(l * 16 + ((w + 3) & 3) * 13312)};
    const u32x4 v96_ = {(unsigned)(l * 16), (unsigned)(l * 16 + 64512 + w * 20480), (unsigned)((l & 2) ? 0xeeeeu : 0x4444u), (unsigned)(61440 + 512 * w + (l16 >> 2) * 64 + (l & 1) * 32 + sw2(lq) * 8 + (l & 2) * 2)};
    const u32x4 v100_ = {(unsigned)(61440 + 2048 + 256 * w + (l16 >> 2) * 64 + (l & 1) * 32 + sw2(lq) * 8 + (l & 2) * 2), (unsigned)(61440 + 512 * (lq >> 1) + 128 * (lq & 1) + (l & 4) * 8 + (l & 8) * 32 + (l & 3) * 8), (unsigned)(um), (unsigned)(umx)};
    const u32x4 v104_ = {(unsigned)(xl(RG_UTX + ((8 * w + (l16 >> 2)) * 4 + (h >> 3)) * 2048 + (h & 1) * 1024 + ((h & 7) >> 1) * 256 + lq * 64 + (l & 3) * 16, L)), (unsigned)(l < 4 || l > 8 ? xl(Z_QW + h * 128 + (l & 3) * 4, L) : (L ? xl(Z_XN + rep * 2048 + (l - 4) * 128, 0) : OOB)), (unsigned)(146432 + l * 16), (unsigned)(148480 + l * 16)};
    asm volatile(
    "s_mov_b32 s28, 0\n"
    "s_cmp_eq_u32 s13, 0\n"
    "s_cselect_b32 s32, 0, 24\n"
    ".Lvo_ho1_%=:\n"
    "s_sub_u32 s32, s32, 1\n"
    "s_cbranch_scc1 .Lvo_hd2_%=\n"
    "s_sleep 17\n"
    "s_branch .Lvo_ho1_%=\n"
    ".Lvo_hd2_%=:\n"
    "s_mul_i32 s41, s12, 3072\n"
    "s_lshl_b32 s42, s12, 13\n"
    "s_lshl_b32 s43, s12, 10\n"
    "s_mov_b32 s49, 0\n"
    "s_add_u32 m0, s42, 0\n"
    "s_nop 0\n"
    "buffer_load_dwordx4 v12, s[4:7], s49 offen offset:0 nt lds\n"
    "buffer_load_dwordx4 v13, s[4:7], s49 offen offset:1024 nt lds\n"
    "buffer_load_dwordx4 v14, s[4:7], s49 offen offset:2048 nt lds\n"
    "buffer_load_dwordx4 v15, s[4:7], s49 offen offset:3072 nt lds\n"
    "s_add_u32 m0, s43, 32768\n"
    "s_nop 0\n"
    "buffer_load_dwordx4 v16, s[4:7], s49 offen nt lds\n"
    "s_mov_b32 s49, 81920\n"
    "s_add_u32 m0, s42, 4096\n"
    "s_nop 0\n"
    "buffer_load_dwordx4 v12, s[4:7], s49 offen offset:0 nt lds\n"
    "buffer_load_dwordx4 v13, s[4:7], s49 offen offset:1024 nt lds\n"
    "buffer_load_dwordx4 v14, s[4:7], s49 offen offset:2048 nt lds\n"
    "buffer_load_dwordx4 v15, s[4:7], s49 offen offset:3072 nt lds\n"
    "s_add_u32 m0, s43, 36864\n"
    "s_nop 0\n"
    "buffer_load_dwordx4 v16, s[4:7], s49 offen nt lds\n"
    "s_mov_b32 s49, 20480\n"
    "s_add_u32 m0, s42, 49152\n"
    "s_nop 0\n"
    "buffer_load_dwordx4 v12, s[4:7], s49 offen offset:0 nt lds\n"
    "buffer_load_dwordx4 v13, s[4:7], s49 offen offset:1024 nt lds\n"
    "buffer_load_dwordx4 v14, s[4:7], s49 offen offset:2048 nt lds\n"
    "buffer_load_dwordx4 v15, s[4:7], s49 offen offset:3072 nt lds\n"
    "s_add_u32 m0, s43, 81920\n"
    "s_nop 0\n"
    "buffer_load_dwordx4 v16, s[4:7], s49 offen nt lds\n"
    "s_mov_b32 s49, 102400\n"
    "s_add_u32 m0, s42, 53248\n"
    "s_nop 0\n"
    "buffer_load_dwordx4 v12, s[4:7], s49 offen offset:0 nt lds\n"
    "buffer_load_dwordx4 v13, s[4:7], s49 offen offset:1024 nt lds\n"
    "buffer_load_dwordx4 v14, s[4:7], s49 offen offset:2048 nt lds\n"
    "buffer_load_dwordx4 v15, s[4:7], s49 offen offset:3072 nt lds\n"
    "s_add_u32 m0, s43, 86016\n"
    "s_nop 0\n"
    "buffer_load_dwordx4 v16, s[4:7], s49 offen nt lds\n"
    "s_mov_b32 s49, 40960\n"
    "s_add_u32 m0, s42, 98304\n"
    "s_nop 0\n"
    "buffer_load_dwordx4 v12, s[4:7], s49 offen offset:0 nt lds\n"
    "buffer_load_dwordx4 v13, s[4:7], s49 offen offset:1024 nt lds\n"
    "buffer_load_dwordx4 v14, s[4:7], s49 offen offset:2048 nt lds\n"
    "buffer_load_dwordx4 v15, s[4:7], s49 offen offset:3072 nt lds\n"
    "s_add_u32 m0, s43, 131072\n"
    "s_nop 0\n"
    "buffer_load_dwordx4 v16, s[4:7], s49 offen nt lds\n"
    "s_mov_b32 s49, 122880\n"
    "s_add_u32 m0, s42, 102400\n"
    "s_nop 0\n"
    "buffer_load_dwordx4 v12, s[4:7], s49 offen offset:0 nt lds\n"
    "buffer_load_dwordx4 v13, s[4:7], s49 offen offset:1024 nt lds\n"
    "buffer_load_dwordx4 v14, s[4:7], s49 offen offset:2048 nt lds\n"
    "buffer_load_dwordx4 v15, s[4:7], s49 offen offset:3072 nt lds\n"
    "s_add_u32 m0, s43, 135168\n"
    "s_nop 0\n"
    "buffer_load_dwordx4 v16, s[4:7], s49 offen nt lds\n"
    "v_mov_b64 v[108:109], -1\n"
    "v_mov_b64 v[110:111], -1\n"
    "s_mov_b32 s49, 0x10000\n"
    "buffer_store_dwordx4 v[108:111], v102, s[0:3], 0 offen sc1\n"
    "buffer_store_dwordx4 v[108:111], v102, s[0:3], s49 offen sc1\n"
    "buffer_store_dwordx2 v[108:109], v103, s[0:3], 0 offen sc1\n"
    "s_waitcnt vmcnt(23)\n"
    "s_barrier\n"
    "v_add_u32 v24, 0, v17\n"
    "v_add_u32 v32, 0, v18\n"
    "v_xor_b32 v33, 32, v32\n"
    "v_xor_b32 v25, 32, v24\n"
    "v_xor_b32 v26, 64, v24\n"
    "v_xor_b32 v27, 96, v24\n"
    "v_xor_b32 v28, 128, v24\n"
    "v_xor_b32 v29, 160, v24\n"
    "v_xor_b32 v30, 192, v24\n"
    "v_xor_b32 v31, 224, v24\n"
    "ds_read_b64_tr_b16 a[0:1], v24 offset:0\n"
    "ds_read_b64_tr_b16 a[2:3], v24 offset:4096\n"
    "ds_read_b64_tr_b16 a[24:25], v25 offset:0\n"
    "ds_read_b64_tr_b16 a[26:27], v25 offset:4096\n"
    "ds_read_b64_tr_b16 a[48:49], v26 offset:0\n"
    "ds_read_b64_tr_b16 a[50:51], v26 offset:4096\n"
    "ds_read_b64_tr_b16 a[72:73], v27 offset:0\n"
    "ds_read_b64_tr_b16 a[74:75], v27 offset:4096\n"
    "ds_read_b64_tr_b16 a[96:97], v28 offset:0\n"
    "ds_read_b64_tr_b16 a[98:99], v28 offset:4096\n"
    "ds_read_b64_tr_b16 a[120:121], v29 offset:0\n"
    "ds_read_b64_tr_b16 a[122:123], v29 offset:4096\n"
    "ds_read_b64_tr_b16 a[144:145], v30 offset:0\n"
    "ds_read_b64_tr_b16 a[146:147], v30 offset:4096\n"
    "ds_read_b64_tr_b16 a[168:169], v31 offset:0\n"
    "ds_read_b64_tr_b16 a[170:171], v31 offset:4096\n"
    "ds_read_b64_tr_b16 a[192:193], v32 offset:0\n"
    "ds_read_b64_tr_b16 a[194:195], v32 offset:4096\n"
    "ds_read_b64_tr_b16 a[216:217], v33 offset:0\n"
    "ds_read_b64_tr_b16 a[218:219], v33 offset:4096\n"
    "s_waitcnt lgkmcnt(0)\n"
    "s_barrier\n"
    "s_mov_b32 s49, 61440\n"
    "s_add_u32 m0, s42, 0\n"
    "s_nop 0\n"
    "buffer_load_dwordx4 v12, s[4:7], s49 offen offset:0 nt lds\n"
    "buffer_load_dwordx4 v13, s[4:7], s49 offen offset:1024 nt lds\n"
    "buffer_load_dwordx4 v14, s[4:7], s49 offen offset:2048 nt lds\n"
    "buffer_load_dwordx4 v15, s[4:7], s49 offen offset:3072 nt lds\n"
    "s_add_u32 m0, s43, 32768\n"
    "s_nop 0\n"
    "buffer_load_dwordx4 v16, s[4:7], s49 offen nt lds\n"
    "s_mov_b32 s49, 143360\n"
    "s_add_u32 m0, s42, 4096\n"
    "s_nop 0\n"
    "buffer_load_dwordx4 v12, s[4:7], s49 offen offset:0 nt lds\n"
    "buffer_load_dwordx4 v13, s[4:7], s49 offen offset:1024 nt lds\n"
    "buffer_load_dwordx4 v14, s[4:7], s49 offen offset:2048 nt lds\n"
    "buffer_load_dwordx4 v15, s[4:7], s49 offen offset:3072 nt lds\n"
    "s_add_u32 m0, s43, 36864\n"
    "s_nop 0\n"
    "buffer_load_dwordx4 v16, s[4:7], s49 offen nt lds\n"
    "s_waitcnt vmcnt(23)\n"
    "s_barrier\n"
    "v_add_u32 v24, 49152, v17\n"
    "v_add_u32 v32, 49152, v18\n"
    "v_xor_b32 v33, 32, v32\n"
    "v_xor_b32 v25, 32, v24\n"
    "v_xor_b32 v26, 64, v24\n"
    "v_xor_b32 v27, 96, v24\n"
    "v_xor_b32 v28, 128, v24\n"
    "v_xor_b32 v29, 160, v24\n"
    "v_xor_b32 v30, 192, v24\n"
    "v_xor_b32 v31, 224, v24\n"
    "ds_read_b64_tr_b16 a[4:5], v24 offset:0\n"
    "ds_read_b64_tr_b16 a[6:7], v24 offset:4096\n"
    "ds_read_b64_tr_b16 a[28:29], v25 offset:0\n"
    "ds_read_b64_tr_b16 a[30:31], v25 offset:4096\n"
    "ds_read_b64_tr_b16 a[52:53], v26 offset:0\n"
    "ds_read_b64_tr_b16 a[54:55], v26 offset:4096\n"
    "ds_read_b64_tr_b16 a[76:77], v27 offset:0\n"
    "ds_read_b64_tr_b16 a[78:79], v27 offset:4096\n"
    "ds_read_b64_tr_b16 a[100:101], v28 offset:0\n"
    "ds_read_b64_tr_b16 a[102:103], v28 offset:4096\n"
    "ds_read_b64_tr_b16 a[124:125], v29 offset:0\n"
    "ds_read_b64_tr_b16 a[126:127], v29 offset:4096\n"
    "ds_read_b64_tr_b16 a[148:149], v30 offset:0\n"
    "ds_read_b64_tr_b16 a[150:151], v30 offset:4096\n"
    "ds_read_b64_tr_b16 a[172:173], v31 offset:0\n"
    "ds_read_b64_tr_b16 a[174:175], v31 offset:4096\n"
    "ds_read_b64_tr_b16 a[196:197], v32 offset:0\n"
    "ds_read_b64_tr_b16 a[198:199], v32 offset:4096\n"
    "ds_read_b64_tr_b16 a[220:221], v33 offset:0\n"
    "ds_read_b64_tr_b16 a[222:223], v33 offset:4096\n"
    "s_waitcnt lgkmcnt(0)\n"
    "s_barrier\n"
    "s_mov_b32 s49, 163840\n"
    "s_add_u32 m0, s42, 49152\n"
    "s_nop 0\n"
    "buffer_load_dwordx4 v12, s[4:7], s49 offen offset:0 nt lds\n"
    "buffer_load_dwordx4 v13, s[4:7], s49 offen offset:1024 nt lds\n"
    "buffer_load_dwordx4 v14, s[4:7], s49 offen offset:2048 nt lds\n"
    "buffer_load_dwordx4 v15, s[4:7], s49 offen offset:3072 nt lds\n"
    "s_add_u32 m0, s43, 81920\n"
    "s_nop 0\n"
    "buffer_load_dwordx4 v16, s[4:7], s49 offen nt lds\n"
    "s_mov_b32 s49, 184320\n"
    "s_add_u32 m0, s42, 53248\n"
    "s_nop 0\n"
    "buffer_load_dwordx4 v12, s[4:7], s49 offen offset:0 nt lds\n"
    "buffer_load_dwordx4 v13, s[4:7], s49 offen offset:1024 nt lds\n"
    "buffer_load_dwordx4 v14, s[4:7], s49 offen offset:2048 nt lds\n"
    "buffer_load_dwordx4 v15, s[4:7], s49 offen offset:3072 nt lds\n"
    "s_add_u32 m0, s43, 86016\n"
    "s_nop 0\n"
    "buffer_load_dwordx4 v16, s[4:7], s49 offen nt lds\n"
    "s_waitcnt vmcnt(23)\n"
    "s_barrier\n"
    "v_add_u32 v24, 98304, v17\n"
    "v_add_u32 v32, 98304, v18\n"
    "v_xor_b32 v33, 32, v32\n"
    "v_xor_b32 v25, 32, v24\n"
    "v_xor_b32 v26, 64, v24\n"
    "v_xor_b32 v27, 96, v24\n"
    "v_xor_b32 v28, 128, v24\n"
    "v_xor_b32 v29, 160, v24\n"
    "v_xor_b32 v30, 192, v24\n"
    "v_xor_b32 v31, 224, v24\n"
    "ds_read_b64_tr_b16 a[8:9], v24 offset:0\n"
    "ds_read_b64_tr_b16 a[10:11], v24 offset:4096\n"
    "ds_read_b64_tr_b16 a[32:33], v25 offset:0\n"
    "ds_read_b64_tr_b16 a[34:35], v25 offset:4096\n"
    "ds_read_b64_tr_b16 a[56:57], v26 offset:0\n"
    "ds_read_b64_tr_b16 a[58:59], v26 offset:4096\n"
    "ds_read_b64_tr_b16 a[80:81], v27 offset:0\n"
    "ds_read_b64_tr_b16 a[82:83], v27 offset:4096\n"
    "ds_read_b64_tr_b16 a[104:105], v28 offset:0\n"
    "ds_read_b64_tr_b16 a[106:107], v28 offset:4096\n"
    "ds_read_b64_tr_b16 a[128:129], v29 offset:0\n"
    "ds_read_b64_tr_b16 a[130:131], v29 offset:4096\n"
    "ds_read_b64_tr_b16 a[152:153], v30 offset:0\n"
    "ds_read_b64_tr_b16 a[154:155], v30 offset:4096\n"
    "ds_read_b64_tr_b16 a[176:177], v31 offset:0\n"
    "ds_read_b64_tr_b16 a[178:179], v31 offset:4096\n"
    "ds_read_b64_tr_b16 a[200:201], v32 offset:0\n"
    "ds_read_b64_tr_b16 a[202:203], v32 offset:4096\n"
    "ds_read_b64_tr_b16 a[224:225], v33 offset:0\n"
    "ds_read_b64_tr_b16 a[226:227], v33 offset:4096\n"
    "s_waitcnt lgkmcnt(0)\n"
    "s_barrier\n"
    "s_mov_b32 s49, 204800\n"
    "s_add_u32 m0, s42, 98304\n"
    "s_nop 0\n"
    "buffer_load_dwordx4 v12, s[4:7], s49 offen offset:0 nt lds\n"
    "buffer_load_dwordx4 v13, s[4:7], s49 offen offset:1024 nt lds\n"
    "buffer_load_dwordx4 v14, s[4:7], s49 offen offset:2048 nt lds\n"
    "buffer_load_dwordx4 v15, s[4:7], s49 offen offset:3072 nt lds\n"
    "s_add_u32 m0, s43, 131072\n"
    "s_nop 0\n"
    "buffer_load_dwordx4 v16, s[4:7], s49 offen nt lds\n"
    "s_mov_b32 s49, 225280\n"
    "s_add_u32 m0, s42, 102400\n"
    "s_nop 0\n"
    "buffer_load_dwordx4 v12, s[4:7], s49 offen offset:0 nt lds\n"
    "buffer_load_dwordx4 v13, s[4:7], s49 offen offset:1024 nt lds\n"
    "buffer_load_dwordx4 v14, s[4:7], s49 offen offset:2048 nt lds\n"
    "buffer_load_dwordx4 v15, s[4:7], s49 offen offset:3072 nt lds\n"
    "s_add_u32 m0, s43, 135168\n"
    "s_nop 0\n"
    "buffer_load_dwordx4 v16, s[4:7], s49 offen nt lds\n"
    "s_waitcnt vmcnt(20)\n"
    "s_barrier\n"
    "v_add_u32 v24, 0, v17\n"
    "v_add_u32 v32, 0, v18\n"
    "v_xor_b32 v33, 32, v32\n"
    "v_xor_b32 v25, 32, v24\n"
    "v_xor_b32 v26, 64, v24\n"
    "v_xor_b32 v27, 96, v24\n"
    "v_xor_b32 v28, 128, v24\n"
    "v_xor_b32 v29, 160, v24\n"
    "v_xor_b32 v30, 192, v24\n"
    "v_xor_b32 v31, 224, v24\n"
    "ds_read_b64_tr_b16 a[12:13], v24 offset:0\n"
    "ds_read_b64_tr_b16 a[14:15], v24 offset:4096\n"
    "ds_read_b64_tr_b16 a[36:37], v25 offset:0\n"
    "ds_read_b64_tr_b16 a[38:39], v25 offset:4096\n"
    "ds_read_b64_tr_b16 a[60:61], v26 offset:0\n"
    "ds_read_b64_tr_b16 a[62:63], v26 offset:4096\n"
    "ds_read_b64_tr_b16 a[84:85], v27 offset:0\n"
    "ds_read_b64_tr_b16 a[86:87], v27 offset:4096\n"
    "ds_read_b64_tr_b16 a[108:109], v28 offset:0\n"
    "ds_read_b64_tr_b16 a[110:111], v28 offset:4096\n"
    "ds_read_b64_tr_b16 a[132:133], v29 offset:0\n"
    "ds_read_b64_tr_b16 a[134:135], v29 offset:4096\n"
    "ds_read_b64_tr_b16 a[156:157], v30 offset:0\n"
    "ds_read_b64_tr_b16 a[158:159], v30 offset:4096\n"
    "ds_read_b64_tr_b16 a[180:181], v31 offset:0\n"
    "ds_read_b64_tr_b16 a[182:183], v31 offset:4096\n"
    "ds_read_b64_tr_b16 a[204:205], v32 offset:0\n"
    "ds_read_b64_tr_b16 a[206:207], v32 offset:4096\n"
    "ds_read_b64_tr_b16 a[228:229], v33 offset:0\n"
    "ds_read_b64_tr_b16 a[230:231], v33 offset:4096\n"
    "s_waitcnt lgkmcnt(0)\n"
    "s_barrier\n"
    "s_mov_b32 s49, 0\n"
    "s_add_u32 m0, s41, 0\n"
    "s_nop 0\n"
    "buffer_load_dwordx4 v0, s[8:11], s49 offen offset:0 nt lds\n"
    "buffer_load_dwordx4 v1, s[8:11], s49 offen offset:1024 nt lds\n"
    "buffer_load_dwordx4 v2, s[8:11], s49 offen offset:2048 nt lds\n"
    "s_mov_b32 s49, 1572864\n"
    "s_add_u32 m0, s41, 12288\n"
    "s_nop 0\n"
    "buffer_load_dwordx4 v0, s[8:11], s49 offen offset:0 nt lds\n"
    "buffer_load_dwordx4 v1, s[8:11], s49 offen offset:1024 nt lds\n"
    "buffer_load_dwordx4 v2, s[8:11], s49 offen offset:2048 nt lds\n"
    "s_mov_b32 s49, 3145728\n"
    "s_add_u32 m0, s41, 24576\n"
    "s_nop 0\n"
    "buffer_load_dwordx4 v0, s[8:11], s49 offen offset:0 nt lds\n"
    "buffer_load_dwordx4 v1, s[8:11], s49 offen offset:1024 nt lds\n"
    "buffer_load_dwordx4 v2, s[8:11], s49 offen offset:2048 nt lds\n"
    "s_mov_b32 s49, 4718592\n"
    "s_add_u32 m0, s41, 36864\n"
    "s_nop 0\n"
    "buffer_load_dwordx4 v0, s[8:11], s49 offen offset:0 nt lds\n"
    "buffer_load_dwordx4 v1, s[8:11], s49 offen offset:1024 nt lds\n"
    "buffer_load_dwordx4 v2, s[8:11], s49 offen offset:2048 nt lds\n"
    "s_waitcnt vmcnt(22)\n"
    "s_barrier\n"
    "v_add_u32 v24, 49152, v17\n"
    "v_add_u32 v32, 49152, v18\n"
    "v_xor_b32 v33, 32, v32\n"
    "v_xor_b32 v25, 32, v24\n"
    "v_xor_b32 v26, 64, v24\n"
    "v_xor_b32 v27, 96, v24\n"
    "v_xor_b32 v28, 128, v24\n"
    "v_xor_b32 v29, 160, v24\n"
    "v_xor_b32 v30, 192, v24\n"
    "v_xor_b32 v31, 224, v24\n"
    "ds_read_b64_tr_b16 a[16:17], v24 offset:0\n"
    "ds_read_b64_tr_b16 a[18:19], v24 offset:4096\n"
    "ds_read_b64_tr_b16 a[40:41], v25 offset:0\n"
    "ds_read_b64_tr_b16 a[42:43], v25 offset:4096\n"
    "ds_read_b64_tr_b16 a[64:65], v26 offset:0\n"
    "ds_read_b64_tr_b16 a[66:67], v26 offset:4096\n"
    "ds_read_b64_tr_b16 a[88:89], v27 offset:0\n"
    "ds_read_b64_tr_b16 a[90:91], v27 offset:4096\n"
    "ds_read_b64_tr_b16 a[112:113], v28 offset:0\n"
    "ds_read_b64_tr_b16 a[114:115], v28 offset:4096\n"
    "ds_read_b64_tr_b16 a[136:137], v29 offset:0\n"
    "ds_read_b64_tr_b16 a[138:139], v29 offset:4096\n"
    "ds_read_b64_tr_b16 a[160:161], v30 offset:0\n"
    "ds_read_b64_tr_b16 a[162:163], v30 offset:4096\n"
    "ds_read_b64_tr_b16 a[184:185], v31 offset:0\n"
    "ds_read_b64_tr_b16 a[186:187], v31 offset:4096\n"
    "ds_read_b64_tr_b16 a[208:209], v32 offset:0\n"
    "ds_read_b64_tr_b16 a[210:211], v32 offset:4096\n"
    "ds_read_b64_tr_b16 a[232:233], v33 offset:0\n"
    "ds_read_b64_tr_b16 a[234:235], v33 offset:4096\n"
    "s_waitcnt lgkmcnt(0)\n"
    "s_barrier\n"
    "s_mov_b32 s49, 393216\n"
    "s_add_u32 m0, s41, 49152\n"
    "s_nop 0\n"
    "buffer_load_dwordx4 v0, s[8:11], s49 offen offset:0 nt lds\n"
    "buffer_load_dwordx4 v1, s[8:11], s49 offen offset:1024 nt lds\n"
    "buffer_load_dwordx4 v2, s[8:11], s49 offen offset:2048 nt lds\n"
    "s_mov_b32 s49, 1966080\n"
    "s_add_u32 m0, s41, 61440\n"
    "s_nop 0\n"
    "buffer_load_dwordx4 v0, s[8:11], s49 offen offset:0 nt lds\n"
    "buffer_load_dwordx4 v1, s[8:11], s49 offen offset:1024 nt lds\n"
    "buffer_load_dwordx4 v2, s[8:11], s49 offen offset:2048 nt lds\n"
    "s_mov_b32 s49, 3538944\n"
    "s_add_u32 m0, s41, 73728\n"
    "s_nop 0\n"
    "buffer_load_dwordx4 v0, s[8:11], s49 offen offset:0 nt lds\n"
    "buffer_load_dwordx4 v1, s[8:11], s49 offen offset:1024 nt lds\n"
    "buffer_load_dwordx4 v2, s[8:11], s49 offen offset:2048 nt lds\n"
    "s_mov_b32 s49, 5111808\n"
    "s_add_u32 m0, s41, 86016\n"
    "s_nop 0\n"
    "buffer_load_dwordx4 v0, s[8:11], s49 offen offset:0 nt lds\n"
    "buffer_load_dwordx4 v1, s[8:11], s49 offen offset:1024 nt lds\n"
    "buffer_load_dwordx4 v2, s[8:11], s49 offen offset:2048 nt lds\n"
    "s_waitcnt vmcnt(24)\n"
    "s_barrier\n"
    "v_add_u32 v24, 98304, v17\n"
    "v_add_u32 v32, 98304, v18\n"
    "v_xor_b32 v33, 32, v32\n"
    "v_xor_b32 v25, 32, v24\n"
    "v_xor_b32 v26, 64, v24\n"
    "v_xor_b32 v27, 96, v24\n"
    "v_xor_b32 v28, 128, v24\n"
    "v_xor_b32 v29, 160, v24\n"
    "v_xor_b32 v30, 192, v24\n"
    "v_xor_b32 v31, 224, v24\n"
    "ds_read_b64_tr_b16 a[20:21], v24 offset:0\n"
    "ds_read_b64_tr_b16 a[22:23], v24 offset:4096\n"
    "ds_read_b64_tr_b16 a[44:45], v25 offset:0\n"
    "ds_read_b64_tr_b16 a[46:47], v25 offset:4096\n"
    "ds_read_b64_tr_b16 a[68:69], v26 offset:0\n"
    "ds_read_b64_tr_b16 a[70:71], v26 offset:4096\n"
    "ds_read_b64_tr_b16 a[92:93], v27 offset:0\n"
    "ds_read_b64_tr_b16 a[94:95], v27 offset:4096\n"
    "ds_read_b64_tr_b16 a[116:117], v28 offset:0\n"
    "ds_read_b64_tr_b16 a[118:119], v28 offset:4096\n"
    "ds_read_b64_tr_b16 a[140:141], v29 offset:0\n"
    "ds_read_b64_tr_b16 a[142:143], v29 offset:4096\n"
    "ds_read_b64_tr_b16 a[164:165], v30 offset:0\n"
    "ds_read_b64_tr_b16 a[166:167], v30 offset:4096\n"
    "ds_read_b64_tr_b16 a[188:189], v31 offset:0\n"
    "ds_read_b64_tr_b16 a[190:191], v31 offset:4096\n"
    "ds_read_b64_tr_b16 a[212:213], v32 offset:0\n"
    "ds_read_b64_tr_b16 a[214:215], v32 offset:4096\n"
    "ds_read_b64_tr_b16 a[236:237], v33 offset:0\n"
    "ds_read_b64_tr_b16 a[238:239], v33 offset:4096\n"
    "s_waitcnt lgkmcnt(0)\n"
    "s_barrier\n"
    "s_mov_b32 s49, 786432\n"
    "s_add_u32 m0, s41, 98304\n"
    "s_nop 0\n"
    "buffer_load_dwordx4 v0, s[8:11], s49 offen offset:0 nt lds\n"
    "buffer_load_dwordx4 v1, s[8:11], s49 offen offset:1024 nt lds\n"
    "buffer_load_dwordx4 v2, s[8:11], s49 offen offset:2048 nt lds\n"
    "s_mov_b32 s49, 2359296\n"
    "s_add_u32 m0, s41, 110592\n"
    "s_nop 0\n"
    "buffer_load_dwordx4 v0, s[8:11], s49 offen offset:0 nt lds\n"
    "buffer_load_dwordx4 v1, s[8:11], s49 offen offset:1024 nt lds\n"
    "buffer_load_dwordx4 v2, s[8:11], s49 offen offset:2048 nt lds\n"
    "s_mov_b32 s49, 3932160\n"
    "s_add_u32 m0, s41, 122880\n"
    "s_nop 0\n"
    "buffer_load_dwordx4 v0, s[8:11], s49 offen offset:0 nt lds\n"
    "buffer_load_dwordx4 v1, s[8:11], s49 offen offset:1024 nt lds\n"
    "buffer_load_dwordx4 v2, s[8:11], s49 offen offset:2048 nt lds\n"
    "s_mov_b32 s49, 5505024\n"
    "s_add_u32 m0, s41, 135168\n"
    "s_nop 0\n"
    "buffer_load_dwordx4 v0, s[8:11], s49 offen offset:0 nt lds\n"
    "buffer_load_dwordx4 v1, s[8:11], s49 offen offset:1024 nt lds\n"
    "buffer_load_dwordx4 v2, s[8:11], s49 offen offset:2048 nt lds\n"
    "s_waitcnt vmcnt(24)\n"
    "s_barrier\n"
    "v_add_u32 v8, 0, v3\n"
    "ds_read_b64_tr_b16 a[240:241], v8 offset:0\n"
    "ds_read_b64_tr_b16 a[242:243], v8 offset:768\n"
    "ds_read_b64_tr_b16 v[176:177], v8 offset:128\n"
    "ds_read_b64_tr_b16 v[178:179], v8 offset:896\n"
    "ds_read_b64_tr_b16 v[12:13], v8 offset:256\n"
    "ds_read_b64_tr_b16 v[14:15], v8 offset:1024\n"
    "v_add_u32 v9, 0, v4\n"
    "ds_read_b64_tr_b16 v[112:113], v9 offset:0\n"
    "ds_read_b64_tr_b16 v[114:115], v9 offset:768\n"
    "ds_read_b64_tr_b16 v[192:193], v9 offset:128\n"
    "ds_read_b64_tr_b16 v[194:195], v9 offset:896\n"
    "ds_read_b64_tr_b16 v[28:29], v9 offset:256\n"
    "ds_read_b64_tr_b16 v[30:31], v9 offset:1024\n"
    "v_add_u32 v10, 0, v5\n"
    "ds_read_b64_tr_b16 v[128:129], v10 offset:0\n"
    "ds_read_b64_tr_b16 v[130:131], v10 offset:768\n"
    "ds_read_b64_tr_b16 v[208:209], v10 offset:128\n"
    "ds_read_b64_tr_b16 v[210:211], v10 offset:896\n"
    "ds_read_b64_tr_b16 v[44:45], v10 offset:256\n"
    "ds_read_b64_tr_b16 v[46:47], v10 offset:1024\n"
    "v_add_u32 v11, 0, v6\n"
    "ds_read_b64_tr_b16 v[144:145], v11 offset:0\n"
    "ds_read_b64_tr_b16 v[146:147], v11 offset:768\n"
    "ds_read_b64_tr_b16 v[224:225], v11 offset:128\n"
    "ds_read_b64_tr_b16 v[226:227], v11 offset:896\n"
    "ds_read_b64_tr_b16 v[60:61], v11 offset:256\n"
    "ds_read_b64_tr_b16 v[62:63], v11 offset:1024\n"
    "s_waitcnt lgkmcnt(0)\n"
    "s_barrier\n"
    "s_mov_b32 s49, 1179648\n"
    "s_add_u32 m0, s41, 0\n"
    "s_nop 0\n"
    "buffer_load_dwordx4 v0, s[8:11], s49 offen offset:0 nt lds\n"
    "buffer_load_dwordx4 v1, s[8:11], s49 offen offset:1024 nt lds\n"
    "buffer_load_dwordx4 v2, s[8:11], s49 offen offset:2048 nt lds\n"
    "s_mov_b32 s49, 2752512\n"
    "s_add_u32 m0, s41, 12288\n"
    "s_nop 0\n"
    "buffer_load_dwordx4 v0, s[8:11], s49 offen offset:0 nt lds\n"
    "buffer_load_dwordx4 v1, s[8:11], s49 offen offset:1024 nt lds\n"
    "buffer_load_dwordx4 v2, s[8:11], s49 offen offset:2048 nt lds\n"
    "s_mov_b32 s49, 4325376\n"
    "s_add_u32 m0, s41, 24576\n"
    "s_nop 0\n"
    "buffer_load_dwordx4 v0, s[8:11], s49 offen offset:0 nt lds\n"
    "buffer_load_dwordx4 v1, s[8:11], s49 offen offset:1024 nt lds\n"
    "buffer_load_dwordx4 v2, s[8:11], s49 offen offset:2048 nt lds\n"
    "s_mov_b32 s49, 5898240\n"
    "s_add_u32 m0, s41, 36864\n"
    "s_nop 0\n"
    "buffer_load_dwordx4 v0, s[8:11], s49 offen offset:0 nt lds\n"
    "buffer_load_dwordx4 v1, s[8:11], s49 offen offset:1024 nt lds\n"
    "buffer_load_dwordx4 v2, s[8:11], s49 offen offset:2048 nt lds\n"
    "s_waitcnt vmcnt(24)\n"
    "s_barrier\n"
    "v_add_u32 v8, 49152, v3\n"
    "ds_read_b64_tr_b16 a[244:245], v8 offset:0\n"
    "ds_read_b64_tr_b16 a[246:247], v8 offset:768\n"
    "ds_read_b64_tr_b16 v[180:181], v8 offset:128\n"
    "ds_read_b64_tr_b16 v[182:183], v8 offset:896\n"
    "ds_read_b64_tr_b16 v[16:17], v8 offset:256\n"
    "ds_read_b64_tr_b16 v[18:19], v8 offset:1024\n"
    "v_add_u32 v9, 49152, v4\n"
    "ds_read_b64_tr_b16 v[116:117], v9 offset:0\n"
    "ds_read_b64_tr_b16 v[118:119], v9 offset:768\n"
    "ds_read_b64_tr_b16 v[196:197], v9 offset:128\n"
    "ds_read_b64_tr_b16 v[198:199], v9 offset:896\n"
    "ds_read_b64_tr_b16 v[32:33], v9 offset:256\n"
    "ds_read_b64_tr_b16 v[34:35], v9 offset:1024\n"
    "v_add_u32 v10, 49152, v5\n"
    "ds_read_b64_tr_b16 v[132:133], v10 offset:0\n"
    "ds_read_b64_tr_b16 v[134:135], v10 offset:768\n"
    "ds_read_b64_tr_b16 v[212:213], v10 offset:128\n"
    "ds_read_b64_tr_b16 v[214:215], v10 offset:896\n"
    "ds_read_b64_tr_b16 v[48:49], v10 offset:256\n"
    "ds_read_b64_tr_b16 v[50:51], v10 offset:1024\n"
    "v_add_u32 v11, 49152, v6\n"
    "ds_read_b64_tr_b16 v[148:149], v11 offset:0\n"
    "ds_read_b64_tr_b16 v[150:151], v11 offset:768\n"
    "ds_read_b64_tr_b16 v[228:229], v11 offset:128\n"
    "ds_read_b64_tr_b16 v[230:231], v11 offset:896\n"
    "ds_read_b64_tr_b16 v[64:65], v11 offset:256\n"
    "ds_read_b64_tr_b16 v[66:67], v11 offset:1024\n"
    "s_waitcnt lgkmcnt(0)\n"
    "s_barrier\n"
    "s_lshl_b32 s48, s12, 11\n"
    "s_add_u32 s48, s48, 53248\n"
    "v_lshrrev_b32 v108, 2, v96\n"
    "s_mov_b32 s50, 0\n"
    "s_mov_b32 s51, s14\n"
    "s_add_u32 s43, s18, s13\n"
    "v_mov_b32 v111, s50\n"
    "s_mov_b64 exec, 0x1f0\n"
    "v_mov_b32 v111, s51\n"
    "s_mov_b64 exec, -1\n"
    "buffer_load_dword v109, v108, s[0:3], s24 offen sc1\n"
    "buffer_load_dword v110, v105, s[0:3], 0 offen sc1\n"
    "s_mov_b32 s49, 6291456\n"
    "s_add_u32 m0, s41, 49152\n"
    "s_nop 0\n"
    "buffer_load_dwordx4 v0, s[8:11], s49 offen offset:0 nt lds\n"
    "buffer_load_dwordx4 v1, s[8:11], s49 offen offset:1024 nt lds\n"
    "buffer_load_dwordx4 v2, s[8:11], s49 offen offset:2048 nt lds\n"
    "s_mov_b32 s49, 6684672\n"
    "s_add_u32 m0, s41, 61440\n"
    "s_nop 0\n"
    "buffer_load_dwordx4 v0, s[8:11], s49 offen offset:0 nt lds\n"
    "buffer_load_dwordx4 v1, s[8:11], s49 offen offset:1024 nt lds\n"
    "buffer_load_dwordx4 v2, s[8:11], s49 offen offset:2048 nt lds\n"
    "s_mov_b32 s49, 7077888\n"
    "s_add_u32 m0, s41, 73728\n"
    "s_nop 0\n"
    "buffer_load_dwordx4 v0, s[8:11], s49 offen offset:0 nt lds\n"
    "buffer_load_dwordx4 v1, s[8:11], s49 offen offset:1024 nt lds\n"
    "buffer_load_dwordx4 v2, s[8:11], s49 offen offset:2048 nt lds\n"
    "s_mov_b32 s49, 7471104\n"
    "s_add_u32 m0, s41, 86016\n"
    "s_nop 0\n"
    "buffer_load_dwordx4 v0, s[8:11], s49 offen offset:0 nt lds\n"
    "buffer_load_dwordx4 v1, s[8:11], s49 offen offset:1024 nt lds\n"
    "buffer_load_dwordx4 v2, s[8:11], s49 offen offset:2048 nt lds\n"
    "s_waitcnt vmcnt(26)\n"
    "s_barrier\n"
    "v_add_u32 v8, 98304, v3\n"
    "ds_read_b64_tr_b16 a[248:249], v8 offset:0\n"
    "ds_read_b64_tr_b16 a[250:251], v8 offset:768\n"
    "ds_read_b64_tr_b16 v[184:185], v8 offset:128\n"
    "ds_read_b64_tr_b16 v[186:187], v8 offset:896\n"
    "ds_read_b64_tr_b16 v[20:21], v8 offset:256\n"
    "ds_read_b64_tr_b16 v[22:23], v8 offset:1024\n"
    "v_add_u32 v9, 98304, v4\n"
    "ds_read_b64_tr_b16 v[120:121], v9 offset:0\n"
    "ds_read_b64_tr_b16 v[122:123], v9 offset:768\n"
    "ds_read_b64_tr_b16 v[200:201], v9 offset:128\n"
    "ds_read_b64_tr_b16 v[202:203], v9 offset:896\n"
    "ds_read_b64_tr_b16 v[36:37], v9 offset:256\n"
    "ds_read_b64_tr_b16 v[38:39], v9 offset:1024\n"
    "v_add_u32 v10, 98304, v5\n"
    "ds_read_b64_tr_b16 v[136:137], v10 offset:0\n"
    "ds_read_b64_tr_b16 v[138:139], v10 offset:768\n"
    "ds_read_b64_tr_b16 v[216:217], v10 offset:128\n"
    "ds_read_b64_tr_b16 v[218:219], v10 offset:896\n"
    "ds_read_b64_tr_b16 v[52:53], v10 offset:256\n"
    "ds_read_b64_tr_b16 v[54:55], v10 offset:1024\n"
    "v_add_u32 v11, 98304, v6\n"
    "ds_read_b64_tr_b16 v[152:153], v11 offset:0\n"
    "ds_read_b64_tr_b16 v[154:155], v11 offset:768\n"
    "ds_read_b64_tr_b16 v[232:233], v11 offset:128\n"
    "ds_read_b64_tr_b16 v[234:235], v11 offset:896\n"
    "ds_read_b64_tr_b16 v[68:69], v11 offset:256\n"
    "ds_read_b64_tr_b16 v[70:71], v11 offset:1024\n"
    "s_waitcnt lgkmcnt(0)\n"
    "s_barrier\n"
    "s_waitcnt vmcnt(14)\n"
    "s_barrier\n"
    "v_add_u32 v8, 0, v3\n"
    "ds_read_b64_tr_b16 a[252:253], v8 offset:0\n"
    "ds_read_b64_tr_b16 a[254:255], v8 offset:768\n"
    "ds_read_b64_tr_b16 v[188:189], v8 offset:128\n"
    "ds_read_b64_tr_b16 v[190:191], v8 offset:896\n"
    "ds_read_b64_tr_b16 v[24:25], v8 offset:256\n"
    "ds_read_b64_tr_b16 v[26:27], v8 offset:1024\n"
    "v_add_u32 v9, 0, v4\n"
    "ds_read_b64_tr_b16 v[124:125], v9 offset:0\n"
    "ds_read_b64_tr_b16 v[126:127], v9 offset:768\n"
    "ds_read_b64_tr_b16 v[204:205], v9 offset:128\n"
    "ds_read_b64_tr_b16 v[206:207], v9 offset:896\n"
    "ds_read_b64_tr_b16 v[40:41], v9 offset:256\n"
    "ds_read_b64_tr_b16 v[42:43], v9 offset:1024\n"
    "v_add_u32 v10, 0, v5\n"
    "ds_read_b64_tr_b16 v[140:141], v10 offset:0\n"
    "ds_read_b64_tr_b16 v[142:143], v10 offset:768\n"
    "ds_read_b64_tr_b16 v[220:221], v10 offset:128\n"
    "ds_read_b64_tr_b16 v[222:223], v10 offset:896\n"
    "ds_read_b64_tr_b16 v[56:57], v10 offset:256\n"
    "ds_read_b64_tr_b16 v[58:59], v10 offset:1024\n"
    "v_add_u32 v11, 0, v6\n"
    "ds_read_b64_tr_b16 v[156:157], v11 offset:0\n"
    "ds_read_b64_tr_b16 v[158:159], v11 offset:768\n"
    "ds_read_b64_tr_b16 v[236:237], v11 offset:128\n"
    "ds_read_b64_tr_b16 v[238:239], v11 offset:896\n"
    "ds_read_b64_tr_b16 v[72:73], v11 offset:256\n"
    "ds_read_b64_tr_b16 v[74:75], v11 offset:1024\n"
    "s_waitcnt lgkmcnt(0)\n"
    "s_barrier\n"
    "s_waitcnt vmcnt(12)\n"
    "v_cmp_gt_i32 vcc, 1, v109\n"
    "s_nop 0\n"
    "s_cbranch_vccnz .Lvo_e0_%=\n"
    "v_cmp_gt_i32 vcc, v111, v110\n"
    "s_nop 0\n"
    "s_cbranch_vccnz .Lvo_e0_%=\n"
    ".Lvo_e1_%=:\n"
    "s_sub_u32 m0, s17, 8192\n"
    "s_nop 0\n"
    "buffer_load_dwordx4 v96, s[0:3], s43 offen offset:0 sc1 lds\n"
    "buffer_load_dwordx4 v96, s[0:3], s43 offen offset:1024 sc1 lds\n"
    "buffer_load_dwordx4 v96, s[0:3], s43 offen offset:2048 sc1 lds\n"
    "buffer_load_dwordx4 v96, s[0:3], s43 offen offset:3072 sc1 lds\n"
    "s_sub_u32 m0, s17, 4096\n"
    "s_add_u32 s43, s43, 4096\n"
    "buffer_load_dwordx4 v96, s[0:3], s43 offen offset:0 sc1 lds\n"
    "buffer_load_dwordx4 v96, s[0:3], s43 offen offset:1024 sc1 lds\n"
    "buffer_load_dwordx4 v96, s[0:3], s43 offen offset:2048 sc1 lds\n"
    "buffer_load_dwordx4 v96, s[0:3], s43 offen offset:3072 sc1 lds\n"
    "s_waitcnt vmcnt(8)\n"
    "s_barrier\n"
    "v_add_u32 v8, 49152, v7\n"
    "ds_read_b64_tr_b16 v[160:161], v8 offset:0\n"
    "ds_read_b64_tr_b16 v[162:163], v8 offset:768\n"
    "ds_read_b64_tr_b16 v[240:241], v8 offset:128\n"
    "ds_read_b64_tr_b16 v[242:243], v8 offset:896\n"
    "ds_read_b64_tr_b16 v[76:77], v8 offset:256\n"
    "ds_read_b64_tr_b16 v[78:79], v8 offset:1024\n"
    "v_add_u32 v9, 61440, v7\n"
    "ds_read_b64_tr_b16 v[164:165], v9 offset:0\n"
    "ds_read_b64_tr_b16 v[166:167], v9 offset:768\n"
    "ds_read_b64_tr_b16 v[244:245], v9 offset:128\n"
    "ds_read_b64_tr_b16 v[246:247], v9 offset:896\n"
    "ds_read_b64_tr_b16 v[80:81], v9 offset:256\n"
    "ds_read_b64_tr_b16 v[82:83], v9 offset:1024\n"
    "v_add_u32 v10, 73728, v7\n"
    "ds_read_b64_tr_b16 v[168:169], v10 offset:0\n"
    "ds_read_b64_tr_b16 v[170:171], v10 offset:768\n"
    "ds_read_b64_tr_b16 v[248:249], v10 offset:128\n"
    "ds_read_b64_tr_b16 v[250:251], v10 offset:896\n"
    "ds_read_b64_tr_b16 v[84:85], v10 offset:256\n"
    "ds_read_b64_tr_b16 v[86:87], v10 offset:1024\n"
    "v_add_u32 v11, 86016, v7\n"
    "ds_read_b64_tr_b16 v[172:173], v11 offset:0\n"
    "ds_read_b64_tr_b16 v[174:175], v11 offset:768\n"
    "ds_read_b64_tr_b16 v[252:253], v11 offset:128\n"
    "ds_read_b64_tr_b16 v[254:255], v11 offset:896\n"
    "ds_read_b64_tr_b16 v[88:89], v11 offset:256\n"
    "ds_read_b64_tr_b16 v[90:91], v11 offset:1024\n"
    "s_waitcnt lgkmcnt(0)\n"
    "s_barrier\n"
    "s_mov_b32 m0, s48\n"
    "s_add_u32 s43, s43, s19\n"
    "buffer_load_dwordx4 v96, s[0:3], s43 offen offset:0 sc1 lds\n"
    "buffer_load_dwordx4 v96, s[0:3], s43 offen offset:1024 sc1 lds\n"
    "ds_write_b128 v97, v[12:15] offset:0\n"
    "ds_write_b128 v97, v[16:19] offset:1024\n"
    "ds_write_b128 v97, v[20:23] offset:2048\n"
    "ds_write_b128 v97, v[24:27] offset:3072\n"
    "ds_write_b128 v97, v[28:31] offset:4096\n"
    "ds_write_b128 v97, v[32:35] offset:5120\n"
    "ds_write_b128 v97, v[36:39] offset:6144\n"
    "ds_write_b128 v97, v[40:43] offset:7168\n"
    "ds_write_b128 v97, v[44:47] offset:8192\n"
    "ds_write_b128 v97, v[48:51] offset:9216\n"
    "ds_write_b128 v97, v[52:55] offset:10240\n"
    "ds_write_b128 v97, v[56:59] offset:11264\n"
    "ds_write_b128 v97, v[60:63] offset:12288\n"
    "ds_write_b128 v97, v[64:67] offset:13312\n"
    "ds_write_b128 v97, v[68:71] offset:14336\n"
    "ds_write_b128 v97, v[72:75] offset:15360\n"
    "ds_write_b128 v97, v[76:79] offset:16384\n"
    "ds_write_b128 v97, v[80:83] offset:17408\n"
    "ds_write_b128 v97, v[84:87] offset:18432\n"
    "ds_write_b128 v97, v[88:91] offset:19456\n"
    "s_mov_b32 s34, -4\n"
    "s_memrealtime s[30:31]\n"
    "s_waitcnt vmcnt(0) lgkmcnt(0)\n"
    "s_barrier\n"
    "ds_read_b128 v[24:27], v92 offset:0\n"
    "ds_read_b128 v[28:31], v92 offset:1024\n"
    "ds_read_b128 v[32:35], v93 offset:0\n"
    "ds_read_b128 v[36:39], v93 offset:1024\n"
    "ds_read_b128 v[40:43], v94 offset:0\n"
    "ds_read_b128 v[44:47], v94 offset:1024\n"
    "s_sub_u32 s46, s30, 1340\n"
    "s_mov_b32 s34, -3\n"
    "s_mov_b32 s35, -5\n"
    "s_mov_b32 s45, s23\n"
    "s_mov_b32 s38, 2130706432\n"
    "s_mov_b32 s39, 2130706432\n"
    "s_waitcnt lgkmcnt(0)\n"
    "s_branch .Lvo_vph_%=\n"
    ".Lvo_take_%=:\n"
    "v_mfma_f32_16x16x32_bf16 v[84:87], v[248:251], v[24:27], v[84:87]\n"
    ".Lvo_mk8x2_%=:\n"
    "s_waitcnt vmcnt(3)\n"
    ".Lvo_mk0x3_%=:\n"
    "ds_read_b128 v[72:75], v92 offset:8192\n"
    "v_cvt_pk_bf16_f32 v40, v64, v65\n"
    "v_mfma_f32_16x16x32_bf16 v[84:87], v[252:255], v[32:35], v[84:87]\n"
    "s_waitcnt vmcnt(2)\n"
    "ds_read_b128 v[76:79], v92 offset:9216\n"
    "v_cvt_pk_bf16_f32 v41, v66, v67\n"
    "v_mfma_f32_16x16x32_bf16 v[68:71], v[164:167], v[44:47], v[68:71]\n"
    "v_mov_b64 v[64:65], 0\n"
    "v_mov_b64 v[66:67], 0\n"
    "v_mfma_f32_16x16x32_bf16 v[68:71], v[168:171], v[28:31], v[68:71]\n"
    "s_waitcnt lgkmcnt(2)\n"
    "ds_read_b128 v[24:27], v97 offset:6144\n"
    "v_cvt_pk_bf16_f32 v42, v84, v85\n"
    "v_mfma_f32_16x16x32_bf16 v[68:71], v[172:175], v[36:39], v[68:71]\n"
    "v_cvt_pk_bf16_f32 v43, v86, v87\n"
    "v_mov_b64 v[84:85], 0\n"
    "v_mfma_f32_16x16x32_bf16 v[88:91], v[244:247], v[44:47], v[88:91]\n"
    "ds_write_b128 v92, v[40:43] offset:11264\n"
    "v_mfma_f32_16x16x32_bf16 v[88:91], v[248:251], v[28:31], v[88:91]\n"
    "s_waitcnt lgkmcnt(2)\n"
    "v_max_u32 v108, v72, v74\n"
    "v_max3_u32 v108, v108, v76, v78\n"
    "v_mfma_f32_16x16x32_bf16 v[88:91], v[252:255], v[36:39], v[88:91]\n"
    "v_cmp_le_u32 vcc, s52, v108\n"
    "v_mov_b64 v[86:87], 0\n"
    "v_smfmac_f32_16x16x64_bf16 v[64:67], v[72:75], a[240:247], v98\n"
    "s_cbranch_vccnz .Lvo_retry_%=\n"
    ".Lvo_go_%=:\n"
    "s_barrier\n"
    ".Lvo_mk1x4_%=:\n"
    "ds_read_b128 v[32:35], v93 offset:8192\n"
    "v_cvt_pk_bf16_f32 v44, v68, v69\n"
    "v_smfmac_f32_16x16x64_bf16 v[64:67], v[76:79], a[248:255], v98\n"
    "ds_read_b128 v[36:39], v93 offset:9216\n"
    "v_cvt_pk_bf16_f32 v45, v70, v71\n"
    "v_smfmac_f32_16x16x64_bf16 v[84:87], v[72:75], v[0:7], v98\n"
    "s_waitcnt vmcnt(1)\n"
    "ds_read_b128 v[80:83], v92 offset:10240\n"
    "v_mov_b64 v[68:69], 0\n"
    "v_mov_b64 v[70:71], 0\n"
    "v_smfmac_f32_16x16x64_bf16 v[84:87], v[76:79], v[8:15], v98\n"
    "ds_read_b128 v[28:31], v97 offset:7168\n"
    "v_cvt_pk_bf16_f32 v46, v88, v89\n"
    "v_smfmac_f32_16x16x64_bf16 v[68:71], v[72:75], v[176:183], v98\n"
    "ds_read_b128 v[0:3], v97 offset:12288\n"
    "v_cvt_pk_bf16_f32 v47, v90, v91\n"
    "v_smfmac_f32_16x16x64_bf16 v[68:71], v[76:79], v[184:191], v98\n"
    "s_waitcnt lgkmcnt(4)\n"
    "ds_read_b128 v[88:91], v94 offset:8192\n"
    "ds_write_b128 v92, v[44:47] offset:12288\n"
    "v_smfmac_f32_16x16x64_bf16 v[84:87], v[32:35], v[16:23], v98\n"
    "ds_read_b128 v[40:43], v94 offset:9216\n"
    "v_smfmac_f32_16x16x64_bf16 v[64:67], v[32:35], v[112:119], v98\n"
    "s_waitcnt lgkmcnt(5)\n"
    "ds_read_b128 v[4:7], v97 offset:13312\n"
    "v_max_u32 v108, v80, v82\n"
    "v_smfmac_f32_16x16x64_bf16 v[68:71], v[32:35], v[192:199], v98\n"
    "s_waitcnt lgkmcnt(5)\n"
    "ds_read_b128 v[44:47], v95 offset:8192\n"
    "v_cmp_eq_u32 vcc, -1, v108\n"
    "v_smfmac_f32_16x16x64_bf16 v[84:87], v[36:39], v[24:31], v98\n"
    "ds_read_b128 v[8:11], v97 offset:14336\n"
    "ds_read_b128 v[12:15], v97 offset:15360\n"
    "v_smfmac_f32_16x16x64_bf16 v[64:67], v[36:39], v[120:127], v98\n"
    "ds_read_b128 v[16:19], v97 offset:16384\n"
    "v_smfmac_f32_16x16x64_bf16 v[68:71], v[36:39], v[200:207], v98\n"
    "s_waitcnt lgkmcnt(6)\n"
    "s_cbranch_vccnz .Lvo_r2_%=\n"
    ".Lvo_go2_%=:\n"
    "s_barrier\n"
    "ds_read_b128 v[72:75], v96 offset:10240\n"
    "v_smfmac_f32_16x16x64_bf16 v[84:87], v[88:91], v[48:55], v98\n"
    "s_mov_b32 m0, s21\n"
    "ds_read_b128 v[80:83], v95 offset:9216\n"
    "ds_read_b128 v[76:79], v96 offset:23552\n"
    "v_smfmac_f32_16x16x64_bf16 v[64:67], v[88:91], v[128:135], v98\n"
    "s_waitcnt vmcnt(0)\n"
    "ds_read_b128 v[20:23], v97 offset:17408\n"
    "v_cmp_gt_i32 vcc, v111, v110\n"
    "v_smfmac_f32_16x16x64_bf16 v[68:71], v[88:91], v[208:215], v98\n"
    "s_waitcnt lgkmcnt(7)\n"
    "s_cbranch_vccnz .Lvo_wf_%=\n"
    ".Lvo_wfb_%=:\n"
    "v_smfmac_f32_16x16x64_bf16 v[64:67], v[40:43], v[136:143], v98\n"
    "buffer_load_dwordx4 v96, s[0:3], s44 offen sc1 lds\n"
    "v_smfmac_f32_16x16x64_bf16 v[64:67], v[44:47], v[144:151], v98\n"
    "s_waitcnt lgkmcnt(3)\n"
    "ds_read_b128 v[24:27], v97 offset:18432\n"
    "v_mov_b64 v[32:33], 0\n"
    "v_smfmac_f32_16x16x64_bf16 v[64:67], v[72:75], v[160:167], v98\n"
    "s_waitcnt lgkmcnt(3)\n"
    "ds_read_b128 v[28:31], v97 offset:19456\n"
    "v_mov_b64 v[34:35], 0\n"
    "v_smfmac_f32_16x16x64_bf16 v[64:67], v[80:83], v[152:159], v98\n"
    "s_waitcnt lgkmcnt(3)\n"
    "v_mov_b64 v[36:37], 0\n"
    "v_smfmac_f32_16x16x64_bf16 v[64:67], v[76:79], v[168:175], v98\n"
    "s_sub_u32 m0, s17, 8192\n"
    "v_mov_b64 v[38:39], 0\n"
    "v_smfmac_f32_16x16x64_bf16 v[84:87], v[40:43], v[56:63], v98\n"
    "v_mov_b64 v[48:49], 0\n"
    "v_smfmac_f32_16x16x64_bf16 v[84:87], v[44:47], v[0:7], v98\n"
    "ds_read_b128 v[0:3], v96 offset:11264\n"
    "s_nop 0\n"
    "v_add_f32 v64, v64, v66\n"
    "v_smfmac_f32_16x16x64_bf16 v[84:87], v[80:83], v[8:15], v98\n"
    "s_waitcnt lgkmcnt(3)\n"
    "ds_read_b128 v[4:7], v96 offset:12288\n"
    "v_add_f32 v65, v65, v67\n"
    "v_smfmac_f32_16x16x64_bf16 v[84:87], v[72:75], v[16:23], v98\n"
    "s_waitcnt lgkmcnt(2)\n"
    "ds_read_b128 v[8:11], v96 offset:24576\n"
    "v_cvt_pk_bf16_f32 v64, v64, v65\n"
    "v_smfmac_f32_16x16x64_bf16 v[84:87], v[76:79], v[24:31], v98\n"
    "ds_write_b32 v99, v64 offset:0\n"
    "v_smfmac_f32_16x16x64_bf16 v[68:71], v[40:43], v[216:223], v98\n"
    "ds_read_b128 v[12:15], v96 offset:25600\n"
    "v_mov_b64 v[50:51], 0\n"
    "v_smfmac_f32_16x16x64_bf16 v[68:71], v[44:47], v[224:231], v98\n"
    "ds_read_b128 v[16:19], v96 offset:37888\n"
    "s_nop 0\n"
    "v_add_f32 v84, v84, v86\n"
    "v_smfmac_f32_16x16x64_bf16 v[68:71], v[80:83], v[232:239], v98\n"
    "ds_read_b128 v[20:23], v96 offset:38912\n"
    "v_add_f32 v85, v85, v87\n"
    "v_smfmac_f32_16x16x64_bf16 v[68:71], v[72:75], v[240:247], v98\n"
    "ds_read_b128 v[24:27], v96 offset:51200\n"
    "v_cvt_pk_bf16_f32 v84, v84, v85\n"
    "v_smfmac_f32_16x16x64_bf16 v[68:71], v[76:79], v[248:255], v98\n"
    "ds_write_b32 v100, v84 offset:0\n"
    "s_waitcnt lgkmcnt(7)\n"
    "v_mfma_f32_16x16x32_bf16 v[40:43], v[0:3], a[0:3], 0\n"
    "ds_read_b128 v[28:31], v96 offset:52224\n"
    "v_mov_b64 v[52:53], 0\n"
    "v_mfma_f32_16x16x32_bf16 v[44:47], v[4:7], a[0:3], 0\n"
    "s_nop 1\n"
    "v_add_f32 v68, v68, v70\n"
    "v_add_f32 v69, v69, v71\n"
    "v_cvt_pk_bf16_f32 v68, v68, v69\n"
    "ds_write_b32 v99, v68 offset:256\n"
    "s_waitcnt lgkmcnt(4)\n"
    "v_mfma_f32_16x16x32_bf16 v[40:43], v[8:11], a[4:7], v[40:43]\n"
    "v_mov_b64 v[54:55], 0\n"
    "v_mov_b64 v[56:57], 0\n"
    "v_mfma_f32_16x16x32_bf16 v[44:47], v[12:15], a[4:7], v[44:47]\n"
    "v_mov_b64 v[58:59], 0\n"
    "v_mov_b64 v[60:61], 0\n"
    "v_mfma_f32_16x16x32_bf16 v[40:43], v[16:19], a[8:11], v[40:43]\n"
    "v_mov_b64 v[62:63], 0\n"
    "v_mov_b64 v[64:65], 0\n"
    "v_mfma_f32_16x16x32_bf16 v[44:47], v[20:23], a[8:11], v[44:47]\n"
    "v_mov_b64 v[66:67], 0\n"
    "v_mov_b64 v[68:69], 0\n"
    "s_waitcnt lgkmcnt(0)\n"
    "s_barrier\n"
    ".Lvo_mk2x5_%=:\n"
    "ds_read_b64_tr_b16 v[72:73], v101 offset:0\n"
    "ds_read_b64_tr_b16 v[74:75], v101 offset:64\n"
    "v_mfma_f32_16x16x32_bf16 v[40:43], v[24:27], a[12:15], v[40:43]\n"
    "ds_read_b64_tr_b16 v[76:77], v101 offset:1024\n"
    "ds_read_b64_tr_b16 v[78:79], v101 offset:1088\n"
    "v_mfma_f32_16x16x32_bf16 v[44:47], v[28:31], a[12:15], v[44:47]\n"
    "ds_read_b64_tr_b16 v[80:81], v101 offset:2048\n"
    "ds_read_b64_tr_b16 v[82:83], v101 offset:2112\n"
    "v_mov_b64 v[70:71], 0\n"
    "v_mov_b64 v[84:85], 0\n"
    "v_mov_b64 v[86:87], 0\n"
    "v_mov_b64 v[88:89], 0\n"
    "v_mov_b64 v[90:91], 0\n"
    "s_waitcnt lgkmcnt(4)\n"
    "v_smfmac_f32_16x16x64_bf16 v[32:35], v[72:75], a[192:199], v98\n"
    "buffer_load_dwordx4 v96, s[0:3], s43 offen offset:0 sc1 lds\n"
    "s_waitcnt lgkmcnt(2)\n"
    "v_smfmac_f32_16x16x64_bf16 v[32:35], v[76:79], a[200:207], v98\n"
    "s_waitcnt lgkmcnt(0)\n"
    "v_smfmac_f32_16x16x64_bf16 v[32:35], v[80:83], a[208:215], v98\n"
    "v_smfmac_f32_16x16x64_bf16 v[36:39], v[72:75], a[216:223], v98\n"
    "buffer_load_dwordx4 v96, s[0:3], s43 offen offset:1024 sc1 lds\n"
    "v_smfmac_f32_16x16x64_bf16 v[36:39], v[76:79], a[224:231], v98\n"
    "s_nop 1\n"
    "v_add_f32 v32, v32, v34\n"
    "v_smfmac_f32_16x16x64_bf16 v[36:39], v[80:83], a[232:239], v98\n"
    "v_add_f32 v33, v33, v35\n"
    "v_smfmac_f32_16x16x64_bf16 v[48:51], v[72:75], a[0:7], v98\n"
    "v_cvt_pk_bf16_f32 v108, v32, v33\n"
    "v_smfmac_f32_16x16x64_bf16 v[48:51], v[76:79], a[8:15], v98\n"
    "s_nop 1\n"
    "v_add_f32 v36, v36, v38\n"
    "v_smfmac_f32_16x16x64_bf16 v[48:51], v[80:83], a[16:23], v98\n"
    "v_add_f32 v37, v37, v39\n"
    "v_smfmac_f32_16x16x64_bf16 v[52:55], v[72:75], a[24:31], v98\n"
    "v_cvt_pk_bf16_f32 v109, v36, v37\n"
    "v_smfmac_f32_16x16x64_bf16 v[52:55], v[76:79], a[32:39], v98\n"
    "s_nop 1\n"
    "v_add_f32 v48, v48, v50\n"
    "v_permlane16_swap_b32 v108, v109\n"
    "v_min_u32 v108, -2, v108\n"
    "v_smfmac_f32_16x16x64_bf16 v[52:55], v[80:83], a[40:47], v98\n"
    "buffer_store_dwordx2 v[108:109], v103, s[0:3], s36 offen sc1\n"
    "v_add_f32 v49, v49, v51\n"
    "v_smfmac_f32_16x16x64_bf16 v[56:59], v[72:75], a[48:55], v98\n"
    "v_cvt_pk_bf16_f32 v48, v48, v49\n"
    "v_smfmac_f32_16x16x64_bf16 v[56:59], v[76:79], a[56:63], v98\n"
    "s_nop 1\n"
    "v_add_f32 v52, v52, v54\n"
    "v_smfmac_f32_16x16x64_bf16 v[56:59], v[80:83], a[64:71], v98\n"
    "v_add_f32 v53, v53, v55\n"
    "v_smfmac_f32_16x16x64_bf16 v[60:63], v[72:75], a[72:79], v98\n"
    "v_cvt_pk_bf16_f32 v49, v52, v53\n"
    "v_smfmac_f32_16x16x64_bf16 v[60:63], v[76:79], a[80:87], v98\n"
    "s_waitcnt vmcnt(3)\n"
    "s_barrier\n"
    "v_add_f32 v56, v56, v58\n"
    "v_smfmac_f32_16x16x64_bf16 v[60:63], v[80:83], a[88:95], v98\n"
    "ds_read2_b64 v[32:35], v106 offset0:0 offset1:128\n"
    "v_add_f32 v57, v57, v59\n"
    "v_smfmac_f32_16x16x64_bf16 v[64:67], v[72:75], a[96:103], v98\n"
    "ds_read2_b64 v[36:39], v106 offset0:1 offset1:129\n"
    "v_cvt_pk_bf16_f32 v50, v56, v57\n"
    "v_smfmac_f32_16x16x64_bf16 v[64:67], v[76:79], a[104:111], v98\n"
    "s_nop 1\n"
    "v_add_f32 v60, v60, v62\n"
    "v_permlane32_swap_b32 v48, v50\n"
    "v_smfmac_f32_16x16x64_bf16 v[64:67], v[80:83], a[112:119], v98\n"
    "v_add_f32 v61, v61, v63\n"
    "v_smfmac_f32_16x16x64_bf16 v[68:71], v[72:75], a[120:127], v98\n"
    "v_cvt_pk_bf16_f32 v51, v60, v61\n"
    "v_smfmac_f32_16x16x64_bf16 v[68:71], v[76:79], a[128:135], v98\n"
    "s_nop 0\n"
    "v_permlane32_swap_b32 v49, v51\n"
    "v_smfmac_f32_16x16x64_bf16 v[68:71], v[80:83], a[136:143], v98\n"
    "s_nop 0\n"
    "v_permlane16_swap_b32 v50, v51\n"
    "v_permlane16_swap_b32 v48, v49\n"
    "v_min_u32 v48, -2, v48\n"
    "v_smfmac_f32_16x16x64_bf16 v[84:87], v[72:75], a[144:151], v98\n"
    "buffer_store_dwordx4 v[48:51], v102, s[0:3], s36 offen sc1\n"
    "v_add_f32 v64, v64, v66\n"
    "v_add_f32 v65, v65, v67\n"
    "v_cvt_pk_bf16_f32 v64, v64, v65\n"
    "v_smfmac_f32_16x16x64_bf16 v[84:87], v[76:79], a[152:159], v98\n"
    "v_add_f32 v68, v68, v70\n"
    "v_smfmac_f32_16x16x64_bf16 v[84:87], v[80:83], a[160:167], v98\n"
    "v_add_f32 v69, v69, v71\n"
    "v_smfmac_f32_16x16x64_bf16 v[88:91], v[72:75], a[168:175], v98\n"
    "v_cvt_pk_bf16_f32 v65, v68, v69\n"
    "v_smfmac_f32_16x16x64_bf16 v[88:91], v[76:79], a[176:183], v98\n"
    "s_nop 1\n"
    "v_add_f32 v84, v84, v86\n"
    "v_smfmac_f32_16x16x64_bf16 v[88:91], v[80:83], a[184:191], v98\n"
    ".Lvo_mk3x6_%=:\n"
    ".Lvo_uph_%=:\n"
    "s_waitcnt lgkmcnt(0)\n"
    "v_add_f32 v85, v85, v87\n"
    "v_mfma_f32_16x16x32_bf16 v[56:59], v[32:35], a[16:19], v[40:43]\n"
    "v_cvt_pk_bf16_f32 v66, v84, v85\n"
    "v_mfma_f32_16x16x32_bf16 v[60:63], v[36:39], a[16:19], v[44:47]\n"
    "s_nop 0\n"
    "v_permlane32_swap_b32 v64, v66\n"
    "v_add_f32 v88, v88, v90\n"
    "v_add_f32 v89, v89, v91\n"
    "v_cvt_pk_bf16_f32 v67, v88, v89\n"
    "s_nop 1\n"
    "v_permlane32_swap_b32 v65, v67\n"
    "s_nop 1\n"
    "v_permlane16_swap_b32 v66, v67\n"
    "v_permlane16_swap_b32 v64, v65\n"
    "v_min_u32 v64, -2, v64\n"
    "buffer_store_dwordx4 v[64:67], v102, s[0:3], s37 offen sc1\n"
    ".Lvo_mkum_%=:\n"
    "s_cmp_eq_u32 s34, 63\n"
    "s_cbranch_scc1 .Lvo_end_%=\n"
    "ds_read2_b64 v[40:43], v107 offset0:0 offset1:128\n"
    "ds_read2_b64 v[44:47], v107 offset0:1 offset1:129\n"
    "v_mfma_f32_16x16x32_bf16 v[48:51], v[0:3], a[24:27], 0\n"
    "v_mfma_f32_16x16x32_bf16 v[52:55], v[4:7], a[24:27], 0\n"
    ".Lvo_mkL0_%=:\n"
    "v_mfma_f32_16x16x32_bf16 v[48:51], v[8:11], a[28:31], v[48:51]\n"
    "v_mfma_f32_16x16x32_bf16 v[52:55], v[12:15], a[28:31], v[52:55]\n"
    "v_mfma_f32_16x16x32_bf16 v[48:51], v[16:19], a[32:35], v[48:51]\n"
    "v_mov_b64 v[88:89], -1\n"
    "v_mov_b64 v[90:91], -1\n"
    "v_mfma_f32_16x16x32_bf16 v[52:55], v[20:23], a[32:35], v[52:55]\n"
    "s_waitcnt lgkmcnt(0)\n"
    "v_mfma_f32_16x16x32_bf16 v[56:59], v[40:43], a[20:23], v[56:59]\n"
    "buffer_load_dwordx4 v96, s[0:3], s43 offen offset:2048 sc1 lds\n"
    "v_mfma_f32_16x16x32_bf16 v[60:63], v[44:47], a[20:23], v[60:63]\n"
    "v_mfma_f32_16x16x32_bf16 v[48:51], v[24:27], a[36:39], v[48:51]\n"
    "s_nop 1\n"
    "v_cvt_pk_bf16_f32 v72, v56, v57\n"
    "v_mfma_f32_16x16x32_bf16 v[52:55], v[28:31], a[36:39], v[52:55]\n"
    "v_cvt_pk_bf16_f32 v74, v58, v59\n"
    "v_cvt_pk_bf16_f32 v73, v60, v61\n"
    "v_mfma_f32_16x16x32_bf16 v[48:51], v[32:35], a[40:43], v[48:51]\n"
    "v_cvt_pk_bf16_f32 v75, v62, v63\n"
    "v_mfma_f32_16x16x32_bf16 v[52:55], v[36:39], a[40:43], v[52:55]\n"
    "buffer_load_dwordx4 v96, s[0:3], s43 offen offset:3072 sc1 lds\n"
    "v_mfma_f32_16x16x32_bf16 v[48:51], v[40:43], a[44:47], v[48:51]\n"
    "s_sub_u32 m0, s17, 4096\n"
    "s_add_u32 s43, s43, 4096\n"
    "v_mfma_f32_16x16x32_bf16 v[52:55], v[44:47], a[44:47], v[52:55]\n"
    "v_mfma_f32_16x16x32_bf16 v[56:59], v[0:3], a[48:51], 0\n"
    "s_nop 1\n"
    "v_cvt_pk_bf16_f32 v76, v48, v49\n"
    "v_mfma_f32_16x16x32_bf16 v[60:63], v[4:7], a[48:51], 0\n"
    "v_cvt_pk_bf16_f32 v78, v50, v51\n"
    "v_cvt_pk_bf16_f32 v77, v52, v53\n"
    "v_mfma_f32_16x16x32_bf16 v[56:59], v[8:11], a[52:55], v[56:59]\n"
    "v_cvt_pk_bf16_f32 v79, v54, v55\n"
    "v_mfma_f32_16x16x32_bf16 v[60:63], v[12:15], a[52:55], v[60:63]\n"
    "buffer_load_dwordx4 v96, s[0:3], s43 offen offset:0 sc1 lds\n"
    "v_mfma_f32_16x16x32_bf16 v[56:59], v[16:19], a[56:59], v[56:59]\n"
    "v_mfma_f32_16x16x32_bf16 v[60:63], v[20:23], a[56:59], v[60:63]\n"
    "v_mfma_f32_16x16x32_bf16 v[56:59], v[24:27], a[60:63], v[56:59]\n"
    "v_mfma_f32_16x16x32_bf16 v[60:63], v[28:31], a[60:63], v[60:63]\n"
    "buffer_load_dwordx4 v96, s[0:3], s43 offen offset:1024 sc1 lds\n"
    "v_mfma_f32_16x16x32_bf16 v[56:59], v[32:35], a[64:67], v[56:59]\n"
    "v_mfma_f32_16x16x32_bf16 v[60:63], v[36:39], a[64:67], v[60:63]\n"
    "v_mfma_f32_16x16x32_bf16 v[56:59], v[40:43], a[68:71], v[56:59]\n"
    "v_mfma_f32_16x16x32_bf16 v[60:63], v[44:47], a[68:71], v[60:63]\n"
    "buffer_load_dwordx4 v96, s[0:3], s43 offen offset:2048 sc1 lds\n"
    "v_mfma_f32_16x16x32_bf16 v[48:51], v[0:3], a[72:75], 0\n"
    "s_nop 1\n"
    "v_cvt_pk_bf16_f32 v80, v56, v57\n"
    "v_mfma_f32_16x16x32_bf16 v[52:55], v[4:7], a[72:75], 0\n"
    "v_cvt_pk_bf16_f32 v82, v58, v59\n"
    "v_cvt_pk_bf16_f32 v81, v60, v61\n"
    "v_mfma_f32_16x16x32_bf16 v[48:51], v[8:11], a[76:79], v[48:51]\n"
    "v_cvt_pk_bf16_f32 v83, v62, v63\n"
    "v_mfma_f32_16x16x32_bf16 v[52:55], v[12:15], a[76:79], v[52:55]\n"
    "buffer_store_dwordx4 v[72:75], v96, s[0:3], s40 offen sc1\n"
    "s_add_u32 s40, s40, 0x10000\n"
    "v_mfma_f32_16x16x32_bf16 v[48:51], v[16:19], a[80:83], v[48:51]\n"
    ".Lvo_mkL1_%=:\n"
    "v_mfma_f32_16x16x32_bf16 v[52:55], v[20:23], a[80:83], v[52:55]\n"
    "v_mfma_f32_16x16x32_bf16 v[48:51], v[24:27], a[84:87], v[48:51]\n"
    "v_mfma_f32_16x16x32_bf16 v[52:55], v[28:31], a[84:87], v[52:55]\n"
    "v_mfma_f32_16x16x32_bf16 v[48:51], v[32:35], a[88:91], v[48:51]\n"
    "v_mfma_f32_16x16x32_bf16 v[52:55], v[36:39], a[88:91], v[52:55]\n"
    "v_mfma_f32_16x16x32_bf16 v[48:51], v[40:43], a[92:95], v[48:51]\n"
    "buffer_load_dwordx4 v96, s[0:3], s43 offen offset:3072 sc1 lds\n"
    "v_mfma_f32_16x16x32_bf16 v[52:55], v[44:47], a[92:95], v[52:55]\n"
    "v_mfma_f32_16x16x32_bf16 v[56:59], v[0:3], a[96:99], 0\n"
    "s_nop 1\n"
    "v_cvt_pk_bf16_f32 v72, v48, v49\n"
    "v_mfma_f32_16x16x32_bf16 v[60:63], v[4:7], a[96:99], 0\n"
    "v_cvt_pk_bf16_f32 v74, v50, v51\n"
    "v_cvt_pk_bf16_f32 v73, v52, v53\n"
    "v_mfma_f32_16x16x32_bf16 v[56:59], v[8:11], a[100:103], v[56:59]\n"
    "buffer_store_dwordx4 v[76:79], v96, s[0:3], s40 offen sc1\n"
    "v_cvt_pk_bf16_f32 v75, v54, v55\n"
    "s_add_u32 s40, s40, 0x10000\n"
    "s_mov_b32 m0, s48\n"
    "v_mfma_f32_16x16x32_bf16 v[60:63], v[12:15], a[100:103], v[60:63]\n"
    "s_add_u32 s43, s43, s19\n"
    "v_mfma_f32_16x16x32_bf16 v[56:59], v[16:19], a[104:107], v[56:59]\n"
    "v_mfma_f32_16x16x32_bf16 v[60:63], v[20:23], a[104:107], v[60:63]\n"
    "v_mfma_f32_16x16x32_bf16 v[56:59], v[24:27], a[108:111], v[56:59]\n"
    "v_mfma_f32_16x16x32_bf16 v[60:63], v[28:31], a[108:111], v[60:63]\n"
    "v_mfma_f32_16x16x32_bf16 v[56:59], v[32:35], a[112:115], v[56:59]\n"
    "v_mfma_f32_16x16x32_bf16 v[60:63], v[36:39], a[112:115], v[60:63]\n"
    "buffer_load_dwordx4 v96, s[0:3], s43 offen offset:0 sc1 lds\n"
    "v_mfma_f32_16x16x32_bf16 v[56:59], v[40:43], a[116:119], v[56:59]\n"
    "v_mfma_f32_16x16x32_bf16 v[60:63], v[44:47], a[116:119], v[60:63]\n"
    "v_mfma_f32_16x16x32_bf16 v[48:51], v[0:3], a[120:123], 0\n"
    "s_nop 1\n"
    "v_cvt_pk_bf16_f32 v76, v56, v57\n"
    "v_mfma_f32_16x16x32_bf16 v[52:55], v[4:7], a[120:123], 0\n"
    "buffer_store_dwordx4 v[80:83], v96, s[0:3], s40 offen sc1\n"
    "v_cvt_pk_bf16_f32 v78, v58, v59\n"
    "v_cvt_pk_bf16_f32 v77, v60, v61\n"
    "s_add_u32 s40, s40, 0x10000\n"
    "v_mfma_f32_16x16x32_bf16 v[48:51], v[8:11], a[124:127], v[48:51]\n"
    "v_cvt_pk_bf16_f32 v79, v62, v63\n"
    "v_mfma_f32_16x16x32_bf16 v[52:55], v[12:15], a[124:127], v[52:55]\n"
    "v_mfma_f32_16x16x32_bf16 v[48:51], v[16:19], a[128:131], v[48:51]\n"
    "v_mfma_f32_16x16x32_bf16 v[52:55], v[20:23], a[128:131], v[52:55]\n"
    "v_mfma_f32_16x16x32_bf16 v[48:51], v[24:27], a[132:135], v[48:51]\n"
    "v_mfma_f32_16x16x32_bf16 v[52:55], v[28:31], a[132:135], v[52:55]\n"
    "v_mfma_f32_16x16x32_bf16 v[48:51], v[32:35], a[136:139], v[48:51]\n"
    "buffer_load_dwordx4 v96, s[0:3], s43 offen offset:1024 sc1 lds\n"
    "v_mfma_f32_16x16x32_bf16 v[52:55], v[36:39], a[136:139], v[52:55]\n"
    "v_mfma_f32_16x16x32_bf16 v[48:51], v[40:43], a[140:143], v[48:51]\n"
    ".Lvo_mkL2_%=:\n"
    "v_mfma_f32_16x16x32_bf16 v[52:55], v[44:47], a[140:143], v[52:55]\n"
    "v_mfma_f32_16x16x32_bf16 v[56:59], v[0:3], a[144:147], 0\n"
    "buffer_store_dwordx4 v[72:75], v96, s[0:3], s40 offen sc1\n"
    "s_nop 0\n"
    "v_cvt_pk_bf16_f32 v80, v48, v49\n"
    "s_add_u32 s40, s40, 0x10000\n"
    "v_mfma_f32_16x16x32_bf16 v[60:63], v[4:7], a[144:147], 0\n"
    "v_cvt_pk_bf16_f32 v82, v50, v51\n"
    "v_cvt_pk_bf16_f32 v81, v52, v53\n"
    "v_mfma_f32_16x16x32_bf16 v[56:59], v[8:11], a[148:151], v[56:59]\n"
    "v_cvt_pk_bf16_f32 v83, v54, v55\n"
    "v_mfma_f32_16x16x32_bf16 v[60:63], v[12:15], a[148:151], v[60:63]\n"
    "v_mfma_f32_16x16x32_bf16 v[56:59], v[16:19], a[152:155], v[56:59]\n"
    "v_mfma_f32_16x16x32_bf16 v[60:63], v[20:23], a[152:155], v[60:63]\n"
    "v_mfma_f32_16x16x32_bf16 v[56:59], v[24:27], a[156:159], v[56:59]\n"
    "v_mfma_f32_16x16x32_bf16 v[60:63], v[28:31], a[156:159], v[60:63]\n"
    "buffer_load_dwordx2 v[108:109], off, s[0:3], s45 sc1\n"
    "v_mfma_f32_16x16x32_bf16 v[56:59], v[32:35], a[160:163], v[56:59]\n"
    "v_mfma_f32_16x16x32_bf16 v[60:63], v[36:39], a[160:163], v[60:63]\n"
    "v_mfma_f32_16x16x32_bf16 v[56:59], v[40:43], a[164:167], v[56:59]\n"
    "v_mfma_f32_16x16x32_bf16 v[60:63], v[44:47], a[164:167], v[60:63]\n"
    "buffer_store_dwordx4 v[76:79], v96, s[0:3], s40 offen sc1\n"
    "s_add_u32 s40, s40, 0x10000\n"
    "v_mfma_f32_16x16x32_bf16 v[48:51], v[0:3], a[168:171], 0\n"
    "s_nop 1\n"
    "v_cvt_pk_bf16_f32 v72, v56, v57\n"
    "v_mfma_f32_16x16x32_bf16 v[52:55], v[4:7], a[168:171], 0\n"
    "v_cvt_pk_bf16_f32 v74, v58, v59\n"
    "v_cvt_pk_bf16_f32 v73, v60, v61\n"
    "v_mfma_f32_16x16x32_bf16 v[48:51], v[8:11], a[172:175], v[48:51]\n"
    "v_cvt_pk_bf16_f32 v75, v62, v63\n"
    "v_mfma_f32_16x16x32_bf16 v[52:55], v[12:15], a[172:175], v[52:55]\n"
    "v_mfma_f32_16x16x32_bf16 v[48:51], v[16:19], a[176:179], v[48:51]\n"
    "v_mfma_f32_16x16x32_bf16 v[52:55], v[20:23], a[176:179], v[52:55]\n"
    "v_mfma_f32_16x16x32_bf16 v[48:51], v[24:27], a[180:183], v[48:51]\n"
    "buffer_store_dwordx4 v[88:91], v102, s[0:3], s38 offen sc1\n"
    "v_mfma_f32_16x16x32_bf16 v[52:55], v[28:31], a[180:183], v[52:55]\n"
    "v_mfma_f32_16x16x32_bf16 v[48:51], v[32:35], a[184:187], v[48:51]\n"
    "v_mfma_f32_16x16x32_bf16 v[52:55], v[36:39], a[184:187], v[52:55]\n"
    "v_mfma_f32_16x16x32_bf16 v[48:51], v[40:43], a[188:191], v[48:51]\n"
    "v_mfma_f32_16x16x32_bf16 v[52:55], v[44:47], a[188:191], v[52:55]\n"
    "v_mfma_f32_16x16x32_bf16 v[56:59], v[0:3], a[192:195], 0\n"
    "s_nop 1\n"
    "v_cvt_pk_bf16_f32 v76, v48, v49\n"
    "v_mfma_f32_16x16x32_bf16 v[60:63], v[4:7], a[192:195], 0\n"
    "buffer_store_dwordx4 v[80:83], v96, s[0:3], s40 offen sc1\n"
    "v_cvt_pk_bf16_f32 v78, v50, v51\n"
    "v_cvt_pk_bf16_f32 v77, v52, v53\n"
    "s_add_u32 s40, s40, 0x10000\n"
    "v_mfma_f32_16x16x32_bf16 v[56:59], v[8:11], a[196:199], v[56:59]\n"
    "v_cvt_pk_bf16_f32 v79, v54, v55\n"
    "v_mfma_f32_16x16x32_bf16 v[60:63], v[12:15], a[196:199], v[60:63]\n"
    "v_mfma_f32_16x16x32_bf16 v[56:59], v[16:19], a[200:203], v[56:59]\n"
    ".Lvo_mkL3_%=:\n"
    "v_mfma_f32_16x16x32_bf16 v[60:63], v[20:23], a[200:203], v[60:63]\n"
    "v_mfma_f32_16x16x32_bf16 v[56:59], v[24:27], a[204:207], v[56:59]\n"
    "v_mfma_f32_16x16x32_bf16 v[60:63], v[28:31], a[204:207], v[60:63]\n"
    "v_mfma_f32_16x16x32_bf16 v[56:59], v[32:35], a[208:211], v[56:59]\n"
    "buffer_store_dwordx4 v[88:91], v102, s[0:3], s39 offen sc1\n"
    "v_mfma_f32_16x16x32_bf16 v[60:63], v[36:39], a[208:211], v[60:63]\n"
    "v_mfma_f32_16x16x32_bf16 v[56:59], v[40:43], a[212:215], v[56:59]\n"
    "v_mfma_f32_16x16x32_bf16 v[60:63], v[44:47], a[212:215], v[60:63]\n"
    "s_waitcnt vmcnt(15)\n"
    "s_barrier\n"
    "v_mfma_f32_16x16x32_bf16 v[48:51], v[24:27], a[228:231], 0\n"
    "s_nop 1\n"
    "v_cvt_pk_bf16_f32 v80, v56, v57\n"
    "v_mfma_f32_16x16x32_bf16 v[52:55], v[28:31], a[228:231], 0\n"
    "v_cvt_pk_bf16_f32 v82, v58, v59\n"
    "v_cvt_pk_bf16_f32 v81, v60, v61\n"
    "v_mfma_f32_16x16x32_bf16 v[48:51], v[32:35], a[232:235], v[48:51]\n"
    "ds_read_b128 v[24:27], v92 offset:0\n"
    "v_cvt_pk_bf16_f32 v83, v62, v63\n"
    "v_mfma_f32_16x16x32_bf16 v[52:55], v[36:39], a[232:235], v[52:55]\n"
    "buffer_store_dwordx4 v[72:75], v96, s[0:3], s40 offen sc1\n"
    "ds_read_b128 v[28:31], v92 offset:1024\n"
    "s_add_u32 s40, s40, 0x10000\n"
    "v_mfma_f32_16x16x32_bf16 v[48:51], v[40:43], a[236:239], v[48:51]\n"
    "ds_read_b128 v[32:35], v93 offset:0\n"
    "v_mfma_f32_16x16x32_bf16 v[52:55], v[44:47], a[236:239], v[52:55]\n"
    "ds_read_b128 v[36:39], v93 offset:1024\n"
    "v_mfma_f32_16x16x32_bf16 v[48:51], v[0:3], a[216:219], v[48:51]\n"
    "ds_read_b128 v[40:43], v94 offset:0\n"
    "v_mfma_f32_16x16x32_bf16 v[52:55], v[4:7], a[216:219], v[52:55]\n"
    "ds_read_b128 v[44:47], v94 offset:1024\n"
    "v_mfma_f32_16x16x32_bf16 v[48:51], v[8:11], a[220:223], v[48:51]\n"
    ".Lvo_mkL4_%=:\n"
    "v_mfma_f32_16x16x32_bf16 v[52:55], v[12:15], a[220:223], v[52:55]\n"
    "v_mfma_f32_16x16x32_bf16 v[48:51], v[16:19], a[224:227], v[48:51]\n"
    "buffer_store_dwordx2 v[88:89], v103, s[0:3], s38 offen sc1\n"
    "v_mfma_f32_16x16x32_bf16 v[52:55], v[20:23], a[224:227], v[52:55]\n"
    ".Lvo_mk4x7_%=:\n"
    ".Lvo_vph_%=:\n"
    "s_waitcnt lgkmcnt(4)\n"
    "v_mfma_f32_16x16x32_bf16 v[64:67], a[240:243], v[24:27], 0\n"
    "s_nop 1\n"
    "v_cvt_pk_bf16_f32 v72, v48, v49\n"
    "v_mfma_f32_16x16x32_bf16 v[84:87], v[176:179], v[24:27], 0\n"
    "s_add_u32 s34, s34, 1\n"
    "v_cvt_pk_bf16_f32 v74, v50, v51\n"
    "v_cvt_pk_bf16_f32 v73, v52, v53\n"
    "v_mfma_f32_16x16x32_bf16 v[68:71], a[240:243], v[28:31], 0\n"
    "s_add_u32 s36, s34, 0\n"
    "ds_read_b128 v[24:27], v95 offset:0\n"
    "v_cvt_pk_bf16_f32 v75, v54, v55\n"
    "v_mfma_f32_16x16x32_bf16 v[88:91], v[176:179], v[28:31], 0\n"
    "s_waitcnt lgkmcnt(3)\n"
    "ds_read_b128 v[28:31], v95 offset:1024\n"
    "s_and_b32 s36, s36, 3\n"
    "v_mfma_f32_16x16x32_bf16 v[64:67], v[112:115], v[32:35], v[64:67]\n"
    "s_lshl_b32 s36, s36, 22\n"
    "s_add_u32 s41, s15, s36\n"
    "s_add_u32 s42, s16, s36\n"
    "v_mfma_f32_16x16x32_bf16 v[84:87], v[192:195], v[32:35], v[84:87]\n"
    "s_add_u32 s37, s36, 0x10000\n"
    "s_add_u32 s50, s34, 3\n"
    "ds_read_b128 v[0:3], v97 offset:0\n"
    "v_mfma_f32_16x16x32_bf16 v[68:71], v[112:115], v[36:39], v[68:71]\n"
    "buffer_store_dwordx4 v[76:79], v96, s[0:3], s40 offen sc1\n"
    "ds_read_b128 v[32:35], v92 offset:2048\n"
    "s_add_u32 s40, s40, 0x10000\n"
    "s_cmp_gt_i32 s50, 64\n"
    "s_cselect_b32 s50, 0, s50\n"
    "v_mfma_f32_16x16x32_bf16 v[88:91], v[192:195], v[36:39], v[88:91]\n"
    "s_waitcnt lgkmcnt(4)\n"
    "ds_read_b128 v[36:39], v92 offset:3072\n"
    "s_add_u32 s51, s34, 4\n"
    "v_mfma_f32_16x16x32_bf16 v[64:67], v[128:131], v[40:43], v[64:67]\n"
    "s_cmp_gt_i32 s51, 64\n"
    "s_cselect_b32 s51, 0, s51\n"
    "s_mul_i32 s51, s51, s14\n"
    "v_mfma_f32_16x16x32_bf16 v[84:87], v[208:211], v[40:43], v[84:87]\n"
    "ds_read_b128 v[4:7], v97 offset:1024\n"
    "v_mov_b32 v111, s50\n"
    "v_mfma_f32_16x16x32_bf16 v[68:71], v[128:131], v[44:47], v[68:71]\n"
    "ds_read_b128 v[40:43], v93 offset:2048\n"
    "v_mfma_f32_16x16x32_bf16 v[88:91], v[208:211], v[44:47], v[88:91]\n"
    "s_waitcnt lgkmcnt(5)\n"
    "ds_read_b128 v[44:47], v93 offset:3072\n"
    "v_mfma_f32_16x16x32_bf16 v[64:67], v[144:147], v[24:27], v[64:67]\n"
    "s_mov_b64 exec, 0x1f0\n"
    "v_mov_b32 v111, s51\n"
    "s_mov_b64 exec, -1\n"
    "v_mfma_f32_16x16x32_bf16 v[84:87], v[224:227], v[24:27], v[84:87]\n"
    "buffer_store_dwordx4 v[80:83], v104, s[0:3], s38 offen sc1\n"
    "ds_read_b128 v[8:11], v97 offset:2048\n"
    "v_mfma_f32_16x16x32_bf16 v[68:71], v[144:147], v[28:31], v[68:71]\n"
    "ds_read_b128 v[24:27], v94 offset:2048\n"
    "v_mfma_f32_16x16x32_bf16 v[88:91], v[224:227], v[28:31], v[88:91]\n"
    "s_waitcnt lgkmcnt(5)\n"
    "ds_read_b128 v[28:31], v94 offset:3072\n"
    "v_mfma_f32_16x16x32_bf16 v[64:67], a[244:247], v[32:35], v[64:67]\n"
    "ds_read_b128 v[12:15], v97 offset:3072\n"
    "v_mfma_f32_16x16x32_bf16 v[84:87], v[180:183], v[32:35], v[84:87]\n"
    "ds_read_b128 v[16:19], v97 offset:4096\n"
    "v_mfma_f32_16x16x32_bf16 v[68:71], a[244:247], v[36:39], v[68:71]\n"
    "ds_read_b128 v[32:35], v95 offset:2048\n"
    "v_mfma_f32_16x16x32_bf16 v[88:91], v[180:183], v[36:39], v[88:91]\n"
    "s_waitcnt lgkmcnt(6)\n"
    "ds_read_b128 v[36:39], v95 offset:3072\n"
    "v_mfma_f32_16x16x32_bf16 v[64:67], v[116:119], v[40:43], v[64:67]\n"
    "buffer_store_dwordx4 v[72:75], v104, s[0:3], s39 offen sc1\n"
    "s_waitcnt vmcnt(11)\n"
    "ds_read_b128 v[20:23], v97 offset:5120\n"
    "v_mfma_f32_16x16x32_bf16 v[84:87], v[196:199], v[40:43], v[84:87]\n"
    "s_barrier\n"
    "ds_read_b128 v[48:51], v97 offset:8192\n"
    "v_mfma_f32_16x16x32_bf16 v[68:71], v[116:119], v[44:47], v[68:71]\n"
    "ds_read_b128 v[40:43], v92 offset:4096\n"
    "v_mfma_f32_16x16x32_bf16 v[88:91], v[196:199], v[44:47], v[88:91]\n"
    "s_waitcnt lgkmcnt(7)\n"
    "ds_read_b128 v[44:47], v92 offset:5120\n"
    "s_waitcnt vmcnt(9)\n"
    "v_mfma_f32_16x16x32_bf16 v[64:67], v[132:135], v[24:27], v[64:67]\n"
    "s_memrealtime s[54:55]\n"
    "v_readfirstlane_b32 s32, v108\n"
    "v_readfirstlane_b32 s33, v109\n"
    ".Lvo_mkL5_%=:\n"
    "v_mfma_f32_16x16x32_bf16 v[84:87], v[212:215], v[24:27], v[84:87]\n"
    "ds_read_b128 v[52:55], v97 offset:9216\n"
    "v_mfma_f32_16x16x32_bf16 v[68:71], v[132:135], v[28:31], v[68:71]\n"
    "s_cmp_eq_u32 s33, s35\n"
    "s_cselect_b32 s46, s32, s46\n"
    "ds_read_b128 v[24:27], v93 offset:4096\n"
    "v_mfma_f32_16x16x32_bf16 v[88:91], v[212:215], v[28:31], v[88:91]\n"
    "s_waitcnt lgkmcnt(6)\n"
    "ds_read_b128 v[28:31], v93 offset:5120\n"
    "s_add_u32 s46, s46, 268\n"
    "v_mfma_f32_16x16x32_bf16 v[64:67], v[148:151], v[32:35], v[64:67]\n"
    "s_sub_u32 s47, s46, 59\n"
    "s_mov_b32 m0, s17\n"
    "ds_read_b128 v[56:59], v97 offset:10240\n"
    "v_mfma_f32_16x16x32_bf16 v[84:87], v[228:231], v[32:35], v[84:87]\n"
    "ds_read_b128 v[60:63], v97 offset:11264\n"
    ".Lvo_mkL6_%=:\n"
    "v_mfma_f32_16x16x32_bf16 v[68:71], v[148:151], v[36:39], v[68:71]\n"
    "ds_read_b128 v[32:35], v94 offset:4096\n"
    "s_sub_u32 s56, s47, s54\n"
    "v_mfma_f32_16x16x32_bf16 v[88:91], v[228:231], v[36:39], v[88:91]\n"
    "s_waitcnt lgkmcnt(6)\n"
    "ds_read_b128 v[36:39], v94 offset:5120\n"
    ".Lvo_mk6x8_%=:\n"
    "s_cmp_gt_i32 s56, 4\n"
    "s_cbranch_scc1 .Lvo_early_%=\n"
    "buffer_load_dwordx4 v96, s[0:3], s41 offen sc1 lds\n"
    "buffer_load_dwordx4 v96, s[0:3], s41 offen offset:1024 sc1 lds\n"
    ".Lvo_asked_%=:\n"
    ".Lvo_mk7x10_%=:\n"
    "v_mfma_f32_16x16x32_bf16 v[64:67], a[248:251], v[40:43], v[64:67]\n"
    "s_add_u32 s49, s34, 3\n"
    "s_lshl_b32 s43, s49, 19\n"
    "s_lshl_b32 s33, s49, 11\n"
    "v_mfma_f32_16x16x32_bf16 v[84:87], v[184:187], v[40:43], v[84:87]\n"
    "s_xor_b32 s33, s33, s13\n"
    "s_and_b32 s33, s33, 0x2000\n"
    "s_add_u32 s43, s43, s33\n"
    ".Lvo_mkM0_%=:\n"
    "v_mfma_f32_16x16x32_bf16 v[68:71], a[248:251], v[44:47], v[68:71]\n"
    "ds_read_b128 v[40:43], v95 offset:4096\n"
    "s_add_u32 s43, s43, s18\n"
    "v_mfma_f32_16x16x32_bf16 v[88:91], v[184:187], v[44:47], v[88:91]\n"
    "s_waitcnt lgkmcnt(5)\n"
    "ds_read_b128 v[44:47], v95 offset:5120\n"
    "s_add_u32 s49, s34, 2\n"
    "v_mfma_f32_16x16x32_bf16 v[64:67], v[120:123], v[24:27], v[64:67]\n"
    "s_lshl_b32 s44, s49, 20\n"
    "s_lshl_b32 s33, s49, 12\n"
    "s_xor_b32 s33, s33, s13\n"
    "v_mfma_f32_16x16x32_bf16 v[84:87], v[200:203], v[24:27], v[84:87]\n"
    "s_and_b32 s33, s33, 0x2000\n"
    "s_add_u32 s44, s44, s33\n"
    "s_add_u32 s44, s44, s20\n"
    "v_mfma_f32_16x16x32_bf16 v[68:71], v[120:123], v[28:31], v[68:71]\n"
    "ds_read_b128 v[24:27], v92 offset:6144\n"
    "s_add_u32 s38, s34, 2\n"
    "v_mfma_f32_16x16x32_bf16 v[88:91], v[200:203], v[28:31], v[88:91]\n"
    "s_waitcnt lgkmcnt(3)\n"
    "ds_read_b128 v[28:31], v92 offset:7168\n"
    "s_and_b32 s38, s38, 3\n"
    "v_mfma_f32_16x16x32_bf16 v[64:67], v[136:139], v[32:35], v[64:67]\n"
    "buffer_load_dwordx4 v96, s[0:3], s42 offen offset:2048 sc1 lds\n"
    "v_mfma_f32_16x16x32_bf16 v[84:87], v[216:219], v[32:35], v[84:87]\n"
    "s_lshl_b32 s38, s38, 22\n"
    "s_add_u32 s39, s38, 0x10000\n"
    "s_add_u32 s40, s22, s38\n"
    "v_mfma_f32_16x16x32_bf16 v[68:71], v[136:139], v[36:39], v[68:71]\n"
    "ds_read_b128 v[32:35], v93 offset:6144\n"
    "s_and_b32 s45, s34, 63\n"
    ".Lvo_mkM1_%=:\n"
    "v_mfma_f32_16x16x32_bf16 v[88:91], v[216:219], v[36:39], v[88:91]\n"
    "s_waitcnt lgkmcnt(3)\n"
    "ds_read_b128 v[36:39], v93 offset:7168\n"
    "s_lshl_b32 s45, s45, 7\n"
    "v_mfma_f32_16x16x32_bf16 v[64:67], v[152:155], v[40:43], v[64:67]\n"
    "s_add_u32 s45, s23, s45\n"
    "s_add_u32 s35, s34, 1\n"
    "v_mfma_f32_16x16x32_bf16 v[84:87], v[232:235], v[40:43], v[84:87]\n"
    "s_cmp_lt_i32 s34, 0\n"
    "s_cselect_b32 s35, -5, s35\n"
    "s_cselect_b32 s52, 0, -1\n"
    "v_mfma_f32_16x16x32_bf16 v[68:71], v[152:155], v[44:47], v[68:71]\n"
    "ds_read_b128 v[40:43], v94 offset:6144\n"
    "v_mfma_f32_16x16x32_bf16 v[88:91], v[232:235], v[44:47], v[88:91]\n"
    "s_waitcnt lgkmcnt(3)\n"
    "ds_read_b128 v[44:47], v94 offset:7168\n"
    "v_mfma_f32_16x16x32_bf16 v[64:67], a[252:255], v[24:27], v[64:67]\n"
    "buffer_load_dword v110, v105, s[0:3], 0 offen sc1\n"
    "v_mfma_f32_16x16x32_bf16 v[84:87], v[188:191], v[24:27], v[84:87]\n"
    "v_mfma_f32_16x16x32_bf16 v[68:71], a[252:255], v[28:31], v[68:71]\n"
    "ds_read_b128 v[24:27], v95 offset:6144\n"
    "v_mfma_f32_16x16x32_bf16 v[88:91], v[188:191], v[28:31], v[88:91]\n"
    "s_waitcnt lgkmcnt(3)\n"
    "ds_read_b128 v[28:31], v95 offset:7168\n"
    "v_mfma_f32_16x16x32_bf16 v[64:67], v[124:127], v[32:35], v[64:67]\n"
    ".Lvo_mkM2_%=:\n"
    "v_mfma_f32_16x16x32_bf16 v[84:87], v[204:207], v[32:35], v[84:87]\n"
    "v_mfma_f32_16x16x32_bf16 v[68:71], v[124:127], v[36:39], v[68:71]\n"
    "ds_read_b128 v[32:35], v96 offset:53248\n"
    "v_mfma_f32_16x16x32_bf16 v[88:91], v[204:207], v[36:39], v[88:91]\n"
    "s_waitcnt lgkmcnt(3)\n"
    "ds_read_b128 v[36:39], v96 offset:54272\n"
    "v_mfma_f32_16x16x32_bf16 v[64:67], v[140:143], v[40:43], v[64:67]\n"
    "v_mfma_f32_16x16x32_bf16 v[84:87], v[220:223], v[40:43], v[84:87]\n"
    "v_mfma_f32_16x16x32_bf16 v[68:71], v[140:143], v[44:47], v[68:71]\n"
    "ds_read_b128 v[40:43], v96 offset:55296\n"
    ".Lvo_mkM3_%=:\n"
    "v_mfma_f32_16x16x32_bf16 v[88:91], v[220:223], v[44:47], v[88:91]\n"
    "s_waitcnt lgkmcnt(3)\n"
    "ds_read_b128 v[44:47], v96 offset:56320\n"
    "v_mfma_f32_16x16x32_bf16 v[64:67], v[156:159], v[24:27], v[64:67]\n"
    "v_mfma_f32_16x16x32_bf16 v[84:87], v[236:239], v[24:27], v[84:87]\n"
    "v_mfma_f32_16x16x32_bf16 v[68:71], v[156:159], v[28:31], v[68:71]\n"
    "ds_read_b128 v[24:27], v96 offset:57344\n"
    "v_mfma_f32_16x16x32_bf16 v[88:91], v[236:239], v[28:31], v[88:91]\n"
    "s_waitcnt lgkmcnt(3)\n"
    "ds_read_b128 v[28:31], v96 offset:58368\n"
    "v_mfma_f32_16x16x32_bf16 v[64:67], v[160:163], v[32:35], v[64:67]\n"
    "v_mfma_f32_16x16x32_bf16 v[84:87], v[240:243], v[32:35], v[84:87]\n"
    "ds_read_b128 v[32:35], v96 offset:59392\n"
    "v_mfma_f32_16x16x32_bf16 v[68:71], v[160:163], v[36:39], v[68:71]\n"
    "v_mfma_f32_16x16x32_bf16 v[88:91], v[240:243], v[36:39], v[88:91]\n"
    "s_waitcnt lgkmcnt(3)\n"
    "ds_read_b128 v[36:39], v96 offset:60416\n"
    "v_mfma_f32_16x16x32_bf16 v[64:67], v[164:167], v[40:43], v[64:67]\n"
    "s_waitcnt lgkmcnt(2)\n"
    "v_mfma_f32_16x16x32_bf16 v[64:67], v[168:171], v[24:27], v[64:67]\n"
    "s_waitcnt lgkmcnt(1)\n"
    "v_mfma_f32_16x16x32_bf16 v[64:67], v[172:175], v[32:35], v[64:67]\n"
    ".Lvo_mkM4_%=:\n"
    "v_mfma_f32_16x16x32_bf16 v[84:87], v[244:247], v[40:43], v[84:87]\n"
    "s_branch .Lvo_take_%=\n"
    ".Lvo_early_%=:\n"
    ".Lvo_rt10_%=:\n"
    "s_memrealtime s[30:31]\n"
    "s_waitcnt lgkmcnt(0)\n"
    "s_sub_u32 s32, s47, s30\n"
    "s_cmp_lt_u32 s32, 600\n"
    "s_cbranch_scc1 .Lvo_rt10_%=\n"
    "buffer_load_dwordx4 v96, s[0:3], s41 offen sc1 lds\n"
    "buffer_load_dwordx4 v96, s[0:3], s41 offen offset:1024 sc1 lds\n"
    "s_branch .Lvo_asked_%=\n"
    ".Lvo_retry_%=:\n"
    "s_nop 7\n"
    "s_nop 7\n"
    "s_cmp_lt_i32 s34, 0\n"
    "s_cbranch_scc0 .Lvo_again_%=\n"
    "v_cvt_pk_bf16_f32 v44, v68, v69\n"
    "v_cvt_pk_bf16_f32 v45, v70, v71\n"
    "v_cvt_pk_bf16_f32 v46, v88, v89\n"
    "v_cvt_pk_bf16_f32 v47, v90, v91\n"
    "ds_write_b128 v92, v[44:47] offset:12288\n"
    "s_waitcnt vmcnt(0) lgkmcnt(0)\n"
    "s_barrier\n"
    "v_cmp_gt_i32 vcc, v111, v110\n"
    "s_nop 0\n"
    "s_cbranch_vccz .Lvo_e2_%=\n"
    ".Lvo_po12_%=:\n"
    "buffer_load_dword v110, v105, s[0:3], 0 offen sc1\n"
    "s_waitcnt vmcnt(0)\n"
    "v_cmp_gt_i32 vcc, v111, v110\n"
    "s_nop 0\n"
    "s_cbranch_vccz .Lvo_pd13_%=\n"
    "s_add_u32 s28, s28, 1\n"
    "s_cmp_lt_u32 s28, 0x40000\n"
    "s_cbranch_scc1 .Lvo_sp14_%=\n"
    "v_mov_b32 v110, 32\n"
    "s_mov_b32 s32, 33728\n"
    "buffer_store_dword v110, off, s[0:3], s32 sc1\n"
    "s_waitcnt vmcnt(0)\n"
    "s_branch .Lvo_pd13_%=\n"
    ".Lvo_sp14_%=:\n"
    "s_sleep 4\n"
    "s_branch .Lvo_po12_%=\n"
    ".Lvo_pd13_%=:\n"
    ".Lvo_e2_%=:\n"
    "s_mov_b32 m0, s21\n"
    "s_nop 0\n"
    "buffer_load_dwordx4 v96, s[0:3], s44 offen sc1 lds\n"
    "s_sub_u32 m0, s17, 8192\n"
    "s_nop 0\n"
    "buffer_load_dwordx4 v96, s[0:3], s43 offen offset:0 sc1 lds\n"
    "buffer_load_dwordx4 v96, s[0:3], s43 offen offset:1024 sc1 lds\n"
    "s_add_u32 s49, s34, 3\n"
    "v_lshlrev_b32 v108, 4, v96\n"
    "v_mov_b32 v109, s49\n"
    "s_mov_b64 exec, 15\n"
    "buffer_store_dword v109, v108, s[0:3], s25 offen sc1\n"
    "s_mov_b64 exec, -1\n"
    "ds_read_b128 v[0:3], v96 offset:11264\n"
    "ds_read_b128 v[4:7], v96 offset:12288\n"
    "ds_read_b128 v[8:11], v96 offset:24576\n"
    "ds_read_b128 v[12:15], v96 offset:25600\n"
    "ds_read_b128 v[16:19], v96 offset:37888\n"
    "ds_read_b128 v[20:23], v96 offset:38912\n"
    "ds_read_b128 v[24:27], v96 offset:51200\n"
    "ds_read_b128 v[28:31], v96 offset:52224\n"
    "s_waitcnt vmcnt(3) lgkmcnt(0)\n"
    "s_barrier\n"
    "ds_read2_b64 v[32:35], v106 offset0:0 offset1:128\n"
    "ds_read2_b64 v[36:39], v106 offset0:1 offset1:129\n"
    "v_mfma_f32_16x16x32_bf16 v[40:43], v[0:3], a[0:3], 0\n"
    "v_mfma_f32_16x16x32_bf16 v[44:47], v[4:7], a[0:3], 0\n"
    "v_mfma_f32_16x16x32_bf16 v[40:43], v[8:11], a[4:7], v[40:43]\n"
    "v_mfma_f32_16x16x32_bf16 v[44:47], v[12:15], a[4:7], v[44:47]\n"
    "v_mfma_f32_16x16x32_bf16 v[40:43], v[16:19], a[8:11], v[40:43]\n"
    "v_mfma_f32_16x16x32_bf16 v[44:47], v[20:23], a[8:11], v[44:47]\n"
    "v_mfma_f32_16x16x32_bf16 v[40:43], v[24:27], a[12:15], v[40:43]\n"
    "v_mfma_f32_16x16x32_bf16 v[44:47], v[28:31], a[12:15], v[44:47]\n"
    "s_waitcnt lgkmcnt(0)\n"
    "s_mov_b32 s37, 2130706432\n"
    "s_mov_b32 s36, 2130706432\n"
    "s_nop 7\n"
    "s_branch .Lvo_uph_%=\n"
    ".Lvo_e0_%=:\n"
    ".Lvo_po15_%=:\n"
    "buffer_load_dword v109, v108, s[0:3], s24 offen sc1\n"
    "s_waitcnt vmcnt(0)\n"
    "v_cmp_gt_i32 vcc, 1, v109\n"
    "s_nop 0\n"
    "s_cbranch_vccz .Lvo_pd16_%=\n"
    "s_add_u32 s28, s28, 1\n"
    "s_cmp_lt_u32 s28, 0x40000\n"
    "s_cbranch_scc1 .Lvo_sp17_%=\n"
    "v_mov_b32 v109, 30\n"
    "s_mov_b32 s32, 33728\n"
    "buffer_store_dword v109, off, s[0:3], s32 sc1\n"
    "s_waitcnt vmcnt(0)\n"
    "s_branch .Lvo_pd16_%=\n"
    ".Lvo_sp17_%=:\n"
    "s_sleep 4\n"
    "s_branch .Lvo_po15_%=\n"
    ".Lvo_pd16_%=:\n"
    ".Lvo_po18_%=:\n"
    "buffer_load_dword v110, v105, s[0:3], 0 offen sc1\n"
    "s_waitcnt vmcnt(0)\n"
    "v_cmp_gt_i32 vcc, v111, v110\n"
    "s_nop 0\n"
    "s_cbranch_vccz .Lvo_pd19_%=\n"
    "s_add_u32 s28, s28, 1\n"
    "s_cmp_lt_u32 s28, 0x40000\n"
    "s_cbranch_scc1 .Lvo_sp20_%=\n"
    "v_mov_b32 v110, 31\n"
    "s_mov_b32 s32, 33728\n"
    "buffer_store_dword v110, off, s[0:3], s32 sc1\n"
    "s_waitcnt vmcnt(0)\n"
    "s_branch .Lvo_pd19_%=\n"
    ".Lvo_sp20_%=:\n"
    "s_sleep 4\n"
    "s_branch .Lvo_po18_%=\n"
    ".Lvo_pd19_%=:\n"
    "s_branch .Lvo_e1_%=\n"
    ".Lvo_again_%=:\n"
    "s_add_u32 s28, s28, 1\n"
    "s_cmp_lt_u32 s28, 0x40000\n"
    "s_cbranch_scc1 .Lvo_sp21_%=\n"
    "v_mov_b32 v108, 2\n"
    "s_mov_b32 s32, 33728\n"
    "buffer_store_dword v108, off, s[0:3], s32 sc1\n"
    "s_waitcnt vmcnt(0)\n"
    "s_branch .Lvo_go_%=\n"
    ".Lvo_sp21_%=:\n"
    "buffer_load_dwordx4 v96, s[0:3], s41 offen sc1 lds\n"
    "buffer_load_dwordx4 v96, s[0:3], s41 offen offset:1024 sc1 lds\n"
    "buffer_load_dwordx4 v96, s[0:3], s42 offen offset:2048 sc1 lds\n"
    "s_waitcnt vmcnt(0)\n"
    "ds_read_b128 v[72:75], v92 offset:8192\n"
    "ds_read_b128 v[76:79], v92 offset:9216\n"
    "s_waitcnt lgkmcnt(0)\n"
    "v_mov_b64 v[64:65], 0\n"
    "v_mov_b64 v[66:67], 0\n"
    "v_max_u32 v108, v72, v74\n"
    "v_max3_u32 v108, v108, v76, v78\n"
    "v_cmp_le_u32 vcc, s52, v108\n"
    "s_nop 0\n"
    "s_cbranch_vccnz .Lvo_again_%=\n"
    "v_smfmac_f32_16x16x64_bf16 v[64:67], v[72:75], a[240:247], v98\n"
    "s_branch .Lvo_go_%=\n"
    ".Lvo_r2_%=:\n"
    "s_add_u32 s28, s28, 1\n"
    "s_cmp_lt_u32 s28, 0x40000\n"
    "s_cbranch_scc1 .Lvo_sp22_%=\n"
    "v_mov_b32 v108, 6\n"
    "s_mov_b32 s32, 33728\n"
    "buffer_store_dword v108, off, s[0:3], s32 sc1\n"
    "s_waitcnt vmcnt(0)\n"
    "s_branch .Lvo_go2_%=\n"
    ".Lvo_sp22_%=:\n"
    "buffer_load_dwordx4 v96, s[0:3], s42 offen offset:2048 sc1 lds\n"
    "s_waitcnt vmcnt(0)\n"
    "ds_read_b128 v[80:83], v92 offset:10240\n"
    "s_waitcnt lgkmcnt(0)\n"
    "v_max_u32 v108, v80, v82\n"
    "v_cmp_eq_u32 vcc, -1, v108\n"
    "s_nop 0\n"
    "s_cbranch_vccnz .Lvo_r2_%=\n"
    "s_branch .Lvo_go2_%=\n"
    ".Lvo_wf_%=:\n"
    ".Lvo_po23_%=:\n"
    "buffer_load_dword v110, v105, s[0:3], 0 offen sc1\n"
    "s_waitcnt vmcnt(0)\n"
    "v_cmp_gt_i32 vcc, v111, v110\n"
    "s_nop 0\n"
    "s_cbranch_vccz .Lvo_pd24_%=\n"
    "s_add_u32 s28, s28, 1\n"
    "s_cmp_lt_u32 s28, 0x40000\n"
    "s_cbranch_scc1 .Lvo_sp25_%=\n"
    "v_mov_b32 v110, 33\n"
    "s_mov_b32 s32, 33728\n"
    "buffer_store_dword v110, off, s[0:3], s32 sc1\n"
    "s_waitcnt vmcnt(0)\n"
    "s_branch .Lvo_pd24_%=\n"
    ".Lvo_sp25_%=:\n"
    "s_sleep 4\n"
    "s_branch .Lvo_po23_%=\n"
    ".Lvo_pd24_%=:\n"
    "s_branch .Lvo_wfb_%=\n"
    ".Lvo_end_%=:\n"
    "s_waitcnt vmcnt(0) lgkmcnt(0)\n"
    "s_endpgm\n"
    :: "{s[0:3]}"(s0_), "{s[4:7]}"(s4_), "{s[8:11]}"(s8_), "{s[12:15]}"(s12_), "{s[16:19]}"(s16_), "{s[20:23]}"(s20_), "{s[24:27]}"(s24_), "{v[0:3]}"(v0_), "{v[4:7]}"(v4_), "{v[8:11]}"(v8_), "{v[12:15]}"(v12_), "{v[16:19]}"(v16_), "{v[92:95]}"(v92_), "{v[96:99]}"(v96_), "{v[100:103]}"(v100_), "{v[104:107]}"(v104_) : "memory", "v255", "a255", "s95");
    __builtin_unreachable();
}
DEV void ct_role(const MegaArgs& A, const int L, const int u, char* smem) {
    ROLE_HEAD;
    Touch<384> tc;
    const int r5 = l / 5, q5 = l - 5 * r5, c0 = q5 < 4 ? 16 * u + 4 * q5 : 512 + 4 * u, tk = 8 * w + r5;
    u32x2 m0;
    {
        const int sg = 2 * u + L, rt = w & 1, j0 = 10 * (w >> 1);
        float* ex = (float*)(smem + 1024);
        u32x4 xa[10];
        const unsigned xo = ((32 * sg + 16 * rt + l16) * D_ + 32 * j0 + 8 * lq) * 2;
#pragma unroll
        for (int j = 0; j < 10; ++j) asm volatile("global_load_dwordx4 %0, %1, %2 offset:%3" : "=&v"(xa[j]) : "v"(xo), "s"(A.x0), "n"(64 * j) : "memory");
        asm volatile("global_load_dwordx2 %0, %1, %2" : "=&v"(m0) : "v"(((r5 & 7) * D_ + c0) * 2), "s"(A.mem0) : "memory");
        asm volatile("s_waitcnt vmcnt(0)" ::: "memory");
#pragma unroll
        for (int j = 0; j < 10; ++j) asm volatile("" : "+v"(xa[j]));
        asm volatile("" : "+v"(m0));
        float s = 0.f, q = 0.f;
#pragma unroll
        for (int j = 0; j < 10; ++j)
#pragma unroll
            for (int e = 0; e < 4; ++e) s += lo16(xa[j][e]) + hi16(xa[j][e]);
        s += __shfl_xor(s, 16); s += __shfl_xor(s, 32);
        ex[threadIdx.x] = s;
        asm volatile("s_waitcnt lgkmcnt(0)\n\ts_barrier" ::: "memory");
        const float mean = (ex[64 * rt + l16] + ex[128 + 64 * rt + l16]) * (1.0f / D_);
#pragma unroll
        for (int j = 0; j < 10; ++j)
#pragma unroll
            for (int e = 0; e < 4; ++e) { const float d = lo16(xa[j][e]) - mean, d2 = hi16(xa[j][e]) - mean; q += d * d; q += d2 * d2; }
        q += __shfl_xor(q, 16); q += __shfl_xor(q, 32);
        ex[256 + threadIdx.x] = q;
        asm volatile("s_waitcnt lgkmcnt(0)\n\ts_barrier" ::: "memory");
        q = ex[256 + 64 * rt + l16] + ex[384 + 64 * rt + l16];
        const float mb = rb(mean), rd = __builtin_amdgcn_rcpf(rb(sqrtf(rb(rb(q * (1.0f / D_)) + 1e-5f))));
#pragma unroll
        for (int j = 0; j < 10; ++j) {
            u32x4 o;
#pragma unroll
            for (int e = 0; e < 4; ++e) { const unsigned d = pk2(lo16(xa[j][e]) - mb, hi16(xa[j][e]) - mb); o[sw2(e)] = pk2(lo16(d) * rd, hi16(d) * rd); }
            bst16(rr, rec(sg, 0) + ((j0 + j) >> 2) * 16384 + ((j0 + j) & 3) * 2048 + rt * 1024 + l * 16, o);
        }
    }
#define XM 5
    const unsigned mpos = l < 40 && q5 < XM ? xl(RG_MEM + w * 16384 + (c0 >> 6) * 1024 + (((c0 >> 4) & 3) * 16 + 4 * (r5 >> 1) + ((c0 >> 1) & 2) + (r5 & 1)) * 16 + (c0 & 8), L) : OOB;
    const unsigned xpos = l < 40 ? (c0 >> 7) * 16384 + ((c0 >> 5) & 3) * 2048 + (tk >> 4) * 1024 + (((c0 >> 3) & 3) * 16 + (tk & 15)) * 16 + (c0 & 4) * 2 : OOB;
    const unsigned rpos = (L || l >= 40) ? xpos + RC_X2 : tk * 1280 + c0 * 2;
    const u32x4 rs = L ? rx : u32x4{lo(A.x0), hi(A.x0), 2048u * 1280u, 0x20000u};
#pragma unroll
    for (int k = 0; k < 4; ++k) bst16(rr, rec(2 * u + (k >> 1), L) + RC_X1 + 65536 + w * 2048 + (k & 1) * 1024 + l * 16, ones);
    bst8(rr, mpos, u32x2{nosent(m0[0]), m0[1]}); bst8(rr, mpos + SLOT, ones2); bst8(rr, mpos + 2 * SLOT, ones2);
    DRAIN_BARRIER();
    bst4(rr, threadIdx.x < 16 ? xl(Z_CT + (l & 7) * 256 + (32 * L + u) * 4, l >> 3) : OOB, 1u);
    if (!((A.layers >> L) & 1)) return;
    const u32x4 s0_ = {RFL(rx[0]), RFL(rx[1]), RFL(rx[2]), RFL(rx[3])};
    const u32x4 s4_ = {RFL(rs[0]), RFL(rs[1]), RFL(rs[2]), RFL(rs[3])};
    const u32x4 s8_ = {RFL(L << 13), RFL(REC0), RFL(xl(RG_UT + (u * 4 + w) * 8192, L)), RFL(xl(RG_UTX + (u * 4 + w) * 2048, L))};
    const u32x4 s12_ = {RFL(xl(RG_UM + (u * 4 + w) * 2048, L)), RFL(xl(RG_UMX + (u * 4 + w) * 512, L)), RFL(xl(RG_PT + w * 16384, L)), RFL(xl(RG_PM + w * 8192, L))};
    const u32x4 s16_ = {RFL(xl(Z_X + 16384 + (u & 1) * 8192, L)), RFL(xl(Z_QV + (u & 3) * 256, L)), RFL(DBG0 + ((L * 128 + 64 + u) * 4 + w) * 2048), RFL(DBG0 + (2 << 20) + ((L * 32 + u) * 4 + w) * 8192)};
    const u32x4 v192_ = {(unsigned)(l * 16), (unsigned)(l * 16 + 4096), (unsigned)((l & 2) ? 0xeeeeu : 0x4444u), (unsigned)(0x4444u)};
    const u32x4 v196_ = {(unsigned)((l16 >> 2) * 256 + lq * 64 + (l & 3) * 16), (unsigned)(lq * 64 + (l & 3) * 16), (unsigned)((lq >> 1) * 2048 + 1024 + (lq & 1) * 256 + l16 * 16), (unsigned)(lq * 2048 + 1536 + l16 * 16)};
    const u32x4 v200_ = {(unsigned)(0 + w * 640 + lq * 160 + l16 * 4), (unsigned)(l16 < 4 ? 0 + w * 640 + lq * 160 + 64 + l16 * 4 : 12800), (unsigned)(0 + (l < 40 ? l : 0) * 16), (unsigned)(mpos)};
    const u32x4 v204_ = {(unsigned)(2560 + w * 2560 + lq * 320 + l16 * 4), (unsigned)(l16 < 4 ? 2560 + w * 2560 + lq * 320 + 64 + l16 * 4 : 12800), (unsigned)(3840 + w * 2560 + lq * 320 + l16 * 4), (unsigned)(l16 < 4 ? 3840 + w * 2560 + lq * 320 + 64 + l16 * 4 : 12800)};
    const u32x4 v208_ = {(unsigned)(2560 + w * 640 + (l < 40 ? l : 0) * 16), (unsigned)(xpos + RC_X1), (unsigned)(rpos), (unsigned)(l < 4 && w == 1 ? xl(Z_X1 + l * 4096 + u * 128, L) : OOB)};
    const u32x4 v212_ = {(unsigned)(l < 16 ? ((u >> 2) & 1) * 16384 + (u & 3) * 2048 + l * 128 : OOB), (unsigned)(0u), (unsigned)(0u), (unsigned)(0u)};
    asm volatile(
    "s_mov_b32 s20, 0\n"
    "s_mov_b32 s28, -2\n"
    "v_lshrrev_b32 v4, 2, v192\n"
    "v_lshrrev_b32 v5, 9, v192\n"
    "v_add_u32 v5, 1, v5\n"
    ".Lct_po1_%=:\n"
    "buffer_load_dword v8, v4, s[0:3], s17 offen sc1\n"
    "s_waitcnt vmcnt(0)\n"
    "v_cmp_gt_i32 vcc, v5, v8\n"
    "s_nop 0\n"
    "s_cbranch_vccz .Lct_pd2_%=\n"
    "s_add_u32 s20, s20, 1\n"
    "s_cmp_lt_u32 s20, 0x40000\n"
    "s_cbranch_scc1 .Lct_sp3_%=\n"
    "v_mov_b32 v8, 40\n"
    "s_mov_b32 s24, 33728\n"
    "buffer_store_dword v8, off, s[0:3], s24 sc1\n"
    "s_waitcnt vmcnt(0)\n"
    "s_branch .Lct_pd2_%=\n"
    ".Lct_sp3_%=:\n"
    "s_sleep 16\n"
    "s_branch .Lct_po1_%=\n"
    ".Lct_pd2_%=:\n"
    "s_memrealtime s[22:23]\n"
    "s_waitcnt lgkmcnt(0)\n"
    "s_sub_u32 s26, s22, 804\n"
    "s_mov_b32 s28, -1\n"
    "s_add_u32 s28, s28, 1\n"
    "s_add_u32 s27, s28, 1\n"
    "s_add_u32 s29, s28, 0\n"
    "s_and_b32 s29, s29, 3\n"
    "s_lshl_b32 s29, s29, 22\n"
    "s_add_u32 s30, s28, 1\n"
    "s_and_b32 s30, s30, 3\n"
    "s_lshl_b32 s30, s30, 22\n"
    "s_add_u32 s31, s28, 3\n"
    "s_and_b32 s31, s31, 3\n"
    "s_lshl_b32 s31, s31, 22\n"
    "s_and_b32 s41, s28, 63\n"
    "s_lshl_b32 s41, s41, 7\n"
    "s_add_u32 s32, s12, s29\n"
    "s_add_u32 s33, s13, s29\n"
    "s_add_u32 s34, s15, s29\n"
    "s_add_u32 s35, s14, s29\n"
    "s_add_u32 s39, s10, s30\n"
    "s_add_u32 s40, s11, s30\n"
    "s_add_u32 s41, s16, s41\n"
    "s_add_u32 s36, s35, 4096\n"
    "s_add_u32 s37, s35, 16384\n"
    "s_add_u32 s38, s35, 20480\n"
    "s_add_u32 s43, s26, 304\n"
    "s_lshl_b32 s45, s28, 19\n"
    "s_lshl_b32 s25, s28, 11\n"
    "s_xor_b32 s25, s25, s8\n"
    "s_and_b32 s25, s25, 0x2000\n"
    "s_add_u32 s45, s45, s25\n"
    "s_add_u32 s45, s45, s9\n"
    "s_mul_i32 s46, s28, 40960\n"
    "s_cmp_eq_u32 s8, 0\n"
    "s_cselect_b32 s46, s46, s45\n"
    "v_mov_b64 v[76:77], 0\n"
    "v_mov_b64 v[78:79], 0\n"
    "v_mov_b64 v[80:81], 0\n"
    "v_mov_b64 v[82:83], 0\n"
    "s_mov_b32 s39, s10\n"
    "s_mov_b32 s40, s11\n"
    "buffer_load_dwordx4 a[0:3], v192, s[0:3], s39 offen offset:0 sc1\n"
    "buffer_load_dwordx4 a[4:7], v192, s[0:3], s39 offen offset:1024 sc1\n"
    "buffer_load_dwordx4 a[8:11], v192, s[0:3], s39 offen offset:2048 sc1\n"
    "buffer_load_dwordx4 a[12:15], v192, s[0:3], s39 offen offset:3072 sc1\n"
    "buffer_load_dwordx4 a[16:19], v193, s[0:3], s39 offen offset:0 sc1\n"
    "buffer_load_dwordx4 a[20:23], v193, s[0:3], s39 offen offset:1024 sc1\n"
    "buffer_load_dwordx4 a[24:27], v193, s[0:3], s39 offen offset:2048 sc1\n"
    "buffer_load_dwordx4 a[28:31], v193, s[0:3], s39 offen offset:3072 sc1\n"
    "buffer_load_dwordx4 v[0:3], v196, s[0:3], s40 offen offset:0 sc1\n"
    "buffer_load_dwordx4 v[4:7], v196, s[0:3], s40 offen offset:1024 sc1\n"
    "s_add_u32 s39, s10, s30\n"
    "s_add_u32 s40, s11, s30\n"
    "buffer_load_dwordx2 v[190:191], off, s[0:3], s41 sc1\n"
    "s_waitcnt vmcnt(0)\n"
    ".Lct_top_%=:\n"
    "s_waitcnt vmcnt(2)\n"
    "v_mov_b64 v[8:9], v[0:1]\n"
    "v_mov_b64 v[10:11], v[2:3]\n"
    "v_mov_b64 v[12:13], v[4:5]\n"
    "v_mov_b64 v[14:15], v[6:7]\n"
    "v_mov_b32_dpp v16, v0 row_shl:4 row_mask:0xf bank_mask:0xf bound_ctrl:1\n"
    "v_mov_b32_dpp v17, v1 row_shl:4 row_mask:0xf bank_mask:0xf bound_ctrl:1\n"
    "v_mov_b32_dpp v18, v2 row_shl:4 row_mask:0xf bank_mask:0xf bound_ctrl:1\n"
    "v_mov_b32_dpp v19, v3 row_shl:4 row_mask:0xf bank_mask:0xf bound_ctrl:1\n"
    "v_mov_b32_dpp v20, v4 row_shl:4 row_mask:0xf bank_mask:0xf bound_ctrl:1\n"
    "v_mov_b32_dpp v21, v5 row_shl:4 row_mask:0xf bank_mask:0xf bound_ctrl:1\n"
    "v_mov_b32_dpp v22, v6 row_shl:4 row_mask:0xf bank_mask:0xf bound_ctrl:1\n"
    "v_mov_b32_dpp v23, v7 row_shl:4 row_mask:0xf bank_mask:0xf bound_ctrl:1\n"
    "v_mov_b32_dpp v24, v0 row_shl:8 row_mask:0xf bank_mask:0xf bound_ctrl:1\n"
    "v_mov_b32_dpp v25, v1 row_shl:8 row_mask:0xf bank_mask:0xf bound_ctrl:1\n"
    "v_mov_b32_dpp v26, v2 row_shl:8 row_mask:0xf bank_mask:0xf bound_ctrl:1\n"
    "v_mov_b32_dpp v27, v3 row_shl:8 row_mask:0xf bank_mask:0xf bound_ctrl:1\n"
    "v_mov_b32_dpp v28, v4 row_shl:8 row_mask:0xf bank_mask:0xf bound_ctrl:1\n"
    "v_mov_b32_dpp v29, v5 row_shl:8 row_mask:0xf bank_mask:0xf bound_ctrl:1\n"
    "v_mov_b32_dpp v30, v6 row_shl:8 row_mask:0xf bank_mask:0xf bound_ctrl:1\n"
    "v_mov_b32_dpp v31, v7 row_shl:8 row_mask:0xf bank_mask:0xf bound_ctrl:1\n"
    "v_mov_b32_dpp v32, v0 row_shl:12 row_mask:0xf bank_mask:0xf bound_ctrl:1\n"
    "v_mov_b32_dpp v33, v1 row_shl:12 row_mask:0xf bank_mask:0xf bound_ctrl:1\n"
    "v_mov_b32_dpp v34, v2 row_shl:12 row_mask:0xf bank_mask:0xf bound_ctrl:1\n"
    "v_mov_b32_dpp v35, v3 row_shl:12 row_mask:0xf bank_mask:0xf bound_ctrl:1\n"
    "v_mov_b32_dpp v36, v4 row_shl:12 row_mask:0xf bank_mask:0xf bound_ctrl:1\n"
    "v_mov_b32_dpp v37, v5 row_shl:12 row_mask:0xf bank_mask:0xf bound_ctrl:1\n"
    "v_mov_b32_dpp v38, v6 row_shl:12 row_mask:0xf bank_mask:0xf bound_ctrl:1\n"
    "v_mov_b32_dpp v39, v7 row_shl:12 row_mask:0xf bank_mask:0xf bound_ctrl:1\n"
    "s_waitcnt vmcnt(1)\n"
    ".Lct_x0_%=:\n"
    "v_readfirstlane_b32 s25, v191\n"
    "v_readfirstlane_b32 s24, v190\n"
    "s_cmp_eq_u32 s25, s27\n"
    "s_cselect_b32 s26, s24, s26\n"
    "s_cbranch_scc0 .Lct_xr0_%=\n"
    ".Lct_xg0_%=:\n"
    "s_add_u32 s43, s26, 97\n"
    "s_add_u32 s44, s26, 109\n"
    ".Lct_mk0x3_%=:\n"
    ".Lct_rt5_%=:\n"
    "s_memrealtime s[22:23]\n"
    "s_waitcnt lgkmcnt(0)\n"
    "s_sub_u32 s24, s43, s22\n"
    "s_cmp_lt_u32 s24, 600\n"
    "s_cbranch_scc1 .Lct_rt5_%=\n"
    "buffer_load_dwordx4 v[40:43], v192, s[0:3], s32 offen offset:0 sc1\n"
    "buffer_load_dwordx4 v[44:47], v192, s[0:3], s32 offen offset:1024 sc1\n"
    "buffer_load_dwordx4 v[48:51], v197, s[0:3], s33 offen offset:0 sc1\n"
    "buffer_load_dwordx4 v[52:55], v197, s[0:3], s33 offen offset:256 sc1\n"
    "v_mov_b32 v148, s28\n"
    "v_mov_b32 v149, s28\n"
    "v_mov_b32 v150, s28\n"
    "v_mov_b32 v151, s28\n"
    "s_waitcnt vmcnt(4)\n"
    "s_barrier\n"
    "buffer_store_dwordx4 v[148:151], v211, s[0:3], 0 offen sc1\n"
    ".Lct_rt6_%=:\n"
    "s_memrealtime s[22:23]\n"
    "s_waitcnt lgkmcnt(0)\n"
    "s_sub_u32 s24, s44, s22\n"
    "s_cmp_lt_u32 s24, 600\n"
    "s_cbranch_scc1 .Lct_rt6_%=\n"
    "buffer_load_dwordx4 v[72:75], v193, s[0:3], s34 offen sc1\n"
    "buffer_load_dwordx4 v[56:59], v192, s[0:3], s34 offen offset:0 sc1\n"
    "buffer_load_dwordx4 v[60:63], v192, s[0:3], s34 offen offset:1024 sc1\n"
    "buffer_load_dwordx4 v[64:67], v192, s[0:3], s34 offen offset:2048 sc1\n"
    "buffer_load_dwordx4 v[68:71], v192, s[0:3], s34 offen offset:3072 sc1\n"
    ".Lct_mk1x6_%=:\n"
    ".Lct_take0_%=:\n"
    "s_waitcnt vmcnt(6)\n"
    "v_max_u32 v184, v40, v44\n"
    "v_max3_u32 v184, v48, v50, v184\n"
    "v_max3_u32 v184, v52, v54, v184\n"
    "s_waitcnt vmcnt(4)\n"
    "v_smfmac_f32_16x16x64_bf16 v[76:79], v[72:75], v[40:47], v194\n"
    "v_max3_u32 v184, v72, v74, v184\n"
    "v_smfmac_f32_16x16x64_bf16 v[80:83], v[72:75], v[48:55], v194\n"
    "s_waitcnt vmcnt(3)\n"
    "v_smfmac_f32_16x16x64_bf16 v[80:83], v[56:59], v[8:15], v194\n"
    "v_max3_u32 v184, v56, v58, v184\n"
    "v_smfmac_f32_16x16x64_bf16 v[76:79], v[56:59], a[0:7], v194\n"
    "s_waitcnt vmcnt(2)\n"
    "v_smfmac_f32_16x16x64_bf16 v[80:83], v[60:63], v[16:23], v194\n"
    "v_max3_u32 v184, v60, v62, v184\n"
    "v_smfmac_f32_16x16x64_bf16 v[76:79], v[60:63], a[8:15], v194\n"
    "s_waitcnt vmcnt(1)\n"
    "v_smfmac_f32_16x16x64_bf16 v[80:83], v[64:67], v[24:31], v194\n"
    "v_max3_u32 v184, v64, v66, v184\n"
    "v_smfmac_f32_16x16x64_bf16 v[76:79], v[64:67], a[16:23], v194\n"
    "s_waitcnt vmcnt(0)\n"
    "v_smfmac_f32_16x16x64_bf16 v[80:83], v[68:71], v[32:39], v194\n"
    "v_max3_u32 v184, v68, v70, v184\n"
    "v_smfmac_f32_16x16x64_bf16 v[76:79], v[68:71], a[24:31], v194\n"
    ".Lct_mk2x7_%=:\n"
    "s_nop 1\n"
    "v_cmp_eq_u32 vcc, -1, v184\n"
    "s_nop 1\n"
    "v_add_f32 v80, v80, v82\n"
    "v_add_f32 v81, v81, v83\n"
    "s_nop 0\n"
    "v_pk_add_f32 v[76:77], v[76:77], v[78:79]\n"
    "ds_write2_b32 v201, v80, v81 offset1:20\n"
    "ds_write2_b32 v200, v76, v77 offset1:20\n"
    "s_cbranch_vccnz .Lct_retry0_%=\n"
    ".Lct_go0_%=:\n"
    "s_waitcnt lgkmcnt(0)\n"
    "s_barrier\n"
    "ds_read_b128 v[164:167], v202 offset:0\n"
    "ds_read_b128 v[168:171], v202 offset:640\n"
    "ds_read_b128 v[172:175], v202 offset:1280\n"
    "ds_read_b128 v[176:179], v202 offset:1920\n"
    "s_waitcnt lgkmcnt(2)\n"
    "v_pk_add_f32 v[164:165], v[164:165], v[168:169]\n"
    "v_pk_add_f32 v[166:167], v[166:167], v[170:171]\n"
    "s_waitcnt lgkmcnt(1)\n"
    "v_pk_add_f32 v[164:165], v[164:165], v[172:173]\n"
    "v_pk_add_f32 v[166:167], v[166:167], v[174:175]\n"
    "s_waitcnt lgkmcnt(0)\n"
    "v_pk_add_f32 v[164:165], v[164:165], v[176:177]\n"
    "v_pk_add_f32 v[166:167], v[166:167], v[178:179]\n"
    "v_cvt_pk_bf16_f32 v164, v164, v165\n"
    "v_cvt_pk_bf16_f32 v165, v166, v167\n"
    "v_min_u32 v164, -2, v164\n"
    "buffer_store_dwordx2 v[164:165], v203, s[0:3], s30 offen sc1\n"
    ".Lct_mk3x8_%=:\n"
    "s_sleep 2\n"
    "v_mov_b64 v[166:167], -1\n"
    "buffer_load_dwordx4 v[132:135], v199, s[0:3], s35 offen offset:0 sc1\n"
    "buffer_load_dwordx4 v[136:139], v199, s[0:3], s37 offen offset:0 sc1\n"
    "buffer_load_dwordx4 v[140:143], v199, s[0:3], s35 offen offset:256 sc1\n"
    "buffer_load_dwordx4 v[144:147], v199, s[0:3], s37 offen offset:256 sc1\n"
    "buffer_load_dwordx4 v[116:119], v198, s[0:3], s35 offen sc1\n"
    "buffer_load_dwordx4 v[84:87], v192, s[0:3], s35 offen offset:0 sc1\n"
    "buffer_load_dwordx4 v[88:91], v192, s[0:3], s35 offen offset:2048 sc1\n"
    "buffer_load_dwordx4 v[120:123], v198, s[0:3], s36 offen sc1\n"
    "buffer_load_dwordx4 v[92:95], v192, s[0:3], s36 offen offset:0 sc1\n"
    "buffer_load_dwordx4 v[96:99], v192, s[0:3], s36 offen offset:2048 sc1\n"
    ".Lct_mk7x9_%=:\n"
    "buffer_load_dwordx4 v[124:127], v198, s[0:3], s37 offen sc1\n"
    "s_waitcnt vmcnt(10)\n"
    "v_max3_u32 v184, v132, v134, v132\n"
    "v_swap_b32 v133, v134\n"
    "s_nop 1\n"
    "v_mfma_f32_16x16x32_bf16 v[148:151], v[132:135], v[40:43], 0\n"
    "v_mfma_f32_16x16x32_bf16 v[152:155], v[132:135], v[48:51], 0\n"
    "buffer_load_dwordx4 v[100:103], v192, s[0:3], s37 offen offset:0 sc1\n"
    "s_waitcnt vmcnt(10)\n"
    "v_max3_u32 v184, v136, v138, v184\n"
    "v_swap_b32 v137, v138\n"
    "s_nop 1\n"
    "v_mfma_f32_16x16x32_bf16 v[148:151], v[136:139], v[44:47], v[148:151]\n"
    "v_mfma_f32_16x16x32_bf16 v[152:155], v[136:139], v[52:55], v[152:155]\n"
    "buffer_load_dwordx4 v[104:107], v192, s[0:3], s37 offen offset:2048 sc1\n"
    "s_waitcnt vmcnt(10)\n"
    "v_max3_u32 v184, v140, v142, v184\n"
    "v_swap_b32 v141, v142\n"
    "s_nop 1\n"
    "v_mfma_f32_16x16x32_bf16 v[156:159], v[140:143], v[40:43], 0\n"
    "v_mfma_f32_16x16x32_bf16 v[160:163], v[140:143], v[48:51], 0\n"
    "buffer_load_dwordx4 v[128:131], v198, s[0:3], s38 offen sc1\n"
    "s_waitcnt vmcnt(10)\n"
    "v_max3_u32 v184, v144, v146, v184\n"
    "v_swap_b32 v145, v146\n"
    "s_nop 1\n"
    "v_mfma_f32_16x16x32_bf16 v[156:159], v[144:147], v[44:47], v[156:159]\n"
    "v_mfma_f32_16x16x32_bf16 v[160:163], v[144:147], v[52:55], v[160:163]\n"
    "buffer_load_dwordx4 v[108:111], v192, s[0:3], s38 offen offset:0 sc1\n"
    "s_waitcnt vmcnt(10)\n"
    "v_max3_u32 v184, v116, v118, v184\n"
    "v_smfmac_f32_16x16x64_bf16 v[148:151], v[116:119], a[0:7], v195\n"
    "v_smfmac_f32_16x16x64_bf16 v[152:155], v[116:119], v[8:15], v195\n"
    "buffer_load_dwordx4 v[112:115], v192, s[0:3], s38 offen offset:2048 sc1\n"
    "s_waitcnt vmcnt(10)\n"
    "v_max3_u32 v184, v84, v86, v184\n"
    "v_swap_b32 v85, v86\n"
    "s_nop 1\n"
    "v_mfma_f32_16x16x32_bf16 v[156:159], v[84:87], a[0:3], v[156:159]\n"
    "v_mfma_f32_16x16x32_bf16 v[160:163], v[84:87], v[8:11], v[160:163]\n"
    "buffer_load_dwordx2 v[186:187], v210, s[4:7], s46 offen sc1\n"
    "s_waitcnt vmcnt(10)\n"
    "v_max3_u32 v184, v88, v90, v184\n"
    "v_swap_b32 v89, v90\n"
    "s_nop 1\n"
    "v_mfma_f32_16x16x32_bf16 v[156:159], v[88:91], a[4:7], v[156:159]\n"
    "v_mfma_f32_16x16x32_bf16 v[160:163], v[88:91], v[12:15], v[160:163]\n"
    "buffer_store_dwordx2 v[166:167], v203, s[0:3], s31 offen sc1\n"
    "s_waitcnt vmcnt(10)\n"
    "v_max3_u32 v184, v120, v122, v184\n"
    "v_smfmac_f32_16x16x64_bf16 v[148:151], v[120:123], a[8:15], v195\n"
    "v_smfmac_f32_16x16x64_bf16 v[152:155], v[120:123], v[16:23], v195\n"
    "buffer_load_dwordx4 a[32:35], v192, s[0:3], s39 offen offset:0 sc1\n"
    "s_waitcnt vmcnt(10)\n"
    "v_max3_u32 v184, v92, v94, v184\n"
    "v_swap_b32 v93, v94\n"
    "s_nop 1\n"
    "v_mfma_f32_16x16x32_bf16 v[156:159], v[92:95], a[8:11], v[156:159]\n"
    "v_mfma_f32_16x16x32_bf16 v[160:163], v[92:95], v[16:19], v[160:163]\n"
    "buffer_load_dwordx4 a[36:39], v192, s[0:3], s39 offen offset:1024 sc1\n"
    "s_waitcnt vmcnt(10)\n"
    "v_max3_u32 v184, v96, v98, v184\n"
    "v_swap_b32 v97, v98\n"
    "s_nop 1\n"
    "v_mfma_f32_16x16x32_bf16 v[156:159], v[96:99], a[12:15], v[156:159]\n"
    "v_mfma_f32_16x16x32_bf16 v[160:163], v[96:99], v[20:23], v[160:163]\n"
    "buffer_load_dwordx4 a[40:43], v192, s[0:3], s39 offen offset:2048 sc1\n"
    "s_waitcnt vmcnt(10)\n"
    "v_max3_u32 v184, v124, v126, v184\n"
    "v_smfmac_f32_16x16x64_bf16 v[148:151], v[124:127], a[16:23], v195\n"
    "v_smfmac_f32_16x16x64_bf16 v[152:155], v[124:127], v[24:31], v195\n"
    "buffer_load_dwordx4 a[44:47], v192, s[0:3], s39 offen offset:3072 sc1\n"
    "s_waitcnt vmcnt(10)\n"
    "v_max3_u32 v184, v100, v102, v184\n"
    "v_swap_b32 v101, v102\n"
    "s_nop 1\n"
    "v_mfma_f32_16x16x32_bf16 v[156:159], v[100:103], a[16:19], v[156:159]\n"
    "v_mfma_f32_16x16x32_bf16 v[160:163], v[100:103], v[24:27], v[160:163]\n"
    "buffer_load_dwordx4 a[48:51], v193, s[0:3], s39 offen offset:0 sc1\n"
    "s_waitcnt vmcnt(10)\n"
    "v_max3_u32 v184, v104, v106, v184\n"
    "v_swap_b32 v105, v106\n"
    "s_nop 1\n"
    "v_mfma_f32_16x16x32_bf16 v[156:159], v[104:107], a[20:23], v[156:159]\n"
    "v_mfma_f32_16x16x32_bf16 v[160:163], v[104:107], v[28:31], v[160:163]\n"
    "buffer_load_dwordx4 a[52:55], v193, s[0:3], s39 offen offset:1024 sc1\n"
    "s_waitcnt vmcnt(10)\n"
    "v_max3_u32 v184, v128, v130, v184\n"
    "v_smfmac_f32_16x16x64_bf16 v[148:151], v[128:131], a[24:31], v195\n"
    "v_smfmac_f32_16x16x64_bf16 v[152:155], v[128:131], v[32:39], v195\n"
    "buffer_load_dwordx4 a[56:59], v193, s[0:3], s39 offen offset:2048 sc1\n"
    "s_waitcnt vmcnt(10)\n"
    "v_max3_u32 v184, v108, v110, v184\n"
    "v_swap_b32 v109, v110\n"
    "s_nop 1\n"
    "v_mfma_f32_16x16x32_bf16 v[156:159], v[108:111], a[24:27], v[156:159]\n"
    "v_mfma_f32_16x16x32_bf16 v[160:163], v[108:111], v[32:35], v[160:163]\n"
    "buffer_load_dwordx4 a[60:63], v193, s[0:3], s39 offen offset:3072 sc1\n"
    "s_waitcnt vmcnt(10)\n"
    "v_max3_u32 v184, v112, v114, v184\n"
    "v_swap_b32 v113, v114\n"
    "s_nop 1\n"
    "v_mfma_f32_16x16x32_bf16 v[156:159], v[112:115], a[28:31], v[156:159]\n"
    "v_mfma_f32_16x16x32_bf16 v[160:163], v[112:115], v[36:39], v[160:163]\n"
    "v_cmp_eq_u32 vcc, -1, v184\n"
    "s_nop 0\n"
    "s_cbranch_vccnz .Lct_tr0_%=\n"
    ".Lct_tg0_%=:\n"
    ".Lct_mk4x10_%=:\n"
    "ds_write2_b32 v204, v148, v149 offset0:0 offset1:20\n"
    "ds_write2_b32 v204, v150, v151 offset0:40 offset1:60\n"
    "ds_write2_b32 v205, v152, v153 offset0:0 offset1:20\n"
    "ds_write2_b32 v205, v154, v155 offset0:40 offset1:60\n"
    "ds_write2_b32 v206, v156, v157 offset0:0 offset1:20\n"
    "ds_write2_b32 v206, v158, v159 offset0:40 offset1:60\n"
    "ds_write2_b32 v207, v160, v161 offset0:0 offset1:20\n"
    "ds_write2_b32 v207, v162, v163 offset0:40 offset1:60\n"
    "s_mov_b32 s42, s45\n"
    "buffer_load_dwordx4 v[0:3], v196, s[0:3], s40 offen offset:0 sc1\n"
    "buffer_load_dwordx4 v[4:7], v196, s[0:3], s40 offen offset:1024 sc1\n"
    "s_add_u32 s28, s28, 1\n"
    "s_add_u32 s27, s28, 1\n"
    "s_add_u32 s29, s28, 0\n"
    "s_and_b32 s29, s29, 3\n"
    "s_lshl_b32 s29, s29, 22\n"
    "s_add_u32 s30, s28, 1\n"
    "s_and_b32 s30, s30, 3\n"
    "s_lshl_b32 s30, s30, 22\n"
    "s_add_u32 s31, s28, 3\n"
    "s_and_b32 s31, s31, 3\n"
    "s_lshl_b32 s31, s31, 22\n"
    "s_and_b32 s41, s28, 63\n"
    "s_lshl_b32 s41, s41, 7\n"
    "s_add_u32 s32, s12, s29\n"
    "s_add_u32 s33, s13, s29\n"
    "s_add_u32 s34, s15, s29\n"
    "s_add_u32 s35, s14, s29\n"
    "s_add_u32 s39, s10, s30\n"
    "s_add_u32 s40, s11, s30\n"
    "s_add_u32 s41, s16, s41\n"
    "s_add_u32 s36, s35, 4096\n"
    "s_add_u32 s37, s35, 16384\n"
    "s_add_u32 s38, s35, 20480\n"
    "s_add_u32 s43, s26, 304\n"
    "s_lshl_b32 s45, s28, 19\n"
    "s_lshl_b32 s25, s28, 11\n"
    "s_xor_b32 s25, s25, s8\n"
    "s_and_b32 s25, s25, 0x2000\n"
    "s_add_u32 s45, s45, s25\n"
    "s_add_u32 s45, s45, s9\n"
    "s_mul_i32 s46, s28, 40960\n"
    "s_cmp_eq_u32 s8, 0\n"
    "s_cselect_b32 s46, s46, s45\n"
    "v_mov_b64 v[76:77], 0\n"
    "v_mov_b64 v[78:79], 0\n"
    "v_mov_b64 v[80:81], 0\n"
    "v_mov_b64 v[82:83], 0\n"
    "s_waitcnt vmcnt(11)\n"
    "v_lshlrev_b32 v148, 16, v186\n"
    "v_and_b32 v149, 0xffff0000, v186\n"
    "v_lshlrev_b32 v150, 16, v187\n"
    "v_and_b32 v151, 0xffff0000, v187\n"
    "s_memrealtime s[22:23]\n"
    "s_waitcnt lgkmcnt(0)\n"
    "s_barrier\n"
    "s_sub_u32 s24, s43, s22\n"
    "s_cmp_gt_i32 s24, 6\n"
    "s_cselect_b32 s24, 2130706432, s41\n"
    "buffer_load_dwordx2 v[190:191], off, s[0:3], s24 sc1\n"
    ".Lct_mk6x11_%=:\n"
    "ds_read_b128 v[164:167], v208 offset:0\n"
    "ds_read_b128 v[168:171], v208 offset:2560\n"
    "ds_read_b128 v[172:175], v208 offset:5120\n"
    "ds_read_b128 v[176:179], v208 offset:7680\n"
    "s_waitcnt lgkmcnt(2)\n"
    "v_pk_add_f32 v[164:165], v[164:165], v[168:169]\n"
    "v_pk_add_f32 v[166:167], v[166:167], v[170:171]\n"
    "s_waitcnt lgkmcnt(1)\n"
    "v_pk_add_f32 v[164:165], v[164:165], v[172:173]\n"
    "v_pk_add_f32 v[166:167], v[166:167], v[174:175]\n"
    "s_waitcnt lgkmcnt(0)\n"
    "v_pk_add_f32 v[164:165], v[164:165], v[176:177]\n"
    "v_pk_add_f32 v[166:167], v[166:167], v[178:179]\n"
    "v_cvt_pk_bf16_f32 v164, v164, v165\n"
    "v_cvt_pk_bf16_f32 v165, v166, v167\n"
    "v_lshlrev_b32 v168, 16, v164\n"
    "v_and_b32 v169, 0xffff0000, v164\n"
    "v_lshlrev_b32 v170, 16, v165\n"
    "v_and_b32 v171, 0xffff0000, v165\n"
    "v_pk_add_f32 v[168:169], v[168:169], v[148:149]\n"
    "v_pk_add_f32 v[170:171], v[170:171], v[150:151]\n"
    "v_cvt_pk_bf16_f32 v164, v168, v169\n"
    "v_cvt_pk_bf16_f32 v165, v170, v171\n"
    "buffer_store_dwordx2 v[164:165], v209, s[0:3], s42 offen sc1\n"
    ".Lct_mk5x12_%=:\n"
    "s_waitcnt vmcnt(2)\n"
    "v_mov_b64 v[8:9], v[0:1]\n"
    "v_mov_b64 v[10:11], v[2:3]\n"
    "v_mov_b64 v[12:13], v[4:5]\n"
    "v_mov_b64 v[14:15], v[6:7]\n"
    "v_mov_b32_dpp v16, v0 row_shl:4 row_mask:0xf bank_mask:0xf bound_ctrl:1\n"
    "v_mov_b32_dpp v17, v1 row_shl:4 row_mask:0xf bank_mask:0xf bound_ctrl:1\n"
    "v_mov_b32_dpp v18, v2 row_shl:4 row_mask:0xf bank_mask:0xf bound_ctrl:1\n"
    "v_mov_b32_dpp v19, v3 row_shl:4 row_mask:0xf bank_mask:0xf bound_ctrl:1\n"
    "v_mov_b32_dpp v20, v4 row_shl:4 row_mask:0xf bank_mask:0xf bound_ctrl:1\n"
    "v_mov_b32_dpp v21, v5 row_shl:4 row_mask:0xf bank_mask:0xf bound_ctrl:1\n"
    "v_mov_b32_dpp v22, v6 row_shl:4 row_mask:0xf bank_mask:0xf bound_ctrl:1\n"
    "v_mov_b32_dpp v23, v7 row_shl:4 row_mask:0xf bank_mask:0xf bound_ctrl:1\n"
    "v_mov_b32_dpp v24, v0 row_shl:8 row_mask:0xf bank_mask:0xf bound_ctrl:1\n"
    "v_mov_b32_dpp v25, v1 row_shl:8 row_mask:0xf bank_mask:0xf bound_ctrl:1\n"
    "v_mov_b32_dpp v26, v2 row_shl:8 row_mask:0xf bank_mask:0xf bound_ctrl:1\n"
    "v_mov_b32_dpp v27, v3 row_shl:8 row_mask:0xf bank_mask:0xf bound_ctrl:1\n"
    "v_mov_b32_dpp v28, v4 row_shl:8 row_mask:0xf bank_mask:0xf bound_ctrl:1\n"
    "v_mov_b32_dpp v29, v5 row_shl:8 row_mask:0xf bank_mask:0xf bound_ctrl:1\n"
    "v_mov_b32_dpp v30, v6 row_shl:8 row_mask:0xf bank_mask:0xf bound_ctrl:1\n"
    "v_mov_b32_dpp v31, v7 row_shl:8 row_mask:0xf bank_mask:0xf bound_ctrl:1\n"
    "v_mov_b32_dpp v32, v0 row_shl:12 row_mask:0xf bank_mask:0xf bound_ctrl:1\n"
    "v_mov_b32_dpp v33, v1 row_shl:12 row_mask:0xf bank_mask:0xf bound_ctrl:1\n"
    "v_mov_b32_dpp v34, v2 row_shl:12 row_mask:0xf bank_mask:0xf bound_ctrl:1\n"
    "v_mov_b32_dpp v35, v3 row_shl:12 row_mask:0xf bank_mask:0xf bound_ctrl:1\n"
    "v_mov_b32_dpp v36, v4 row_shl:12 row_mask:0xf bank_mask:0xf bound_ctrl:1\n"
    "v_mov_b32_dpp v37, v5 row_shl:12 row_mask:0xf bank_mask:0xf bound_ctrl:1\n"
    "v_mov_b32_dpp v38, v6 row_shl:12 row_mask:0xf bank_mask:0xf bound_ctrl:1\n"
    "v_mov_b32_dpp v39, v7 row_shl:12 row_mask:0xf bank_mask:0xf bound_ctrl:1\n"
    "s_waitcnt vmcnt(1)\n"
    ".Lct_x1_%=:\n"
    "v_readfirstlane_b32 s25, v191\n"
    "v_readfirstlane_b32 s24, v190\n"
    "s_cmp_eq_u32 s25, s27\n"
    "s_cselect_b32 s26, s24, s26\n"
    "s_cbranch_scc0 .Lct_xr1_%=\n"
    ".Lct_xg1_%=:\n"
    "s_add_u32 s43, s26, 97\n"
    "s_add_u32 s44, s26, 109\n"
    ".Lct_mk0x17_%=:\n"
    ".Lct_rt19_%=:\n"
    "s_memrealtime s[22:23]\n"
    "s_waitcnt lgkmcnt(0)\n"
    "s_sub_u32 s24, s43, s22\n"
    "s_cmp_lt_u32 s24, 600\n"
    "s_cbranch_scc1 .Lct_rt19_%=\n"
    "buffer_load_dwordx4 v[40:43], v192, s[0:3], s32 offen offset:0 sc1\n"
    "buffer_load_dwordx4 v[44:47], v192, s[0:3], s32 offen offset:1024 sc1\n"
    "buffer_load_dwordx4 v[48:51], v197, s[0:3], s33 offen offset:0 sc1\n"
    "buffer_load_dwordx4 v[52:55], v197, s[0:3], s33 offen offset:256 sc1\n"
    "v_mov_b32 v148, s28\n"
    "v_mov_b32 v149, s28\n"
    "v_mov_b32 v150, s28\n"
    "v_mov_b32 v151, s28\n"
    "s_waitcnt vmcnt(4)\n"
    "s_barrier\n"
    "buffer_store_dwordx4 v[148:151], v211, s[0:3], 0 offen sc1\n"
    ".Lct_rt20_%=:\n"
    "s_memrealtime s[22:23]\n"
    "s_waitcnt lgkmcnt(0)\n"
    "s_sub_u32 s24, s44, s22\n"
    "s_cmp_lt_u32 s24, 600\n"
    "s_cbranch_scc1 .Lct_rt20_%=\n"
    "buffer_load_dwordx4 v[72:75], v193, s[0:3], s34 offen sc1\n"
    "buffer_load_dwordx4 v[56:59], v192, s[0:3], s34 offen offset:0 sc1\n"
    "buffer_load_dwordx4 v[60:63], v192, s[0:3], s34 offen offset:1024 sc1\n"
    "buffer_load_dwordx4 v[64:67], v192, s[0:3], s34 offen offset:2048 sc1\n"
    "buffer_load_dwordx4 v[68:71], v192, s[0:3], s34 offen offset:3072 sc1\n"
    ".Lct_mk1x20_%=:\n"
    ".Lct_take1_%=:\n"
    "s_waitcnt vmcnt(6)\n"
    "v_max_u32 v184, v40, v44\n"
    "v_max3_u32 v184, v48, v50, v184\n"
    "v_max3_u32 v184, v52, v54, v184\n"
    "s_waitcnt vmcnt(4)\n"
    "v_smfmac_f32_16x16x64_bf16 v[76:79], v[72:75], v[40:47], v194\n"
    "v_max3_u32 v184, v72, v74, v184\n"
    "v_smfmac_f32_16x16x64_bf16 v[80:83], v[72:75], v[48:55], v194\n"
    "s_waitcnt vmcnt(3)\n"
    "v_smfmac_f32_16x16x64_bf16 v[80:83], v[56:59], v[8:15], v194\n"
    "v_max3_u32 v184, v56, v58, v184\n"
    "v_smfmac_f32_16x16x64_bf16 v[76:79], v[56:59], a[32:39], v194\n"
    "s_waitcnt vmcnt(2)\n"
    "v_smfmac_f32_16x16x64_bf16 v[80:83], v[60:63], v[16:23], v194\n"
    "v_max3_u32 v184, v60, v62, v184\n"
    "v_smfmac_f32_16x16x64_bf16 v[76:79], v[60:63], a[40:47], v194\n"
    "s_waitcnt vmcnt(1)\n"
    "v_smfmac_f32_16x16x64_bf16 v[80:83], v[64:67], v[24:31], v194\n"
    "v_max3_u32 v184, v64, v66, v184\n"
    "v_smfmac_f32_16x16x64_bf16 v[76:79], v[64:67], a[48:55], v194\n"
    "s_waitcnt vmcnt(0)\n"
    "v_smfmac_f32_16x16x64_bf16 v[80:83], v[68:71], v[32:39], v194\n"
    "v_max3_u32 v184, v68, v70, v184\n"
    "v_smfmac_f32_16x16x64_bf16 v[76:79], v[68:71], a[56:63], v194\n"
    ".Lct_mk2x21_%=:\n"
    "s_nop 1\n"
    "v_cmp_eq_u32 vcc, -1, v184\n"
    "s_nop 1\n"
    "v_add_f32 v80, v80, v82\n"
    "v_add_f32 v81, v81, v83\n"
    "s_nop 0\n"
    "v_pk_add_f32 v[76:77], v[76:77], v[78:79]\n"
    "ds_write2_b32 v201, v80, v81 offset1:20\n"
    "ds_write2_b32 v200, v76, v77 offset1:20\n"
    "s_cbranch_vccnz .Lct_retry1_%=\n"
    ".Lct_go1_%=:\n"
    "s_waitcnt lgkmcnt(0)\n"
    "s_barrier\n"
    "ds_read_b128 v[164:167], v202 offset:0\n"
    "ds_read_b128 v[168:171], v202 offset:640\n"
    "ds_read_b128 v[172:175], v202 offset:1280\n"
    "ds_read_b128 v[176:179], v202 offset:1920\n"
    "s_waitcnt lgkmcnt(2)\n"
    "v_pk_add_f32 v[164:165], v[164:165], v[168:169]\n"
    "v_pk_add_f32 v[166:167], v[166:167], v[170:171]\n"
    "s_waitcnt lgkmcnt(1)\n"
    "v_pk_add_f32 v[164:165], v[164:165], v[172:173]\n"
    "v_pk_add_f32 v[166:167], v[166:167], v[174:175]\n"
    "s_waitcnt lgkmcnt(0)\n"
    "v_pk_add_f32 v[164:165], v[164:165], v[176:177]\n"
    "v_pk_add_f32 v[166:167], v[166:167], v[178:179]\n"
    "v_cvt_pk_bf16_f32 v164, v164, v165\n"
    "v_cvt_pk_bf16_f32 v165, v166, v167\n"
    "v_min_u32 v164, -2, v164\n"
    "buffer_store_dwordx2 v[164:165], v203, s[0:3], s30 offen sc1\n"
    ".Lct_mk3x22_%=:\n"
    "s_sleep 2\n"
    "v_mov_b64 v[166:167], -1\n"
    "buffer_load_dwordx4 v[132:135], v199, s[0:3], s35 offen offset:0 sc1\n"
    "buffer_load_dwordx4 v[136:139], v199, s[0:3], s37 offen offset:0 sc1\n"
    "buffer_load_dwordx4 v[140:143], v199, s[0:3], s35 offen offset:256 sc1\n"
    "buffer_load_dwordx4 v[144:147], v199, s[0:3], s37 offen offset:256 sc1\n"
    "buffer_load_dwordx4 v[116:119], v198, s[0:3], s35 offen sc1\n"
    "buffer_load_dwordx4 v[84:87], v192, s[0:3], s35 offen offset:0 sc1\n"
    "buffer_load_dwordx4 v[88:91], v192, s[0:3], s35 offen offset:2048 sc1\n"
    "buffer_load_dwordx4 v[120:123], v198, s[0:3], s36 offen sc1\n"
    "buffer_load_dwordx4 v[92:95], v192, s[0:3], s36 offen offset:0 sc1\n"
    "buffer_load_dwordx4 v[96:99], v192, s[0:3], s36 offen offset:2048 sc1\n"
    ".Lct_mk7x23_%=:\n"
    "buffer_load_dwordx4 v[124:127], v198, s[0:3], s37 offen sc1\n"
    "s_waitcnt vmcnt(10)\n"
    "v_max3_u32 v184, v132, v134, v132\n"
    "v_swap_b32 v133, v134\n"
    "s_nop 1\n"
    "v_mfma_f32_16x16x32_bf16 v[148:151], v[132:135], v[40:43], 0\n"
    "v_mfma_f32_16x16x32_bf16 v[152:155], v[132:135], v[48:51], 0\n"
    "buffer_load_dwordx4 v[100:103], v192, s[0:3], s37 offen offset:0 sc1\n"
    "s_waitcnt vmcnt(10)\n"
    "v_max3_u32 v184, v136, v138, v184\n"
    "v_swap_b32 v137, v138\n"
    "s_nop 1\n"
    "v_mfma_f32_16x16x32_bf16 v[148:151], v[136:139], v[44:47], v[148:151]\n"
    "v_mfma_f32_16x16x32_bf16 v[152:155], v[136:139], v[52:55], v[152:155]\n"
    "buffer_load_dwordx4 v[104:107], v192, s[0:3], s37 offen offset:2048 sc1\n"
    "s_waitcnt vmcnt(10)\n"
    "v_max3_u32 v184, v140, v142, v184\n"
    "v_swap_b32 v141, v142\n"
    "s_nop 1\n"
    "v_mfma_f32_16x16x32_bf16 v[156:159], v[140:143], v[40:43], 0\n"
    "v_mfma_f32_16x16x32_bf16 v[160:163], v[140:143], v[48:51], 0\n"
    "buffer_load_dwordx4 v[128:131], v198, s[0:3], s38 offen sc1\n"
    "s_waitcnt vmcnt(10)\n"
    "v_max3_u32 v184, v144, v146, v184\n"
    "v_swap_b32 v145, v146\n"
    "s_nop 1\n"
    "v_mfma_f32_16x16x32_bf16 v[156:159], v[144:147], v[44:47], v[156:159]\n"
    "v_mfma_f32_16x16x32_bf16 v[160:163], v[144:147], v[52:55], v[160:163]\n"
    "buffer_load_dwordx4 v[108:111], v192, s[0:3], s38 offen offset:0 sc1\n"
    "s_waitcnt vmcnt(10)\n"
    "v_max3_u32 v184, v116, v118, v184\n"
    "v_smfmac_f32_16x16x64_bf16 v[148:151], v[116:119], a[32:39], v195\n"
    "v_smfmac_f32_16x16x64_bf16 v[152:155], v[116:119], v[8:15], v195\n"
    "buffer_load_dwordx4 v[112:115], v192, s[0:3], s38 offen offset:2048 sc1\n"
    "s_waitcnt vmcnt(10)\n"
    "v_max3_u32 v184, v84, v86, v184\n"
    "v_swap_b32 v85, v86\n"
    "s_nop 1\n"
    "v_mfma_f32_16x16x32_bf16 v[156:159], v[84:87], a[32:35], v[156:159]\n"
    "v_mfma_f32_16x16x32_bf16 v[160:163], v[84:87], v[8:11], v[160:163]\n"
    "buffer_load_dwordx2 v[186:187], v210, s[4:7], s46 offen sc1\n"
    "s_waitcnt vmcnt(10)\n"
    "v_max3_u32 v184, v88, v90, v184\n"
    "v_swap_b32 v89, v90\n"
    "s_nop 1\n"
    "v_mfma_f32_16x16x32_bf16 v[156:159], v[88:91], a[36:39], v[156:159]\n"
    "v_mfma_f32_16x16x32_bf16 v[160:163], v[88:91], v[12:15], v[160:163]\n"
    "buffer_store_dwordx2 v[166:167], v203, s[0:3], s31 offen sc1\n"
    "s_waitcnt vmcnt(10)\n"
    "v_max3_u32 v184, v120, v122, v184\n"
    "v_smfmac_f32_16x16x64_bf16 v[148:151], v[120:123], a[40:47], v195\n"
    "v_smfmac_f32_16x16x64_bf16 v[152:155], v[120:123], v[16:23], v195\n"
    "buffer_load_dwordx4 a[0:3], v192, s[0:3], s39 offen offset:0 sc1\n"
    "s_waitcnt vmcnt(10)\n"
    "v_max3_u32 v184, v92, v94, v184\n"
    "v_swap_b32 v93, v94\n"
    "s_nop 1\n"
    "v_mfma_f32_16x16x32_bf16 v[156:159], v[92:95], a[40:43], v[156:159]\n"
    "v_mfma_f32_16x16x32_bf16 v[160:163], v[92:95], v[16:19], v[160:163]\n"
    "buffer_load_dwordx4 a[4:7], v192, s[0:3], s39 offen offset:1024 sc1\n"
    "s_waitcnt vmcnt(10)\n"
    "v_max3_u32 v184, v96, v98, v184\n"
    "v_swap_b32 v97, v98\n"
    "s_nop 1\n"
    "v_mfma_f32_16x16x32_bf16 v[156:159], v[96:99], a[44:47], v[156:159]\n"
    "v_mfma_f32_16x16x32_bf16 v[160:163], v[96:99], v[20:23], v[160:163]\n"
    "buffer_load_dwordx4 a[8:11], v192, s[0:3], s39 offen offset:2048 sc1\n"
    "s_waitcnt vmcnt(10)\n"
    "v_max3_u32 v184, v124, v126, v184\n"
    "v_smfmac_f32_16x16x64_bf16 v[148:151], v[124:127], a[48:55], v195\n"
    "v_smfmac_f32_16x16x64_bf16 v[152:155], v[124:127], v[24:31], v195\n"
    "buffer_load_dwordx4 a[12:15], v192, s[0:3], s39 offen offset:3072 sc1\n"
    "s_waitcnt vmcnt(10)\n"
    "v_max3_u32 v184, v100, v102, v184\n"
    "v_swap_b32 v101, v102\n"
    "s_nop 1\n"
    "v_mfma_f32_16x16x32_bf16 v[156:159], v[100:103], a[48:51], v[156:159]\n"
    "v_mfma_f32_16x16x32_bf16 v[160:163], v[100:103], v[24:27], v[160:163]\n"
    "buffer_load_dwordx4 a[16:19], v193, s[0:3], s39 offen offset:0 sc1\n"
    "s_waitcnt vmcnt(10)\n"
    "v_max3_u32 v184, v104, v106, v184\n"
    "v_swap_b32 v105, v106\n"
    "s_nop 1\n"
    "v_mfma_f32_16x16x32_bf16 v[156:159], v[104:107], a[52:55], v[156:159]\n"
    "v_mfma_f32_16x16x32_bf16 v[160:163], v[104:107], v[28:31], v[160:163]\n"
    "buffer_load_dwordx4 a[20:23], v193, s[0:3], s39 offen offset:1024 sc1\n"
    "s_waitcnt vmcnt(10)\n"
    "v_max3_u32 v184, v128, v130, v184\n"
    "v_smfmac_f32_16x16x64_bf16 v[148:151], v[128:131], a[56:63], v195\n"
    "v_smfmac_f32_16x16x64_bf16 v[152:155], v[128:131], v[32:39], v195\n"
    "buffer_load_dwordx4 a[24:27], v193, s[0:3], s39 offen offset:2048 sc1\n"
    "s_waitcnt vmcnt(10)\n"
    "v_max3_u32 v184, v108, v110, v184\n"
    "v_swap_b32 v109, v110\n"
    "s_nop 1\n"
    "v_mfma_f32_16x16x32_bf16 v[156:159], v[108:111], a[56:59], v[156:159]\n"
    "v_mfma_f32_16x16x32_bf16 v[160:163], v[108:111], v[32:35], v[160:163]\n"
    "buffer_load_dwordx4 a[28:31], v193, s[0:3], s39 offen offset:3072 sc1\n"
    "s_waitcnt vmcnt(10)\n"
    "v_max3_u32 v184, v112, v114, v184\n"
    "v_swap_b32 v113, v114\n"
    "s_nop 1\n"
    "v_mfma_f32_16x16x32_bf16 v[156:159], v[112:115], a[60:63], v[156:159]\n"
    "v_mfma_f32_16x16x32_bf16 v[160:163], v[112:115], v[36:39], v[160:163]\n"
    "v_cmp_eq_u32 vcc, -1, v184\n"
    "s_nop 0\n"
    "s_cbranch_vccnz .Lct_tr1_%=\n"
    ".Lct_tg1_%=:\n"
    ".Lct_mk4x24_%=:\n"
    "ds_write2_b32 v204, v148, v149 offset0:0 offset1:20\n"
    "ds_write2_b32 v204, v150, v151 offset0:40 offset1:60\n"
    "ds_write2_b32 v205, v152, v153 offset0:0 offset1:20\n"
    "ds_write2_b32 v205, v154, v155 offset0:40 offset1:60\n"
    "ds_write2_b32 v206, v156, v157 offset0:0 offset1:20\n"
    "ds_write2_b32 v206, v158, v159 offset0:40 offset1:60\n"
    "ds_write2_b32 v207, v160, v161 offset0:0 offset1:20\n"
    "ds_write2_b32 v207, v162, v163 offset0:40 offset1:60\n"
    "s_mov_b32 s42, s45\n"
    "buffer_load_dwordx4 v[0:3], v196, s[0:3], s40 offen offset:0 sc1\n"
    "buffer_load_dwordx4 v[4:7], v196, s[0:3], s40 offen offset:1024 sc1\n"
    "s_add_u32 s28, s28, 1\n"
    "s_add_u32 s27, s28, 1\n"
    "s_add_u32 s29, s28, 0\n"
    "s_and_b32 s29, s29, 3\n"
    "s_lshl_b32 s29, s29, 22\n"
    "s_add_u32 s30, s28, 1\n"
    "s_and_b32 s30, s30, 3\n"
    "s_lshl_b32 s30, s30, 22\n"
    "s_add_u32 s31, s28, 3\n"
    "s_and_b32 s31, s31, 3\n"
    "s_lshl_b32 s31, s31, 22\n"
    "s_and_b32 s41, s28, 63\n"
    "s_lshl_b32 s41, s41, 7\n"
    "s_add_u32 s32, s12, s29\n"
    "s_add_u32 s33, s13, s29\n"
    "s_add_u32 s34, s15, s29\n"
    "s_add_u32 s35, s14, s29\n"
    "s_add_u32 s39, s10, s30\n"
    "s_add_u32 s40, s11, s30\n"
    "s_add_u32 s41, s16, s41\n"
    "s_add_u32 s36, s35, 4096\n"
    "s_add_u32 s37, s35, 16384\n"
    "s_add_u32 s38, s35, 20480\n"
    "s_add_u32 s43, s26, 304\n"
    "s_lshl_b32 s45, s28, 19\n"
    "s_lshl_b32 s25, s28, 11\n"
    "s_xor_b32 s25, s25, s8\n"
    "s_and_b32 s25, s25, 0x2000\n"
    "s_add_u32 s45, s45, s25\n"
    "s_add_u32 s45, s45, s9\n"
    "s_mul_i32 s46, s28, 40960\n"
    "s_cmp_eq_u32 s8, 0\n"
    "s_cselect_b32 s46, s46, s45\n"
    "v_mov_b64 v[76:77], 0\n"
    "v_mov_b64 v[78:79], 0\n"
    "v_mov_b64 v[80:81], 0\n"
    "v_mov_b64 v[82:83], 0\n"
    "s_waitcnt vmcnt(11)\n"
    "v_lshlrev_b32 v148, 16, v186\n"
    "v_and_b32 v149, 0xffff0000, v186\n"
    "v_lshlrev_b32 v150, 16, v187\n"
    "v_and_b32 v151, 0xffff0000, v187\n"
    "s_memrealtime s[22:23]\n"
    "s_waitcnt lgkmcnt(0)\n"
    "s_barrier\n"
    "s_sub_u32 s24, s43, s22\n"
    "s_cmp_gt_i32 s24, 6\n"
    "s_cselect_b32 s24, 2130706432, s41\n"
    "buffer_load_dwordx2 v[190:191], off, s[0:3], s24 sc1\n"
    ".Lct_mk6x25_%=:\n"
    "ds_read_b128 v[164:167], v208 offset:0\n"
    "ds_read_b128 v[168:171], v208 offset:2560\n"
    "ds_read_b128 v[172:175], v208 offset:5120\n"
    "ds_read_b128 v[176:179], v208 offset:7680\n"
    "s_waitcnt lgkmcnt(2)\n"
    "v_pk_add_f32 v[164:165], v[164:165], v[168:169]\n"
    "v_pk_add_f32 v[166:167], v[166:167], v[170:171]\n"
    "s_waitcnt lgkmcnt(1)\n"
    "v_pk_add_f32 v[164:165], v[164:165], v[172:173]\n"
    "v_pk_add_f32 v[166:167], v[166:167], v[174:175]\n"
    "s_waitcnt lgkmcnt(0)\n"
    "v_pk_add_f32 v[164:165], v[164:165], v[176:177]\n"
    "v_pk_add_f32 v[166:167], v[166:167], v[178:179]\n"
    "v_cvt_pk_bf16_f32 v164, v164, v165\n"
    "v_cvt_pk_bf16_f32 v165, v166, v167\n"
    "v_lshlrev_b32 v168, 16, v164\n"
    "v_and_b32 v169, 0xffff0000, v164\n"
    "v_lshlrev_b32 v170, 16, v165\n"
    "v_and_b32 v171, 0xffff0000, v165\n"
    "v_pk_add_f32 v[168:169], v[168:169], v[148:149]\n"
    "v_pk_add_f32 v[170:171], v[170:171], v[150:151]\n"
    "v_cvt_pk_bf16_f32 v164, v168, v169\n"
    "v_cvt_pk_bf16_f32 v165, v170, v171\n"
    "buffer_store_dwordx2 v[164:165], v209, s[0:3], s42 offen sc1\n"
    ".Lct_mk5x26_%=:\n"
    "s_cmp_lt_u32 s28, 64\n"
    "s_cbranch_scc1 .Lct_top_%=\n"
    "v_mov_b32 v148, s28\n"
    "v_mov_b32 v149, s28\n"
    "v_mov_b32 v150, s28\n"
    "v_mov_b32 v151, s28\n"
    "s_waitcnt vmcnt(0)\n"
    "s_barrier\n"
    "buffer_store_dwordx4 v[148:151], v211, s[0:3], 0 offen sc1\n"
    "s_waitcnt vmcnt(0) lgkmcnt(0)\n"
    "s_endpgm\n"
    ".Lct_xr0_%=:\n"
    "s_add_u32 s20, s20, 1\n"
    "s_cmp_lt_u32 s20, 0x40000\n"
    "s_cbranch_scc1 .Lct_sp14_%=\n"
    "v_mov_b32 v184, 42\n"
    "s_mov_b32 s24, 33728\n"
    "buffer_store_dword v184, off, s[0:3], s24 sc1\n"
    "s_waitcnt vmcnt(0)\n"
    "s_branch .Lct_xg0_%=\n"
    ".Lct_sp14_%=:\n"
    ".Lct_rt15_%=:\n"
    "s_memrealtime s[22:23]\n"
    "s_waitcnt lgkmcnt(0)\n"
    "s_sub_u32 s24, s43, s22\n"
    "s_cmp_lt_u32 s24, 600\n"
    "s_cbranch_scc1 .Lct_rt15_%=\n"
    "s_add_u32 s43, s22, 44\n"
    "buffer_load_dwordx2 v[190:191], off, s[0:3], s41 sc1\n"
    "s_waitcnt vmcnt(0)\n"
    "s_branch .Lct_x0_%=\n"
    ".Lct_retry0_%=:\n"
    "s_add_u32 s20, s20, 1\n"
    "s_cmp_lt_u32 s20, 0x40000\n"
    "s_cbranch_scc1 .Lct_sp16_%=\n"
    "v_mov_b32 v184, 3\n"
    "s_mov_b32 s24, 33728\n"
    "buffer_store_dword v184, off, s[0:3], s24 sc1\n"
    "s_waitcnt vmcnt(0)\n"
    "s_branch .Lct_go0_%=\n"
    ".Lct_sp16_%=:\n"
    "s_waitcnt vmcnt(0) lgkmcnt(0)\n"
    "v_mov_b64 v[76:77], 0\n"
    "v_mov_b64 v[78:79], 0\n"
    "v_mov_b64 v[80:81], 0\n"
    "v_mov_b64 v[82:83], 0\n"
    "buffer_load_dwordx4 v[40:43], v192, s[0:3], s32 offen offset:0 sc1\n"
    "buffer_load_dwordx4 v[44:47], v192, s[0:3], s32 offen offset:1024 sc1\n"
    "buffer_load_dwordx4 v[48:51], v197, s[0:3], s33 offen offset:0 sc1\n"
    "buffer_load_dwordx4 v[52:55], v197, s[0:3], s33 offen offset:256 sc1\n"
    "buffer_store_dwordx4 v[148:151], v211, s[0:3], 0 offen sc1\n"
    "buffer_load_dwordx4 v[72:75], v193, s[0:3], s34 offen sc1\n"
    "buffer_load_dwordx4 v[56:59], v192, s[0:3], s34 offen offset:0 sc1\n"
    "buffer_load_dwordx4 v[60:63], v192, s[0:3], s34 offen offset:1024 sc1\n"
    "buffer_load_dwordx4 v[64:67], v192, s[0:3], s34 offen offset:2048 sc1\n"
    "buffer_load_dwordx4 v[68:71], v192, s[0:3], s34 offen offset:3072 sc1\n"
    "s_waitcnt vmcnt(0)\n"
    "s_branch .Lct_take0_%=\n"
    ".Lct_tr0_%=:\n"
    "s_add_u32 s20, s20, 1\n"
    "s_cmp_lt_u32 s20, 0x40000\n"
    "s_cbranch_scc1 .Lct_sp17_%=\n"
    "v_mov_b32 v184, 4\n"
    "s_mov_b32 s24, 33728\n"
    "buffer_store_dword v184, off, s[0:3], s24 sc1\n"
    "s_waitcnt vmcnt(0)\n"
    "s_branch .Lct_tg0_%=\n"
    ".Lct_sp17_%=:\n"
    "buffer_load_dwordx4 v[132:135], v199, s[0:3], s35 offen offset:0 sc1\n"
    "buffer_load_dwordx4 v[136:139], v199, s[0:3], s37 offen offset:0 sc1\n"
    "buffer_load_dwordx4 v[140:143], v199, s[0:3], s35 offen offset:256 sc1\n"
    "buffer_load_dwordx4 v[144:147], v199, s[0:3], s37 offen offset:256 sc1\n"
    "buffer_load_dwordx4 v[116:119], v198, s[0:3], s35 offen sc1\n"
    "buffer_load_dwordx4 v[84:87], v192, s[0:3], s35 offen offset:0 sc1\n"
    "buffer_load_dwordx4 v[88:91], v192, s[0:3], s35 offen offset:2048 sc1\n"
    "buffer_load_dwordx4 v[120:123], v198, s[0:3], s36 offen sc1\n"
    "buffer_load_dwordx4 v[92:95], v192, s[0:3], s36 offen offset:0 sc1\n"
    "buffer_load_dwordx4 v[96:99], v192, s[0:3], s36 offen offset:2048 sc1\n"
    "buffer_load_dwordx4 v[124:127], v198, s[0:3], s37 offen sc1\n"
    "buffer_load_dwordx4 v[100:103], v192, s[0:3], s37 offen offset:0 sc1\n"
    "buffer_load_dwordx4 v[104:107], v192, s[0:3], s37 offen offset:2048 sc1\n"
    "buffer_load_dwordx4 v[128:131], v198, s[0:3], s38 offen sc1\n"
    "buffer_load_dwordx4 v[108:111], v192, s[0:3], s38 offen offset:0 sc1\n"
    "buffer_load_dwordx4 v[112:115], v192, s[0:3], s38 offen offset:2048 sc1\n"
    "s_waitcnt vmcnt(0)\n"
    "v_max3_u32 v184, v132, v134, v132\n"
    "v_swap_b32 v133, v134\n"
    "s_nop 1\n"
    "v_mfma_f32_16x16x32_bf16 v[148:151], v[132:135], v[40:43], 0\n"
    "v_mfma_f32_16x16x32_bf16 v[152:155], v[132:135], v[48:51], 0\n"
    "v_max3_u32 v184, v136, v138, v184\n"
    "v_swap_b32 v137, v138\n"
    "s_nop 1\n"
    "v_mfma_f32_16x16x32_bf16 v[148:151], v[136:139], v[44:47], v[148:151]\n"
    "v_mfma_f32_16x16x32_bf16 v[152:155], v[136:139], v[52:55], v[152:155]\n"
    "v_max3_u32 v184, v140, v142, v184\n"
    "v_swap_b32 v141, v142\n"
    "s_nop 1\n"
    "v_mfma_f32_16x16x32_bf16 v[156:159], v[140:143], v[40:43], 0\n"
    "v_mfma_f32_16x16x32_bf16 v[160:163], v[140:143], v[48:51], 0\n"
    "v_max3_u32 v184, v144, v146, v184\n"
    "v_swap_b32 v145, v146\n"
    "s_nop 1\n"
    "v_mfma_f32_16x16x32_bf16 v[156:159], v[144:147], v[44:47], v[156:159]\n"
    "v_mfma_f32_16x16x32_bf16 v[160:163], v[144:147], v[52:55], v[160:163]\n"
    "v_max3_u32 v184, v116, v118, v184\n"
    "v_smfmac_f32_16x16x64_bf16 v[148:151], v[116:119], a[0:7], v195\n"
    "v_smfmac_f32_16x16x64_bf16 v[152:155], v[116:119], v[8:15], v195\n"
    "v_max3_u32 v184, v84, v86, v184\n"
    "v_swap_b32 v85, v86\n"
    "s_nop 1\n"
    "v_mfma_f32_16x16x32_bf16 v[156:159], v[84:87], a[0:3], v[156:159]\n"
    "v_mfma_f32_16x16x32_bf16 v[160:163], v[84:87], v[8:11], v[160:163]\n"
    "v_max3_u32 v184, v88, v90, v184\n"
    "v_swap_b32 v89, v90\n"
    "s_nop 1\n"
    "v_mfma_f32_16x16x32_bf16 v[156:159], v[88:91], a[4:7], v[156:159]\n"
    "v_mfma_f32_16x16x32_bf16 v[160:163], v[88:91], v[12:15], v[160:163]\n"
    "v_max3_u32 v184, v120, v122, v184\n"
    "v_smfmac_f32_16x16x64_bf16 v[148:151], v[120:123], a[8:15], v195\n"
    "v_smfmac_f32_16x16x64_bf16 v[152:155], v[120:123], v[16:23], v195\n"
    "v_max3_u32 v184, v92, v94, v184\n"
    "v_swap_b32 v93, v94\n"
    "s_nop 1\n"
    "v_mfma_f32_16x16x32_bf16 v[156:159], v[92:95], a[8:11], v[156:159]\n"
    "v_mfma_f32_16x16x32_bf16 v[160:163], v[92:95], v[16:19], v[160:163]\n"
    "v_max3_u32 v184, v96, v98, v184\n"
    "v_swap_b32 v97, v98\n"
    "s_nop 1\n"
    "v_mfma_f32_16x16x32_bf16 v[156:159], v[96:99], a[12:15], v[156:159]\n"
    "v_mfma_f32_16x16x32_bf16 v[160:163], v[96:99], v[20:23], v[160:163]\n"
    "v_max3_u32 v184, v124, v126, v184\n"
    "v_smfmac_f32_16x16x64_bf16 v[148:151], v[124:127], a[16:23], v195\n"
    "v_smfmac_f32_16x16x64_bf16 v[152:155], v[124:127], v[24:31], v195\n"
    "v_max3_u32 v184, v100, v102, v184\n"
    "v_swap_b32 v101, v102\n"
    "s_nop 1\n"
    "v_mfma_f32_16x16x32_bf16 v[156:159], v[100:103], a[16:19], v[156:159]\n"
    "v_mfma_f32_16x16x32_bf16 v[160:163], v[100:103], v[24:27], v[160:163]\n"
    "v_max3_u32 v184, v104, v106, v184\n"
    "v_swap_b32 v105, v106\n"
    "s_nop 1\n"
    "v_mfma_f32_16x16x32_bf16 v[156:159], v[104:107], a[20:23], v[156:159]\n"
    "v_mfma_f32_16x16x32_bf16 v[160:163], v[104:107], v[28:31], v[160:163]\n"
    "v_max3_u32 v184, v128, v130, v184\n"
    "v_smfmac_f32_16x16x64_bf16 v[148:151], v[128:131], a[24:31], v195\n"
    "v_smfmac_f32_16x16x64_bf16 v[152:155], v[128:131], v[32:39], v195\n"
    "v_max3_u32 v184, v108, v110, v184\n"
    "v_swap_b32 v109, v110\n"
    "s_nop 1\n"
    "v_mfma_f32_16x16x32_bf16 v[156:159], v[108:111], a[24:27], v[156:159]\n"
    "v_mfma_f32_16x16x32_bf16 v[160:163], v[108:111], v[32:35], v[160:163]\n"
    "v_max3_u32 v184, v112, v114, v184\n"
    "v_swap_b32 v113, v114\n"
    "s_nop 1\n"
    "v_mfma_f32_16x16x32_bf16 v[156:159], v[112:115], a[28:31], v[156:159]\n"
    "v_mfma_f32_16x16x32_bf16 v[160:163], v[112:115], v[36:39], v[160:163]\n"
    "v_cmp_eq_u32 vcc, -1, v184\n"
    "s_nop 0\n"
    "s_cbranch_vccnz .Lct_tr0_%=\n"
    "s_nop 7\n"
    "s_branch .Lct_tg0_%=\n"
    ".Lct_xr1_%=:\n"
    "s_add_u32 s20, s20, 1\n"
    "s_cmp_lt_u32 s20, 0x40000\n"
    "s_cbranch_scc1 .Lct_sp28_%=\n"
    "v_mov_b32 v184, 42\n"
    "s_mov_b32 s24, 33728\n"
    "buffer_store_dword v184, off, s[0:3], s24 sc1\n"
    "s_waitcnt vmcnt(0)\n"
    "s_branch .Lct_xg1_%=\n"
    ".Lct_sp28_%=:\n"
    ".Lct_rt29_%=:\n"
    "s_memrealtime s[22:23]\n"
    "s_waitcnt lgkmcnt(0)\n"
    "s_sub_u32 s24, s43, s22\n"
    "s_cmp_lt_u32 s24, 600\n"
    "s_cbranch_scc1 .Lct_rt29_%=\n"
    "s_add_u32 s43, s22, 44\n"
    "buffer_load_dwordx2 v[190:191], off, s[0:3], s41 sc1\n"
    "s_waitcnt vmcnt(0)\n"
    "s_branch .Lct_x1_%=\n"
    ".Lct_retry1_%=:\n"
    "s_add_u32 s20, s20, 1\n"
    "s_cmp_lt_u32 s20, 0x40000\n"
    "s_cbranch_scc1 .Lct_sp30_%=\n"
    "v_mov_b32 v184, 3\n"
    "s_mov_b32 s24, 33728\n"
    "buffer_store_dword v184, off, s[0:3], s24 sc1\n"
    "s_waitcnt vmcnt(0)\n"
    "s_branch .Lct_go1_%=\n"
    ".Lct_sp30_%=:\n"
    "s_waitcnt vmcnt(0) lgkmcnt(0)\n"
    "v_mov_b64 v[76:77], 0\n"
    "v_mov_b64 v[78:79], 0\n"
    "v_mov_b64 v[80:81], 0\n"
    "v_mov_b64 v[82:83], 0\n"
    "buffer_load_dwordx4 v[40:43], v192, s[0:3], s32 offen offset:0 sc1\n"
    "buffer_load_dwordx4 v[44:47], v192, s[0:3], s32 offen offset:1024 sc1\n"
    "buffer_load_dwordx4 v[48:51], v197, s[0:3], s33 offen offset:0 sc1\n"
    "buffer_load_dwordx4 v[52:55], v197, s[0:3], s33 offen offset:256 sc1\n"
    "buffer_store_dwordx4 v[148:151], v211, s[0:3], 0 offen sc1\n"
    "buffer_load_dwordx4 v[72:75], v193, s[0:3], s34 offen sc1\n"
    "buffer_load_dwordx4 v[56:59], v192, s[0:3], s34 offen offset:0 sc1\n"
    "buffer_load_dwordx4 v[60:63], v192, s[0:3], s34 offen offset:1024 sc1\n"
    "buffer_load_dwordx4 v[64:67], v192, s[0:3], s34 offen offset:2048 sc1\n"
    "buffer_load_dwordx4 v[68:71], v192, s[0:3], s34 offen offset:3072 sc1\n"
    "s_waitcnt vmcnt(0)\n"
    "s_branch .Lct_take1_%=\n"
    ".Lct_tr1_%=:\n"
    "s_add_u32 s20, s20, 1\n"
    "s_cmp_lt_u32 s20, 0x40000\n"
    "s_cbranch_scc1 .Lct_sp31_%=\n"
    "v_mov_b32 v184, 4\n"
    "s_mov_b32 s24, 33728\n"
    "buffer_store_dword v184, off, s[0:3], s24 sc1\n"
    "s_waitcnt vmcnt(0)\n"
    "s_branch .Lct_tg1_%=\n"
    ".Lct_sp31_%=:\n"
    "buffer_load_dwordx4 v[132:135], v199, s[0:3], s35 offen offset:0 sc1\n"
    "buffer_load_dwordx4 v[136:139], v199, s[0:3], s37 offen offset:0 sc1\n"
    "buffer_load_dwordx4 v[140:143], v199, s[0:3], s35 offen offset:256 sc1\n"
    "buffer_load_dwordx4 v[144:147], v199, s[0:3], s37 offen offset:256 sc1\n"
    "buffer_load_dwordx4 v[116:119], v198, s[0:3], s35 offen sc1\n"
    "buffer_load_dwordx4 v[84:87], v192, s[0:3], s35 offen offset:0 sc1\n"
    "buffer_load_dwordx4 v[88:91], v192, s[0:3], s35 offen offset:2048 sc1\n"
    "buffer_load_dwordx4 v[120:123], v198, s[0:3], s36 offen sc1\n"
    "buffer_load_dwordx4 v[92:95], v192, s[0:3], s36 offen offset:0 sc1\n"
    "buffer_load_dwordx4 v[96:99], v192, s[0:3], s36 offen offset:2048 sc1\n"
    "buffer_load_dwordx4 v[124:127], v198, s[0:3], s37 offen sc1\n"
    "buffer_load_dwordx4 v[100:103], v192, s[0:3], s37 offen offset:0 sc1\n"
    "buffer_load_dwordx4 v[104:107], v192, s[0:3], s37 offen offset:2048 sc1\n"
    "buffer_load_dwordx4 v[128:131], v198, s[0:3], s38 offen sc1\n"
    "buffer_load_dwordx4 v[108:111], v192, s[0:3], s38 offen offset:0 sc1\n"
    "buffer_load_dwordx4 v[112:115], v192, s[0:3], s38 offen offset:2048 sc1\n"
    "s_waitcnt vmcnt(0)\n"
    "v_max3_u32 v184, v132, v134, v132\n"
    "v_swap_b32 v133, v134\n"
    "s_nop 1\n"
    "v_mfma_f32_16x16x32_bf16 v[148:151], v[132:135], v[40:43], 0\n"
    "v_mfma_f32_16x16x32_bf16 v[152:155], v[132:135], v[48:51], 0\n"
    "v_max3_u32 v184, v136, v138, v184\n"
    "v_swap_b32 v137, v138\n"
    "s_nop 1\n"
    "v_mfma_f32_16x16x32_bf16 v[148:151], v[136:139], v[44:47], v[148:151]\n"
    "v_mfma_f32_16x16x32_bf16 v[152:155], v[136:139], v[52:55], v[152:155]\n"
    "v_max3_u32 v184, v140, v142, v184\n"
    "v_swap_b32 v141, v142\n"
    "s_nop 1\n"
    "v_mfma_f32_16x16x32_bf16 v[156:159], v[140:143], v[40:43], 0\n"
    "v_mfma_f32_16x16x32_bf16 v[160:163], v[140:143], v[48:51], 0\n"
    "v_max3_u32 v184, v144, v146, v184\n"
    "v_swap_b32 v145, v146\n"
    "s_nop 1\n"
    "v_mfma_f32_16x16x32_bf16 v[156:159], v[144:147], v[44:47], v[156:159]\n"
    "v_mfma_f32_16x16x32_bf16 v[160:163], v[144:147], v[52:55], v[160:163]\n"
    "v_max3_u32 v184, v116, v118, v184\n"
    "v_smfmac_f32_16x16x64_bf16 v[148:151], v[116:119], a[32:39], v195\n"
    "v_smfmac_f32_16x16x64_bf16 v[152:155], v[116:119], v[8:15], v195\n"
    "v_max3_u32 v184, v84, v86, v184\n"
    "v_swap_b32 v85, v86\n"
    "s_nop 1\n"
    "v_mfma_f32_16x16x32_bf16 v[156:159], v[84:87], a[32:35], v[156:159]\n"
    "v_mfma_f32_16x16x32_bf16 v[160:163], v[84:87], v[8:11], v[160:163]\n"
    "v_max3_u32 v184, v88, v90, v184\n"
    "v_swap_b32 v89, v90\n"
    "s_nop 1\n"
    "v_mfma_f32_16x16x32_bf16 v[156:159], v[88:91], a[36:39], v[156:159]\n"
    "v_mfma_f32_16x16x32_bf16 v[160:163], v[88:91], v[12:15], v[160:163]\n"
    "v_max3_u32 v184, v120, v122, v184\n"
    "v_smfmac_f32_16x16x64_bf16 v[148:151], v[120:123], a[40:47], v195\n"
    "v_smfmac_f32_16x16x64_bf16 v[152:155], v[120:123], v[16:23], v195\n"
    "v_max3_u32 v184, v92, v94, v184\n"
    "v_swap_b32 v93, v94\n"
    "s_nop 1\n"
    "v_mfma_f32_16x16x32_bf16 v[156:159], v[92:95], a[40:43], v[156:159]\n"
    "v_mfma_f32_16x16x32_bf16 v[160:163], v[92:95], v[16:19], v[160:163]\n"
    "v_max3_u32 v184, v96, v98, v184\n"
    "v_swap_b32 v97, v98\n"
    "s_nop 1\n"
    "v_mfma_f32_16x16x32_bf16 v[156:159], v[96:99], a[44:47], v[156:159]\n"
    "v_mfma_f32_16x16x32_bf16 v[160:163], v[96:99], v[20:23], v[160:163]\n"
    "v_max3_u32 v184, v124, v126, v184\n"
    "v_smfmac_f32_16x16x64_bf16 v[148:151], v[124:127], a[48:55], v195\n"
    "v_smfmac_f32_16x16x64_bf16 v[152:155], v[124:127], v[24:31], v195\n"
    "v_max3_u32 v184, v100, v102, v184\n"
    "v_swap_b32 v101, v102\n"
    "s_nop 1\n"
    "v_mfma_f32_16x16x32_bf16 v[156:159], v[100:103], a[48:51], v[156:159]\n"
    "v_mfma_f32_16x16x32_bf16 v[160:163], v[100:103], v[24:27], v[160:163]\n"
    "v_max3_u32 v184, v104, v106, v184\n"
    "v_swap_b32 v105, v106\n"
    "s_nop 1\n"
    "v_mfma_f32_16x16x32_bf16 v[156:159], v[104:107], a[52:55], v[156:159]\n"
    "v_mfma_f32_16x16x32_bf16 v[160:163], v[104:107], v[28:31], v[160:163]\n"
    "v_max3_u32 v184, v128, v130, v184\n"
    "v_smfmac_f32_16x16x64_bf16 v[148:151], v[128:131], a[56:63], v195\n"
    "v_smfmac_f32_16x16x64_bf16 v[152:155], v[128:131], v[32:39], v195\n"
    "v_max3_u32 v184, v108, v110, v184\n"
    "v_swap_b32 v109, v110\n"
    "s_nop 1\n"
    "v_mfma_f32_16x16x32_bf16 v[156:159], v[108:111], a[56:59], v[156:159]\n"
    "v_mfma_f32_16x16x32_bf16 v[160:163], v[108:111], v[32:35], v[160:163]\n"
    "v_max3_u32 v184, v112, v114, v184\n"
    "v_swap_b32 v113, v114\n"
    "s_nop 1\n"
    "v_mfma_f32_16x16x32_bf16 v[156:159], v[112:115], a[60:63], v[156:159]\n"
    "v_mfma_f32_16x16x32_bf16 v[160:163], v[112:115], v[36:39], v[160:163]\n"
    "v_cmp_eq_u32 vcc, -1, v184\n"
    "s_nop 0\n"
    "s_cbranch_vccnz .Lct_tr1_%=\n"
    "s_nop 7\n"
    "s_branch .Lct_tg1_%=\n"
    :: "{s[0:3]}"(s0_), "{s[4:7]}"(s4_), "{s[8:11]}"(s8_), "{s[12:15]}"(s12_), "{s[16:19]}"(s16_), "{v[192:195]}"(v192_), "{v[196:199]}"(v196_), "{v[200:203]}"(v200_), "{v[204:207]}"(v204_), "{v[208:211]}"(v208_), "{v[212:215]}"(v212_) : "memory", "v255", "a255", "s95");
    __builtin_unreachable();
}
DEV void qw_role(const MegaArgs& A, const int L, const int h, const int rep) {
    ROLE_HEAD;
    const u32x4 s0_ = {RFL(rx[0]), RFL(rx[1]), RFL(rx[2]), RFL(rx[3])};
    const u32x4 s4_ = {RFL(lo(A.Wq + h * 192 + 64)), RFL(hi(A.Wq + h * 192 + 64)), RFL(WBYTES - h * 384 - 128), RFL(0x20000u)};
    const u32x4 s8_ = {RFL(lo(A.Wv + h * 192 + 128)), RFL(hi(A.Wv + h * 192 + 128)), RFL(WBYTES - h * 384 - 256), RFL(0x20000u)};
    const u32x4 s12_ = {RFL(w), RFL(REC0 + w * 2048), RFL(QT0 + h * 32768), RFL((A.layers & 2) ? 64 : 0)};
    const u32x4 s16_ = {RFL(xl(Z_CT + (h & 7) * 256, L)), RFL(xl(Z_XN + rep * 2048, 0)), RFL(DBG0 + ((L * 128 + 112 + (h >> 3) * 4 + (h & 3)) * 4 + w) * 2048), RFL(0u)};
    const u32x4 v0_ = {(unsigned)((8 * w + (l >> 3)) * 12288 + ((l & 7) ^ (((8 * w + (l >> 3)) >> 1) & 6)) * 16), (unsigned)((8 * w + (l >> 3)) * 12288 + ((l & 7) ^ (((8 * w + (l >> 3)) >> 1) & 6)) * 16 + 128), (unsigned)((8 * lq + ((l >> 2) & 1) + ((l >> 1) & 4)) * 128 + (w ^ (((8 * lq + ((l >> 2) & 1) + ((l >> 1) & 4)) >> 2) & 3)) * 32 + (l & 3) * 8), (unsigned)(0u)};
    const u32x4 v64_ = {(unsigned)(l * 16), (unsigned)(l * 16 + 40960), (unsigned)(l * 16 + w * 2048), (unsigned)(l * 16 + w * 1024 + 16384)};
    const u32x4 v68_ = {(unsigned)(l < 20 ? (l >> 2) * 128 + (l & 3) * 4 : 0), (unsigned)(l == 0 ? xl(Z_QW + h * 128 + w * 4, 0) : OOB), (unsigned)(0u), (unsigned)(0u)};
    asm volatile(
    "s_mov_b32 s20, 0\n"
    "s_lshl_b32 s32, s12, 10\n"
    "s_mov_b32 s38, 0\n"
    "s_add_u32 m0, s32, 0\n"
    "s_nop 0\n"
    "buffer_load_dwordx4 v0, s[4:7], s38 offen nt lds\n"
    "s_add_u32 m0, s32, 4096\n"
    "s_nop 0\n"
    "buffer_load_dwordx4 v1, s[4:7], s38 offen nt lds\n"
    "s_add_u32 m0, s32, 8192\n"
    "s_nop 0\n"
    "buffer_load_dwordx4 v0, s[8:11], s38 offen nt lds\n"
    "s_mov_b32 s38, 1572864\n"
    "s_add_u32 m0, s32, 12288\n"
    "s_nop 0\n"
    "buffer_load_dwordx4 v0, s[4:7], s38 offen nt lds\n"
    "s_add_u32 m0, s32, 16384\n"
    "s_nop 0\n"
    "buffer_load_dwordx4 v1, s[4:7], s38 offen nt lds\n"
    "s_add_u32 m0, s32, 20480\n"
    "s_nop 0\n"
    "buffer_load_dwordx4 v0, s[8:11], s38 offen nt lds\n"
    "s_mov_b32 s38, 3145728\n"
    "s_add_u32 m0, s32, 24576\n"
    "s_nop 0\n"
    "buffer_load_dwordx4 v0, s[4:7], s38 offen nt lds\n"
    "s_add_u32 m0, s32, 28672\n"
    "s_nop 0\n"
    "buffer_load_dwordx4 v1, s[4:7], s38 offen nt lds\n"
    "s_add_u32 m0, s32, 32768\n"
    "s_nop 0\n"
    "buffer_load_dwordx4 v0, s[8:11], s38 offen nt lds\n"
    "s_mov_b32 s38, 4718592\n"
    "s_add_u32 m0, s32, 36864\n"
    "s_nop 0\n"
    "buffer_load_dwordx4 v0, s[4:7], s38 offen nt lds\n"
    "s_add_u32 m0, s32, 40960\n"
    "s_nop 0\n"
    "buffer_load_dwordx4 v1, s[4:7], s38 offen nt lds\n"
    "s_add_u32 m0, s32, 45056\n"
    "s_nop 0\n"
    "buffer_load_dwordx4 v0, s[8:11], s38 offen nt lds\n"
    "s_mov_b32 s38, 393216\n"
    "s_add_u32 m0, s32, 49152\n"
    "s_nop 0\n"
    "buffer_load_dwordx4 v0, s[4:7], s38 offen nt lds\n"
    "s_add_u32 m0, s32, 53248\n"
    "s_nop 0\n"
    "buffer_load_dwordx4 v1, s[4:7], s38 offen nt lds\n"
    "s_add_u32 m0, s32, 57344\n"
    "s_nop 0\n"
    "buffer_load_dwordx4 v0, s[8:11], s38 offen nt lds\n"
    "s_mov_b32 s38, 1966080\n"
    "s_add_u32 m0, s32, 61440\n"
    "s_nop 0\n"
    "buffer_load_dwordx4 v0, s[4:7], s38 offen nt lds\n"
    "s_add_u32 m0, s32, 65536\n"
    "s_nop 0\n"
    "buffer_load_dwordx4 v1, s[4:7], s38 offen nt lds\n"
    "s_add_u32 m0, s32, 69632\n"
    "s_nop 0\n"
    "buffer_load_dwordx4 v0, s[8:11], s38 offen nt lds\n"
    "s_mov_b32 s38, 3538944\n"
    "s_add_u32 m0, s32, 73728\n"
    "s_nop 0\n"
    "buffer_load_dwordx4 v0, s[4:7], s38 offen nt lds\n"
    "s_add_u32 m0, s32, 77824\n"
    "s_nop 0\n"
    "buffer_load_dwordx4 v1, s[4:7], s38 offen nt lds\n"
    "s_add_u32 m0, s32, 81920\n"
    "s_nop 0\n"
    "buffer_load_dwordx4 v0, s[8:11], s38 offen nt lds\n"
    "s_mov_b32 s38, 5111808\n"
    "s_add_u32 m0, s32, 86016\n"
    "s_nop 0\n"
    "buffer_load_dwordx4 v0, s[4:7], s38 offen nt lds\n"
    "s_add_u32 m0, s32, 90112\n"
    "s_nop 0\n"
    "buffer_load_dwordx4 v1, s[4:7], s38 offen nt lds\n"
    "s_add_u32 m0, s32, 94208\n"
    "s_nop 0\n"
    "buffer_load_dwordx4 v0, s[8:11], s38 offen nt lds\n"
    "s_mov_b32 s38, 786432\n"
    "s_add_u32 m0, s32, 98304\n"
    "s_nop 0\n"
    "buffer_load_dwordx4 v0, s[4:7], s38 offen nt lds\n"
    "s_add_u32 m0, s32, 102400\n"
    "s_nop 0\n"
    "buffer_load_dwordx4 v1, s[4:7], s38 offen nt lds\n"
    "s_add_u32 m0, s32, 106496\n"
    "s_nop 0\n"
    "buffer_load_dwordx4 v0, s[8:11], s38 offen nt lds\n"
    "s_mov_b32 s38, 2359296\n"
    "s_add_u32 m0, s32, 110592\n"
    "s_nop 0\n"
    "buffer_load_dwordx4 v0, s[4:7], s38 offen nt lds\n"
    "s_add_u32 m0, s32, 114688\n"
    "s_nop 0\n"
    "buffer_load_dwordx4 v1, s[4:7], s38 offen nt lds\n"
    "s_add_u32 m0, s32, 118784\n"
    "s_nop 0\n"
    "buffer_load_dwordx4 v0, s[8:11], s38 offen nt lds\n"
    "s_mov_b32 s38, 3932160\n"
    "s_add_u32 m0, s32, 122880\n"
    "s_nop 0\n"
    "buffer_load_dwordx4 v0, s[4:7], s38 offen nt lds\n"
    "s_add_u32 m0, s32, 126976\n"
    "s_nop 0\n"
    "buffer_load_dwordx4 v1, s[4:7], s38 offen nt lds\n"
    "s_add_u32 m0, s32, 131072\n"
    "s_nop 0\n"
    "buffer_load_dwordx4 v0, s[8:11], s38 offen nt lds\n"
    "s_mov_b32 s38, 5505024\n"
    "s_add_u32 m0, s32, 135168\n"
    "s_nop 0\n"
    "buffer_load_dwordx4 v0, s[4:7], s38 offen nt lds\n"
    "s_add_u32 m0, s32, 139264\n"
    "s_nop 0\n"
    "buffer_load_dwordx4 v1, s[4:7], s38 offen nt lds\n"
    "s_add_u32 m0, s32, 143360\n"
    "s_nop 0\n"
    "buffer_load_dwordx4 v0, s[8:11], s38 offen nt lds\n"
    "s_waitcnt vmcnt(24)\n"
    "s_barrier\n"
    "v_add_u32 v8, 0, v2\n"
    "ds_read_b64_tr_b16 a[0:1], v8 offset:0\n"
    "ds_read_b64_tr_b16 a[2:3], v8 offset:256\n"
    "ds_read_b64_tr_b16 a[80:81], v8 offset:4096\n"
    "ds_read_b64_tr_b16 a[82:83], v8 offset:4352\n"
    "ds_read_b64_tr_b16 a[160:161], v8 offset:8192\n"
    "ds_read_b64_tr_b16 a[162:163], v8 offset:8448\n"
    "ds_read_b64_tr_b16 a[16:17], v8 offset:12288\n"
    "ds_read_b64_tr_b16 a[18:19], v8 offset:12544\n"
    "ds_read_b64_tr_b16 a[96:97], v8 offset:16384\n"
    "ds_read_b64_tr_b16 a[98:99], v8 offset:16640\n"
    "ds_read_b64_tr_b16 a[176:177], v8 offset:20480\n"
    "ds_read_b64_tr_b16 a[178:179], v8 offset:20736\n"
    "ds_read_b64_tr_b16 a[32:33], v8 offset:24576\n"
    "ds_read_b64_tr_b16 a[34:35], v8 offset:24832\n"
    "ds_read_b64_tr_b16 a[112:113], v8 offset:28672\n"
    "ds_read_b64_tr_b16 a[114:115], v8 offset:28928\n"
    "ds_read_b64_tr_b16 a[192:193], v8 offset:32768\n"
    "ds_read_b64_tr_b16 a[194:195], v8 offset:33024\n"
    "ds_read_b64_tr_b16 a[48:49], v8 offset:36864\n"
    "ds_read_b64_tr_b16 a[50:51], v8 offset:37120\n"
    "ds_read_b64_tr_b16 a[128:129], v8 offset:40960\n"
    "ds_read_b64_tr_b16 a[130:131], v8 offset:41216\n"
    "ds_read_b64_tr_b16 a[208:209], v8 offset:45056\n"
    "ds_read_b64_tr_b16 a[210:211], v8 offset:45312\n"
    "s_waitcnt lgkmcnt(0)\n"
    "s_barrier\n"
    "s_mov_b32 s38, 1179648\n"
    "s_add_u32 m0, s32, 0\n"
    "s_nop 0\n"
    "buffer_load_dwordx4 v0, s[4:7], s38 offen nt lds\n"
    "s_add_u32 m0, s32, 4096\n"
    "s_nop 0\n"
    "buffer_load_dwordx4 v1, s[4:7], s38 offen nt lds\n"
    "s_add_u32 m0, s32, 8192\n"
    "s_nop 0\n"
    "buffer_load_dwordx4 v0, s[8:11], s38 offen nt lds\n"
    "s_mov_b32 s38, 2752512\n"
    "s_add_u32 m0, s32, 12288\n"
    "s_nop 0\n"
    "buffer_load_dwordx4 v0, s[4:7], s38 offen nt lds\n"
    "s_add_u32 m0, s32, 16384\n"
    "s_nop 0\n"
    "buffer_load_dwordx4 v1, s[4:7], s38 offen nt lds\n"
    "s_add_u32 m0, s32, 20480\n"
    "s_nop 0\n"
    "buffer_load_dwordx4 v0, s[8:11], s38 offen nt lds\n"
    "s_mov_b32 s38, 4325376\n"
    "s_add_u32 m0, s32, 24576\n"
    "s_nop 0\n"
    "buffer_load_dwordx4 v0, s[4:7], s38 offen nt lds\n"
    "s_add_u32 m0, s32, 28672\n"
    "s_nop 0\n"
    "buffer_load_dwordx4 v1, s[4:7], s38 offen nt lds\n"
    "s_add_u32 m0, s32, 32768\n"
    "s_nop 0\n"
    "buffer_load_dwordx4 v0, s[8:11], s38 offen nt lds\n"
    "s_mov_b32 s38, 5898240\n"
    "s_add_u32 m0, s32, 36864\n"
    "s_nop 0\n"
    "buffer_load_dwordx4 v0, s[4:7], s38 offen nt lds\n"
    "s_add_u32 m0, s32, 40960\n"
    "s_nop 0\n"
    "buffer_load_dwordx4 v1, s[4:7], s38 offen nt lds\n"
    "s_add_u32 m0, s32, 45056\n"
    "s_nop 0\n"
    "buffer_load_dwordx4 v0, s[8:11], s38 offen nt lds\n"
    "s_waitcnt vmcnt(24)\n"
    "s_barrier\n"
    "v_add_u32 v8, 49152, v2\n"
    "ds_read_b64_tr_b16 a[4:5], v8 offset:0\n"
    "ds_read_b64_tr_b16 a[6:7], v8 offset:256\n"
    "ds_read_b64_tr_b16 a[84:85], v8 offset:4096\n"
    "ds_read_b64_tr_b16 a[86:87], v8 offset:4352\n"
    "ds_read_b64_tr_b16 a[164:165], v8 offset:8192\n"
    "ds_read_b64_tr_b16 a[166:167], v8 offset:8448\n"
    "ds_read_b64_tr_b16 a[20:21], v8 offset:12288\n"
    "ds_read_b64_tr_b16 a[22:23], v8 offset:12544\n"
    "ds_read_b64_tr_b16 a[100:101], v8 offset:16384\n"
    "ds_read_b64_tr_b16 a[102:103], v8 offset:16640\n"
    "ds_read_b64_tr_b16 a[180:181], v8 offset:20480\n"
    "ds_read_b64_tr_b16 a[182:183], v8 offset:20736\n"
    "ds_read_b64_tr_b16 a[36:37], v8 offset:24576\n"
    "ds_read_b64_tr_b16 a[38:39], v8 offset:24832\n"
    "ds_read_b64_tr_b16 a[116:117], v8 offset:28672\n"
    "ds_read_b64_tr_b16 a[118:119], v8 offset:28928\n"
    "ds_read_b64_tr_b16 a[196:197], v8 offset:32768\n"
    "ds_read_b64_tr_b16 a[198:199], v8 offset:33024\n"
    "ds_read_b64_tr_b16 a[52:53], v8 offset:36864\n"
    "ds_read_b64_tr_b16 a[54:55], v8 offset:37120\n"
    "ds_read_b64_tr_b16 a[132:133], v8 offset:40960\n"
    "ds_read_b64_tr_b16 a[134:135], v8 offset:41216\n"
    "ds_read_b64_tr_b16 a[212:213], v8 offset:45056\n"
    "ds_read_b64_tr_b16 a[214:215], v8 offset:45312\n"
    "s_waitcnt lgkmcnt(0)\n"
    "s_barrier\n"
    "s_mov_b32 s38, 6291456\n"
    "s_add_u32 m0, s32, 49152\n"
    "s_nop 0\n"
    "buffer_load_dwordx4 v0, s[4:7], s38 offen nt lds\n"
    "s_add_u32 m0, s32, 53248\n"
    "s_nop 0\n"
    "buffer_load_dwordx4 v1, s[4:7], s38 offen nt lds\n"
    "s_add_u32 m0, s32, 57344\n"
    "s_nop 0\n"
    "buffer_load_dwordx4 v0, s[8:11], s38 offen nt lds\n"
    "s_mov_b32 s38, 6684672\n"
    "s_add_u32 m0, s32, 61440\n"
    "s_nop 0\n"
    "buffer_load_dwordx4 v0, s[4:7], s38 offen nt lds\n"
    "s_add_u32 m0, s32, 65536\n"
    "s_nop 0\n"
    "buffer_load_dwordx4 v1, s[4:7], s38 offen nt lds\n"
    "s_add_u32 m0, s32, 69632\n"
    "s_nop 0\n"
    "buffer_load_dwordx4 v0, s[8:11], s38 offen nt lds\n"
    "s_mov_b32 s38, 7077888\n"
    "s_add_u32 m0, s32, 73728\n"
    "s_nop 0\n"
    "buffer_load_dwordx4 v0, s[4:7], s38 offen nt lds\n"
    "s_add_u32 m0, s32, 77824\n"
    "s_nop 0\n"
    "buffer_load_dwordx4 v1, s[4:7], s38 offen nt lds\n"
    "s_add_u32 m0, s32, 81920\n"
    "s_nop 0\n"
    "buffer_load_dwordx4 v0, s[8:11], s38 offen nt lds\n"
    "s_mov_b32 s38, 7471104\n"
    "s_add_u32 m0, s32, 86016\n"
    "s_nop 0\n"
    "buffer_load_dwordx4 v0, s[4:7], s38 offen nt lds\n"
    "s_add_u32 m0, s32, 90112\n"
    "s_nop 0\n"
    "buffer_load_dwordx4 v1, s[4:7], s38 offen nt lds\n"
    "s_add_u32 m0, s32, 94208\n"
    "s_nop 0\n"
    "buffer_load_dwordx4 v0, s[8:11], s38 offen nt lds\n"
    "s_waitcnt vmcnt(24)\n"
    "s_barrier\n"
    "v_add_u32 v8, 98304, v2\n"
    "ds_read_b64_tr_b16 a[8:9], v8 offset:0\n"
    "ds_read_b64_tr_b16 a[10:11], v8 offset:256\n"
    "ds_read_b64_tr_b16 a[88:89], v8 offset:4096\n"
    "ds_read_b64_tr_b16 a[90:91], v8 offset:4352\n"
    "ds_read_b64_tr_b16 a[168:169], v8 offset:8192\n"
    "ds_read_b64_tr_b16 a[170:171], v8 offset:8448\n"
    "ds_read_b64_tr_b16 a[24:25], v8 offset:12288\n"
    "ds_read_b64_tr_b16 a[26:27], v8 offset:12544\n"
    "ds_read_b64_tr_b16 a[104:105], v8 offset:16384\n"
    "ds_read_b64_tr_b16 a[106:107], v8 offset:16640\n"
    "ds_read_b64_tr_b16 a[184:185], v8 offset:20480\n"
    "ds_read_b64_tr_b16 a[186:187], v8 offset:20736\n"
    "ds_read_b64_tr_b16 a[40:41], v8 offset:24576\n"
    "ds_read_b64_tr_b16 a[42:43], v8 offset:24832\n"
    "ds_read_b64_tr_b16 a[120:121], v8 offset:28672\n"
    "ds_read_b64_tr_b16 a[122:123], v8 offset:28928\n"
    "ds_read_b64_tr_b16 a[200:201], v8 offset:32768\n"
    "ds_read_b64_tr_b16 a[202:203], v8 offset:33024\n"
    "ds_read_b64_tr_b16 a[56:57], v8 offset:36864\n"
    "ds_read_b64_tr_b16 a[58:59], v8 offset:37120\n"
    "ds_read_b64_tr_b16 a[136:137], v8 offset:40960\n"
    "ds_read_b64_tr_b16 a[138:139], v8 offset:41216\n"
    "ds_read_b64_tr_b16 a[216:217], v8 offset:45056\n"
    "ds_read_b64_tr_b16 a[218:219], v8 offset:45312\n"
    "s_waitcnt lgkmcnt(0)\n"
    "s_barrier\n"
    "s_waitcnt vmcnt(12)\n"
    "s_barrier\n"
    "v_add_u32 v8, 0, v2\n"
    "ds_read_b64_tr_b16 a[12:13], v8 offset:0\n"
    "ds_read_b64_tr_b16 a[14:15], v8 offset:256\n"
    "ds_read_b64_tr_b16 a[92:93], v8 offset:4096\n"
    "ds_read_b64_tr_b16 a[94:95], v8 offset:4352\n"
    "ds_read_b64_tr_b16 a[172:173], v8 offset:8192\n"
    "ds_read_b64_tr_b16 a[174:175], v8 offset:8448\n"
    "ds_read_b64_tr_b16 a[28:29], v8 offset:12288\n"
    "ds_read_b64_tr_b16 a[30:31], v8 offset:12544\n"
    "ds_read_b64_tr_b16 a[108:109], v8 offset:16384\n"
    "ds_read_b64_tr_b16 a[110:111], v8 offset:16640\n"
    "ds_read_b64_tr_b16 a[188:189], v8 offset:20480\n"
    "ds_read_b64_tr_b16 a[190:191], v8 offset:20736\n"
    "ds_read_b64_tr_b16 a[44:45], v8 offset:24576\n"
    "ds_read_b64_tr_b16 a[46:47], v8 offset:24832\n"
    "ds_read_b64_tr_b16 a[124:125], v8 offset:28672\n"
    "ds_read_b64_tr_b16 a[126:127], v8 offset:28928\n"
    "ds_read_b64_tr_b16 a[204:205], v8 offset:32768\n"
    "ds_read_b64_tr_b16 a[206:207], v8 offset:33024\n"
    "ds_read_b64_tr_b16 a[60:61], v8 offset:36864\n"
    "ds_read_b64_tr_b16 a[62:63], v8 offset:37120\n"
    "ds_read_b64_tr_b16 a[140:141], v8 offset:40960\n"
    "ds_read_b64_tr_b16 a[142:143], v8 offset:41216\n"
    "ds_read_b64_tr_b16 a[220:221], v8 offset:45056\n"
    "ds_read_b64_tr_b16 a[222:223], v8 offset:45312\n"
    "s_waitcnt lgkmcnt(0)\n"
    "s_barrier\n"
    "s_waitcnt vmcnt(0)\n"
    "s_barrier\n"
    "v_add_u32 v8, 49152, v2\n"
    "ds_read_b64_tr_b16 a[64:65], v8 offset:0\n"
    "ds_read_b64_tr_b16 a[66:67], v8 offset:256\n"
    "ds_read_b64_tr_b16 a[144:145], v8 offset:4096\n"
    "ds_read_b64_tr_b16 a[146:147], v8 offset:4352\n"
    "ds_read_b64_tr_b16 a[224:225], v8 offset:8192\n"
    "ds_read_b64_tr_b16 a[226:227], v8 offset:8448\n"
    "ds_read_b64_tr_b16 a[68:69], v8 offset:12288\n"
    "ds_read_b64_tr_b16 a[70:71], v8 offset:12544\n"
    "ds_read_b64_tr_b16 a[148:149], v8 offset:16384\n"
    "ds_read_b64_tr_b16 a[150:151], v8 offset:16640\n"
    "ds_read_b64_tr_b16 a[228:229], v8 offset:20480\n"
    "ds_read_b64_tr_b16 a[230:231], v8 offset:20736\n"
    "ds_read_b64_tr_b16 a[72:73], v8 offset:24576\n"
    "ds_read_b64_tr_b16 a[74:75], v8 offset:24832\n"
    "ds_read_b64_tr_b16 a[152:153], v8 offset:28672\n"
    "ds_read_b64_tr_b16 a[154:155], v8 offset:28928\n"
    "ds_read_b64_tr_b16 a[232:233], v8 offset:32768\n"
    "ds_read_b64_tr_b16 a[234:235], v8 offset:33024\n"
    "ds_read_b64_tr_b16 a[76:77], v8 offset:36864\n"
    "ds_read_b64_tr_b16 a[78:79], v8 offset:37120\n"
    "ds_read_b64_tr_b16 a[156:157], v8 offset:40960\n"
    "ds_read_b64_tr_b16 a[158:159], v8 offset:41216\n"
    "ds_read_b64_tr_b16 a[236:237], v8 offset:45056\n"
    "ds_read_b64_tr_b16 a[238:239], v8 offset:45312\n"
    "s_waitcnt lgkmcnt(0)\n"
    "s_barrier\n"
    "s_mov_b32 s26, -2\n"
    "v_lshrrev_b32 v4, 2, v64\n"
    ".Lqw_po1_%=:\n"
    "buffer_load_dword v8, v4, s[0:3], s16 offen sc1\n"
    "s_waitcnt vmcnt(0)\n"
    "v_cmp_gt_i32 vcc, 1, v8\n"
    "s_nop 0\n"
    "s_cbranch_vccz .Lqw_pd2_%=\n"
    "s_add_u32 s20, s20, 1\n"
    "s_cmp_lt_u32 s20, 0x40000\n"
    "s_cbranch_scc1 .Lqw_sp3_%=\n"
    "v_mov_b32 v8, 50\n"
    "s_mov_b32 s24, 33728\n"
    "buffer_store_dword v8, off, s[0:3], s24 sc1\n"
    "s_waitcnt vmcnt(0)\n"
    "s_branch .Lqw_pd2_%=\n"
    ".Lqw_sp3_%=:\n"
    "s_sleep 4\n"
    "s_branch .Lqw_po1_%=\n"
    ".Lqw_pd2_%=:\n"
    "s_mov_b32 s26, -1\n"
    "s_mov_b32 s35, 0\n"
    "s_lshl_b32 s37, s12, 11\n"
    "s_mov_b32 s45, 0\n"
    "v_mov_b32 v60, 0\n"
    "s_mov_b32 s43, 2130706432\n"
    "s_mov_b32 s41, 2130706432\n"
    "s_mov_b32 s46, 2130706432\n"
    "s_mov_b32 s34, 2130706432\n"
    "s_add_u32 s26, s26, 1\n"
    "s_sub_u32 s27, s26, 14\n"
    "s_ashr_i32 s28, s27, 1\n"
    "s_and_b32 s29, s27, 1\n"
    "s_add_u32 s30, s28, 14\n"
    "s_cmp_lt_i32 s27, 0\n"
    "s_cselect_b32 s29, 0, s29\n"
    "s_cselect_b32 s30, s26, s30\n"
    "s_cmp_eq_u32 s29, 1\n"
    "s_cselect_b32 s30, s28, s30\n"
    "s_cselect_b32 s27, s15, 64\n"
    "s_lshl_b32 s31, s29, 13\n"
    "s_add_u32 s40, s30, 1\n"
    "s_lshl_b32 s32, s30, 19\n"
    "s_lshl_b32 s25, s30, 11\n"
    "s_xor_b32 s25, s25, s31\n"
    "s_and_b32 s25, s25, 0x2000\n"
    "s_add_u32 s32, s32, s25\n"
    "s_add_u32 s32, s32, s13\n"
    "s_lshl_b32 s33, s30, 20\n"
    "s_lshl_b32 s25, s30, 12\n"
    "s_xor_b32 s25, s25, s31\n"
    "s_and_b32 s25, s25, 0x2000\n"
    "s_add_u32 s33, s33, s25\n"
    "s_add_u32 s33, s33, s14\n"
    "s_mul_i32 s47, s40, s29\n"
    "s_cmp_lt_u32 s30, s27\n"
    "s_cselect_b32 s36, 1, 0\n"
    "s_cselect_b32 s32, s32, 2130706432\n"
    "s_cmp_lt_u32 s30, s27\n"
    "s_cselect_b32 s39, s31, 2130706432\n"
    "s_cselect_b32 s47, s47, 0\n"
    "s_cmp_gt_u32 s47, 0\n"
    "s_cselect_b32 s38, s17, 2130706432\n"
    ".Lqw_top_%=:\n"
    "ds_read_b128 v[24:27], v65 offset:0\n"
    "ds_read_b128 v[28:31], v65 offset:1024\n"
    "ds_read_b128 v[32:35], v65 offset:2048\n"
    "ds_read_b128 v[36:39], v65 offset:3072\n"
    "ds_read_b128 v[40:43], v65 offset:4096\n"
    "ds_read_b128 v[44:47], v65 offset:5120\n"
    "v_cmp_gt_i32 vcc, s47, v60\n"
    "s_nop 0\n"
    "s_cbranch_vccnz .Lqw_wx_%=\n"
    ".Lqw_wxb_%=:\n"
    "s_cmp_eq_u32 s35, 0\n"
    "s_cbranch_scc1 .Lqw_skip_%=\n"
    ".Lqw_mk0x3_%=:\n"
    "s_waitcnt lgkmcnt(4)\n"
    "v_mfma_f32_16x16x32_bf16 v[0:3], a[0:3], v[24:27], 0\n"
    "buffer_store_dwordx4 v[48:51], v66, s[0:3], s46 offen offset:0 sc1\n"
    "v_mfma_f32_16x16x32_bf16 v[4:7], a[0:3], v[28:31], 0\n"
    "v_mfma_f32_16x16x32_bf16 v[8:11], a[80:83], v[24:27], 0\n"
    "v_mfma_f32_16x16x32_bf16 v[12:15], a[80:83], v[28:31], 0\n"
    "v_mfma_f32_16x16x32_bf16 v[16:19], a[160:163], v[24:27], 0\n"
    "ds_read_b128 v[24:27], v65 offset:6144\n"
    "v_mfma_f32_16x16x32_bf16 v[20:23], a[160:163], v[28:31], 0\n"
    "ds_read_b128 v[28:31], v65 offset:7168\n"
    "s_waitcnt lgkmcnt(4)\n"
    "v_mfma_f32_16x16x32_bf16 v[0:3], a[4:7], v[32:35], v[0:3]\n"
    "v_mfma_f32_16x16x32_bf16 v[4:7], a[4:7], v[36:39], v[4:7]\n"
    "buffer_store_dwordx4 v[52:55], v66, s[0:3], s46 offen offset:1024 sc1\n"
    "v_mfma_f32_16x16x32_bf16 v[8:11], a[84:87], v[32:35], v[8:11]\n"
    "v_mfma_f32_16x16x32_bf16 v[12:15], a[84:87], v[36:39], v[12:15]\n"
    "v_mfma_f32_16x16x32_bf16 v[16:19], a[164:167], v[32:35], v[16:19]\n"
    "ds_read_b128 v[32:35], v65 offset:8192\n"
    "v_mfma_f32_16x16x32_bf16 v[20:23], a[164:167], v[36:39], v[20:23]\n"
    "ds_read_b128 v[36:39], v65 offset:9216\n"
    "s_waitcnt lgkmcnt(4)\n"
    "v_mfma_f32_16x16x32_bf16 v[0:3], a[8:11], v[40:43], v[0:3]\n"
    "v_mfma_f32_16x16x32_bf16 v[4:7], a[8:11], v[44:47], v[4:7]\n"
    "v_mfma_f32_16x16x32_bf16 v[8:11], a[88:91], v[40:43], v[8:11]\n"
    "buffer_store_dwordx4 v[56:59], v67, s[0:3], s46 offen sc1\n"
    "s_add_u32 m0, s37, 0\n"
    "v_mfma_f32_16x16x32_bf16 v[12:15], a[88:91], v[44:47], v[12:15]\n"
    "v_mfma_f32_16x16x32_bf16 v[16:19], a[168:171], v[40:43], v[16:19]\n"
    "ds_read_b128 v[40:43], v65 offset:10240\n"
    "v_mfma_f32_16x16x32_bf16 v[20:23], a[168:171], v[44:47], v[20:23]\n"
    "ds_read_b128 v[44:47], v65 offset:11264\n"
    "s_waitcnt lgkmcnt(4)\n"
    "v_mfma_f32_16x16x32_bf16 v[0:3], a[12:15], v[24:27], v[0:3]\n"
    "v_mfma_f32_16x16x32_bf16 v[4:7], a[12:15], v[28:31], v[4:7]\n"
    "v_mfma_f32_16x16x32_bf16 v[8:11], a[92:95], v[24:27], v[8:11]\n"
    "v_mfma_f32_16x16x32_bf16 v[12:15], a[92:95], v[28:31], v[12:15]\n"
    "buffer_load_dwordx4 v64, s[0:3], s32 offen sc1 lds\n"
    "v_mfma_f32_16x16x32_bf16 v[16:19], a[172:175], v[24:27], v[16:19]\n"
    "ds_read_b128 v[24:27], v65 offset:12288\n"
    "v_mfma_f32_16x16x32_bf16 v[20:23], a[172:175], v[28:31], v[20:23]\n"
    "ds_read_b128 v[28:31], v65 offset:13312\n"
    "s_waitcnt lgkmcnt(4)\n"
    "v_mfma_f32_16x16x32_bf16 v[0:3], a[16:19], v[32:35], v[0:3]\n"
    "v_mfma_f32_16x16x32_bf16 v[4:7], a[16:19], v[36:39], v[4:7]\n"
    "buffer_load_dwordx4 v64, s[0:3], s32 offen offset:1024 sc1 lds\n"
    "v_mfma_f32_16x16x32_bf16 v[8:11], a[96:99], v[32:35], v[8:11]\n"
    "s_add_u32 m0, s37, 8192\n"
    "s_add_u32 s32, s32, 0x4000\n"
    "v_mfma_f32_16x16x32_bf16 v[12:15], a[96:99], v[36:39], v[12:15]\n"
    "v_mfma_f32_16x16x32_bf16 v[16:19], a[176:179], v[32:35], v[16:19]\n"
    "ds_read_b128 v[32:35], v65 offset:14336\n"
    "v_mfma_f32_16x16x32_bf16 v[20:23], a[176:179], v[36:39], v[20:23]\n"
    "ds_read_b128 v[36:39], v65 offset:15360\n"
    "s_waitcnt lgkmcnt(4)\n"
    "v_mfma_f32_16x16x32_bf16 v[0:3], a[20:23], v[40:43], v[0:3]\n"
    "buffer_load_dwordx4 v64, s[0:3], s32 offen sc1 lds\n"
    "v_mfma_f32_16x16x32_bf16 v[4:7], a[20:23], v[44:47], v[4:7]\n"
    "v_mfma_f32_16x16x32_bf16 v[8:11], a[100:103], v[40:43], v[8:11]\n"
    "v_mfma_f32_16x16x32_bf16 v[12:15], a[100:103], v[44:47], v[12:15]\n"
    "v_mfma_f32_16x16x32_bf16 v[16:19], a[180:183], v[40:43], v[16:19]\n"
    "ds_read_b128 v[40:43], v65 offset:16384\n"
    "v_mfma_f32_16x16x32_bf16 v[20:23], a[180:183], v[44:47], v[20:23]\n"
    "ds_read_b128 v[44:47], v65 offset:17408\n"
    "s_waitcnt lgkmcnt(4)\n"
    "v_mfma_f32_16x16x32_bf16 v[0:3], a[24:27], v[24:27], v[0:3]\n"
    "buffer_load_dwordx4 v64, s[0:3], s32 offen offset:1024 sc1 lds\n"
    "v_mfma_f32_16x16x32_bf16 v[4:7], a[24:27], v[28:31], v[4:7]\n"
    "s_add_u32 m0, s37, 16384\n"
    "s_add_u32 s32, s32, 0x4000\n"
    "v_mfma_f32_16x16x32_bf16 v[8:11], a[104:107], v[24:27], v[8:11]\n"
    "v_mfma_f32_16x16x32_bf16 v[12:15], a[104:107], v[28:31], v[12:15]\n"
    "v_mfma_f32_16x16x32_bf16 v[16:19], a[184:187], v[24:27], v[16:19]\n"
    "ds_read_b128 v[24:27], v65 offset:18432\n"
    "v_mfma_f32_16x16x32_bf16 v[20:23], a[184:187], v[28:31], v[20:23]\n"
    "ds_read_b128 v[28:31], v65 offset:19456\n"
    "s_waitcnt lgkmcnt(4)\n"
    "v_mfma_f32_16x16x32_bf16 v[0:3], a[28:31], v[32:35], v[0:3]\n"
    "buffer_load_dwordx4 v64, s[0:3], s32 offen sc1 lds\n"
    "v_mfma_f32_16x16x32_bf16 v[4:7], a[28:31], v[36:39], v[4:7]\n"
    "v_mfma_f32_16x16x32_bf16 v[8:11], a[108:111], v[32:35], v[8:11]\n"
    "v_mfma_f32_16x16x32_bf16 v[12:15], a[108:111], v[36:39], v[12:15]\n"
    "v_mfma_f32_16x16x32_bf16 v[16:19], a[188:191], v[32:35], v[16:19]\n"
    "ds_read_b128 v[32:35], v65 offset:20480\n"
    "v_mfma_f32_16x16x32_bf16 v[20:23], a[188:191], v[36:39], v[20:23]\n"
    "ds_read_b128 v[36:39], v65 offset:21504\n"
    "s_waitcnt lgkmcnt(4)\n"
    "v_mfma_f32_16x16x32_bf16 v[0:3], a[32:35], v[40:43], v[0:3]\n"
    "buffer_load_dwordx4 v64, s[0:3], s32 offen offset:1024 sc1 lds\n"
    "v_mfma_f32_16x16x32_bf16 v[4:7], a[32:35], v[44:47], v[4:7]\n"
    "s_add_u32 m0, s37, 24576\n"
    "s_add_u32 s32, s32, 0x4000\n"
    "v_mfma_f32_16x16x32_bf16 v[8:11], a[112:115], v[40:43], v[8:11]\n"
    "v_mfma_f32_16x16x32_bf16 v[12:15], a[112:115], v[44:47], v[12:15]\n"
    "v_mfma_f32_16x16x32_bf16 v[16:19], a[192:195], v[40:43], v[16:19]\n"
    "ds_read_b128 v[40:43], v65 offset:22528\n"
    "v_mfma_f32_16x16x32_bf16 v[20:23], a[192:195], v[44:47], v[20:23]\n"
    "ds_read_b128 v[44:47], v65 offset:23552\n"
    "s_waitcnt lgkmcnt(4)\n"
    "v_mfma_f32_16x16x32_bf16 v[0:3], a[36:39], v[24:27], v[0:3]\n"
    "buffer_load_dwordx4 v64, s[0:3], s32 offen sc1 lds\n"
    "v_mfma_f32_16x16x32_bf16 v[4:7], a[36:39], v[28:31], v[4:7]\n"
    "v_mfma_f32_16x16x32_bf16 v[8:11], a[116:119], v[24:27], v[8:11]\n"
    "v_mfma_f32_16x16x32_bf16 v[12:15], a[116:119], v[28:31], v[12:15]\n"
    "v_mfma_f32_16x16x32_bf16 v[16:19], a[196:199], v[24:27], v[16:19]\n"
    "ds_read_b128 v[24:27], v65 offset:24576\n"
    "v_mfma_f32_16x16x32_bf16 v[20:23], a[196:199], v[28:31], v[20:23]\n"
    "ds_read_b128 v[28:31], v65 offset:25600\n"
    "s_waitcnt lgkmcnt(4)\n"
    "v_mfma_f32_16x16x32_bf16 v[0:3], a[40:43], v[32:35], v[0:3]\n"
    "buffer_load_dwordx4 v64, s[0:3], s32 offen offset:1024 sc1 lds\n"
    "v_mfma_f32_16x16x32_bf16 v[4:7], a[40:43], v[36:39], v[4:7]\n"
    "s_add_u32 m0, s37, 32768\n"
    "s_add_u32 s32, s32, 0x4000\n"
    "v_mfma_f32_16x16x32_bf16 v[8:11], a[120:123], v[32:35], v[8:11]\n"
    "v_mfma_f32_16x16x32_bf16 v[12:15], a[120:123], v[36:39], v[12:15]\n"
    "v_mfma_f32_16x16x32_bf16 v[16:19], a[200:203], v[32:35], v[16:19]\n"
    "ds_read_b128 v[32:35], v65 offset:26624\n"
    "v_mfma_f32_16x16x32_bf16 v[20:23], a[200:203], v[36:39], v[20:23]\n"
    "ds_read_b128 v[36:39], v65 offset:27648\n"
    "s_waitcnt lgkmcnt(4)\n"
    "v_mfma_f32_16x16x32_bf16 v[0:3], a[44:47], v[40:43], v[0:3]\n"
    "buffer_load_dwordx4 v64, s[0:3], s32 offen sc1 lds\n"
    "v_mfma_f32_16x16x32_bf16 v[4:7], a[44:47], v[44:47], v[4:7]\n"
    "v_mfma_f32_16x16x32_bf16 v[8:11], a[124:127], v[40:43], v[8:11]\n"
    "v_mfma_f32_16x16x32_bf16 v[12:15], a[124:127], v[44:47], v[12:15]\n"
    "v_mfma_f32_16x16x32_bf16 v[16:19], a[204:207], v[40:43], v[16:19]\n"
    "ds_read_b128 v[40:43], v65 offset:28672\n"
    "v_mfma_f32_16x16x32_bf16 v[20:23], a[204:207], v[44:47], v[20:23]\n"
    "ds_read_b128 v[44:47], v65 offset:29696\n"
    "s_waitcnt lgkmcnt(4)\n"
    "v_mfma_f32_16x16x32_bf16 v[0:3], a[48:51], v[24:27], v[0:3]\n"
    "buffer_load_dwordx4 v64, s[0:3], s32 offen offset:1024 sc1 lds\n"
    "v_mfma_f32_16x16x32_bf16 v[4:7], a[48:51], v[28:31], v[4:7]\n"
    "s_waitcnt vmcnt(10)\n"
    "v_mov_b32 v61, s44\n"
    "v_mfma_f32_16x16x32_bf16 v[8:11], a[128:131], v[24:27], v[8:11]\n"
    "v_mfma_f32_16x16x32_bf16 v[12:15], a[128:131], v[28:31], v[12:15]\n"
    "v_mfma_f32_16x16x32_bf16 v[16:19], a[208:211], v[24:27], v[16:19]\n"
    "buffer_store_dword v61, v69, s[0:3], s43 offen sc1\n"
    "ds_read_b128 v[24:27], v65 offset:30720\n"
    "v_mfma_f32_16x16x32_bf16 v[20:23], a[208:211], v[28:31], v[20:23]\n"
    "s_mov_b32 s46, s34\n"
    "ds_read_b128 v[28:31], v65 offset:31744\n"
    "s_waitcnt lgkmcnt(4)\n"
    "v_mfma_f32_16x16x32_bf16 v[0:3], a[52:55], v[32:35], v[0:3]\n"
    "s_mov_b32 s43, s41\n"
    "s_mov_b32 s44, s42\n"
    "s_mov_b32 s41, s39\n"
    "v_mfma_f32_16x16x32_bf16 v[4:7], a[52:55], v[36:39], v[4:7]\n"
    "s_mov_b32 s42, s40\n"
    "s_mov_b32 s34, s33\n"
    "s_mov_b32 s35, s36\n"
    "v_mfma_f32_16x16x32_bf16 v[8:11], a[132:135], v[32:35], v[8:11]\n"
    "s_xor_b32 s37, s37, 40960\n"
    "s_add_u32 s26, s26, 1\n"
    "s_sub_u32 s27, s26, 14\n"
    "v_mfma_f32_16x16x32_bf16 v[12:15], a[132:135], v[36:39], v[12:15]\n"
    "s_ashr_i32 s28, s27, 1\n"
    "s_and_b32 s29, s27, 1\n"
    "s_add_u32 s30, s28, 14\n"
    "v_mfma_f32_16x16x32_bf16 v[16:19], a[212:215], v[32:35], v[16:19]\n"
    "ds_read_b128 v[32:35], v65 offset:32768\n"
    "v_mfma_f32_16x16x32_bf16 v[20:23], a[212:215], v[36:39], v[20:23]\n"
    "ds_read_b128 v[36:39], v65 offset:33792\n"
    "s_waitcnt lgkmcnt(4)\n"
    "v_mfma_f32_16x16x32_bf16 v[0:3], a[56:59], v[40:43], v[0:3]\n"
    "s_cmp_lt_i32 s27, 0\n"
    "s_cselect_b32 s29, 0, s29\n"
    "s_cselect_b32 s30, s26, s30\n"
    "v_mfma_f32_16x16x32_bf16 v[4:7], a[56:59], v[44:47], v[4:7]\n"
    "s_cmp_eq_u32 s29, 1\n"
    "s_cselect_b32 s30, s28, s30\n"
    "s_cselect_b32 s27, s15, 64\n"
    "v_mfma_f32_16x16x32_bf16 v[8:11], a[136:139], v[40:43], v[8:11]\n"
    "s_lshl_b32 s31, s29, 13\n"
    "s_add_u32 s40, s30, 1\n"
    "s_lshl_b32 s32, s30, 19\n"
    "v_mfma_f32_16x16x32_bf16 v[12:15], a[136:139], v[44:47], v[12:15]\n"
    "s_lshl_b32 s25, s30, 11\n"
    "s_xor_b32 s25, s25, s31\n"
    "s_and_b32 s25, s25, 0x2000\n"
    "v_mfma_f32_16x16x32_bf16 v[16:19], a[216:219], v[40:43], v[16:19]\n"
    "ds_read_b128 v[40:43], v65 offset:34816\n"
    "s_add_u32 s32, s32, s25\n"
    "v_mfma_f32_16x16x32_bf16 v[20:23], a[216:219], v[44:47], v[20:23]\n"
    "s_add_u32 s32, s32, s13\n"
    "ds_read_b128 v[44:47], v65 offset:35840\n"
    "s_waitcnt lgkmcnt(4)\n"
    "v_mfma_f32_16x16x32_bf16 v[0:3], a[60:63], v[24:27], v[0:3]\n"
    "s_lshl_b32 s33, s30, 20\n"
    "s_lshl_b32 s25, s30, 12\n"
    "s_xor_b32 s25, s25, s31\n"
    "v_mfma_f32_16x16x32_bf16 v[4:7], a[60:63], v[28:31], v[4:7]\n"
    "s_and_b32 s25, s25, 0x2000\n"
    "s_add_u32 s33, s33, s25\n"
    "s_add_u32 s33, s33, s14\n"
    "v_mfma_f32_16x16x32_bf16 v[8:11], a[140:143], v[24:27], v[8:11]\n"
    "s_mul_i32 s47, s40, s29\n"
    "v_mfma_f32_16x16x32_bf16 v[12:15], a[140:143], v[28:31], v[12:15]\n"
    "s_cmp_lt_u32 s30, s27\n"
    "s_cselect_b32 s36, 1, 0\n"
    "s_cselect_b32 s32, s32, 2130706432\n"
    "v_mfma_f32_16x16x32_bf16 v[16:19], a[220:223], v[24:27], v[16:19]\n"
    "ds_read_b128 v[24:27], v65 offset:36864\n"
    "v_mfma_f32_16x16x32_bf16 v[20:23], a[220:223], v[28:31], v[20:23]\n"
    "ds_read_b128 v[28:31], v65 offset:37888\n"
    "s_waitcnt lgkmcnt(4)\n"
    "v_mfma_f32_16x16x32_bf16 v[0:3], a[64:67], v[32:35], v[0:3]\n"
    "s_cmp_lt_u32 s30, s27\n"
    "s_cselect_b32 s39, s31, 2130706432\n"
    "s_cselect_b32 s47, s47, 0\n"
    "v_mfma_f32_16x16x32_bf16 v[4:7], a[64:67], v[36:39], v[4:7]\n"
    "s_cmp_gt_u32 s47, 0\n"
    "s_cselect_b32 s38, s17, 2130706432\n"
    "v_mfma_f32_16x16x32_bf16 v[8:11], a[144:147], v[32:35], v[8:11]\n"
    "buffer_load_dword v60, v68, s[0:3], s38 offen sc1\n"
    "v_mfma_f32_16x16x32_bf16 v[12:15], a[144:147], v[36:39], v[12:15]\n"
    "v_mfma_f32_16x16x32_bf16 v[16:19], a[224:227], v[32:35], v[16:19]\n"
    "ds_read_b128 v[32:35], v65 offset:38912\n"
    "v_mfma_f32_16x16x32_bf16 v[20:23], a[224:227], v[36:39], v[20:23]\n"
    "ds_read_b128 v[36:39], v65 offset:39936\n"
    "s_waitcnt lgkmcnt(4)\n"
    "v_mfma_f32_16x16x32_bf16 v[0:3], a[68:71], v[40:43], v[0:3]\n"
    "v_mfma_f32_16x16x32_bf16 v[4:7], a[68:71], v[44:47], v[4:7]\n"
    "v_mfma_f32_16x16x32_bf16 v[8:11], a[148:151], v[40:43], v[8:11]\n"
    "v_mfma_f32_16x16x32_bf16 v[12:15], a[148:151], v[44:47], v[12:15]\n"
    "v_mfma_f32_16x16x32_bf16 v[16:19], a[228:231], v[40:43], v[16:19]\n"
    "v_mfma_f32_16x16x32_bf16 v[20:23], a[228:231], v[44:47], v[20:23]\n"
    "s_waitcnt lgkmcnt(2)\n"
    "v_mfma_f32_16x16x32_bf16 v[0:3], a[72:75], v[24:27], v[0:3]\n"
    "v_mfma_f32_16x16x32_bf16 v[4:7], a[72:75], v[28:31], v[4:7]\n"
    "v_mfma_f32_16x16x32_bf16 v[8:11], a[152:155], v[24:27], v[8:11]\n"
    "v_mfma_f32_16x16x32_bf16 v[12:15], a[152:155], v[28:31], v[12:15]\n"
    "v_mfma_f32_16x16x32_bf16 v[16:19], a[232:235], v[24:27], v[16:19]\n"
    "v_mfma_f32_16x16x32_bf16 v[20:23], a[232:235], v[28:31], v[20:23]\n"
    "s_waitcnt lgkmcnt(0)\n"
    "v_mfma_f32_16x16x32_bf16 v[0:3], a[76:79], v[32:35], v[0:3]\n"
    "v_mfma_f32_16x16x32_bf16 v[4:7], a[76:79], v[36:39], v[4:7]\n"
    "v_mfma_f32_16x16x32_bf16 v[8:11], a[156:159], v[32:35], v[8:11]\n"
    "v_mfma_f32_16x16x32_bf16 v[12:15], a[156:159], v[36:39], v[12:15]\n"
    "v_mfma_f32_16x16x32_bf16 v[16:19], a[236:239], v[32:35], v[16:19]\n"
    "v_mfma_f32_16x16x32_bf16 v[20:23], a[236:239], v[36:39], v[20:23]\n"
    "v_cvt_pk_bf16_f32 v48, v0, v1\n"
    "v_cvt_pk_bf16_f32 v49, v2, v3\n"
    "v_cvt_pk_bf16_f32 v50, v8, v9\n"
    "v_cvt_pk_bf16_f32 v51, v10, v11\n"
    "v_cvt_pk_bf16_f32 v52, v4, v5\n"
    "v_cvt_pk_bf16_f32 v53, v6, v7\n"
    "v_cvt_pk_bf16_f32 v54, v12, v13\n"
    "v_cvt_pk_bf16_f32 v55, v14, v15\n"
    "v_cvt_pk_bf16_f32 v56, v16, v17\n"
    "v_cvt_pk_bf16_f32 v57, v18, v19\n"
    "v_cvt_pk_bf16_f32 v58, v20, v21\n"
    "v_cvt_pk_bf16_f32 v59, v22, v23\n"
    ".Lqw_mk1x4_%=:\n"
    ".Lqw_join_%=:\n"
    "v_xor_b32 v65, 40960, v65\n"
    "s_waitcnt vmcnt(0) lgkmcnt(0)\n"
    "s_barrier\n"
    ".Lqw_mk2x5_%=:\n"
    "s_cmp_le_u32 s26, 142\n"
    "s_cbranch_scc1 .Lqw_top_%=\n"
    "buffer_store_dwordx4 v[48:51], v66, s[0:3], s46 offen offset:0 sc1\n"
    "buffer_store_dwordx4 v[52:55], v66, s[0:3], s46 offen offset:1024 sc1\n"
    "buffer_store_dwordx4 v[56:59], v67, s[0:3], s46 offen sc1\n"
    "s_waitcnt vmcnt(0)\n"
    "v_mov_b32 v61, s44\n"
    "buffer_store_dword v61, v69, s[0:3], s43 offen sc1\n"
    "s_waitcnt vmcnt(0) lgkmcnt(0)\n"
    "s_endpgm\n"
    ".Lqw_wx_%=:\n"
    "buffer_store_dwordx4 v[48:51], v66, s[0:3], s46 offen offset:0 sc1\n"
    "buffer_store_dwordx4 v[52:55], v66, s[0:3], s46 offen offset:1024 sc1\n"
    "buffer_store_dwordx4 v[56:59], v67, s[0:3], s46 offen sc1\n"
    "s_waitcnt vmcnt(0)\n"
    "v_mov_b32 v61, s44\n"
    "buffer_store_dword v61, v69, s[0:3], s43 offen sc1\n"
    "s_mov_b32 s43, 2130706432\n"
    "s_mov_b32 s46, 2130706432\n"
    ".Lqw_rt7_%=:\n"
    "s_memrealtime s[22:23]\n"
    "s_waitcnt lgkmcnt(0)\n"
    "s_sub_u32 s24, s45, s22\n"
    "s_cmp_lt_u32 s24, 600\n"
    "s_cbranch_scc1 .Lqw_rt7_%=\n"
    ".Lqw_po8_%=:\n"
    "buffer_load_dword v60, v68, s[0:3], s17 offen sc1\n"
    "s_waitcnt vmcnt(0)\n"
    "s_memrealtime s[22:23]\n"
    "s_waitcnt lgkmcnt(0)\n"
    "s_add_u32 s45, s22, 236\n"
    "v_cmp_gt_i32 vcc, s47, v60\n"
    "s_nop 0\n"
    "s_cbranch_vccz .Lqw_pd9_%=\n"
    "s_add_u32 s20, s20, 1\n"
    "s_cmp_lt_u32 s20, 0x40000\n"
    "s_cbranch_scc1 .Lqw_sp10_%=\n"
    "v_mov_b32 v60, 51\n"
    "s_mov_b32 s24, 33728\n"
    "buffer_store_dword v60, off, s[0:3], s24 sc1\n"
    "s_waitcnt vmcnt(0)\n"
    "s_branch .Lqw_pd9_%=\n"
    ".Lqw_sp10_%=:\n"
    "s_sleep 10\n"
    "s_branch .Lqw_po8_%=\n"
    ".Lqw_pd9_%=:\n"
    "s_branch .Lqw_wxb_%=\n"
    ".Lqw_skip_%=:\n"
    "buffer_store_dwordx4 v[48:51], v66, s[0:3], s46 offen offset:0 sc1\n"
    "buffer_store_dwordx4 v[52:55], v66, s[0:3], s46 offen offset:1024 sc1\n"
    "buffer_store_dwordx4 v[56:59], v67, s[0:3], s46 offen sc1\n"
    "s_add_u32 m0, s37, 0\n"
    "s_nop 0\n"
    "buffer_load_dwordx4 v64, s[0:3], s32 offen sc1 lds\n"
    "buffer_load_dwordx4 v64, s[0:3], s32 offen offset:1024 sc1 lds\n"
    "s_add_u32 m0, s37, 8192\n"
    "s_add_u32 s32, s32, 0x4000\n"
    "buffer_load_dwordx4 v64, s[0:3], s32 offen sc1 lds\n"
    "buffer_load_dwordx4 v64, s[0:3], s32 offen offset:1024 sc1 lds\n"
    "s_add_u32 m0, s37, 16384\n"
    "s_add_u32 s32, s32, 0x4000\n"
    "buffer_load_dwordx4 v64, s[0:3], s32 offen sc1 lds\n"
    "buffer_load_dwordx4 v64, s[0:3], s32 offen offset:1024 sc1 lds\n"
    "s_add_u32 m0, s37, 24576\n"
    "s_add_u32 s32, s32, 0x4000\n"
    "buffer_load_dwordx4 v64, s[0:3], s32 offen sc1 lds\n"
    "buffer_load_dwordx4 v64, s[0:3], s32 offen offset:1024 sc1 lds\n"
    "s_add_u32 m0, s37, 32768\n"
    "s_add_u32 s32, s32, 0x4000\n"
    "buffer_load_dwordx4 v64, s[0:3], s32 offen sc1 lds\n"
    "buffer_load_dwordx4 v64, s[0:3], s32 offen offset:1024 sc1 lds\n"
    "s_waitcnt vmcnt(0)\n"
    "v_mov_b32 v61, s44\n"
    "buffer_store_dword v61, v69, s[0:3], s43 offen sc1\n"
    "s_mov_b32 s34, 2130706432\n"
    "s_mov_b32 s46, s34\n"
    "s_mov_b32 s43, s41\n"
    "s_mov_b32 s44, s42\n"
    "s_mov_b32 s41, s39\n"
    "s_mov_b32 s42, s40\n"
    "s_mov_b32 s34, s33\n"
    "s_mov_b32 s35, s36\n"
    "s_xor_b32 s37, s37, 40960\n"
    "s_add_u32 s26, s26, 1\n"
    "s_sub_u32 s27, s26, 14\n"
    "s_ashr_i32 s28, s27, 1\n"
    "s_and_b32 s29, s27, 1\n"
    "s_add_u32 s30, s28, 14\n"
    "s_cmp_lt_i32 s27, 0\n"
    "s_cselect_b32 s29, 0, s29\n"
    "s_cselect_b32 s30, s26, s30\n"
    "s_cmp_eq_u32 s29, 1\n"
    "s_cselect_b32 s30, s28, s30\n"
    "s_cselect_b32 s27, s15, 64\n"
    "s_lshl_b32 s31, s29, 13\n"
    "s_add_u32 s40, s30, 1\n"
    "s_lshl_b32 s32, s30, 19\n"
    "s_lshl_b32 s25, s30, 11\n"
    "s_xor_b32 s25, s25, s31\n"
    "s_and_b32 s25, s25, 0x2000\n"
    "s_add_u32 s32, s32, s25\n"
    "s_add_u32 s32, s32, s13\n"
    "s_lshl_b32 s33, s30, 20\n"
    "s_lshl_b32 s25, s30, 12\n"
    "s_xor_b32 s25, s25, s31\n"
    "s_and_b32 s25, s25, 0x2000\n"
    "s_add_u32 s33, s33, s25\n"
    "s_add_u32 s33, s33, s14\n"
    "s_mul_i32 s47, s40, s29\n"
    "s_cmp_lt_u32 s30, s27\n"
    "s_cselect_b32 s36, 1, 0\n"
    "s_cselect_b32 s32, s32, 2130706432\n"
    "s_cmp_lt_u32 s30, s27\n"
    "s_cselect_b32 s39, s31, 2130706432\n"
    "s_cselect_b32 s47, s47, 0\n"
    "s_cmp_gt_u32 s47, 0\n"
    "s_cselect_b32 s38, s17, 2130706432\n"
    "buffer_load_dword v60, v68, s[0:3], s38 offen sc1\n"
    "s_branch .Lqw_join_%=\n"
    :: "{s[0:3]}"(s0_), "{s[4:7]}"(s4_), "{s[8:11]}"(s8_), "{s[12:15]}"(s12_), "{s[16:19]}"(s16_), "{v[0:3]}"(v0_), "{v[64:67]}"(v64_), "{v[68:71]}"(v68_) : "memory", "v255", "a255", "s95");
    __builtin_unreachable();
}
DEV void b1_role(const MegaArgs& A, const int L, const int i, const int rep) {
    ROLE_HEAD;
    const int qd = 4 * w + lq, sw = (l16 & 6) << 3, hw = w < 3 ? w : 2, hd = 3 * i + w;
    const bool live = w < 3 && hd < 32;
    const unsigned om = live ? RC_O - RC_X1 - w * 2048 + (hd >> 3) * 16384 + ((hd >> 1) & 3) * 2048 + (lq & 1) * 1024 + (2 * (hd & 1) + (lq >> 1)) * 256 + l16 * 16 : OOB;
    const unsigned ox = live && l < 16 ? RC_O - RC_X1 - w * 2048 + 65536 + hd * 256 + l16 * 16 : OOB;
    u64 wm[4];
#pragma unroll
    for (int e = 0; e < 4; ++e) { const int d = l16 + 16 - 16 * (e >> 1) - 4 * lq - 2 * (e & 1) - (l16 & 1); wm[e] = __ballot(d >= 0 && d <= 14); }
    const u32x4 s0_ = {RFL(rx[0]), RFL(rx[1]), RFL(rx[2]), RFL(rx[3])};
    const u32x4 s4_ = {RFL(lo(A.wqkv + 60 * i + 16 * w)), RFL(hi(A.wqkv + 60 * i + 16 * w)), RFL((640 * 1920 - 60 * i - 16 * w) * 2), RFL(0x20000u)};
    const u32x4 s8_ = {RFL((unsigned)wm[0]), RFL((unsigned)(wm[0] >> 32)), RFL((unsigned)wm[1]), RFL((unsigned)(wm[1] >> 32))};
    const u32x4 s12_ = {RFL((unsigned)wm[2]), RFL((unsigned)(wm[2] >> 32)), RFL((unsigned)wm[3]), RFL((unsigned)(wm[3] >> 32))};
    const u32x4 s16_ = {RFL(w == 3 ? 0xf000f000u : 0u), RFL(w == 3 ? 0xf000f000u : 0u), RFL(w), RFL(L << 13)};
    const u32x4 s20_ = {RFL(REC0 + RC_X1 + w * 2048), RFL(xl(Z_X1 + rep * 4096, L)), RFL(DBG0 + ((L * 128 + 96 + i) * 4 + w) * 2048), RFL(0u)};
    const u32x4 v0_ = {(unsigned)((l >> 1) * 3840 + (l & 1) * 16 + 0), (unsigned)((l >> 1) * 3840 + (l & 1) * 16 + 256), (unsigned)((l >> 1) * 3840 + (l & 1) * 16 + 512), (unsigned)(w * 40960 + (8 * lq + (l16 >> 2)) * 32 + (l & 3) * 8)};
    const u32x4 v224_ = {(unsigned)(l * 16 + ((w + 0) & 3) * 2048), (unsigned)(l * 16 + ((w + 1) & 3) * 2048), (unsigned)(l * 16 + ((w + 2) & 3) * 2048), (unsigned)(l * 16 + ((w + 3) & 3) * 2048)};
    const u32x4 v228_ = {(unsigned)(l * 16), (unsigned)((l & 31) * 128), (unsigned)(l < 4 && w == 3 ? xl(Z_O + l * 2048 + i * 128, L) : OOB), (unsigned)(qd == 15 ? 86016 : 40960 + (qd / 5) * 2048 + l16 * 64 + (((qd % 5) * 8) ^ sw))};
    const u32x4 v232_ = {(unsigned)(qd == 15 ? 86016 : 49152 + (qd / 5) * 4096 + l16 * 64 + (((qd % 5) * 8) ^ sw)), (unsigned)(qd == 15 ? 86016 : 61440 + (qd / 5) * 4096 + l16 * 64 + (((qd % 5) * 8) ^ sw)), (unsigned)(40960 + hw * 2048 + l16 * 64 + ((lq * 16) ^ sw)), (unsigned)(49152 + hw * 4096 + l16 * 64 + ((lq * 16) ^ sw))};
    const u32x4 v236_ = {(unsigned)(51200 + hw * 4096 + l16 * 64 + ((lq * 16) ^ sw)), (unsigned)(61440 + hw * 4096 + (l >> 2) * 64 + ((0 + (l & 3) * 8) ^ (((l >> 2) & 6) << 3))), (unsigned)(63488 + hw * 4096 + (l >> 2) * 64 + ((0 + (l & 3) * 8) ^ (((l >> 2) & 6) << 3))), (unsigned)(61440 + hw * 4096 + (l >> 2) * 64 + ((32 + (l & 3) * 8) ^ (((l >> 2) & 6) << 3)))};
    const u32x4 v240_ = {(unsigned)(63488 + hw * 4096 + (l >> 2) * 64 + ((32 + (l & 3) * 8) ^ (((l >> 2) & 6) << 3))), (unsigned)(73728 + w * 2048 + l * 16), (unsigned)(73728 + (l >> 5) * 1024 + ((l16 >> 2) * 16 + l16) * 16 + (l & 3) * 4), (unsigned)(81920 + w * 512 + l * 4)};
    const u32x4 v244_ = {(unsigned)(81920 + 3 * 512 + (l >> 5) * 256 + (48 + l16) * 4), (unsigned)(om), (unsigned)(ox), (unsigned)((l & 1) ? 0x01000c0cu : 0x0c0c0100u)};
    const u32x4 v248_ = {(unsigned)((l & 1) ? 0x03020c0cu : 0x0c0c0302u), (unsigned)(0xff800000u), (unsigned)(0u), (unsigned)(0u)};
    asm volatile(
    "s_mov_b32 s24, 0\n"
    "s_cmp_eq_u32 s19, 0\n"
    "s_cselect_b32 s28, 16, 50\n"
    ".Lb1_ho1_%=:\n"
    "s_sub_u32 s28, s28, 1\n"
    "s_cbranch_scc1 .Lb1_hd2_%=\n"
    "s_sleep 17\n"
    "s_branch .Lb1_ho1_%=\n"
    ".Lb1_hd2_%=:\n"
    "s_mul_i32 s32, s18, 40960\n"
    "s_add_u32 s35, s18, 0\n"
    "s_and_b32 s35, s35, 3\n"
    "s_add_u32 s35, s35, 0\n"
    "s_mul_i32 s35, s35, 122880\n"
    "s_add_u32 m0, s32, 0\n"
    "s_nop 0\n"
    "buffer_load_dwordx4 v0, s[4:7], s35 offen offset:0 lds\n"
    "buffer_load_dwordx4 v1, s[4:7], s35 offen offset:1024 lds\n"
    "buffer_load_dwordx4 v2, s[4:7], s35 offen offset:2048 lds\n"
    "s_add_u32 s35, s18, 1\n"
    "s_and_b32 s35, s35, 3\n"
    "s_add_u32 s35, s35, 0\n"
    "s_mul_i32 s35, s35, 122880\n"
    "s_add_u32 m0, s32, 3072\n"
    "s_nop 0\n"
    "buffer_load_dwordx4 v0, s[4:7], s35 offen offset:0 lds\n"
    "buffer_load_dwordx4 v1, s[4:7], s35 offen offset:1024 lds\n"
    "buffer_load_dwordx4 v2, s[4:7], s35 offen offset:2048 lds\n"
    "s_add_u32 s35, s18, 2\n"
    "s_and_b32 s35, s35, 3\n"
    "s_add_u32 s35, s35, 0\n"
    "s_mul_i32 s35, s35, 122880\n"
    "s_add_u32 m0, s32, 6144\n"
    "s_nop 0\n"
    "buffer_load_dwordx4 v0, s[4:7], s35 offen offset:0 lds\n"
    "buffer_load_dwordx4 v1, s[4:7], s35 offen offset:1024 lds\n"
    "buffer_load_dwordx4 v2, s[4:7], s35 offen offset:2048 lds\n"
    "s_add_u32 s35, s18, 3\n"
    "s_and_b32 s35, s35, 3\n"
    "s_add_u32 s35, s35, 0\n"
    "s_mul_i32 s35, s35, 122880\n"
    "s_add_u32 m0, s32, 9216\n"
    "s_nop 0\n"
    "buffer_load_dwordx4 v0, s[4:7], s35 offen offset:0 lds\n"
    "buffer_load_dwordx4 v1, s[4:7], s35 offen offset:1024 lds\n"
    "buffer_load_dwordx4 v2, s[4:7], s35 offen offset:2048 lds\n"
    "s_add_u32 s35, s18, 0\n"
    "s_and_b32 s35, s35, 3\n"
    "s_add_u32 s35, s35, 4\n"
    "s_mul_i32 s35, s35, 122880\n"
    "s_add_u32 m0, s32, 12288\n"
    "s_nop 0\n"
    "buffer_load_dwordx4 v0, s[4:7], s35 offen offset:0 lds\n"
    "buffer_load_dwordx4 v1, s[4:7], s35 offen offset:1024 lds\n"
    "buffer_load_dwordx4 v2, s[4:7], s35 offen offset:2048 lds\n"
    "s_add_u32 s35, s18, 1\n"
    "s_and_b32 s35, s35, 3\n"
    "s_add_u32 s35, s35, 4\n"
    "s_mul_i32 s35, s35, 122880\n"
    "s_add_u32 m0, s32, 15360\n"
    "s_nop 0\n"
    "buffer_load_dwordx4 v0, s[4:7], s35 offen offset:0 lds\n"
    "buffer_load_dwordx4 v1, s[4:7], s35 offen offset:1024 lds\n"
    "buffer_load_dwordx4 v2, s[4:7], s35 offen offset:2048 lds\n"
    "s_add_u32 s35, s18, 2\n"
    "s_and_b32 s35, s35, 3\n"
    "s_add_u32 s35, s35, 4\n"
    "s_mul_i32 s35, s35, 122880\n"
    "s_add_u32 m0, s32, 18432\n"
    "s_nop 0\n"
    "buffer_load_dwordx4 v0, s[4:7], s35 offen offset:0 lds\n"
    "buffer_load_dwordx4 v1, s[4:7], s35 offen offset:1024 lds\n"
    "buffer_load_dwordx4 v2, s[4:7], s35 offen offset:2048 lds\n"
    "s_add_u32 s35, s18, 3\n"
    "s_and_b32 s35, s35, 3\n"
    "s_add_u32 s35, s35, 4\n"
    "s_mul_i32 s35, s35, 122880\n"
    "s_add_u32 m0, s32, 21504\n"
    "s_nop 0\n"
    "buffer_load_dwordx4 v0, s[4:7], s35 offen offset:0 lds\n"
    "buffer_load_dwordx4 v1, s[4:7], s35 offen offset:1024 lds\n"
    "buffer_load_dwordx4 v2, s[4:7], s35 offen offset:2048 lds\n"
    "s_add_u32 s35, s18, 0\n"
    "s_and_b32 s35, s35, 3\n"
    "s_add_u32 s35, s35, 8\n"
    "s_mul_i32 s35, s35, 122880\n"
    "s_add_u32 m0, s32, 24576\n"
    "s_nop 0\n"
    "buffer_load_dwordx4 v0, s[4:7], s35 offen offset:0 lds\n"
    "buffer_load_dwordx4 v1, s[4:7], s35 offen offset:1024 lds\n"
    "buffer_load_dwordx4 v2, s[4:7], s35 offen offset:2048 lds\n"
    "s_add_u32 s35, s18, 1\n"
    "s_and_b32 s35, s35, 3\n"
    "s_add_u32 s35, s35, 8\n"
    "s_mul_i32 s35, s35, 122880\n"
    "s_add_u32 m0, s32, 27648\n"
    "s_nop 0\n"
    "buffer_load_dwordx4 v0, s[4:7], s35 offen offset:0 lds\n"
    "buffer_load_dwordx4 v1, s[4:7], s35 offen offset:1024 lds\n"
    "buffer_load_dwordx4 v2, s[4:7], s35 offen offset:2048 lds\n"
    "s_add_u32 s35, s18, 2\n"
    "s_and_b32 s35, s35, 3\n"
    "s_add_u32 s35, s35, 8\n"
    "s_mul_i32 s35, s35, 122880\n"
    "s_add_u32 m0, s32, 30720\n"
    "s_nop 0\n"
    "buffer_load_dwordx4 v0, s[4:7], s35 offen offset:0 lds\n"
    "buffer_load_dwordx4 v1, s[4:7], s35 offen offset:1024 lds\n"
    "buffer_load_dwordx4 v2, s[4:7], s35 offen offset:2048 lds\n"
    "s_add_u32 s35, s18, 3\n"
    "s_and_b32 s35, s35, 3\n"
    "s_add_u32 s35, s35, 8\n"
    "s_mul_i32 s35, s35, 122880\n"
    "s_add_u32 m0, s32, 33792\n"
    "s_nop 0\n"
    "buffer_load_dwordx4 v0, s[4:7], s35 offen offset:0 lds\n"
    "buffer_load_dwordx4 v1, s[4:7], s35 offen offset:1024 lds\n"
    "buffer_load_dwordx4 v2, s[4:7], s35 offen offset:2048 lds\n"
    "s_add_u32 s35, s18, 0\n"
    "s_and_b32 s35, s35, 3\n"
    "s_add_u32 s35, s35, 12\n"
    "s_mul_i32 s35, s35, 122880\n"
    "s_add_u32 m0, s32, 36864\n"
    "s_nop 0\n"
    "buffer_load_dwordx4 v0, s[4:7], s35 offen offset:0 lds\n"
    "buffer_load_dwordx4 v1, s[4:7], s35 offen offset:1024 lds\n"
    "buffer_load_dwordx4 v2, s[4:7], s35 offen offset:2048 lds\n"
    "s_waitcnt vmcnt(36)\n"
    "ds_read_b64_tr_b16 a[0:1], v3 offset:0\n"
    "ds_read_b64_tr_b16 a[2:3], v3 offset:128\n"
    "ds_read_b64_tr_b16 a[80:81], v3 offset:1024\n"
    "ds_read_b64_tr_b16 a[82:83], v3 offset:1152\n"
    "ds_read_b64_tr_b16 a[160:161], v3 offset:2048\n"
    "ds_read_b64_tr_b16 a[162:163], v3 offset:2176\n"
    "s_waitcnt lgkmcnt(0)\n"
    "s_add_u32 s35, s18, 1\n"
    "s_and_b32 s35, s35, 3\n"
    "s_add_u32 s35, s35, 12\n"
    "s_mul_i32 s35, s35, 122880\n"
    "s_add_u32 m0, s32, 0\n"
    "s_nop 0\n"
    "buffer_load_dwordx4 v0, s[4:7], s35 offen offset:0 lds\n"
    "buffer_load_dwordx4 v1, s[4:7], s35 offen offset:1024 lds\n"
    "buffer_load_dwordx4 v2, s[4:7], s35 offen offset:2048 lds\n"
    "s_waitcnt vmcnt(36)\n"
    "ds_read_b64_tr_b16 a[4:5], v3 offset:3072\n"
    "ds_read_b64_tr_b16 a[6:7], v3 offset:3200\n"
    "ds_read_b64_tr_b16 a[84:85], v3 offset:4096\n"
    "ds_read_b64_tr_b16 a[86:87], v3 offset:4224\n"
    "ds_read_b64_tr_b16 a[164:165], v3 offset:5120\n"
    "ds_read_b64_tr_b16 a[166:167], v3 offset:5248\n"
    "s_waitcnt lgkmcnt(0)\n"
    "s_add_u32 s35, s18, 2\n"
    "s_and_b32 s35, s35, 3\n"
    "s_add_u32 s35, s35, 12\n"
    "s_mul_i32 s35, s35, 122880\n"
    "s_add_u32 m0, s32, 3072\n"
    "s_nop 0\n"
    "buffer_load_dwordx4 v0, s[4:7], s35 offen offset:0 lds\n"
    "buffer_load_dwordx4 v1, s[4:7], s35 offen offset:1024 lds\n"
    "buffer_load_dwordx4 v2, s[4:7], s35 offen offset:2048 lds\n"
    "s_waitcnt vmcnt(36)\n"
    "ds_read_b64_tr_b16 a[8:9], v3 offset:6144\n"
    "ds_read_b64_tr_b16 a[10:11], v3 offset:6272\n"
    "ds_read_b64_tr_b16 a[88:89], v3 offset:7168\n"
    "ds_read_b64_tr_b16 a[90:91], v3 offset:7296\n"
    "ds_read_b64_tr_b16 a[168:169], v3 offset:8192\n"
    "ds_read_b64_tr_b16 a[170:171], v3 offset:8320\n"
    "s_waitcnt lgkmcnt(0)\n"
    "s_add_u32 s35, s18, 3\n"
    "s_and_b32 s35, s35, 3\n"
    "s_add_u32 s35, s35, 12\n"
    "s_mul_i32 s35, s35, 122880\n"
    "s_add_u32 m0, s32, 6144\n"
    "s_nop 0\n"
    "buffer_load_dwordx4 v0, s[4:7], s35 offen offset:0 lds\n"
    "buffer_load_dwordx4 v1, s[4:7], s35 offen offset:1024 lds\n"
    "buffer_load_dwordx4 v2, s[4:7], s35 offen offset:2048 lds\n"
    "s_waitcnt vmcnt(36)\n"
    "ds_read_b64_tr_b16 a[12:13], v3 offset:9216\n"
    "ds_read_b64_tr_b16 a[14:15], v3 offset:9344\n"
    "ds_read_b64_tr_b16 a[92:93], v3 offset:10240\n"
    "ds_read_b64_tr_b16 a[94:95], v3 offset:10368\n"
    "ds_read_b64_tr_b16 a[172:173], v3 offset:11264\n"
    "ds_read_b64_tr_b16 a[174:175], v3 offset:11392\n"
    "s_waitcnt lgkmcnt(0)\n"
    "s_add_u32 s35, s18, 0\n"
    "s_and_b32 s35, s35, 3\n"
    "s_add_u32 s35, s35, 16\n"
    "s_mul_i32 s35, s35, 122880\n"
    "s_add_u32 m0, s32, 9216\n"
    "s_nop 0\n"
    "buffer_load_dwordx4 v0, s[4:7], s35 offen offset:0 lds\n"
    "buffer_load_dwordx4 v1, s[4:7], s35 offen offset:1024 lds\n"
    "buffer_load_dwordx4 v2, s[4:7], s35 offen offset:2048 lds\n"
    "s_waitcnt vmcnt(36)\n"
    "ds_read_b64_tr_b16 a[16:17], v3 offset:12288\n"
    "ds_read_b64_tr_b16 a[18:19], v3 offset:12416\n"
    "ds_read_b64_tr_b16 a[96:97], v3 offset:13312\n"
    "ds_read_b64_tr_b16 a[98:99], v3 offset:13440\n"
    "ds_read_b64_tr_b16 a[176:177], v3 offset:14336\n"
    "ds_read_b64_tr_b16 a[178:179], v3 offset:14464\n"
    "s_waitcnt lgkmcnt(0)\n"
    "s_add_u32 s35, s18, 1\n"
    "s_and_b32 s35, s35, 3\n"
    "s_add_u32 s35, s35, 16\n"
    "s_mul_i32 s35, s35, 122880\n"
    "s_add_u32 m0, s32, 12288\n"
    "s_nop 0\n"
    "buffer_load_dwordx4 v0, s[4:7], s35 offen offset:0 lds\n"
    "buffer_load_dwordx4 v1, s[4:7], s35 offen offset:1024 lds\n"
    "buffer_load_dwordx4 v2, s[4:7], s35 offen offset:2048 lds\n"
    "s_waitcnt vmcnt(36)\n"
    "ds_read_b64_tr_b16 a[20:21], v3 offset:15360\n"
    "ds_read_b64_tr_b16 a[22:23], v3 offset:15488\n"
    "ds_read_b64_tr_b16 a[100:101], v3 offset:16384\n"
    "ds_read_b64_tr_b16 a[102:103], v3 offset:16512\n"
    "ds_read_b64_tr_b16 a[180:181], v3 offset:17408\n"
    "ds_read_b64_tr_b16 a[182:183], v3 offset:17536\n"
    "s_waitcnt lgkmcnt(0)\n"
    "s_add_u32 s35, s18, 2\n"
    "s_and_b32 s35, s35, 3\n"
    "s_add_u32 s35, s35, 16\n"
    "s_mul_i32 s35, s35, 122880\n"
    "s_add_u32 m0, s32, 15360\n"
    "s_nop 0\n"
    "buffer_load_dwordx4 v0, s[4:7], s35 offen offset:0 lds\n"
    "buffer_load_dwordx4 v1, s[4:7], s35 offen offset:1024 lds\n"
    "buffer_load_dwordx4 v2, s[4:7], s35 offen offset:2048 lds\n"
    "s_waitcnt vmcnt(36)\n"
    "ds_read_b64_tr_b16 a[24:25], v3 offset:18432\n"
    "ds_read_b64_tr_b16 a[26:27], v3 offset:18560\n"
    "ds_read_b64_tr_b16 a[104:105], v3 offset:19456\n"
    "ds_read_b64_tr_b16 a[106:107], v3 offset:19584\n"
    "ds_read_b64_tr_b16 a[184:185], v3 offset:20480\n"
    "ds_read_b64_tr_b16 a[186:187], v3 offset:20608\n"
    "s_waitcnt lgkmcnt(0)\n"
    "s_add_u32 s35, s18, 3\n"
    "s_and_b32 s35, s35, 3\n"
    "s_add_u32 s35, s35, 16\n"
    "s_mul_i32 s35, s35, 122880\n"
    "s_add_u32 m0, s32, 18432\n"
    "s_nop 0\n"
    "buffer_load_dwordx4 v0, s[4:7], s35 offen offset:0 lds\n"
    "buffer_load_dwordx4 v1, s[4:7], s35 offen offset:1024 lds\n"
    "buffer_load_dwordx4 v2, s[4:7], s35 offen offset:2048 lds\n"
    "s_waitcnt vmcnt(36)\n"
    "ds_read_b64_tr_b16 a[28:29], v3 offset:21504\n"
    "ds_read_b64_tr_b16 a[30:31], v3 offset:21632\n"
    "ds_read_b64_tr_b16 a[108:109], v3 offset:22528\n"
    "ds_read_b64_tr_b16 a[110:111], v3 offset:22656\n"
    "ds_read_b64_tr_b16 a[188:189], v3 offset:23552\n"
    "ds_read_b64_tr_b16 a[190:191], v3 offset:23680\n"
    "s_waitcnt lgkmcnt(0)\n"
    "s_waitcnt vmcnt(33)\n"
    "ds_read_b64_tr_b16 a[32:33], v3 offset:24576\n"
    "ds_read_b64_tr_b16 a[34:35], v3 offset:24704\n"
    "ds_read_b64_tr_b16 a[112:113], v3 offset:25600\n"
    "ds_read_b64_tr_b16 a[114:115], v3 offset:25728\n"
    "ds_read_b64_tr_b16 a[192:193], v3 offset:26624\n"
    "ds_read_b64_tr_b16 a[194:195], v3 offset:26752\n"
    "s_waitcnt lgkmcnt(0)\n"
    "s_waitcnt vmcnt(30)\n"
    "ds_read_b64_tr_b16 a[36:37], v3 offset:27648\n"
    "ds_read_b64_tr_b16 a[38:39], v3 offset:27776\n"
    "ds_read_b64_tr_b16 a[116:117], v3 offset:28672\n"
    "ds_read_b64_tr_b16 a[118:119], v3 offset:28800\n"
    "ds_read_b64_tr_b16 a[196:197], v3 offset:29696\n"
    "ds_read_b64_tr_b16 a[198:199], v3 offset:29824\n"
    "s_waitcnt lgkmcnt(0)\n"
    "s_waitcnt vmcnt(27)\n"
    "ds_read_b64_tr_b16 a[40:41], v3 offset:30720\n"
    "ds_read_b64_tr_b16 a[42:43], v3 offset:30848\n"
    "ds_read_b64_tr_b16 a[120:121], v3 offset:31744\n"
    "ds_read_b64_tr_b16 a[122:123], v3 offset:31872\n"
    "ds_read_b64_tr_b16 a[200:201], v3 offset:32768\n"
    "ds_read_b64_tr_b16 a[202:203], v3 offset:32896\n"
    "s_waitcnt lgkmcnt(0)\n"
    "s_waitcnt vmcnt(24)\n"
    "ds_read_b64_tr_b16 a[44:45], v3 offset:33792\n"
    "ds_read_b64_tr_b16 a[46:47], v3 offset:33920\n"
    "ds_read_b64_tr_b16 a[124:125], v3 offset:34816\n"
    "ds_read_b64_tr_b16 a[126:127], v3 offset:34944\n"
    "ds_read_b64_tr_b16 a[204:205], v3 offset:35840\n"
    "ds_read_b64_tr_b16 a[206:207], v3 offset:35968\n"
    "s_waitcnt lgkmcnt(0)\n"
    "s_waitcnt vmcnt(21)\n"
    "ds_read_b64_tr_b16 a[48:49], v3 offset:36864\n"
    "ds_read_b64_tr_b16 a[50:51], v3 offset:36992\n"
    "ds_read_b64_tr_b16 a[128:129], v3 offset:37888\n"
    "ds_read_b64_tr_b16 a[130:131], v3 offset:38016\n"
    "ds_read_b64_tr_b16 a[208:209], v3 offset:38912\n"
    "ds_read_b64_tr_b16 a[210:211], v3 offset:39040\n"
    "s_waitcnt lgkmcnt(0)\n"
    "s_waitcnt vmcnt(18)\n"
    "ds_read_b64_tr_b16 a[52:53], v3 offset:0\n"
    "ds_read_b64_tr_b16 a[54:55], v3 offset:128\n"
    "ds_read_b64_tr_b16 a[132:133], v3 offset:1024\n"
    "ds_read_b64_tr_b16 a[134:135], v3 offset:1152\n"
    "ds_read_b64_tr_b16 a[212:213], v3 offset:2048\n"
    "ds_read_b64_tr_b16 a[214:215], v3 offset:2176\n"
    "s_waitcnt lgkmcnt(0)\n"
    "s_waitcnt vmcnt(15)\n"
    "ds_read_b64_tr_b16 a[56:57], v3 offset:3072\n"
    "ds_read_b64_tr_b16 a[58:59], v3 offset:3200\n"
    "ds_read_b64_tr_b16 a[136:137], v3 offset:4096\n"
    "ds_read_b64_tr_b16 a[138:139], v3 offset:4224\n"
    "ds_read_b64_tr_b16 a[216:217], v3 offset:5120\n"
    "ds_read_b64_tr_b16 a[218:219], v3 offset:5248\n"
    "s_waitcnt lgkmcnt(0)\n"
    "s_waitcnt vmcnt(12)\n"
    "ds_read_b64_tr_b16 a[60:61], v3 offset:6144\n"
    "ds_read_b64_tr_b16 a[62:63], v3 offset:6272\n"
    "ds_read_b64_tr_b16 a[140:141], v3 offset:7168\n"
    "ds_read_b64_tr_b16 a[142:143], v3 offset:7296\n"
    "ds_read_b64_tr_b16 a[220:221], v3 offset:8192\n"
    "ds_read_b64_tr_b16 a[222:223], v3 offset:8320\n"
    "s_waitcnt lgkmcnt(0)\n"
    "s_waitcnt vmcnt(9)\n"
    "ds_read_b64_tr_b16 a[64:65], v3 offset:9216\n"
    "ds_read_b64_tr_b16 a[66:67], v3 offset:9344\n"
    "ds_read_b64_tr_b16 a[144:145], v3 offset:10240\n"
    "ds_read_b64_tr_b16 a[146:147], v3 offset:10368\n"
    "ds_read_b64_tr_b16 a[224:225], v3 offset:11264\n"
    "ds_read_b64_tr_b16 a[226:227], v3 offset:11392\n"
    "s_waitcnt lgkmcnt(0)\n"
    "s_waitcnt vmcnt(6)\n"
    "ds_read_b64_tr_b16 a[68:69], v3 offset:12288\n"
    "ds_read_b64_tr_b16 a[70:71], v3 offset:12416\n"
    "ds_read_b64_tr_b16 a[148:149], v3 offset:13312\n"
    "ds_read_b64_tr_b16 a[150:151], v3 offset:13440\n"
    "ds_read_b64_tr_b16 a[228:229], v3 offset:14336\n"
    "ds_read_b64_tr_b16 a[230:231], v3 offset:14464\n"
    "s_waitcnt lgkmcnt(0)\n"
    "s_waitcnt vmcnt(3)\n"
    "ds_read_b64_tr_b16 a[72:73], v3 offset:15360\n"
    "ds_read_b64_tr_b16 a[74:75], v3 offset:15488\n"
    "ds_read_b64_tr_b16 a[152:153], v3 offset:16384\n"
    "ds_read_b64_tr_b16 a[154:155], v3 offset:16512\n"
    "ds_read_b64_tr_b16 a[232:233], v3 offset:17408\n"
    "ds_read_b64_tr_b16 a[234:235], v3 offset:17536\n"
    "s_waitcnt lgkmcnt(0)\n"
    "s_waitcnt vmcnt(0)\n"
    "ds_read_b64_tr_b16 a[76:77], v3 offset:18432\n"
    "ds_read_b64_tr_b16 a[78:79], v3 offset:18560\n"
    "ds_read_b64_tr_b16 a[156:157], v3 offset:19456\n"
    "ds_read_b64_tr_b16 a[158:159], v3 offset:19584\n"
    "ds_read_b64_tr_b16 a[236:237], v3 offset:20480\n"
    "ds_read_b64_tr_b16 a[238:239], v3 offset:20608\n"
    "s_waitcnt lgkmcnt(0)\n"
    "s_waitcnt lgkmcnt(0)\n"
    "s_barrier\n"
    "v_mov_b32 v32, 0x3f803f80\n"
    "v_mov_b32 v36, 0\n"
    "v_mov_b32 v33, 0x3f803f80\n"
    "v_mov_b32 v37, 0\n"
    "v_mov_b32 v34, 0x3f803f80\n"
    "v_mov_b32 v38, 0\n"
    "v_mov_b32 v35, 0x3f803f80\n"
    "v_mov_b32 v39, 0\n"
    "s_nop 0\n"
    "v_mfma_f32_16x16x32_bf16 v[96:99], a[0:3], v[32:35], 0\n"
    "v_mfma_f32_16x16x32_bf16 v[100:103], a[80:83], v[32:35], 0\n"
    "v_mfma_f32_16x16x32_bf16 v[104:107], a[160:163], v[32:35], 0\n"
    "v_mfma_f32_16x16x32_bf16 v[96:99], a[4:7], v[32:35], v[96:99]\n"
    "v_mfma_f32_16x16x32_bf16 v[100:103], a[84:87], v[32:35], v[100:103]\n"
    "v_mfma_f32_16x16x32_bf16 v[104:107], a[164:167], v[32:35], v[104:107]\n"
    "v_mfma_f32_16x16x32_bf16 v[96:99], a[8:11], v[32:35], v[96:99]\n"
    "v_mfma_f32_16x16x32_bf16 v[100:103], a[88:91], v[32:35], v[100:103]\n"
    "v_mfma_f32_16x16x32_bf16 v[104:107], a[168:171], v[32:35], v[104:107]\n"
    "v_mfma_f32_16x16x32_bf16 v[96:99], a[12:15], v[32:35], v[96:99]\n"
    "v_mfma_f32_16x16x32_bf16 v[100:103], a[92:95], v[32:35], v[100:103]\n"
    "v_mfma_f32_16x16x32_bf16 v[104:107], a[172:175], v[32:35], v[104:107]\n"
    "v_mfma_f32_16x16x32_bf16 v[96:99], a[16:19], v[32:35], v[96:99]\n"
    "v_mfma_f32_16x16x32_bf16 v[100:103], a[96:99], v[32:35], v[100:103]\n"
    "v_mfma_f32_16x16x32_bf16 v[104:107], a[176:179], v[32:35], v[104:107]\n"
    "v_mfma_f32_16x16x32_bf16 v[96:99], a[20:23], v[32:35], v[96:99]\n"
    "v_mfma_f32_16x16x32_bf16 v[100:103], a[100:103], v[32:35], v[100:103]\n"
    "v_mfma_f32_16x16x32_bf16 v[104:107], a[180:183], v[32:35], v[104:107]\n"
    "v_mfma_f32_16x16x32_bf16 v[96:99], a[24:27], v[32:35], v[96:99]\n"
    "v_mfma_f32_16x16x32_bf16 v[100:103], a[104:107], v[32:35], v[100:103]\n"
    "v_mfma_f32_16x16x32_bf16 v[104:107], a[184:187], v[32:35], v[104:107]\n"
    "v_mfma_f32_16x16x32_bf16 v[96:99], a[28:31], v[32:35], v[96:99]\n"
    "v_mfma_f32_16x16x32_bf16 v[100:103], a[108:111], v[32:35], v[100:103]\n"
    "v_mfma_f32_16x16x32_bf16 v[104:107], a[188:191], v[32:35], v[104:107]\n"
    "v_mfma_f32_16x16x32_bf16 v[96:99], a[32:35], v[32:35], v[96:99]\n"
    "v_mfma_f32_16x16x32_bf16 v[100:103], a[112:115], v[32:35], v[100:103]\n"
    "v_mfma_f32_16x16x32_bf16 v[104:107], a[192:195], v[32:35], v[104:107]\n"
    "v_mfma_f32_16x16x32_bf16 v[96:99], a[36:39], v[32:35], v[96:99]\n"
    "v_mfma_f32_16x16x32_bf16 v[100:103], a[116:119], v[32:35], v[100:103]\n"
    "v_mfma_f32_16x16x32_bf16 v[104:107], a[196:199], v[32:35], v[104:107]\n"
    "v_mfma_f32_16x16x32_bf16 v[96:99], a[40:43], v[32:35], v[96:99]\n"
    "v_mfma_f32_16x16x32_bf16 v[100:103], a[120:123], v[32:35], v[100:103]\n"
    "v_mfma_f32_16x16x32_bf16 v[104:107], a[200:203], v[32:35], v[104:107]\n"
    "v_mfma_f32_16x16x32_bf16 v[96:99], a[44:47], v[32:35], v[96:99]\n"
    "v_mfma_f32_16x16x32_bf16 v[100:103], a[124:127], v[32:35], v[100:103]\n"
    "v_mfma_f32_16x16x32_bf16 v[104:107], a[204:207], v[32:35], v[104:107]\n"
    "v_mfma_f32_16x16x32_bf16 v[96:99], a[48:51], v[32:35], v[96:99]\n"
    "v_mfma_f32_16x16x32_bf16 v[100:103], a[128:131], v[32:35], v[100:103]\n"
    "v_mfma_f32_16x16x32_bf16 v[104:107], a[208:211], v[32:35], v[104:107]\n"
    "v_mfma_f32_16x16x32_bf16 v[96:99], a[52:55], v[32:35], v[96:99]\n"
    "v_mfma_f32_16x16x32_bf16 v[100:103], a[132:135], v[32:35], v[100:103]\n"
    "v_mfma_f32_16x16x32_bf16 v[104:107], a[212:215], v[32:35], v[104:107]\n"
    "v_mfma_f32_16x16x32_bf16 v[96:99], a[56:59], v[32:35], v[96:99]\n"
    "v_mfma_f32_16x16x32_bf16 v[100:103], a[136:139], v[32:35], v[100:103]\n"
    "v_mfma_f32_16x16x32_bf16 v[104:107], a[216:219], v[32:35], v[104:107]\n"
    "v_mfma_f32_16x16x32_bf16 v[96:99], a[60:63], v[32:35], v[96:99]\n"
    "v_mfma_f32_16x16x32_bf16 v[100:103], a[140:143], v[32:35], v[100:103]\n"
    "v_mfma_f32_16x16x32_bf16 v[104:107], a[220:223], v[32:35], v[104:107]\n"
    "v_mfma_f32_16x16x32_bf16 v[96:99], a[64:67], v[32:35], v[96:99]\n"
    "v_mfma_f32_16x16x32_bf16 v[100:103], a[144:147], v[32:35], v[100:103]\n"
    "v_mfma_f32_16x16x32_bf16 v[104:107], a[224:227], v[32:35], v[104:107]\n"
    "v_mfma_f32_16x16x32_bf16 v[96:99], a[68:71], v[32:35], v[96:99]\n"
    "v_mfma_f32_16x16x32_bf16 v[100:103], a[148:151], v[32:35], v[100:103]\n"
    "v_mfma_f32_16x16x32_bf16 v[104:107], a[228:231], v[32:35], v[104:107]\n"
    "v_mfma_f32_16x16x32_bf16 v[96:99], a[72:75], v[32:35], v[96:99]\n"
    "v_mfma_f32_16x16x32_bf16 v[100:103], a[152:155], v[32:35], v[100:103]\n"
    "v_mfma_f32_16x16x32_bf16 v[104:107], a[232:235], v[32:35], v[104:107]\n"
    "v_mfma_f32_16x16x32_bf16 v[96:99], a[76:79], v[32:35], v[96:99]\n"
    "v_mfma_f32_16x16x32_bf16 v[100:103], a[156:159], v[32:35], v[100:103]\n"
    "v_mfma_f32_16x16x32_bf16 v[104:107], a[236:239], v[32:35], v[104:107]\n"
    "s_lshl_b32 s35, s18, 13\n"
    "v_add_u32 v8, s35, v228\n"
    "ds_write_b128 v8, v[36:39] offset:40960\n"
    "ds_write_b128 v8, v[36:39] offset:41984\n"
    "ds_write_b128 v8, v[36:39] offset:43008\n"
    "ds_write_b128 v8, v[36:39] offset:44032\n"
    "ds_write_b128 v8, v[36:39] offset:45056\n"
    "ds_write_b128 v8, v[36:39] offset:46080\n"
    "ds_write_b128 v8, v[36:39] offset:47104\n"
    "ds_write_b128 v8, v[36:39] offset:48128\n"
    "s_nop 7\n"
    "s_nop 7\n"
    "s_mov_b64 exec, s[16:17]\n"
    "v_accvgpr_write_b32 a0, v32\n"
    "v_accvgpr_write_b32 a1, v32\n"
    "v_accvgpr_write_b32 a2, v32\n"
    "v_accvgpr_write_b32 a3, v32\n"
    "v_accvgpr_write_b32 a4, v32\n"
    "v_accvgpr_write_b32 a5, v32\n"
    "v_accvgpr_write_b32 a6, v32\n"
    "v_accvgpr_write_b32 a7, v32\n"
    "v_accvgpr_write_b32 a8, v32\n"
    "v_accvgpr_write_b32 a9, v32\n"
    "v_accvgpr_write_b32 a10, v32\n"
    "v_accvgpr_write_b32 a11, v32\n"
    "v_accvgpr_write_b32 a12, v32\n"
    "v_accvgpr_write_b32 a13, v32\n"
    "v_accvgpr_write_b32 a14, v32\n"
    "v_accvgpr_write_b32 a15, v32\n"
    "v_accvgpr_write_b32 a16, v32\n"
    "v_accvgpr_write_b32 a17, v32\n"
    "v_accvgpr_write_b32 a18, v32\n"
    "v_accvgpr_write_b32 a19, v32\n"
    "v_accvgpr_write_b32 a20, v32\n"
    "v_accvgpr_write_b32 a21, v32\n"
    "v_accvgpr_write_b32 a22, v32\n"
    "v_accvgpr_write_b32 a23, v32\n"
    "v_accvgpr_write_b32 a24, v32\n"
    "v_accvgpr_write_b32 a25, v32\n"
    "v_accvgpr_write_b32 a26, v32\n"
    "v_accvgpr_write_b32 a27, v32\n"
    "v_accvgpr_write_b32 a28, v32\n"
    "v_accvgpr_write_b32 a29, v32\n"
    "v_accvgpr_write_b32 a30, v32\n"
    "v_accvgpr_write_b32 a31, v32\n"
    "v_accvgpr_write_b32 a32, v32\n"
    "v_accvgpr_write_b32 a33, v32\n"
    "v_accvgpr_write_b32 a34, v32\n"
    "v_accvgpr_write_b32 a35, v32\n"
    "v_accvgpr_write_b32 a36, v32\n"
    "v_accvgpr_write_b32 a37, v32\n"
    "v_accvgpr_write_b32 a38, v32\n"
    "v_accvgpr_write_b32 a39, v32\n"
    "v_accvgpr_write_b32 a40, v32\n"
    "v_accvgpr_write_b32 a41, v32\n"
    "v_accvgpr_write_b32 a42, v32\n"
    "v_accvgpr_write_b32 a43, v32\n"
    "v_accvgpr_write_b32 a44, v32\n"
    "v_accvgpr_write_b32 a45, v32\n"
    "v_accvgpr_write_b32 a46, v32\n"
    "v_accvgpr_write_b32 a47, v32\n"
    "v_accvgpr_write_b32 a48, v32\n"
    "v_accvgpr_write_b32 a49, v32\n"
    "v_accvgpr_write_b32 a50, v32\n"
    "v_accvgpr_write_b32 a51, v32\n"
    "v_accvgpr_write_b32 a52, v32\n"
    "v_accvgpr_write_b32 a53, v32\n"
    "v_accvgpr_write_b32 a54, v32\n"
    "v_accvgpr_write_b32 a55, v32\n"
    "v_accvgpr_write_b32 a56, v32\n"
    "v_accvgpr_write_b32 a57, v32\n"
    "v_accvgpr_write_b32 a58, v32\n"
    "v_accvgpr_write_b32 a59, v32\n"
    "v_accvgpr_write_b32 a60, v32\n"
    "v_accvgpr_write_b32 a61, v32\n"
    "v_accvgpr_write_b32 a62, v32\n"
    "v_accvgpr_write_b32 a63, v32\n"
    "v_accvgpr_write_b32 a64, v32\n"
    "v_accvgpr_write_b32 a65, v32\n"
    "v_accvgpr_write_b32 a66, v32\n"
    "v_accvgpr_write_b32 a67, v32\n"
    "v_accvgpr_write_b32 a68, v32\n"
    "v_accvgpr_write_b32 a69, v32\n"
    "v_accvgpr_write_b32 a70, v32\n"
    "v_accvgpr_write_b32 a71, v32\n"
    "v_accvgpr_write_b32 a72, v32\n"
    "v_accvgpr_write_b32 a73, v32\n"
    "v_accvgpr_write_b32 a74, v32\n"
    "v_accvgpr_write_b32 a75, v32\n"
    "v_accvgpr_write_b32 a76, v32\n"
    "v_accvgpr_write_b32 a77, v32\n"
    "v_accvgpr_write_b32 a78, v32\n"
    "v_accvgpr_write_b32 a79, v32\n"
    "s_mov_b64 exec, -1\n"
    "s_mov_b32 s30, -2\n"
    "s_mov_b32 s30, 0\n"
    "s_mov_b32 s31, 1\n"
    "s_lshl_b32 s34, s18, 11\n"
    "s_add_u32 s32, s20, s19\n"
    "s_mov_b32 s37, 0x3ea52b5c\n"
    "s_mov_b64 s[38:39], 0\n"
    "s_mov_b64 s[40:41], 0\n"
    "s_mov_b32 s36, 0\n"
    "v_mov_b32 v108, 0\n"
    "s_mov_b32 s42, 0xaaaaaaaa\n"
    "s_mov_b32 s43, 0xaaaaaaaa\n"
    "s_waitcnt lgkmcnt(0)\n"
    ".Lb1_top_%=:\n"
    "s_cmp_lg_u32 s18, 0\n"
    "s_cbranch_scc1 .Lb1_pd4_%=\n"
    "s_add_u32 s29, s26, 252\n"
    "v_min3_i32 v108, v108, v109, v110\n"
    "v_min_i32 v108, v108, v111\n"
    "v_cmp_gt_i32 vcc, s31, v108\n"
    "s_nop 0\n"
    "s_cbranch_vccz .Lb1_pd4_%=\n"
    ".Lb1_rt5_%=:\n"
    "s_memrealtime s[26:27]\n"
    "s_waitcnt lgkmcnt(0)\n"
    "s_sub_u32 s28, s36, s26\n"
    "s_cmp_lt_u32 s28, 600\n"
    "s_cbranch_scc1 .Lb1_rt5_%=\n"
    ".Lb1_po3_%=:\n"
    "buffer_load_dwordx4 v[108:111], v229, s[0:3], s21 offen sc1\n"
    "s_memrealtime s[26:27]\n"
    "s_waitcnt vmcnt(0) lgkmcnt(0)\n"
    "s_add_u32 s29, s26, 252\n"
    "v_min3_i32 v108, v108, v109, v110\n"
    "v_min_i32 v108, v108, v111\n"
    "v_cmp_gt_i32 vcc, s31, v108\n"
    "s_nop 0\n"
    "s_cbranch_vccz .Lb1_pd4_%=\n"
    "s_add_u32 s24, s24, 1\n"
    "s_cmp_lt_u32 s24, 0x40000\n"
    "s_cbranch_scc1 .Lb1_sp6_%=\n"
    "v_mov_b32 v108, 60\n"
    "s_mov_b32 s28, 33728\n"
    "buffer_store_dword v108, off, s[0:3], s28 sc1\n"
    "s_waitcnt vmcnt(0)\n"
    "s_branch .Lb1_pd4_%=\n"
    ".Lb1_sp6_%=:\n"
    "s_sleep 6\n"
    "s_branch .Lb1_po3_%=\n"
    ".Lb1_pd4_%=:\n"
    "s_barrier\n"
    "s_mov_b32 s36, s29\n"
    ".Lb1_mk0x6_%=:\n"
    "s_mov_b32 s33, s32\n"
    "s_add_u32 m0, s34, 0\n"
    "s_nop 0\n"
    "buffer_load_dwordx4 v228, s[0:3], s32 offen sc1 lds\n"
    "buffer_load_dwordx4 v228, s[0:3], s32 offen offset:1024 sc1 lds\n"
    "s_add_u32 m0, s34, 8192\n"
    "s_add_u32 s32, s32, 0x4000\n"
    "buffer_load_dwordx4 v228, s[0:3], s32 offen sc1 lds\n"
    "buffer_load_dwordx4 v228, s[0:3], s32 offen offset:1024 sc1 lds\n"
    "s_add_u32 m0, s34, 16384\n"
    "s_add_u32 s32, s32, 0x4000\n"
    "buffer_load_dwordx4 v228, s[0:3], s32 offen sc1 lds\n"
    "buffer_load_dwordx4 v228, s[0:3], s32 offen offset:1024 sc1 lds\n"
    "s_add_u32 m0, s34, 24576\n"
    "s_add_u32 s32, s32, 0x4000\n"
    "buffer_load_dwordx4 v228, s[0:3], s32 offen sc1 lds\n"
    "buffer_load_dwordx4 v228, s[0:3], s32 offen offset:1024 sc1 lds\n"
    "s_add_u32 m0, s34, 32768\n"
    "s_add_u32 s32, s32, 0x4000\n"
    "buffer_load_dwordx4 v228, s[0:3], s32 offen sc1 lds\n"
    "buffer_load_dwordx4 v228, s[0:3], s32 offen offset:1024 sc1 lds\n"
    "s_waitcnt vmcnt(8)\n"
    "s_barrier\n"
    ".Lb1_mk1x7_%=:\n"
    "ds_read_b128 v[32:35], v224 offset:0\n"
    "ds_read_b128 v[36:39], v224 offset:1024\n"
    "ds_read_b128 v[40:43], v225 offset:0\n"
    "ds_read_b128 v[44:47], v225 offset:1024\n"
    "s_waitcnt lgkmcnt(2)\n"
    "v_mfma_f32_16x16x32_bf16 v[24:27], v[32:35], v[32:35], 0\n"
    "ds_read_b128 v[48:51], v226 offset:0\n"
    "ds_read_b128 v[52:55], v226 offset:1024\n"
    "v_mfma_f32_16x16x32_bf16 v[28:31], v[36:39], v[36:39], 0\n"
    "v_mfma_f32_16x16x32_bf16 v[0:3], a[0:3], v[32:35], 0\n"
    "v_mfma_f32_16x16x32_bf16 v[4:7], a[0:3], v[36:39], 0\n"
    "v_mfma_f32_16x16x32_bf16 v[8:11], a[80:83], v[32:35], 0\n"
    "s_add_u32 s35, s30, 1\n"
    "s_lshl_b32 s32, s35, 19\n"
    "s_lshl_b32 s29, s35, 11\n"
    "v_mfma_f32_16x16x32_bf16 v[12:15], a[80:83], v[36:39], 0\n"
    "s_xor_b32 s29, s29, s19\n"
    "s_and_b32 s29, s29, 0x2000\n"
    "s_add_u32 s32, s32, s29\n"
    "v_mfma_f32_16x16x32_bf16 v[16:19], a[160:163], v[32:35], 0\n"
    "s_add_u32 s32, s32, s20\n"
    "v_xor_b32 v232, 0x800, v232\n"
    "v_xor_b32 v233, 0x800, v233\n"
    "v_mfma_f32_16x16x32_bf16 v[20:23], a[160:163], v[36:39], 0\n"
    "s_waitcnt lgkmcnt(2)\n"
    "v_xor_b32 v235, 0x800, v235\n"
    "v_xor_b32 v236, 0x800, v236\n"
    "v_mfma_f32_16x16x32_bf16 v[0:3], a[4:7], v[40:43], v[0:3]\n"
    "ds_read_b128 v[56:59], v227 offset:0\n"
    "ds_read_b128 v[60:63], v227 offset:1024\n"
    "v_mfma_f32_16x16x32_bf16 v[4:7], a[4:7], v[44:47], v[4:7]\n"
    "v_xor_b32 v237, 0x800, v237\n"
    "v_xor_b32 v238, 0x800, v238\n"
    "v_mfma_f32_16x16x32_bf16 v[8:11], a[84:87], v[40:43], v[8:11]\n"
    "v_xor_b32 v239, 0x800, v239\n"
    "v_xor_b32 v240, 0x800, v240\n"
    "v_mfma_f32_16x16x32_bf16 v[12:15], a[84:87], v[44:47], v[12:15]\n"
    "v_mov_b32 v116, s31\n"
    "v_mov_b32 v117, s31\n"
    "v_mfma_f32_16x16x32_bf16 v[16:19], a[164:167], v[40:43], v[16:19]\n"
    "v_mov_b32 v118, s31\n"
    "v_mov_b32 v119, s31\n"
    "v_mfma_f32_16x16x32_bf16 v[20:23], a[164:167], v[44:47], v[20:23]\n"
    "s_waitcnt lgkmcnt(2)\n"
    "v_mfma_f32_16x16x32_bf16 v[0:3], a[8:11], v[48:51], v[0:3]\n"
    "s_waitcnt vmcnt(6)\n"
    "s_barrier\n"
    "ds_read_b128 v[64:67], v224 offset:8192\n"
    "v_mfma_f32_16x16x32_bf16 v[4:7], a[8:11], v[52:55], v[4:7]\n"
    "ds_read_b128 v[68:71], v224 offset:9216\n"
    "v_mfma_f32_16x16x32_bf16 v[8:11], a[88:91], v[48:51], v[8:11]\n"
    "v_mfma_f32_16x16x32_bf16 v[12:15], a[88:91], v[52:55], v[12:15]\n"
    "v_mfma_f32_16x16x32_bf16 v[16:19], a[168:171], v[48:51], v[16:19]\n"
    "v_mfma_f32_16x16x32_bf16 v[20:23], a[168:171], v[52:55], v[20:23]\n"
    "s_waitcnt lgkmcnt(2)\n"
    "v_mfma_f32_16x16x32_bf16 v[0:3], a[12:15], v[56:59], v[0:3]\n"
    "ds_read_b128 v[72:75], v225 offset:8192\n"
    "ds_read_b128 v[76:79], v225 offset:9216\n"
    "v_mfma_f32_16x16x32_bf16 v[4:7], a[12:15], v[60:63], v[4:7]\n"
    "v_mfma_f32_16x16x32_bf16 v[8:11], a[92:95], v[56:59], v[8:11]\n"
    "v_mfma_f32_16x16x32_bf16 v[12:15], a[92:95], v[60:63], v[12:15]\n"
    "v_mfma_f32_16x16x32_bf16 v[16:19], a[172:175], v[56:59], v[16:19]\n"
    "v_mfma_f32_16x16x32_bf16 v[20:23], a[172:175], v[60:63], v[20:23]\n"
    "s_waitcnt lgkmcnt(2)\n"
    "v_mfma_f32_16x16x32_bf16 v[24:27], v[64:67], v[64:67], v[24:27]\n"
    "ds_read_b128 v[80:83], v226 offset:8192\n"
    "ds_read_b128 v[84:87], v226 offset:9216\n"
    "v_mfma_f32_16x16x32_bf16 v[28:31], v[68:71], v[68:71], v[28:31]\n"
    "v_mfma_f32_16x16x32_bf16 v[0:3], a[16:19], v[64:67], v[0:3]\n"
    "v_mfma_f32_16x16x32_bf16 v[4:7], a[16:19], v[68:71], v[4:7]\n"
    "v_mfma_f32_16x16x32_bf16 v[8:11], a[96:99], v[64:67], v[8:11]\n"
    "v_mfma_f32_16x16x32_bf16 v[12:15], a[96:99], v[68:71], v[12:15]\n"
    "v_mfma_f32_16x16x32_bf16 v[16:19], a[176:179], v[64:67], v[16:19]\n"
    "v_mfma_f32_16x16x32_bf16 v[20:23], a[176:179], v[68:71], v[20:23]\n"
    "s_waitcnt lgkmcnt(2)\n"
    "v_mfma_f32_16x16x32_bf16 v[0:3], a[20:23], v[72:75], v[0:3]\n"
    "ds_read_b128 v[88:91], v227 offset:8192\n"
    "ds_read_b128 v[92:95], v227 offset:9216\n"
    "v_mfma_f32_16x16x32_bf16 v[4:7], a[20:23], v[76:79], v[4:7]\n"
    "v_mfma_f32_16x16x32_bf16 v[8:11], a[100:103], v[72:75], v[8:11]\n"
    "v_mfma_f32_16x16x32_bf16 v[12:15], a[100:103], v[76:79], v[12:15]\n"
    "v_mfma_f32_16x16x32_bf16 v[16:19], a[180:183], v[72:75], v[16:19]\n"
    "v_mfma_f32_16x16x32_bf16 v[20:23], a[180:183], v[76:79], v[20:23]\n"
    "s_waitcnt lgkmcnt(2)\n"
    "v_mfma_f32_16x16x32_bf16 v[0:3], a[24:27], v[80:83], v[0:3]\n"
    "s_waitcnt vmcnt(4)\n"
    "s_barrier\n"
    "ds_read_b128 v[32:35], v224 offset:16384\n"
    "v_mfma_f32_16x16x32_bf16 v[4:7], a[24:27], v[84:87], v[4:7]\n"
    "ds_read_b128 v[36:39], v224 offset:17408\n"
    "v_mfma_f32_16x16x32_bf16 v[8:11], a[104:107], v[80:83], v[8:11]\n"
    "v_mfma_f32_16x16x32_bf16 v[12:15], a[104:107], v[84:87], v[12:15]\n"
    "v_mfma_f32_16x16x32_bf16 v[16:19], a[184:187], v[80:83], v[16:19]\n"
    "v_mfma_f32_16x16x32_bf16 v[20:23], a[184:187], v[84:87], v[20:23]\n"
    "s_waitcnt lgkmcnt(2)\n"
    "v_mfma_f32_16x16x32_bf16 v[0:3], a[28:31], v[88:91], v[0:3]\n"
    "ds_read_b128 v[40:43], v225 offset:16384\n"
    "ds_read_b128 v[44:47], v225 offset:17408\n"
    "v_mfma_f32_16x16x32_bf16 v[4:7], a[28:31], v[92:95], v[4:7]\n"
    "v_mfma_f32_16x16x32_bf16 v[8:11], a[108:111], v[88:91], v[8:11]\n"
    "v_mfma_f32_16x16x32_bf16 v[12:15], a[108:111], v[92:95], v[12:15]\n"
    "v_mfma_f32_16x16x32_bf16 v[16:19], a[188:191], v[88:91], v[16:19]\n"
    "v_mfma_f32_16x16x32_bf16 v[20:23], a[188:191], v[92:95], v[20:23]\n"
    "s_waitcnt lgkmcnt(2)\n"
    "v_mfma_f32_16x16x32_bf16 v[24:27], v[32:35], v[32:35], v[24:27]\n"
    "ds_read_b128 v[48:51], v226 offset:16384\n"
    "ds_read_b128 v[52:55], v226 offset:17408\n"
    "v_mfma_f32_16x16x32_bf16 v[28:31], v[36:39], v[36:39], v[28:31]\n"
    "v_mfma_f32_16x16x32_bf16 v[0:3], a[32:35], v[32:35], v[0:3]\n"
    "v_mfma_f32_16x16x32_bf16 v[4:7], a[32:35], v[36:39], v[4:7]\n"
    "v_mfma_f32_16x16x32_bf16 v[8:11], a[112:115], v[32:35], v[8:11]\n"
    "v_mfma_f32_16x16x32_bf16 v[12:15], a[112:115], v[36:39], v[12:15]\n"
    "v_mfma_f32_16x16x32_bf16 v[16:19], a[192:195], v[32:35], v[16:19]\n"
    "v_mfma_f32_16x16x32_bf16 v[20:23], a[192:195], v[36:39], v[20:23]\n"
    "s_waitcnt lgkmcnt(2)\n"
    "v_mfma_f32_16x16x32_bf16 v[0:3], a[36:39], v[40:43], v[0:3]\n"
    "ds_read_b128 v[56:59], v227 offset:16384\n"
    "ds_read_b128 v[60:63], v227 offset:17408\n"
    "v_mfma_f32_16x16x32_bf16 v[4:7], a[36:39], v[44:47], v[4:7]\n"
    "v_mfma_f32_16x16x32_bf16 v[8:11], a[116:119], v[40:43], v[8:11]\n"
    "v_mfma_f32_16x16x32_bf16 v[12:15], a[116:119], v[44:47], v[12:15]\n"
    "v_mfma_f32_16x16x32_bf16 v[16:19], a[196:199], v[40:43], v[16:19]\n"
    "v_mfma_f32_16x16x32_bf16 v[20:23], a[196:199], v[44:47], v[20:23]\n"
    "s_waitcnt lgkmcnt(2)\n"
    "v_mfma_f32_16x16x32_bf16 v[0:3], a[40:43], v[48:51], v[0:3]\n"
    "s_waitcnt vmcnt(2)\n"
    "s_barrier\n"
    "ds_read_b128 v[64:67], v224 offset:24576\n"
    "v_mfma_f32_16x16x32_bf16 v[4:7], a[40:43], v[52:55], v[4:7]\n"
    "ds_read_b128 v[68:71], v224 offset:25600\n"
    "ds_read_b128 v[72:75], v225 offset:24576\n"
    "v_mfma_f32_16x16x32_bf16 v[8:11], a[120:123], v[48:51], v[8:11]\n"
    "v_mfma_f32_16x16x32_bf16 v[12:15], a[120:123], v[52:55], v[12:15]\n"
    "ds_read_b128 v[76:79], v225 offset:25600\n"
    "v_mfma_f32_16x16x32_bf16 v[16:19], a[200:203], v[48:51], v[16:19]\n"
    "ds_read_b128 v[80:83], v226 offset:24576\n"
    "ds_read_b128 v[84:87], v226 offset:25600\n"
    "v_mfma_f32_16x16x32_bf16 v[20:23], a[200:203], v[52:55], v[20:23]\n"
    "s_waitcnt lgkmcnt(6)\n"
    "v_mfma_f32_16x16x32_bf16 v[0:3], a[44:47], v[56:59], v[0:3]\n"
    "ds_read_b128 v[88:91], v227 offset:24576\n"
    "v_mfma_f32_16x16x32_bf16 v[4:7], a[44:47], v[60:63], v[4:7]\n"
    "ds_read_b128 v[92:95], v227 offset:25600\n"
    "s_waitcnt vmcnt(0)\n"
    "v_mfma_f32_16x16x32_bf16 v[8:11], a[124:127], v[56:59], v[8:11]\n"
    "s_barrier\n"
    "ds_read_b128 v[32:35], v224 offset:32768\n"
    "v_mfma_f32_16x16x32_bf16 v[12:15], a[124:127], v[60:63], v[12:15]\n"
    "ds_read_b128 v[36:39], v224 offset:33792\n"
    "v_mfma_f32_16x16x32_bf16 v[16:19], a[204:207], v[56:59], v[16:19]\n"
    "ds_read_b128 v[40:43], v225 offset:32768\n"
    "v_mfma_f32_16x16x32_bf16 v[20:23], a[204:207], v[60:63], v[20:23]\n"
    "s_waitcnt lgkmcnt(9)\n"
    "ds_read_b128 v[44:47], v225 offset:33792\n"
    "v_mfma_f32_16x16x32_bf16 v[24:27], v[64:67], v[64:67], v[24:27]\n"
    "ds_read_b128 v[48:51], v226 offset:32768\n"
    "v_mfma_f32_16x16x32_bf16 v[28:31], v[68:71], v[68:71], v[28:31]\n"
    "ds_read_b128 v[52:55], v226 offset:33792\n"
    "v_mfma_f32_16x16x32_bf16 v[0:3], a[48:51], v[64:67], v[0:3]\n"
    "ds_read_b128 v[56:59], v227 offset:32768\n"
    "v_mfma_f32_16x16x32_bf16 v[4:7], a[48:51], v[68:71], v[4:7]\n"
    "s_waitcnt lgkmcnt(11)\n"
    "ds_read_b128 v[60:63], v227 offset:33792\n"
    "v_mfma_f32_16x16x32_bf16 v[0:3], a[52:55], v[72:75], v[0:3]\n"
    "v_mfma_f32_16x16x32_bf16 v[4:7], a[52:55], v[76:79], v[4:7]\n"
    "s_waitcnt lgkmcnt(10)\n"
    "v_mfma_f32_16x16x32_bf16 v[0:3], a[56:59], v[80:83], v[0:3]\n"
    "v_mfma_f32_16x16x32_bf16 v[4:7], a[56:59], v[84:87], v[4:7]\n"
    "s_waitcnt lgkmcnt(8)\n"
    "v_mfma_f32_16x16x32_bf16 v[0:3], a[60:63], v[88:91], v[0:3]\n"
    "v_mfma_f32_16x16x32_bf16 v[4:7], a[60:63], v[92:95], v[4:7]\n"
    "s_waitcnt lgkmcnt(6)\n"
    "v_mfma_f32_16x16x32_bf16 v[24:27], v[32:35], v[32:35], v[24:27]\n"
    "v_mfma_f32_16x16x32_bf16 v[28:31], v[36:39], v[36:39], v[28:31]\n"
    "v_mfma_f32_16x16x32_bf16 v[0:3], a[64:67], v[32:35], v[0:3]\n"
    "v_mfma_f32_16x16x32_bf16 v[4:7], a[64:67], v[36:39], v[4:7]\n"
    "s_waitcnt lgkmcnt(4)\n"
    "v_mfma_f32_16x16x32_bf16 v[0:3], a[68:71], v[40:43], v[0:3]\n"
    "v_mfma_f32_16x16x32_bf16 v[4:7], a[68:71], v[44:47], v[4:7]\n"
    "s_waitcnt lgkmcnt(2)\n"
    "v_mfma_f32_16x16x32_bf16 v[0:3], a[72:75], v[48:51], v[0:3]\n"
    "v_mfma_f32_16x16x32_bf16 v[4:7], a[72:75], v[52:55], v[4:7]\n"
    "s_waitcnt lgkmcnt(0)\n"
    "v_mfma_f32_16x16x32_bf16 v[0:3], a[76:79], v[56:59], v[0:3]\n"
    "v_mfma_f32_16x16x32_bf16 v[4:7], a[76:79], v[60:63], v[4:7]\n"
    "ds_write_b128 v241, v[24:27]\n"
    "v_mfma_f32_16x16x32_bf16 v[8:11], a[128:131], v[64:67], v[8:11]\n"
    "ds_write_b128 v241, v[28:31] offset:1024\n"
    "v_mfma_f32_16x16x32_bf16 v[12:15], a[128:131], v[68:71], v[12:15]\n"
    "ds_write2st64_b32 v243, v0, v4 offset1:1\n"
    "v_mfma_f32_16x16x32_bf16 v[8:11], a[132:135], v[72:75], v[8:11]\n"
    "s_waitcnt lgkmcnt(0)\n"
    "s_barrier\n"
    "ds_read2st64_b32 v[24:25], v242 offset1:8\n"
    "v_mfma_f32_16x16x32_bf16 v[12:15], a[132:135], v[76:79], v[12:15]\n"
    "ds_read2st64_b32 v[26:27], v242 offset0:16 offset1:24\n"
    "ds_read_b32 v108, v244\n"
    "v_mfma_f32_16x16x32_bf16 v[8:11], a[136:139], v[80:83], v[8:11]\n"
    "v_mfma_f32_16x16x32_bf16 v[12:15], a[136:139], v[84:87], v[12:15]\n"
    "v_mfma_f32_16x16x32_bf16 v[8:11], a[140:143], v[88:91], v[8:11]\n"
    "v_mfma_f32_16x16x32_bf16 v[12:15], a[140:143], v[92:95], v[12:15]\n"
    "v_mfma_f32_16x16x32_bf16 v[8:11], a[144:147], v[32:35], v[8:11]\n"
    "s_waitcnt lgkmcnt(0)\n"
    "v_add_f32 v24, v24, v26\n"
    "v_add_f32 v25, v25, v27\n"
    "v_mfma_f32_16x16x32_bf16 v[12:15], a[144:147], v[36:39], v[12:15]\n"
    "v_add_f32 v112, v24, v25\n"
    "v_mul_f32 v108, 0x3acccccd, v108\n"
    "v_mfma_f32_16x16x32_bf16 v[8:11], a[148:151], v[40:43], v[8:11]\n"
    "v_mul_f32 v112, 0x3acccccd, v112\n"
    "v_fma_f32 v112, -v108, v108, v112\n"
    "v_mfma_f32_16x16x32_bf16 v[12:15], a[148:151], v[44:47], v[12:15]\n"
    "v_max_f32 v112, 0, v112\n"
    "v_cvt_pk_bf16_f32 v108, v108, v112\n"
    "v_mfma_f32_16x16x32_bf16 v[8:11], a[152:155], v[48:51], v[8:11]\n"
    "v_and_b32 v112, 0xffff0000, v108\n"
    "v_lshlrev_b32 v108, 16, v108\n"
    "v_mfma_f32_16x16x32_bf16 v[12:15], a[152:155], v[52:55], v[12:15]\n"
    "v_add_f32 v112, 0x3727c5ac, v112\n"
    "v_mov_b32 v110, v108\n"
    "v_mfma_f32_16x16x32_bf16 v[8:11], a[156:159], v[56:59], v[8:11]\n"
    "v_cvt_pk_bf16_f32 v112, v112, v112\n"
    "v_lshlrev_b32 v112, 16, v112\n"
    "v_mfma_f32_16x16x32_bf16 v[12:15], a[156:159], v[60:63], v[12:15]\n"
    "v_sqrt_f32 v112, v112\n"
    "v_mfma_f32_16x16x32_bf16 v[16:19], a[208:211], v[64:67], v[16:19]\n"
    "v_permlane32_swap_b32 v108, v110\n"
    "v_mfma_f32_16x16x32_bf16 v[20:23], a[208:211], v[68:71], v[20:23]\n"
    "v_cvt_pk_bf16_f32 v112, v112, v112\n"
    "v_lshlrev_b32 v112, 16, v112\n"
    "v_mfma_f32_16x16x32_bf16 v[16:19], a[212:215], v[72:75], v[16:19]\n"
    "v_rcp_f32 v112, v112\n"
    "v_mfma_f32_16x16x32_bf16 v[20:23], a[212:215], v[76:79], v[20:23]\n"
    "v_mov_b32 v114, v112\n"
    "v_mfma_f32_16x16x32_bf16 v[16:19], a[216:219], v[80:83], v[16:19]\n"
    "s_nop 0\n"
    "v_permlane32_swap_b32 v112, v114\n"
    "v_mfma_f32_16x16x32_bf16 v[20:23], a[216:219], v[84:87], v[20:23]\n"
    "v_fma_f32 v0, v96, -v108, v0\n"
    "v_fma_f32 v1, v97, -v108, v1\n"
    "v_mfma_f32_16x16x32_bf16 v[16:19], a[220:223], v[88:91], v[16:19]\n"
    "v_fma_f32 v2, v98, -v108, v2\n"
    "v_fma_f32 v3, v99, -v108, v3\n"
    "v_mfma_f32_16x16x32_bf16 v[20:23], a[220:223], v[92:95], v[20:23]\n"
    "v_fma_f32 v4, v96, -v110, v4\n"
    "v_fma_f32 v5, v97, -v110, v5\n"
    "v_mfma_f32_16x16x32_bf16 v[16:19], a[224:227], v[32:35], v[16:19]\n"
    "v_fma_f32 v6, v98, -v110, v6\n"
    "v_fma_f32 v7, v99, -v110, v7\n"
    "v_mfma_f32_16x16x32_bf16 v[20:23], a[224:227], v[36:39], v[20:23]\n"
    "v_mul_f32 v0, v0, v112\n"
    "v_mul_f32 v1, v1, v112\n"
    "v_mfma_f32_16x16x32_bf16 v[16:19], a[228:231], v[40:43], v[16:19]\n"
    "v_mul_f32 v2, v2, v112\n"
    "v_mul_f32 v3, v3, v112\n"
    "v_mfma_f32_16x16x32_bf16 v[20:23], a[228:231], v[44:47], v[20:23]\n"
    "v_mul_f32 v4, v4, v114\n"
    "v_mul_f32 v5, v5, v114\n"
    "v_mfma_f32_16x16x32_bf16 v[16:19], a[232:235], v[48:51], v[16:19]\n"
    "v_mul_f32 v6, v6, v114\n"
    "v_mul_f32 v7, v7, v114\n"
    "v_mfma_f32_16x16x32_bf16 v[20:23], a[232:235], v[52:55], v[20:23]\n"
    "v_cvt_pk_bf16_f32 v0, v0, v1\n"
    "v_cvt_pk_bf16_f32 v1, v2, v3\n"
    "v_mfma_f32_16x16x32_bf16 v[16:19], a[236:239], v[56:59], v[16:19]\n"
    "ds_write_b64 v231, v[0:1] offset:0\n"
    "v_mfma_f32_16x16x32_bf16 v[20:23], a[236:239], v[60:63], v[20:23]\n"
    "v_cvt_pk_bf16_f32 v4, v4, v5\n"
    "v_cvt_pk_bf16_f32 v5, v6, v7\n"
    ".Lb1_mk2x8_%=:\n"
    "ds_write_b64 v231, v[4:5] offset:1024\n"
    "v_pk_fma_f32 v[8:9], v[100:101], v[108:109], v[8:9] op_sel_hi:[1,0,1] neg_lo:[0,1,0] neg_hi:[0,1,0]\n"
    "v_pk_fma_f32 v[10:11], v[102:103], v[108:109], v[10:11] op_sel_hi:[1,0,1] neg_lo:[0,1,0] neg_hi:[0,1,0]\n"
    "v_pk_fma_f32 v[12:13], v[100:101], v[110:111], v[12:13] op_sel_hi:[1,0,1] neg_lo:[0,1,0] neg_hi:[0,1,0]\n"
    "v_pk_fma_f32 v[14:15], v[102:103], v[110:111], v[14:15] op_sel_hi:[1,0,1] neg_lo:[0,1,0] neg_hi:[0,1,0]\n"
    "v_pk_mul_f32 v[8:9], v[8:9], v[112:113] op_sel_hi:[1,0]\n"
    "v_pk_mul_f32 v[10:11], v[10:11], v[112:113] op_sel_hi:[1,0]\n"
    "v_pk_mul_f32 v[12:13], v[12:13], v[114:115] op_sel_hi:[1,0]\n"
    "v_pk_mul_f32 v[14:15], v[14:15], v[114:115] op_sel_hi:[1,0]\n"
    "v_cvt_pk_bf16_f32 v8, v8, v9\n"
    "v_cvt_pk_bf16_f32 v9, v10, v11\n"
    "ds_write_b64 v232, v[8:9] offset:0\n"
    "v_cvt_pk_bf16_f32 v12, v12, v13\n"
    "v_cvt_pk_bf16_f32 v13, v14, v15\n"
    "ds_write_b64 v232, v[12:13] offset:1024\n"
    "v_pk_fma_f32 v[16:17], v[104:105], v[108:109], v[16:17] op_sel_hi:[1,0,1] neg_lo:[0,1,0] neg_hi:[0,1,0]\n"
    "v_pk_fma_f32 v[18:19], v[106:107], v[108:109], v[18:19] op_sel_hi:[1,0,1] neg_lo:[0,1,0] neg_hi:[0,1,0]\n"
    "v_pk_fma_f32 v[20:21], v[104:105], v[110:111], v[20:21] op_sel_hi:[1,0,1] neg_lo:[0,1,0] neg_hi:[0,1,0]\n"
    "v_pk_fma_f32 v[22:23], v[106:107], v[110:111], v[22:23] op_sel_hi:[1,0,1] neg_lo:[0,1,0] neg_hi:[0,1,0]\n"
    "v_pk_mul_f32 v[16:17], v[16:17], v[112:113] op_sel_hi:[1,0]\n"
    "v_pk_mul_f32 v[18:19], v[18:19], v[112:113] op_sel_hi:[1,0]\n"
    "v_pk_mul_f32 v[20:21], v[20:21], v[114:115] op_sel_hi:[1,0]\n"
    "v_pk_mul_f32 v[22:23], v[22:23], v[114:115] op_sel_hi:[1,0]\n"
    "v_cvt_pk_bf16_f32 v16, v16, v17\n"
    "v_cvt_pk_bf16_f32 v17, v18, v19\n"
    "ds_write_b64 v233, v[16:17] offset:0\n"
    "v_cvt_pk_bf16_f32 v20, v20, v21\n"
    "v_cvt_pk_bf16_f32 v21, v22, v23\n"
    "ds_write_b64 v233, v[20:21] offset:1024\n"
    "s_waitcnt lgkmcnt(0)\n"
    "s_barrier\n"
    ".Lb1_mk3x9_%=:\n"
    "ds_read_b128 v[32:35], v236 offset:1024\n"
    "ds_read_b128 v[44:47], v234 offset:0\n"
    "ds_read_b128 v[36:39], v235 offset:0\n"
    "ds_read_b128 v[48:51], v234 offset:1024\n"
    "ds_read_b128 v[40:43], v235 offset:1024\n"
    "ds_read_b64_tr_b16 v[52:53], v238 offset:1024\n"
    "ds_read_b64_tr_b16 v[54:55], v237 offset:0\n"
    "ds_read_b64_tr_b16 v[56:57], v237 offset:1024\n"
    "ds_read_b64_tr_b16 v[58:59], v240 offset:1024\n"
    "ds_read_b64_tr_b16 v[60:61], v239 offset:0\n"
    "ds_read_b64_tr_b16 v[62:63], v239 offset:1024\n"
    "s_waitcnt lgkmcnt(9)\n"
    "v_mfma_f32_16x16x32_bf16 v[64:67], v[32:35], v[44:47], 0\n"
    "s_waitcnt lgkmcnt(8)\n"
    "v_mfma_f32_16x16x32_bf16 v[68:71], v[36:39], v[44:47], 0\n"
    "s_waitcnt lgkmcnt(7)\n"
    "v_mfma_f32_16x16x32_bf16 v[72:75], v[36:39], v[48:51], 0\n"
    "s_waitcnt lgkmcnt(6)\n"
    "v_mfma_f32_16x16x32_bf16 v[76:79], v[40:43], v[48:51], 0\n"
    "v_cndmask_b32 v64, v64, v65, s[42:43]\n"
    "v_cndmask_b32 v65, v66, v67, s[42:43]\n"
    "v_cndmask_b32 v66, v68, v69, s[42:43]\n"
    "v_cndmask_b32 v67, v70, v71, s[42:43]\n"
    "s_nop 0\n"
    "v_cndmask_b32 v68, v72, v73, s[42:43]\n"
    "v_cndmask_b32 v69, v74, v75, s[42:43]\n"
    "s_nop 0\n"
    "v_cndmask_b32 v70, v76, v77, s[42:43]\n"
    "v_cndmask_b32 v71, v78, v79, s[42:43]\n"
    "v_cndmask_b32 v64, v249, v64, s[38:39]\n"
    "v_cndmask_b32 v65, v249, v65, s[40:41]\n"
    "v_cndmask_b32 v66, v249, v66, s[12:13]\n"
    "v_cndmask_b32 v67, v249, v67, s[14:15]\n"
    "v_cndmask_b32 v68, v249, v68, s[8:9]\n"
    "v_cndmask_b32 v69, v249, v69, s[10:11]\n"
    "v_cndmask_b32 v70, v249, v70, s[12:13]\n"
    "v_cndmask_b32 v71, v249, v71, s[14:15]\n"
    "v_max3_f32 v72, v64, v65, v66\n"
    "v_max3_f32 v74, v68, v69, v70\n"
    "v_max_f32 v72, v72, v67\n"
    "v_max_f32 v74, v74, v71\n"
    "s_nop 1\n"
    "v_permlane16_swap_b32 v72, v74\n"
    "v_max_f32 v72, v72, v74\n"
    "v_mov_b32 v74, v72\n"
    "s_nop 1\n"
    "v_permlane32_swap_b32 v72, v74\n"
    "v_max_f32 v72, v72, v74\n"
    "v_mul_f32 v72, s37, v72\n"
    "v_mov_b32 v74, v72\n"
    "s_nop 1\n"
    "v_permlane16_swap_b32 v72, v74\n"
    "v_fma_f32 v64, v64, s37, -v72\n"
    "v_fma_f32 v65, v65, s37, -v72\n"
    "v_fma_f32 v66, v66, s37, -v72\n"
    "v_fma_f32 v67, v67, s37, -v72\n"
    "v_fma_f32 v68, v68, s37, -v74\n"
    "v_fma_f32 v69, v69, s37, -v74\n"
    "v_fma_f32 v70, v70, s37, -v74\n"
    "v_fma_f32 v71, v71, s37, -v74\n"
    "v_exp_f32 v64, v64\n"
    "v_exp_f32 v65, v65\n"
    "v_exp_f32 v66, v66\n"
    "v_exp_f32 v67, v67\n"
    "v_exp_f32 v68, v68\n"
    "v_exp_f32 v69, v69\n"
    "v_exp_f32 v70, v70\n"
    "v_exp_f32 v71, v71\n"
    "v_pk_add_f32 v[72:73], v[64:65], v[66:67]\n"
    "v_pk_add_f32 v[74:75], v[68:69], v[70:71]\n"
    "v_add_f32 v72, v72, v73\n"
    "v_add_f32 v74, v74, v75\n"
    "s_nop 1\n"
    "v_permlane16_swap_b32 v72, v74\n"
    "v_add_f32 v72, v72, v74\n"
    "v_mov_b32 v74, v72\n"
    "s_nop 1\n"
    "v_permlane32_swap_b32 v72, v74\n"
    "v_add_f32 v72, v72, v74\n"
    "v_rcp_f32 v72, v72\n"
    "s_nop 0\n"
    "v_mov_b32 v74, v72\n"
    "s_nop 1\n"
    "v_permlane16_swap_b32 v72, v74\n"
    "v_pk_mul_f32 v[64:65], v[64:65], v[72:73] op_sel_hi:[1,0]\n"
    "v_pk_mul_f32 v[66:67], v[66:67], v[72:73] op_sel_hi:[1,0]\n"
    "v_pk_mul_f32 v[68:69], v[68:69], v[74:75] op_sel_hi:[1,0]\n"
    "v_pk_mul_f32 v[70:71], v[70:71], v[74:75] op_sel_hi:[1,0]\n"
    "v_cvt_pk_bf16_f32 v64, v64, v65\n"
    "v_cvt_pk_bf16_f32 v66, v66, v67\n"
    "v_cvt_pk_bf16_f32 v68, v68, v69\n"
    "v_cvt_pk_bf16_f32 v70, v70, v71\n"
    "v_perm_b32 v80, v64, v64, v247\n"
    "v_perm_b32 v81, v64, v64, v248\n"
    "v_perm_b32 v82, v66, v66, v247\n"
    "v_perm_b32 v83, v66, v66, v248\n"
    "v_perm_b32 v84, v68, v68, v247\n"
    "v_perm_b32 v85, v68, v68, v248\n"
    "v_perm_b32 v86, v70, v70, v247\n"
    "v_perm_b32 v87, v70, v70, v248\n"
    "s_waitcnt lgkmcnt(0)\n"
    "v_mfma_f32_16x16x32_bf16 v[0:3], v[52:55], v[80:83], 0\n"
    "v_mfma_f32_16x16x32_bf16 v[4:7], v[54:57], v[84:87], 0\n"
    "v_mfma_f32_16x16x32_bf16 v[8:11], v[58:61], v[80:83], 0\n"
    "v_mfma_f32_16x16x32_bf16 v[12:15], v[60:63], v[84:87], 0\n"
    "s_mov_b64 s[38:39], s[8:9]\n"
    "s_mov_b64 s[40:41], s[10:11]\n"
    "v_cvt_pk_bf16_f32 v16, v0, v1\n"
    "v_cvt_pk_bf16_f32 v17, v2, v3\n"
    "v_cvt_pk_bf16_f32 v18, v4, v5\n"
    "v_cvt_pk_bf16_f32 v19, v6, v7\n"
    "s_nop 0\n"
    "v_permlane16_swap_b32 v16, v18\n"
    "v_permlane16_swap_b32 v17, v19\n"
    "buffer_store_dwordx4 v[16:19], v245, s[0:3], s33 offen sc1\n"
    "v_cvt_pk_bf16_f32 v20, v8, v9\n"
    "v_cvt_pk_bf16_f32 v21, v10, v11\n"
    "v_cvt_pk_bf16_f32 v22, v12, v13\n"
    "v_cvt_pk_bf16_f32 v23, v14, v15\n"
    "buffer_store_dwordx4 v[20:23], v246, s[0:3], s33 offen sc1\n"
    ".Lb1_mk4x10_%=:\n"
    "v_mov_b32 v108, 0\n"
    "s_memrealtime s[26:27]\n"
    "s_cmp_lg_u32 s18, 0\n"
    "s_waitcnt lgkmcnt(0)\n"
    "s_cbranch_scc1 .Lb1_pl12_%=\n"
    "s_sub_u32 s28, s36, s26\n"
    "s_cmp_gt_i32 s28, 40\n"
    "s_cbranch_scc1 .Lb1_pl12_%=\n"
    "buffer_load_dwordx4 v[108:111], v229, s[0:3], s21 offen sc1\n"
    ".Lb1_pl12_%=:\n"
    "s_waitcnt vmcnt(0)\n"
    "s_barrier\n"
    "buffer_store_dwordx4 v[116:119], v230, s[0:3], 0 offen sc1\n"
    ".Lb1_mk5x12_%=:\n"
    "s_mov_b32 s30, s31\n"
    "s_add_u32 s31, s31, 1\n"
    "s_cmp_lt_u32 s30, 64\n"
    "s_cbranch_scc1 .Lb1_top_%=\n"
    "s_waitcnt vmcnt(0) lgkmcnt(0)\n"
    "s_endpgm\n"
    :: "{s[0:3]}"(s0_), "{s[4:7]}"(s4_), "{s[8:11]}"(s8_), "{s[12:15]}"(s12_), "{s[16:19]}"(s16_), "{s[20:23]}"(s20_), "{v[0:3]}"(v0_), "{v[224:227]}"(v224_), "{v[228:231]}"(v228_), "{v[232:235]}"(v232_), "{v[236:239]}"(v236_), "{v[240:243]}"(v240_), "{v[244:247]}"(v244_), "{v[248:251]}"(v248_) : "memory", "v255", "a255", "s95");
    __builtin_unreachable();
}
DEV void b2_role(const MegaArgs& A, const int L, const int c, const int rep) {
    ROLE_HEAD;
    const int b = 4 * c + w;
    const u32x4 ro = L ? u32x4{lo(A.out), hi(A.out), 2048u * 1280u, 0x20000u} : rx;
    if (L == 0) {
#pragma unroll
        for (int k = 0; k < 16; ++k) bst16(rr, ST0 + b * 32768 + (k >> 3) * 16384 + (k & 7) * 1024 + l * 16, ones);
        DRAIN_BARRIER();
        bst4(rr, threadIdx.x == 0 ? xl(Z_ST + c * 128, 0) : OOB, 1u);
    }
    const u32x4 s0_ = {RFL(rx[0]), RFL(rx[1]), RFL(rx[2]), RFL(rx[3])};
    const u32x4 s4_ = {RFL(lo(A.wo + 32 * b)), RFL(hi(A.wo + 32 * b)), RFL((640 * 640 - 32 * b) * 2), RFL(0x20000u)};
    const u32x4 s8_ = {RFL(ro[0]), RFL(ro[1]), RFL(ro[2]), RFL(ro[3])};
    const u32x4 s12_ = {RFL(w), RFL(L << 13), RFL(REC0 + RC_O + w * 2048), RFL(xl(Z_O + rep * 2048, L))};
    const u32x4 s16_ = {RFL(xl(Z_ST, 0)), RFL(ST0), RFL(DBG0 + ((L * 128 + 107 + c) * 4 + w) * 2048), RFL(0u)};
    const u32x4 v0_ = {(unsigned)((0 + (l >> 2)) * 1280 + (l & 3) * 16 - 0), (unsigned)((16 + (l >> 2)) * 1280 + (l & 3) * 16 - 1024), (unsigned)((32 + (l >> 2)) * 1280 + (l & 3) * 16 - 2048), (unsigned)((48 + (l >> 2)) * 1280 + (l & 3) * 16 - 3072)};
    const u32x4 v4_ = {(unsigned)(w * 40960 + (8 * (lq & 1) + 20 * (lq >> 1) + (l16 >> 2)) * 64 + (l & 3) * 16), (unsigned)(w * 40960 + (20 * lq + 16 + (l16 >> 2)) * 64 + (l & 3) * 16), (unsigned)((l < 5 ? l : 0) * 128), (unsigned)(0u)};
    const u32x4 v224_ = {(unsigned)(l * 16), (unsigned)((l < 11 ? l : 0) * 128), (unsigned)(l < 4 && w == 1 ? xl(Z_XN + l * 2048 + c * 128, 0) : OOB), (unsigned)(c * 16384 + l * 16)};
    const u32x4 v228_ = {(unsigned)(L ? (l16 + 0) * 1280 + b * 64 + lq * 16 : RC_X2 + c * 16384 + 0 + l * 16), (unsigned)(L ? (l16 + 16) * 1280 + b * 64 + lq * 16 : RC_X2 + c * 16384 + 1024 + l * 16), (unsigned)(l < 16 ? b * 32768 + l * 16 : OOB), (unsigned)((0 + lq) * 32768 + l16 * 16)};
    const u32x4 v232_ = {(unsigned)((4 + lq) * 32768 + l16 * 16), (unsigned)((8 + lq) * 32768 + l16 * 16), (unsigned)((12 + lq) * 32768 + l16 * 16), (unsigned)((16 + lq) * 32768 + l16 * 16)};
    asm volatile(
    "s_mov_b32 s20, 0\n"
    "s_cmp_eq_u32 s13, 0\n"
    "s_cselect_b32 s24, 34, 60\n"
    ".Lb2_ho1_%=:\n"
    "s_sub_u32 s24, s24, 1\n"
    "s_cbranch_scc1 .Lb2_hd2_%=\n"
    "s_sleep 17\n"
    "s_branch .Lb2_ho1_%=\n"
    ".Lb2_hd2_%=:\n"
    "s_mul_i32 s28, s12, 40960\n"
    "s_mov_b32 s34, 0\n"
    "s_add_u32 m0, s28, 0\n"
    "s_nop 0\n"
    "buffer_load_dwordx4 v0, s[4:7], s34 offen offset:0 lds\n"
    "buffer_load_dwordx4 v1, s[4:7], s34 offen offset:1024 lds\n"
    "buffer_load_dwordx4 v2, s[4:7], s34 offen offset:2048 lds\n"
    "buffer_load_dwordx4 v3, s[4:7], s34 offen offset:3072 lds\n"
    "s_mov_b32 s34, 81920\n"
    "s_add_u32 m0, s28, 4096\n"
    "s_nop 0\n"
    "buffer_load_dwordx4 v0, s[4:7], s34 offen offset:0 lds\n"
    "buffer_load_dwordx4 v1, s[4:7], s34 offen offset:1024 lds\n"
    "buffer_load_dwordx4 v2, s[4:7], s34 offen offset:2048 lds\n"
    "buffer_load_dwordx4 v3, s[4:7], s34 offen offset:3072 lds\n"
    "s_mov_b32 s34, 163840\n"
    "s_add_u32 m0, s28, 8192\n"
    "s_nop 0\n"
    "buffer_load_dwordx4 v0, s[4:7], s34 offen offset:0 lds\n"
    "buffer_load_dwordx4 v1, s[4:7], s34 offen offset:1024 lds\n"
    "buffer_load_dwordx4 v2, s[4:7], s34 offen offset:2048 lds\n"
    "buffer_load_dwordx4 v3, s[4:7], s34 offen offset:3072 lds\n"
    "s_mov_b32 s34, 245760\n"
    "s_add_u32 m0, s28, 12288\n"
    "s_nop 0\n"
    "buffer_load_dwordx4 v0, s[4:7], s34 offen offset:0 lds\n"
    "buffer_load_dwordx4 v1, s[4:7], s34 offen offset:1024 lds\n"
    "buffer_load_dwordx4 v2, s[4:7], s34 offen offset:2048 lds\n"
    "buffer_load_dwordx4 v3, s[4:7], s34 offen offset:3072 lds\n"
    "s_mov_b32 s34, 327680\n"
    "s_add_u32 m0, s28, 16384\n"
    "s_nop 0\n"
    "buffer_load_dwordx4 v0, s[4:7], s34 offen offset:0 lds\n"
    "buffer_load_dwordx4 v1, s[4:7], s34 offen offset:1024 lds\n"
    "buffer_load_dwordx4 v2, s[4:7], s34 offen offset:2048 lds\n"
    "buffer_load_dwordx4 v3, s[4:7], s34 offen offset:3072 lds\n"
    "s_mov_b32 s34, 409600\n"
    "s_add_u32 m0, s28, 20480\n"
    "s_nop 0\n"
    "buffer_load_dwordx4 v0, s[4:7], s34 offen offset:0 lds\n"
    "buffer_load_dwordx4 v1, s[4:7], s34 offen offset:1024 lds\n"
    "buffer_load_dwordx4 v2, s[4:7], s34 offen offset:2048 lds\n"
    "buffer_load_dwordx4 v3, s[4:7], s34 offen offset:3072 lds\n"
    "s_mov_b32 s34, 491520\n"
    "s_add_u32 m0, s28, 24576\n"
    "s_nop 0\n"
    "buffer_load_dwordx4 v0, s[4:7], s34 offen offset:0 lds\n"
    "buffer_load_dwordx4 v1, s[4:7], s34 offen offset:1024 lds\n"
    "buffer_load_dwordx4 v2, s[4:7], s34 offen offset:2048 lds\n"
    "buffer_load_dwordx4 v3, s[4:7], s34 offen offset:3072 lds\n"
    "s_mov_b32 s34, 573440\n"
    "s_add_u32 m0, s28, 28672\n"
    "s_nop 0\n"
    "buffer_load_dwordx4 v0, s[4:7], s34 offen offset:0 lds\n"
    "buffer_load_dwordx4 v1, s[4:7], s34 offen offset:1024 lds\n"
    "buffer_load_dwordx4 v2, s[4:7], s34 offen offset:2048 lds\n"
    "buffer_load_dwordx4 v3, s[4:7], s34 offen offset:3072 lds\n"
    "s_mov_b32 s34, 655360\n"
    "s_add_u32 m0, s28, 32768\n"
    "s_nop 0\n"
    "buffer_load_dwordx4 v0, s[4:7], s34 offen offset:0 lds\n"
    "buffer_load_dwordx4 v1, s[4:7], s34 offen offset:1024 lds\n"
    "buffer_load_dwordx4 v2, s[4:7], s34 offen offset:2048 lds\n"
    "buffer_load_dwordx4 v3, s[4:7], s34 offen offset:3072 lds\n"
    "s_mov_b32 s34, 737280\n"
    "s_add_u32 m0, s28, 36864\n"
    "s_nop 0\n"
    "buffer_load_dwordx4 v0, s[4:7], s34 offen offset:0 lds\n"
    "buffer_load_dwordx4 v1, s[4:7], s34 offen offset:1024 lds\n"
    "buffer_load_dwordx4 v2, s[4:7], s34 offen offset:2048 lds\n"
    "buffer_load_dwordx4 v3, s[4:7], s34 offen offset:3072 lds\n"
    "s_waitcnt vmcnt(0)\n"
    "ds_read_b64_tr_b16 a[0:1], v4 offset:0\n"
    "ds_read_b64_tr_b16 a[2:3], v4 offset:256\n"
    "ds_read_b64_tr_b16 a[80:81], v4 offset:8\n"
    "ds_read_b64_tr_b16 a[82:83], v4 offset:264\n"
    "s_waitcnt lgkmcnt(0)\n"
    "ds_read_b64_tr_b16 a[4:5], v4 offset:2560\n"
    "ds_read_b64_tr_b16 a[6:7], v4 offset:2816\n"
    "ds_read_b64_tr_b16 a[84:85], v4 offset:2568\n"
    "ds_read_b64_tr_b16 a[86:87], v4 offset:2824\n"
    "s_waitcnt lgkmcnt(0)\n"
    "ds_read_b64_tr_b16 a[8:9], v4 offset:5120\n"
    "ds_read_b64_tr_b16 a[10:11], v4 offset:5376\n"
    "ds_read_b64_tr_b16 a[88:89], v4 offset:5128\n"
    "ds_read_b64_tr_b16 a[90:91], v4 offset:5384\n"
    "s_waitcnt lgkmcnt(0)\n"
    "ds_read_b64_tr_b16 a[12:13], v4 offset:7680\n"
    "ds_read_b64_tr_b16 a[14:15], v4 offset:7936\n"
    "ds_read_b64_tr_b16 a[92:93], v4 offset:7688\n"
    "ds_read_b64_tr_b16 a[94:95], v4 offset:7944\n"
    "s_waitcnt lgkmcnt(0)\n"
    "ds_read_b64_tr_b16 a[16:17], v4 offset:10240\n"
    "ds_read_b64_tr_b16 a[18:19], v4 offset:10496\n"
    "ds_read_b64_tr_b16 a[96:97], v4 offset:10248\n"
    "ds_read_b64_tr_b16 a[98:99], v4 offset:10504\n"
    "s_waitcnt lgkmcnt(0)\n"
    "ds_read_b64_tr_b16 a[20:21], v4 offset:12800\n"
    "ds_read_b64_tr_b16 a[22:23], v4 offset:13056\n"
    "ds_read_b64_tr_b16 a[100:101], v4 offset:12808\n"
    "ds_read_b64_tr_b16 a[102:103], v4 offset:13064\n"
    "s_waitcnt lgkmcnt(0)\n"
    "ds_read_b64_tr_b16 a[24:25], v4 offset:15360\n"
    "ds_read_b64_tr_b16 a[26:27], v4 offset:15616\n"
    "ds_read_b64_tr_b16 a[104:105], v4 offset:15368\n"
    "ds_read_b64_tr_b16 a[106:107], v4 offset:15624\n"
    "s_waitcnt lgkmcnt(0)\n"
    "ds_read_b64_tr_b16 a[28:29], v4 offset:17920\n"
    "ds_read_b64_tr_b16 a[30:31], v4 offset:18176\n"
    "ds_read_b64_tr_b16 a[108:109], v4 offset:17928\n"
    "ds_read_b64_tr_b16 a[110:111], v4 offset:18184\n"
    "s_waitcnt lgkmcnt(0)\n"
    "ds_read_b64_tr_b16 a[32:33], v4 offset:20480\n"
    "ds_read_b64_tr_b16 a[34:35], v4 offset:20736\n"
    "ds_read_b64_tr_b16 a[112:113], v4 offset:20488\n"
    "ds_read_b64_tr_b16 a[114:115], v4 offset:20744\n"
    "s_waitcnt lgkmcnt(0)\n"
    "ds_read_b64_tr_b16 a[36:37], v4 offset:23040\n"
    "ds_read_b64_tr_b16 a[38:39], v4 offset:23296\n"
    "ds_read_b64_tr_b16 a[116:117], v4 offset:23048\n"
    "ds_read_b64_tr_b16 a[118:119], v4 offset:23304\n"
    "s_waitcnt lgkmcnt(0)\n"
    "ds_read_b64_tr_b16 a[40:41], v4 offset:25600\n"
    "ds_read_b64_tr_b16 a[42:43], v4 offset:25856\n"
    "ds_read_b64_tr_b16 a[120:121], v4 offset:25608\n"
    "ds_read_b64_tr_b16 a[122:123], v4 offset:25864\n"
    "s_waitcnt lgkmcnt(0)\n"
    "ds_read_b64_tr_b16 a[44:45], v4 offset:28160\n"
    "ds_read_b64_tr_b16 a[46:47], v4 offset:28416\n"
    "ds_read_b64_tr_b16 a[124:125], v4 offset:28168\n"
    "ds_read_b64_tr_b16 a[126:127], v4 offset:28424\n"
    "s_waitcnt lgkmcnt(0)\n"
    "ds_read_b64_tr_b16 a[48:49], v4 offset:30720\n"
    "ds_read_b64_tr_b16 a[50:51], v4 offset:30976\n"
    "ds_read_b64_tr_b16 a[128:129], v4 offset:30728\n"
    "ds_read_b64_tr_b16 a[130:131], v4 offset:30984\n"
    "s_waitcnt lgkmcnt(0)\n"
    "ds_read_b64_tr_b16 a[52:53], v4 offset:33280\n"
    "ds_read_b64_tr_b16 a[54:55], v4 offset:33536\n"
    "ds_read_b64_tr_b16 a[132:133], v4 offset:33288\n"
    "ds_read_b64_tr_b16 a[134:135], v4 offset:33544\n"
    "s_waitcnt lgkmcnt(0)\n"
    "ds_read_b64_tr_b16 a[56:57], v4 offset:35840\n"
    "ds_read_b64_tr_b16 a[58:59], v4 offset:36096\n"
    "ds_read_b64_tr_b16 a[136:137], v4 offset:35848\n"
    "ds_read_b64_tr_b16 a[138:139], v4 offset:36104\n"
    "s_waitcnt lgkmcnt(0)\n"
    "ds_read_b64_tr_b16 a[60:61], v4 offset:38400\n"
    "ds_read_b64_tr_b16 a[62:63], v4 offset:38656\n"
    "ds_read_b64_tr_b16 a[140:141], v4 offset:38408\n"
    "ds_read_b64_tr_b16 a[142:143], v4 offset:38664\n"
    "s_waitcnt lgkmcnt(0)\n"
    "ds_read_b64_tr_b16 a[64:65], v5 offset:0\n"
    "ds_read_b64_tr_b16 a[66:67], v5 offset:5120\n"
    "ds_read_b64_tr_b16 a[144:145], v5 offset:8\n"
    "ds_read_b64_tr_b16 a[146:147], v5 offset:5128\n"
    "s_waitcnt lgkmcnt(0)\n"
    "ds_read_b64_tr_b16 a[68:69], v5 offset:10240\n"
    "ds_read_b64_tr_b16 a[70:71], v5 offset:15360\n"
    "ds_read_b64_tr_b16 a[148:149], v5 offset:10248\n"
    "ds_read_b64_tr_b16 a[150:151], v5 offset:15368\n"
    "s_waitcnt lgkmcnt(0)\n"
    "ds_read_b64_tr_b16 a[72:73], v5 offset:20480\n"
    "ds_read_b64_tr_b16 a[74:75], v5 offset:25600\n"
    "ds_read_b64_tr_b16 a[152:153], v5 offset:20488\n"
    "ds_read_b64_tr_b16 a[154:155], v5 offset:25608\n"
    "s_waitcnt lgkmcnt(0)\n"
    "ds_read_b64_tr_b16 a[76:77], v5 offset:30720\n"
    "ds_read_b64_tr_b16 a[78:79], v5 offset:35840\n"
    "ds_read_b64_tr_b16 a[156:157], v5 offset:30728\n"
    "ds_read_b64_tr_b16 a[158:159], v5 offset:35848\n"
    "s_waitcnt lgkmcnt(0)\n"
    "s_mov_b32 s26, -2\n"
    ".Lb2_po3_%=:\n"
    "buffer_load_dword v8, v6, s[0:3], s16 offen sc1\n"
    "s_waitcnt vmcnt(0)\n"
    "v_cmp_gt_i32 vcc, 1, v8\n"
    "s_nop 0\n"
    "s_cbranch_vccz .Lb2_pd4_%=\n"
    "s_add_u32 s20, s20, 1\n"
    "s_cmp_lt_u32 s20, 0x40000\n"
    "s_cbranch_scc1 .Lb2_sp5_%=\n"
    "v_mov_b32 v8, 70\n"
    "s_mov_b32 s24, 33728\n"
    "buffer_store_dword v8, off, s[0:3], s24 sc1\n"
    "s_waitcnt vmcnt(0)\n"
    "s_branch .Lb2_pd4_%=\n"
    ".Lb2_sp5_%=:\n"
    "s_sleep 4\n"
    "s_branch .Lb2_po3_%=\n"
    ".Lb2_pd4_%=:\n"
    "s_mov_b32 s26, 0\n"
    "s_lshl_b32 s33, s12, 11\n"
    "s_mov_b32 s35, 0\n"
    "v_mov_b32 v84, 0\n"
    ".Lb2_top_%=:\n"
    "s_lshl_b32 s28, s26, 19\n"
    "s_lshl_b32 s25, s26, 11\n"
    "s_xor_b32 s25, s25, s13\n"
    "s_and_b32 s25, s25, 0x2000\n"
    "s_add_u32 s28, s28, s25\n"
    "s_add_u32 s28, s28, s14\n"
    "s_add_u32 s27, s26, 1\n"
    "s_sub_u32 s29, s28, 81920\n"
    "s_sub_u32 s31, s28, 163840\n"
    "s_xor_b32 s31, s31, 0x2000\n"
    "s_mul_i32 s30, s26, 40960\n"
    "s_cmp_eq_u32 s13, 0\n"
    "s_cselect_b32 s30, s31, s30\n"
    "s_and_b32 s32, s26, 32\n"
    "s_add_u32 s32, s32, s26\n"
    "s_lshl_b32 s32, s32, 8\n"
    "s_add_u32 s32, s32, s17\n"
    "s_cmp_lg_u32 s12, 0\n"
    "s_cbranch_scc1 .Lb2_pd7_%=\n"
    "s_add_u32 s25, s22, 252\n"
    "v_min3_i32 v84, v84, v85, v86\n"
    "v_min_i32 v84, v84, v87\n"
    "v_cmp_gt_i32 vcc, s27, v84\n"
    "s_nop 0\n"
    "s_cbranch_vccz .Lb2_pd7_%=\n"
    ".Lb2_rt8_%=:\n"
    "s_memrealtime s[22:23]\n"
    "s_waitcnt lgkmcnt(0)\n"
    "s_sub_u32 s24, s35, s22\n"
    "s_cmp_lt_u32 s24, 600\n"
    "s_cbranch_scc1 .Lb2_rt8_%=\n"
    ".Lb2_po6_%=:\n"
    "buffer_load_dwordx4 v[84:87], v225, s[0:3], s15 offen sc1\n"
    "s_memrealtime s[22:23]\n"
    "s_waitcnt vmcnt(0) lgkmcnt(0)\n"
    "s_add_u32 s25, s22, 252\n"
    "v_min3_i32 v84, v84, v85, v86\n"
    "v_min_i32 v84, v84, v87\n"
    "v_cmp_gt_i32 vcc, s27, v84\n"
    "s_nop 0\n"
    "s_cbranch_vccz .Lb2_pd7_%=\n"
    "s_add_u32 s20, s20, 1\n"
    "s_cmp_lt_u32 s20, 0x40000\n"
    "s_cbranch_scc1 .Lb2_sp9_%=\n"
    "v_mov_b32 v84, 71\n"
    "s_mov_b32 s24, 33728\n"
    "buffer_store_dword v84, off, s[0:3], s24 sc1\n"
    "s_waitcnt vmcnt(0)\n"
    "s_branch .Lb2_pd7_%=\n"
    ".Lb2_sp9_%=:\n"
    "s_sleep 6\n"
    "s_branch .Lb2_po6_%=\n"
    ".Lb2_pd7_%=:\n"
    "s_barrier\n"
    "s_mov_b32 s35, s25\n"
    ".Lb2_mk0x9_%=:\n"
    "s_add_u32 m0, s33, 0\n"
    "s_nop 0\n"
    "buffer_load_dwordx4 v224, s[0:3], s28 offen sc1 lds\n"
    "buffer_load_dwordx4 v224, s[0:3], s28 offen offset:1024 sc1 lds\n"
    "s_add_u32 m0, s33, 8192\n"
    "s_add_u32 s28, s28, 0x4000\n"
    "buffer_load_dwordx4 v224, s[0:3], s28 offen sc1 lds\n"
    "buffer_load_dwordx4 v224, s[0:3], s28 offen offset:1024 sc1 lds\n"
    "s_add_u32 m0, s33, 16384\n"
    "s_add_u32 s28, s28, 0x4000\n"
    "buffer_load_dwordx4 v224, s[0:3], s28 offen sc1 lds\n"
    "buffer_load_dwordx4 v224, s[0:3], s28 offen offset:1024 sc1 lds\n"
    "s_add_u32 m0, s33, 24576\n"
    "s_add_u32 s28, s28, 0x4000\n"
    "buffer_load_dwordx4 v224, s[0:3], s28 offen sc1 lds\n"
    "buffer_load_dwordx4 v224, s[0:3], s28 offen offset:1024 sc1 lds\n"
    "s_add_u32 m0, s33, 32768\n"
    "s_add_u32 s28, s28, 0x4000\n"
    "buffer_load_dwordx4 v224, s[0:3], s28 offen sc1 lds\n"
    "buffer_load_dwordx4 v224, s[0:3], s28 offen offset:1024 sc1 lds\n"
    "buffer_load_dwordx4 v[48:51], v227, s[0:3], s29 offen offset:0 sc1\n"
    "buffer_load_dwordx4 v[52:55], v227, s[0:3], s29 offen offset:1024 sc1\n"
    "s_waitcnt vmcnt(10)\n"
    "s_barrier\n"
    ".Lb2_mk1x10_%=:\n"
    "ds_read_b128 v[16:19], v224 offset:0\n"
    "ds_read_b128 v[20:23], v224 offset:1024\n"
    "ds_read_b128 v[24:27], v224 offset:2048\n"
    "ds_read_b128 v[28:31], v224 offset:3072\n"
    "ds_read_b128 v[32:35], v224 offset:4096\n"
    "ds_read_b128 v[36:39], v224 offset:5120\n"
    "s_waitcnt lgkmcnt(4)\n"
    "v_mfma_f32_16x16x32_bf16 v[0:3], a[0:3], v[16:19], 0\n"
    "ds_read_b128 v[40:43], v224 offset:6144\n"
    "ds_read_b128 v[44:47], v224 offset:7168\n"
    "v_mfma_f32_16x16x32_bf16 v[8:11], a[0:3], v[20:23], 0\n"
    "v_mfma_f32_16x16x32_bf16 v[4:7], a[80:83], v[16:19], 0\n"
    "v_mfma_f32_16x16x32_bf16 v[12:15], a[80:83], v[20:23], 0\n"
    "s_waitcnt lgkmcnt(4)\n"
    "v_mfma_f32_16x16x32_bf16 v[0:3], a[4:7], v[24:27], v[0:3]\n"
    "s_waitcnt vmcnt(8)\n"
    "s_barrier\n"
    "ds_read_b128 v[16:19], v224 offset:8192\n"
    "v_mfma_f32_16x16x32_bf16 v[8:11], a[4:7], v[28:31], v[8:11]\n"
    "ds_read_b128 v[20:23], v224 offset:9216\n"
    "v_mfma_f32_16x16x32_bf16 v[4:7], a[84:87], v[24:27], v[4:7]\n"
    "v_mfma_f32_16x16x32_bf16 v[12:15], a[84:87], v[28:31], v[12:15]\n"
    "s_waitcnt lgkmcnt(4)\n"
    "v_mfma_f32_16x16x32_bf16 v[0:3], a[8:11], v[32:35], v[0:3]\n"
    "ds_read_b128 v[24:27], v224 offset:10240\n"
    "ds_read_b128 v[28:31], v224 offset:11264\n"
    "v_mfma_f32_16x16x32_bf16 v[8:11], a[8:11], v[36:39], v[8:11]\n"
    "v_mfma_f32_16x16x32_bf16 v[4:7], a[88:91], v[32:35], v[4:7]\n"
    "v_mfma_f32_16x16x32_bf16 v[12:15], a[88:91], v[36:39], v[12:15]\n"
    "s_waitcnt lgkmcnt(4)\n"
    "v_mfma_f32_16x16x32_bf16 v[0:3], a[12:15], v[40:43], v[0:3]\n"
    "ds_read_b128 v[32:35], v224 offset:12288\n"
    "ds_read_b128 v[36:39], v224 offset:13312\n"
    "v_mfma_f32_16x16x32_bf16 v[8:11], a[12:15], v[44:47], v[8:11]\n"
    "v_mfma_f32_16x16x32_bf16 v[4:7], a[92:95], v[40:43], v[4:7]\n"
    "v_mfma_f32_16x16x32_bf16 v[12:15], a[92:95], v[44:47], v[12:15]\n"
    "s_waitcnt lgkmcnt(4)\n"
    "v_mfma_f32_16x16x32_bf16 v[0:3], a[16:19], v[16:19], v[0:3]\n"
    "ds_read_b128 v[40:43], v224 offset:14336\n"
    "ds_read_b128 v[44:47], v224 offset:15360\n"
    "v_mfma_f32_16x16x32_bf16 v[8:11], a[16:19], v[20:23], v[8:11]\n"
    "v_mfma_f32_16x16x32_bf16 v[4:7], a[96:99], v[16:19], v[4:7]\n"
    "v_mfma_f32_16x16x32_bf16 v[12:15], a[96:99], v[20:23], v[12:15]\n"
    "s_waitcnt lgkmcnt(4)\n"
    "v_mfma_f32_16x16x32_bf16 v[0:3], a[20:23], v[24:27], v[0:3]\n"
    "s_waitcnt vmcnt(6)\n"
    "s_barrier\n"
    "ds_read_b128 v[16:19], v224 offset:16384\n"
    "v_mfma_f32_16x16x32_bf16 v[8:11], a[20:23], v[28:31], v[8:11]\n"
    "ds_read_b128 v[20:23], v224 offset:17408\n"
    "v_mfma_f32_16x16x32_bf16 v[4:7], a[100:103], v[24:27], v[4:7]\n"
    "v_mfma_f32_16x16x32_bf16 v[12:15], a[100:103], v[28:31], v[12:15]\n"
    "s_waitcnt lgkmcnt(4)\n"
    "v_mfma_f32_16x16x32_bf16 v[0:3], a[24:27], v[32:35], v[0:3]\n"
    "ds_read_b128 v[24:27], v224 offset:18432\n"
    "ds_read_b128 v[28:31], v224 offset:19456\n"
    "v_mfma_f32_16x16x32_bf16 v[8:11], a[24:27], v[36:39], v[8:11]\n"
    "v_mfma_f32_16x16x32_bf16 v[4:7], a[104:107], v[32:35], v[4:7]\n"
    "v_mfma_f32_16x16x32_bf16 v[12:15], a[104:107], v[36:39], v[12:15]\n"
    "s_waitcnt lgkmcnt(4)\n"
    "v_mfma_f32_16x16x32_bf16 v[0:3], a[28:31], v[40:43], v[0:3]\n"
    "ds_read_b128 v[32:35], v224 offset:20480\n"
    "ds_read_b128 v[36:39], v224 offset:21504\n"
    "v_mfma_f32_16x16x32_bf16 v[8:11], a[28:31], v[44:47], v[8:11]\n"
    "v_mfma_f32_16x16x32_bf16 v[4:7], a[108:111], v[40:43], v[4:7]\n"
    "v_mfma_f32_16x16x32_bf16 v[12:15], a[108:111], v[44:47], v[12:15]\n"
    "s_waitcnt lgkmcnt(4)\n"
    "v_mfma_f32_16x16x32_bf16 v[0:3], a[32:35], v[16:19], v[0:3]\n"
    "ds_read_b128 v[40:43], v224 offset:22528\n"
    "ds_read_b128 v[44:47], v224 offset:23552\n"
    "v_mfma_f32_16x16x32_bf16 v[8:11], a[32:35], v[20:23], v[8:11]\n"
    "v_mfma_f32_16x16x32_bf16 v[4:7], a[112:115], v[16:19], v[4:7]\n"
    "v_mfma_f32_16x16x32_bf16 v[12:15], a[112:115], v[20:23], v[12:15]\n"
    "s_waitcnt lgkmcnt(4)\n"
    "v_mfma_f32_16x16x32_bf16 v[0:3], a[36:39], v[24:27], v[0:3]\n"
    "s_waitcnt vmcnt(4)\n"
    "s_barrier\n"
    "ds_read_b128 v[16:19], v224 offset:24576\n"
    "v_mfma_f32_16x16x32_bf16 v[8:11], a[36:39], v[28:31], v[8:11]\n"
    "ds_read_b128 v[20:23], v224 offset:25600\n"
    "v_mfma_f32_16x16x32_bf16 v[4:7], a[116:119], v[24:27], v[4:7]\n"
    "v_mfma_f32_16x16x32_bf16 v[12:15], a[116:119], v[28:31], v[12:15]\n"
    "s_waitcnt lgkmcnt(4)\n"
    "v_mfma_f32_16x16x32_bf16 v[0:3], a[40:43], v[32:35], v[0:3]\n"
    "ds_read_b128 v[24:27], v224 offset:26624\n"
    "ds_read_b128 v[28:31], v224 offset:27648\n"
    "v_mfma_f32_16x16x32_bf16 v[8:11], a[40:43], v[36:39], v[8:11]\n"
    "v_mfma_f32_16x16x32_bf16 v[4:7], a[120:123], v[32:35], v[4:7]\n"
    "v_mfma_f32_16x16x32_bf16 v[12:15], a[120:123], v[36:39], v[12:15]\n"
    "s_waitcnt lgkmcnt(4)\n"
    "v_mfma_f32_16x16x32_bf16 v[0:3], a[44:47], v[40:43], v[0:3]\n"
    "ds_read_b128 v[32:35], v224 offset:28672\n"
    "ds_read_b128 v[36:39], v224 offset:29696\n"
    "v_mfma_f32_16x16x32_bf16 v[8:11], a[44:47], v[44:47], v[8:11]\n"
    "v_mfma_f32_16x16x32_bf16 v[4:7], a[124:127], v[40:43], v[4:7]\n"
    "v_mfma_f32_16x16x32_bf16 v[12:15], a[124:127], v[44:47], v[12:15]\n"
    "s_waitcnt lgkmcnt(4)\n"
    "v_mfma_f32_16x16x32_bf16 v[0:3], a[48:51], v[16:19], v[0:3]\n"
    "ds_read_b128 v[40:43], v224 offset:30720\n"
    "ds_read_b128 v[44:47], v224 offset:31744\n"
    "v_mfma_f32_16x16x32_bf16 v[8:11], a[48:51], v[20:23], v[8:11]\n"
    "v_mfma_f32_16x16x32_bf16 v[4:7], a[128:131], v[16:19], v[4:7]\n"
    "v_mfma_f32_16x16x32_bf16 v[12:15], a[128:131], v[20:23], v[12:15]\n"
    "s_waitcnt lgkmcnt(4)\n"
    "v_mfma_f32_16x16x32_bf16 v[0:3], a[52:55], v[24:27], v[0:3]\n"
    "s_waitcnt vmcnt(2)\n"
    "s_barrier\n"
    "ds_read_b128 v[16:19], v224 offset:32768\n"
    "v_mfma_f32_16x16x32_bf16 v[8:11], a[52:55], v[28:31], v[8:11]\n"
    "ds_read_b128 v[20:23], v224 offset:33792\n"
    "v_mfma_f32_16x16x32_bf16 v[4:7], a[132:135], v[24:27], v[4:7]\n"
    "v_mfma_f32_16x16x32_bf16 v[12:15], a[132:135], v[28:31], v[12:15]\n"
    "s_waitcnt lgkmcnt(4)\n"
    "v_mfma_f32_16x16x32_bf16 v[0:3], a[56:59], v[32:35], v[0:3]\n"
    "ds_read_b128 v[24:27], v224 offset:34816\n"
    "ds_read_b128 v[28:31], v224 offset:35840\n"
    "v_mfma_f32_16x16x32_bf16 v[8:11], a[56:59], v[36:39], v[8:11]\n"
    "v_mfma_f32_16x16x32_bf16 v[4:7], a[136:139], v[32:35], v[4:7]\n"
    "s_waitcnt vmcnt(0)\n"
    "v_lshlrev_b32 v96, 16, v48\n"
    "v_and_b32 v97, 0xffff0000, v48\n"
    "v_mfma_f32_16x16x32_bf16 v[12:15], a[136:139], v[36:39], v[12:15]\n"
    "s_waitcnt lgkmcnt(4)\n"
    "v_lshlrev_b32 v98, 16, v49\n"
    "v_and_b32 v99, 0xffff0000, v49\n"
    "v_mfma_f32_16x16x32_bf16 v[0:3], a[60:63], v[40:43], v[0:3]\n"
    "s_waitcnt lgkmcnt(2)\n"
    "ds_read_b128 v[32:35], v224 offset:36864\n"
    "ds_read_b128 v[36:39], v224 offset:37888\n"
    "v_mfma_f32_16x16x32_bf16 v[8:11], a[60:63], v[44:47], v[8:11]\n"
    "v_swap_b32 v18, v20\n"
    "v_mfma_f32_16x16x32_bf16 v[4:7], a[140:143], v[40:43], v[4:7]\n"
    "v_swap_b32 v19, v21\n"
    "v_mfma_f32_16x16x32_bf16 v[12:15], a[140:143], v[44:47], v[12:15]\n"
    "v_lshlrev_b32 v100, 16, v50\n"
    "v_and_b32 v101, 0xffff0000, v50\n"
    "v_mfma_f32_16x16x32_bf16 v[0:3], a[64:67], v[16:19], v[0:3]\n"
    "s_waitcnt lgkmcnt(2)\n"
    "ds_read_b128 v[40:43], v224 offset:38912\n"
    "ds_read_b128 v[44:47], v224 offset:39936\n"
    "v_mfma_f32_16x16x32_bf16 v[8:11], a[64:67], v[20:23], v[8:11]\n"
    "v_swap_b32 v26, v28\n"
    "v_mfma_f32_16x16x32_bf16 v[4:7], a[144:147], v[16:19], v[4:7]\n"
    "v_swap_b32 v27, v29\n"
    "v_mfma_f32_16x16x32_bf16 v[12:15], a[144:147], v[20:23], v[12:15]\n"
    "v_lshlrev_b32 v102, 16, v51\n"
    "v_and_b32 v103, 0xffff0000, v51\n"
    "v_mfma_f32_16x16x32_bf16 v[0:3], a[68:71], v[24:27], v[0:3]\n"
    "s_waitcnt lgkmcnt(2)\n"
    "v_lshlrev_b32 v104, 16, v52\n"
    "v_and_b32 v105, 0xffff0000, v52\n"
    "v_mfma_f32_16x16x32_bf16 v[8:11], a[68:71], v[28:31], v[8:11]\n"
    "v_swap_b32 v34, v36\n"
    "v_mfma_f32_16x16x32_bf16 v[4:7], a[148:151], v[24:27], v[4:7]\n"
    "v_swap_b32 v35, v37\n"
    "v_mfma_f32_16x16x32_bf16 v[12:15], a[148:151], v[28:31], v[12:15]\n"
    "v_lshlrev_b32 v106, 16, v53\n"
    "v_and_b32 v107, 0xffff0000, v53\n"
    "v_mfma_f32_16x16x32_bf16 v[0:3], a[72:75], v[32:35], v[0:3]\n"
    "s_waitcnt lgkmcnt(0)\n"
    "v_lshlrev_b32 v108, 16, v54\n"
    "v_and_b32 v109, 0xffff0000, v54\n"
    "v_mfma_f32_16x16x32_bf16 v[8:11], a[72:75], v[36:39], v[8:11]\n"
    "v_swap_b32 v42, v44\n"
    "v_mfma_f32_16x16x32_bf16 v[4:7], a[152:155], v[32:35], v[4:7]\n"
    "v_swap_b32 v43, v45\n"
    "v_mfma_f32_16x16x32_bf16 v[12:15], a[152:155], v[36:39], v[12:15]\n"
    "v_lshlrev_b32 v110, 16, v55\n"
    "v_and_b32 v111, 0xffff0000, v55\n"
    "v_mfma_f32_16x16x32_bf16 v[0:3], a[76:79], v[40:43], v[0:3]\n"
    "v_mfma_f32_16x16x32_bf16 v[8:11], a[76:79], v[44:47], v[8:11]\n"
    "v_mfma_f32_16x16x32_bf16 v[4:7], a[156:159], v[40:43], v[4:7]\n"
    "v_mfma_f32_16x16x32_bf16 v[12:15], a[156:159], v[44:47], v[12:15]\n"
    ".Lb2_mk2x11_%=:\n"
    "v_pk_add_f32 v[0:1], v[0:1], v[96:97]\n"
    "v_pk_add_f32 v[2:3], v[2:3], v[98:99]\n"
    "v_pk_add_f32 v[8:9], v[8:9], v[104:105]\n"
    "v_pk_add_f32 v[10:11], v[10:11], v[106:107]\n"
    "s_nop 0\n"
    "v_pk_add_f32 v[4:5], v[4:5], v[100:101]\n"
    "v_pk_add_f32 v[6:7], v[6:7], v[102:103]\n"
    "s_nop 0\n"
    "v_pk_add_f32 v[12:13], v[12:13], v[108:109]\n"
    "v_pk_add_f32 v[14:15], v[14:15], v[110:111]\n"
    "s_cmp_lg_u32 s13, 0\n"
    "s_cbranch_scc1 .Lb2_out_%=\n"
    "v_pk_add_f32 v[64:65], v[0:1], v[2:3]\n"
    "v_pk_add_f32 v[68:69], v[8:9], v[10:11]\n"
    "v_pk_mul_f32 v[66:67], v[0:1], v[0:1]\n"
    "v_pk_mul_f32 v[70:71], v[8:9], v[8:9]\n"
    "v_pk_add_f32 v[96:97], v[4:5], v[6:7]\n"
    "v_pk_add_f32 v[98:99], v[12:13], v[14:15]\n"
    "v_pk_fma_f32 v[66:67], v[2:3], v[2:3], v[66:67]\n"
    "v_pk_fma_f32 v[70:71], v[10:11], v[10:11], v[70:71]\n"
    "v_pk_add_f32 v[64:65], v[64:65], v[96:97]\n"
    "v_pk_add_f32 v[68:69], v[68:69], v[98:99]\n"
    "v_pk_fma_f32 v[66:67], v[4:5], v[4:5], v[66:67]\n"
    "v_pk_fma_f32 v[70:71], v[12:13], v[12:13], v[70:71]\n"
    "v_add_f32 v64, v64, v65\n"
    "v_add_f32 v68, v68, v69\n"
    "v_pk_fma_f32 v[66:67], v[6:7], v[6:7], v[66:67]\n"
    "v_pk_fma_f32 v[70:71], v[14:15], v[14:15], v[70:71]\n"
    "v_add_f32 v66, v66, v67\n"
    "v_add_f32 v70, v70, v71\n"
    "s_nop 0\n"
    "v_permlane16_swap_b32 v64, v66\n"
    "v_permlane16_swap_b32 v68, v70\n"
    "v_add_f32 v64, v64, v66\n"
    "v_add_f32 v68, v68, v70\n"
    "s_nop 1\n"
    "v_permlane32_swap_b32 v64, v68\n"
    "v_add_f32 v64, v64, v68\n"
    "v_min_u32 v64, -2, v64\n"
    "v_mov_b32 v65, v64\n"
    "v_cvt_pk_bf16_f32 v56, v0, v1\n"
    "v_cvt_pk_bf16_f32 v57, v2, v3\n"
    "v_permlane16_swap_b32 v64, v65\n"
    "v_mov_b32 v66, v64\n"
    "v_mov_b32 v67, v65\n"
    "v_cvt_pk_bf16_f32 v58, v4, v5\n"
    "v_cvt_pk_bf16_f32 v59, v6, v7\n"
    "v_permlane32_swap_b32 v64, v66\n"
    "v_permlane32_swap_b32 v65, v67\n"
    "buffer_store_dwordx4 v[64:67], v230, s[0:3], s32 offen sc1\n"
    "v_cvt_pk_bf16_f32 v60, v8, v9\n"
    "v_cvt_pk_bf16_f32 v61, v10, v11\n"
    "v_cvt_pk_bf16_f32 v62, v12, v13\n"
    "v_cvt_pk_bf16_f32 v63, v14, v15\n"
    ".Lb2_mk3x12_%=:\n"
    "v_lshlrev_b32 v0, 16, v56\n"
    "v_and_b32 v1, 0xffff0000, v56\n"
    "v_lshlrev_b32 v2, 16, v57\n"
    "v_and_b32 v3, 0xffff0000, v57\n"
    "v_lshlrev_b32 v4, 16, v58\n"
    "v_and_b32 v5, 0xffff0000, v58\n"
    "v_lshlrev_b32 v6, 16, v59\n"
    "v_and_b32 v7, 0xffff0000, v59\n"
    "v_lshlrev_b32 v8, 16, v60\n"
    "v_and_b32 v9, 0xffff0000, v60\n"
    "v_lshlrev_b32 v10, 16, v61\n"
    "v_and_b32 v11, 0xffff0000, v61\n"
    "v_lshlrev_b32 v12, 16, v62\n"
    "v_and_b32 v13, 0xffff0000, v62\n"
    "v_lshlrev_b32 v14, 16, v63\n"
    "v_and_b32 v15, 0xffff0000, v63\n"
    "s_sleep 6\n"
    "buffer_load_dwordx4 v[64:67], v231, s[0:3], s32 offen sc1\n"
    "buffer_load_dwordx4 v[68:71], v232, s[0:3], s32 offen sc1\n"
    "buffer_load_dwordx4 v[72:75], v233, s[0:3], s32 offen sc1\n"
    "buffer_load_dwordx4 v[76:79], v234, s[0:3], s32 offen sc1\n"
    "buffer_load_dwordx4 v[80:83], v235, s[0:3], s32 offen sc1\n"
    "buffer_store_dwordx4 v[56:59], v228, s[8:11], s30 offen sc1\n"
    "buffer_store_dwordx4 v[60:63], v229, s[8:11], s30 offen sc1\n"
    ".Lb2_sl_%=:\n"
    "s_waitcnt vmcnt(6)\n"
    "v_max_u32 v84, v64, v64\n"
    "s_waitcnt vmcnt(5)\n"
    "v_max_u32 v84, v68, v84\n"
    "v_pk_add_f32 v[64:65], v[64:65], v[68:69]\n"
    "v_pk_add_f32 v[66:67], v[66:67], v[70:71]\n"
    "s_waitcnt vmcnt(4)\n"
    "v_max_u32 v84, v72, v84\n"
    "v_pk_add_f32 v[64:65], v[64:65], v[72:73]\n"
    "v_pk_add_f32 v[66:67], v[66:67], v[74:75]\n"
    "s_waitcnt vmcnt(3)\n"
    "v_max_u32 v84, v76, v84\n"
    "v_pk_add_f32 v[64:65], v[64:65], v[76:77]\n"
    "v_pk_add_f32 v[66:67], v[66:67], v[78:79]\n"
    "s_waitcnt vmcnt(2)\n"
    "v_max_u32 v84, v80, v84\n"
    "v_pk_add_f32 v[64:65], v[64:65], v[80:81]\n"
    "v_pk_add_f32 v[66:67], v[66:67], v[82:83]\n"
    "v_cmp_eq_u32 vcc, -1, v84\n"
    "s_nop 0\n"
    "s_cbranch_vccnz .Lb2_sr_%=\n"
    ".Lb2_sg_%=:\n"
    ".Lb2_mk4x13_%=:\n"
    "v_permlane16_swap_b32 v64, v65\n"
    "v_permlane16_swap_b32 v66, v67\n"
    "v_add_f32 v64, v64, v65\n"
    "v_add_f32 v66, v66, v67\n"
    "s_nop 1\n"
    "v_permlane32_swap_b32 v64, v66\n"
    "v_add_f32 v64, v64, v66\n"
    "v_mov_b32 v66, v64\n"
    "s_nop 1\n"
    "v_permlane16_swap_b32 v64, v66\n"
    "v_mul_f32 v64, 0x3acccccd, v64\n"
    "v_mul_f32 v66, 0x3acccccd, v66\n"
    "v_fma_f32 v66, -v64, v64, v66\n"
    "v_max_f32 v66, 0, v66\n"
    "v_cvt_pk_bf16_f32 v64, v64, v66\n"
    "v_and_b32 v66, 0xffff0000, v64\n"
    "v_lshlrev_b32 v64, 16, v64\n"
    "v_add_f32 v66, 0x3727c5ac, v66\n"
    "v_mov_b32 v68, v64\n"
    "v_cvt_pk_bf16_f32 v66, v66, v66\n"
    "v_lshlrev_b32 v66, 16, v66\n"
    "v_sqrt_f32 v66, v66\n"
    "v_permlane32_swap_b32 v64, v68\n"
    "v_cvt_pk_bf16_f32 v66, v66, v66\n"
    "v_lshlrev_b32 v66, 16, v66\n"
    "v_rcp_f32 v66, v66\n"
    "s_nop 0\n"
    "v_mov_b32 v70, v66\n"
    "s_nop 1\n"
    "v_permlane32_swap_b32 v66, v70\n"
    "v_mul_f32 v64, v64, v66\n"
    "v_mul_f32 v68, v68, v70\n"
    "v_pk_fma_f32 v[0:1], v[0:1], v[66:67], v[64:65] op_sel_hi:[1,0,0] neg_lo:[0,0,1] neg_hi:[0,0,1]\n"
    "v_pk_fma_f32 v[2:3], v[2:3], v[66:67], v[64:65] op_sel_hi:[1,0,0] neg_lo:[0,0,1] neg_hi:[0,0,1]\n"
    "v_pk_fma_f32 v[4:5], v[4:5], v[66:67], v[64:65] op_sel_hi:[1,0,0] neg_lo:[0,0,1] neg_hi:[0,0,1]\n"
    "v_pk_fma_f32 v[6:7], v[6:7], v[66:67], v[64:65] op_sel_hi:[1,0,0] neg_lo:[0,0,1] neg_hi:[0,0,1]\n"
    "v_cvt_pk_bf16_f32 v96, v0, v1\n"
    "v_cvt_pk_bf16_f32 v98, v2, v3\n"
    "v_cvt_pk_bf16_f32 v97, v4, v5\n"
    "v_cvt_pk_bf16_f32 v99, v6, v7\n"
    "buffer_store_dwordx4 v[96:99], v227, s[0:3], s31 offen offset:0 sc1\n"
    "v_pk_fma_f32 v[8:9], v[8:9], v[70:71], v[68:69] op_sel_hi:[1,0,0] neg_lo:[0,0,1] neg_hi:[0,0,1]\n"
    "v_pk_fma_f32 v[10:11], v[10:11], v[70:71], v[68:69] op_sel_hi:[1,0,0] neg_lo:[0,0,1] neg_hi:[0,0,1]\n"
    "v_pk_fma_f32 v[12:13], v[12:13], v[70:71], v[68:69] op_sel_hi:[1,0,0] neg_lo:[0,0,1] neg_hi:[0,0,1]\n"
    "v_pk_fma_f32 v[14:15], v[14:15], v[70:71], v[68:69] op_sel_hi:[1,0,0] neg_lo:[0,0,1] neg_hi:[0,0,1]\n"
    "v_cvt_pk_bf16_f32 v100, v8, v9\n"
    "v_cvt_pk_bf16_f32 v102, v10, v11\n"
    "v_cvt_pk_bf16_f32 v101, v12, v13\n"
    "v_cvt_pk_bf16_f32 v103, v14, v15\n"
    "buffer_store_dwordx4 v[100:103], v227, s[0:3], s31 offen offset:1024 sc1\n"
    "v_mov_b32 v88, s27\n"
    "v_mov_b32 v89, s27\n"
    "v_mov_b32 v90, s27\n"
    "v_mov_b32 v91, s27\n"
    ".Lb2_mk5x14_%=:\n"
    "v_mov_b32 v84, 0\n"
    "s_memrealtime s[22:23]\n"
    "s_cmp_lg_u32 s12, 0\n"
    "s_waitcnt lgkmcnt(0)\n"
    "s_cbranch_scc1 .Lb2_pl16_%=\n"
    "s_sub_u32 s24, s35, s22\n"
    "s_cmp_gt_i32 s24, 40\n"
    "s_cbranch_scc1 .Lb2_pl16_%=\n"
    "buffer_load_dwordx4 v[84:87], v225, s[0:3], s15 offen sc1\n"
    ".Lb2_pl16_%=:\n"
    "s_waitcnt vmcnt(0)\n"
    "s_barrier\n"
    "buffer_store_dwordx4 v[88:91], v226, s[0:3], 0 offen sc1\n"
    ".Lb2_mk6x16_%=:\n"
    ".Lb2_nx_%=:\n"
    "s_mov_b32 s26, s27\n"
    "s_cmp_lt_u32 s26, 64\n"
    "s_cbranch_scc1 .Lb2_top_%=\n"
    "s_waitcnt vmcnt(0) lgkmcnt(0)\n"
    "s_endpgm\n"
    ".Lb2_out_%=:\n"
    "v_cvt_pk_bf16_f32 v56, v0, v1\n"
    "v_cvt_pk_bf16_f32 v57, v2, v3\n"
    "v_cvt_pk_bf16_f32 v58, v4, v5\n"
    "v_cvt_pk_bf16_f32 v59, v6, v7\n"
    "v_cvt_pk_bf16_f32 v60, v8, v9\n"
    "v_cvt_pk_bf16_f32 v61, v10, v11\n"
    "v_cvt_pk_bf16_f32 v62, v12, v13\n"
    "v_cvt_pk_bf16_f32 v63, v14, v15\n"
    "buffer_store_dwordx4 v[56:59], v228, s[8:11], s30 offen\n"
    "buffer_store_dwordx4 v[60:63], v229, s[8:11], s30 offen\n"
    "v_mov_b32 v84, 0\n"
    "s_branch .Lb2_nx_%=\n"
    ".Lb2_sr_%=:\n"
    "s_add_u32 s20, s20, 1\n"
    "s_cmp_lt_u32 s20, 0x40000\n"
    "s_cbranch_scc1 .Lb2_sp18_%=\n"
    "v_mov_b32 v84, 72\n"
    "s_mov_b32 s24, 33728\n"
    "buffer_store_dword v84, off, s[0:3], s24 sc1\n"
    "s_waitcnt vmcnt(0)\n"
    "s_branch .Lb2_sg_%=\n"
    ".Lb2_sp18_%=:\n"
    "s_sleep 2\n"
    "buffer_load_dwordx4 v[64:67], v231, s[0:3], s32 offen sc1\n"
    "buffer_load_dwordx4 v[68:71], v232, s[0:3], s32 offen sc1\n"
    "buffer_load_dwordx4 v[72:75], v233, s[0:3], s32 offen sc1\n"
    "buffer_load_dwordx4 v[76:79], v234, s[0:3], s32 offen sc1\n"
    "buffer_load_dwordx4 v[80:83], v235, s[0:3], s32 offen sc1\n"
    "s_waitcnt vmcnt(0)\n"
    "s_branch .Lb2_sl_%=\n"
    :: "{s[0:3]}"(s0_), "{s[4:7]}"(s4_), "{s[8:11]}"(s8_), "{s[12:15]}"(s12_), "{s[16:19]}"(s16_), "{v[0:3]}"(v0_), "{v[4:7]}"(v4_), "{v[224:227]}"(v224_), "{v[228:231]}"(v228_), "{v[232:235]}"(v232_) : "memory", "v255", "a255", "s95");
    __builtin_unreachable();
}
constexpr int MEGA_LDS = 163840;
__global__ void __launch_bounds__(256, 1) mega_kernel(MegaArgs A) {
    extern __shared__ __attribute__((aligned(16))) char smem[];
    int* sRole = (int*)smem;
    const int x4 = (int)(__builtin_amdgcn_s_getreg((3 << 11) | 20) & 3u);
    if (threadIdx.x < 64) {
        const rsrc_t rf = mk_rsrc(arena_base(A.ar), ARENA);
        unsigned tm[2];
        const unsigned pr = (blockIdx.x >> 7) * 16384 + (blockIdx.x & 127) * 64;
        if (threadIdx.x < 2) bst4(rf, pr + threadIdx.x * 8192, 0u);
        DRAIN();
#pragma unroll
        for (int k = 0; k < 2; ++k) {
            const unsigned long long t0 = __builtin_amdgcn_s_memtime();
            if (threadIdx.x == 0) bst4(rf, pr + k * 8192, 0u);
            DRAIN();
            tm[k] = (unsigned)(__builtin_amdgcn_s_memtime() - t0);
        }
        if (threadIdx.x == 0) {
            unsigned* cnt = (unsigned*)(arena_base(A.ar) + xl(Z_TK, 0));
            int role = -1;
            auto take = [&](int pool) {
                if (role >= 0) return;
                const int k = (int)__hip_atomic_fetch_add(cnt + 16 * pool, 1u, RLX_AGENT), x = pool & 3;
                if (k < 32) role = (pool >> 2) * 128 + (k < 24 ? (k >> 3) * 32 + 8 * x + (k & 7) : 96 + ((k - 24) >> 2) * 16 + 4 * x + (k & 3));
            };
            take((tm[1] < tm[0] ? 4 : 0) + x4);
            for (int p = 0; p < 8; ++p) take(p);
            *sRole = role;
        }
    }
    __syncthreads();
    const int role = RFL(*sRole), L = role >> 7, r = role & 127;
    __syncthreads();
    const bool on = (A.layers >> L) & 1;
    if (r < 32) { if (on) qk_role(A, L, r, x4); }
    else if (r < 64) { if (on) vo_role(A, L, r - 32, x4); }
    else if (r < 96) ct_role(A, L, r - 64, smem);
    else if (r < 112) { if (on) { if (r < 107) b1_role(A, L, r - 96, x4); else b2_role(A, L, r - 107, x4); } }
    else qw_role(A, L, 8 * ((r - 112) >> 2) + 4 * L + (r & 3), x4);
}

#define LAYERS 3
extern "C" void kernel_launch(void* const* d_in, const int* in_sizes, int n_in, void* d_out, int out_size, void* d_ws, size_t ws_size, hipStream_t stream) {
    MegaArgs A;
    A.x0 = (const bf16_t*)d_in[0]; A.mem0 = (const bf16_t*)d_in[1]; A.Wq = (const bf16_t*)d_in[2]; A.Wk = (const bf16_t*)d_in[3]; A.Wv = (const bf16_t*)d_in[4];
    A.Wo = (const bf16_t*)d_in[5]; A.wqkv = (const bf16_t*)d_in[8]; A.wo = (const bf16_t*)d_in[9];
    A.ar = (char*)d_ws + 266240;
    A.out = (bf16_t*)d_out; A.layers = LAYERS; A.pad = 0;
    (void)hipFuncSetAttribute((const void*)mega_kernel, hipFuncAttributeMaxDynamicSharedMemorySize, MEGA_LDS);
    k_zero<<<2 * (Z_END - Z_TK) / 4096, 256, 0, stream>>>(A.ar);
    mega_kernel<<<256, 256, MEGA_LDS, stream>>>(A);
}
```

```cpp
#include <hip/hip_runtime.h>
#include <hip/hip_bf16.h>
#include <stdint.h>

typedef unsigned short bf16_t;
typedef short bf16x8 __attribute__((ext_vector_type(8)));
typedef short bf16x4 __attribute__((ext_vector_type(4)));
typedef float f32x4 __attribute__((ext_vector_type(4)));
typedef unsigned u32x4 __attribute__((ext_vector_type(4)));
typedef unsigned u32x2 __attribute__((ext_vector_type(2)));
typedef unsigned long long u64;
typedef __bf16 bf16v2 __attribute__((ext_vector_type(2)));
typedef float f32v2 __attribute__((ext_vector_type(2)));
#define DEV __device__ __forceinline__
DEV unsigned pk2(float a, float b) { f32v2 f = {a, b}; bf16v2 r = __builtin_convertvector(f, bf16v2); return __builtin_bit_cast(unsigned, r); }
DEV float rb(float a) { return __uint_as_float((pk2(a, 0.f) & 0xffffu) << 16); }
DEV float lo16(unsigned u) { return __uint_as_float(u << 16); }
DEV float hi16(unsigned u) { return __uint_as_float(u & 0xffff0000u); }
constexpr int D_ = 640;
#define RLX_AGENT __ATOMIC_RELAXED, __HIP_MEMORY_SCOPE_AGENT
#define AUX_SC1 16
typedef __amdgpu_buffer_rsrc_t rsrc_t;
DEV rsrc_t mk_rsrc(const void* p, unsigned bytes) { return __builtin_amdgcn_make_buffer_rsrc((void*)p, 0, (int)bytes, 0x00020000); }
DEV void bst16(rsrc_t r, unsigned off, u32x4 v) { __builtin_amdgcn_raw_buffer_store_b128(v, r, off, 0, AUX_SC1); }
DEV void bst8(rsrc_t r, unsigned off, u32x2 v) { __builtin_amdgcn_raw_buffer_store_b64(v, r, off, 0, AUX_SC1); }
DEV void bst4(rsrc_t r, unsigned off, unsigned v) { __builtin_amdgcn_raw_buffer_store_b32(v, r, off, 0, AUX_SC1); }
#define DRAIN() asm volatile("s_waitcnt vmcnt(0)" ::: "memory")
#define DRAIN_BARRIER() asm volatile("s_waitcnt vmcnt(0) lgkmcnt(0)\n\ts_barrier" ::: "memory")
#define RFL(x) ((unsigned)__builtin_amdgcn_readfirstlane((int)(x)))
DEV unsigned lo(const void* p) { return (unsigned)(size_t)p; }
DEV unsigned hi(const void* p) { return (unsigned)((size_t)p >> 32) & 0xffffu; }
DEV unsigned nosent(unsigned x) { return min(x, 0xfffffffeu); }
DEV int sw2(int x) { return ((x & 1) << 1) | (x >> 1); }

DEV unsigned xl(unsigned o, int side) { const unsigned ph = ((o >> 13) << 14) | (o & 8191u); return ph | ((((ph >> 21) ^ side) & 1u) << 13); }
constexpr unsigned Z_TK = 16384, Z_QV = Z_TK + 1024, Z_CT = Z_QV + 1024, Z_QW = Z_CT + 2048, Z_X = 3 * 8192, Z_X1 = 7 * 8192, Z_XN = 9 * 8192, Z_O = 10 * 8192, Z_ST = 11 * 8192, Z_END = 12 * 8192;
constexpr unsigned RG_UT = 1u << 21, RG_UTX = RG_UT + (1u << 20), RG_UM = RG_UTX + 262144, RG_UMX = RG_UM + 262144, RG_PT = RG_UMX + 65536, RG_PM = RG_PT + 65536, RG_MEM = RG_PM + 32768;
constexpr unsigned SLOT = 1u << 22;
constexpr unsigned REC0 = 20u << 20, RC_X1 = 5 * 16384, RC_O = 10 * 16384, RC_X2 = 15 * 16384, ST0 = 1u << 20, QT0 = 52u << 20, DBG0 = 116u << 20, ARENA = 120u << 20;
DEV unsigned rec(int s, int L) { return REC0 + (s << 19) + ((((s >> 2) ^ L) & 1) << 13); }
constexpr unsigned OOB = 0x7f000000u, WBYTES = 640u * 6144u * 2u;
DEV char* arena_base(char* p) { return p + ((0 - (size_t)p) & 0x3fffff); }

struct MegaArgs {
    const bf16_t* x0; const bf16_t* mem0; const bf16_t* Wq; const bf16_t* Wk; const bf16_t* Wv; const bf16_t* Wo; const bf16_t* wqkv; const bf16_t* wo;
    char* ar; bf16_t* out; int layers, pad;
};
DEV bool all_drawn(char* ar) { return __all(__hip_atomic_load((unsigned*)(arena_base(ar) + xl(Z_TK, 0)) + 16 * (threadIdx.x & 7), RLX_AGENT) >= 32u); }
DEV void wait_drawn(char* ar) { while (!all_drawn(ar)) __builtin_amdgcn_s_sleep(8); asm volatile("" ::: "memory"); }
#define ROLE_HEAD const int l = threadIdx.x & 63, l16 = l & 15, lq = l >> 4, w = RFL(threadIdx.x >> 6); char* const ab = arena_base(A.ar); \
    const rsrc_t rr = mk_rsrc(ab, ARENA); const u32x4 rx = {lo(ab), hi(ab), ARENA, 0x20000u}; const u32x4 ones = {~0u, ~0u, ~0u, ~0u}; const u32x2 ones2 = {~0u, ~0u}
DEV void qk_role(const MegaArgs& A, const int L, const int h, const int rep) {
    ROLE_HEAD;
#define XP 10
    const int i = 4 * (w & 1) + lq, p = l16 < 8 ? l16 >> 2 : l16 & 1, rho = 4 * (i >> 1) + 2 * p + (i & 1), tp = 4 * w + lq;
    const unsigned pm = (w >= 2 || l16 >= XP) ? OOB : xl(RG_PM + (h >> 3) * 8192 +
        (l16 < 8 ? ((h & 7) >> 1) * 1024 + ((2 * (h & 1) + ((l16 >> 1) & 1)) * 16 + rho) * 16 + (l16 & 1) * 8 : 4096 + (((h & 7) >> 1) * 16 + rho) * 16 + (h & 1) * 8), L);
    const unsigned pt = xl(RG_PT + h * 2048 + tp * 16 + (l16 < 8 ? (l16 & 3) * 256 + (l16 >> 2) * 8 : (l16 & 1) * 8 + (l16 < 10 ? 1792 : (l16 < 14 ? 1024 + ((l16 - 10) >> 1) * 256 : 1536))), L);
    u64 cm[4];
    const int k0 = (l16 >= 4 && l16 < 8) ? 4 * (l16 - 4) : ((l16 >= 10 && l16 < 14) ? 4 * (l16 - 10) : -4);
#pragma unroll
    for (int r = 0; r < 4; ++r) cm[r] = __ballot(k0 + r <= tp);
    const u32x4 s0_ = {RFL(rx[0]), RFL(rx[1]), RFL(rx[2]), RFL(rx[3])};
    const u32x4 s4_ = {RFL(lo(A.Wk + h * 192)), RFL(hi(A.Wk + h * 192)), RFL(WBYTES - h * 384), RFL(0x20000u)};
    const u32x4 s8_ = {RFL(lo(A.Wq + h * 192)), RFL(hi(A.Wq + h * 192)), RFL(WBYTES - h * 384), RFL(0x20000u)};
    const u32x4 s12_ = {RFL((unsigned)cm[0]), RFL((unsigned)(cm[0] >> 32)), RFL((unsigned)cm[1]), RFL((unsigned)(cm[1] >> 32))};
    const u32x4 s16_ = {RFL((unsigned)cm[2]), RFL((unsigned)(cm[2] >> 32)), RFL((unsigned)cm[3]), RFL((unsigned)(cm[3] >> 32))};
    const u32x4 s20_ = {RFL(w < 2 ? ~0u : 1u), RFL(w < 2 ? ~0u : 0u), RFL(w), RFL(L << 13)};
    const u32x4 s24_ = {RFL(xl(RG_MEM + rep * 16384, L) + w * 2048), RFL(w < 2 ? xl(RG_MEM + rep * 16384 + 8192, L) + w * 1024 - 2048 : OOB), RFL(w * 11264 + 8192), RFL(REC0 + w * 16384)};
    const u32x4 s28_ = {RFL(65536 - 4096 - w * 14336), RFL(QT0 + h * 32768 + w * 2048), RFL(h == 0 && w == 0 ? xl(Z_X, L) : OOB), RFL(xl(Z_X, L))};
    const u32x4 s32_ = {RFL(xl(Z_CT + (h & 7) * 256, L)), RFL(xl(Z_XN + rep * 2048, 0)), RFL(xl(Z_QW + h * 128, L)), RFL(w == 0 ? xl(Z_QV + h * 4, L) : OOB)};
    const u32x4 s36_ = {RFL(DBG0 + ((L * 128 + h) * 4 + w) * 2048), RFL(0u), RFL(0u), RFL(0u)};
    const u32x4 v0_ = {(unsigned)(((3 * w + 0) * 64 + l) / 24 * 12288 + ((((3 * w + 0) * 64 + l) % 24) ^ ((((3 * w + 0) * 64 + l) / 48) & 6)) * 16 - 0), (unsigned)(((3 * w + 1) * 64 + l) / 24 * 12288 + ((((3 * w + 1) * 64 + l) % 24) ^ ((((3 * w + 1) * 64 + l) / 48) & 6)) * 16 - 1024), (unsigned)(((3 * w + 2) * 64 + l) / 24 * 12288 + ((((3 * w + 2) * 64 + l) % 24) ^ ((((3 * w + 2) * 64 + l) / 48) & 6)) * 16 - 2048), (unsigned)((8 * lq + ((l >> 2) & 1) + ((l >> 1) & 4)) * 384 + (w ^ (((8 * lq + ((l >> 2) & 1) + ((l >> 1) & 4)) >> 2) & 3)) * 32 + (l & 3) * 8 + ((w + 0) & 3) * 12288)};
    const u32x4 v4_ = {(unsigned)((8 * lq + ((l >> 2) & 1) + ((l >> 1) & 4)) * 384 + (w ^ (((8 * lq + ((l >> 2) & 1) + ((l >> 1) & 4)) >> 2) & 3)) * 32 + (l & 3) * 8 + ((w + 1) & 3) * 12288), (unsigned)((8 * lq + ((l >> 2) & 1) + ((l >> 1) & 4)) * 384 + (w ^ (((8 * lq + ((l >> 2) & 1) + ((l >> 1) & 4)) >> 2) & 3)) * 32 + (l & 3) * 8 + ((w + 2) & 3) * 12288), (unsigned)((8 * lq + ((l >> 2) & 1) + ((l >> 1) & 4)) * 384 + (w ^ (((8 * lq + ((l >> 2) & 1) + ((l >> 1) & 4)) >> 2) & 3)) * 32 + (l & 3) * 8 + ((w + 3) & 3) * 12288), (unsigned)((8 * lq + ((l >> 2) & 1) + ((l >> 1) & 4)) * 384 + (w ^ (((8 * lq + ((l >> 2) & 1) + ((l >> 1) & 4)) >> 2) & 3)) * 32 + (l & 3) * 8)};
    const u32x4 v92_ = {(unsigned)(l * 16 + ((w + 0) & 3) * 11264), (unsigned)(l * 16 + ((w + 1) & 3) * 11264), (unsigned)(l * 16 + ((w + 2) & 3) * 11264), (unsigned)(l * 16 + ((w + 3) & 3) * 11264)};
    const u32x4 v96_ = {(unsigned)(l * 16), (unsigned)(l * 16 + 53248 + w * 20480), (unsigned)((l & 2) ? 0xeeeeu : 0x4444u), (unsigned)(0xff800000u)};
    const u32x4 v100_ = {(unsigned)(l16 == 0 + 4 * lq ? 0x3f80u : (l16 == 0 + 4 * lq + 1 ? 0x3f800000u : 0u)), (unsigned)(l16 == 2 + 4 * lq ? 0x3f80u : (l16 == 2 + 4 * lq + 1 ? 0x3f800000u : 0u)), (unsigned)(135168 + w * 1936 + (2 * (l16 >> 2) + (l & 1)) * 240 + (l & 2) * 32 + lq * 16), (unsigned)(135168 + (l16 < 10 ? (4 * (w & 1) + lq) * 240 + l16 * 16 : 1920))};
    const u32x4 v104_ = {(unsigned)(142912 + w * 4352 + l16 * 272 + lq * 16), (unsigned)(142912 + (4 * w + lq) * 272 + l16 * 16), (unsigned)(pm), (unsigned)(pt)};
    asm volatile(
    "s_mov_b32 s40, 0\n"
    "s_mov_b64 s[42:43], 0\n"
    "s_mov_b32 s45, 0\n"
    "s_cmp_eq_u32 s23, 0\n"
    "s_cselect_b32 s44, 0, 24\n"
    ".Lqk_ho1_%=:\n"
    "s_sub_u32 s44, s44, 1\n"
    "s_cbranch_scc1 .Lqk_hd2_%=\n"
    "s_sleep 17\n"
    "s_branch .Lqk_ho1_%=\n"
    ".Lqk_hd2_%=:\n"
    "s_mul_i32 s51, s22, 3072\n"
    "s_mov_b32 s60, 0\n"
    "s_add_u32 m0, s51, 0\n"
    "s_nop 0\n"
    "buffer_load_dwordx4 v0, s[8:11], s60 offen offset:0 nt lds\n"
    "buffer_load_dwordx4 v1, s[8:11], s60 offen offset:1024 nt lds\n"
    "buffer_load_dwordx4 v2, s[8:11], s60 offen offset:2048 nt lds\n"
    "s_mov_b32 s60, 1572864\n"
    "s_add_u32 m0, s51, 12288\n"
    "s_nop 0\n"
    "buffer_load_dwordx4 v0, s[8:11], s60 offen offset:0 nt lds\n"
    "buffer_load_dwordx4 v1, s[8:11], s60 offen offset:1024 nt lds\n"
    "buffer_load_dwordx4 v2, s[8:11], s60 offen offset:2048 nt lds\n"
    "s_mov_b32 s60, 3145728\n"
    "s_add_u32 m0, s51, 24576\n"
    "s_nop 0\n"
    "buffer_load_dwordx4 v0, s[8:11], s60 offen offset:0 nt lds\n"
    "buffer_load_dwordx4 v1, s[8:11], s60 offen offset:1024 nt lds\n"
    "buffer_load_dwordx4 v2, s[8:11], s60 offen offset:2048 nt lds\n"
    "s_mov_b32 s60, 4718592\n"
    "s_add_u32 m0, s51, 36864\n"
    "s_nop 0\n"
    "buffer_load_dwordx4 v0, s[8:11], s60 offen offset:0 nt lds\n"
    "buffer_load_dwordx4 v1, s[8:11], s60 offen offset:1024 nt lds\n"
    "buffer_load_dwordx4 v2, s[8:11], s60 offen offset:2048 nt lds\n"
    "s_mov_b32 s60, 393216\n"
    "s_add_u32 m0, s51, 49152\n"
    "s_nop 0\n"
    "buffer_load_dwordx4 v0, s[8:11], s60 offen offset:0 nt lds\n"
    "buffer_load_dwordx4 v1, s[8:11], s60 offen offset:1024 nt lds\n"
    "buffer_load_dwordx4 v2, s[8:11], s60 offen offset:2048 nt lds\n"
    "s_mov_b32 s60, 1966080\n"
    "s_add_u32 m0, s51, 61440\n"
    "s_nop 0\n"
    "buffer_load_dwordx4 v0, s[8:11], s60 offen offset:0 nt lds\n"
    "buffer_load_dwordx4 v1, s[8:11], s60 offen offset:1024 nt lds\n"
    "buffer_load_dwordx4 v2, s[8:11], s60 offen offset:2048 nt lds\n"
    "s_mov_b32 s60, 3538944\n"
    "s_add_u32 m0, s51, 73728\n"
    "s_nop 0\n"
    "buffer_load_dwordx4 v0, s[8:11], s60 offen offset:0 nt lds\n"
    "buffer_load_dwordx4 v1, s[8:11], s60 offen offset:1024 nt lds\n"
    "buffer_load_dwordx4 v2, s[8:11], s60 offen offset:2048 nt lds\n"
    "s_mov_b32 s60, 5111808\n"
    "s_add_u32 m0, s51, 86016\n"
    "s_nop 0\n"
    "buffer_load_dwordx4 v0, s[8:11], s60 offen offset:0 nt lds\n"
    "buffer_load_dwordx4 v1, s[8:11], s60 offen offset:1024 nt lds\n"
    "buffer_load_dwordx4 v2, s[8:11], s60 offen offset:2048 nt lds\n"
    "s_mov_b32 s60, 786432\n"
    "s_add_u32 m0, s51, 98304\n"
    "s_nop 0\n"
    "buffer_load_dwordx4 v0, s[8:11], s60 offen offset:0 nt lds\n"
    "buffer_load_dwordx4 v1, s[8:11], s60 offen offset:1024 nt lds\n"
    "buffer_load_dwordx4 v2, s[8:11], s60 offen offset:2048 nt lds\n"
    "s_mov_b32 s60, 2359296\n"
    "s_add_u32 m0, s51, 110592\n"
    "s_nop 0\n"
    "buffer_load_dwordx4 v0, s[8:11], s60 offen offset:0 nt lds\n"
    "buffer_load_dwordx4 v1, s[8:11], s60 offen offset:1024 nt lds\n"
    "buffer_load_dwordx4 v2, s[8:11], s60 offen offset:2048 nt lds\n"
    "s_mov_b32 s60, 3932160\n"
    "s_add_u32 m0, s51, 122880\n"
    "s_nop 0\n"
    "buffer_load_dwordx4 v0, s[8:11], s60 offen offset:0 nt lds\n"
    "buffer_load_dwordx4 v1, s[8:11], s60 offen offset:1024 nt lds\n"
    "buffer_load_dwordx4 v2, s[8:11], s60 offen offset:2048 nt lds\n"
    "s_mov_b32 s60, 5505024\n"
    "s_add_u32 m0, s51, 135168\n"
    "s_nop 0\n"
    "buffer_load_dwordx4 v0, s[8:11], s60 offen offset:0 nt lds\n"
    "buffer_load_dwordx4 v1, s[8:11], s60 offen offset:1024 nt lds\n"
    "buffer_load_dwordx4 v2, s[8:11], s60 offen offset:2048 nt lds\n"
    "v_mov_b64 v[108:109], -1\n"
    "buffer_store_dwordx2 v[108:109], v106, s[0:3], 0 offen sc1\n"
    "buffer_store_dwordx2 v[108:109], v107, s[0:3], 0 offen sc1\n"
    "s_waitcnt vmcnt(26)\n"
    "s_barrier\n"
    "v_add_u32 v8, 0, v3\n"
    "ds_read_b64_tr_b16 a[240:241], v8 offset:0\n"
    "ds_read_b64_tr_b16 a[242:243], v8 offset:768\n"
    "ds_read_b64_tr_b16 v[176:177], v8 offset:128\n"
    "ds_read_b64_tr_b16 v[178:179], v8 offset:896\n"
    "ds_read_b64_tr_b16 v[12:13], v8 offset:256\n"
    "ds_read_b64_tr_b16 v[14:15], v8 offset:1024\n"
    "v_add_u32 v9, 0, v4\n"
    "ds_read_b64_tr_b16 v[112:113], v9 offset:0\n"
    "ds_read_b64_tr_b16 v[114:115], v9 offset:768\n"
    "ds_read_b64_tr_b16 v[192:193], v9 offset:128\n"
    "ds_read_b64_tr_b16 v[194:195], v9 offset:896\n"
    "ds_read_b64_tr_b16 v[28:29], v9 offset:256\n"
    "ds_read_b64_tr_b16 v[30:31], v9 offset:1024\n"
    "v_add_u32 v10, 0, v5\n"
    "ds_read_b64_tr_b16 v[128:129], v10 offset:0\n"
    "ds_read_b64_tr_b16 v[130:131], v10 offset:768\n"
    "ds_read_b64_tr_b16 v[208:209], v10 offset:128\n"
    "ds_read_b64_tr_b16 v[210:211], v10 offset:896\n"
    "ds_read_b64_tr_b16 v[44:45], v10 offset:256\n"
    "ds_read_b64_tr_b16 v[46:47], v10 offset:1024\n"
    "v_add_u32 v11, 0, v6\n"
    "ds_read_b64_tr_b16 v[144:145], v11 offset:0\n"
    "ds_read_b64_tr_b16 v[146:147], v11 offset:768\n"
    "ds_read_b64_tr_b16 v[224:225], v11 offset:128\n"
    "ds_read_b64_tr_b16 v[226:227], v11 offset:896\n"
    "ds_read_b64_tr_b16 v[60:61], v11 offset:256\n"
    "ds_read_b64_tr_b16 v[62:63], v11 offset:1024\n"
    "s_waitcnt lgkmcnt(0)\n"
    "s_barrier\n"
    "s_mov_b32 s60, 1179648\n"
    "s_add_u32 m0, s51, 0\n"
    "s_nop 0\n"
    "buffer_load_dwordx4 v0, s[8:11], s60 offen offset:0 nt lds\n"
    "buffer_load_dwordx4 v1, s[8:11], s60 offen offset:1024 nt lds\n"
    "buffer_load_dwordx4 v2, s[8:11], s60 offen offset:2048 nt lds\n"
    "s_mov_b32 s60, 2752512\n"
    "s_add_u32 m0, s51, 12288\n"
    "s_nop 0\n"
    "buffer_load_dwordx4 v0, s[8:11], s60 offen offset:0 nt lds\n"
    "buffer_load_dwordx4 v1, s[8:11], s60 offen offset:1024 nt lds\n"
    "buffer_load_dwordx4 v2, s[8:11], s60 offen offset:2048 nt lds\n"
    "s_mov_b32 s60, 4325376\n"
    "s_add_u32 m0, s51, 24576\n"
    "s_nop 0\n"
    "buffer_load_dwordx4 v0, s[8:11], s60 offen offset:0 nt lds\n"
    "buffer_load_dwordx4 v1, s[8:11], s60 offen offset:1024 nt lds\n"
    "buffer_load_dwordx4 v2, s[8:11], s60 offen offset:2048 nt lds\n"
    "s_mov_b32 s60, 5898240\n"
    "s_add_u32 m0, s51, 36864\n"
    "s_nop 0\n"
    "buffer_load_dwordx4 v0, s[8:11], s60 offen offset:0 nt lds\n"
    "buffer_load_dwordx4 v1, s[8:11], s60 offen offset:1024 nt lds\n"
    "buffer_load_dwordx4 v2, s[8:11], s60 offen offset:2048 nt lds\n"
    "v_and_b32 v108, 0x70, v96\n"
    "v_lshlrev_b32 v108, 2, v108\n"
    "v_or_b32 v108, 0x8000, v108\n"
    "buffer_load_dword v109, v108, s[0:3], 0 offen sc1\n"
    "s_waitcnt vmcnt(27)\n"
    "s_barrier\n"
    "v_add_u32 v8, 49152, v3\n"
    "ds_read_b64_tr_b16 a[244:245], v8 offset:0\n"
    "ds_read_b64_tr_b16 a[246:247], v8 offset:768\n"
    "ds_read_b64_tr_b16 v[180:181], v8 offset:128\n"
    "ds_read_b64_tr_b16 v[182:183], v8 offset:896\n"
    "ds_read_b64_tr_b16 v[16:17], v8 offset:256\n"
    "ds_read_b64_tr_b16 v[18:19], v8 offset:1024\n"
    "v_add_u32 v9, 49152, v4\n"
    "ds_read_b64_tr_b16 v[116:117], v9 offset:0\n"
    "ds_read_b64_tr_b16 v[118:119], v9 offset:768\n"
    "ds_read_b64_tr_b16 v[196:197], v9 offset:128\n"
    "ds_read_b64_tr_b16 v[198:199], v9 offset:896\n"
    "ds_read_b64_tr_b16 v[32:33], v9 offset:256\n"
    "ds_read_b64_tr_b16 v[34:35], v9 offset:1024\n"
    "v_add_u32 v10, 49152, v5\n"
    "ds_read_b64_tr_b16 v[132:133], v10 offset:0\n"
    "ds_read_b64_tr_b16 v[134:135], v10 offset:768\n"
    "ds_read_b64_tr_b16 v[212:213], v10 offset:128\n"
    "ds_read_b64_tr_b16 v[214:215], v10 offset:896\n"
    "ds_read_b64_tr_b16 v[48:49], v10 offset:256\n"
    "ds_read_b64_tr_b16 v[50:51], v10 offset:1024\n"
    "v_add_u32 v11, 49152, v6\n"
    "ds_read_b64_tr_b16 v[148:149], v11 offset:0\n"
    "ds_read_b64_tr_b16 v[150:151], v11 offset:768\n"
    "ds_read_b64_tr_b16 v[228:229], v11 offset:128\n"
    "ds_read_b64_tr_b16 v[230:231], v11 offset:896\n"
    "ds_read_b64_tr_b16 v[64:65], v11 offset:256\n"
    "ds_read_b64_tr_b16 v[66:67], v11 offset:1024\n"
    "s_waitcnt lgkmcnt(0)\n"
    "s_barrier\n"
    "s_mov_b32 s60, 6291456\n"
    "s_add_u32 m0, s51, 49152\n"
    "s_nop 0\n"
    "buffer_load_dwordx4 v0, s[8:11], s60 offen offset:0 nt lds\n"
    "buffer_load_dwordx4 v1, s[8:11], s60 offen offset:1024 nt lds\n"
    "buffer_load_dwordx4 v2, s[8:11], s60 offen offset:2048 nt lds\n"
    "s_mov_b32 s60, 6684672\n"
    "s_add_u32 m0, s51, 61440\n"
    "s_nop 0\n"
    "buffer_load_dwordx4 v0, s[8:11], s60 offen offset:0 nt lds\n"
    "buffer_load_dwordx4 v1, s[8:11], s60 offen offset:1024 nt lds\n"
    "buffer_load_dwordx4 v2, s[8:11], s60 offen offset:2048 nt lds\n"
    "s_mov_b32 s60, 7077888\n"
    "s_add_u32 m0, s51, 73728\n"
    "s_nop 0\n"
    "buffer_load_dwordx4 v0, s[8:11], s60 offen offset:0 nt lds\n"
    "buffer_load_dwordx4 v1, s[8:11], s60 offen offset:1024 nt lds\n"
    "buffer_load_dwordx4 v2, s[8:11], s60 offen offset:2048 nt lds\n"
    "s_mov_b32 s60, 7471104\n"
    "s_add_u32 m0, s51, 86016\n"
    "s_nop 0\n"
    "buffer_load_dwordx4 v0, s[8:11], s60 offen offset:0 nt lds\n"
    "buffer_load_dwordx4 v1, s[8:11], s60 offen offset:1024 nt lds\n"
    "buffer_load_dwordx4 v2, s[8:11], s60 offen offset:2048 nt lds\n"
    "s_waitcnt vmcnt(27)\n"
    "s_barrier\n"
    "v_add_u32 v8, 98304, v3\n"
    "ds_read_b64_tr_b16 a[248:249], v8 offset:0\n"
    "ds_read_b64_tr_b16 a[250:251], v8 offset:768\n"
    "ds_read_b64_tr_b16 v[184:185], v8 offset:128\n"
    "ds_read_b64_tr_b16 v[186:187], v8 offset:896\n"
    "ds_read_b64_tr_b16 v[20:21], v8 offset:256\n"
    "ds_read_b64_tr_b16 v[22:23], v8 offset:1024\n"
    "v_add_u32 v9, 98304, v4\n"
    "ds_read_b64_tr_b16 v[120:121], v9 offset:0\n"
    "ds_read_b64_tr_b16 v[122:123], v9 offset:768\n"
    "ds_read_b64_tr_b16 v[200:201], v9 offset:128\n"
    "ds_read_b64_tr_b16 v[202:203], v9 offset:896\n"
    "ds_read_b64_tr_b16 v[36:37], v9 offset:256\n"
    "ds_read_b64_tr_b16 v[38:39], v9 offset:1024\n"
    "v_add_u32 v10, 98304, v5\n"
    "ds_read_b64_tr_b16 v[136:137], v10 offset:0\n"
    "ds_read_b64_tr_b16 v[138:139], v10 offset:768\n"
    "ds_read_b64_tr_b16 v[216:217], v10 offset:128\n"
    "ds_read_b64_tr_b16 v[218:219], v10 offset:896\n"
    "ds_read_b64_tr_b16 v[52:53], v10 offset:256\n"
    "ds_read_b64_tr_b16 v[54:55], v10 offset:1024\n"
    "v_add_u32 v11, 98304, v6\n"
    "ds_read_b64_tr_b16 v[152:153], v11 offset:0\n"
    "ds_read_b64_tr_b16 v[154:155], v11 offset:768\n"
    "ds_read_b64_tr_b16 v[232:233], v11 offset:128\n"
    "ds_read_b64_tr_b16 v[234:235], v11 offset:896\n"
    "ds_read_b64_tr_b16 v[68:69], v11 offset:256\n"
    "ds_read_b64_tr_b16 v[70:71], v11 offset:1024\n"
    "s_waitcnt lgkmcnt(0)\n"
    "s_barrier\n"
    "s_mov_b32 s60, 0\n"
    "s_add_u32 m0, s51, 98304\n"
    "s_nop 0\n"
    "buffer_load_dwordx4 v0, s[4:7], s60 offen offset:0 nt lds\n"
    "buffer_load_dwordx4 v1, s[4:7], s60 offen offset:1024 nt lds\n"
    "buffer_load_dwordx4 v2, s[4:7], s60 offen offset:2048 nt lds\n"
    "s_mov_b32 s60, 1572864\n"
    "s_add_u32 m0, s51, 110592\n"
    "s_nop 0\n"
    "buffer_load_dwordx4 v0, s[4:7], s60 offen offset:0 nt lds\n"
    "buffer_load_dwordx4 v1, s[4:7], s60 offen offset:1024 nt lds\n"
    "buffer_load_dwordx4 v2, s[4:7], s60 offen offset:2048 nt lds\n"
    "s_mov_b32 s60, 3145728\n"
    "s_add_u32 m0, s51, 122880\n"
    "s_nop 0\n"
    "buffer_load_dwordx4 v0, s[4:7], s60 offen offset:0 nt lds\n"
    "buffer_load_dwordx4 v1, s[4:7], s60 offen offset:1024 nt lds\n"
    "buffer_load_dwordx4 v2, s[4:7], s60 offen offset:2048 nt lds\n"
    "s_mov_b32 s60, 4718592\n"
    "s_add_u32 m0, s51, 135168\n"
    "s_nop 0\n"
    "buffer_load_dwordx4 v0, s[4:7], s60 offen offset:0 nt lds\n"
    "buffer_load_dwordx4 v1, s[4:7], s60 offen offset:1024 nt lds\n"
    "buffer_load_dwordx4 v2, s[4:7], s60 offen offset:2048 nt lds\n"
    "s_waitcnt vmcnt(25)\n"
    "s_barrier\n"
    "v_add_u32 v8, 0, v3\n"
    "ds_read_b64_tr_b16 a[252:253], v8 offset:0\n"
    "ds_read_b64_tr_b16 a[254:255], v8 offset:768\n"
    "ds_read_b64_tr_b16 v[188:189], v8 offset:128\n"
    "ds_read_b64_tr_b16 v[190:191], v8 offset:896\n"
    "ds_read_b64_tr_b16 v[24:25], v8 offset:256\n"
    "ds_read_b64_tr_b16 v[26:27], v8 offset:1024\n"
    "v_add_u32 v9, 0, v4\n"
    "ds_read_b64_tr_b16 v[124:125], v9 offset:0\n"
    "ds_read_b64_tr_b16 v[126:127], v9 offset:768\n"
    "ds_read_b64_tr_b16 v[204:205], v9 offset:128\n"
    "ds_read_b64_tr_b16 v[206:207], v9 offset:896\n"
    "ds_read_b64_tr_b16 v[40:41], v9 offset:256\n"
    "ds_read_b64_tr_b16 v[42:43], v9 offset:1024\n"
    "v_add_u32 v10, 0, v5\n"
    "ds_read_b64_tr_b16 v[140:141], v10 offset:0\n"
    "ds_read_b64_tr_b16 v[142:143], v10 offset:768\n"
    "ds_read_b64_tr_b16 v[220:221], v10 offset:128\n"
    "ds_read_b64_tr_b16 v[222:223], v10 offset:896\n"
    "ds_read_b64_tr_b16 v[56:57], v10 offset:256\n"
    "ds_read_b64_tr_b16 v[58:59], v10 offset:1024\n"
    "v_add_u32 v11, 0, v6\n"
    "ds_read_b64_tr_b16 v[156:157], v11 offset:0\n"
    "ds_read_b64_tr_b16 v[158:159], v11 offset:768\n"
    "ds_read_b64_tr_b16 v[236:237], v11 offset:128\n"
    "ds_read_b64_tr_b16 v[238:239], v11 offset:896\n"
    "ds_read_b64_tr_b16 v[72:73], v11 offset:256\n"
    "ds_read_b64_tr_b16 v[74:75], v11 offset:1024\n"
    "s_waitcnt lgkmcnt(0)\n"
    "s_barrier\n"
    "s_mov_b32 s60, 393216\n"
    "s_add_u32 m0, s51, 0\n"
    "s_nop 0\n"
    "buffer_load_dwordx4 v0, s[4:7], s60 offen offset:0 nt lds\n"
    "buffer_load_dwordx4 v1, s[4:7], s60 offen offset:1024 nt lds\n"
    "buffer_load_dwordx4 v2, s[4:7], s60 offen offset:2048 nt lds\n"
    "s_mov_b32 s60, 1966080\n"
    "s_add_u32 m0, s51, 12288\n"
    "s_nop 0\n"
    "buffer_load_dwordx4 v0, s[4:7], s60 offen offset:0 nt lds\n"
    "buffer_load_dwordx4 v1, s[4:7], s60 offen offset:1024 nt lds\n"
    "buffer_load_dwordx4 v2, s[4:7], s60 offen offset:2048 nt lds\n"
    "s_mov_b32 s60, 3538944\n"
    "s_add_u32 m0, s51, 24576\n"
    "s_nop 0\n"
    "buffer_load_dwordx4 v0, s[4:7], s60 offen offset:0 nt lds\n"
    "buffer_load_dwordx4 v1, s[4:7], s60 offen offset:1024 nt lds\n"
    "buffer_load_dwordx4 v2, s[4:7], s60 offen offset:2048 nt lds\n"
    "s_mov_b32 s60, 5111808\n"
    "s_add_u32 m0, s51, 36864\n"
    "s_nop 0\n"
    "buffer_load_dwordx4 v0, s[4:7], s60 offen offset:0 nt lds\n"
    "buffer_load_dwordx4 v1, s[4:7], s60 offen offset:1024 nt lds\n"
    "buffer_load_dwordx4 v2, s[4:7], s60 offen offset:2048 nt lds\n"
    "s_waitcnt vmcnt(24)\n"
    "s_barrier\n"
    "v_add_u32 v8, 49152, v7\n"
    "ds_read_b64_tr_b16 v[160:161], v8 offset:0\n"
    "ds_read_b64_tr_b16 v[162:163], v8 offset:768\n"
    "ds_read_b64_tr_b16 v[240:241], v8 offset:128\n"
    "ds_read_b64_tr_b16 v[242:243], v8 offset:896\n"
    "ds_read_b64_tr_b16 v[76:77], v8 offset:256\n"
    "ds_read_b64_tr_b16 v[78:79], v8 offset:1024\n"
    "v_add_u32 v9, 61440, v7\n"
    "ds_read_b64_tr_b16 v[164:165], v9 offset:0\n"
    "ds_read_b64_tr_b16 v[166:167], v9 offset:768\n"
    "ds_read_b64_tr_b16 v[244:245], v9 offset:128\n"
    "ds_read_b64_tr_b16 v[246:247], v9 offset:896\n"
    "ds_read_b64_tr_b16 v[80:81], v9 offset:256\n"
    "ds_read_b64_tr_b16 v[82:83], v9 offset:1024\n"
    "v_add_u32 v10, 73728, v7\n"
    "ds_read_b64_tr_b16 v[168:169], v10 offset:0\n"
    "ds_read_b64_tr_b16 v[170:171], v10 offset:768\n"
    "ds_read_b64_tr_b16 v[248:249], v10 offset:128\n"
    "ds_read_b64_tr_b16 v[250:251], v10 offset:896\n"
    "ds_read_b64_tr_b16 v[84:85], v10 offset:256\n"
    "ds_read_b64_tr_b16 v[86:87], v10 offset:1024\n"
    "v_add_u32 v11, 86016, v7\n"
    "ds_read_b64_tr_b16 v[172:173], v11 offset:0\n"
    "ds_read_b64_tr_b16 v[174:175], v11 offset:768\n"
    "ds_read_b64_tr_b16 v[252:253], v11 offset:128\n"
    "ds_read_b64_tr_b16 v[254:255], v11 offset:896\n"
    "ds_read_b64_tr_b16 v[88:89], v11 offset:256\n"
    "ds_read_b64_tr_b16 v[90:91], v11 offset:1024\n"
    "s_waitcnt lgkmcnt(0)\n"
    "s_barrier\n"
    "s_waitcnt vmcnt(36)\n"
    "v_cmp_gt_i32 vcc, 32, v109\n"
    "s_nop 0\n"
    "s_cbranch_vccnz .Lqk_tk0_%=\n"
    ".Lqk_tk1_%=:\n"
    "s_mov_b32 s60, 786432\n"
    "s_add_u32 m0, s51, 49152\n"
    "s_nop 0\n"
    "buffer_load_dwordx4 v0, s[4:7], s60 offen offset:0 nt lds\n"
    "buffer_load_dwordx4 v1, s[4:7], s60 offen offset:1024 nt lds\n"
    "buffer_load_dwordx4 v2, s[4:7], s60 offen offset:2048 nt lds\n"
    "s_mov_b32 s60, 2359296\n"
    "s_add_u32 m0, s51, 61440\n"
    "s_nop 0\n"
    "buffer_load_dwordx4 v0, s[4:7], s60 offen offset:0 nt lds\n"
    "buffer_load_dwordx4 v1, s[4:7], s60 offen offset:1024 nt lds\n"
    "buffer_load_dwordx4 v2, s[4:7], s60 offen offset:2048 nt lds\n"
    "s_mov_b32 s60, 3932160\n"
    "s_add_u32 m0, s51, 73728\n"
    "s_nop 0\n"
    "buffer_load_dwordx4 v0, s[4:7], s60 offen offset:0 nt lds\n"
    "buffer_load_dwordx4 v1, s[4:7], s60 offen offset:1024 nt lds\n"
    "buffer_load_dwordx4 v2, s[4:7], s60 offen offset:2048 nt lds\n"
    "s_mov_b32 s60, 5505024\n"
    "s_add_u32 m0, s51, 86016\n"
    "s_nop 0\n"
    "buffer_load_dwordx4 v0, s[4:7], s60 offen offset:0 nt lds\n"
    "buffer_load_dwordx4 v1, s[4:7], s60 offen offset:1024 nt lds\n"
    "buffer_load_dwordx4 v2, s[4:7], s60 offen offset:2048 nt lds\n"
    "v_lshlrev_b32 v108, 4, v96\n"
    "v_mov_b32 v109, 1\n"
    "s_mov_b64 exec, 15\n"
    "buffer_store_dword v109, v108, s[0:3], s35 offen sc1\n"
    "s_mov_b64 exec, -1\n"
    "s_waitcnt vmcnt(25)\n"
    "s_barrier\n"
    "v_add_u32 v8, 98304, v3\n"
    "ds_read_b64_tr_b16 a[0:1], v8 offset:0\n"
    "ds_read_b64_tr_b16 a[2:3], v8 offset:768\n"
    "ds_read_b64_tr_b16 a[80:81], v8 offset:128\n"
    "ds_read_b64_tr_b16 a[82:83], v8 offset:896\n"
    "ds_read_b64_tr_b16 a[160:161], v8 offset:256\n"
    "ds_read_b64_tr_b16 a[162:163], v8 offset:1024\n"
    "v_add_u32 v9, 98304, v4\n"
    "ds_read_b64_tr_b16 a[16:17], v9 offset:0\n"
    "ds_read_b64_tr_b16 a[18:19], v9 offset:768\n"
    "ds_read_b64_tr_b16 a[96:97], v9 offset:128\n"
    "ds_read_b64_tr_b16 a[98:99], v9 offset:896\n"
    "ds_read_b64_tr_b16 a[176:177], v9 offset:256\n"
    "ds_read_b64_tr_b16 a[178:179], v9 offset:1024\n"
    "v_add_u32 v10, 98304, v5\n"
    "ds_read_b64_tr_b16 a[32:33], v10 offset:0\n"
    "ds_read_b64_tr_b16 a[34:35], v10 offset:768\n"
    "ds_read_b64_tr_b16 a[112:113], v10 offset:128\n"
    "ds_read_b64_tr_b16 a[114:115], v10 offset:896\n"
    "ds_read_b64_tr_b16 a[192:193], v10 offset:256\n"
    "ds_read_b64_tr_b16 a[194:195], v10 offset:1024\n"
    "v_add_u32 v11, 98304, v6\n"
    "ds_read_b64_tr_b16 a[48:49], v11 offset:0\n"
    "ds_read_b64_tr_b16 a[50:51], v11 offset:768\n"
    "ds_read_b64_tr_b16 a[128:129], v11 offset:128\n"
    "ds_read_b64_tr_b16 a[130:131], v11 offset:896\n"
    "ds_read_b64_tr_b16 a[208:209], v11 offset:256\n"
    "ds_read_b64_tr_b16 a[210:211], v11 offset:1024\n"
    "s_waitcnt lgkmcnt(0)\n"
    "s_barrier\n"
    "s_mov_b32 s60, 1179648\n"
    "s_add_u32 m0, s51, 98304\n"
    "s_nop 0\n"
    "buffer_load_dwordx4 v0, s[4:7], s60 offen offset:0 nt lds\n"
    "buffer_load_dwordx4 v1, s[4:7], s60 offen offset:1024 nt lds\n"
    "buffer_load_dwordx4 v2, s[4:7], s60 offen offset:2048 nt lds\n"
    "s_mov_b32 s60, 2752512\n"
    "s_add_u32 m0, s51, 110592\n"
    "s_nop 0\n"
    "buffer_load_dwordx4 v0, s[4:7], s60 offen offset:0 nt lds\n"
    "buffer_load_dwordx4 v1, s[4:7], s60 offen offset:1024 nt lds\n"
    "buffer_load_dwordx4 v2, s[4:7], s60 offen offset:2048 nt lds\n"
    "s_mov_b32 s60, 4325376\n"
    "s_add_u32 m0, s51, 122880\n"
    "s_nop 0\n"
    "buffer_load_dwordx4 v0, s[4:7], s60 offen offset:0 nt lds\n"
    "buffer_load_dwordx4 v1, s[4:7], s60 offen offset:1024 nt lds\n"
    "buffer_load_dwordx4 v2, s[4:7], s60 offen offset:2048 nt lds\n"
    "s_mov_b32 s60, 5898240\n"
    "s_add_u32 m0, s51, 135168\n"
    "s_nop 0\n"
    "buffer_load_dwordx4 v0, s[4:7], s60 offen offset:0 nt lds\n"
    "buffer_load_dwordx4 v1, s[4:7], s60 offen offset:1024 nt lds\n"
    "buffer_load_dwordx4 v2, s[4:7], s60 offen offset:2048 nt lds\n"
    "s_waitcnt vmcnt(25)\n"
    "s_barrier\n"
    "v_add_u32 v8, 0, v3\n"
    "ds_read_b64_tr_b16 a[4:5], v8 offset:0\n"
    "ds_read_b64_tr_b16 a[6:7], v8 offset:768\n"
    "ds_read_b64_tr_b16 a[84:85], v8 offset:128\n"
    "ds_read_b64_tr_b16 a[86:87], v8 offset:896\n"
    "ds_read_b64_tr_b16 a[164:165], v8 offset:256\n"
    "ds_read_b64_tr_b16 a[166:167], v8 offset:1024\n"
    "v_add_u32 v9, 0, v4\n"
    "ds_read_b64_tr_b16 a[20:21], v9 offset:0\n"
    "ds_read_b64_tr_b16 a[22:23], v9 offset:768\n"
    "ds_read_b64_tr_b16 a[100:101], v9 offset:128\n"
    "ds_read_b64_tr_b16 a[102:103], v9 offset:896\n"
    "ds_read_b64_tr_b16 a[180:181], v9 offset:256\n"
    "ds_read_b64_tr_b16 a[182:183], v9 offset:1024\n"
    "v_add_u32 v10, 0, v5\n"
    "ds_read_b64_tr_b16 a[36:37], v10 offset:0\n"
    "ds_read_b64_tr_b16 a[38:39], v10 offset:768\n"
    "ds_read_b64_tr_b16 a[116:117], v10 offset:128\n"
    "ds_read_b64_tr_b16 a[118:119], v10 offset:896\n"
    "ds_read_b64_tr_b16 a[196:197], v10 offset:256\n"
    "ds_read_b64_tr_b16 a[198:199], v10 offset:1024\n"
    "v_add_u32 v11, 0, v6\n"
    "ds_read_b64_tr_b16 a[52:53], v11 offset:0\n"
    "ds_read_b64_tr_b16 a[54:55], v11 offset:768\n"
    "ds_read_b64_tr_b16 a[132:133], v11 offset:128\n"
    "ds_read_b64_tr_b16 a[134:135], v11 offset:896\n"
    "ds_read_b64_tr_b16 a[212:213], v11 offset:256\n"
    "ds_read_b64_tr_b16 a[214:215], v11 offset:1024\n"
    "s_waitcnt lgkmcnt(0)\n"
    "s_barrier\n"
    "s_mov_b32 s60, 6291456\n"
    "s_add_u32 m0, s51, 0\n"
    "s_nop 0\n"
    "buffer_load_dwordx4 v0, s[4:7], s60 offen offset:0 nt lds\n"
    "buffer_load_dwordx4 v1, s[4:7], s60 offen offset:1024 nt lds\n"
    "buffer_load_dwordx4 v2, s[4:7], s60 offen offset:2048 nt lds\n"
    "s_mov_b32 s60, 6684672\n"
    "s_add_u32 m0, s51, 12288\n"
    "s_nop 0\n"
    "buffer_load_dwordx4 v0, s[4:7], s60 offen offset:0 nt lds\n"
    "buffer_load_dwordx4 v1, s[4:7], s60 offen offset:1024 nt lds\n"
    "buffer_load_dwordx4 v2, s[4:7], s60 offen offset:2048 nt lds\n"
    "s_mov_b32 s60, 7077888\n"
    "s_add_u32 m0, s51, 24576\n"
    "s_nop 0\n"
    "buffer_load_dwordx4 v0, s[4:7], s60 offen offset:0 nt lds\n"
    "buffer_load_dwordx4 v1, s[4:7], s60 offen offset:1024 nt lds\n"
    "buffer_load_dwordx4 v2, s[4:7], s60 offen offset:2048 nt lds\n"
    "s_mov_b32 s60, 7471104\n"
    "s_add_u32 m0, s51, 36864\n"
    "s_nop 0\n"
    "buffer_load_dwordx4 v0, s[4:7], s60 offen offset:0 nt lds\n"
    "buffer_load_dwordx4 v1, s[4:7], s60 offen offset:1024 nt lds\n"
    "buffer_load_dwordx4 v2, s[4:7], s60 offen offset:2048 nt lds\n"
    "s_waitcnt vmcnt(25)\n"
    "s_barrier\n"
    "v_add_u32 v8, 49152, v3\n"
    "ds_read_b64_tr_b16 a[8:9], v8 offset:0\n"
    "ds_read_b64_tr_b16 a[10:11], v8 offset:768\n"
    "ds_read_b64_tr_b16 a[88:89], v8 offset:128\n"
    "ds_read_b64_tr_b16 a[90:91], v8 offset:896\n"
    "ds_read_b64_tr_b16 a[168:169], v8 offset:256\n"
    "ds_read_b64_tr_b16 a[170:171], v8 offset:1024\n"
    "v_add_u32 v9, 49152, v4\n"
    "ds_read_b64_tr_b16 a[24:25], v9 offset:0\n"
    "ds_read_b64_tr_b16 a[26:27], v9 offset:768\n"
    "ds_read_b64_tr_b16 a[104:105], v9 offset:128\n"
    "ds_read_b64_tr_b16 a[106:107], v9 offset:896\n"
    "ds_read_b64_tr_b16 a[184:185], v9 offset:256\n"
    "ds_read_b64_tr_b16 a[186:187], v9 offset:1024\n"
    "v_add_u32 v10, 49152, v5\n"
    "ds_read_b64_tr_b16 a[40:41], v10 offset:0\n"
    "ds_read_b64_tr_b16 a[42:43], v10 offset:768\n"
    "ds_read_b64_tr_b16 a[120:121], v10 offset:128\n"
    "ds_read_b64_tr_b16 a[122:123], v10 offset:896\n"
    "ds_read_b64_tr_b16 a[200:201], v10 offset:256\n"
    "ds_read_b64_tr_b16 a[202:203], v10 offset:1024\n"
    "v_add_u32 v11, 49152, v6\n"
    "ds_read_b64_tr_b16 a[56:57], v11 offset:0\n"
    "ds_read_b64_tr_b16 a[58:59], v11 offset:768\n"
    "ds_read_b64_tr_b16 a[136:137], v11 offset:128\n"
    "ds_read_b64_tr_b16 a[138:139], v11 offset:896\n"
    "ds_read_b64_tr_b16 a[216:217], v11 offset:256\n"
    "ds_read_b64_tr_b16 a[218:219], v11 offset:1024\n"
    "s_waitcnt lgkmcnt(0)\n"
    "s_barrier\n"
    "s_waitcnt vmcnt(12)\n"
    "s_barrier\n"
    "v_add_u32 v8, 98304, v3\n"
    "ds_read_b64_tr_b16 a[12:13], v8 offset:0\n"
    "ds_read_b64_tr_b16 a[14:15], v8 offset:768\n"
    "ds_read_b64_tr_b16 a[92:93], v8 offset:128\n"
    "ds_read_b64_tr_b16 a[94:95], v8 offset:896\n"
    "ds_read_b64_tr_b16 a[172:173], v8 offset:256\n"
    "ds_read_b64_tr_b16 a[174:175], v8 offset:1024\n"
    "v_add_u32 v9, 98304, v4\n"
    "ds_read_b64_tr_b16 a[28:29], v9 offset:0\n"
    "ds_read_b64_tr_b16 a[30:31], v9 offset:768\n"
    "ds_read_b64_tr_b16 a[108:109], v9 offset:128\n"
    "ds_read_b64_tr_b16 a[110:111], v9 offset:896\n"
    "ds_read_b64_tr_b16 a[188:189], v9 offset:256\n"
    "ds_read_b64_tr_b16 a[190:191], v9 offset:1024\n"
    "v_add_u32 v10, 98304, v5\n"
    "ds_read_b64_tr_b16 a[44:45], v10 offset:0\n"
    "ds_read_b64_tr_b16 a[46:47], v10 offset:768\n"
    "ds_read_b64_tr_b16 a[124:125], v10 offset:128\n"
    "ds_read_b64_tr_b16 a[126:127], v10 offset:896\n"
    "ds_read_b64_tr_b16 a[204:205], v10 offset:256\n"
    "ds_read_b64_tr_b16 a[206:207], v10 offset:1024\n"
    "v_add_u32 v11, 98304, v6\n"
    "ds_read_b64_tr_b16 a[60:61], v11 offset:0\n"
    "ds_read_b64_tr_b16 a[62:63], v11 offset:768\n"
    "ds_read_b64_tr_b16 a[140:141], v11 offset:128\n"
    "ds_read_b64_tr_b16 a[142:143], v11 offset:896\n"
    "ds_read_b64_tr_b16 a[220:221], v11 offset:256\n"
    "ds_read_b64_tr_b16 a[222:223], v11 offset:1024\n"
    "s_waitcnt lgkmcnt(0)\n"
    "s_barrier\n"
    "s_waitcnt vmcnt(0)\n"
    "s_barrier\n"
    "v_add_u32 v8, 0, v7\n"
    "ds_read_b64_tr_b16 a[64:65], v8 offset:0\n"
    "ds_read_b64_tr_b16 a[66:67], v8 offset:768\n"
    "ds_read_b64_tr_b16 a[144:145], v8 offset:128\n"
    "ds_read_b64_tr_b16 a[146:147], v8 offset:896\n"
    "ds_read_b64_tr_b16 a[224:225], v8 offset:256\n"
    "ds_read_b64_tr_b16 a[226:227], v8 offset:1024\n"
    "v_add_u32 v9, 12288, v7\n"
    "ds_read_b64_tr_b16 a[68:69], v9 offset:0\n"
    "ds_read_b64_tr_b16 a[70:71], v9 offset:768\n"
    "ds_read_b64_tr_b16 a[148:149], v9 offset:128\n"
    "ds_read_b64_tr_b16 a[150:151], v9 offset:896\n"
    "ds_read_b64_tr_b16 a[228:229], v9 offset:256\n"
    "ds_read_b64_tr_b16 a[230:231], v9 offset:1024\n"
    "v_add_u32 v10, 24576, v7\n"
    "ds_read_b64_tr_b16 a[72:73], v10 offset:0\n"
    "ds_read_b64_tr_b16 a[74:75], v10 offset:768\n"
    "ds_read_b64_tr_b16 a[152:153], v10 offset:128\n"
    "ds_read_b64_tr_b16 a[154:155], v10 offset:896\n"
    "ds_read_b64_tr_b16 a[232:233], v10 offset:256\n"
    "ds_read_b64_tr_b16 a[234:235], v10 offset:1024\n"
    "v_add_u32 v11, 36864, v7\n"
    "ds_read_b64_tr_b16 a[76:77], v11 offset:0\n"
    "ds_read_b64_tr_b16 a[78:79], v11 offset:768\n"
    "ds_read_b64_tr_b16 a[156:157], v11 offset:128\n"
    "ds_read_b64_tr_b16 a[158:159], v11 offset:896\n"
    "ds_read_b64_tr_b16 a[236:237], v11 offset:256\n"
    "ds_read_b64_tr_b16 a[238:239], v11 offset:1024\n"
    "s_waitcnt lgkmcnt(0)\n"
    "s_barrier\n"
    "ds_write_b128 v97, v[12:15] offset:0\n"
    "ds_write_b128 v97, v[16:19] offset:1024\n"
    "ds_write_b128 v97, v[20:23] offset:2048\n"
    "ds_write_b128 v97, v[24:27] offset:3072\n"
    "ds_write_b128 v97, v[28:31] offset:4096\n"
    "ds_write_b128 v97, v[32:35] offset:5120\n"
    "ds_write_b128 v97, v[36:39] offset:6144\n"
    "ds_write_b128 v97, v[40:43] offset:7168\n"
    "ds_write_b128 v97, v[44:47] offset:8192\n"
    "ds_write_b128 v97, v[48:51] offset:9216\n"
    "ds_write_b128 v97, v[52:55] offset:10240\n"
    "ds_write_b128 v97, v[56:59] offset:11264\n"
    "ds_write_b128 v97, v[60:63] offset:12288\n"
    "ds_write_b128 v97, v[64:67] offset:13312\n"
    "ds_write_b128 v97, v[68:71] offset:14336\n"
    "ds_write_b128 v97, v[72:75] offset:15360\n"
    "ds_write_b128 v97, v[76:79] offset:16384\n"
    "ds_write_b128 v97, v[80:83] offset:17408\n"
    "ds_write_b128 v97, v[84:87] offset:18432\n"
    "ds_write_b128 v97, v[88:91] offset:19456\n"
    "s_mov_b32 s46, -2\n"
    "s_mov_b32 s62, 1037384597\n"
    "s_mov_b32 s63, 1037384597\n"
    "s_mov_b32 s64, 858993459\n"
    "s_mov_b32 s65, 858993459\n"
    "s_mov_b32 s66, 67044351\n"
    "s_mov_b32 s67, 67044351\n"
    "v_mov_b32 v8, v99\n"
    "v_mov_b32 v9, v99\n"
    "v_mov_b32 v10, 137088\n"
    "ds_write_b64 v10, v[8:9] offset:0\n"
    "ds_write_b64 v10, v[8:9] offset:8\n"
    "ds_write_b64 v10, v[8:9] offset:1936\n"
    "ds_write_b64 v10, v[8:9] offset:1944\n"
    "ds_write_b64 v10, v[8:9] offset:3872\n"
    "ds_write_b64 v10, v[8:9] offset:3880\n"
    "ds_write_b64 v10, v[8:9] offset:5808\n"
    "ds_write_b64 v10, v[8:9] offset:5816\n"
    "s_lshl_b32 s59, s22, 11\n"
    "s_add_u32 s59, s59, 45056\n"
    "v_lshrrev_b32 v4, 2, v96\n"
    "v_and_b32 v6, 12, v4\n"
    "v_lshlrev_b32 v5, 1, v96\n"
    "v_and_b32 v5, 0x380, v5\n"
    "v_min_u32 v5, 0x200, v5\n"
    "v_add_u32 v5, v5, v6\n"
    "s_lshr_b32 s60, s23, 13\n"
    "s_mul_i32 s60, s60, 3\n"
    ".Lqk_po6_%=:\n"
    "buffer_load_dword v8, v4, s[0:3], s32 offen sc1\n"
    "s_waitcnt vmcnt(0)\n"
    "v_cmp_gt_i32 vcc, 1, v8\n"
    "s_nop 0\n"
    "s_cbranch_vccz .Lqk_pd7_%=\n"
    "s_add_u32 s40, s40, 1\n"
    "s_cmp_lt_u32 s40, 0x40000\n"
    "s_cbranch_scc1 .Lqk_sp8_%=\n"
    "v_mov_b32 v8, 20\n"
    "s_mov_b32 s44, 33728\n"
    "buffer_store_dword v8, off, s[0:3], s44 sc1\n"
    "s_waitcnt vmcnt(0)\n"
    "s_branch .Lqk_pd7_%=\n"
    ".Lqk_sp8_%=:\n"
    "s_sleep 4\n"
    "s_branch .Lqk_po6_%=\n"
    ".Lqk_pd7_%=:\n"
    ".Lqk_po9_%=:\n"
    "buffer_load_dword v8, v5, s[0:3], s33 offen sc1\n"
    "s_waitcnt vmcnt(0)\n"
    "v_cmp_gt_i32 vcc, s60, v8\n"
    "s_nop 0\n"
    "s_cbranch_vccz .Lqk_pd10_%=\n"
    "s_add_u32 s40, s40, 1\n"
    "s_cmp_lt_u32 s40, 0x40000\n"
    "s_cbranch_scc1 .Lqk_sp11_%=\n"
    "v_mov_b32 v8, 21\n"
    "s_mov_b32 s44, 33728\n"
    "buffer_store_dword v8, off, s[0:3], s44 sc1\n"
    "s_waitcnt vmcnt(0)\n"
    "s_branch .Lqk_pd10_%=\n"
    ".Lqk_sp11_%=:\n"
    "s_sleep 4\n"
    "s_branch .Lqk_po9_%=\n"
    ".Lqk_pd10_%=:\n"
    "s_add_u32 s53, s27, s23\n"
    "s_sub_u32 m0, s26, 8192\n"
    "s_nop 0\n"
    "buffer_load_dwordx4 v96, s[0:3], s53 offen offset:0 sc1 lds\n"
    "buffer_load_dwordx4 v96, s[0:3], s53 offen offset:1024 sc1 lds\n"
    "buffer_load_dwordx4 v96, s[0:3], s53 offen offset:2048 sc1 lds\n"
    "buffer_load_dwordx4 v96, s[0:3], s53 offen offset:3072 sc1 lds\n"
    "s_sub_u32 m0, s26, 4096\n"
    "s_add_u32 s53, s53, 4096\n"
    "buffer_load_dwordx4 v96, s[0:3], s53 offen offset:0 sc1 lds\n"
    "buffer_load_dwordx4 v96, s[0:3], s53 offen offset:1024 sc1 lds\n"
    ".Lqk_po12_%=:\n"
    "buffer_load_dword v8, v6, s[0:3], s34 offen sc1\n"
    "s_waitcnt vmcnt(0)\n"
    "v_cmp_gt_i32 vcc, 2, v8\n"
    "s_nop 0\n"
    "s_cbranch_vccz .Lqk_pd13_%=\n"
    "s_add_u32 s40, s40, 1\n"
    "s_cmp_lt_u32 s40, 0x40000\n"
    "s_cbranch_scc1 .Lqk_sp14_%=\n"
    "v_mov_b32 v8, 22\n"
    "s_mov_b32 s44, 33728\n"
    "buffer_store_dword v8, off, s[0:3], s44 sc1\n"
    "s_waitcnt vmcnt(0)\n"
    "s_branch .Lqk_pd13_%=\n"
    ".Lqk_sp14_%=:\n"
    "s_sleep 4\n"
    "s_branch .Lqk_po12_%=\n"
    ".Lqk_pd13_%=:\n"
    "s_memrealtime s[42:43]\n"
    "s_waitcnt lgkmcnt(0)\n"
    "s_sub_u32 s57, s42, 804\n"
    "s_mov_b32 s46, -1\n"
    "s_mov_b32 s47, -5\n"
    "s_mov_b32 s50, 2130706432\n"
    "s_mov_b32 s48, 2130706432\n"
    "s_mov_b32 s49, 4194304\n"
    "s_mov_b32 s55, s31\n"
    "s_add_u32 s54, s29, s23\n"
    "s_barrier\n"
    "v_mov_b32 v110, v104\n"
    "ds_read_b128 v[32:35], v92 offset:0\n"
    "ds_read_b128 v[36:39], v92 offset:1024\n"
    "ds_read_b128 v[40:43], v93 offset:0\n"
    "ds_read_b128 v[44:47], v93 offset:1024\n"
    "ds_read_b128 v[48:51], v94 offset:0\n"
    "ds_read_b128 v[52:55], v94 offset:1024\n"
    "s_waitcnt lgkmcnt(0)\n"
    "s_branch .Lqk_bulk_%=\n"
    ".Lqk_take_%=:\n"
    "s_waitcnt vmcnt(2)\n"
    ".Lqk_mk0x14_%=:\n"
    "ds_read_b128 v[56:59], v92 offset:8192\n"
    "s_waitcnt vmcnt(1)\n"
    "ds_read_b128 v[60:63], v92 offset:9216\n"
    "s_waitcnt lgkmcnt(1)\n"
    "v_smfmac_f32_16x16x64_bf16 v[8:11], v[56:59], v[32:39], v98\n"
    "v_max_u32 v108, v56, v58\n"
    "v_smfmac_f32_16x16x64_bf16 v[12:15], v[56:59], a[160:167], v98\n"
    "s_waitcnt lgkmcnt(0)\n"
    "ds_read_b128 v[32:35], v97 offset:6144\n"
    "v_max3_u32 v108, v108, v60, v62\n"
    "v_smfmac_f32_16x16x64_bf16 v[0:3], v[56:59], v[176:183], v98\n"
    "ds_read_b128 v[36:39], v97 offset:7168\n"
    "v_cmp_eq_u32 vcc, -1, v108\n"
    "v_smfmac_f32_16x16x64_bf16 v[4:7], v[56:59], a[80:87], v98\n"
    "s_cbranch_vccnz .Lqk_retry_%=\n"
    ".Lqk_go_%=:\n"
    "s_barrier\n"
    ".Lqk_mk1x15_%=:\n"
    "v_smfmac_f32_16x16x64_bf16 v[16:19], v[56:59], a[240:247], v98\n"
    "ds_read_b128 v[24:27], v93 offset:8192\n"
    "ds_read_b128 v[28:31], v93 offset:9216\n"
    "v_smfmac_f32_16x16x64_bf16 v[20:23], v[56:59], a[0:7], v98\n"
    "s_waitcnt vmcnt(0)\n"
    "ds_read_b128 v[64:67], v92 offset:10240\n"
    "v_smfmac_f32_16x16x64_bf16 v[8:11], v[60:63], v[40:47], v98\n"
    "ds_read_b128 v[56:59], v94 offset:9216\n"
    "v_smfmac_f32_16x16x64_bf16 v[12:15], v[60:63], a[168:175], v98\n"
    "ds_read_b128 v[40:43], v97 offset:8192\n"
    "ds_read_b128 v[44:47], v97 offset:9216\n"
    "v_smfmac_f32_16x16x64_bf16 v[0:3], v[60:63], v[184:191], v98\n"
    "v_smfmac_f32_16x16x64_bf16 v[4:7], v[60:63], a[88:95], v98\n"
    "v_smfmac_f32_16x16x64_bf16 v[16:19], v[60:63], a[248:255], v98\n"
    "s_waitcnt lgkmcnt(3)\n"
    "v_max_u32 v108, v64, v66\n"
    "v_smfmac_f32_16x16x64_bf16 v[20:23], v[60:63], a[8:15], v98\n"
    "v_cmp_eq_u32 vcc, -1, v108\n"
    "v_smfmac_f32_16x16x64_bf16 v[8:11], v[24:27], v[48:55], v98\n"
    "s_cbranch_vccnz .Lqk_r2_%=\n"
    ".Lqk_go2_%=:\n"
    "s_barrier\n"
    "v_smfmac_f32_16x16x64_bf16 v[12:15], v[24:27], a[176:183], v98\n"
    "s_memrealtime s[68:69]\n"
    "ds_read_b128 v[64:67], v94 offset:8192\n"
    "ds_read_b128 v[60:63], v95 offset:8192\n"
    "v_smfmac_f32_16x16x64_bf16 v[0:3], v[24:27], v[192:199], v98\n"
    "ds_read_b128 v[48:51], v97 offset:10240\n"
    "ds_read_b128 v[52:55], v97 offset:11264\n"
    "v_smfmac_f32_16x16x64_bf16 v[4:7], v[24:27], a[96:103], v98\n"
    "v_mov_b32 v109, s47\n"
    "v_smfmac_f32_16x16x64_bf16 v[16:19], v[24:27], v[112:119], v98\n"
    "v_lshlrev_b32 v110, 10, v96\n"
    "v_smfmac_f32_16x16x64_bf16 v[20:23], v[24:27], a[16:23], v98\n"
    "ds_read_b128 v[24:27], v95 offset:9216\n"
    "s_mov_b64 vcc, s[64:65]\n"
    "v_smfmac_f32_16x16x64_bf16 v[8:11], v[28:31], v[32:39], v98\n"
    "ds_read_b128 v[32:35], v97 offset:12288\n"
    "ds_read_b128 v[36:39], v97 offset:13312\n"
    "v_smfmac_f32_16x16x64_bf16 v[12:15], v[28:31], a[184:191], v98\n"
    "v_smfmac_f32_16x16x64_bf16 v[0:3], v[28:31], v[200:207], v98\n"
    "v_smfmac_f32_16x16x64_bf16 v[4:7], v[28:31], a[104:111], v98\n"
    "v_smfmac_f32_16x16x64_bf16 v[16:19], v[28:31], v[120:127], v98\n"
    "v_smfmac_f32_16x16x64_bf16 v[20:23], v[28:31], a[24:31], v98\n"
    "s_waitcnt lgkmcnt(0)\n"
    "v_smfmac_f32_16x16x64_bf16 v[8:11], v[64:67], v[40:47], v98\n"
    "s_sub_u32 s44, s68, s57\n"
    "ds_read_b128 v[40:43], v97 offset:14336\n"
    "ds_read_b128 v[44:47], v97 offset:15360\n"
    "v_smfmac_f32_16x16x64_bf16 v[12:15], v[64:67], a[192:199], v98\n"
    "ds_read_b128 v[28:31], v96 offset:10240\n"
    "s_add_u32 s45, s68, 18\n"
    "v_smfmac_f32_16x16x64_bf16 v[0:3], v[64:67], v[208:215], v98\n"
    "s_cmp_gt_i32 s44, 6\n"
    "s_cselect_b32 s57, s45, s57\n"
    "v_mov_b32 v108, s57\n"
    "v_smfmac_f32_16x16x64_bf16 v[4:7], v[64:67], a[112:119], v98\n"
    "s_mov_b64 exec, 15\n"
    "buffer_store_dwordx2 v[108:109], v110, s[0:3], s56 offen sc1\n"
    "s_mov_b64 exec, -1\n"
    "v_smfmac_f32_16x16x64_bf16 v[16:19], v[64:67], v[128:135], v98\n"
    "v_smfmac_f32_16x16x64_bf16 v[20:23], v[64:67], a[32:39], v98\n"
    "v_bfe_u32 v111, v96, 8, 2\n"
    "v_smfmac_f32_16x16x64_bf16 v[8:11], v[56:59], v[48:55], v98\n"
    "ds_read_b128 v[48:51], v97 offset:16384\n"
    "ds_read_b128 v[52:55], v97 offset:17408\n"
    "v_smfmac_f32_16x16x64_bf16 v[12:15], v[56:59], a[200:207], v98\n"
    "ds_read_b128 v[64:67], v96 offset:21504\n"
    "v_mad_i32_i24 v110, v111, -8, v104\n"
    "v_smfmac_f32_16x16x64_bf16 v[8:11], v[60:63], v[32:39], v98\n"
    "ds_read_b128 v[32:35], v97 offset:18432\n"
    "ds_read_b128 v[36:39], v97 offset:19456\n"
    "v_smfmac_f32_16x16x64_bf16 v[12:15], v[60:63], a[208:215], v98\n"
    "s_waitcnt lgkmcnt(5)\n"
    "v_mad_i32_i24 v111, v111, -8, v102\n"
    "v_smfmac_f32_16x16x64_bf16 v[8:11], v[24:27], v[40:47], v98\n"
    "v_smfmac_f32_16x16x64_bf16 v[12:15], v[24:27], a[216:223], v98\n"
    "v_smfmac_f32_16x16x64_bf16 v[0:3], v[56:59], v[216:223], v98\n"
    "v_smfmac_f32_16x16x64_bf16 v[4:7], v[56:59], a[120:127], v98\n"
    "s_waitcnt lgkmcnt(3)\n"
    "v_smfmac_f32_16x16x64_bf16 v[8:11], v[28:31], v[48:55], v98\n"
    "v_smfmac_f32_16x16x64_bf16 v[12:15], v[28:31], a[224:231], v98\n"
    "s_waitcnt lgkmcnt(0)\n"
    "v_smfmac_f32_16x16x64_bf16 v[8:11], v[64:67], v[32:39], v98\n"
    "v_smfmac_f32_16x16x64_bf16 v[12:15], v[64:67], a[232:239], v98\n"
    "ds_read_b128 v[32:35], v92 offset:0\n"
    "ds_read_b128 v[36:39], v92 offset:1024\n"
    "v_smfmac_f32_16x16x64_bf16 v[0:3], v[60:63], v[224:231], v98\n"
    "s_nop 1\n"
    "v_add_f32 v8, v8, v10\n"
    "v_smfmac_f32_16x16x64_bf16 v[4:7], v[60:63], a[128:135], v98\n"
    "ds_read_b128 v[40:43], v93 offset:0\n"
    "v_add_f32 v9, v9, v11\n"
    "v_smfmac_f32_16x16x64_bf16 v[0:3], v[24:27], v[232:239], v98\n"
    "ds_read_b128 v[44:47], v93 offset:1024\n"
    "v_cvt_pk_bf16_f32 v8, v8, v9\n"
    "v_smfmac_f32_16x16x64_bf16 v[0:3], v[28:31], v[240:247], v98\n"
    "ds_read_b128 v[48:51], v94 offset:0\n"
    "v_add_f32 v12, v12, v14\n"
    "v_smfmac_f32_16x16x64_bf16 v[0:3], v[64:67], v[248:255], v98\n"
    "ds_read_b128 v[52:55], v94 offset:1024\n"
    "v_add_f32 v13, v13, v15\n"
    "v_smfmac_f32_16x16x64_bf16 v[4:7], v[24:27], a[136:143], v98\n"
    "v_cvt_pk_bf16_f32 v9, v12, v13\n"
    "v_smfmac_f32_16x16x64_bf16 v[4:7], v[28:31], a[144:151], v98\n"
    "s_nop 0\n"
    "v_mfma_f32_16x16x16_bf16 v[12:15], v[8:9], v[100:101], 0\n"
    "v_add_f32 v0, v0, v2\n"
    "v_add_f32 v1, v1, v3\n"
    "v_smfmac_f32_16x16x64_bf16 v[4:7], v[64:67], a[152:159], v98\n"
    "v_cvt_pk_bf16_f32 v0, v0, v1\n"
    "v_smfmac_f32_16x16x64_bf16 v[16:19], v[56:59], v[136:143], v98\n"
    "s_nop 1\n"
    "v_cvt_pk_bf16_f32 v15, v14, v15\n"
    "v_smfmac_f32_16x16x64_bf16 v[20:23], v[56:59], a[40:47], v98\n"
    "s_nop 0\n"
    "v_add_f32 v4, v4, v6\n"
    "v_smfmac_f32_16x16x64_bf16 v[16:19], v[60:63], v[144:151], v98\n"
    "v_add_f32 v5, v5, v7\n"
    "v_smfmac_f32_16x16x64_bf16 v[20:23], v[60:63], a[48:55], v98\n"
    "v_cvt_pk_bf16_f32 v1, v4, v5\n"
    "v_smfmac_f32_16x16x64_bf16 v[16:19], v[24:27], v[152:159], v98\n"
    "s_nop 0\n"
    "v_mfma_f32_16x16x16_bf16 v[4:7], v[0:1], v[100:101], 0\n"
    "v_cvt_pk_bf16_f32 v14, v12, v13\n"
    "v_smfmac_f32_16x16x64_bf16 v[20:23], v[24:27], a[56:63], v98\n"
    "v_smfmac_f32_16x16x64_bf16 v[16:19], v[28:31], v[160:167], v98\n"
    "s_nop 1\n"
    "v_cvt_pk_bf16_f32 v13, v6, v7\n"
    "v_smfmac_f32_16x16x64_bf16 v[20:23], v[28:31], a[64:71], v98\n"
    "v_cvt_pk_bf16_f32 v12, v4, v5\n"
    "v_smfmac_f32_16x16x64_bf16 v[16:19], v[64:67], v[168:175], v98\n"
    "v_smfmac_f32_16x16x64_bf16 v[20:23], v[64:67], a[72:79], v98\n"
    ".Lqk_mk2x16_%=:\n"
    "v_mfma_f32_16x16x32_bf16 v[24:27], v[12:15], v[12:15], 0\n"
    "s_nop 1\n"
    "v_add_f32 v16, v16, v18\n"
    "v_add_f32 v17, v17, v19\n"
    "v_cvt_pk_bf16_f32 v16, v16, v17\n"
    "v_add_f32 v20, v20, v22\n"
    "v_add_f32 v21, v21, v23\n"
    "v_cvt_pk_bf16_f32 v17, v20, v21\n"
    "s_nop 1\n"
    "v_mfma_f32_16x16x16_bf16 v[20:23], v[16:17], v[100:101], 0\n"
    "v_mfma_f32_16x16x32_bf16 v[0:3], v[72:75], v[12:15], 0\n"
    "v_mfma_f32_16x16x32_bf16 v[8:11], v[68:71], v[12:15], 0\n"
    "s_nop 1\n"
    "v_cvt_pk_bf16_f32 v17, v22, v23\n"
    "v_cvt_pk_bf16_f32 v16, v20, v21\n"
    "s_nop 1\n"
    "v_mfma_f32_16x16x16_bf16 v[24:27], v[16:17], v[16:17], v[24:27]\n"
    "v_mfma_f32_16x16x16_bf16 v[0:3], v[78:79], v[16:17], v[0:3]\n"
    "v_mfma_f32_16x16x16_bf16 v[8:11], v[76:77], v[16:17], v[8:11]\n"
    "s_nop 1\n"
    "ds_write_b64 v111, v[26:27] offset:128\n"
    "v_mfma_f32_16x16x32_bf16 v[56:59], v[12:15], v[80:83], 0\n"
    "s_nop 3\n"
    "v_cndmask_b32_dpp v8, v0, v8, vcc quad_perm:[0,1,0,1] row_mask:0xf bank_mask:0xf\n"
    "v_cndmask_b32_dpp v9, v1, v9, vcc quad_perm:[0,1,0,1] row_mask:0xf bank_mask:0xf\n"
    "v_cndmask_b32_dpp v10, v2, v10, vcc quad_perm:[0,1,0,1] row_mask:0xf bank_mask:0xf\n"
    "v_cndmask_b32_dpp v11, v3, v11, vcc quad_perm:[0,1,0,1] row_mask:0xf bank_mask:0xf\n"
    "ds_write_b128 v102, v[8:11]\n"
    "v_mfma_f32_16x16x32_bf16 v[60:63], v[12:15], v[84:87], 0\n"
    "s_mov_b64 exec, s[20:21]\n"
    "s_waitcnt lgkmcnt(0)\n"
    "s_barrier\n"
    ".Lqk_mk3x17_%=:\n"
    "ds_read_b128 v[72:75], v103 offset:0\n"
    "ds_read_b128 v[76:79], v103 offset:1936\n"
    "ds_read_b128 v[80:83], v103 offset:3872\n"
    "ds_read_b128 v[84:87], v103 offset:5808\n"
    "s_mov_b64 exec, -1\n"
    "v_mfma_f32_16x16x16_bf16 v[56:59], v[16:17], v[88:89], v[56:59]\n"
    "v_mfma_f32_16x16x16_bf16 v[60:63], v[16:17], v[90:91], v[60:63]\n"
    "s_waitcnt lgkmcnt(2)\n"
    "v_pk_add_f32 v[72:73], v[72:73], v[76:77]\n"
    "v_pk_add_f32 v[74:75], v[74:75], v[78:79]\n"
    "s_waitcnt lgkmcnt(1)\n"
    "v_pk_add_f32 v[72:73], v[72:73], v[80:81]\n"
    "v_pk_add_f32 v[74:75], v[74:75], v[82:83]\n"
    "s_waitcnt lgkmcnt(0)\n"
    "v_pk_add_f32 v[72:73], v[72:73], v[84:85]\n"
    "v_pk_add_f32 v[74:75], v[74:75], v[86:87]\n"
    "v_max3_f32 v76, v72, v73, v74\n"
    "v_max_f32 v76, v76, v75\n"
    "v_pk_mul_f32 v[72:73], v[72:73], s[62:63]\n"
    "v_pk_mul_f32 v[74:75], v[74:75], s[62:63]\n"
    "v_max_f32_dpp v76, v76, v76 row_ror:8 row_mask:0xf bank_mask:0xf\n"
    "s_nop 1\n"
    "v_max_f32_dpp v76, v76, v76 row_ror:4 row_mask:0xf bank_mask:0xf\n"
    "s_nop 1\n"
    "v_max_f32_dpp v76, v76, v76 row_ror:2 row_mask:0xf bank_mask:0xf\n"
    "s_nop 1\n"
    "v_max_f32_dpp v76, v76, v76 row_ror:1 row_mask:0xf bank_mask:0xf\n"
    "v_pk_fma_f32 v[72:73], v[76:77], s[62:63], v[72:73] op_sel_hi:[0,1,1] neg_lo:[1,0,0] neg_hi:[1,0,0]\n"
    "v_pk_fma_f32 v[74:75], v[76:77], s[62:63], v[74:75] op_sel_hi:[0,1,1] neg_lo:[1,0,0] neg_hi:[1,0,0]\n"
    "v_exp_f32 v72, v72\n"
    "v_exp_f32 v74, v74\n"
    "v_exp_f32 v73, v73\n"
    "v_exp_f32 v75, v75\n"
    "v_add_f32 v76, v72, v74\n"
    "v_add_f32 v77, v73, v75\n"
    "v_add_f32 v76, v76, v77\n"
    "s_nop 1\n"
    "v_add_f32_dpp v76, v76, v76 row_ror:8 row_mask:0xf bank_mask:0xf\n"
    "s_nop 1\n"
    "v_add_f32_dpp v76, v76, v76 row_ror:4 row_mask:0xf bank_mask:0xf\n"
    "s_nop 1\n"
    "v_add_f32_dpp v76, v76, v76 row_ror:2 row_mask:0xf bank_mask:0xf\n"
    "s_nop 1\n"
    "v_add_f32_dpp v76, v76, v76 row_ror:1 row_mask:0xf bank_mask:0xf\n"
    "v_rcp_f32 v76, v76\n"
    "s_nop 0\n"
    "v_pk_mul_f32 v[72:73], v[72:73], v[76:77] op_sel_hi:[1,0] clamp\n"
    "v_pk_mul_f32 v[74:75], v[74:75], v[76:77] op_sel_hi:[1,0] clamp\n"
    "v_cvt_pk_bf16_f32 v72, v72, v73\n"
    "v_cvt_pk_bf16_f32 v73, v74, v75\n"
    "buffer_store_dwordx2 v[72:73], v106, s[0:3], s48 offen sc1\n"
    ".Lqk_mk4x18_%=:\n"
    ".Lqk_bulk_%=:\n"
    "v_mfma_f32_16x16x32_bf16 v[0:3], a[80:83], v[32:35], 0\n"
    "ds_write_b64 v110, v[58:59] offset:224\n"
    "v_mfma_f32_16x16x32_bf16 v[4:7], a[80:83], v[36:39], 0\n"
    "ds_write_b64 v110, v[62:63] offset:128\n"
    ".Lqk_mkL0_%=:\n"
    "v_mfma_f32_16x16x32_bf16 v[8:11], a[160:163], v[32:35], 0\n"
    "v_mfma_f32_16x16x32_bf16 v[12:15], a[160:163], v[36:39], 0\n"
    "v_mfma_f32_16x16x32_bf16 v[16:19], a[0:3], v[32:35], 0\n"
    "v_mfma_f32_16x16x32_bf16 v[20:23], a[0:3], v[36:39], 0\n"
    "s_waitcnt lgkmcnt(0)\n"
    "s_barrier\n"
    "ds_read_b128 v[56:59], v105 offset:0\n"
    "v_mfma_f32_16x16x32_bf16 v[24:27], a[240:243], v[32:35], 0\n"
    "ds_read_b128 v[60:63], v105 offset:4352\n"
    "v_mfma_f32_16x16x32_bf16 v[28:31], a[240:243], v[36:39], 0\n"
    "v_mfma_f32_16x16x32_bf16 v[0:3], a[96:99], v[40:43], v[0:3]\n"
    "s_sub_u32 m0, s26, 4096\n"
    "ds_read_b128 v[32:35], v95 offset:0\n"
    "ds_read_b128 v[36:39], v95 offset:1024\n"
    "v_mfma_f32_16x16x32_bf16 v[4:7], a[96:99], v[44:47], v[4:7]\n"
    "ds_read_b128 v[64:67], v105 offset:8704\n"
    "v_mfma_f32_16x16x32_bf16 v[8:11], a[176:179], v[40:43], v[8:11]\n"
    "buffer_load_dwordx4 v96, s[0:3], s53 offen offset:2048 sc1 lds\n"
    "v_mfma_f32_16x16x32_bf16 v[12:15], a[176:179], v[44:47], v[12:15]\n"
    "ds_read_b128 v[68:71], v105 offset:13056\n"
    "v_mfma_f32_16x16x32_bf16 v[16:19], a[16:19], v[40:43], v[16:19]\n"
    "v_mfma_f32_16x16x32_bf16 v[20:23], a[16:19], v[44:47], v[20:23]\n"
    "v_mfma_f32_16x16x32_bf16 v[24:27], v[112:115], v[40:43], v[24:27]\n"
    "buffer_load_dwordx4 v96, s[0:3], s53 offen offset:3072 sc1 lds\n"
    "v_mfma_f32_16x16x32_bf16 v[28:31], v[112:115], v[44:47], v[28:31]\n"
    "s_mov_b32 m0, s59\n"
    "s_add_u32 s53, s53, s28\n"
    "s_waitcnt lgkmcnt(4)\n"
    "v_mfma_f32_16x16x32_bf16 v[0:3], a[112:115], v[48:51], v[0:3]\n"
    "ds_read_b128 v[40:43], v92 offset:2048\n"
    "ds_read_b128 v[44:47], v92 offset:3072\n"
    "v_mfma_f32_16x16x32_bf16 v[4:7], a[112:115], v[52:55], v[4:7]\n"
    "v_add_f32 v56, v56, v60\n"
    "v_mfma_f32_16x16x32_bf16 v[8:11], a[192:195], v[48:51], v[8:11]\n"
    "buffer_load_dwordx4 v96, s[0:3], s53 offen offset:0 sc1 lds\n"
    "v_mfma_f32_16x16x32_bf16 v[12:15], a[192:195], v[52:55], v[12:15]\n"
    "v_add_f32 v57, v57, v61\n"
    "v_add_f32 v58, v58, v62\n"
    "v_mfma_f32_16x16x32_bf16 v[16:19], a[32:35], v[48:51], v[16:19]\n"
    "v_add_f32 v59, v59, v63\n"
    "s_waitcnt lgkmcnt(3)\n"
    "v_mfma_f32_16x16x32_bf16 v[20:23], a[32:35], v[52:55], v[20:23]\n"
    "v_add_f32 v56, v56, v64\n"
    ".Lqk_mkL1_%=:\n"
    "v_mfma_f32_16x16x32_bf16 v[24:27], v[128:131], v[48:51], v[24:27]\n"
    "buffer_load_dwordx4 v96, s[0:3], s53 offen offset:1024 sc1 lds\n"
    "v_mfma_f32_16x16x32_bf16 v[28:31], v[128:131], v[52:55], v[28:31]\n"
    "s_waitcnt lgkmcnt(4)\n"
    "v_mov_b64 v[110:111], -1\n"
    "v_add_f32 v57, v57, v65\n"
    "v_mfma_f32_16x16x32_bf16 v[0:3], a[128:131], v[32:35], v[0:3]\n"
    "ds_read_b128 v[48:51], v93 offset:2048\n"
    "ds_read_b128 v[52:55], v93 offset:3072\n"
    "v_mfma_f32_16x16x32_bf16 v[4:7], a[128:131], v[36:39], v[4:7]\n"
    "v_add_f32 v58, v58, v66\n"
    "v_mfma_f32_16x16x32_bf16 v[8:11], a[208:211], v[32:35], v[8:11]\n"
    "buffer_load_dwordx4 v[80:83], v96, s[0:3], s54 offen sc1\n"
    "v_mfma_f32_16x16x32_bf16 v[12:15], a[208:211], v[36:39], v[12:15]\n"
    "v_add_f32 v59, v59, v67\n"
    "s_waitcnt lgkmcnt(4)\n"
    "v_mfma_f32_16x16x32_bf16 v[16:19], a[48:51], v[32:35], v[16:19]\n"
    "v_add_f32 v56, v56, v68\n"
    "v_add_f32 v57, v57, v69\n"
    "v_mfma_f32_16x16x32_bf16 v[20:23], a[48:51], v[36:39], v[20:23]\n"
    "v_add_f32 v58, v58, v70\n"
    "v_mfma_f32_16x16x32_bf16 v[24:27], v[144:147], v[32:35], v[24:27]\n"
    "buffer_load_dwordx4 v[84:87], v96, s[0:3], s54 offen offset:1024 sc1\n"
    "v_mfma_f32_16x16x32_bf16 v[28:31], v[144:147], v[36:39], v[28:31]\n"
    "s_waitcnt lgkmcnt(2)\n"
    "v_add_f32 v59, v59, v71\n"
    "s_barrier\n"
    "v_mfma_f32_16x16x32_bf16 v[0:3], a[84:87], v[40:43], v[0:3]\n"
    "ds_read_b128 v[32:35], v94 offset:2048\n"
    "ds_read_b128 v[36:39], v94 offset:3072\n"
    "v_mfma_f32_16x16x32_bf16 v[4:7], a[84:87], v[44:47], v[4:7]\n"
    "v_cndmask_b32 v56, v99, v56, s[12:13]\n"
    "v_cndmask_b32 v57, v99, v57, s[14:15]\n"
    "v_mfma_f32_16x16x32_bf16 v[8:11], a[164:167], v[40:43], v[8:11]\n"
    "buffer_store_dwordx2 v[110:111], v106, s[0:3], s49 offen sc1\n"
    "v_cndmask_b32 v58, v99, v58, s[16:17]\n"
    "v_mfma_f32_16x16x32_bf16 v[12:15], a[164:167], v[44:47], v[12:15]\n"
    "v_cndmask_b32 v59, v99, v59, s[18:19]\n"
    "v_max3_f32 v72, v56, v57, v58\n"
    "v_mfma_f32_16x16x32_bf16 v[16:19], a[4:7], v[40:43], v[16:19]\n"
    "v_max_f32 v72, v72, v59\n"
    "v_cndmask_b32 v74, v99, v72, s[66:67]\n"
    "v_mfma_f32_16x16x32_bf16 v[20:23], a[4:7], v[44:47], v[20:23]\n"
    "v_cndmask_b32 v75, v72, v99, s[66:67]\n"
    "v_max_f32_dpp v74, v74, v74 row_ror:8 row_mask:0xf bank_mask:0xf\n"
    "v_mfma_f32_16x16x32_bf16 v[24:27], a[244:247], v[40:43], v[24:27]\n"
    "v_max_f32_dpp v75, v75, v75 row_ror:8 row_mask:0xf bank_mask:0xf\n"
    "v_max_f32_dpp v74, v74, v74 row_ror:4 row_mask:0xf bank_mask:0xf\n"
    "v_mfma_f32_16x16x32_bf16 v[28:31], a[244:247], v[44:47], v[28:31]\n"
    "s_waitcnt lgkmcnt(2)\n"
    "v_max_f32_dpp v75, v75, v75 row_ror:4 row_mask:0xf bank_mask:0xf\n"
    "v_max_f32_dpp v74, v74, v74 row_ror:2 row_mask:0xf bank_mask:0xf\n"
    "v_mfma_f32_16x16x32_bf16 v[0:3], a[100:103], v[48:51], v[0:3]\n"
    "ds_read_b128 v[40:43], v95 offset:2048\n"
    "ds_read_b128 v[44:47], v95 offset:3072\n"
    "v_mfma_f32_16x16x32_bf16 v[4:7], a[100:103], v[52:55], v[4:7]\n"
    "v_max_f32_dpp v75, v75, v75 row_ror:2 row_mask:0xf bank_mask:0xf\n"
    "v_max_f32_dpp v74, v74, v74 row_ror:1 row_mask:0xf bank_mask:0xf\n"
    "v_mfma_f32_16x16x32_bf16 v[8:11], a[180:183], v[48:51], v[8:11]\n"
    "buffer_store_dwordx2 v[110:111], v107, s[0:3], s49 offen sc1\n"
    "v_max_f32_dpp v75, v75, v75 row_ror:1 row_mask:0xf bank_mask:0xf\n"
    ".Lqk_mkL2_%=:\n"
    "v_mfma_f32_16x16x32_bf16 v[12:15], a[180:183], v[52:55], v[12:15]\n"
    "v_cndmask_b32 v72, v75, v74, s[66:67]\n"
    "v_mul_f32 v72, s62, v72\n"
    "v_mfma_f32_16x16x32_bf16 v[16:19], a[20:23], v[48:51], v[16:19]\n"
    "v_fma_f32 v56, v56, s62, -v72\n"
    "v_fma_f32 v57, v57, s62, -v72\n"
    "v_mfma_f32_16x16x32_bf16 v[20:23], a[20:23], v[52:55], v[20:23]\n"
    "v_fma_f32 v58, v58, s62, -v72\n"
    "v_fma_f32 v59, v59, s62, -v72\n"
    "v_mfma_f32_16x16x32_bf16 v[24:27], v[116:119], v[48:51], v[24:27]\n"
    "v_exp_f32 v56, v56\n"
    "v_mfma_f32_16x16x32_bf16 v[28:31], v[116:119], v[52:55], v[28:31]\n"
    "s_waitcnt lgkmcnt(2)\n"
    "v_exp_f32 v58, v58\n"
    "v_mfma_f32_16x16x32_bf16 v[0:3], a[116:119], v[32:35], v[0:3]\n"
    "ds_read_b128 v[48:51], v92 offset:4096\n"
    "ds_read_b128 v[52:55], v92 offset:5120\n"
    "v_mfma_f32_16x16x32_bf16 v[4:7], a[116:119], v[36:39], v[4:7]\n"
    "v_mfma_f32_16x16x32_bf16 v[8:11], a[196:199], v[32:35], v[8:11]\n"
    "buffer_load_dwordx2 v[108:109], off, s[0:3], s55 sc1\n"
    "v_mfma_f32_16x16x32_bf16 v[12:15], a[196:199], v[36:39], v[12:15]\n"
    "v_exp_f32 v57, v57\n"
    "v_mfma_f32_16x16x32_bf16 v[16:19], a[36:39], v[32:35], v[16:19]\n"
    "v_exp_f32 v59, v59\n"
    "v_mfma_f32_16x16x32_bf16 v[20:23], a[36:39], v[36:39], v[20:23]\n"
    "v_add_f32 v72, v56, v58\n"
    "v_add_f32 v73, v57, v59\n"
    "v_mfma_f32_16x16x32_bf16 v[24:27], v[132:135], v[32:35], v[24:27]\n"
    "v_add_f32 v72, v72, v73\n"
    "v_cndmask_b32 v74, 0, v72, s[66:67]\n"
    "v_mfma_f32_16x16x32_bf16 v[28:31], v[132:135], v[36:39], v[28:31]\n"
    "s_waitcnt lgkmcnt(2)\n"
    "v_cndmask_b32 v75, v72, 0, s[66:67]\n"
    "v_add_f32_dpp v74, v74, v74 row_ror:8 row_mask:0xf bank_mask:0xf\n"
    "v_mfma_f32_16x16x32_bf16 v[0:3], a[132:135], v[40:43], v[0:3]\n"
    "ds_read_b128 v[32:35], v93 offset:4096\n"
    "ds_read_b128 v[36:39], v93 offset:5120\n"
    "v_mfma_f32_16x16x32_bf16 v[4:7], a[132:135], v[44:47], v[4:7]\n"
    "v_add_f32_dpp v75, v75, v75 row_ror:8 row_mask:0xf bank_mask:0xf\n"
    "v_add_f32_dpp v74, v74, v74 row_ror:4 row_mask:0xf bank_mask:0xf\n"
    "v_mfma_f32_16x16x32_bf16 v[8:11], a[212:215], v[40:43], v[8:11]\n"
    "v_add_f32_dpp v75, v75, v75 row_ror:4 row_mask:0xf bank_mask:0xf\n"
    "v_add_f32_dpp v74, v74, v74 row_ror:2 row_mask:0xf bank_mask:0xf\n"
    "v_mfma_f32_16x16x32_bf16 v[12:15], a[212:215], v[44:47], v[12:15]\n"
    "v_add_f32_dpp v75, v75, v75 row_ror:2 row_mask:0xf bank_mask:0xf\n"
    "v_add_f32_dpp v74, v74, v74 row_ror:1 row_mask:0xf bank_mask:0xf\n"
    "v_mfma_f32_16x16x32_bf16 v[16:19], a[52:55], v[40:43], v[16:19]\n"
    "v_add_f32_dpp v75, v75, v75 row_ror:1 row_mask:0xf bank_mask:0xf\n"
    "v_cndmask_b32 v72, v75, v74, s[66:67]\n"
    "v_mfma_f32_16x16x32_bf16 v[20:23], a[52:55], v[44:47], v[20:23]\n"
    "v_rcp_f32 v72, v72\n"
    "v_mfma_f32_16x16x32_bf16 v[24:27], v[148:151], v[40:43], v[24:27]\n"
    "v_mul_f32 v56, v56, v72 clamp\n"
    "v_mul_f32 v57, v57, v72 clamp\n"
    "v_mfma_f32_16x16x32_bf16 v[28:31], v[148:151], v[44:47], v[28:31]\n"
    "s_waitcnt lgkmcnt(2)\n"
    "v_mul_f32 v58, v58, v72 clamp\n"
    "v_mul_f32 v59, v59, v72 clamp\n"
    "v_mfma_f32_16x16x32_bf16 v[0:3], a[88:91], v[48:51], v[0:3]\n"
    "ds_read_b128 v[40:43], v94 offset:4096\n"
    "ds_read_b128 v[44:47], v94 offset:5120\n"
    "v_mfma_f32_16x16x32_bf16 v[4:7], a[88:91], v[52:55], v[4:7]\n"
    "v_cvt_pk_bf16_f32 v56, v56, v57\n"
    "v_cvt_pk_bf16_f32 v57, v58, v59\n"
    "v_mfma_f32_16x16x32_bf16 v[8:11], a[168:171], v[48:51], v[8:11]\n"
    "buffer_store_dwordx2 v[56:57], v107, s[0:3], s50 offen sc1\n"
    ".Lqk_mkL3_%=:\n"
    "v_mfma_f32_16x16x32_bf16 v[12:15], a[168:171], v[52:55], v[12:15]\n"
    "s_cmp_eq_u32 s46, 63\n"
    "s_cbranch_scc1 .Lqk_end_%=\n"
    "v_mfma_f32_16x16x32_bf16 v[16:19], a[8:11], v[48:51], v[16:19]\n"
    "s_add_u32 s46, s46, 1\n"
    "s_add_u32 s60, s46, 1\n"
    "s_lshl_b32 s53, s60, 19\n"
    "v_mfma_f32_16x16x32_bf16 v[20:23], a[8:11], v[52:55], v[20:23]\n"
    "s_lshl_b32 s45, s60, 11\n"
    "s_xor_b32 s45, s45, s23\n"
    "s_and_b32 s45, s45, 0x2000\n"
    "v_mfma_f32_16x16x32_bf16 v[24:27], a[248:251], v[48:51], v[24:27]\n"
    "s_add_u32 s53, s53, s45\n"
    "s_add_u32 s53, s53, s27\n"
    "s_add_u32 s48, s46, 0\n"
    "v_mfma_f32_16x16x32_bf16 v[28:31], a[248:251], v[52:55], v[28:31]\n"
    "s_waitcnt lgkmcnt(2)\n"
    "s_and_b32 s48, s48, 3\n"
    "s_lshl_b32 s48, s48, 22\n"
    "v_mfma_f32_16x16x32_bf16 v[0:3], a[104:107], v[32:35], v[0:3]\n"
    "s_add_u32 s49, s46, 2\n"
    "ds_read_b128 v[48:51], v95 offset:4096\n"
    "ds_read_b128 v[52:55], v95 offset:5120\n"
    "v_mfma_f32_16x16x32_bf16 v[4:7], a[104:107], v[36:39], v[4:7]\n"
    "s_and_b32 s49, s49, 3\n"
    "s_lshl_b32 s49, s49, 22\n"
    "s_and_b32 s55, s46, 63\n"
    "v_mfma_f32_16x16x32_bf16 v[8:11], a[184:187], v[32:35], v[8:11]\n"
    "s_lshl_b32 s55, s55, 7\n"
    "s_mov_b32 s50, s48\n"
    "s_add_u32 s51, s24, s48\n"
    "v_mfma_f32_16x16x32_bf16 v[12:15], a[184:187], v[36:39], v[12:15]\n"
    "s_add_u32 s52, s25, s48\n"
    "s_add_u32 s56, s30, s55\n"
    "s_add_u32 s55, s31, s55\n"
    "v_mfma_f32_16x16x32_bf16 v[16:19], a[24:27], v[32:35], v[16:19]\n"
    "s_lshl_b32 s54, s60, 20\n"
    "s_lshl_b32 s45, s60, 12\n"
    "s_xor_b32 s45, s45, s23\n"
    "v_mfma_f32_16x16x32_bf16 v[20:23], a[24:27], v[36:39], v[20:23]\n"
    "s_and_b32 s45, s45, 0x2000\n"
    "s_add_u32 s54, s54, s45\n"
    "s_add_u32 s54, s54, s29\n"
    "v_mfma_f32_16x16x32_bf16 v[24:27], v[120:123], v[32:35], v[24:27]\n"
    "v_mfma_f32_16x16x32_bf16 v[28:31], v[120:123], v[36:39], v[28:31]\n"
    "s_waitcnt lgkmcnt(2)\n"
    "s_waitcnt vmcnt(6) lgkmcnt(0)\n"
    "s_barrier\n"
    "v_mfma_f32_16x16x32_bf16 v[0:3], a[120:123], v[40:43], v[0:3]\n"
    "s_sub_u32 m0, s26, 8192\n"
    "ds_read_b128 v[32:35], v92 offset:6144\n"
    "ds_read_b128 v[36:39], v92 offset:7168\n"
    "v_mfma_f32_16x16x32_bf16 v[4:7], a[120:123], v[44:47], v[4:7]\n"
    "buffer_load_dwordx4 v96, s[0:3], s53 offen offset:0 sc1 lds\n"
    "v_mfma_f32_16x16x32_bf16 v[8:11], a[200:203], v[40:43], v[8:11]\n"
    ".Lqk_mkL4_%=:\n"
    "v_mfma_f32_16x16x32_bf16 v[12:15], a[200:203], v[44:47], v[12:15]\n"
    "v_mfma_f32_16x16x32_bf16 v[16:19], a[40:43], v[40:43], v[16:19]\n"
    "v_mfma_f32_16x16x32_bf16 v[20:23], a[40:43], v[44:47], v[20:23]\n"
    "buffer_load_dwordx4 v96, s[0:3], s53 offen offset:1024 sc1 lds\n"
    "v_mfma_f32_16x16x32_bf16 v[24:27], v[136:139], v[40:43], v[24:27]\n"
    "v_mfma_f32_16x16x32_bf16 v[28:31], v[136:139], v[44:47], v[28:31]\n"
    "s_waitcnt lgkmcnt(2)\n"
    "v_mfma_f32_16x16x32_bf16 v[0:3], a[136:139], v[48:51], v[0:3]\n"
    "ds_read_b128 v[40:43], v93 offset:6144\n"
    "ds_read_b128 v[44:47], v93 offset:7168\n"
    "v_mfma_f32_16x16x32_bf16 v[4:7], a[136:139], v[52:55], v[4:7]\n"
    "buffer_load_dwordx4 v96, s[0:3], s53 offen offset:2048 sc1 lds\n"
    "v_mfma_f32_16x16x32_bf16 v[8:11], a[216:219], v[48:51], v[8:11]\n"
    "v_mfma_f32_16x16x32_bf16 v[12:15], a[216:219], v[52:55], v[12:15]\n"
    "v_mfma_f32_16x16x32_bf16 v[16:19], a[56:59], v[48:51], v[16:19]\n"
    "v_mfma_f32_16x16x32_bf16 v[20:23], a[56:59], v[52:55], v[20:23]\n"
    "buffer_load_dwordx4 v96, s[0:3], s53 offen offset:3072 sc1 lds\n"
    "v_mfma_f32_16x16x32_bf16 v[24:27], v[152:155], v[48:51], v[24:27]\n"
    "s_sub_u32 m0, s26, 4096\n"
    "s_add_u32 s53, s53, 4096\n"
    "v_mfma_f32_16x16x32_bf16 v[28:31], v[152:155], v[52:55], v[28:31]\n"
    "s_waitcnt lgkmcnt(2)\n"
    "v_mfma_f32_16x16x32_bf16 v[0:3], a[92:95], v[32:35], v[0:3]\n"
    "ds_read_b128 v[48:51], v94 offset:6144\n"
    "ds_read_b128 v[52:55], v94 offset:7168\n"
    "v_mfma_f32_16x16x32_bf16 v[4:7], a[92:95], v[36:39], v[4:7]\n"
    "buffer_load_dwordx4 v96, s[0:3], s53 offen offset:0 sc1 lds\n"
    "v_mfma_f32_16x16x32_bf16 v[8:11], a[172:175], v[32:35], v[8:11]\n"
    "v_mfma_f32_16x16x32_bf16 v[12:15], a[172:175], v[36:39], v[12:15]\n"
    "v_mfma_f32_16x16x32_bf16 v[16:19], a[12:15], v[32:35], v[16:19]\n"
    "v_mfma_f32_16x16x32_bf16 v[20:23], a[12:15], v[36:39], v[20:23]\n"
    "buffer_load_dwordx4 v96, s[0:3], s53 offen offset:1024 sc1 lds\n"
    "v_mfma_f32_16x16x32_bf16 v[24:27], a[252:255], v[32:35], v[24:27]\n"
    "v_mfma_f32_16x16x32_bf16 v[28:31], a[252:255], v[36:39], v[28:31]\n"
    "s_waitcnt lgkmcnt(2)\n"
    "v_mfma_f32_16x16x32_bf16 v[0:3], a[108:111], v[40:43], v[0:3]\n"
    "ds_read_b128 v[32:35], v95 offset:6144\n"
    "ds_read_b128 v[36:39], v95 offset:7168\n"
    "v_mfma_f32_16x16x32_bf16 v[4:7], a[108:111], v[44:47], v[4:7]\n"
    "v_mfma_f32_16x16x32_bf16 v[8:11], a[188:191], v[40:43], v[8:11]\n"
    ".Lqk_mkL5_%=:\n"
    "v_mfma_f32_16x16x32_bf16 v[12:15], a[188:191], v[44:47], v[12:15]\n"
    "v_mfma_f32_16x16x32_bf16 v[16:19], a[28:31], v[40:43], v[16:19]\n"
    "v_mfma_f32_16x16x32_bf16 v[20:23], a[28:31], v[44:47], v[20:23]\n"
    "v_mfma_f32_16x16x32_bf16 v[24:27], v[124:127], v[40:43], v[24:27]\n"
    "v_mfma_f32_16x16x32_bf16 v[28:31], v[124:127], v[44:47], v[28:31]\n"
    "s_waitcnt lgkmcnt(2)\n"
    "v_mfma_f32_16x16x32_bf16 v[0:3], a[124:127], v[48:51], v[0:3]\n"
    "ds_read_b128 v[40:43], v96 offset:45056\n"
    "ds_read_b128 v[44:47], v96 offset:46080\n"
    "v_mfma_f32_16x16x32_bf16 v[4:7], a[124:127], v[52:55], v[4:7]\n"
    "v_mfma_f32_16x16x32_bf16 v[8:11], a[204:207], v[48:51], v[8:11]\n"
    "v_mfma_f32_16x16x32_bf16 v[12:15], a[204:207], v[52:55], v[12:15]\n"
    "v_mfma_f32_16x16x32_bf16 v[16:19], a[44:47], v[48:51], v[16:19]\n"
    "v_mfma_f32_16x16x32_bf16 v[20:23], a[44:47], v[52:55], v[20:23]\n"
    "v_mfma_f32_16x16x32_bf16 v[24:27], v[140:143], v[48:51], v[24:27]\n"
    "v_mfma_f32_16x16x32_bf16 v[28:31], v[140:143], v[52:55], v[28:31]\n"
    "s_waitcnt lgkmcnt(2)\n"
    "v_mfma_f32_16x16x32_bf16 v[0:3], a[140:143], v[32:35], v[0:3]\n"
    "ds_read_b128 v[48:51], v96 offset:47104\n"
    "ds_read_b128 v[52:55], v96 offset:48128\n"
    "v_mfma_f32_16x16x32_bf16 v[4:7], a[140:143], v[36:39], v[4:7]\n"
    "v_mfma_f32_16x16x32_bf16 v[8:11], a[220:223], v[32:35], v[8:11]\n"
    "v_mfma_f32_16x16x32_bf16 v[12:15], a[220:223], v[36:39], v[12:15]\n"
    "v_mfma_f32_16x16x32_bf16 v[16:19], a[60:63], v[32:35], v[16:19]\n"
    "v_mfma_f32_16x16x32_bf16 v[20:23], a[60:63], v[36:39], v[20:23]\n"
    "v_mfma_f32_16x16x32_bf16 v[24:27], v[156:159], v[32:35], v[24:27]\n"
    "v_mfma_f32_16x16x32_bf16 v[28:31], v[156:159], v[36:39], v[28:31]\n"
    "s_waitcnt lgkmcnt(2)\n"
    "v_mfma_f32_16x16x32_bf16 v[0:3], a[144:147], v[40:43], v[0:3]\n"
    "s_waitcnt vmcnt(7)\n"
    "ds_read_b128 v[32:35], v96 offset:49152\n"
    "ds_read_b128 v[36:39], v96 offset:50176\n"
    "s_memrealtime s[70:71]\n"
    "v_mfma_f32_16x16x32_bf16 v[4:7], a[144:147], v[44:47], v[4:7]\n"
    "v_readfirstlane_b32 s44, v108\n"
    "v_readfirstlane_b32 s45, v109\n"
    "v_mfma_f32_16x16x32_bf16 v[8:11], a[224:227], v[40:43], v[8:11]\n"
    "v_mfma_f32_16x16x32_bf16 v[12:15], a[224:227], v[44:47], v[12:15]\n"
    "s_cmp_eq_u32 s45, s47\n"
    "s_cselect_b32 s57, s44, s57\n"
    "s_add_u32 s57, s57, 268\n"
    "v_mfma_f32_16x16x32_bf16 v[16:19], a[64:67], v[40:43], v[16:19]\n"
    "s_sub_u32 s58, s57, 59\n"
    "s_mov_b32 s47, s60\n"
    "s_mov_b32 m0, s26\n"
    "v_mfma_f32_16x16x32_bf16 v[20:23], a[64:67], v[44:47], v[20:23]\n"
    ".Lqk_mkL6_%=:\n"
    "v_mfma_f32_16x16x32_bf16 v[24:27], v[160:163], v[40:43], v[24:27]\n"
    "s_sub_u32 s72, s58, s70\n"
    "v_mfma_f32_16x16x32_bf16 v[28:31], v[160:163], v[44:47], v[28:31]\n"
    "s_waitcnt lgkmcnt(2)\n"
    ".Lqk_mk6x19_%=:\n"
    "s_cmp_gt_i32 s72, 4\n"
    "s_cbranch_scc1 .Lqk_early_%=\n"
    "buffer_load_dwordx4 v96, s[0:3], s51 offen sc1 lds\n"
    "buffer_load_dwordx4 v96, s[0:3], s51 offen offset:1024 sc1 lds\n"
    ".Lqk_asked_%=:\n"
    ".Lqk_mk7x21_%=:\n"
    "v_mfma_f32_16x16x32_bf16 v[0:3], a[148:151], v[48:51], v[0:3]\n"
    "ds_read_b128 v[40:43], v96 offset:51200\n"
    "ds_read_b128 v[44:47], v96 offset:52224\n"
    "v_mfma_f32_16x16x32_bf16 v[4:7], a[148:151], v[52:55], v[4:7]\n"
    ".Lqk_mkM0_%=:\n"
    "v_mfma_f32_16x16x32_bf16 v[8:11], a[228:231], v[48:51], v[8:11]\n"
    "v_mfma_f32_16x16x32_bf16 v[12:15], a[228:231], v[52:55], v[12:15]\n"
    "v_mfma_f32_16x16x32_bf16 v[16:19], a[68:71], v[48:51], v[16:19]\n"
    "v_mfma_f32_16x16x32_bf16 v[20:23], a[68:71], v[52:55], v[20:23]\n"
    ".Lqk_mkM1_%=:\n"
    "v_mfma_f32_16x16x32_bf16 v[24:27], v[164:167], v[48:51], v[24:27]\n"
    "v_mfma_f32_16x16x32_bf16 v[28:31], v[164:167], v[52:55], v[28:31]\n"
    "s_waitcnt lgkmcnt(2)\n"
    "v_mfma_f32_16x16x32_bf16 v[0:3], a[152:155], v[32:35], v[0:3]\n"
    "buffer_load_dwordx4 v96, s[0:3], s52 offen offset:2048 sc1 lds\n"
    "v_mfma_f32_16x16x32_bf16 v[4:7], a[152:155], v[36:39], v[4:7]\n"
    "v_mfma_f32_16x16x32_bf16 v[8:11], a[232:235], v[32:35], v[8:11]\n"
    ".Lqk_mkM2_%=:\n"
    "v_mfma_f32_16x16x32_bf16 v[12:15], a[232:235], v[36:39], v[12:15]\n"
    "v_mfma_f32_16x16x32_bf16 v[16:19], a[72:75], v[32:35], v[16:19]\n"
    "v_mfma_f32_16x16x32_bf16 v[20:23], a[72:75], v[36:39], v[20:23]\n"
    "v_mfma_f32_16x16x32_bf16 v[24:27], v[168:171], v[32:35], v[24:27]\n"
    ".Lqk_mkM3_%=:\n"
    "v_mfma_f32_16x16x32_bf16 v[28:31], v[168:171], v[36:39], v[28:31]\n"
    "s_waitcnt lgkmcnt(0)\n"
    "v_mfma_f32_16x16x32_bf16 v[0:3], a[156:159], v[40:43], v[0:3]\n"
    "v_mfma_f32_16x16x32_bf16 v[4:7], a[156:159], v[44:47], v[4:7]\n"
    "v_mfma_f32_16x16x32_bf16 v[8:11], a[236:239], v[40:43], v[8:11]\n"
    "v_mfma_f32_16x16x32_bf16 v[12:15], a[236:239], v[44:47], v[12:15]\n"
    "v_mfma_f32_16x16x32_bf16 v[16:19], a[76:79], v[40:43], v[16:19]\n"
    "v_mfma_f32_16x16x32_bf16 v[20:23], a[76:79], v[44:47], v[20:23]\n"
    "v_mfma_f32_16x16x32_bf16 v[24:27], v[172:175], v[40:43], v[24:27]\n"
    ".Lqk_mkM4_%=:\n"
    "v_mfma_f32_16x16x32_bf16 v[28:31], v[172:175], v[44:47], v[28:31]\n"
    "ds_read_b128 v[32:35], v97 offset:0\n"
    "ds_read_b128 v[36:39], v97 offset:1024\n"
    "ds_read_b128 v[40:43], v97 offset:2048\n"
    "ds_read_b128 v[44:47], v97 offset:3072\n"
    "ds_read_b128 v[48:51], v97 offset:4096\n"
    "ds_read_b128 v[52:55], v97 offset:5120\n"
    "v_cvt_pk_bf16_f32 v68, v0, v1\n"
    "v_cvt_pk_bf16_f32 v69, v2, v3\n"
    "v_cvt_pk_bf16_f32 v70, v8, v9\n"
    "v_cvt_pk_bf16_f32 v71, v10, v11\n"
    "v_cvt_pk_bf16_f32 v72, v4, v5\n"
    "v_cvt_pk_bf16_f32 v73, v6, v7\n"
    "v_cvt_pk_bf16_f32 v74, v12, v13\n"
    "v_cvt_pk_bf16_f32 v75, v14, v15\n"
    "v_mfma_f32_16x16x32_bf16 v[108:111], v[68:71], v[84:87], 0\n"
    "v_cvt_pk_bf16_f32 v88, v24, v25\n"
    "v_cvt_pk_bf16_f32 v89, v26, v27\n"
    "v_cvt_pk_bf16_f32 v90, v28, v29\n"
    "v_cvt_pk_bf16_f32 v91, v30, v31\n"
    "v_mfma_f32_16x16x32_bf16 v[24:27], v[72:75], v[84:87], 0\n"
    "v_cvt_pk_bf16_f32 v76, v16, v17\n"
    "v_cvt_pk_bf16_f32 v77, v18, v19\n"
    "v_cvt_pk_bf16_f32 v78, v20, v21\n"
    "v_cvt_pk_bf16_f32 v79, v22, v23\n"
    "v_mfma_f32_16x16x32_bf16 v[28:31], v[68:71], v[80:83], 0\n"
    "v_mov_b64 v[0:1], 0\n"
    "v_mov_b64 v[2:3], 0\n"
    "v_mfma_f32_16x16x16_bf16 v[108:111], v[76:77], v[90:91], v[108:111]\n"
    "v_mov_b64 v[4:5], 0\n"
    "v_mov_b64 v[6:7], 0\n"
    "v_mfma_f32_16x16x16_bf16 v[24:27], v[78:79], v[90:91], v[24:27]\n"
    "v_mov_b64 v[8:9], 0\n"
    "v_mov_b64 v[10:11], 0\n"
    "v_mfma_f32_16x16x16_bf16 v[28:31], v[76:77], v[88:89], v[28:31]\n"
    "v_mov_b64 v[12:13], 0\n"
    "v_mov_b64 v[14:15], 0\n"
    "v_mov_b64 v[16:17], 0\n"
    "v_mov_b64 v[18:19], 0\n"
    "v_mov_b64 v[20:21], 0\n"
    "v_mov_b64 v[22:23], 0\n"
    "ds_write_b128 v104, v[108:111]\n"
    "ds_write_b128 v104, v[24:27] offset:64\n"
    "ds_write_b128 v104, v[28:31] offset:160\n"
    ".Lqk_mk8x22_%=:\n"
    "s_branch .Lqk_take_%=\n"
    ".Lqk_tk0_%=:\n"
    ".Lqk_po3_%=:\n"
    "buffer_load_dword v109, v108, s[0:3], 0 offen sc1\n"
    "s_waitcnt vmcnt(0)\n"
    "v_cmp_gt_i32 vcc, 32, v109\n"
    "s_nop 0\n"
    "s_cbranch_vccz .Lqk_pd4_%=\n"
    "s_add_u32 s40, s40, 1\n"
    "s_cmp_lt_u32 s40, 0x40000\n"
    "s_cbranch_scc1 .Lqk_sp5_%=\n"
    "v_mov_b32 v109, 90\n"
    "s_mov_b32 s44, 33728\n"
    "buffer_store_dword v109, off, s[0:3], s44 sc1\n"
    "s_waitcnt vmcnt(0)\n"
    "s_branch .Lqk_pd4_%=\n"
    ".Lqk_sp5_%=:\n"
    "s_sleep 8\n"
    "s_branch .Lqk_po3_%=\n"
    ".Lqk_pd4_%=:\n"
    "s_branch .Lqk_tk1_%=\n"
    ".Lqk_early_%=:\n"
    ".Lqk_rt21_%=:\n"
    "s_memrealtime s[42:43]\n"
    "s_waitcnt lgkmcnt(0)\n"
    "s_sub_u32 s44, s58, s42\n"
    "s_cmp_lt_u32 s44, 600\n"
    "s_cbranch_scc1 .Lqk_rt21_%=\n"
    "buffer_load_dwordx4 v96, s[0:3], s51 offen sc1 lds\n"
    "buffer_load_dwordx4 v96, s[0:3], s51 offen offset:1024 sc1 lds\n"
    "s_branch .Lqk_asked_%=\n"
    ".Lqk_retry_%=:\n"
    "s_add_u32 s40, s40, 1\n"
    "s_cmp_lt_u32 s40, 0x40000\n"
    "s_cbranch_scc1 .Lqk_sp24_%=\n"
    "v_mov_b32 v108, 1\n"
    "s_mov_b32 s44, 33728\n"
    "buffer_store_dword v108, off, s[0:3], s44 sc1\n"
    "s_waitcnt vmcnt(0)\n"
    "s_branch .Lqk_go_%=\n"
    ".Lqk_sp24_%=:\n"
    "s_waitcnt lgkmcnt(0)\n"
    "s_nop 7\n"
    "ds_read_b128 v[32:35], v97 offset:0\n"
    "ds_read_b128 v[36:39], v97 offset:1024\n"
    "v_mov_b64 v[0:1], 0\n"
    "v_mov_b64 v[2:3], 0\n"
    "v_mov_b64 v[4:5], 0\n"
    "v_mov_b64 v[6:7], 0\n"
    "v_mov_b64 v[8:9], 0\n"
    "v_mov_b64 v[10:11], 0\n"
    "v_mov_b64 v[12:13], 0\n"
    "v_mov_b64 v[14:15], 0\n"
    "v_mov_b64 v[16:17], 0\n"
    "v_mov_b64 v[18:19], 0\n"
    "v_mov_b64 v[20:21], 0\n"
    "v_mov_b64 v[22:23], 0\n"
    "buffer_load_dwordx4 v96, s[0:3], s51 offen sc1 lds\n"
    "buffer_load_dwordx4 v96, s[0:3], s51 offen offset:1024 sc1 lds\n"
    "buffer_load_dwordx4 v96, s[0:3], s52 offen offset:2048 sc1 lds\n"
    "s_waitcnt vmcnt(0)\n"
    "s_branch .Lqk_take_%=\n"
    ".Lqk_r2_%=:\n"
    "s_add_u32 s40, s40, 1\n"
    "s_cmp_lt_u32 s40, 0x40000\n"
    "s_cbranch_scc1 .Lqk_sp25_%=\n"
    "v_mov_b32 v108, 5\n"
    "s_mov_b32 s44, 33728\n"
    "buffer_store_dword v108, off, s[0:3], s44 sc1\n"
    "s_waitcnt vmcnt(0)\n"
    "s_branch .Lqk_go2_%=\n"
    ".Lqk_sp25_%=:\n"
    "buffer_load_dwordx4 v96, s[0:3], s52 offen offset:2048 sc1 lds\n"
    "s_waitcnt vmcnt(0)\n"
    "ds_read_b128 v[64:67], v92 offset:10240\n"
    "s_waitcnt lgkmcnt(0)\n"
    "v_max_u32 v108, v64, v66\n"
    "v_cmp_eq_u32 vcc, -1, v108\n"
    "s_nop 0\n"
    "s_cbranch_vccnz .Lqk_r2_%=\n"
    "s_branch .Lqk_go2_%=\n"
    ".Lqk_end_%=:\n"
    "s_waitcnt vmcnt(0) lgkmcnt(0)\n"
    "s_endpgm\n"
    :: "{s[0:3]}"(s0_), "{s[4:7]}"(s4_), "{s[8:11]}"(s8_), "{s[12:15]}"(s12_), "{s[16:19]}"(s16_), "{s[20:23]}"(s20_), "{s[24:27]}"(s24_), "{s[28:31]}"(s28_), "{s[32:35]}"(s32_), "{s[36:39]}"(s36_), "{v[0:3]}"(v0_), "{v[4:7]}"(v4_), "{v[92:95]}"(v92_), "{v[96:99]}"(v96_), "{v[100:103]}"(v100_), "{v[104:107]}"(v104_) : "memory", "v255", "a255", "s95");
    __builtin_unreachable();
}
DEV void vo_role(const MegaArgs& A, const int L, const int h, const int rep) {
    ROLE_HEAD;
    const unsigned um = xl(RG_UM + ((8 * w + lq) * 4 + (h >> 3)) * 2048 + (h & 7) * 256 + l16 * 16, L);
#define XU 0
    const unsigned umx = XU ? OOB : xl(RG_UMX + ((4 * (2 * w + (lq & 1)) + (l16 >> 2)) * 4 + (h >> 3)) * 512 + (h & 7) * 64 + (l & 3) * 16 + (lq >> 1) * 8, L);
    const u32x4 s0_ = {RFL(rx[0]), RFL(rx[1]), RFL(rx[2]), RFL(rx[3])};
    const u32x4 s4_ = {RFL(lo(A.Wo + h * 192 * 640)), RFL(hi(A.Wo + h * 192 * 640)), RFL(192u * 1280u), RFL(0x20000u)};
    const u32x4 s8_ = {RFL(lo(A.Wv + h * 192)), RFL(hi(A.Wv + h * 192)), RFL(WBYTES - h * 384), RFL(0x20000u)};
    const u32x4 s12_ = {RFL(w), RFL(L << 13), RFL(L), RFL(xl(RG_MEM + rep * 16384, L) + w * 2048)};
    const u32x4 s16_ = {RFL(w < 2 ? xl(RG_MEM + rep * 16384 + 8192, L) + w * 1024 - 2048 : OOB), RFL(w * 13312 + 8192), RFL(REC0 + w * 16384), RFL(65536 - 4096 - w * 14336)};
    const u32x4 s20_ = {RFL(QT0 + h * 32768 + 16384 + w * 1024), RFL(146432 + w * 1024), RFL(xl(RG_UT + (32 * w + (h >> 3)) * 8192 + (h & 7) * 1024, L)), RFL(xl(Z_X + 8192, L))};
    const u32x4 s24_ = {RFL(xl(Z_CT + (h & 7) * 256, L)), RFL(w == 0 ? xl(Z_QV + (32 + h) * 4, L) : OOB), RFL(DBG0 + ((L * 128 + 32 + h) * 4 + w) * 2048), RFL(0u)};
    const u32x4 v0_ = {(unsigned)(((3 * w + 0) * 64 + l) / 24 * 12288 + ((((3 * w + 0) * 64 + l) % 24) ^ ((((3 * w + 0) * 64 + l) / 48) & 6)) * 16 - 0), (unsigned)(((3 * w + 1) * 64 + l) / 24 * 12288 + ((((3 * w + 1) * 64 + l) % 24) ^ ((((3 * w + 1) * 64 + l) / 48) & 6)) * 16 - 1024), (unsigned)(((3 * w + 2) * 64 + l) / 24 * 12288 + ((((3 * w + 2) * 64 + l) % 24) ^ ((((3 * w + 2) * 64 + l) / 48) & 6)) * 16 - 2048), (unsigned)((8 * lq + ((l >> 2) & 1) + ((l >> 1) & 4)) * 384 + (w ^ (((8 * lq + ((l >> 2) & 1) + ((l >> 1) & 4)) >> 2) & 3)) * 32 + (l & 3) * 8 + ((w + 0) & 3) * 12288)};
    const u32x4 v4_ = {(unsigned)((8 * lq + ((l >> 2) & 1) + ((l >> 1) & 4)) * 384 + (w ^ (((8 * lq + ((l >> 2) & 1) + ((l >> 1) & 4)) >> 2) & 3)) * 32 + (l & 3) * 8 + ((w + 1) & 3) * 12288), (unsigned)((8 * lq + ((l >> 2) & 1) + ((l >> 1) & 4)) * 384 + (w ^ (((8 * lq + ((l >> 2) & 1) + ((l >> 1) & 4)) >> 2) & 3)) * 32 + (l & 3) * 8 + ((w + 2) & 3) * 12288), (unsigned)((8 * lq + ((l >> 2) & 1) + ((l >> 1) & 4)) * 384 + (w ^ (((8 * lq + ((l >> 2) & 1) + ((l >> 1) & 4)) >> 2) & 3)) * 32 + (l & 3) * 8 + ((w + 3) & 3) * 12288), (unsigned)((8 * lq + ((l >> 2) & 1) + ((l >> 1) & 4)) * 384 + (w ^ (((8 * lq + ((l >> 2) & 1) + ((l >> 1) & 4)) >> 2) & 3)) * 32 + (l & 3) * 8)};
    const u32x4 v8_ = {(unsigned)(0), (unsigned)(0), (unsigned)(0), (unsigned)(0)};
    const u32x4 v12_ = {(unsigned)((4 * 0 + lq) * 1280 + 256 * w + ((l16 ^ (2 * ((4 * 0 + lq) & 7))) * 16) - 0), (unsigned)((4 * 1 + lq) * 1280 + 256 * w + ((l16 ^ (2 * ((4 * 1 + lq) & 7))) * 16) - 1024), (unsigned)((4 * 2 + lq) * 1280 + 256 * w + ((l16 ^ (2 * ((4 * 2 + lq) & 7))) * 16) - 2048), (unsigned)((4 * 3 + lq) * 1280 + 256 * w + ((l16 ^ (2 * ((4 * 3 + lq) & 7))) * 16) - 3072)};
    const u32x4 v16_ = {(unsigned)((4 * w + lq) * 1280 + 1024 + ((l16 ^ (2 * ((4 * w + lq) & 7))) * 16)), (unsigned)(w * 8192 + (l >> 2) * 256 + ((l >> 2) & 7) * 32 + (l & 3) * 8), (unsigned)(32768 + (l >> 2) * 256 + (((l >> 2) & 7) ^ (2 * w)) * 32 + (l & 3) * 8), (unsigned)(0u)};
    const u32x4 v92_ = {(unsigned)(l * 16 + ((w + 0) & 3) * 13312), (unsigned)(l * 16 + ((w + 1) & 3) * 13312), (unsigned)(l * 16 + ((w + 2) & 3) * 13312), (unsigned)(l * 16 + ((w + 3) & 3) * 13312)};
    const u32x4 v96_ = {(unsigned)(l * 16), (unsigned)(l * 16 + 64512 + w * 20480), (unsigned)((l & 2) ? 0xeeeeu : 0x4444u), (unsigned)(61440 + 512 * w + (l16 >> 2) * 64 + (l & 1) * 32 + sw2(lq) * 8 + (l & 2) * 2)};
    const u32x4 v100_ = {(unsigned)(61440 + 2048 + 256 * w + (l16 >> 2) * 64 + (l & 1) * 32 + sw2(lq) * 8 + (l & 2) * 2), (unsigned)(61440 + 512 * (lq >> 1) + 128 * (lq & 1) + (l & 4) * 8 + (l & 8) * 32 + (l & 3) * 8), (unsigned)(um), (unsigned)(umx)};
    const u32x4 v104_ = {(unsigned)(xl(RG_UTX + ((8 * w + (l16 >> 2)) * 4 + (h >> 3)) * 2048 + (h & 1) * 1024 + ((h & 7) >> 1) * 256 + lq * 64 + (l & 3) * 16, L)), (unsigned)(l < 4 || l > 8 ? xl(Z_QW + h * 128 + (l & 3) * 4, L) : (L ? xl(Z_XN + rep * 2048 + (l - 4) * 128, 0) : OOB)), (unsigned)(146432 + l * 16), (unsigned)(148480 + l * 16)};
    asm volatile(
    "s_mov_b32 s28, 0\n"
    "s_mov_b64 s[30:31], 0\n"
    "s_mov_b32 s33, 0\n"
    "s_cmp_eq_u32 s13, 0\n"
    "s_cselect_b32 s32, 0, 24\n"
    ".Lvo_ho1_%=:\n"
    "s_sub_u32 s32, s32, 1\n"
    "s_cbranch_scc1 .Lvo_hd2_%=\n"
    "s_sleep 17\n"
    "s_branch .Lvo_ho1_%=\n"
    ".Lvo_hd2_%=:\n"
    "s_mul_i32 s41, s12, 3072\n"
    "s_lshl_b32 s42, s12, 13\n"
    "s_lshl_b32 s43, s12, 10\n"
    "s_mov_b32 s49, 0\n"
    "s_add_u32 m0, s42, 0\n"
    "s_nop 0\n"
    "buffer_load_dwordx4 v12, s[4:7], s49 offen offset:0 nt lds\n"
    "buffer_load_dwordx4 v13, s[4:7], s49 offen offset:1024 nt lds\n"
    "buffer_load_dwordx4 v14, s[4:7], s49 offen offset:2048 nt lds\n"
    "buffer_load_dwordx4 v15, s[4:7], s49 offen offset:3072 nt lds\n"
    "s_add_u32 m0, s43, 32768\n"
    "s_nop 0\n"
    "buffer_load_dwordx4 v16, s[4:7], s49 offen nt lds\n"
    "s_mov_b32 s49, 81920\n"
    "s_add_u32 m0, s42, 4096\n"
    "s_nop 0\n"
    "buffer_load_dwordx4 v12, s[4:7], s49 offen offset:0 nt lds\n"
    "buffer_load_dwordx4 v13, s[4:7], s49 offen offset:1024 nt lds\n"
    "buffer_load_dwordx4 v14, s[4:7], s49 offen offset:2048 nt lds\n"
    "buffer_load_dwordx4 v15, s[4:7], s49 offen offset:3072 nt lds\n"
    "s_add_u32 m0, s43, 36864\n"
    "s_nop 0\n"
    "buffer_load_dwordx4 v16, s[4:7], s49 offen nt lds\n"
    "s_mov_b32 s49, 20480\n"
    "s_add_u32 m0, s42, 49152\n"
    "s_nop 0\n"
    "buffer_load_dwordx4 v12, s[4:7], s49 offen offset:0 nt lds\n"
    "buffer_load_dwordx4 v13, s[4:7], s49 offen offset:1024 nt lds\n"
    "buffer_load_dwordx4 v14, s[4:7], s49 offen offset:2048 nt lds\n"
    "buffer_load_dwordx4 v15, s[4:7], s49 offen offset:3072 nt lds\n"
    "s_add_u32 m0, s43, 81920\n"
    "s_nop 0\n"
    "buffer_load_dwordx4 v16, s[4:7], s49 offen nt lds\n"
    "s_mov_b32 s49, 102400\n"
    "s_add_u32 m0, s42, 53248\n"
    "s_nop 0\n"
    "buffer_load_dwordx4 v12, s[4:7], s49 offen offset:0 nt lds\n"
    "buffer_load_dwordx4 v13, s[4:7], s49 offen offset:1024 nt lds\n"
    "buffer_load_dwordx4 v14, s[4:7], s49 offen offset:2048 nt lds\n"
    "buffer_load_dwordx4 v15, s[4:7], s49 offen offset:3072 nt lds\n"
    "s_add_u32 m0, s43, 86016\n"
    "s_nop 0\n"
    "buffer_load_dwordx4 v16, s[4:7], s49 offen nt lds\n"
    "s_mov_b32 s49, 40960\n"
    "s_add_u32 m0, s42, 98304\n"
    "s_nop 0\n"
    "buffer_load_dwordx4 v12, s[4:7], s49 offen offset:0 nt lds\n"
    "buffer_load_dwordx4 v13, s[4:7], s49 offen offset:1024 nt lds\n"
    "buffer_load_dwordx4 v14, s[4:7], s49 offen offset:2048 nt lds\n"
    "buffer_load_dwordx4 v15, s[4:7], s49 offen offset:3072 nt lds\n"
    "s_add_u32 m0, s43, 131072\n"
    "s_nop 0\n"
    "buffer_load_dwordx4 v16, s[4:7], s49 offen nt lds\n"
    "s_mov_b32 s49, 122880\n"
    "s_add_u32 m0, s42, 102400\n"
    "s_nop 0\n"
    "buffer_load_dwordx4 v12, s[4:7], s49 offen offset:0 nt lds\n"
    "buffer_load_dwordx4 v13, s[4:7], s49 offen offset:1024 nt lds\n"
    "buffer_load_dwordx4 v14, s[4:7], s49 offen offset:2048 nt lds\n"
    "buffer_load_dwordx4 v15, s[4:7], s49 offen offset:3072 nt lds\n"
    "s_add_u32 m0, s43, 135168\n"
    "s_nop 0\n"
    "buffer_load_dwordx4 v16, s[4:7], s49 offen nt lds\n"
    "v_mov_b64 v[108:109], -1\n"
    "v_mov_b64 v[110:111], -1\n"
    "s_mov_b32 s49, 0x10000\n"
    "buffer_store_dwordx4 v[108:111], v102, s[0:3], 0 offen sc1\n"
    "buffer_store_dwordx4 v[108:111], v102, s[0:3], s49 offen sc1\n"
    "buffer_store_dwordx2 v[108:109], v103, s[0:3], 0 offen sc1\n"
    "s_waitcnt vmcnt(23)\n"
    "s_barrier\n"
    "v_add_u32 v24, 0, v17\n"
    "v_add_u32 v32, 0, v18\n"
    "v_xor_b32 v33, 32, v32\n"
    "v_xor_b32 v25, 32, v24\n"
    "v_xor_b32 v26, 64, v24\n"
    "v_xor_b32 v27, 96, v24\n"
    "v_xor_b32 v28, 128, v24\n"
    "v_xor_b32 v29, 160, v24\n"
    "v_xor_b32 v30, 192, v24\n"
    "v_xor_b32 v31, 224, v24\n"
    "ds_read_b64_tr_b16 a[0:1], v24 offset:0\n"
    "ds_read_b64_tr_b16 a[2:3], v24 offset:4096\n"
    "ds_read_b64_tr_b16 a[24:25], v25 offset:0\n"
    "ds_read_b64_tr_b16 a[26:27], v25 offset:4096\n"
    "ds_read_b64_tr_b16 a[48:49], v26 offset:0\n"
    "ds_read_b64_tr_b16 a[50:51], v26 offset:4096\n"
    "ds_read_b64_tr_b16 a[72:73], v27 offset:0\n"
    "ds_read_b64_tr_b16 a[74:75], v27 offset:4096\n"
    "ds_read_b64_tr_b16 a[96:97], v28 offset:0\n"
    "ds_read_b64_tr_b16 a[98:99], v28 offset:4096\n"
    "ds_read_b64_tr_b16 a[120:121], v29 offset:0\n"
    "ds_read_b64_tr_b16 a[122:123], v29 offset:4096\n"
    "ds_read_b64_tr_b16 a[144:145], v30 offset:0\n"
    "ds_read_b64_tr_b16 a[146:147], v30 offset:4096\n"
    "ds_read_b64_tr_b16 a[168:169], v31 offset:0\n"
    "ds_read_b64_tr_b16 a[170:171], v31 offset:4096\n"
    "ds_read_b64_tr_b16 a[192:193], v32 offset:0\n"
    "ds_read_b64_tr_b16 a[194:195], v32 offset:4096\n"
    "ds_read_b64_tr_b16 a[216:217], v33 offset:0\n"
    "ds_read_b64_tr_b16 a[218:219], v33 offset:4096\n"
    "s_waitcnt lgkmcnt(0)\n"
    "s_barrier\n"
    "s_mov_b32 s49, 61440\n"
    "s_add_u32 m0, s42, 0\n"
    "s_nop 0\n"
    "buffer_load_dwordx4 v12, s[4:7], s49 offen offset:0 nt lds\n"
    "buffer_load_dwordx4 v13, s[4:7], s49 offen offset:1024 nt lds\n"
    "buffer_load_dwordx4 v14, s[4:7], s49 offen offset:2048 nt lds\n"
    "buffer_load_dwordx4 v15, s[4:7], s49 offen offset:3072 nt lds\n"
    "s_add_u32 m0, s43, 32768\n"
    "s_nop 0\n"
    "buffer_load_dwordx4 v16, s[4:7], s49 offen nt lds\n"
    "s_mov_b32 s49, 143360\n"
    "s_add_u32 m0, s42, 4096\n"
    "s_nop 0\n"
    "buffer_load_dwordx4 v12, s[4:7], s49 offen offset:0 nt lds\n"
    "buffer_load_dwordx4 v13, s[4:7], s49 offen offset:1024 nt lds\n"
    "buffer_load_dwordx4 v14, s[4:7], s49 offen offset:2048 nt lds\n"
    "buffer_load_dwordx4 v15, s[4:7], s49 offen offset:3072 nt lds\n"
    "s_add_u32 m0, s43, 36864\n"
    "s_nop 0\n"
    "buffer_load_dwordx4 v16, s[4:7], s49 offen nt lds\n"
    "s_waitcnt vmcnt(23)\n"
    "s_barrier\n"
    "v_add_u32 v24, 49152, v17\n"
    "v_add_u32 v32, 49152, v18\n"
    "v_xor_b32 v33, 32, v32\n"
    "v_xor_b32 v25, 32, v24\n"
    "v_xor_b32 v26, 64, v24\n"
    "v_xor_b32 v27, 96, v24\n"
    "v_xor_b32 v28, 128, v24\n"
    "v_xor_b32 v29, 160, v24\n"
    "v_xor_b32 v30, 192, v24\n"
    "v_xor_b32 v31, 224, v24\n"
    "ds_read_b64_tr_b16 a[4:5], v24 offset:0\n"
    "ds_read_b64_tr_b16 a[6:7], v24 offset:4096\n"
    "ds_read_b64_tr_b16 a[28:29], v25 offset:0\n"
    "ds_read_b64_tr_b16 a[30:31], v25 offset:4096\n"
    "ds_read_b64_tr_b16 a[52:53], v26 offset:0\n"
    "ds_read_b64_tr_b16 a[54:55], v26 offset:4096\n"
    "ds_read_b64_tr_b16 a[76:77], v27 offset:0\n"
    "ds_read_b64_tr_b16 a[78:79], v27 offset:4096\n"
    "ds_read_b64_tr_b16 a[100:101], v28 offset:0\n"
    "ds_read_b64_tr_b16 a[102:103], v28 offset:4096\n"
    "ds_read_b64_tr_b16 a[124:125], v29 offset:0\n"
    "ds_read_b64_tr_b16 a[126:127], v29 offset:4096\n"
    "ds_read_b64_tr_b16 a[148:149], v30 offset:0\n"
    "ds_read_b64_tr_b16 a[150:151], v30 offset:4096\n"
    "ds_read_b64_tr_b16 a[172:173], v31 offset:0\n"
    "ds_read_b64_tr_b16 a[174:175], v31 offset:4096\n"
    "ds_read_b64_tr_b16 a[196:197], v32 offset:0\n"
    "ds_read_b64_tr_b16 a[198:199], v32 offset:4096\n"
    "ds_read_b64_tr_b16 a[220:221], v33 offset:0\n"
    "ds_read_b64_tr_b16 a[222:223], v33 offset:4096\n"
    "s_waitcnt lgkmcnt(0)\n"
    "s_barrier\n"
    "s_mov_b32 s49, 163840\n"
    "s_add_u32 m0, s42, 49152\n"
    "s_nop 0\n"
    "buffer_load_dwordx4 v12, s[4:7], s49 offen offset:0 nt lds\n"
    "buffer_load_dwordx4 v13, s[4:7], s49 offen offset:1024 nt lds\n"
    "buffer_load_dwordx4 v14, s[4:7], s49 offen offset:2048 nt lds\n"
    "buffer_load_dwordx4 v15, s[4:7], s49 offen offset:3072 nt lds\n"
    "s_add_u32 m0, s43, 81920\n"
    "s_nop 0\n"
    "buffer_load_dwordx4 v16, s[4:7], s49 offen nt lds\n"
    "s_mov_b32 s49, 184320\n"
    "s_add_u32 m0, s42, 53248\n"
    "s_nop 0\n"
    "buffer_load_dwordx4 v12, s[4:7], s49 offen offset:0 nt lds\n"
    "buffer_load_dwordx4 v13, s[4:7], s49 offen offset:1024 nt lds\n"
    "buffer_load_dwordx4 v14, s[4:7], s49 offen offset:2048 nt lds\n"
    "buffer_load_dwordx4 v15, s[4:7], s49 offen offset:3072 nt lds\n"
    "s_add_u32 m0, s43, 86016\n"
    "s_nop 0\n"
    "buffer_load_dwordx4 v16, s[4:7], s49 offen nt lds\n"
    "v_and_b32 v108, 0x70, v96\n"
    "v_lshlrev_b32 v108, 2, v108\n"
    "v_or_b32 v108, 0x8000, v108\n"
    "buffer_load_dword v109, v108, s[0:3], 0 offen sc1\n"
    "s_waitcnt vmcnt(24)\n"
    "s_barrier\n"
    "v_add_u32 v24, 98304, v17\n"
    "v_add_u32 v32, 98304, v18\n"
    "v_xor_b32 v33, 32, v32\n"
    "v_xor_b32 v25, 32, v24\n"
    "v_xor_b32 v26, 64, v24\n"
    "v_xor_b32 v27, 96, v24\n"
    "v_xor_b32 v28, 128, v24\n"
    "v_xor_b32 v29, 160, v24\n"
    "v_xor_b32 v30, 192, v24\n"
    "v_xor_b32 v31, 224, v24\n"
    "ds_read_b64_tr_b16 a[8:9], v24 offset:0\n"
    "ds_read_b64_tr_b16 a[10:11], v24 offset:4096\n"
    "ds_read_b64_tr_b16 a[32:33], v25 offset:0\n"
    "ds_read_b64_tr_b16 a[34:35], v25 offset:4096\n"
    "ds_read_b64_tr_b16 a[56:57], v26 offset:0\n"
    "ds_read_b64_tr_b16 a[58:59], v26 offset:4096\n"
    "ds_read_b64_tr_b16 a[80:81], v27 offset:0\n"
    "ds_read_b64_tr_b16 a[82:83], v27 offset:4096\n"
    "ds_read_b64_tr_b16 a[104:105], v28 offset:0\n"
    "ds_read_b64_tr_b16 a[106:107], v28 offset:4096\n"
    "ds_read_b64_tr_b16 a[128:129], v29 offset:0\n"
    "ds_read_b64_tr_b16 a[130:131], v29 offset:4096\n"
    "ds_read_b64_tr_b16 a[152:153], v30 offset:0\n"
    "ds_read_b64_tr_b16 a[154:155], v30 offset:4096\n"
    "ds_read_b64_tr_b16 a[176:177], v31 offset:0\n"
    "ds_read_b64_tr_b16 a[178:179], v31 offset:4096\n"
    "ds_read_b64_tr_b16 a[200:201], v32 offset:0\n"
    "ds_read_b64_tr_b16 a[202:203], v32 offset:4096\n"
    "ds_read_b64_tr_b16 a[224:225], v33 offset:0\n"
    "ds_read_b64_tr_b16 a[226:227], v33 offset:4096\n"
    "s_waitcnt lgkmcnt(0)\n"
    "s_barrier\n"
    "s_mov_b32 s49, 204800\n"
    "s_add_u32 m0, s42, 98304\n"
    "s_nop 0\n"
    "buffer_load_dwordx4 v12, s[4:7], s49 offen offset:0 nt lds\n"
    "buffer_load_dwordx4 v13, s[4:7], s49 offen offset:1024 nt lds\n"
    "buffer_load_dwordx4 v14, s[4:7], s49 offen offset:2048 nt lds\n"
    "buffer_load_dwordx4 v15, s[4:7], s49 offen offset:3072 nt lds\n"
    "s_add_u32 m0, s43, 131072\n"
    "s_nop 0\n"
    "buffer_load_dwordx4 v16, s[4:7], s49 offen nt lds\n"
    "s_mov_b32 s49, 225280\n"
    "s_add_u32 m0, s42, 102400\n"
    "s_nop 0\n"
    "buffer_load_dwordx4 v12, s[4:7], s49 offen offset:0 nt lds\n"
    "buffer_load_dwordx4 v13, s[4:7], s49 offen offset:1024 nt lds\n"
    "buffer_load_dwordx4 v14, s[4:7], s49 offen offset:2048 nt lds\n"
    "buffer_load_dwordx4 v15, s[4:7], s49 offen offset:3072 nt lds\n"
    "s_add_u32 m0, s43, 135168\n"
    "s_nop 0\n"
    "buffer_load_dwordx4 v16, s[4:7], s49 offen nt lds\n"
    "s_waitcnt vmcnt(21)\n"
    "s_barrier\n"
    "v_add_u32 v24, 0, v17\n"
    "v_add_u32 v32, 0, v18\n"
    "v_xor_b32 v33, 32, v32\n"
    "v_xor_b32 v25, 32, v24\n"
    "v_xor_b32 v26, 64, v24\n"
    "v_xor_b32 v27, 96, v24\n"
    "v_xor_b32 v28, 128, v24\n"
    "v_xor_b32 v29, 160, v24\n"
    "v_xor_b32 v30, 192, v24\n"
    "v_xor_b32 v31, 224, v24\n"
    "ds_read_b64_tr_b16 a[12:13], v24 offset:0\n"
    "ds_read_b64_tr_b16 a[14:15], v24 offset:4096\n"
    "ds_read_b64_tr_b16 a[36:37], v25 offset:0\n"
    "ds_read_b64_tr_b16 a[38:39], v25 offset:4096\n"
    "ds_read_b64_tr_b16 a[60:61], v26 offset:0\n"
    "ds_read_b64_tr_b16 a[62:63], v26 offset:4096\n"
    "ds_read_b64_tr_b16 a[84:85], v27 offset:0\n"
    "ds_read_b64_tr_b16 a[86:87], v27 offset:4096\n"
    "ds_read_b64_tr_b16 a[108:109], v28 offset:0\n"
    "ds_read_b64_tr_b16 a[110:111], v28 offset:4096\n"
    "ds_read_b64_tr_b16 a[132:133], v29 offset:0\n"
    "ds_read_b64_tr_b16 a[134:135], v29 offset:4096\n"
    "ds_read_b64_tr_b16 a[156:157], v30 offset:0\n"
    "ds_read_b64_tr_b16 a[158:159], v30 offset:4096\n"
    "ds_read_b64_tr_b16 a[180:181], v31 offset:0\n"
    "ds_read_b64_tr_b16 a[182:183], v31 offset:4096\n"
    "ds_read_b64_tr_b16 a[204:205], v32 offset:0\n"
    "ds_read_b64_tr_b16 a[206:207], v32 offset:4096\n"
    "ds_read_b64_tr_b16 a[228:229], v33 offset:0\n"
    "ds_read_b64_tr_b16 a[230:231], v33 offset:4096\n"
    "s_waitcnt lgkmcnt(0)\n"
    "s_barrier\n"
    "s_mov_b32 s49, 0\n"
    "s_add_u32 m0, s41, 0\n"
    "s_nop 0\n"
    "buffer_load_dwordx4 v0, s[8:11], s49 offen offset:0 nt lds\n"
    "buffer_load_dwordx4 v1, s[8:11], s49 offen offset:1024 nt lds\n"
    "buffer_load_dwordx4 v2, s[8:11], s49 offen offset:2048 nt lds\n"
    "s_mov_b32 s49, 1572864\n"
    "s_add_u32 m0, s41, 12288\n"
    "s_nop 0\n"
    "buffer_load_dwordx4 v0, s[8:11], s49 offen offset:0 nt lds\n"
    "buffer_load_dwordx4 v1, s[8:11], s49 offen offset:1024 nt lds\n"
    "buffer_load_dwordx4 v2, s[8:11], s49 offen offset:2048 nt lds\n"
    "s_mov_b32 s49, 3145728\n"
    "s_add_u32 m0, s41, 24576\n"
    "s_nop 0\n"
    "buffer_load_dwordx4 v0, s[8:11], s49 offen offset:0 nt lds\n"
    "buffer_load_dwordx4 v1, s[8:11], s49 offen offset:1024 nt lds\n"
    "buffer_load_dwordx4 v2, s[8:11], s49 offen offset:2048 nt lds\n"
    "s_mov_b32 s49, 4718592\n"
    "s_add_u32 m0, s41, 36864\n"
    "s_nop 0\n"
    "buffer_load_dwordx4 v0, s[8:11], s49 offen offset:0 nt lds\n"
    "buffer_load_dwordx4 v1, s[8:11], s49 offen offset:1024 nt lds\n"
    "buffer_load_dwordx4 v2, s[8:11], s49 offen offset:2048 nt lds\n"
    "s_waitcnt vmcnt(23)\n"
    "s_barrier\n"
    "v_add_u32 v24, 49152, v17\n"
    "v_add_u32 v32, 49152, v18\n"
    "v_xor_b32 v33, 32, v32\n"
    "v_xor_b32 v25, 32, v24\n"
    "v_xor_b32 v26, 64, v24\n"
    "v_xor_b32 v27, 96, v24\n"
    "v_xor_b32 v28, 128, v24\n"
    "v_xor_b32 v29, 160, v24\n"
    "v_xor_b32 v30, 192, v24\n"
    "v_xor_b32 v31, 224, v24\n"
    "ds_read_b64_tr_b16 a[16:17], v24 offset:0\n"
    "ds_read_b64_tr_b16 a[18:19], v24 offset:4096\n"
    "ds_read_b64_tr_b16 a[40:41], v25 offset:0\n"
    "ds_read_b64_tr_b16 a[42:43], v25 offset:4096\n"
    "ds_read_b64_tr_b16 a[64:65], v26 offset:0\n"
    "ds_read_b64_tr_b16 a[66:67], v26 offset:4096\n"
    "ds_read_b64_tr_b16 a[88:89], v27 offset:0\n"
    "ds_read_b64_tr_b16 a[90:91], v27 offset:4096\n"
    "ds_read_b64_tr_b16 a[112:113], v28 offset:0\n"
    "ds_read_b64_tr_b16 a[114:115], v28 offset:4096\n"
    "ds_read_b64_tr_b16 a[136:137], v29 offset:0\n"
    "ds_read_b64_tr_b16 a[138:139], v29 offset:4096\n"
    "ds_read_b64_tr_b16 a[160:161], v30 offset:0\n"
    "ds_read_b64_tr_b16 a[162:163], v30 offset:4096\n"
    "ds_read_b64_tr_b16 a[184:185], v31 offset:0\n"
    "ds_read_b64_tr_b16 a[186:187], v31 offset:4096\n"
    "ds_read_b64_tr_b16 a[208:209], v32 offset:0\n"
    "ds_read_b64_tr_b16 a[210:211], v32 offset:4096\n"
    "ds_read_b64_tr_b16 a[232:233], v33 offset:0\n"
    "ds_read_b64_tr_b16 a[234:235], v33 offset:4096\n"
    "s_waitcnt lgkmcnt(0)\n"
    "s_barrier\n"
    "s_mov_b32 s49, 393216\n"
    "s_add_u32 m0, s41, 49152\n"
    "s_nop 0\n"
    "buffer_load_dwordx4 v0, s[8:11], s49 offen offset:0 nt lds\n"
    "buffer_load_dwordx4 v1, s[8:11], s49 offen offset:1024 nt lds\n"
    "buffer_load_dwordx4 v2, s[8:11], s49 offen offset:2048 nt lds\n"
    "s_mov_b32 s49, 1966080\n"
    "s_add_u32 m0, s41, 61440\n"
    "s_nop 0\n"
    "buffer_load_dwordx4 v0, s[8:11], s49 offen offset:0 nt lds\n"
    "buffer_load_dwordx4 v1, s[8:11], s49 offen offset:1024 nt lds\n"
    "buffer_load_dwordx4 v2, s[8:11], s49 offen offset:2048 nt lds\n"
    "s_mov_b32 s49, 3538944\n"
    "s_add_u32 m0, s41, 73728\n"
    "s_nop 0\n"
    "buffer_load_dwordx4 v0, s[8:11], s49 offen offset:0 nt lds\n"
    "buffer_load_dwordx4 v1, s[8:11], s49 offen offset:1024 nt lds\n"
    "buffer_load_dwordx4 v2, s[8:11], s49 offen offset:2048 nt lds\n"
    "s_mov_b32 s49, 5111808\n"
    "s_add_u32 m0, s41, 86016\n"
    "s_nop 0\n"
    "buffer_load_dwordx4 v0, s[8:11], s49 offen offset:0 nt lds\n"
    "buffer_load_dwordx4 v1, s[8:11], s49 offen offset:1024 nt lds\n"
    "buffer_load_dwordx4 v2, s[8:11], s49 offen offset:2048 nt lds\n"
    "s_waitcnt vmcnt(24)\n"
    "s_barrier\n"
    "v_add_u32 v24, 98304, v17\n"
    "v_add_u32 v32, 98304, v18\n"
    "v_xor_b32 v33, 32, v32\n"
    "v_xor_b32 v25, 32, v24\n"
    "v_xor_b32 v26, 64, v24\n"
    "v_xor_b32 v27, 96, v24\n"
    "v_xor_b32 v28, 128, v24\n"
    "v_xor_b32 v29, 160, v24\n"
    "v_xor_b32 v30, 192, v24\n"
    "v_xor_b32 v31, 224, v24\n"
    "ds_read_b64_tr_b16 a[20:21], v24 offset:0\n"
    "ds_read_b64_tr_b16 a[22:23], v24 offset:4096\n"
    "ds_read_b64_tr_b16 a[44:45], v25 offset:0\n"
    "ds_read_b64_tr_b16 a[46:47], v25 offset:4096\n"
    "ds_read_b64_tr_b16 a[68:69], v26 offset:0\n"
    "ds_read_b64_tr_b16 a[70:71], v26 offset:4096\n"
    "ds_read_b64_tr_b16 a[92:93], v27 offset:0\n"
    "ds_read_b64_tr_b16 a[94:95], v27 offset:4096\n"
    "ds_read_b64_tr_b16 a[116:117], v28 offset:0\n"
    "ds_read_b64_tr_b16 a[118:119], v28 offset:4096\n"
    "ds_read_b64_tr_b16 a[140:141], v29 offset:0\n"
    "ds_read_b64_tr_b16 a[142:143], v29 offset:4096\n"
    "ds_read_b64_tr_b16 a[164:165], v30 offset:0\n"
    "ds_read_b64_tr_b16 a[166:167], v30 offset:4096\n"
    "ds_read_b64_tr_b16 a[188:189], v31 offset:0\n"
    "ds_read_b64_tr_b16 a[190:191], v31 offset:4096\n"
    "ds_read_b64_tr_b16 a[212:213], v32 offset:0\n"
    "ds_read_b64_tr_b16 a[214:215], v32 offset:4096\n"
    "ds_read_b64_tr_b16 a[236:237], v33 offset:0\n"
    "ds_read_b64_tr_b16 a[238:239], v33 offset:4096\n"
    "s_waitcnt lgkmcnt(0)\n"
    "s_barrier\n"
    "s_mov_b32 s49, 786432\n"
    "s_add_u32 m0, s41, 98304\n"
    "s_nop 0\n"
    "buffer_load_dwordx4 v0, s[8:11], s49 offen offset:0 nt lds\n"
    "buffer_load_dwordx4 v1, s[8:11], s49 offen offset:1024 nt lds\n"
    "buffer_load_dwordx4 v2, s[8:11], s49 offen offset:2048 nt lds\n"
    "s_mov_b32 s49, 2359296\n"
    "s_add_u32 m0, s41, 110592\n"
    "s_nop 0\n"
    "buffer_load_dwordx4 v0, s[8:11], s49 offen offset:0 nt lds\n"
    "buffer_load_dwordx4 v1, s[8:11], s49 offen offset:1024 nt lds\n"
    "buffer_load_dwordx4 v2, s[8:11], s49 offen offset:2048 nt lds\n"
    "s_mov_b32 s49, 3932160\n"
    "s_add_u32 m0, s41, 122880\n"
    "s_nop 0\n"
    "buffer_load_dwordx4 v0, s[8:11], s49 offen offset:0 nt lds\n"
    "buffer_load_dwordx4 v1, s[8:11], s49 offen offset:1024 nt lds\n"
    "buffer_load_dwordx4 v2, s[8:11], s49 offen offset:2048 nt lds\n"
    "s_mov_b32 s49, 5505024\n"
    "s_add_u32 m0, s41, 135168\n"
    "s_nop 0\n"
    "buffer_load_dwordx4 v0, s[8:11], s49 offen offset:0 nt lds\n"
    "buffer_load_dwordx4 v1, s[8:11], s49 offen offset:1024 nt lds\n"
    "buffer_load_dwordx4 v2, s[8:11], s49 offen offset:2048 nt lds\n"
    "s_waitcnt vmcnt(24)\n"
    "s_barrier\n"
    "v_add_u32 v8, 0, v3\n"
    "ds_read_b64_tr_b16 a[240:241], v8 offset:0\n"
    "ds_read_b64_tr_b16 a[242:243], v8 offset:768\n"
    "ds_read_b64_tr_b16 v[176:177], v8 offset:128\n"
    "ds_read_b64_tr_b16 v[178:179], v8 offset:896\n"
    "ds_read_b64_tr_b16 v[12:13], v8 offset:256\n"
    "ds_read_b64_tr_b16 v[14:15], v8 offset:1024\n"
    "v_add_u32 v9, 0, v4\n"
    "ds_read_b64_tr_b16 v[112:113], v9 offset:0\n"
    "ds_read_b64_tr_b16 v[114:115], v9 offset:768\n"
    "ds_read_b64_tr_b16 v[192:193], v9 offset:128\n"
    "ds_read_b64_tr_b16 v[194:195], v9 offset:896\n"
    "ds_read_b64_tr_b16 v[28:29], v9 offset:256\n"
    "ds_read_b64_tr_b16 v[30:31], v9 offset:1024\n"
    "v_add_u32 v10, 0, v5\n"
    "ds_read_b64_tr_b16 v[128:129], v10 offset:0\n"
    "ds_read_b64_tr_b16 v[130:131], v10 offset:768\n"
    "ds_read_b64_tr_b16 v[208:209], v10 offset:128\n"
    "ds_read_b64_tr_b16 v[210:211], v10 offset:896\n"
    "ds_read_b64_tr_b16 v[44:45], v10 offset:256\n"
    "ds_read_b64_tr_b16 v[46:47], v10 offset:1024\n"
    "v_add_u32 v11, 0, v6\n"
    "ds_read_b64_tr_b16 v[144:145], v11 offset:0\n"
    "ds_read_b64_tr_b16 v[146:147], v11 offset:768\n"
    "ds_read_b64_tr_b16 v[224:225], v11 offset:128\n"
    "ds_read_b64_tr_b16 v[226:227], v11 offset:896\n"
    "ds_read_b64_tr_b16 v[60:61], v11 offset:256\n"
    "ds_read_b64_tr_b16 v[62:63], v11 offset:1024\n"
    "s_waitcnt lgkmcnt(0)\n"
    "s_barrier\n"
    "s_mov_b32 s49, 1179648\n"
    "s_add_u32 m0, s41, 0\n"
    "s_nop 0\n"
    "buffer_load_dwordx4 v0, s[8:11], s49 offen offset:0 nt lds\n"
    "buffer_load_dwordx4 v1, s[8:11], s49 offen offset:1024 nt lds\n"
    "buffer_load_dwordx4 v2, s[8:11], s49 offen offset:2048 nt lds\n"
    "s_mov_b32 s49, 2752512\n"
    "s_add_u32 m0, s41, 12288\n"
    "s_nop 0\n"
    "buffer_load_dwordx4 v0, s[8:11], s49 offen offset:0 nt lds\n"
    "buffer_load_dwordx4 v1, s[8:11], s49 offen offset:1024 nt lds\n"
    "buffer_load_dwordx4 v2, s[8:11], s49 offen offset:2048 nt lds\n"
    "s_mov_b32 s49, 4325376\n"
    "s_add_u32 m0, s41, 24576\n"
    "s_nop 0\n"
    "buffer_load_dwordx4 v0, s[8:11], s49 offen offset:0 nt lds\n"
    "buffer_load_dwordx4 v1, s[8:11], s49 offen offset:1024 nt lds\n"
    "buffer_load_dwordx4 v2, s[8:11], s49 offen offset:2048 nt lds\n"
    "s_mov_b32 s49, 5898240\n"
    "s_add_u32 m0, s41, 36864\n"
    "s_nop 0\n"
    "buffer_load_dwordx4 v0, s[8:11], s49 offen offset:0 nt lds\n"
    "buffer_load_dwordx4 v1, s[8:11], s49 offen offset:1024 nt lds\n"
    "buffer_load_dwordx4 v2, s[8:11], s49 offen offset:2048 nt lds\n"
    "s_waitcnt vmcnt(24)\n"
    "s_barrier\n"
    "v_add_u32 v8, 49152, v3\n"
    "ds_read_b64_tr_b16 a[244:245], v8 offset:0\n"
    "ds_read_b64_tr_b16 a[246:247], v8 offset:768\n"
    "ds_read_b64_tr_b16 v[180:181], v8 offset:128\n"
    "ds_read_b64_tr_b16 v[182:183], v8 offset:896\n"
    "ds_read_b64_tr_b16 v[16:17], v8 offset:256\n"
    "ds_read_b64_tr_b16 v[18:19], v8 offset:1024\n"
    "v_add_u32 v9, 49152, v4\n"
    "ds_read_b64_tr_b16 v[116:117], v9 offset:0\n"
    "ds_read_b64_tr_b16 v[118:119], v9 offset:768\n"
    "ds_read_b64_tr_b16 v[196:197], v9 offset:128\n"
    "ds_read_b64_tr_b16 v[198:199], v9 offset:896\n"
    "ds_read_b64_tr_b16 v[32:33], v9 offset:256\n"
    "ds_read_b64_tr_b16 v[34:35], v9 offset:1024\n"
    "v_add_u32 v10, 49152, v5\n"
    "ds_read_b64_tr_b16 v[132:133], v10 offset:0\n"
    "ds_read_b64_tr_b16 v[134:135], v10 offset:768\n"
    "ds_read_b64_tr_b16 v[212:213], v10 offset:128\n"
    "ds_read_b64_tr_b16 v[214:215], v10 offset:896\n"
    "ds_read_b64_tr_b16 v[48:49], v10 offset:256\n"
    "ds_read_b64_tr_b16 v[50:51], v10 offset:1024\n"
    "v_add_u32 v11, 49152, v6\n"
    "ds_read_b64_tr_b16 v[148:149], v11 offset:0\n"
    "ds_read_b64_tr_b16 v[150:151], v11 offset:768\n"
    "ds_read_b64_tr_b16 v[228:229], v11 offset:128\n"
    "ds_read_b64_tr_b16 v[230:231], v11 offset:896\n"
    "ds_read_b64_tr_b16 v[64:65], v11 offset:256\n"
    "ds_read_b64_tr_b16 v[66:67], v11 offset:1024\n"
    "s_waitcnt lgkmcnt(0)\n"
    "s_barrier\n"
    "s_waitcnt vmcnt(58)\n"
    "v_cmp_gt_i32 vcc, 32, v109\n"
    "s_nop 0\n"
    "s_cbranch_vccnz .Lvo_tk0_%=\n"
    ".Lvo_tk1_%=:\n"
    "s_lshl_b32 s48, s12, 11\n"
    "s_add_u32 s48, s48, 53248\n"
    "v_lshrrev_b32 v108, 2, v96\n"
    "s_mov_b32 s50, 0\n"
    "s_mov_b32 s51, s14\n"
    "s_add_u32 s43, s18, s13\n"
    "v_mov_b32 v111, s50\n"
    "s_mov_b64 exec, 0x1f0\n"
    "v_mov_b32 v111, s51\n"
    "s_mov_b64 exec, -1\n"
    "buffer_load_dword v109, v108, s[0:3], s24 offen sc1\n"
    "buffer_load_dword v110, v105, s[0:3], 0 offen sc1\n"
    "s_mov_b32 s49, 6291456\n"
    "s_add_u32 m0, s41, 49152\n"
    "s_nop 0\n"
    "buffer_load_dwordx4 v0, s[8:11], s49 offen offset:0 nt lds\n"
    "buffer_load_dwordx4 v1, s[8:11], s49 offen offset:1024 nt lds\n"
    "buffer_load_dwordx4 v2, s[8:11], s49 offen offset:2048 nt lds\n"
    "s_mov_b32 s49, 6684672\n"
    "s_add_u32 m0, s41, 61440\n"
    "s_nop 0\n"
    "buffer_load_dwordx4 v0, s[8:11], s49 offen offset:0 nt lds\n"
    "buffer_load_dwordx4 v1, s[8:11], s49 offen offset:1024 nt lds\n"
    "buffer_load_dwordx4 v2, s[8:11], s49 offen offset:2048 nt lds\n"
    "s_mov_b32 s49, 7077888\n"
    "s_add_u32 m0, s41, 73728\n"
    "s_nop 0\n"
    "buffer_load_dwordx4 v0, s[8:11], s49 offen offset:0 nt lds\n"
    "buffer_load_dwordx4 v1, s[8:11], s49 offen offset:1024 nt lds\n"
    "buffer_load_dwordx4 v2, s[8:11], s49 offen offset:2048 nt lds\n"
    "s_mov_b32 s49, 7471104\n"
    "s_add_u32 m0, s41, 86016\n"
    "s_nop 0\n"
    "buffer_load_dwordx4 v0, s[8:11], s49 offen offset:0 nt lds\n"
    "buffer_load_dwordx4 v1, s[8:11], s49 offen offset:1024 nt lds\n"
    "buffer_load_dwordx4 v2, s[8:11], s49 offen offset:2048 nt lds\n"
    "s_waitcnt vmcnt(26)\n"
    "s_barrier\n"
    "v_add_u32 v8, 98304, v3\n"
    "ds_read_b64_tr_b16 a[248:249], v8 offset:0\n"
    "ds_read_b64_tr_b16 a[250:251], v8 offset:768\n"
    "ds_read_b64_tr_b16 v[184:185], v8 offset:128\n"
    "ds_read_b64_tr_b16 v[186:187], v8 offset:896\n"
    "ds_read_b64_tr_b16 v[20:21], v8 offset:256\n"
    "ds_read_b64_tr_b16 v[22:23], v8 offset:1024\n"
    "v_add_u32 v9, 98304, v4\n"
    "ds_read_b64_tr_b16 v[120:121], v9 offset:0\n"
    "ds_read_b64_tr_b16 v[122:123], v9 offset:768\n"
    "ds_read_b64_tr_b16 v[200:201], v9 offset:128\n"
    "ds_read_b64_tr_b16 v[202:203], v9 offset:896\n"
    "ds_read_b64_tr_b16 v[36:37], v9 offset:256\n"
    "ds_read_b64_tr_b16 v[38:39], v9 offset:1024\n"
    "v_add_u32 v10, 98304, v5\n"
    "ds_read_b64_tr_b16 v[136:137], v10 offset:0\n"
    "ds_read_b64_tr_b16 v[138:139], v10 offset:768\n"
    "ds_read_b64_tr_b16 v[216:217], v10 offset:128\n"
    "ds_read_b64_tr_b16 v[218:219], v10 offset:896\n"
    "ds_read_b64_tr_b16 v[52:53], v10 offset:256\n"
    "ds_read_b64_tr_b16 v[54:55], v10 offset:1024\n"
    "v_add_u32 v11, 98304, v6\n"
    "ds_read_b64_tr_b16 v[152:153], v11 offset:0\n"
    "ds_read_b64_tr_b16 v[154:155], v11 offset:768\n"
    "ds_read_b64_tr_b16 v[232:233], v11 offset:128\n"
    "ds_read_b64_tr_b16 v[234:235], v11 offset:896\n"
    "ds_read_b64_tr_b16 v[68:69], v11 offset:256\n"
    "ds_read_b64_tr_b16 v[70:71], v11 offset:1024\n"
    "s_waitcnt lgkmcnt(0)\n"
    "s_barrier\n"
    "s_waitcnt vmcnt(14)\n"
    "s_barrier\n"
    "v_add_u32 v8, 0, v3\n"
    "ds_read_b64_tr_b16 a[252:253], v8 offset:0\n"
    "ds_read_b64_tr_b16 a[254:255], v8 offset:768\n"
    "ds_read_b64_tr_b16 v[188:189], v8 offset:128\n"
    "ds_read_b64_tr_b16 v[190:191], v8 offset:896\n"
    "ds_read_b64_tr_b16 v[24:25], v8 offset:256\n"
    "ds_read_b64_tr_b16 v[26:27], v8 offset:1024\n"
    "v_add_u32 v9, 0, v4\n"
    "ds_read_b64_tr_b16 v[124:125], v9 offset:0\n"
    "ds_read_b64_tr_b16 v[126:127], v9 offset:768\n"
    "ds_read_b64_tr_b16 v[204:205], v9 offset:128\n"
    "ds_read_b64_tr_b16 v[206:207], v9 offset:896\n"
    "ds_read_b64_tr_b16 v[40:41], v9 offset:256\n"
    "ds_read_b64_tr_b16 v[42:43], v9 offset:1024\n"
    "v_add_u32 v10, 0, v5\n"
    "ds_read_b64_tr_b16 v[140:141], v10 offset:0\n"
    "ds_read_b64_tr_b16 v[142:143], v10 offset:768\n"
    "ds_read_b64_tr_b16 v[220:221], v10 offset:128\n"
    "ds_read_b64_tr_b16 v[222:223], v10 offset:896\n"
    "ds_read_b64_tr_b16 v[56:57], v10 offset:256\n"
    "ds_read_b64_tr_b16 v[58:59], v10 offset:1024\n"
    "v_add_u32 v11, 0, v6\n"
    "ds_read_b64_tr_b16 v[156:157], v11 offset:0\n"
    "ds_read_b64_tr_b16 v[158:159], v11 offset:768\n"
    "ds_read_b64_tr_b16 v[236:237], v11 offset:128\n"
    "ds_read_b64_tr_b16 v[238:239], v11 offset:896\n"
    "ds_read_b64_tr_b16 v[72:73], v11 offset:256\n"
    "ds_read_b64_tr_b16 v[74:75], v11 offset:1024\n"
    "s_waitcnt lgkmcnt(0)\n"
    "s_barrier\n"
    "s_waitcnt vmcnt(12)\n"
    "v_cmp_gt_i32 vcc, 1, v109\n"
    "s_nop 0\n"
    "s_cbranch_vccnz .Lvo_e0_%=\n"
    "v_cmp_gt_i32 vcc, v111, v110\n"
    "s_nop 0\n"
    "s_cbranch_vccnz .Lvo_e0_%=\n"
    ".Lvo_e1_%=:\n"
    "s_sub_u32 m0, s17, 8192\n"
    "s_nop 0\n"
    "buffer_load_dwordx4 v96, s[0:3], s43 offen offset:0 sc1 lds\n"
    "buffer_load_dwordx4 v96, s[0:3], s43 offen offset:1024 sc1 lds\n"
    "buffer_load_dwordx4 v96, s[0:3], s43 offen offset:2048 sc1 lds\n"
    "buffer_load_dwordx4 v96, s[0:3], s43 offen offset:3072 sc1 lds\n"
    "s_sub_u32 m0, s17, 4096\n"
    "s_add_u32 s43, s43, 4096\n"
    "buffer_load_dwordx4 v96, s[0:3], s43 offen offset:0 sc1 lds\n"
    "buffer_load_dwordx4 v96, s[0:3], s43 offen offset:1024 sc1 lds\n"
    "buffer_load_dwordx4 v96, s[0:3], s43 offen offset:2048 sc1 lds\n"
    "buffer_load_dwordx4 v96, s[0:3], s43 offen offset:3072 sc1 lds\n"
    "s_waitcnt vmcnt(8)\n"
    "s_barrier\n"
    "v_add_u32 v8, 49152, v7\n"
    "ds_read_b64_tr_b16 v[160:161], v8 offset:0\n"
    "ds_read_b64_tr_b16 v[162:163], v8 offset:768\n"
    "ds_read_b64_tr_b16 v[240:241], v8 offset:128\n"
    "ds_read_b64_tr_b16 v[242:243], v8 offset:896\n"
    "ds_read_b64_tr_b16 v[76:77], v8 offset:256\n"
    "ds_read_b64_tr_b16 v[78:79], v8 offset:1024\n"
    "v_add_u32 v9, 61440, v7\n"
    "ds_read_b64_tr_b16 v[164:165], v9 offset:0\n"
    "ds_read_b64_tr_b16 v[166:167], v9 offset:768\n"
    "ds_read_b64_tr_b16 v[244:245], v9 offset:128\n"
    "ds_read_b64_tr_b16 v[246:247], v9 offset:896\n"
    "ds_read_b64_tr_b16 v[80:81], v9 offset:256\n"
    "ds_read_b64_tr_b16 v[82:83], v9 offset:1024\n"
    "v_add_u32 v10, 73728, v7\n"
    "ds_read_b64_tr_b16 v[168:169], v10 offset:0\n"
    "ds_read_b64_tr_b16 v[170:171], v10 offset:768\n"
    "ds_read_b64_tr_b16 v[248:249], v10 offset:128\n"
    "ds_read_b64_tr_b16 v[250:251], v10 offset:896\n"
    "ds_read_b64_tr_b16 v[84:85], v10 offset:256\n"
    "ds_read_b64_tr_b16 v[86:87], v10 offset:1024\n"
    "v_add_u32 v11, 86016, v7\n"
    "ds_read_b64_tr_b16 v[172:173], v11 offset:0\n"
    "ds_read_b64_tr_b16 v[174:175], v11 offset:768\n"
    "ds_read_b64_tr_b16 v[252:253], v11 offset:128\n"
    "ds_read_b64_tr_b16 v[254:255], v11 offset:896\n"
    "ds_read_b64_tr_b16 v[88:89], v11 offset:256\n"
    "ds_read_b64_tr_b16 v[90:91], v11 offset:1024\n"
    "s_waitcnt lgkmcnt(0)\n"
    "s_barrier\n"
    "s_mov_b32 m0, s48\n"
    "s_add_u32 s43, s43, s19\n"
    "buffer_load_dwordx4 v96, s[0:3], s43 offen offset:0 sc1 lds\n"
    "buffer_load_dwordx4 v96, s[0:3], s43 offen offset:1024 sc1 lds\n"
    "ds_write_b128 v97, v[12:15] offset:0\n"
    "ds_write_b128 v97, v[16:19] offset:1024\n"
    "ds_write_b128 v97, v[20:23] offset:2048\n"
    "ds_write_b128 v97, v[24:27] offset:3072\n"
    "ds_write_b128 v97, v[28:31] offset:4096\n"
    "ds_write_b128 v97, v[32:35] offset:5120\n"
    "ds_write_b128 v97, v[36:39] offset:6144\n"
    "ds_write_b128 v97, v[40:43] offset:7168\n"
    "ds_write_b128 v97, v[44:47] offset:8192\n"
    "ds_write_b128 v97, v[48:51] offset:9216\n"
    "ds_write_b128 v97, v[52:55] offset:10240\n"
    "ds_write_b128 v97, v[56:59] offset:11264\n"
    "ds_write_b128 v97, v[60:63] offset:12288\n"
    "ds_write_b128 v97, v[64:67] offset:13312\n"
    "ds_write_b128 v97, v[68:71] offset:14336\n"
    "ds_write_b128 v97, v[72:75] offset:15360\n"
    "ds_write_b128 v97, v[76:79] offset:16384\n"
    "ds_write_b128 v97, v[80:83] offset:17408\n"
    "ds_write_b128 v97, v[84:87] offset:18432\n"
    "ds_write_b128 v97, v[88:91] offset:19456\n"
    "s_mov_b32 s34, -4\n"
    "s_memrealtime s[30:31]\n"
    "s_waitcnt vmcnt(0) lgkmcnt(0)\n"
    "s_barrier\n"
    "ds_read_b128 v[24:27], v92 offset:0\n"
    "ds_read_b128 v[28:31], v92 offset:1024\n"
    "ds_read_b128 v[32:35], v93 offset:0\n"
    "ds_read_b128 v[36:39], v93 offset:1024\n"
    "ds_read_b128 v[40:43], v94 offset:0\n"
    "ds_read_b128 v[44:47], v94 offset:1024\n"
    "s_sub_u32 s46, s30, 1340\n"
    "s_mov_b32 s34, -3\n"
    "s_mov_b32 s35, -5\n"
    "s_mov_b32 s45, s23\n"
    "s_mov_b32 s38, 2130706432\n"
    "s_mov_b32 s39, 2130706432\n"
    "s_mov_b32 s40, 2130706432\n"
    "s_waitcnt lgkmcnt(0)\n"
    "s_branch .Lvo_vph_%=\n"
    ".Lvo_take_%=:\n"
    "v_mfma_f32_16x16x32_bf16 v[84:87], v[248:251], v[24:27], v[84:87]\n"
    ".Lvo_mk8x5_%=:\n"
    "s_waitcnt vmcnt(3)\n"
    ".Lvo_mk0x6_%=:\n"
    "ds_read_b128 v[72:75], v92 offset:8192\n"
    "v_cvt_pk_bf16_f32 v40, v64, v65\n"
    "v_mfma_f32_16x16x32_bf16 v[84:87], v[252:255], v[32:35], v[84:87]\n"
    "s_waitcnt vmcnt(2)\n"
    "ds_read_b128 v[76:79], v92 offset:9216\n"
    "v_cvt_pk_bf16_f32 v41, v66, v67\n"
    "v_mfma_f32_16x16x32_bf16 v[68:71], v[164:167], v[44:47], v[68:71]\n"
    "v_mov_b64 v[64:65], 0\n"
    "v_mov_b64 v[66:67], 0\n"
    "v_mfma_f32_16x16x32_bf16 v[68:71], v[168:171], v[28:31], v[68:71]\n"
    "s_waitcnt lgkmcnt(2)\n"
    "ds_read_b128 v[24:27], v97 offset:6144\n"
    "v_cvt_pk_bf16_f32 v42, v84, v85\n"
    "v_mfma_f32_16x16x32_bf16 v[68:71], v[172:175], v[36:39], v[68:71]\n"
    "v_cvt_pk_bf16_f32 v43, v86, v87\n"
    "v_mov_b64 v[84:85], 0\n"
    "v_mfma_f32_16x16x32_bf16 v[88:91], v[244:247], v[44:47], v[88:91]\n"
    "ds_write_b128 v92, v[40:43] offset:11264\n"
    "v_mfma_f32_16x16x32_bf16 v[88:91], v[248:251], v[28:31], v[88:91]\n"
    "s_waitcnt lgkmcnt(2)\n"
    "v_max_u32 v108, v72, v74\n"
    "v_max3_u32 v108, v108, v76, v78\n"
    "v_mfma_f32_16x16x32_bf16 v[88:91], v[252:255], v[36:39], v[88:91]\n"
    "v_cmp_le_u32 vcc, s52, v108\n"
    "v_mov_b64 v[86:87], 0\n"
    "v_smfmac_f32_16x16x64_bf16 v[64:67], v[72:75], a[240:247], v98\n"
    "s_cbranch_vccnz .Lvo_retry_%=\n"
    ".Lvo_go_%=:\n"
    "s_barrier\n"
    ".Lvo_mk1x7_%=:\n"
    "ds_read_b128 v[32:35], v93 offset:8192\n"
    "v_cvt_pk_bf16_f32 v44, v68, v69\n"
    "v_smfmac_f32_16x16x64_bf16 v[64:67], v[76:79], a[248:255], v98\n"
    "ds_read_b128 v[36:39], v93 offset:9216\n"
    "v_cvt_pk_bf16_f32 v45, v70, v71\n"
    "v_smfmac_f32_16x16x64_bf16 v[84:87], v[72:75], v[0:7], v98\n"
    "s_waitcnt vmcnt(1)\n"
    "ds_read_b128 v[80:83], v92 offset:10240\n"
    "v_mov_b64 v[68:69], 0\n"
    "v_mov_b64 v[70:71], 0\n"
    "v_smfmac_f32_16x16x64_bf16 v[84:87], v[76:79], v[8:15], v98\n"
    "ds_read_b128 v[28:31], v97 offset:7168\n"
    "v_cvt_pk_bf16_f32 v46, v88, v89\n"
    "v_smfmac_f32_16x16x64_bf16 v[68:71], v[72:75], v[176:183], v98\n"
    "ds_read_b128 v[0:3], v97 offset:12288\n"
    "v_cvt_pk_bf16_f32 v47, v90, v91\n"
    "v_smfmac_f32_16x16x64_bf16 v[68:71], v[76:79], v[184:191], v98\n"
    "s_waitcnt lgkmcnt(4)\n"
    "ds_read_b128 v[88:91], v94 offset:8192\n"
    "ds_write_b128 v92, v[44:47] offset:12288\n"
    "v_smfmac_f32_16x16x64_bf16 v[84:87], v[32:35], v[16:23], v98\n"
    "ds_read_b128 v[40:43], v94 offset:9216\n"
    "v_smfmac_f32_16x16x64_bf16 v[64:67], v[32:35], v[112:119], v98\n"
    "s_waitcnt lgkmcnt(5)\n"
    "ds_read_b128 v[4:7], v97 offset:13312\n"
    "v_max_u32 v108, v80, v82\n"
    "v_smfmac_f32_16x16x64_bf16 v[68:71], v[32:35], v[192:199], v98\n"
    "s_waitcnt lgkmcnt(5)\n"
    "ds_read_b128 v[44:47], v95 offset:8192\n"
    "v_cmp_eq_u32 vcc, -1, v108\n"
    "v_smfmac_f32_16x16x64_bf16 v[84:87], v[36:39], v[24:31], v98\n"
    "ds_read_b128 v[8:11], v97 offset:14336\n"
    "ds_read_b128 v[12:15], v97 offset:15360\n"
    "v_smfmac_f32_16x16x64_bf16 v[64:67], v[36:39], v[120:127], v98\n"
    "ds_read_b128 v[16:19], v97 offset:16384\n"
    "v_smfmac_f32_16x16x64_bf16 v[68:71], v[36:39], v[200:207], v98\n"
    "s_waitcnt lgkmcnt(6)\n"
    "s_cbranch_vccnz .Lvo_r2_%=\n"
    ".Lvo_go2_%=:\n"
    "s_barrier\n"
    "ds_read_b128 v[72:75], v96 offset:10240\n"
    "v_smfmac_f32_16x16x64_bf16 v[84:87], v[88:91], v[48:55], v98\n"
    "s_mov_b32 m0, s21\n"
    "ds_read_b128 v[80:83], v95 offset:9216\n"
    "ds_read_b128 v[76:79], v96 offset:23552\n"
    "v_smfmac_f32_16x16x64_bf16 v[64:67], v[88:91], v[128:135], v98\n"
    "s_waitcnt vmcnt(0)\n"
    "ds_read_b128 v[20:23], v97 offset:17408\n"
    "v_cmp_gt_i32 vcc, v111, v110\n"
    "v_smfmac_f32_16x16x64_bf16 v[68:71], v[88:91], v[208:215], v98\n"
    "s_waitcnt lgkmcnt(7)\n"
    "s_cbranch_vccnz .Lvo_wf_%=\n"
    ".Lvo_wfb_%=:\n"
    "v_smfmac_f32_16x16x64_bf16 v[64:67], v[40:43], v[136:143], v98\n"
    "buffer_load_dwordx4 v96, s[0:3], s44 offen sc1 lds\n"
    "v_smfmac_f32_16x16x64_bf16 v[64:67], v[44:47], v[144:151], v98\n"
    "s_waitcnt lgkmcnt(3)\n"
    "ds_read_b128 v[24:27], v97 offset:18432\n"
    "v_mov_b64 v[32:33], 0\n"
    "v_smfmac_f32_16x16x64_bf16 v[64:67], v[72:75], v[160:167], v98\n"
    "s_waitcnt lgkmcnt(3)\n"
    "ds_read_b128 v[28:31], v97 offset:19456\n"
    "v_mov_b64 v[34:35], 0\n"
    "v_smfmac_f32_16x16x64_bf16 v[64:67], v[80:83], v[152:159], v98\n"
    "s_waitcnt lgkmcnt(3)\n"
    "v_mov_b64 v[36:37], 0\n"
    "v_smfmac_f32_16x16x64_bf16 v[64:67], v[76:79], v[168:175], v98\n"
    "s_sub_u32 m0, s17, 8192\n"
    "v_mov_b64 v[38:39], 0\n"
    "v_smfmac_f32_16x16x64_bf16 v[84:87], v[40:43], v[56:63], v98\n"
    "v_mov_b64 v[48:49], 0\n"
    "v_smfmac_f32_16x16x64_bf16 v[84:87], v[44:47], v[0:7], v98\n"
    "ds_read_b128 v[0:3], v96 offset:11264\n"
    "s_nop 0\n"
    "v_add_f32 v64, v64, v66\n"
    "v_smfmac_f32_16x16x64_bf16 v[84:87], v[80:83], v[8:15], v98\n"
    "s_waitcnt lgkmcnt(3)\n"
    "ds_read_b128 v[4:7], v96 offset:12288\n"
    "v_add_f32 v65, v65, v67\n"
    "v_smfmac_f32_16x16x64_bf16 v[84:87], v[72:75], v[16:23], v98\n"
    "s_waitcnt lgkmcnt(2)\n"
    "ds_read_b128 v[8:11], v96 offset:24576\n"
    "v_cvt_pk_bf16_f32 v64, v64, v65\n"
    "v_smfmac_f32_16x16x64_bf16 v[84:87], v[76:79], v[24:31], v98\n"
    "ds_write_b32 v99, v64 offset:0\n"
    "v_smfmac_f32_16x16x64_bf16 v[68:71], v[40:43], v[216:223], v98\n"
    "ds_read_b128 v[12:15], v96 offset:25600\n"
    "v_mov_b64 v[50:51], 0\n"
    "v_smfmac_f32_16x16x64_bf16 v[68:71], v[44:47], v[224:231], v98\n"
    "ds_read_b128 v[16:19], v96 offset:37888\n"
    "s_nop 0\n"
    "v_add_f32 v84, v84, v86\n"
    "v_smfmac_f32_16x16x64_bf16 v[68:71], v[80:83], v[232:239], v98\n"
    "ds_read_b128 v[20:23], v96 offset:38912\n"
    "v_add_f32 v85, v85, v87\n"
    "v_smfmac_f32_16x16x64_bf16 v[68:71], v[72:75], v[240:247], v98\n"
    "ds_read_b128 v[24:27], v96 offset:51200\n"
    "v_cvt_pk_bf16_f32 v84, v84, v85\n"
    "v_smfmac_f32_16x16x64_bf16 v[68:71], v[76:79], v[248:255], v98\n"
    "ds_write_b32 v100, v84 offset:0\n"
    "s_waitcnt lgkmcnt(7)\n"
    "v_mfma_f32_16x16x32_bf16 v[40:43], v[0:3], a[0:3], 0\n"
    "ds_read_b128 v[28:31], v96 offset:52224\n"
    "v_mov_b64 v[52:53], 0\n"
    "v_mfma_f32_16x16x32_bf16 v[44:47], v[4:7], a[0:3], 0\n"
    "s_nop 1\n"
    "v_add_f32 v68, v68, v70\n"
    "v_add_f32 v69, v69, v71\n"
    "v_cvt_pk_bf16_f32 v68, v68, v69\n"
    "ds_write_b32 v99, v68 offset:256\n"
    "s_waitcnt lgkmcnt(4)\n"
    "v_mfma_f32_16x16x32_bf16 v[40:43], v[8:11], a[4:7], v[40:43]\n"
    "v_mov_b64 v[54:55], 0\n"
    "v_mov_b64 v[56:57], 0\n"
    "v_mfma_f32_16x16x32_bf16 v[44:47], v[12:15], a[4:7], v[44:47]\n"
    "v_mov_b64 v[58:59], 0\n"
    "v_mov_b64 v[60:61], 0\n"
    "v_mfma_f32_16x16x32_bf16 v[40:43], v[16:19], a[8:11], v[40:43]\n"
    "v_mov_b64 v[62:63], 0\n"
    "v_mov_b64 v[64:65], 0\n"
    "v_mfma_f32_16x16x32_bf16 v[44:47], v[20:23], a[8:11], v[44:47]\n"
    "v_mov_b64 v[66:67], 0\n"
    "v_mov_b64 v[68:69], 0\n"
    "s_waitcnt lgkmcnt(0)\n"
    "s_barrier\n"
    ".Lvo_mk2x8_%=:\n"
    "ds_read_b64_tr_b16 v[72:73], v101 offset:0\n"
    "ds_read_b64_tr_b16 v[74:75], v101 offset:64\n"
    "v_mfma_f32_16x16x32_bf16 v[40:43], v[24:27], a[12:15], v[40:43]\n"
    "ds_read_b64_tr_b16 v[76:77], v101 offset:1024\n"
    "ds_read_b64_tr_b16 v[78:79], v101 offset:1088\n"
    "v_mfma_f32_16x16x32_bf16 v[44:47], v[28:31], a[12:15], v[44:47]\n"
    "ds_read_b64_tr_b16 v[80:81], v101 offset:2048\n"
    "ds_read_b64_tr_b16 v[82:83], v101 offset:2112\n"
    "v_mov_b64 v[70:71], 0\n"
    "v_mov_b64 v[84:85], 0\n"
    "v_mov_b64 v[86:87], 0\n"
    "v_mov_b64 v[88:89], 0\n"
    "v_mov_b64 v[90:91], 0\n"
    "s_waitcnt lgkmcnt(4)\n"
    "v_smfmac_f32_16x16x64_bf16 v[32:35], v[72:75], a[192:199], v98\n"
    "buffer_load_dwordx4 v96, s[0:3], s43 offen offset:0 sc1 lds\n"
    "s_waitcnt lgkmcnt(2)\n"
    "v_smfmac_f32_16x16x64_bf16 v[32:35], v[76:79], a[200:207], v98\n"
    "s_waitcnt lgkmcnt(0)\n"
    "v_smfmac_f32_16x16x64_bf16 v[32:35], v[80:83], a[208:215], v98\n"
    "v_smfmac_f32_16x16x64_bf16 v[36:39], v[72:75], a[216:223], v98\n"
    "buffer_load_dwordx4 v96, s[0:3], s43 offen offset:1024 sc1 lds\n"
    "v_smfmac_f32_16x16x64_bf16 v[36:39], v[76:79], a[224:231], v98\n"
    "s_nop 1\n"
    "v_add_f32 v32, v32, v34\n"
    "v_smfmac_f32_16x16x64_bf16 v[36:39], v[80:83], a[232:239], v98\n"
    "v_add_f32 v33, v33, v35\n"
    "v_smfmac_f32_16x16x64_bf16 v[48:51], v[72:75], a[0:7], v98\n"
    "v_cvt_pk_bf16_f32 v108, v32, v33\n"
    "v_smfmac_f32_16x16x64_bf16 v[48:51], v[76:79], a[8:15], v98\n"
    "s_nop 1\n"
    "v_add_f32 v36, v36, v38\n"
    "v_smfmac_f32_16x16x64_bf16 v[48:51], v[80:83], a[16:23], v98\n"
    "v_add_f32 v37, v37, v39\n"
    "v_smfmac_f32_16x16x64_bf16 v[52:55], v[72:75], a[24:31], v98\n"
    "v_cvt_pk_bf16_f32 v109, v36, v37\n"
    "v_smfmac_f32_16x16x64_bf16 v[52:55], v[76:79], a[32:39], v98\n"
    "s_nop 1\n"
    "v_add_f32 v48, v48, v50\n"
    "v_permlane16_swap_b32 v108, v109\n"
    "v_min_u32 v108, -2, v108\n"
    "v_smfmac_f32_16x16x64_bf16 v[52:55], v[80:83], a[40:47], v98\n"
    "buffer_store_dwordx2 v[108:109], v103, s[0:3], s36 offen sc1\n"
    "v_add_f32 v49, v49, v51\n"
    "v_smfmac_f32_16x16x64_bf16 v[56:59], v[72:75], a[48:55], v98\n"
    "v_cvt_pk_bf16_f32 v48, v48, v49\n"
    "v_smfmac_f32_16x16x64_bf16 v[56:59], v[76:79], a[56:63], v98\n"
    "s_nop 1\n"
    "v_add_f32 v52, v52, v54\n"
    "v_smfmac_f32_16x16x64_bf16 v[56:59], v[80:83], a[64:71], v98\n"
    "v_add_f32 v53, v53, v55\n"
    "v_smfmac_f32_16x16x64_bf16 v[60:63], v[72:75], a[72:79], v98\n"
    "v_cvt_pk_bf16_f32 v49, v52, v53\n"
    "v_smfmac_f32_16x16x64_bf16 v[60:63], v[76:79], a[80:87], v98\n"
    "s_waitcnt vmcnt(3)\n"
    "s_barrier\n"
    "v_add_f32 v56, v56, v58\n"
    "v_smfmac_f32_16x16x64_bf16 v[60:63], v[80:83], a[88:95], v98\n"
    "ds_read2_b64 v[32:35], v106 offset0:0 offset1:128\n"
    "v_add_f32 v57, v57, v59\n"
    "v_smfmac_f32_16x16x64_bf16 v[64:67], v[72:75], a[96:103], v98\n"
    "ds_read2_b64 v[36:39], v106 offset0:1 offset1:129\n"
    "v_cvt_pk_bf16_f32 v50, v56, v57\n"
    "v_smfmac_f32_16x16x64_bf16 v[64:67], v[76:79], a[104:111], v98\n"
    "s_nop 1\n"
    "v_add_f32 v60, v60, v62\n"
    "v_permlane32_swap_b32 v48, v50\n"
    "v_smfmac_f32_16x16x64_bf16 v[64:67], v[80:83], a[112:119], v98\n"
    "v_add_f32 v61, v61, v63\n"
    "v_smfmac_f32_16x16x64_bf16 v[68:71], v[72:75], a[120:127], v98\n"
    "v_cvt_pk_bf16_f32 v51, v60, v61\n"
    "v_smfmac_f32_16x16x64_bf16 v[68:71], v[76:79], a[128:135], v98\n"
    "s_nop 0\n"
    "v_permlane32_swap_b32 v49, v51\n"
    "v_smfmac_f32_16x16x64_bf16 v[68:71], v[80:83], a[136:143], v98\n"
    "s_nop 0\n"
    "v_permlane16_swap_b32 v50, v51\n"
    "v_permlane16_swap_b32 v48, v49\n"
    "v_min_u32 v48, -2, v48\n"
    "v_smfmac_f32_16x16x64_bf16 v[84:87], v[72:75], a[144:151], v98\n"
    "buffer_store_dwordx4 v[48:51], v102, s[0:3], s36 offen sc1\n"
    "v_add_f32 v64, v64, v66\n"
    "v_add_f32 v65, v65, v67\n"
    "v_cvt_pk_bf16_f32 v64, v64, v65\n"
    "v_smfmac_f32_16x16x64_bf16 v[84:87], v[76:79], a[152:159], v98\n"
    "v_add_f32 v68, v68, v70\n"
    "v_smfmac_f32_16x16x64_bf16 v[84:87], v[80:83], a[160:167], v98\n"
    "v_add_f32 v69, v69, v71\n"
    "v_smfmac_f32_16x16x64_bf16 v[88:91], v[72:75], a[168:175], v98\n"
    "v_cvt_pk_bf16_f32 v65, v68, v69\n"
    "v_smfmac_f32_16x16x64_bf16 v[88:91], v[76:79], a[176:183], v98\n"
    "s_nop 1\n"
    "v_add_f32 v84, v84, v86\n"
    "v_smfmac_f32_16x16x64_bf16 v[88:91], v[80:83], a[184:191], v98\n"
    ".Lvo_mk3x9_%=:\n"
    ".Lvo_uph_%=:\n"
    "s_waitcnt lgkmcnt(0)\n"
    "v_add_f32 v85, v85, v87\n"
    "v_mfma_f32_16x16x32_bf16 v[56:59], v[32:35], a[16:19], v[40:43]\n"
    "v_cvt_pk_bf16_f32 v66, v84, v85\n"
    "v_mfma_f32_16x16x32_bf16 v[60:63], v[36:39], a[16:19], v[44:47]\n"
    "s_nop 0\n"
    "v_permlane32_swap_b32 v64, v66\n"
    "v_add_f32 v88, v88, v90\n"
    "v_add_f32 v89, v89, v91\n"
    "v_cvt_pk_bf16_f32 v67, v88, v89\n"
    "s_nop 1\n"
    "v_permlane32_swap_b32 v65, v67\n"
    "s_nop 1\n"
    "v_permlane16_swap_b32 v66, v67\n"
    "v_permlane16_swap_b32 v64, v65\n"
    "v_min_u32 v64, -2, v64\n"
    "buffer_store_dwordx4 v[64:67], v102, s[0:3], s37 offen sc1\n"
    ".Lvo_mkum_%=:\n"
    "s_cmp_eq_u32 s34, 63\n"
    "s_cbranch_scc1 .Lvo_end_%=\n"
    "ds_read2_b64 v[40:43], v107 offset0:0 offset1:128\n"
    "ds_read2_b64 v[44:47], v107 offset0:1 offset1:129\n"
    "v_mfma_f32_16x16x32_bf16 v[48:51], v[0:3], a[24:27], 0\n"
    "v_mfma_f32_16x16x32_bf16 v[52:55], v[4:7], a[24:27], 0\n"
    ".Lvo_mkL0_%=:\n"
    "v_mfma_f32_16x16x32_bf16 v[48:51], v[8:11], a[28:31], v[48:51]\n"
    "v_mfma_f32_16x16x32_bf16 v[52:55], v[12:15], a[28:31], v[52:55]\n"
    "v_mfma_f32_16x16x32_bf16 v[48:51], v[16:19], a[32:35], v[48:51]\n"
    "v_mov_b64 v[88:89], -1\n"
    "v_mov_b64 v[90:91], -1\n"
    "v_mfma_f32_16x16x32_bf16 v[52:55], v[20:23], a[32:35], v[52:55]\n"
    "s_waitcnt lgkmcnt(0)\n"
    "v_mfma_f32_16x16x32_bf16 v[56:59], v[40:43], a[20:23], v[56:59]\n"
    "buffer_load_dwordx4 v96, s[0:3], s43 offen offset:2048 sc1 lds\n"
    "v_mfma_f32_16x16x32_bf16 v[60:63], v[44:47], a[20:23], v[60:63]\n"
    "v_mfma_f32_16x16x32_bf16 v[48:51], v[24:27], a[36:39], v[48:51]\n"
    "s_nop 1\n"
    "v_cvt_pk_bf16_f32 v72, v56, v57\n"
    "v_mfma_f32_16x16x32_bf16 v[52:55], v[28:31], a[36:39], v[52:55]\n"
    "v_cvt_pk_bf16_f32 v74, v58, v59\n"
    "v_cvt_pk_bf16_f32 v73, v60, v61\n"
    "v_mfma_f32_16x16x32_bf16 v[48:51], v[32:35], a[40:43], v[48:51]\n"
    "v_cvt_pk_bf16_f32 v75, v62, v63\n"
    "v_mfma_f32_16x16x32_bf16 v[52:55], v[36:39], a[40:43], v[52:55]\n"
    "buffer_load_dwordx4 v96, s[0:3], s43 offen offset:3072 sc1 lds\n"
    "v_mfma_f32_16x16x32_bf16 v[48:51], v[40:43], a[44:47], v[48:51]\n"
    "s_sub_u32 m0, s17, 4096\n"
    "s_add_u32 s43, s43, 4096\n"
    "v_mfma_f32_16x16x32_bf16 v[52:55], v[44:47], a[44:47], v[52:55]\n"
    "v_mfma_f32_16x16x32_bf16 v[56:59], v[0:3], a[48:51], 0\n"
    "s_nop 1\n"
    "v_cvt_pk_bf16_f32 v76, v48, v49\n"
    "v_mfma_f32_16x16x32_bf16 v[60:63], v[4:7], a[48:51], 0\n"
    "v_cvt_pk_bf16_f32 v78, v50, v51\n"
    "v_cvt_pk_bf16_f32 v77, v52, v53\n"
    "v_mfma_f32_16x16x32_bf16 v[56:59], v[8:11], a[52:55], v[56:59]\n"
    "v_cvt_pk_bf16_f32 v79, v54, v55\n"
    "v_mfma_f32_16x16x32_bf16 v[60:63], v[12:15], a[52:55], v[60:63]\n"
    "buffer_load_dwordx4 v96, s[0:3], s43 offen offset:0 sc1 lds\n"
    "v_mfma_f32_16x16x32_bf16 v[56:59], v[16:19], a[56:59], v[56:59]\n"
    "v_mfma_f32_16x16x32_bf16 v[60:63], v[20:23], a[56:59], v[60:63]\n"
    "v_mfma_f32_16x16x32_bf16 v[56:59], v[24:27], a[60:63], v[56:59]\n"
    "v_mfma_f32_16x16x32_bf16 v[60:63], v[28:31], a[60:63], v[60:63]\n"
    "buffer_load_dwordx4 v96, s[0:3], s43 offen offset:1024 sc1 lds\n"
    "v_mfma_f32_16x16x32_bf16 v[56:59], v[32:35], a[64:67], v[56:59]\n"
    "v_mfma_f32_16x16x32_bf16 v[60:63], v[36:39], a[64:67], v[60:63]\n"
    "v_mfma_f32_16x16x32_bf16 v[56:59], v[40:43], a[68:71], v[56:59]\n"
    "v_mfma_f32_16x16x32_bf16 v[60:63], v[44:47], a[68:71], v[60:63]\n"
    "buffer_load_dwordx4 v96, s[0:3], s43 offen offset:2048 sc1 lds\n"
    "v_mfma_f32_16x16x32_bf16 v[48:51], v[0:3], a[72:75], 0\n"
    "s_nop 1\n"
    "v_cvt_pk_bf16_f32 v80, v56, v57\n"
    "v_mfma_f32_16x16x32_bf16 v[52:55], v[4:7], a[72:75], 0\n"
    "v_cvt_pk_bf16_f32 v82, v58, v59\n"
    "v_cvt_pk_bf16_f32 v81, v60, v61\n"
    "v_mfma_f32_16x16x32_bf16 v[48:51], v[8:11], a[76:79], v[48:51]\n"
    "v_cvt_pk_bf16_f32 v83, v62, v63\n"
    "v_mfma_f32_16x16x32_bf16 v[52:55], v[12:15], a[76:79], v[52:55]\n"
    "buffer_store_dwordx4 v[72:75], v96, s[0:3], s40 offen sc1\n"
    "s_add_u32 s40, s40, 0x10000\n"
    "v_mfma_f32_16x16x32_bf16 v[48:51], v[16:19], a[80:83], v[48:51]\n"
    ".Lvo_mkL1_%=:\n"
    "v_mfma_f32_16x16x32_bf16 v[52:55], v[20:23], a[80:83], v[52:55]\n"
    "v_mfma_f32_16x16x32_bf16 v[48:51], v[24:27], a[84:87], v[48:51]\n"
    "v_mfma_f32_16x16x32_bf16 v[52:55], v[28:31], a[84:87], v[52:55]\n"
    "v_mfma_f32_16x16x32_bf16 v[48:51], v[32:35], a[88:91], v[48:51]\n"
    "v_mfma_f32_16x16x32_bf16 v[52:55], v[36:39], a[88:91], v[52:55]\n"
    "v_mfma_f32_16x16x32_bf16 v[48:51], v[40:43], a[92:95], v[48:51]\n"
    "buffer_load_dwordx4 v96, s[0:3], s43 offen offset:3072 sc1 lds\n"
    "v_mfma_f32_16x16x32_bf16 v[52:55], v[44:47], a[92:95], v[52:55]\n"
    "v_mfma_f32_16x16x32_bf16 v[56:59], v[0:3], a[96:99], 0\n"
    "s_nop 1\n"
    "v_cvt_pk_bf16_f32 v72, v48, v49\n"
    "v_mfma_f32_16x16x32_bf16 v[60:63], v[4:7], a[96:99], 0\n"
    "v_cvt_pk_bf16_f32 v74, v50, v51\n"
    "v_cvt_pk_bf16_f32 v73, v52, v53\n"
    "v_mfma_f32_16x16x32_bf16 v[56:59], v[8:11], a[100:103], v[56:59]\n"
    "buffer_store_dwordx4 v[76:79], v96, s[0:3], s40 offen sc1\n"
    "v_cvt_pk_bf16_f32 v75, v54, v55\n"
    "s_add_u32 s40, s40, 0x10000\n"
    "s_mov_b32 m0, s48\n"
    "v_mfma_f32_16x16x32_bf16 v[60:63], v[12:15], a[100:103], v[60:63]\n"
    "s_add_u32 s43, s43, s19\n"
    "v_mfma_f32_16x16x32_bf16 v[56:59], v[16:19], a[104:107], v[56:59]\n"
    "v_mfma_f32_16x16x32_bf16 v[60:63], v[20:23], a[104:107], v[60:63]\n"
    "v_mfma_f32_16x16x32_bf16 v[56:59], v[24:27], a[108:111], v[56:59]\n"
    "v_mfma_f32_16x16x32_bf16 v[60:63], v[28:31], a[108:111], v[60:63]\n"
    "v_mfma_f32_16x16x32_bf16 v[56:59], v[32:35], a[112:115], v[56:59]\n"
    "v_mfma_f32_16x16x32_bf16 v[60:63], v[36:39], a[112:115], v[60:63]\n"
    "buffer_load_dwordx4 v96, s[0:3], s43 offen offset:0 sc1 lds\n"
    "v_mfma_f32_16x16x32_bf16 v[56:59], v[40:43], a[116:119], v[56:59]\n"
    "v_mfma_f32_16x16x32_bf16 v[60:63], v[44:47], a[116:119], v[60:63]\n"
    "v_mfma_f32_16x16x32_bf16 v[48:51], v[0:3], a[120:123], 0\n"
    "s_nop 1\n"
    "v_cvt_pk_bf16_f32 v76, v56, v57\n"
    "v_mfma_f32_16x16x32_bf16 v[52:55], v[4:7], a[120:123], 0\n"
    "buffer_store_dwordx4 v[80:83], v96, s[0:3], s40 offen sc1\n"
    "v_cvt_pk_bf16_f32 v78, v58, v59\n"
    "v_cvt_pk_bf16_f32 v77, v60, v61\n"
    "s_add_u32 s40, s40, 0x10000\n"
    "v_mfma_f32_16x16x32_bf16 v[48:51], v[8:11], a[124:127], v[48:51]\n"
    "v_cvt_pk_bf16_f32 v79, v62, v63\n"
    "v_mfma_f32_16x16x32_bf16 v[52:55], v[12:15], a[124:127], v[52:55]\n"
    "v_mfma_f32_16x16x32_bf16 v[48:51], v[16:19], a[128:131], v[48:51]\n"
    "v_mfma_f32_16x16x32_bf16 v[52:55], v[20:23], a[128:131], v[52:55]\n"
    "v_mfma_f32_16x16x32_bf16 v[48:51], v[24:27], a[132:135], v[48:51]\n"
    "v_mfma_f32_16x16x32_bf16 v[52:55], v[28:31], a[132:135], v[52:55]\n"
    "v_mfma_f32_16x16x32_bf16 v[48:51], v[32:35], a[136:139], v[48:51]\n"
    "buffer_load_dwordx4 v96, s[0:3], s43 offen offset:1024 sc1 lds\n"
    "v_mfma_f32_16x16x32_bf16 v[52:55], v[36:39], a[136:139], v[52:55]\n"
    "v_mfma_f32_16x16x32_bf16 v[48:51], v[40:43], a[140:143], v[48:51]\n"
    ".Lvo_mkL2_%=:\n"
    "v_mfma_f32_16x16x32_bf16 v[52:55], v[44:47], a[140:143], v[52:55]\n"
    "v_mfma_f32_16x16x32_bf16 v[56:59], v[0:3], a[144:147], 0\n"
    "buffer_store_dwordx4 v[72:75], v96, s[0:3], s40 offen sc1\n"
    "s_nop 0\n"
    "v_cvt_pk_bf16_f32 v80, v48, v49\n"
    "s_add_u32 s40, s40, 0x10000\n"
    "v_mfma_f32_16x16x32_bf16 v[60:63], v[4:7], a[144:147], 0\n"
    "v_cvt_pk_bf16_f32 v82, v50, v51\n"
    "v_cvt_pk_bf16_f32 v81, v52, v53\n"
    "v_mfma_f32_16x16x32_bf16 v[56:59], v[8:11], a[148:151], v[56:59]\n"
    "v_cvt_pk_bf16_f32 v83, v54, v55\n"
    "v_mfma_f32_16x16x32_bf16 v[60:63], v[12:15], a[148:151], v[60:63]\n"
    "v_mfma_f32_16x16x32_bf16 v[56:59], v[16:19], a[152:155], v[56:59]\n"
    "v_mfma_f32_16x16x32_bf16 v[60:63], v[20:23], a[152:155], v[60:63]\n"
    "v_mfma_f32_16x16x32_bf16 v[56:59], v[24:27], a[156:159], v[56:59]\n"
    "v_mfma_f32_16x16x32_bf16 v[60:63], v[28:31], a[156:159], v[60:63]\n"
    "buffer_load_dwordx2 v[108:109], off, s[0:3], s45 sc1\n"
    "v_mfma_f32_16x16x32_bf16 v[56:59], v[32:35], a[160:163], v[56:59]\n"
    "v_mfma_f32_16x16x32_bf16 v[60:63], v[36:39], a[160:163], v[60:63]\n"
    "v_mfma_f32_16x16x32_bf16 v[56:59], v[40:43], a[164:167], v[56:59]\n"
    "v_mfma_f32_16x16x32_bf16 v[60:63], v[44:47], a[164:167], v[60:63]\n"
    "buffer_store_dwordx4 v[76:79], v96, s[0:3], s40 offen sc1\n"
    "s_add_u32 s40, s40, 0x10000\n"
    "v_mfma_f32_16x16x32_bf16 v[48:51], v[0:3], a[168:171], 0\n"
    "s_nop 1\n"
    "v_cvt_pk_bf16_f32 v72, v56, v57\n"
    "v_mfma_f32_16x16x32_bf16 v[52:55], v[4:7], a[168:171], 0\n"
    "v_cvt_pk_bf16_f32 v74, v58, v59\n"
    "v_cvt_pk_bf16_f32 v73, v60, v61\n"
    "v_mfma_f32_16x16x32_bf16 v[48:51], v[8:11], a[172:175], v[48:51]\n"
    "v_cvt_pk_bf16_f32 v75, v62, v63\n"
    "v_mfma_f32_16x16x32_bf16 v[52:55], v[12:15], a[172:175], v[52:55]\n"
    "v_mfma_f32_16x16x32_bf16 v[48:51], v[16:19], a[176:179], v[48:51]\n"
    "v_mfma_f32_16x16x32_bf16 v[52:55], v[20:23], a[176:179], v[52:55]\n"
    "v_mfma_f32_16x16x32_bf16 v[48:51], v[24:27], a[180:183], v[48:51]\n"
    "buffer_store_dwordx4 v[88:91], v102, s[0:3], s38 offen sc1\n"
    "v_mfma_f32_16x16x32_bf16 v[52:55], v[28:31], a[180:183], v[52:55]\n"
    "v_mfma_f32_16x16x32_bf16 v[48:51], v[32:35], a[184:187], v[48:51]\n"
    "v_mfma_f32_16x16x32_bf16 v[52:55], v[36:39], a[184:187], v[52:55]\n"
    "v_mfma_f32_16x16x32_bf16 v[48:51], v[40:43], a[188:191], v[48:51]\n"
    "v_mfma_f32_16x16x32_bf16 v[52:55], v[44:47], a[188:191], v[52:55]\n"
    "v_mfma_f32_16x16x32_bf16 v[56:59], v[0:3], a[192:195], 0\n"
    "s_nop 1\n"
    "v_cvt_pk_bf16_f32 v76, v48, v49\n"
    "v_mfma_f32_16x16x32_bf16 v[60:63], v[4:7], a[192:195], 0\n"
    "buffer_store_dwordx4 v[80:83], v96, s[0:3], s40 offen sc1\n"
    "v_cvt_pk_bf16_f32 v78, v50, v51\n"
    "v_cvt_pk_bf16_f32 v77, v52, v53\n"
    "s_add_u32 s40, s40, 0x10000\n"
    "v_mfma_f32_16x16x32_bf16 v[56:59], v[8:11], a[196:199], v[56:59]\n"
    "v_cvt_pk_bf16_f32 v79, v54, v55\n"
    "v_mfma_f32_16x16x32_bf16 v[60:63], v[12:15], a[196:199], v[60:63]\n"
    "v_mfma_f32_16x16x32_bf16 v[56:59], v[16:19], a[200:203], v[56:59]\n"
    ".Lvo_mkL3_%=:\n"
    "v_mfma_f32_16x16x32_bf16 v[60:63], v[20:23], a[200:203], v[60:63]\n"
    "v_mfma_f32_16x16x32_bf16 v[56:59], v[24:27], a[204:207], v[56:59]\n"
    "v_mfma_f32_16x16x32_bf16 v[60:63], v[28:31], a[204:207], v[60:63]\n"
    "v_mfma_f32_16x16x32_bf16 v[56:59], v[32:35], a[208:211], v[56:59]\n"
    "buffer_store_dwordx4 v[88:91], v102, s[0:3], s39 offen sc1\n"
    "v_mfma_f32_16x16x32_bf16 v[60:63], v[36:39], a[208:211], v[60:63]\n"
    "v_mfma_f32_16x16x32_bf16 v[56:59], v[40:43], a[212:215], v[56:59]\n"
    "v_mfma_f32_16x16x32_bf16 v[60:63], v[44:47], a[212:215], v[60:63]\n"
    "s_waitcnt vmcnt(15)\n"
    "s_barrier\n"
    "v_mfma_f32_16x16x32_bf16 v[48:51], v[24:27], a[228:231], 0\n"
    "s_nop 1\n"
    "v_cvt_pk_bf16_f32 v80, v56, v57\n"
    "v_mfma_f32_16x16x32_bf16 v[52:55], v[28:31], a[228:231], 0\n"
    "v_cvt_pk_bf16_f32 v82, v58, v59\n"
    "v_cvt_pk_bf16_f32 v81, v60, v61\n"
    "v_mfma_f32_16x16x32_bf16 v[48:51], v[32:35], a[232:235], v[48:51]\n"
    "ds_read_b128 v[24:27], v92 offset:0\n"
    "v_cvt_pk_bf16_f32 v83, v62, v63\n"
    "v_mfma_f32_16x16x32_bf16 v[52:55], v[36:39], a[232:235], v[52:55]\n"
    "buffer_store_dwordx4 v[72:75], v96, s[0:3], s40 offen sc1\n"
    "ds_read_b128 v[28:31], v92 offset:1024\n"
    "s_add_u32 s40, s40, 0x10000\n"
    "v_mfma_f32_16x16x32_bf16 v[48:51], v[40:43], a[236:239], v[48:51]\n"
    "ds_read_b128 v[32:35], v93 offset:0\n"
    "v_mfma_f32_16x16x32_bf16 v[52:55], v[44:47], a[236:239], v[52:55]\n"
    "ds_read_b128 v[36:39], v93 offset:1024\n"
    "v_mfma_f32_16x16x32_bf16 v[48:51], v[0:3], a[216:219], v[48:51]\n"
    "ds_read_b128 v[40:43], v94 offset:0\n"
    "v_mfma_f32_16x16x32_bf16 v[52:55], v[4:7], a[216:219], v[52:55]\n"
    "ds_read_b128 v[44:47], v94 offset:1024\n"
    "v_mfma_f32_16x16x32_bf16 v[48:51], v[8:11], a[220:223], v[48:51]\n"
    ".Lvo_mkL4_%=:\n"
    "v_mfma_f32_16x16x32_bf16 v[52:55], v[12:15], a[220:223], v[52:55]\n"
    "v_mfma_f32_16x16x32_bf16 v[48:51], v[16:19], a[224:227], v[48:51]\n"
    "buffer_store_dwordx2 v[88:89], v103, s[0:3], s38 offen sc1\n"
    "v_mfma_f32_16x16x32_bf16 v[52:55], v[20:23], a[224:227], v[52:55]\n"
    ".Lvo_mk4x10_%=:\n"
    ".Lvo_vph_%=:\n"
    "s_waitcnt lgkmcnt(4)\n"
    "v_mfma_f32_16x16x32_bf16 v[64:67], a[240:243], v[24:27], 0\n"
    "s_nop 1\n"
    "v_cvt_pk_bf16_f32 v72, v48, v49\n"
    "v_mfma_f32_16x16x32_bf16 v[84:87], v[176:179], v[24:27], 0\n"
    "s_add_u32 s34, s34, 1\n"
    "v_cvt_pk_bf16_f32 v74, v50, v51\n"
    "v_cvt_pk_bf16_f32 v73, v52, v53\n"
    "v_mfma_f32_16x16x32_bf16 v[68:71], a[240:243], v[28:31], 0\n"
    "s_add_u32 s36, s34, 0\n"
    "ds_read_b128 v[24:27], v95 offset:0\n"
    "v_cvt_pk_bf16_f32 v75, v54, v55\n"
    "v_mfma_f32_16x16x32_bf16 v[88:91], v[176:179], v[28:31], 0\n"
    "s_waitcnt lgkmcnt(3)\n"
    "ds_read_b128 v[28:31], v95 offset:1024\n"
    "s_and_b32 s36, s36, 3\n"
    "v_mfma_f32_16x16x32_bf16 v[64:67], v[112:115], v[32:35], v[64:67]\n"
    "s_lshl_b32 s36, s36, 22\n"
    "s_add_u32 s41, s15, s36\n"
    "s_add_u32 s42, s16, s36\n"
    "v_mfma_f32_16x16x32_bf16 v[84:87], v[192:195], v[32:35], v[84:87]\n"
    "s_add_u32 s37, s36, 0x10000\n"
    "s_add_u32 s50, s34, 3\n"
    "ds_read_b128 v[0:3], v97 offset:0\n"
    "v_mfma_f32_16x16x32_bf16 v[68:71], v[112:115], v[36:39], v[68:71]\n"
    "buffer_store_dwordx4 v[76:79], v96, s[0:3], s40 offen sc1\n"
    "ds_read_b128 v[32:35], v92 offset:2048\n"
    "s_add_u32 s40, s40, 0x10000\n"
    "s_cmp_gt_i32 s50, 64\n"
    "s_cselect_b32 s50, 0, s50\n"
    "v_mfma_f32_16x16x32_bf16 v[88:91], v[192:195], v[36:39], v[88:91]\n"
    "s_waitcnt lgkmcnt(4)\n"
    "ds_read_b128 v[36:39], v92 offset:3072\n"
    "s_add_u32 s51, s34, 4\n"
    "v_mfma_f32_16x16x32_bf16 v[64:67], v[128:131], v[40:43], v[64:67]\n"
    "s_cmp_gt_i32 s51, 64\n"
    "s_cselect_b32 s51, 0, s51\n"
    "s_mul_i32 s51, s51, s14\n"
    "v_mfma_f32_16x16x32_bf16 v[84:87], v[208:211], v[40:43], v[84:87]\n"
    "ds_read_b128 v[4:7], v97 offset:1024\n"
    "v_mov_b32 v111, s50\n"
    "v_mfma_f32_16x16x32_bf16 v[68:71], v[128:131], v[44:47], v[68:71]\n"
    "ds_read_b128 v[40:43], v93 offset:2048\n"
    "v_mfma_f32_16x16x32_bf16 v[88:91], v[208:211], v[44:47], v[88:91]\n"
    "s_waitcnt lgkmcnt(5)\n"
    "ds_read_b128 v[44:47], v93 offset:3072\n"
    "v_mfma_f32_16x16x32_bf16 v[64:67], v[144:147], v[24:27], v[64:67]\n"
    "s_mov_b64 exec, 0x1f0\n"
    "v_mov_b32 v111, s51\n"
    "s_mov_b64 exec, -1\n"
    "v_mfma_f32_16x16x32_bf16 v[84:87], v[224:227], v[24:27], v[84:87]\n"
    "buffer_store_dwordx4 v[80:83], v104, s[0:3], s38 offen sc1\n"
    "ds_read_b128 v[8:11], v97 offset:2048\n"
    "v_mfma_f32_16x16x32_bf16 v[68:71], v[144:147], v[28:31], v[68:71]\n"
    "ds_read_b128 v[24:27], v94 offset:2048\n"
    "v_mfma_f32_16x16x32_bf16 v[88:91], v[224:227], v[28:31], v[88:91]\n"
    "s_waitcnt lgkmcnt(5)\n"
    "ds_read_b128 v[28:31], v94 offset:3072\n"
    "v_mfma_f32_16x16x32_bf16 v[64:67], a[244:247], v[32:35], v[64:67]\n"
    "ds_read_b128 v[12:15], v97 offset:3072\n"
    "v_mfma_f32_16x16x32_bf16 v[84:87], v[180:183], v[32:35], v[84:87]\n"
    "ds_read_b128 v[16:19], v97 offset:4096\n"
    "v_mfma_f32_16x16x32_bf16 v[68:71], a[244:247], v[36:39], v[68:71]\n"
    "ds_read_b128 v[32:35], v95 offset:2048\n"
    "v_mfma_f32_16x16x32_bf16 v[88:91], v[180:183], v[36:39], v[88:91]\n"
    "s_waitcnt lgkmcnt(6)\n"
    "ds_read_b128 v[36:39], v95 offset:3072\n"
    "v_mfma_f32_16x16x32_bf16 v[64:67], v[116:119], v[40:43], v[64:67]\n"
    "buffer_store_dwordx4 v[72:75], v104, s[0:3], s39 offen sc1\n"
    "s_waitcnt vmcnt(11)\n"
    "ds_read_b128 v[20:23], v97 offset:5120\n"
    "v_mfma_f32_16x16x32_bf16 v[84:87], v[196:199], v[40:43], v[84:87]\n"
    "s_barrier\n"
    "ds_read_b128 v[48:51], v97 offset:8192\n"
    "v_mfma_f32_16x16x32_bf16 v[68:71], v[116:119], v[44:47], v[68:71]\n"
    "ds_read_b128 v[40:43], v92 offset:4096\n"
    "v_mfma_f32_16x16x32_bf16 v[88:91], v[196:199], v[44:47], v[88:91]\n"
    "s_waitcnt lgkmcnt(7)\n"
    "ds_read_b128 v[44:47], v92 offset:5120\n"
    "s_waitcnt vmcnt(9)\n"
    "v_mfma_f32_16x16x32_bf16 v[64:67], v[132:135], v[24:27], v[64:67]\n"
    "s_memrealtime s[54:55]\n"
    "v_readfirstlane_b32 s32, v108\n"
    "v_readfirstlane_b32 s33, v109\n"
    ".Lvo_mkL5_%=:\n"
    "v_mfma_f32_16x16x32_bf16 v[84:87], v[212:215], v[24:27], v[84:87]\n"
    "ds_read_b128 v[52:55], v97 offset:9216\n"
    "v_mfma_f32_16x16x32_bf16 v[68:71], v[132:135], v[28:31], v[68:71]\n"
    "s_cmp_eq_u32 s33, s35\n"
    "s_cselect_b32 s46, s32, s46\n"
    "ds_read_b128 v[24:27], v93 offset:4096\n"
    "v_mfma_f32_16x16x32_bf16 v[88:91], v[212:215], v[28:31], v[88:91]\n"
    "s_waitcnt lgkmcnt(6)\n"
    "ds_read_b128 v[28:31], v93 offset:5120\n"
    "s_add_u32 s46, s46, 268\n"
    "v_mfma_f32_16x16x32_bf16 v[64:67], v[148:151], v[32:35], v[64:67]\n"
    "s_sub_u32 s47, s46, 59\n"
    "s_mov_b32 m0, s17\n"
    "ds_read_b128 v[56:59], v97 offset:10240\n"
    "v_mfma_f32_16x16x32_bf16 v[84:87], v[228:231], v[32:35], v[84:87]\n"
    "ds_read_b128 v[60:63], v97 offset:11264\n"
    ".Lvo_mkL6_%=:\n"
    "v_mfma_f32_16x16x32_bf16 v[68:71], v[148:151], v[36:39], v[68:71]\n"
    "ds_read_b128 v[32:35], v94 offset:4096\n"
    "s_sub_u32 s56, s47, s54\n"
    "v_mfma_f32_16x16x32_bf16 v[88:91], v[228:231], v[36:39], v[88:91]\n"
    "s_waitcnt lgkmcnt(6)\n"
    "ds_read_b128 v[36:39], v94 offset:5120\n"
    ".Lvo_mk6x11_%=:\n"
    "s_cmp_gt_i32 s56, 4\n"
    "s_cbranch_scc1 .Lvo_early_%=\n"
    "buffer_load_dwordx4 v96, s[0:3], s41 offen sc1 lds\n"
    "buffer_load_dwordx4 v96, s[0:3], s41 offen offset:1024 sc1 lds\n"
    ".Lvo_asked_%=:\n"
    ".Lvo_mk7x13_%=:\n"
    "v_mfma_f32_16x16x32_bf16 v[64:67], a[248:251], v[40:43], v[64:67]\n"
    "s_add_u32 s49, s34, 3\n"
    "s_lshl_b32 s43, s49, 19\n"
    "s_lshl_b32 s33, s49, 11\n"
    "v_mfma_f32_16x16x32_bf16 v[84:87], v[184:187], v[40:43], v[84:87]\n"
    "s_xor_b32 s33, s33, s13\n"
    "s_and_b32 s33, s33, 0x2000\n"
    "s_add_u32 s43, s43, s33\n"
    ".Lvo_mkM0_%=:\n"
    "v_mfma_f32_16x16x32_bf16 v[68:71], a[248:251], v[44:47], v[68:71]\n"
    "ds_read_b128 v[40:43], v95 offset:4096\n"
    "s_add_u32 s43, s43, s18\n"
    "v_mfma_f32_16x16x32_bf16 v[88:91], v[184:187], v[44:47], v[88:91]\n"
    "s_waitcnt lgkmcnt(5)\n"
    "ds_read_b128 v[44:47], v95 offset:5120\n"
    "s_add_u32 s49, s34, 2\n"
    "v_mfma_f32_16x16x32_bf16 v[64:67], v[120:123], v[24:27], v[64:67]\n"
    "s_lshl_b32 s44, s49, 20\n"
    "s_lshl_b32 s33, s49, 12\n"
    "s_xor_b32 s33, s33, s13\n"
    "v_mfma_f32_16x16x32_bf16 v[84:87], v[200:203], v[24:27], v[84:87]\n"
    "s_and_b32 s33, s33, 0x2000\n"
    "s_add_u32 s44, s44, s33\n"
    "s_add_u32 s44, s44, s20\n"
    "v_mfma_f32_16x16x32_bf16 v[68:71], v[120:123], v[28:31], v[68:71]\n"
    "ds_read_b128 v[24:27], v92 offset:6144\n"
    "s_add_u32 s38, s34, 2\n"
    "v_mfma_f32_16x16x32_bf16 v[88:91], v[200:203], v[28:31], v[88:91]\n"
    "s_waitcnt lgkmcnt(3)\n"
    "ds_read_b128 v[28:31], v92 offset:7168\n"
    "s_and_b32 s38, s38, 3\n"
    "v_mfma_f32_16x16x32_bf16 v[64:67], v[136:139], v[32:35], v[64:67]\n"
    "buffer_load_dwordx4 v96, s[0:3], s42 offen offset:2048 sc1 lds\n"
    "v_mfma_f32_16x16x32_bf16 v[84:87], v[216:219], v[32:35], v[84:87]\n"
    "s_lshl_b32 s38, s38, 22\n"
    "s_add_u32 s39, s38, 0x10000\n"
    "s_add_u32 s40, s22, s38\n"
    "v_mfma_f32_16x16x32_bf16 v[68:71], v[136:139], v[36:39], v[68:71]\n"
    "ds_read_b128 v[32:35], v93 offset:6144\n"
    "s_and_b32 s45, s34, 63\n"
    ".Lvo_mkM1_%=:\n"
    "v_mfma_f32_16x16x32_bf16 v[88:91], v[216:219], v[36:39], v[88:91]\n"
    "s_waitcnt lgkmcnt(3)\n"
    "ds_read_b128 v[36:39], v93 offset:7168\n"
    "s_lshl_b32 s45, s45, 7\n"
    "v_mfma_f32_16x16x32_bf16 v[64:67], v[152:155], v[40:43], v[64:67]\n"
    "s_add_u32 s45, s23, s45\n"
    "s_add_u32 s35, s34, 1\n"
    "v_mfma_f32_16x16x32_bf16 v[84:87], v[232:235], v[40:43], v[84:87]\n"
    "s_cmp_lt_i32 s34, 0\n"
    "s_cselect_b32 s35, -5, s35\n"
    "s_cselect_b32 s52, 0, -1\n"
    "v_mfma_f32_16x16x32_bf16 v[68:71], v[152:155], v[44:47], v[68:71]\n"
    "ds_read_b128 v[40:43], v94 offset:6144\n"
    "v_mfma_f32_16x16x32_bf16 v[88:91], v[232:235], v[44:47], v[88:91]\n"
    "s_waitcnt lgkmcnt(3)\n"
    "ds_read_b128 v[44:47], v94 offset:7168\n"
    "v_mfma_f32_16x16x32_bf16 v[64:67], a[252:255], v[24:27], v[64:67]\n"
    "buffer_load_dword v110, v105, s[0:3], 0 offen sc1\n"
    "v_mfma_f32_16x16x32_bf16 v[84:87], v[188:191], v[24:27], v[84:87]\n"
    "v_mfma_f32_16x16x32_bf16 v[68:71], a[252:255], v[28:31], v[68:71]\n"
    "ds_read_b128 v[24:27], v95 offset:6144\n"
    "v_mfma_f32_16x16x32_bf16 v[88:91], v[188:191], v[28:31], v[88:91]\n"
    "s_waitcnt lgkmcnt(3)\n"
    "ds_read_b128 v[28:31], v95 offset:7168\n"
    "v_mfma_f32_16x16x32_bf16 v[64:67], v[124:127], v[32:35], v[64:67]\n"
    ".Lvo_mkM2_%=:\n"
    "v_mfma_f32_16x16x32_bf16 v[84:87], v[204:207], v[32:35], v[84:87]\n"
    "v_mfma_f32_16x16x32_bf16 v[68:71], v[124:127], v[36:39], v[68:71]\n"
    "ds_read_b128 v[32:35], v96 offset:53248\n"
    "v_mfma_f32_16x16x32_bf16 v[88:91], v[204:207], v[36:39], v[88:91]\n"
    "s_waitcnt lgkmcnt(3)\n"
    "ds_read_b128 v[36:39], v96 offset:54272\n"
    "v_mfma_f32_16x16x32_bf16 v[64:67], v[140:143], v[40:43], v[64:67]\n"
    "v_mfma_f32_16x16x32_bf16 v[84:87], v[220:223], v[40:43], v[84:87]\n"
    "v_mfma_f32_16x16x32_bf16 v[68:71], v[140:143], v[44:47], v[68:71]\n"
    "ds_read_b128 v[40:43], v96 offset:55296\n"
    ".Lvo_mkM3_%=:\n"
    "v_mfma_f32_16x16x32_bf16 v[88:91], v[220:223], v[44:47], v[88:91]\n"
    "s_waitcnt lgkmcnt(3)\n"
    "ds_read_b128 v[44:47], v96 offset:56320\n"
    "v_mfma_f32_16x16x32_bf16 v[64:67], v[156:159], v[24:27], v[64:67]\n"
    "v_mfma_f32_16x16x32_bf16 v[84:87], v[236:239], v[24:27], v[84:87]\n"
    "v_mfma_f32_16x16x32_bf16 v[68:71], v[156:159], v[28:31], v[68:71]\n"
    "ds_read_b128 v[24:27], v96 offset:57344\n"
    "v_mfma_f32_16x16x32_bf16 v[88:91], v[236:239], v[28:31], v[88:91]\n"
    "s_waitcnt lgkmcnt(3)\n"
    "ds_read_b128 v[28:31], v96 offset:58368\n"
    "v_mfma_f32_16x16x32_bf16 v[64:67], v[160:163], v[32:35], v[64:67]\n"
    "v_mfma_f32_16x16x32_bf16 v[84:87], v[240:243], v[32:35], v[84:87]\n"
    "ds_read_b128 v[32:35], v96 offset:59392\n"
    "v_mfma_f32_16x16x32_bf16 v[68:71], v[160:163], v[36:39], v[68:71]\n"
    "v_mfma_f32_16x16x32_bf16 v[88:91], v[240:243], v[36:39], v[88:91]\n"
    "s_waitcnt lgkmcnt(3)\n"
    "ds_read_b128 v[36:39], v96 offset:60416\n"
    "v_mfma_f32_16x16x32_bf16 v[64:67], v[164:167], v[40:43], v[64:67]\n"
    "s_waitcnt lgkmcnt(2)\n"
    "v_mfma_f32_16x16x32_bf16 v[64:67], v[168:171], v[24:27], v[64:67]\n"
    "s_waitcnt lgkmcnt(1)\n"
    "v_mfma_f32_16x16x32_bf16 v[64:67], v[172:175], v[32:35], v[64:67]\n"
    ".Lvo_mkM4_%=:\n"
    "v_mfma_f32_16x16x32_bf16 v[84:87], v[244:247], v[40:43], v[84:87]\n"
    "s_branch .Lvo_take_%=\n"
    ".Lvo_tk0_%=:\n"
    ".Lvo_po3_%=:\n"
    "buffer_load_dword v109, v108, s[0:3], 0 offen sc1\n"
    "s_waitcnt vmcnt(0)\n"
    "v_cmp_gt_i32 vcc, 32, v109\n"
    "s_nop 0\n"
    "s_cbranch_vccz .Lvo_pd4_%=\n"
    "s_add_u32 s28, s28, 1\n"
    "s_cmp_lt_u32 s28, 0x40000\n"
    "s_cbranch_scc1 .Lvo_sp5_%=\n"
    "v_mov_b32 v109, 90\n"
    "s_mov_b32 s32, 33728\n"
    "buffer_store_dword v109, off, s[0:3], s32 sc1\n"
    "s_waitcnt vmcnt(0)\n"
    "s_branch .Lvo_pd4_%=\n"
    ".Lvo_sp5_%=:\n"
    "s_sleep 8\n"
    "s_branch .Lvo_po3_%=\n"
    ".Lvo_pd4_%=:\n"
    "s_branch .Lvo_tk1_%=\n"
    ".Lvo_early_%=:\n"
    ".Lvo_rt13_%=:\n"
    "s_memrealtime s[30:31]\n"
    "s_waitcnt lgkmcnt(0)\n"
    "s_sub_u32 s32, s47, s30\n"
    "s_cmp_lt_u32 s32, 600\n"
    "s_cbranch_scc1 .Lvo_rt13_%=\n"
    "buffer_load_dwordx4 v96, s[0:3], s41 offen sc1 lds\n"
    "buffer_load_dwordx4 v96, s[0:3], s41 offen offset:1024 sc1 lds\n"
    "s_branch .Lvo_asked_%=\n"
    ".Lvo_retry_%=:\n"
    "s_nop 7\n"
    "s_nop 7\n"
    "s_cmp_lt_i32 s34, 0\n"
    "s_cbranch_scc0 .Lvo_again_%=\n"
    "v_cvt_pk_bf16_f32 v44, v68, v69\n"
    "v_cvt_pk_bf16_f32 v45, v70, v71\n"
    "v_cvt_pk_bf16_f32 v46, v88, v89\n"
    "v_cvt_pk_bf16_f32 v47, v90, v91\n"
    "ds_write_b128 v92, v[44:47] offset:12288\n"
    "s_waitcnt vmcnt(0) lgkmcnt(0)\n"
    "s_barrier\n"
    "v_cmp_gt_i32 vcc, v111, v110\n"
    "s_nop 0\n"
    "s_cbranch_vccz .Lvo_e2_%=\n"
    ".Lvo_po15_%=:\n"
    "buffer_load_dword v110, v105, s[0:3], 0 offen sc1\n"
    "s_waitcnt vmcnt(0)\n"
    "v_cmp_gt_i32 vcc, v111, v110\n"
    "s_nop 0\n"
    "s_cbranch_vccz .Lvo_pd16_%=\n"
    "s_add_u32 s28, s28, 1\n"
    "s_cmp_lt_u32 s28, 0x40000\n"
    "s_cbranch_scc1 .Lvo_sp17_%=\n"
    "v_mov_b32 v110, 32\n"
    "s_mov_b32 s32, 33728\n"
    "buffer_store_dword v110, off, s[0:3], s32 sc1\n"
    "s_waitcnt vmcnt(0)\n"
    "s_branch .Lvo_pd16_%=\n"
    ".Lvo_sp17_%=:\n"
    "s_sleep 4\n"
    "s_branch .Lvo_po15_%=\n"
    ".Lvo_pd16_%=:\n"
    ".Lvo_e2_%=:\n"
    "s_mov_b32 m0, s21\n"
    "s_nop 0\n"
    "buffer_load_dwordx4 v96, s[0:3], s44 offen sc1 lds\n"
    "s_sub_u32 m0, s17, 8192\n"
    "s_nop 0\n"
    "buffer_load_dwordx4 v96, s[0:3], s43 offen offset:0 sc1 lds\n"
    "buffer_load_dwordx4 v96, s[0:3], s43 offen offset:1024 sc1 lds\n"
    "s_add_u32 s49, s34, 3\n"
    "v_lshlrev_b32 v108, 4, v96\n"
    "v_mov_b32 v109, s49\n"
    "s_mov_b64 exec, 15\n"
    "buffer_store_dword v109, v108, s[0:3], s25 offen sc1\n"
    "s_mov_b64 exec, -1\n"
    "ds_read_b128 v[0:3], v96 offset:11264\n"
    "ds_read_b128 v[4:7], v96 offset:12288\n"
    "ds_read_b128 v[8:11], v96 offset:24576\n"
    "ds_read_b128 v[12:15], v96 offset:25600\n"
    "ds_read_b128 v[16:19], v96 offset:37888\n"
    "ds_read_b128 v[20:23], v96 offset:38912\n"
    "ds_read_b128 v[24:27], v96 offset:51200\n"
    "ds_read_b128 v[28:31], v96 offset:52224\n"
    "s_waitcnt vmcnt(3) lgkmcnt(0)\n"
    "s_barrier\n"
    "ds_read2_b64 v[32:35], v106 offset0:0 offset1:128\n"
    "ds_read2_b64 v[36:39], v106 offset0:1 offset1:129\n"
    "v_mfma_f32_16x16x32_bf16 v[40:43], v[0:3], a[0:3], 0\n"
    "v_mfma_f32_16x16x32_bf16 v[44:47], v[4:7], a[0:3], 0\n"
    "v_mfma_f32_16x16x32_bf16 v[40:43], v[8:11], a[4:7], v[40:43]\n"
    "v_mfma_f32_16x16x32_bf16 v[44:47], v[12:15], a[4:7], v[44:47]\n"
    "v_mfma_f32_16x16x32_bf16 v[40:43], v[16:19], a[8:11], v[40:43]\n"
    "v_mfma_f32_16x16x32_bf16 v[44:47], v[20:23], a[8:11], v[44:47]\n"
    "v_mfma_f32_16x16x32_bf16 v[40:43], v[24:27], a[12:15], v[40:43]\n"
    "v_mfma_f32_16x16x32_bf16 v[44:47], v[28:31], a[12:15], v[44:47]\n"
    "s_waitcnt lgkmcnt(0)\n"
    "s_mov_b32 s37, 2130706432\n"
    "s_mov_b32 s36, 2130706432\n"
    "s_nop 7\n"
    "s_branch .Lvo_uph_%=\n"
    ".Lvo_e0_%=:\n"
    ".Lvo_po18_%=:\n"
    "buffer_load_dword v109, v108, s[0:3], s24 offen sc1\n"
    "s_waitcnt vmcnt(0)\n"
    "v_cmp_gt_i32 vcc, 1, v109\n"
    "s_nop 0\n"
    "s_cbranch_vccz .Lvo_pd19_%=\n"
    "s_add_u32 s28, s28, 1\n"
    "s_cmp_lt_u32 s28, 0x40000\n"
    "s_cbranch_scc1 .Lvo_sp20_%=\n"
    "v_mov_b32 v109, 30\n"
    "s_mov_b32 s32, 33728\n"
    "buffer_store_dword v109, off, s[0:3], s32 sc1\n"
    "s_waitcnt vmcnt(0)\n"
    "s_branch .Lvo_pd19_%=\n"
    ".Lvo_sp20_%=:\n"
    "s_sleep 4\n"
    "s_branch .Lvo_po18_%=\n"
    ".Lvo_pd19_%=:\n"
    ".Lvo_po21_%=:\n"
    "buffer_load_dword v110, v105, s[0:3], 0 offen sc1\n"
    "s_waitcnt vmcnt(0)\n"
    "v_cmp_gt_i32 vcc, v111, v110\n"
    "s_nop 0\n"
    "s_cbranch_vccz .Lvo_pd22_%=\n"
    "s_add_u32 s28, s28, 1\n"
    "s_cmp_lt_u32 s28, 0x40000\n"
    "s_cbranch_scc1 .Lvo_sp23_%=\n"
    "v_mov_b32 v110, 31\n"
    "s_mov_b32 s32, 33728\n"
    "buffer_store_dword v110, off, s[0:3], s32 sc1\n"
    "s_waitcnt vmcnt(0)\n"
    "s_branch .Lvo_pd22_%=\n"
    ".Lvo_sp23_%=:\n"
    "s_sleep 4\n"
    "s_branch .Lvo_po21_%=\n"
    ".Lvo_pd22_%=:\n"
    "s_branch .Lvo_e1_%=\n"
    ".Lvo_again_%=:\n"
    "s_add_u32 s28, s28, 1\n"
    "s_cmp_lt_u32 s28, 0x40000\n"
    "s_cbranch_scc1 .Lvo_sp24_%=\n"
    "v_mov_b32 v108, 2\n"
    "s_mov_b32 s32, 33728\n"
    "buffer_store_dword v108, off, s[0:3], s32 sc1\n"
    "s_waitcnt vmcnt(0)\n"
    "s_branch .Lvo_go_%=\n"
    ".Lvo_sp24_%=:\n"
    "buffer_load_dwordx4 v96, s[0:3], s41 offen sc1 lds\n"
    "buffer_load_dwordx4 v96, s[0:3], s41 offen offset:1024 sc1 lds\n"
    "buffer_load_dwordx4 v96, s[0:3], s42 offen offset:2048 sc1 lds\n"
    "s_waitcnt vmcnt(0)\n"
    "ds_read_b128 v[72:75], v92 offset:8192\n"
    "ds_read_b128 v[76:79], v92 offset:9216\n"
    "s_waitcnt lgkmcnt(0)\n"
    "v_mov_b64 v[64:65], 0\n"
    "v_mov_b64 v[66:67], 0\n"
    "v_max_u32 v108, v72, v74\n"
    "v_max3_u32 v108, v108, v76, v78\n"
    "v_cmp_le_u32 vcc, s52, v108\n"
    "s_nop 0\n"
    "s_cbranch_vccnz .Lvo_again_%=\n"
    "v_smfmac_f32_16x16x64_bf16 v[64:67], v[72:75], a[240:247], v98\n"
    "s_branch .Lvo_go_%=\n"
    ".Lvo_r2_%=:\n"
    "s_add_u32 s28, s28, 1\n"
    "s_cmp_lt_u32 s28, 0x40000\n"
    "s_cbranch_scc1 .Lvo_sp25_%=\n"
    "v_mov_b32 v108, 6\n"
    "s_mov_b32 s32, 33728\n"
    "buffer_store_dword v108, off, s[0:3], s32 sc1\n"
    "s_waitcnt vmcnt(0)\n"
    "s_branch .Lvo_go2_%=\n"
    ".Lvo_sp25_%=:\n"
    "buffer_load_dwordx4 v96, s[0:3], s42 offen offset:2048 sc1 lds\n"
    "s_waitcnt vmcnt(0)\n"
    "ds_read_b128 v[80:83], v92 offset:10240\n"
    "s_waitcnt lgkmcnt(0)\n"
    "v_max_u32 v108, v80, v82\n"
    "v_cmp_eq_u32 vcc, -1, v108\n"
    "s_nop 0\n"
    "s_cbranch_vccnz .Lvo_r2_%=\n"
    "s_branch .Lvo_go2_%=\n"
    ".Lvo_wf_%=:\n"
    ".Lvo_po26_%=:\n"
    "buffer_load_dword v110, v105, s[0:3], 0 offen sc1\n"
    "s_waitcnt vmcnt(0)\n"
    "v_cmp_gt_i32 vcc, v111, v110\n"
    "s_nop 0\n"
    "s_cbranch_vccz .Lvo_pd27_%=\n"
    "s_add_u32 s28, s28, 1\n"
    "s_cmp_lt_u32 s28, 0x40000\n"
    "s_cbranch_scc1 .Lvo_sp28_%=\n"
    "v_mov_b32 v110, 33\n"
    "s_mov_b32 s32, 33728\n"
    "buffer_store_dword v110, off, s[0:3], s32 sc1\n"
    "s_waitcnt vmcnt(0)\n"
    "s_branch .Lvo_pd27_%=\n"
    ".Lvo_sp28_%=:\n"
    "s_sleep 4\n"
    "s_branch .Lvo_po26_%=\n"
    ".Lvo_pd27_%=:\n"
    "s_branch .Lvo_wfb_%=\n"
    ".Lvo_end_%=:\n"
    "s_waitcnt vmcnt(0) lgkmcnt(0)\n"
    "s_endpgm\n"
    :: "{s[0:3]}"(s0_), "{s[4:7]}"(s4_), "{s[8:11]}"(s8_), "{s[12:15]}"(s12_), "{s[16:19]}"(s16_), "{s[20:23]}"(s20_), "{s[24:27]}"(s24_), "{v[0:3]}"(v0_), "{v[4:7]}"(v4_), "{v[8:11]}"(v8_), "{v[12:15]}"(v12_), "{v[16:19]}"(v16_), "{v[92:95]}"(v92_), "{v[96:99]}"(v96_), "{v[100:103]}"(v100_), "{v[104:107]}"(v104_) : "memory", "v255", "a255", "s95");
    __builtin_unreachable();
}
DEV void ct_role(const MegaArgs& A, const int L, const int u, char* smem) {
    ROLE_HEAD;
    const int r5 = l / 5, q5 = l - 5 * r5, c0 = q5 < 4 ? 16 * u + 4 * q5 : 512 + 4 * u, tk = 8 * w + r5;
    u32x2 m0;
    {
        const int sg = 2 * u + L, rt = w & 1, j0 = 10 * (w >> 1);
        float* ex = (float*)(smem + 1024);
        u32x4 xa[10];
        const unsigned xo = ((32 * sg + 16 * rt + l16) * D_ + 32 * j0 + 8 * lq) * 2;
#pragma unroll
        for (int j = 0; j < 10; ++j) asm volatile("global_load_dwordx4 %0, %1, %2 offset:%3" : "=&v"(xa[j]) : "v"(xo), "s"(A.x0), "n"(64 * j) : "memory");
        asm volatile("global_load_dwordx2 %0, %1, %2" : "=&v"(m0) : "v"(((r5 & 7) * D_ + c0) * 2), "s"(A.mem0) : "memory");
        asm volatile("s_waitcnt vmcnt(0)" ::: "memory");
#pragma unroll
        for (int j = 0; j < 10; ++j) asm volatile("" : "+v"(xa[j]));
        asm volatile("" : "+v"(m0));
        float s = 0.f, q = 0.f;
#pragma unroll
        for (int j = 0; j < 10; ++j)
#pragma unroll
            for (int e = 0; e < 4; ++e) s += lo16(xa[j][e]) + hi16(xa[j][e]);
        s += __shfl_xor(s, 16); s += __shfl_xor(s, 32);
        ex[threadIdx.x] = s;
        asm volatile("s_waitcnt lgkmcnt(0)\n\ts_barrier" ::: "memory");
        const float mean = (ex[64 * rt + l16] + ex[128 + 64 * rt + l16]) * (1.0f / D_);
#pragma unroll
        for (int j = 0; j < 10; ++j)
#pragma unroll
            for (int e = 0; e < 4; ++e) { const float d = lo16(xa[j][e]) - mean, d2 = hi16(xa[j][e]) - mean; q += d * d; q += d2 * d2; }
        q += __shfl_xor(q, 16); q += __shfl_xor(q, 32);
        ex[256 + threadIdx.x] = q;
        asm volatile("s_waitcnt lgkmcnt(0)\n\ts_barrier" ::: "memory");
        q = ex[256 + 64 * rt + l16] + ex[384 + 64 * rt + l16];
        const float mb = rb(mean), rd = __builtin_amdgcn_rcpf(rb(sqrtf(rb(rb(q * (1.0f / D_)) + 1e-5f))));
#pragma unroll
        for (int j = 0; j < 10; ++j) {
            u32x4 o;
#pragma unroll
            for (int e = 0; e < 4; ++e) { const unsigned d = pk2(lo16(xa[j][e]) - mb, hi16(xa[j][e]) - mb); o[sw2(e)] = pk2(lo16(d) * rd, hi16(d) * rd); }
            bst16(rr, rec(sg, 0) + ((j0 + j) >> 2) * 16384 + ((j0 + j) & 3) * 2048 + rt * 1024 + l * 16, o);
        }
    }
#define XM 5
    const unsigned mpos = l < 40 && q5 < XM ? xl(RG_MEM + w * 16384 + (c0 >> 6) * 1024 + (((c0 >> 4) & 3) * 16 + 4 * (r5 >> 1) + ((c0 >> 1) & 2) + (r5 & 1)) * 16 + (c0 & 8), L) : OOB;
    const unsigned xpos = l < 40 ? (c0 >> 7) * 16384 + ((c0 >> 5) & 3) * 2048 + (tk >> 4) * 1024 + (((c0 >> 3) & 3) * 16 + (tk & 15)) * 16 + (c0 & 4) * 2 : OOB;
    const unsigned rpos = (L || l >= 40) ? xpos + RC_X2 : tk * 1280 + c0 * 2;
    const u32x4 rs = L ? rx : u32x4{lo(A.x0), hi(A.x0), 2048u * 1280u, 0x20000u};
#pragma unroll
    for (int k = 0; k < 4; ++k) bst16(rr, rec(2 * u + (k >> 1), L) + RC_X1 + 65536 + w * 2048 + (k & 1) * 1024 + l * 16, ones);
    bst8(rr, mpos, u32x2{nosent(m0[0]), m0[1]}); bst8(rr, mpos + SLOT, ones2); bst8(rr, mpos + 2 * SLOT, ones2);
    wait_drawn(A.ar);
    DRAIN_BARRIER();
    bst4(rr, threadIdx.x < 16 ? xl(Z_CT + (l & 7) * 256 + (32 * L + u) * 4, l >> 3) : OOB, 1u);
    if (!((A.layers >> L) & 1)) return;
    const u32x4 s0_ = {RFL(rx[0]), RFL(rx[1]), RFL(rx[2]), RFL(rx[3])};
    const u32x4 s4_ = {RFL(rs[0]), RFL(rs[1]), RFL(rs[2]), RFL(rs[3])};
    const u32x4 s8_ = {RFL(L << 13), RFL(REC0), RFL(xl(RG_UT + (u * 4 + w) * 8192, L)), RFL(xl(RG_UTX + (u * 4 + w) * 2048, L))};
    const u32x4 s12_ = {RFL(xl(RG_UM + (u * 4 + w) * 2048, L)), RFL(xl(RG_UMX + (u * 4 + w) * 512, L)), RFL(xl(RG_PT + w * 16384, L)), RFL(xl(RG_PM + w * 8192, L))};
    const u32x4 s16_ = {RFL(xl(Z_X + 16384 + (u & 1) * 8192, L)), RFL(xl(Z_QV + (u & 3) * 256, L)), RFL(DBG0 + ((L * 128 + 64 + u) * 4 + w) * 2048), RFL(DBG0 + (2 << 20) + ((L * 32 + u) * 4 + w) * 8192)};
    const u32x4 v192_ = {(unsigned)(l * 16), (unsigned)(l * 16 + 4096), (unsigned)((l & 2) ? 0xeeeeu : 0x4444u), (unsigned)(0x4444u)};
    const u32x4 v196_ = {(unsigned)((l16 >> 2) * 256 + lq * 64 + (l & 3) * 16), (unsigned)(lq * 64 + (l & 3) * 16), (unsigned)((lq >> 1) * 2048 + 1024 + (lq & 1) * 256 + l16 * 16), (unsigned)(lq * 2048 + 1536 + l16 * 16)};
    const u32x4 v200_ = {(unsigned)(0 + w * 640 + lq * 160 + l16 * 4), (unsigned)(l16 < 4 ? 0 + w * 640 + lq * 160 + 64 + l16 * 4 : 12800), (unsigned)(0 + (l < 40 ? l : 0) * 16), (unsigned)(mpos)};
    const u32x4 v204_ = {(unsigned)(2560 + w * 2560 + lq * 320 + l16 * 4), (unsigned)(l16 < 4 ? 2560 + w * 2560 + lq * 320 + 64 + l16 * 4 : 12800), (unsigned)(3840 + w * 2560 + lq * 320 + l16 * 4), (unsigned)(l16 < 4 ? 3840 + w * 2560 + lq * 320 + 64 + l16 * 4 : 12800)};
    const u32x4 v208_ = {(unsigned)(2560 + w * 640 + (l < 40 ? l : 0) * 16), (unsigned)(xpos + RC_X1), (unsigned)(rpos), (unsigned)(l < 4 && w == 1 ? xl(Z_X1 + l * 4096 + u * 128, L) : OOB)};
    const u32x4 v212_ = {(unsigned)(l < 16 ? ((u >> 2) & 1) * 16384 + (u & 3) * 2048 + l * 128 : OOB), (unsigned)(0u), (unsigned)(0u), (unsigned)(0u)};
    asm volatile(
    "s_mov_b32 s20, 0\n"
    "s_mov_b64 s[22:23], 0\n"
    "s_mov_b32 s25, 0\n"
    "s_mov_b32 s28, -2\n"
    "v_lshrrev_b32 v4, 2, v192\n"
    "v_lshrrev_b32 v5, 9, v192\n"
    "v_add_u32 v5, 1, v5\n"
    ".Lct_po1_%=:\n"
    "buffer_load_dword v8, v4, s[0:3], s17 offen sc1\n"
    "s_waitcnt vmcnt(0)\n"
    "v_cmp_gt_i32 vcc, v5, v8\n"
    "s_nop 0\n"
    "s_cbranch_vccz .Lct_pd2_%=\n"
    "s_add_u32 s20, s20, 1\n"
    "s_cmp_lt_u32 s20, 0x40000\n"
    "s_cbranch_scc1 .Lct_sp3_%=\n"
    "v_mov_b32 v8, 40\n"
    "s_mov_b32 s24, 33728\n"
    "buffer_store_dword v8, off, s[0:3], s24 sc1\n"
    "s_waitcnt vmcnt(0)\n"
    "s_branch .Lct_pd2_%=\n"
    ".Lct_sp3_%=:\n"
    "s_sleep 16\n"
    "s_branch .Lct_po1_%=\n"
    ".Lct_pd2_%=:\n"
    "s_memrealtime s[22:23]\n"
    "s_waitcnt lgkmcnt(0)\n"
    "s_sub_u32 s26, s22, 804\n"
    "s_mov_b32 s28, -1\n"
    "s_add_u32 s28, s28, 1\n"
    "s_add_u32 s27, s28, 1\n"
    "s_add_u32 s29, s28, 0\n"
    "s_and_b32 s29, s29, 3\n"
    "s_lshl_b32 s29, s29, 22\n"
    "s_add_u32 s30, s28, 1\n"
    "s_and_b32 s30, s30, 3\n"
    "s_lshl_b32 s30, s30, 22\n"
    "s_add_u32 s31, s28, 3\n"
    "s_and_b32 s31, s31, 3\n"
    "s_lshl_b32 s31, s31, 22\n"
    "s_and_b32 s41, s28, 63\n"
    "s_lshl_b32 s41, s41, 7\n"
    "s_add_u32 s32, s12, s29\n"
    "s_add_u32 s33, s13, s29\n"
    "s_add_u32 s34, s15, s29\n"
    "s_add_u32 s35, s14, s29\n"
    "s_add_u32 s39, s10, s30\n"
    "s_add_u32 s40, s11, s30\n"
    "s_add_u32 s41, s16, s41\n"
    "s_add_u32 s36, s35, 4096\n"
    "s_add_u32 s37, s35, 16384\n"
    "s_add_u32 s38, s35, 20480\n"
    "s_add_u32 s43, s26, 304\n"
    "s_lshl_b32 s45, s28, 19\n"
    "s_lshl_b32 s25, s28, 11\n"
    "s_xor_b32 s25, s25, s8\n"
    "s_and_b32 s25, s25, 0x2000\n"
    "s_add_u32 s45, s45, s25\n"
    "s_add_u32 s45, s45, s9\n"
    "s_mul_i32 s46, s28, 40960\n"
    "s_cmp_eq_u32 s8, 0\n"
    "s_cselect_b32 s46, s46, s45\n"
    "v_mov_b64 v[76:77], 0\n"
    "v_mov_b64 v[78:79], 0\n"
    "v_mov_b64 v[80:81], 0\n"
    "v_mov_b64 v[82:83], 0\n"
    "s_mov_b32 s39, s10\n"
    "s_mov_b32 s40, s11\n"
    "buffer_load_dwordx4 a[0:3], v192, s[0:3], s39 offen offset:0 sc1\n"
    "buffer_load_dwordx4 a[4:7], v192, s[0:3], s39 offen offset:1024 sc1\n"
    "buffer_load_dwordx4 a[8:11], v192, s[0:3], s39 offen offset:2048 sc1\n"
    "buffer_load_dwordx4 a[12:15], v192, s[0:3], s39 offen offset:3072 sc1\n"
    "buffer_load_dwordx4 a[16:19], v193, s[0:3], s39 offen offset:0 sc1\n"
    "buffer_load_dwordx4 a[20:23], v193, s[0:3], s39 offen offset:1024 sc1\n"
    "buffer_load_dwordx4 a[24:27], v193, s[0:3], s39 offen offset:2048 sc1\n"
    "buffer_load_dwordx4 a[28:31], v193, s[0:3], s39 offen offset:3072 sc1\n"
    "buffer_load_dwordx4 v[0:3], v196, s[0:3], s40 offen offset:0 sc1\n"
    "buffer_load_dwordx4 v[4:7], v196, s[0:3], s40 offen offset:1024 sc1\n"
    "s_add_u32 s39, s10, s30\n"
    "s_add_u32 s40, s11, s30\n"
    "buffer_load_dwordx2 v[190:191], off, s[0:3], s41 sc1\n"
    "s_waitcnt vmcnt(0)\n"
    ".Lct_top_%=:\n"
    "s_waitcnt vmcnt(2)\n"
    "v_mov_b64 v[8:9], v[0:1]\n"
    "v_mov_b64 v[10:11], v[2:3]\n"
    "v_mov_b64 v[12:13], v[4:5]\n"
    "v_mov_b64 v[14:15], v[6:7]\n"
    "v_mov_b32_dpp v16, v0 row_shl:4 row_mask:0xf bank_mask:0xf bound_ctrl:1\n"
    "v_mov_b32_dpp v17, v1 row_shl:4 row_mask:0xf bank_mask:0xf bound_ctrl:1\n"
    "v_mov_b32_dpp v18, v2 row_shl:4 row_mask:0xf bank_mask:0xf bound_ctrl:1\n"
    "v_mov_b32_dpp v19, v3 row_shl:4 row_mask:0xf bank_mask:0xf bound_ctrl:1\n"
    "v_mov_b32_dpp v20, v4 row_shl:4 row_mask:0xf bank_mask:0xf bound_ctrl:1\n"
    "v_mov_b32_dpp v21, v5 row_shl:4 row_mask:0xf bank_mask:0xf bound_ctrl:1\n"
    "v_mov_b32_dpp v22, v6 row_shl:4 row_mask:0xf bank_mask:0xf bound_ctrl:1\n"
    "v_mov_b32_dpp v23, v7 row_shl:4 row_mask:0xf bank_mask:0xf bound_ctrl:1\n"
    "v_mov_b32_dpp v24, v0 row_shl:8 row_mask:0xf bank_mask:0xf bound_ctrl:1\n"
    "v_mov_b32_dpp v25, v1 row_shl:8 row_mask:0xf bank_mask:0xf bound_ctrl:1\n"
    "v_mov_b32_dpp v26, v2 row_shl:8 row_mask:0xf bank_mask:0xf bound_ctrl:1\n"
    "v_mov_b32_dpp v27, v3 row_shl:8 row_mask:0xf bank_mask:0xf bound_ctrl:1\n"
    "v_mov_b32_dpp v28, v4 row_shl:8 row_mask:0xf bank_mask:0xf bound_ctrl:1\n"
    "v_mov_b32_dpp v29, v5 row_shl:8 row_mask:0xf bank_mask:0xf bound_ctrl:1\n"
    "v_mov_b32_dpp v30, v6 row_shl:8 row_mask:0xf bank_mask:0xf bound_ctrl:1\n"
    "v_mov_b32_dpp v31, v7 row_shl:8 row_mask:0xf bank_mask:0xf bound_ctrl:1\n"
    "v_mov_b32_dpp v32, v0 row_shl:12 row_mask:0xf bank_mask:0xf bound_ctrl:1\n"
    "v_mov_b32_dpp v33, v1 row_shl:12 row_mask:0xf bank_mask:0xf bound_ctrl:1\n"
    "v_mov_b32_dpp v34, v2 row_shl:12 row_mask:0xf bank_mask:0xf bound_ctrl:1\n"
    "v_mov_b32_dpp v35, v3 row_shl:12 row_mask:0xf bank_mask:0xf bound_ctrl:1\n"
    "v_mov_b32_dpp v36, v4 row_shl:12 row_mask:0xf bank_mask:0xf bound_ctrl:1\n"
    "v_mov_b32_dpp v37, v5 row_shl:12 row_mask:0xf bank_mask:0xf bound_ctrl:1\n"
    "v_mov_b32_dpp v38, v6 row_shl:12 row_mask:0xf bank_mask:0xf bound_ctrl:1\n"
    "v_mov_b32_dpp v39, v7 row_shl:12 row_mask:0xf bank_mask:0xf bound_ctrl:1\n"
    "s_waitcnt vmcnt(1)\n"
    ".Lct_x0_%=:\n"
    "v_readfirstlane_b32 s25, v191\n"
    "v_readfirstlane_b32 s24, v190\n"
    "s_cmp_eq_u32 s25, s27\n"
    "s_cselect_b32 s26, s24, s26\n"
    "s_cbranch_scc0 .Lct_xr0_%=\n"
    ".Lct_xg0_%=:\n"
    "s_add_u32 s43, s26, 97\n"
    "s_add_u32 s44, s26, 109\n"
    ".Lct_mk0x3_%=:\n"
    ".Lct_rt5_%=:\n"
    "s_memrealtime s[22:23]\n"
    "s_waitcnt lgkmcnt(0)\n"
    "s_sub_u32 s24, s43, s22\n"
    "s_cmp_lt_u32 s24, 600\n"
    "s_cbranch_scc1 .Lct_rt5_%=\n"
    "buffer_load_dwordx4 v[40:43], v192, s[0:3], s32 offen offset:0 sc1\n"
    "buffer_load_dwordx4 v[44:47], v192, s[0:3], s32 offen offset:1024 sc1\n"
    "buffer_load_dwordx4 v[48:51], v197, s[0:3], s33 offen offset:0 sc1\n"
    "buffer_load_dwordx4 v[52:55], v197, s[0:3], s33 offen offset:256 sc1\n"
    "v_mov_b32 v148, s28\n"
    "v_mov_b32 v149, s28\n"
    "v_mov_b32 v150, s28\n"
    "v_mov_b32 v151, s28\n"
    "s_waitcnt vmcnt(4)\n"
    "s_barrier\n"
    "buffer_store_dwordx4 v[148:151], v211, s[0:3], 0 offen sc1\n"
    ".Lct_rt6_%=:\n"
    "s_memrealtime s[22:23]\n"
    "s_waitcnt lgkmcnt(0)\n"
    "s_sub_u32 s24, s44, s22\n"
    "s_cmp_lt_u32 s24, 600\n"
    "s_cbranch_scc1 .Lct_rt6_%=\n"
    "buffer_load_dwordx4 v[72:75], v193, s[0:3], s34 offen sc1\n"
    "buffer_load_dwordx4 v[56:59], v192, s[0:3], s34 offen offset:0 sc1\n"
    "buffer_load_dwordx4 v[60:63], v192, s[0:3], s34 offen offset:1024 sc1\n"
    "buffer_load_dwordx4 v[64:67], v192, s[0:3], s34 offen offset:2048 sc1\n"
    "buffer_load_dwordx4 v[68:71], v192, s[0:3], s34 offen offset:3072 sc1\n"
    ".Lct_mk1x6_%=:\n"
    ".Lct_take0_%=:\n"
    "s_waitcnt vmcnt(6)\n"
    "v_max_u32 v184, v40, v44\n"
    "v_max3_u32 v184, v48, v50, v184\n"
    "v_max3_u32 v184, v52, v54, v184\n"
    "s_waitcnt vmcnt(4)\n"
    "v_smfmac_f32_16x16x64_bf16 v[76:79], v[72:75], v[40:47], v194\n"
    "v_max3_u32 v184, v72, v74, v184\n"
    "v_smfmac_f32_16x16x64_bf16 v[80:83], v[72:75], v[48:55], v194\n"
    "s_waitcnt vmcnt(3)\n"
    "v_smfmac_f32_16x16x64_bf16 v[80:83], v[56:59], v[8:15], v194\n"
    "v_max3_u32 v184, v56, v58, v184\n"
    "v_smfmac_f32_16x16x64_bf16 v[76:79], v[56:59], a[0:7], v194\n"
    "s_waitcnt vmcnt(2)\n"
    "v_smfmac_f32_16x16x64_bf16 v[80:83], v[60:63], v[16:23], v194\n"
    "v_max3_u32 v184, v60, v62, v184\n"
    "v_smfmac_f32_16x16x64_bf16 v[76:79], v[60:63], a[8:15], v194\n"
    "s_waitcnt vmcnt(1)\n"
    "v_smfmac_f32_16x16x64_bf16 v[80:83], v[64:67], v[24:31], v194\n"
    "v_max3_u32 v184, v64, v66, v184\n"
    "v_smfmac_f32_16x16x64_bf16 v[76:79], v[64:67], a[16:23], v194\n"
    "s_waitcnt vmcnt(0)\n"
    "v_smfmac_f32_16x16x64_bf16 v[80:83], v[68:71], v[32:39], v194\n"
    "v_max3_u32 v184, v68, v70, v184\n"
    "v_smfmac_f32_16x16x64_bf16 v[76:79], v[68:71], a[24:31], v194\n"
    ".Lct_mk2x7_%=:\n"
    "s_nop 1\n"
    "v_cmp_eq_u32 vcc, -1, v184\n"
    "s_nop 1\n"
    "v_add_f32 v80, v80, v82\n"
    "v_add_f32 v81, v81, v83\n"
    "s_nop 0\n"
    "v_pk_add_f32 v[76:77], v[76:77], v[78:79]\n"
    "ds_write2_b32 v201, v80, v81 offset1:20\n"
    "ds_write2_b32 v200, v76, v77 offset1:20\n"
    "s_cbranch_vccnz .Lct_retry0_%=\n"
    ".Lct_go0_%=:\n"
    "s_waitcnt lgkmcnt(0)\n"
    "s_barrier\n"
    "ds_read_b128 v[164:167], v202 offset:0\n"
    "ds_read_b128 v[168:171], v202 offset:640\n"
    "ds_read_b128 v[172:175], v202 offset:1280\n"
    "ds_read_b128 v[176:179], v202 offset:1920\n"
    "s_waitcnt lgkmcnt(2)\n"
    "v_pk_add_f32 v[164:165], v[164:165], v[168:169]\n"
    "v_pk_add_f32 v[166:167], v[166:167], v[170:171]\n"
    "s_waitcnt lgkmcnt(1)\n"
    "v_pk_add_f32 v[164:165], v[164:165], v[172:173]\n"
    "v_pk_add_f32 v[166:167], v[166:167], v[174:175]\n"
    "s_waitcnt lgkmcnt(0)\n"
    "v_pk_add_f32 v[164:165], v[164:165], v[176:177]\n"
    "v_pk_add_f32 v[166:167], v[166:167], v[178:179]\n"
    "v_cvt_pk_bf16_f32 v164, v164, v165\n"
    "v_cvt_pk_bf16_f32 v165, v166, v167\n"
    "v_min_u32 v164, -2, v164\n"
    "buffer_store_dwordx2 v[164:165], v203, s[0:3], s30 offen sc1\n"
    ".Lct_mk3x8_%=:\n"
    "s_sleep 2\n"
    "v_mov_b64 v[166:167], -1\n"
    "buffer_load_dwordx4 v[132:135], v199, s[0:3], s35 offen offset:0 sc1\n"
    "buffer_load_dwordx4 v[136:139], v199, s[0:3], s37 offen offset:0 sc1\n"
    "buffer_load_dwordx4 v[140:143], v199, s[0:3], s35 offen offset:256 sc1\n"
    "buffer_load_dwordx4 v[144:147], v199, s[0:3], s37 offen offset:256 sc1\n"
    "buffer_load_dwordx4 v[116:119], v198, s[0:3], s35 offen sc1\n"
    "buffer_load_dwordx4 v[84:87], v192, s[0:3], s35 offen offset:0 sc1\n"
    "buffer_load_dwordx4 v[88:91], v192, s[0:3], s35 offen offset:2048 sc1\n"
    "buffer_load_dwordx4 v[120:123], v198, s[0:3], s36 offen sc1\n"
    "buffer_load_dwordx4 v[92:95], v192, s[0:3], s36 offen offset:0 sc1\n"
    "buffer_load_dwordx4 v[96:99], v192, s[0:3], s36 offen offset:2048 sc1\n"
    ".Lct_mk7x9_%=:\n"
    "buffer_load_dwordx4 v[124:127], v198, s[0:3], s37 offen sc1\n"
    "s_waitcnt vmcnt(10)\n"
    "v_max3_u32 v184, v132, v134, v132\n"
    "v_swap_b32 v133, v134\n"
    "s_nop 1\n"
    "v_mfma_f32_16x16x32_bf16 v[148:151], v[132:135], v[40:43], 0\n"
    "v_mfma_f32_16x16x32_bf16 v[152:155], v[132:135], v[48:51], 0\n"
    "buffer_load_dwordx4 v[100:103], v192, s[0:3], s37 offen offset:0 sc1\n"
    "s_waitcnt vmcnt(10)\n"
    "v_max3_u32 v184, v136, v138, v184\n"
    "v_swap_b32 v137, v138\n"
    "s_nop 1\n"
    "v_mfma_f32_16x16x32_bf16 v[148:151], v[136:139], v[44:47], v[148:151]\n"
    "v_mfma_f32_16x16x32_bf16 v[152:155], v[136:139], v[52:55], v[152:155]\n"
    "buffer_load_dwordx4 v[104:107], v192, s[0:3], s37 offen offset:2048 sc1\n"
    "s_waitcnt vmcnt(10)\n"
    "v_max3_u32 v184, v140, v142, v184\n"
    "v_swap_b32 v141, v142\n"
    "s_nop 1\n"
    "v_mfma_f32_16x16x32_bf16 v[156:159], v[140:143], v[40:43], 0\n"
    "v_mfma_f32_16x16x32_bf16 v[160:163], v[140:143], v[48:51], 0\n"
    "buffer_load_dwordx4 v[128:131], v198, s[0:3], s38 offen sc1\n"
    "s_waitcnt vmcnt(10)\n"
    "v_max3_u32 v184, v144, v146, v184\n"
    "v_swap_b32 v145, v146\n"
    "s_nop 1\n"
    "v_mfma_f32_16x16x32_bf16 v[156:159], v[144:147], v[44:47], v[156:159]\n"
    "v_mfma_f32_16x16x32_bf16 v[160:163], v[144:147], v[52:55], v[160:163]\n"
    "buffer_load_dwordx4 v[108:111], v192, s[0:3], s38 offen offset:0 sc1\n"
    "s_waitcnt vmcnt(10)\n"
    "v_max3_u32 v184, v116, v118, v184\n"
    "v_smfmac_f32_16x16x64_bf16 v[148:151], v[116:119], a[0:7], v195\n"
    "v_smfmac_f32_16x16x64_bf16 v[152:155], v[116:119], v[8:15], v195\n"
    "buffer_load_dwordx4 v[112:115], v192, s[0:3], s38 offen offset:2048 sc1\n"
    "s_waitcnt vmcnt(10)\n"
    "v_max3_u32 v184, v84, v86, v184\n"
    "v_swap_b32 v85, v86\n"
    "s_nop 1\n"
    "v_mfma_f32_16x16x32_bf16 v[156:159], v[84:87], a[0:3], v[156:159]\n"
    "v_mfma_f32_16x16x32_bf16 v[160:163], v[84:87], v[8:11], v[160:163]\n"
    "buffer_load_dwordx2 v[186:187], v210, s[4:7], s46 offen sc1\n"
    "s_waitcnt vmcnt(10)\n"
    "v_max3_u32 v184, v88, v90, v184\n"
    "v_swap_b32 v89, v90\n"
    "s_nop 1\n"
    "v_mfma_f32_16x16x32_bf16 v[156:159], v[88:91], a[4:7], v[156:159]\n"
    "v_mfma_f32_16x16x32_bf16 v[160:163], v[88:91], v[12:15], v[160:163]\n"
    "buffer_store_dwordx2 v[166:167], v203, s[0:3], s31 offen sc1\n"
    "s_waitcnt vmcnt(10)\n"
    "v_max3_u32 v184, v120, v122, v184\n"
    "v_smfmac_f32_16x16x64_bf16 v[148:151], v[120:123], a[8:15], v195\n"
    "v_smfmac_f32_16x16x64_bf16 v[152:155], v[120:123], v[16:23], v195\n"
    "buffer_load_dwordx4 a[32:35], v192, s[0:3], s39 offen offset:0 sc1\n"
    "s_waitcnt vmcnt(10)\n"
    "v_max3_u32 v184, v92, v94, v184\n"
    "v_swap_b32 v93, v94\n"
    "s_nop 1\n"
    "v_mfma_f32_16x16x32_bf16 v[156:159], v[92:95], a[8:11], v[156:159]\n"
    "v_mfma_f32_16x16x32_bf16 v[160:163], v[92:95], v[16:19], v[160:163]\n"
    "buffer_load_dwordx4 a[36:39], v192, s[0:3], s39 offen offset:1024 sc1\n"
    "s_waitcnt vmcnt(10)\n"
    "v_max3_u32 v184, v96, v98, v184\n"
    "v_swap_b32 v97, v98\n"
    "s_nop 1\n"
    "v_mfma_f32_16x16x32_bf16 v[156:159], v[96:99], a[12:15], v[156:159]\n"
    "v_mfma_f32_16x16x32_bf16 v[160:163], v[96:99], v[20:23], v[160:163]\n"
    "buffer_load_dwordx4 a[40:43], v192, s[0:3], s39 offen offset:2048 sc1\n"
    "s_waitcnt vmcnt(10)\n"
    "v_max3_u32 v184, v124, v126, v184\n"
    "v_smfmac_f32_16x16x64_bf16 v[148:151], v[124:127], a[16:23], v195\n"
    "v_smfmac_f32_16x16x64_bf16 v[152:155], v[124:127], v[24:31], v195\n"
    "buffer_load_dwordx4 a[44:47], v192, s[0:3], s39 offen offset:3072 sc1\n"
    "s_waitcnt vmcnt(10)\n"
    "v_max3_u32 v184, v100, v102, v184\n"
    "v_swap_b32 v101, v102\n"
    "s_nop 1\n"
    "v_mfma_f32_16x16x32_bf16 v[156:159], v[100:103], a[16:19], v[156:159]\n"
    "v_mfma_f32_16x16x32_bf16 v[160:163], v[100:103], v[24:27], v[160:163]\n"
    "buffer_load_dwordx4 a[48:51], v193, s[0:3], s39 offen offset:0 sc1\n"
    "s_waitcnt vmcnt(10)\n"
    "v_max3_u32 v184, v104, v106, v184\n"
    "v_swap_b32 v105, v106\n"
    "s_nop 1\n"
    "v_mfma_f32_16x16x32_bf16 v[156:159], v[104:107], a[20:23], v[156:159]\n"
    "v_mfma_f32_16x16x32_bf16 v[160:163], v[104:107], v[28:31], v[160:163]\n"
    "buffer_load_dwordx4 a[52:55], v193, s[0:3], s39 offen offset:1024 sc1\n"
    "s_waitcnt vmcnt(10)\n"
    "v_max3_u32 v184, v128, v130, v184\n"
    "v_smfmac_f32_16x16x64_bf16 v[148:151], v[128:131], a[24:31], v195\n"
    "v_smfmac_f32_16x16x64_bf16 v[152:155], v[128:131], v[32:39], v195\n"
    "buffer_load_dwordx4 a[56:59], v193, s[0:3], s39 offen offset:2048 sc1\n"
    "s_waitcnt vmcnt(10)\n"
    "v_max3_u32 v184, v108, v110, v184\n"
    "v_swap_b32 v109, v110\n"
    "s_nop 1\n"
    "v_mfma_f32_16x16x32_bf16 v[156:159], v[108:111], a[24:27], v[156:159]\n"
    "v_mfma_f32_16x16x32_bf16 v[160:163], v[108:111], v[32:35], v[160:163]\n"
    "buffer_load_dwordx4 a[60:63], v193, s[0:3], s39 offen offset:3072 sc1\n"
    "s_waitcnt vmcnt(10)\n"
    "v_max3_u32 v184, v112, v114, v184\n"
    "v_swap_b32 v113, v114\n"
    "s_nop 1\n"
    "v_mfma_f32_16x16x32_bf16 v[156:159], v[112:115], a[28:31], v[156:159]\n"
    "v_mfma_f32_16x16x32_bf16 v[160:163], v[112:115], v[36:39], v[160:163]\n"
    "v_cmp_eq_u32 vcc, -1, v184\n"
    "s_nop 0\n"
    "s_cbranch_vccnz .Lct_tr0_%=\n"
    ".Lct_tg0_%=:\n"
    ".Lct_mk4x10_%=:\n"
    "ds_write2_b32 v204, v148, v149 offset0:0 offset1:20\n"
    "ds_write2_b32 v204, v150, v151 offset0:40 offset1:60\n"
    "ds_write2_b32 v205, v152, v153 offset0:0 offset1:20\n"
    "ds_write2_b32 v205, v154, v155 offset0:40 offset1:60\n"
    "ds_write2_b32 v206, v156, v157 offset0:0 offset1:20\n"
    "ds_write2_b32 v206, v158, v159 offset0:40 offset1:60\n"
    "ds_write2_b32 v207, v160, v161 offset0:0 offset1:20\n"
    "ds_write2_b32 v207, v162, v163 offset0:40 offset1:60\n"
    "s_mov_b32 s42, s45\n"
    "buffer_load_dwordx4 v[0:3], v196, s[0:3], s40 offen offset:0 sc1\n"
    "buffer_load_dwordx4 v[4:7], v196, s[0:3], s40 offen offset:1024 sc1\n"
    "s_add_u32 s28, s28, 1\n"
    "s_add_u32 s27, s28, 1\n"
    "s_add_u32 s29, s28, 0\n"
    "s_and_b32 s29, s29, 3\n"
    "s_lshl_b32 s29, s29, 22\n"
    "s_add_u32 s30, s28, 1\n"
    "s_and_b32 s30, s30, 3\n"
    "s_lshl_b32 s30, s30, 22\n"
    "s_add_u32 s31, s28, 3\n"
    "s_and_b32 s31, s31, 3\n"
    "s_lshl_b32 s31, s31, 22\n"
    "s_and_b32 s41, s28, 63\n"
    "s_lshl_b32 s41, s41, 7\n"
    "s_add_u32 s32, s12, s29\n"
    "s_add_u32 s33, s13, s29\n"
    "s_add_u32 s34, s15, s29\n"
    "s_add_u32 s35, s14, s29\n"
    "s_add_u32 s39, s10, s30\n"
    "s_add_u32 s40, s11, s30\n"
    "s_add_u32 s41, s16, s41\n"
    "s_add_u32 s36, s35, 4096\n"
    "s_add_u32 s37, s35, 16384\n"
    "s_add_u32 s38, s35, 20480\n"
    "s_add_u32 s43, s26, 304\n"
    "s_lshl_b32 s45, s28, 19\n"
    "s_lshl_b32 s25, s28, 11\n"
    "s_xor_b32 s25, s25, s8\n"
    "s_and_b32 s25, s25, 0x2000\n"
    "s_add_u32 s45, s45, s25\n"
    "s_add_u32 s45, s45, s9\n"
    "s_mul_i32 s46, s28, 40960\n"
    "s_cmp_eq_u32 s8, 0\n"
    "s_cselect_b32 s46, s46, s45\n"
    "v_mov_b64 v[76:77], 0\n"
    "v_mov_b64 v[78:79], 0\n"
    "v_mov_b64 v[80:81], 0\n"
    "v_mov_b64 v[82:83], 0\n"
    "s_waitcnt vmcnt(11)\n"
    "v_lshlrev_b32 v148, 16, v186\n"
    "v_and_b32 v149, 0xffff0000, v186\n"
    "v_lshlrev_b32 v150, 16, v187\n"
    "v_and_b32 v151, 0xffff0000, v187\n"
    "s_memrealtime s[22:23]\n"
    "s_waitcnt lgkmcnt(0)\n"
    "s_barrier\n"
    "s_sub_u32 s24, s43, s22\n"
    "s_cmp_gt_i32 s24, 6\n"
    "s_cselect_b32 s24, 2130706432, s41\n"
    "buffer_load_dwordx2 v[190:191], off, s[0:3], s24 sc1\n"
    ".Lct_mk6x11_%=:\n"
    "ds_read_b128 v[164:167], v208 offset:0\n"
    "ds_read_b128 v[168:171], v208 offset:2560\n"
    "ds_read_b128 v[172:175], v208 offset:5120\n"
    "ds_read_b128 v[176:179], v208 offset:7680\n"
    "s_waitcnt lgkmcnt(2)\n"
    "v_pk_add_f32 v[164:165], v[164:165], v[168:169]\n"
    "v_pk_add_f32 v[166:167], v[166:167], v[170:171]\n"
    "s_waitcnt lgkmcnt(1)\n"
    "v_pk_add_f32 v[164:165], v[164:165], v[172:173]\n"
    "v_pk_add_f32 v[166:167], v[166:167], v[174:175]\n"
    "s_waitcnt lgkmcnt(0)\n"
    "v_pk_add_f32 v[164:165], v[164:165], v[176:177]\n"
    "v_pk_add_f32 v[166:167], v[166:167], v[178:179]\n"
    "v_cvt_pk_bf16_f32 v164, v164, v165\n"
    "v_cvt_pk_bf16_f32 v165, v166, v167\n"
    "v_lshlrev_b32 v168, 16, v164\n"
    "v_and_b32 v169, 0xffff0000, v164\n"
    "v_lshlrev_b32 v170, 16, v165\n"
    "v_and_b32 v171, 0xffff0000, v165\n"
    "v_pk_add_f32 v[168:169], v[168:169], v[148:149]\n"
    "v_pk_add_f32 v[170:171], v[170:171], v[150:151]\n"
    "v_cvt_pk_bf16_f32 v164, v168, v169\n"
    "v_cvt_pk_bf16_f32 v165, v170, v171\n"
    "buffer_store_dwordx2 v[164:165], v209, s[0:3], s42 offen sc1\n"
    ".Lct_mk5x12_%=:\n"
    "s_waitcnt vmcnt(2)\n"
    "v_mov_b64 v[8:9], v[0:1]\n"
    "v_mov_b64 v[10:11], v[2:3]\n"
    "v_mov_b64 v[12:13], v[4:5]\n"
    "v_mov_b64 v[14:15], v[6:7]\n"
    "v_mov_b32_dpp v16, v0 row_shl:4 row_mask:0xf bank_mask:0xf bound_ctrl:1\n"
    "v_mov_b32_dpp v17, v1 row_shl:4 row_mask:0xf bank_mask:0xf bound_ctrl:1\n"
    "v_mov_b32_dpp v18, v2 row_shl:4 row_mask:0xf bank_mask:0xf bound_ctrl:1\n"
    "v_mov_b32_dpp v19, v3 row_shl:4 row_mask:0xf bank_mask:0xf bound_ctrl:1\n"
    "v_mov_b32_dpp v20, v4 row_shl:4 row_mask:0xf bank_mask:0xf bound_ctrl:1\n"
    "v_mov_b32_dpp v21, v5 row_shl:4 row_mask:0xf bank_mask:0xf bound_ctrl:1\n"
    "v_mov_b32_dpp v22, v6 row_shl:4 row_mask:0xf bank_mask:0xf bound_ctrl:1\n"
    "v_mov_b32_dpp v23, v7 row_shl:4 row_mask:0xf bank_mask:0xf bound_ctrl:1\n"
    "v_mov_b32_dpp v24, v0 row_shl:8 row_mask:0xf bank_mask:0xf bound_ctrl:1\n"
    "v_mov_b32_dpp v25, v1 row_shl:8 row_mask:0xf bank_mask:0xf bound_ctrl:1\n"
    "v_mov_b32_dpp v26, v2 row_shl:8 row_mask:0xf bank_mask:0xf bound_ctrl:1\n"
    "v_mov_b32_dpp v27, v3 row_shl:8 row_mask:0xf bank_mask:0xf bound_ctrl:1\n"
    "v_mov_b32_dpp v28, v4 row_shl:8 row_mask:0xf bank_mask:0xf bound_ctrl:1\n"
    "v_mov_b32_dpp v29, v5 row_shl:8 row_mask:0xf bank_mask:0xf bound_ctrl:1\n"
    "v_mov_b32_dpp v30, v6 row_shl:8 row_mask:0xf bank_mask:0xf bound_ctrl:1\n"
    "v_mov_b32_dpp v31, v7 row_shl:8 row_mask:0xf bank_mask:0xf bound_ctrl:1\n"
    "v_mov_b32_dpp v32, v0 row_shl:12 row_mask:0xf bank_mask:0xf bound_ctrl:1\n"
    "v_mov_b32_dpp v33, v1 row_shl:12 row_mask:0xf bank_mask:0xf bound_ctrl:1\n"
    "v_mov_b32_dpp v34, v2 row_shl:12 row_mask:0xf bank_mask:0xf bound_ctrl:1\n"
    "v_mov_b32_dpp v35, v3 row_shl:12 row_mask:0xf bank_mask:0xf bound_ctrl:1\n"
    "v_mov_b32_dpp v36, v4 row_shl:12 row_mask:0xf bank_mask:0xf bound_ctrl:1\n"
    "v_mov_b32_dpp v37, v5 row_shl:12 row_mask:0xf bank_mask:0xf bound_ctrl:1\n"
    "v_mov_b32_dpp v38, v6 row_shl:12 row_mask:0xf bank_mask:0xf bound_ctrl:1\n"
    "v_mov_b32_dpp v39, v7 row_shl:12 row_mask:0xf bank_mask:0xf bound_ctrl:1\n"
    "s_waitcnt vmcnt(1)\n"
    ".Lct_x1_%=:\n"
    "v_readfirstlane_b32 s25, v191\n"
    "v_readfirstlane_b32 s24, v190\n"
    "s_cmp_eq_u32 s25, s27\n"
    "s_cselect_b32 s26, s24, s26\n"
    "s_cbranch_scc0 .Lct_xr1_%=\n"
    ".Lct_xg1_%=:\n"
    "s_add_u32 s43, s26, 97\n"
    "s_add_u32 s44, s26, 109\n"
    ".Lct_mk0x17_%=:\n"
    ".Lct_rt19_%=:\n"
    "s_memrealtime s[22:23]\n"
    "s_waitcnt lgkmcnt(0)\n"
    "s_sub_u32 s24, s43, s22\n"
    "s_cmp_lt_u32 s24, 600\n"
    "s_cbranch_scc1 .Lct_rt19_%=\n"
    "buffer_load_dwordx4 v[40:43], v192, s[0:3], s32 offen offset:0 sc1\n"
    "buffer_load_dwordx4 v[44:47], v192, s[0:3], s32 offen offset:1024 sc1\n"
    "buffer_load_dwordx4 v[48:51], v197, s[0:3], s33 offen offset:0 sc1\n"
    "buffer_load_dwordx4 v[52:55], v197, s[0:3], s33 offen offset:256 sc1\n"
    "v_mov_b32 v148, s28\n"
    "v_mov_b32 v149, s28\n"
    "v_mov_b32 v150, s28\n"
    "v_mov_b32 v151, s28\n"
    "s_waitcnt vmcnt(4)\n"
    "s_barrier\n"
    "buffer_store_dwordx4 v[148:151], v211, s[0:3], 0 offen sc1\n"
    ".Lct_rt20_%=:\n"
    "s_memrealtime s[22:23]\n"
    "s_waitcnt lgkmcnt(0)\n"
    "s_sub_u32 s24, s44, s22\n"
    "s_cmp_lt_u32 s24, 600\n"
    "s_cbranch_scc1 .Lct_rt20_%=\n"
    "buffer_load_dwordx4 v[72:75], v193, s[0:3], s34 offen sc1\n"
    "buffer_load_dwordx4 v[56:59], v192, s[0:3], s34 offen offset:0 sc1\n"
    "buffer_load_dwordx4 v[60:63], v192, s[0:3], s34 offen offset:1024 sc1\n"
    "buffer_load_dwordx4 v[64:67], v192, s[0:3], s34 offen offset:2048 sc1\n"
    "buffer_load_dwordx4 v[68:71], v192, s[0:3], s34 offen offset:3072 sc1\n"
    ".Lct_mk1x20_%=:\n"
    ".Lct_take1_%=:\n"
    "s_waitcnt vmcnt(6)\n"
    "v_max_u32 v184, v40, v44\n"
    "v_max3_u32 v184, v48, v50, v184\n"
    "v_max3_u32 v184, v52, v54, v184\n"
    "s_waitcnt vmcnt(4)\n"
    "v_smfmac_f32_16x16x64_bf16 v[76:79], v[72:75], v[40:47], v194\n"
    "v_max3_u32 v184, v72, v74, v184\n"
    "v_smfmac_f32_16x16x64_bf16 v[80:83], v[72:75], v[48:55], v194\n"
    "s_waitcnt vmcnt(3)\n"
    "v_smfmac_f32_16x16x64_bf16 v[80:83], v[56:59], v[8:15], v194\n"
    "v_max3_u32 v184, v56, v58, v184\n"
    "v_smfmac_f32_16x16x64_bf16 v[76:79], v[56:59], a[32:39], v194\n"
    "s_waitcnt vmcnt(2)\n"
    "v_smfmac_f32_16x16x64_bf16 v[80:83], v[60:63], v[16:23], v194\n"
    "v_max3_u32 v184, v60, v62, v184\n"
    "v_smfmac_f32_16x16x64_bf16 v[76:79], v[60:63], a[40:47], v194\n"
    "s_waitcnt vmcnt(1)\n"
    "v_smfmac_f32_16x16x64_bf16 v[80:83], v[64:67], v[24:31], v194\n"
    "v_max3_u32 v184, v64, v66, v184\n"
    "v_smfmac_f32_16x16x64_bf16 v[76:79], v[64:67], a[48:55], v194\n"
    "s_waitcnt vmcnt(0)\n"
    "v_smfmac_f32_16x16x64_bf16 v[80:83], v[68:71], v[32:39], v194\n"
    "v_max3_u32 v184, v68, v70, v184\n"
    "v_smfmac_f32_16x16x64_bf16 v[76:79], v[68:71], a[56:63], v194\n"
    ".Lct_mk2x21_%=:\n"
    "s_nop 1\n"
    "v_cmp_eq_u32 vcc, -1, v184\n"
    "s_nop 1\n"
    "v_add_f32 v80, v80, v82\n"
    "v_add_f32 v81, v81, v83\n"
    "s_nop 0\n"
    "v_pk_add_f32 v[76:77], v[76:77], v[78:79]\n"
    "ds_write2_b32 v201, v80, v81 offset1:20\n"
    "ds_write2_b32 v200, v76, v77 offset1:20\n"
    "s_cbranch_vccnz .Lct_retry1_%=\n"
    ".Lct_go1_%=:\n"
    "s_waitcnt lgkmcnt(0)\n"
    "s_barrier\n"
    "ds_read_b128 v[164:167], v202 offset:0\n"
    "ds_read_b128 v[168:171], v202 offset:640\n"
    "ds_read_b128 v[172:175], v202 offset:1280\n"
    "ds_read_b128 v[176:179], v202 offset:1920\n"
    "s_waitcnt lgkmcnt(2)\n"
    "v_pk_add_f32 v[164:165], v[164:165], v[168:169]\n"
    "v_pk_add_f32 v[166:167], v[166:167], v[170:171]\n"
    "s_waitcnt lgkmcnt(1)\n"
    "v_pk_add_f32 v[164:165], v[164:165], v[172:173]\n"
    "v_pk_add_f32 v[166:167], v[166:167], v[174:175]\n"
    "s_waitcnt lgkmcnt(0)\n"
    "v_pk_add_f32 v[164:165], v[164:165], v[176:177]\n"
    "v_pk_add_f32 v[166:167], v[166:167], v[178:179]\n"
    "v_cvt_pk_bf16_f32 v164, v164, v165\n"
    "v_cvt_pk_bf16_f32 v165, v166, v167\n"
    "v_min_u32 v164, -2, v164\n"
    "buffer_store_dwordx2 v[164:165], v203, s[0:3], s30 offen sc1\n"
    ".Lct_mk3x22_%=:\n"
    "s_sleep 2\n"
    "v_mov_b64 v[166:167], -1\n"
    "buffer_load_dwordx4 v[132:135], v199, s[0:3], s35 offen offset:0 sc1\n"
    "buffer_load_dwordx4 v[136:139], v199, s[0:3], s37 offen offset:0 sc1\n"
    "buffer_load_dwordx4 v[140:143], v199, s[0:3], s35 offen offset:256 sc1\n"
    "buffer_load_dwordx4 v[144:147], v199, s[0:3], s37 offen offset:256 sc1\n"
    "buffer_load_dwordx4 v[116:119], v198, s[0:3], s35 offen sc1\n"
    "buffer_load_dwordx4 v[84:87], v192, s[0:3], s35 offen offset:0 sc1\n"
    "buffer_load_dwordx4 v[88:91], v192, s[0:3], s35 offen offset:2048 sc1\n"
    "buffer_load_dwordx4 v[120:123], v198, s[0:3], s36 offen sc1\n"
    "buffer_load_dwordx4 v[92:95], v192, s[0:3], s36 offen offset:0 sc1\n"
    "buffer_load_dwordx4 v[96:99], v192, s[0:3], s36 offen offset:2048 sc1\n"
    ".Lct_mk7x23_%=:\n"
    "buffer_load_dwordx4 v[124:127], v198, s[0:3], s37 offen sc1\n"
    "s_waitcnt vmcnt(10)\n"
    "v_max3_u32 v184, v132, v134, v132\n"
    "v_swap_b32 v133, v134\n"
    "s_nop 1\n"
    "v_mfma_f32_16x16x32_bf16 v[148:151], v[132:135], v[40:43], 0\n"
    "v_mfma_f32_16x16x32_bf16 v[152:155], v[132:135], v[48:51], 0\n"
    "buffer_load_dwordx4 v[100:103], v192, s[0:3], s37 offen offset:0 sc1\n"
    "s_waitcnt vmcnt(10)\n"
    "v_max3_u32 v184, v136, v138, v184\n"
    "v_swap_b32 v137, v138\n"
    "s_nop 1\n"
    "v_mfma_f32_16x16x32_bf16 v[148:151], v[136:139], v[44:47], v[148:151]\n"
    "v_mfma_f32_16x16x32_bf16 v[152:155], v[136:139], v[52:55], v[152:155]\n"
    "buffer_load_dwordx4 v[104:107], v192, s[0:3], s37 offen offset:2048 sc1\n"
    "s_waitcnt vmcnt(10)\n"
    "v_max3_u32 v184, v140, v142, v184\n"
    "v_swap_b32 v141, v142\n"
    "s_nop 1\n"
    "v_mfma_f32_16x16x32_bf16 v[156:159], v[140:143], v[40:43], 0\n"
    "v_mfma_f32_16x16x32_bf16 v[160:163], v[140:143], v[48:51], 0\n"
    "buffer_load_dwordx4 v[128:131], v198, s[0:3], s38 offen sc1\n"
    "s_waitcnt vmcnt(10)\n"
    "v_max3_u32 v184, v144, v146, v184\n"
    "v_swap_b32 v145, v146\n"
    "s_nop 1\n"
    "v_mfma_f32_16x16x32_bf16 v[156:159], v[144:147], v[44:47], v[156:159]\n"
    "v_mfma_f32_16x16x32_bf16 v[160:163], v[144:147], v[52:55], v[160:163]\n"
    "buffer_load_dwordx4 v[108:111], v192, s[0:3], s38 offen offset:0 sc1\n"
    "s_waitcnt vmcnt(10)\n"
    "v_max3_u32 v184, v116, v118, v184\n"
    "v_smfmac_f32_16x16x64_bf16 v[148:151], v[116:119], a[32:39], v195\n"
    "v_smfmac_f32_16x16x64_bf16 v[152:155], v[116:119], v[8:15], v195\n"
    "buffer_load_dwordx4 v[112:115], v192, s[0:3], s38 offen offset:2048 sc1\n"
    "s_waitcnt vmcnt(10)\n"
    "v_max3_u32 v184, v84, v86, v184\n"
    "v_swap_b32 v85, v86\n"
    "s_nop 1\n"
    "v_mfma_f32_16x16x32_bf16 v[156:159], v[84:87], a[32:35], v[156:159]\n"
    "v_mfma_f32_16x16x32_bf16 v[160:163], v[84:87], v[8:11], v[160:163]\n"
    "buffer_load_dwordx2 v[186:187], v210, s[4:7], s46 offen sc1\n"
    "s_waitcnt vmcnt(10)\n"
    "v_max3_u32 v184, v88, v90, v184\n"
    "v_swap_b32 v89, v90\n"
    "s_nop 1\n"
    "v_mfma_f32_16x16x32_bf16 v[156:159], v[88:91], a[36:39], v[156:159]\n"
    "v_mfma_f32_16x16x32_bf16 v[160:163], v[88:91], v[12:15], v[160:163]\n"
    "buffer_store_dwordx2 v[166:167], v203, s[0:3], s31 offen sc1\n"
    "s_waitcnt vmcnt(10)\n"
    "v_max3_u32 v184, v120, v122, v184\n"
    "v_smfmac_f32_16x16x64_bf16 v[148:151], v[120:123], a[40:47], v195\n"
    "v_smfmac_f32_16x16x64_bf16 v[152:155], v[120:123], v[16:23], v195\n"
    "buffer_load_dwordx4 a[0:3], v192, s[0:3], s39 offen offset:0 sc1\n"
    "s_waitcnt vmcnt(10)\n"
    "v_max3_u32 v184, v92, v94, v184\n"
    "v_swap_b32 v93, v94\n"
    "s_nop 1\n"
    "v_mfma_f32_16x16x32_bf16 v[156:159], v[92:95], a[40:43], v[156:159]\n"
    "v_mfma_f32_16x16x32_bf16 v[160:163], v[92:95], v[16:19], v[160:163]\n"
    "buffer_load_dwordx4 a[4:7], v192, s[0:3], s39 offen offset:1024 sc1\n"
    "s_waitcnt vmcnt(10)\n"
    "v_max3_u32 v184, v96, v98, v184\n"
    "v_swap_b32 v97, v98\n"
    "s_nop 1\n"
    "v_mfma_f32_16x16x32_bf16 v[156:159], v[96:99], a[44:47], v[156:159]\n"
    "v_mfma_f32_16x16x32_bf16 v[160:163], v[96:99], v[20:23], v[160:163]\n"
    "buffer_load_dwordx4 a[8:11], v192, s[0:3], s39 offen offset:2048 sc1\n"
    "s_waitcnt vmcnt(10)\n"
    "v_max3_u32 v184, v124, v126, v184\n"
    "v_smfmac_f32_16x16x64_bf16 v[148:151], v[124:127], a[48:55], v195\n"
    "v_smfmac_f32_16x16x64_bf16 v[152:155], v[124:127], v[24:31], v195\n"
    "buffer_load_dwordx4 a[12:15], v192, s[0:3], s39 offen offset:3072 sc1\n"
    "s_waitcnt vmcnt(10)\n"
    "v_max3_u32 v184, v100, v102, v184\n"
    "v_swap_b32 v101, v102\n"
    "s_nop 1\n"
    "v_mfma_f32_16x16x32_bf16 v[156:159], v[100:103], a[48:51], v[156:159]\n"
    "v_mfma_f32_16x16x32_bf16 v[160:163], v[100:103], v[24:27], v[160:163]\n"
    "buffer_load_dwordx4 a[16:19], v193, s[0:3], s39 offen offset:0 sc1\n"
    "s_waitcnt vmcnt(10)\n"
    "v_max3_u32 v184, v104, v106, v184\n"
    "v_swap_b32 v105, v106\n"
    "s_nop 1\n"
    "v_mfma_f32_16x16x32_bf16 v[156:159], v[104:107], a[52:55], v[156:159]\n"
    "v_mfma_f32_16x16x32_bf16 v[160:163], v[104:107], v[28:31], v[160:163]\n"
    "buffer_load_dwordx4 a[20:23], v193, s[0:3], s39 offen offset:1024 sc1\n"
    "s_waitcnt vmcnt(10)\n"
    "v_max3_u32 v184, v128, v130, v184\n"
    "v_smfmac_f32_16x16x64_bf16 v[148:151], v[128:131], a[56:63], v195\n"
    "v_smfmac_f32_16x16x64_bf16 v[152:155], v[128:131], v[32:39], v195\n"
    "buffer_load_dwordx4 a[24:27], v193, s[0:3], s39 offen offset:2048 sc1\n"
    "s_waitcnt vmcnt(10)\n"
    "v_max3_u32 v184, v108, v110, v184\n"
    "v_swap_b32 v109, v110\n"
    "s_nop 1\n"
    "v_mfma_f32_16x16x32_bf16 v[156:159], v[108:111], a[56:59], v[156:159]\n"
    "v_mfma_f32_16x16x32_bf16 v[160:163], v[108:111], v[32:35], v[160:163]\n"
    "buffer_load_dwordx4 a[28:31], v193, s[0:3], s39 offen offset:3072 sc1\n"
    "s_waitcnt vmcnt(10)\n"
    "v_max3_u32 v184, v112, v114, v184\n"
    "v_swap_b32 v113, v114\n"
    "s_nop 1\n"
    "v_mfma_f32_16x16x32_bf16 v[156:159], v[112:115], a[60:63], v[156:159]\n"
    "v_mfma_f32_16x16x32_bf16 v[160:163], v[112:115], v[36:39], v[160:163]\n"
    "v_cmp_eq_u32 vcc, -1, v184\n"
    "s_nop 0\n"
    "s_cbranch_vccnz .Lct_tr1_%=\n"
    ".Lct_tg1_%=:\n"
    ".Lct_mk4x24_%=:\n"
    "ds_write2_b32 v204, v148, v149 offset0:0 offset1:20\n"
    "ds_write2_b32 v204, v150, v151 offset0:40 offset1:60\n"
    "ds_write2_b32 v205, v152, v153 offset0:0 offset1:20\n"
    "ds_write2_b32 v205, v154, v155 offset0:40 offset1:60\n"
    "ds_write2_b32 v206, v156, v157 offset0:0 offset1:20\n"
    "ds_write2_b32 v206, v158, v159 offset0:40 offset1:60\n"
    "ds_write2_b32 v207, v160, v161 offset0:0 offset1:20\n"
    "ds_write2_b32 v207, v162, v163 offset0:40 offset1:60\n"
    "s_mov_b32 s42, s45\n"
    "buffer_load_dwordx4 v[0:3], v196, s[0:3], s40 offen offset:0 sc1\n"
    "buffer_load_dwordx4 v[4:7], v196, s[0:3], s40 offen offset:1024 sc1\n"
    "s_add_u32 s28, s28, 1\n"
    "s_add_u32 s27, s28, 1\n"
    "s_add_u32 s29, s28, 0\n"
    "s_and_b32 s29, s29, 3\n"
    "s_lshl_b32 s29, s29, 22\n"
    "s_add_u32 s30, s28, 1\n"
    "s_and_b32 s30, s30, 3\n"
    "s_lshl_b32 s30, s30, 22\n"
    "s_add_u32 s31, s28, 3\n"
    "s_and_b32 s31, s31, 3\n"
    "s_lshl_b32 s31, s31, 22\n"
    "s_and_b32 s41, s28, 63\n"
    "s_lshl_b32 s41, s41, 7\n"
    "s_add_u32 s32, s12, s29\n"
    "s_add_u32 s33, s13, s29\n"
    "s_add_u32 s34, s15, s29\n"
    "s_add_u32 s35, s14, s29\n"
    "s_add_u32 s39, s10, s30\n"
    "s_add_u32 s40, s11, s30\n"
    "s_add_u32 s41, s16, s41\n"
    "s_add_u32 s36, s35, 4096\n"
    "s_add_u32 s37, s35, 16384\n"
    "s_add_u32 s38, s35, 20480\n"
    "s_add_u32 s43, s26, 304\n"
    "s_lshl_b32 s45, s28, 19\n"
    "s_lshl_b32 s25, s28, 11\n"
    "s_xor_b32 s25, s25, s8\n"
    "s_and_b32 s25, s25, 0x2000\n"
    "s_add_u32 s45, s45, s25\n"
    "s_add_u32 s45, s45, s9\n"
    "s_mul_i32 s46, s28, 40960\n"
    "s_cmp_eq_u32 s8, 0\n"
    "s_cselect_b32 s46, s46, s45\n"
    "v_mov_b64 v[76:77], 0\n"
    "v_mov_b64 v[78:79], 0\n"
    "v_mov_b64 v[80:81], 0\n"
    "v_mov_b64 v[82:83], 0\n"
    "s_waitcnt vmcnt(11)\n"
    "v_lshlrev_b32 v148, 16, v186\n"
    "v_and_b32 v149, 0xffff0000, v186\n"
    "v_lshlrev_b32 v150, 16, v187\n"
    "v_and_b32 v151, 0xffff0000, v187\n"
    "s_memrealtime s[22:23]\n"
    "s_waitcnt lgkmcnt(0)\n"
    "s_barrier\n"
    "s_sub_u32 s24, s43, s22\n"
    "s_cmp_gt_i32 s24, 6\n"
    "s_cselect_b32 s24, 2130706432, s41\n"
    "buffer_load_dwordx2 v[190:191], off, s[0:3], s24 sc1\n"
    ".Lct_mk6x25_%=:\n"
    "ds_read_b128 v[164:167], v208 offset:0\n"
    "ds_read_b128 v[168:171], v208 offset:2560\n"
    "ds_read_b128 v[172:175], v208 offset:5120\n"
    "ds_read_b128 v[176:179], v208 offset:7680\n"
    "s_waitcnt lgkmcnt(2)\n"
    "v_pk_add_f32 v[164:165], v[164:165], v[168:169]\n"
    "v_pk_add_f32 v[166:167], v[166:167], v[170:171]\n"
    "s_waitcnt lgkmcnt(1)\n"
    "v_pk_add_f32 v[164:165], v[164:165], v[172:173]\n"
    "v_pk_add_f32 v[166:167], v[166:167], v[174:175]\n"
    "s_waitcnt lgkmcnt(0)\n"
    "v_pk_add_f32 v[164:165], v[164:165], v[176:177]\n"
    "v_pk_add_f32 v[166:167], v[166:167], v[178:179]\n"
    "v_cvt_pk_bf16_f32 v164, v164, v165\n"
    "v_cvt_pk_bf16_f32 v165, v166, v167\n"
    "v_lshlrev_b32 v168, 16, v164\n"
    "v_and_b32 v169, 0xffff0000, v164\n"
    "v_lshlrev_b32 v170, 16, v165\n"
    "v_and_b32 v171, 0xffff0000, v165\n"
    "v_pk_add_f32 v[168:169], v[168:169], v[148:149]\n"
    "v_pk_add_f32 v[170:171], v[170:171], v[150:151]\n"
    "v_cvt_pk_bf16_f32 v164, v168, v169\n"
    "v_cvt_pk_bf16_f32 v165, v170, v171\n"
    "buffer_store_dwordx2 v[164:165], v209, s[0:3], s42 offen sc1\n"
    ".Lct_mk5x26_%=:\n"
    "s_cmp_lt_u32 s28, 64\n"
    "s_cbranch_scc1 .Lct_top_%=\n"
    "v_mov_b32 v148, s28\n"
    "v_mov_b32 v149, s28\n"
    "v_mov_b32 v150, s28\n"
    "v_mov_b32 v151, s28\n"
    "s_waitcnt vmcnt(0)\n"
    "s_barrier\n"
    "buffer_store_dwordx4 v[148:151], v211, s[0:3], 0 offen sc1\n"
    "s_waitcnt vmcnt(0) lgkmcnt(0)\n"
    "s_endpgm\n"
    ".Lct_xr0_%=:\n"
    "s_add_u32 s20, s20, 1\n"
    "s_cmp_lt_u32 s20, 0x40000\n"
    "s_cbranch_scc1 .Lct_sp14_%=\n"
    "v_mov_b32 v184, 42\n"
    "s_mov_b32 s24, 33728\n"
    "buffer_store_dword v184, off, s[0:3], s24 sc1\n"
    "s_waitcnt vmcnt(0)\n"
    "s_branch .Lct_xg0_%=\n"
    ".Lct_sp14_%=:\n"
    ".Lct_rt15_%=:\n"
    "s_memrealtime s[22:23]\n"
    "s_waitcnt lgkmcnt(0)\n"
    "s_sub_u32 s24, s43, s22\n"
    "s_cmp_lt_u32 s24, 600\n"
    "s_cbranch_scc1 .Lct_rt15_%=\n"
    "s_add_u32 s43, s22, 44\n"
    "buffer_load_dwordx2 v[190:191], off, s[0:3], s41 sc1\n"
    "s_waitcnt vmcnt(0)\n"
    "s_branch .Lct_x0_%=\n"
    ".Lct_retry0_%=:\n"
    "s_add_u32 s20, s20, 1\n"
    "s_cmp_lt_u32 s20, 0x40000\n"
    "s_cbranch_scc1 .Lct_sp16_%=\n"
    "v_mov_b32 v184, 3\n"
    "s_mov_b32 s24, 33728\n"
    "buffer_store_dword v184, off, s[0:3], s24 sc1\n"
    "s_waitcnt vmcnt(0)\n"
    "s_branch .Lct_go0_%=\n"
    ".Lct_sp16_%=:\n"
    "s_waitcnt vmcnt(0) lgkmcnt(0)\n"
    "v_mov_b64 v[76:77], 0\n"
    "v_mov_b64 v[78:79], 0\n"
    "v_mov_b64 v[80:81], 0\n"
    "v_mov_b64 v[82:83], 0\n"
    "buffer_load_dwordx4 v[40:43], v192, s[0:3], s32 offen offset:0 sc1\n"
    "buffer_load_dwordx4 v[44:47], v192, s[0:3], s32 offen offset:1024 sc1\n"
    "buffer_load_dwordx4 v[48:51], v197, s[0:3], s33 offen offset:0 sc1\n"
    "buffer_load_dwordx4 v[52:55], v197, s[0:3], s33 offen offset:256 sc1\n"
    "buffer_store_dwordx4 v[148:151], v211, s[0:3], 0 offen sc1\n"
    "buffer_load_dwordx4 v[72:75], v193, s[0:3], s34 offen sc1\n"
    "buffer_load_dwordx4 v[56:59], v192, s[0:3], s34 offen offset:0 sc1\n"
    "buffer_load_dwordx4 v[60:63], v192, s[0:3], s34 offen offset:1024 sc1\n"
    "buffer_load_dwordx4 v[64:67], v192, s[0:3], s34 offen offset:2048 sc1\n"
    "buffer_load_dwordx4 v[68:71], v192, s[0:3], s34 offen offset:3072 sc1\n"
    "s_waitcnt vmcnt(0)\n"
    "s_branch .Lct_take0_%=\n"
    ".Lct_tr0_%=:\n"
    "s_add_u32 s20, s20, 1\n"
    "s_cmp_lt_u32 s20, 0x40000\n"
    "s_cbranch_scc1 .Lct_sp17_%=\n"
    "v_mov_b32 v184, 4\n"
    "s_mov_b32 s24, 33728\n"
    "buffer_store_dword v184, off, s[0:3], s24 sc1\n"
    "s_waitcnt vmcnt(0)\n"
    "s_branch .Lct_tg0_%=\n"
    ".Lct_sp17_%=:\n"
    "buffer_load_dwordx4 v[132:135], v199, s[0:3], s35 offen offset:0 sc1\n"
    "buffer_load_dwordx4 v[136:139], v199, s[0:3], s37 offen offset:0 sc1\n"
    "buffer_load_dwordx4 v[140:143], v199, s[0:3], s35 offen offset:256 sc1\n"
    "buffer_load_dwordx4 v[144:147], v199, s[0:3], s37 offen offset:256 sc1\n"
    "buffer_load_dwordx4 v[116:119], v198, s[0:3], s35 offen sc1\n"
    "buffer_load_dwordx4 v[84:87], v192, s[0:3], s35 offen offset:0 sc1\n"
    "buffer_load_dwordx4 v[88:91], v192, s[0:3], s35 offen offset:2048 sc1\n"
    "buffer_load_dwordx4 v[120:123], v198, s[0:3], s36 offen sc1\n"
    "buffer_load_dwordx4 v[92:95], v192, s[0:3], s36 offen offset:0 sc1\n"
    "buffer_load_dwordx4 v[96:99], v192, s[0:3], s36 offen offset:2048 sc1\n"
    "buffer_load_dwordx4 v[124:127], v198, s[0:3], s37 offen sc1\n"
    "buffer_load_dwordx4 v[100:103], v192, s[0:3], s37 offen offset:0 sc1\n"
    "buffer_load_dwordx4 v[104:107], v192, s[0:3], s37 offen offset:2048 sc1\n"
    "buffer_load_dwordx4 v[128:131], v198, s[0:3], s38 offen sc1\n"
    "buffer_load_dwordx4 v[108:111], v192, s[0:3], s38 offen offset:0 sc1\n"
    "buffer_load_dwordx4 v[112:115], v192, s[0:3], s38 offen offset:2048 sc1\n"
    "s_waitcnt vmcnt(0)\n"
    "v_max3_u32 v184, v132, v134, v132\n"
    "v_swap_b32 v133, v134\n"
    "s_nop 1\n"
    "v_mfma_f32_16x16x32_bf16 v[148:151], v[132:135], v[40:43], 0\n"
    "v_mfma_f32_16x16x32_bf16 v[152:155], v[132:135], v[48:51], 0\n"
    "v_max3_u32 v184, v136, v138, v184\n"
    "v_swap_b32 v137, v138\n"
    "s_nop 1\n"
    "v_mfma_f32_16x16x32_bf16 v[148:151], v[136:139], v[44:47], v[148:151]\n"
    "v_mfma_f32_16x16x32_bf16 v[152:155], v[136:139], v[52:55], v[152:155]\n"
    "v_max3_u32 v184, v140, v142, v184\n"
    "v_swap_b32 v141, v142\n"
    "s_nop 1\n"
    "v_mfma_f32_16x16x32_bf16 v[156:159], v[140:143], v[40:43], 0\n"
    "v_mfma_f32_16x16x32_bf16 v[160:163], v[140:143], v[48:51], 0\n"
    "v_max3_u32 v184, v144, v146, v184\n"
    "v_swap_b32 v145, v146\n"
    "s_nop 1\n"
    "v_mfma_f32_16x16x32_bf16 v[156:159], v[144:147], v[44:47], v[156:159]\n"
    "v_mfma_f32_16x16x32_bf16 v[160:163], v[144:147], v[52:55], v[160:163]\n"
    "v_max3_u32 v184, v116, v118, v184\n"
    "v_smfmac_f32_16x16x64_bf16 v[148:151], v[116:119], a[0:7], v195\n"
    "v_smfmac_f32_16x16x64_bf16 v[152:155], v[116:119], v[8:15], v195\n"
    "v_max3_u32 v184, v84, v86, v184\n"
    "v_swap_b32 v85, v86\n"
    "s_nop 1\n"
    "v_mfma_f32_16x16x32_bf16 v[156:159], v[84:87], a[0:3], v[156:159]\n"
    "v_mfma_f32_16x16x32_bf16 v[160:163], v[84:87], v[8:11], v[160:163]\n"
    "v_max3_u32 v184, v88, v90, v184\n"
    "v_swap_b32 v89, v90\n"
    "s_nop 1\n"
    "v_mfma_f32_16x16x32_bf16 v[156:159], v[88:91], a[4:7], v[156:159]\n"
    "v_mfma_f32_16x16x32_bf16 v[160:163], v[88:91], v[12:15], v[160:163]\n"
    "v_max3_u32 v184, v120, v122, v184\n"
    "v_smfmac_f32_16x16x64_bf16 v[148:151], v[120:123], a[8:15], v195\n"
    "v_smfmac_f32_16x16x64_bf16 v[152:155], v[120:123], v[16:23], v195\n"
    "v_max3_u32 v184, v92, v94, v184\n"
    "v_swap_b32 v93, v94\n"
    "s_nop 1\n"
    "v_mfma_f32_16x16x32_bf16 v[156:159], v[92:95], a[8:11], v[156:159]\n"
    "v_mfma_f32_16x16x32_bf16 v[160:163], v[92:95], v[16:19], v[160:163]\n"
    "v_max3_u32 v184, v96, v98, v184\n"
    "v_swap_b32 v97, v98\n"
    "s_nop 1\n"
    "v_mfma_f32_16x16x32_bf16 v[156:159], v[96:99], a[12:15], v[156:159]\n"
    "v_mfma_f32_16x16x32_bf16 v[160:163], v[96:99], v[20:23], v[160:163]\n"
    "v_max3_u32 v184, v124, v126, v184\n"
    "v_smfmac_f32_16x16x64_bf16 v[148:151], v[124:127], a[16:23], v195\n"
    "v_smfmac_f32_16x16x64_bf16 v[152:155], v[124:127], v[24:31], v195\n"
    "v_max3_u32 v184, v100, v102, v184\n"
    "v_swap_b32 v101, v102\n"
    "s_nop 1\n"
    "v_mfma_f32_16x16x32_bf16 v[156:159], v[100:103], a[16:19], v[156:159]\n"
    "v_mfma_f32_16x16x32_bf16 v[160:163], v[100:103], v[24:27], v[160:163]\n"
    "v_max3_u32 v184, v104, v106, v184\n"
    "v_swap_b32 v105, v106\n"
    "s_nop 1\n"
    "v_mfma_f32_16x16x32_bf16 v[156:159], v[104:107], a[20:23], v[156:159]\n"
    "v_mfma_f32_16x16x32_bf16 v[160:163], v[104:107], v[28:31], v[160:163]\n"
    "v_max3_u32 v184, v128, v130, v184\n"
    "v_smfmac_f32_16x16x64_bf16 v[148:151], v[128:131], a[24:31], v195\n"
    "v_smfmac_f32_16x16x64_bf16 v[152:155], v[128:131], v[32:39], v195\n"
    "v_max3_u32 v184, v108, v110, v184\n"
    "v_swap_b32 v109, v110\n"
    "s_nop 1\n"
    "v_mfma_f32_16x16x32_bf16 v[156:159], v[108:111], a[24:27], v[156:159]\n"
    "v_mfma_f32_16x16x32_bf16 v[160:163], v[108:111], v[32:35], v[160:163]\n"
    "v_max3_u32 v184, v112, v114, v184\n"
    "v_swap_b32 v113, v114\n"
    "s_nop 1\n"
    "v_mfma_f32_16x16x32_bf16 v[156:159], v[112:115], a[28:31], v[156:159]\n"
    "v_mfma_f32_16x16x32_bf16 v[160:163], v[112:115], v[36:39], v[160:163]\n"
    "v_cmp_eq_u32 vcc, -1, v184\n"
    "s_nop 0\n"
    "s_cbranch_vccnz .Lct_tr0_%=\n"
    "s_nop 7\n"
    "s_branch .Lct_tg0_%=\n"
    ".Lct_xr1_%=:\n"
    "s_add_u32 s20, s20, 1\n"
    "s_cmp_lt_u32 s20, 0x40000\n"
    "s_cbranch_scc1 .Lct_sp28_%=\n"
    "v_mov_b32 v184, 42\n"
    "s_mov_b32 s24, 33728\n"
    "buffer_store_dword v184, off, s[0:3], s24 sc1\n"
    "s_waitcnt vmcnt(0)\n"
    "s_branch .Lct_xg1_%=\n"
    ".Lct_sp28_%=:\n"
    ".Lct_rt29_%=:\n"
    "s_memrealtime s[22:23]\n"
    "s_waitcnt lgkmcnt(0)\n"
    "s_sub_u32 s24, s43, s22\n"
    "s_cmp_lt_u32 s24, 600\n"
    "s_cbranch_scc1 .Lct_rt29_%=\n"
    "s_add_u32 s43, s22, 44\n"
    "buffer_load_dwordx2 v[190:191], off, s[0:3], s41 sc1\n"
    "s_waitcnt vmcnt(0)\n"
    "s_branch .Lct_x1_%=\n"
    ".Lct_retry1_%=:\n"
    "s_add_u32 s20, s20, 1\n"
    "s_cmp_lt_u32 s20, 0x40000\n"
    "s_cbranch_scc1 .Lct_sp30_%=\n"
    "v_mov_b32 v184, 3\n"
    "s_mov_b32 s24, 33728\n"
    "buffer_store_dword v184, off, s[0:3], s24 sc1\n"
    "s_waitcnt vmcnt(0)\n"
    "s_branch .Lct_go1_%=\n"
    ".Lct_sp30_%=:\n"
    "s_waitcnt vmcnt(0) lgkmcnt(0)\n"
    "v_mov_b64 v[76:77], 0\n"
    "v_mov_b64 v[78:79], 0\n"
    "v_mov_b64 v[80:81], 0\n"
    "v_mov_b64 v[82:83], 0\n"
    "buffer_load_dwordx4 v[40:43], v192, s[0:3], s32 offen offset:0 sc1\n"
    "buffer_load_dwordx4 v[44:47], v192, s[0:3], s32 offen offset:1024 sc1\n"
    "buffer_load_dwordx4 v[48:51], v197, s[0:3], s33 offen offset:0 sc1\n"
    "buffer_load_dwordx4 v[52:55], v197, s[0:3], s33 offen offset:256 sc1\n"
    "buffer_store_dwordx4 v[148:151], v211, s[0:3], 0 offen sc1\n"
    "buffer_load_dwordx4 v[72:75], v193, s[0:3], s34 offen sc1\n"
    "buffer_load_dwordx4 v[56:59], v192, s[0:3], s34 offen offset:0 sc1\n"
    "buffer_load_dwordx4 v[60:63], v192, s[0:3], s34 offen offset:1024 sc1\n"
    "buffer_load_dwordx4 v[64:67], v192, s[0:3], s34 offen offset:2048 sc1\n"
    "buffer_load_dwordx4 v[68:71], v192, s[0:3], s34 offen offset:3072 sc1\n"
    "s_waitcnt vmcnt(0)\n"
    "s_branch .Lct_take1_%=\n"
    ".Lct_tr1_%=:\n"
    "s_add_u32 s20, s20, 1\n"
    "s_cmp_lt_u32 s20, 0x40000\n"
    "s_cbranch_scc1 .Lct_sp31_%=\n"
    "v_mov_b32 v184, 4\n"
    "s_mov_b32 s24, 33728\n"
    "buffer_store_dword v184, off, s[0:3], s24 sc1\n"
    "s_waitcnt vmcnt(0)\n"
    "s_branch .Lct_tg1_%=\n"
    ".Lct_sp31_%=:\n"
    "buffer_load_dwordx4 v[132:135], v199, s[0:3], s35 offen offset:0 sc1\n"
    "buffer_load_dwordx4 v[136:139], v199, s[0:3], s37 offen offset:0 sc1\n"
    "buffer_load_dwordx4 v[140:143], v199, s[0:3], s35 offen offset:256 sc1\n"
    "buffer_load_dwordx4 v[144:147], v199, s[0:3], s37 offen offset:256 sc1\n"
    "buffer_load_dwordx4 v[116:119], v198, s[0:3], s35 offen sc1\n"
    "buffer_load_dwordx4 v[84:87], v192, s[0:3], s35 offen offset:0 sc1\n"
    "buffer_load_dwordx4 v[88:91], v192, s[0:3], s35 offen offset:2048 sc1\n"
    "buffer_load_dwordx4 v[120:123], v198, s[0:3], s36 offen sc1\n"
    "buffer_load_dwordx4 v[92:95], v192, s[0:3], s36 offen offset:0 sc1\n"
    "buffer_load_dwordx4 v[96:99], v192, s[0:3], s36 offen offset:2048 sc1\n"
    "buffer_load_dwordx4 v[124:127], v198, s[0:3], s37 offen sc1\n"
    "buffer_load_dwordx4 v[100:103], v192, s[0:3], s37 offen offset:0 sc1\n"
    "buffer_load_dwordx4 v[104:107], v192, s[0:3], s37 offen offset:2048 sc1\n"
    "buffer_load_dwordx4 v[128:131], v198, s[0:3], s38 offen sc1\n"
    "buffer_load_dwordx4 v[108:111], v192, s[0:3], s38 offen offset:0 sc1\n"
    "buffer_load_dwordx4 v[112:115], v192, s[0:3], s38 offen offset:2048 sc1\n"
    "s_waitcnt vmcnt(0)\n"
    "v_max3_u32 v184, v132, v134, v132\n"
    "v_swap_b32 v133, v134\n"
    "s_nop 1\n"
    "v_mfma_f32_16x16x32_bf16 v[148:151], v[132:135], v[40:43], 0\n"
    "v_mfma_f32_16x16x32_bf16 v[152:155], v[132:135], v[48:51], 0\n"
    "v_max3_u32 v184, v136, v138, v184\n"
    "v_swap_b32 v137, v138\n"
    "s_nop 1\n"
    "v_mfma_f32_16x16x32_bf16 v[148:151], v[136:139], v[44:47], v[148:151]\n"
    "v_mfma_f32_16x16x32_bf16 v[152:155], v[136:139], v[52:55], v[152:155]\n"
    "v_max3_u32 v184, v140, v142, v184\n"
    "v_swap_b32 v141, v142\n"
    "s_nop 1\n"
    "v_mfma_f32_16x16x32_bf16 v[156:159], v[140:143], v[40:43], 0\n"
    "v_mfma_f32_16x16x32_bf16 v[160:163], v[140:143], v[48:51], 0\n"
    "v_max3_u32 v184, v144, v146, v184\n"
    "v_swap_b32 v145, v146\n"
    "s_nop 1\n"
    "v_mfma_f32_16x16x32_bf16 v[156:159], v[144:147], v[44:47], v[156:159]\n"
    "v_mfma_f32_16x16x32_bf16 v[160:163], v[144:147], v[52:55], v[160:163]\n"
    "v_max3_u32 v184, v116, v118, v184\n"
    "v_smfmac_f32_16x16x64_bf16 v[148:151], v[116:119], a[32:39], v195\n"
    "v_smfmac_f32_16x16x64_bf16 v[152:155], v[116:119], v[8:15], v195\n"
    "v_max3_u32 v184, v84, v86, v184\n"
    "v_swap_b32 v85, v86\n"
    "s_nop 1\n"
    "v_mfma_f32_16x16x32_bf16 v[156:159], v[84:87], a[32:35], v[156:159]\n"
    "v_mfma_f32_16x16x32_bf16 v[160:163], v[84:87], v[8:11], v[160:163]\n"
    "v_max3_u32 v184, v88, v90, v184\n"
    "v_swap_b32 v89, v90\n"
    "s_nop 1\n"
    "v_mfma_f32_16x16x32_bf16 v[156:159], v[88:91], a[36:39], v[156:159]\n"
    "v_mfma_f32_16x16x32_bf16 v[160:163], v[88:91], v[12:15], v[160:163]\n"
    "v_max3_u32 v184, v120, v122, v184\n"
    "v_smfmac_f32_16x16x64_bf16 v[148:151], v[120:123], a[40:47], v195\n"
    "v_smfmac_f32_16x16x64_bf16 v[152:155], v[120:123], v[16:23], v195\n"
    "v_max3_u32 v184, v92, v94, v184\n"
    "v_swap_b32 v93, v94\n"
    "s_nop 1\n"
    "v_mfma_f32_16x16x32_bf16 v[156:159], v[92:95], a[40:43], v[156:159]\n"
    "v_mfma_f32_16x16x32_bf16 v[160:163], v[92:95], v[16:19], v[160:163]\n"
    "v_max3_u32 v184, v96, v98, v184\n"
    "v_swap_b32 v97, v98\n"
    "s_nop 1\n"
    "v_mfma_f32_16x16x32_bf16 v[156:159], v[96:99], a[44:47], v[156:159]\n"
    "v_mfma_f32_16x16x32_bf16 v[160:163], v[96:99], v[20:23], v[160:163]\n"
    "v_max3_u32 v184, v124, v126, v184\n"
    "v_smfmac_f32_16x16x64_bf16 v[148:151], v[124:127], a[48:55], v195\n"
    "v_smfmac_f32_16x16x64_bf16 v[152:155], v[124:127], v[24:31], v195\n"
    "v_max3_u32 v184, v100, v102, v184\n"
    "v_swap_b32 v101, v102\n"
    "s_nop 1\n"
    "v_mfma_f32_16x16x32_bf16 v[156:159], v[100:103], a[48:51], v[156:159]\n"
    "v_mfma_f32_16x16x32_bf16 v[160:163], v[100:103], v[24:27], v[160:163]\n"
    "v_max3_u32 v184, v104, v106, v184\n"
    "v_swap_b32 v105, v106\n"
    "s_nop 1\n"
    "v_mfma_f32_16x16x32_bf16 v[156:159], v[104:107], a[52:55], v[156:159]\n"
    "v_mfma_f32_16x16x32_bf16 v[160:163], v[104:107], v[28:31], v[160:163]\n"
    "v_max3_u32 v184, v128, v130, v184\n"
    "v_smfmac_f32_16x16x64_bf16 v[148:151], v[128:131], a[56:63], v195\n"
    "v_smfmac_f32_16x16x64_bf16 v[152:155], v[128:131], v[32:39], v195\n"
    "v_max3_u32 v184, v108, v110, v184\n"
    "v_swap_b32 v109, v110\n"
    "s_nop 1\n"
    "v_mfma_f32_16x16x32_bf16 v[156:159], v[108:111], a[56:59], v[156:159]\n"
    "v_mfma_f32_16x16x32_bf16 v[160:163], v[108:111], v[32:35], v[160:163]\n"
    "v_max3_u32 v184, v112, v114, v184\n"
    "v_swap_b32 v113, v114\n"
    "s_nop 1\n"
    "v_mfma_f32_16x16x32_bf16 v[156:159], v[112:115], a[60:63], v[156:159]\n"
    "v_mfma_f32_16x16x32_bf16 v[160:163], v[112:115], v[36:39], v[160:163]\n"
    "v_cmp_eq_u32 vcc, -1, v184\n"
    "s_nop 0\n"
    "s_cbranch_vccnz .Lct_tr1_%=\n"
    "s_nop 7\n"
    "s_branch .Lct_tg1_%=\n"
    :: "{s[0:3]}"(s0_), "{s[4:7]}"(s4_), "{s[8:11]}"(s8_), "{s[12:15]}"(s12_), "{s[16:19]}"(s16_), "{v[192:195]}"(v192_), "{v[196:199]}"(v196_), "{v[200:203]}"(v200_), "{v[204:207]}"(v204_), "{v[208:211]}"(v208_), "{v[212:215]}"(v212_) : "memory", "v255", "a255", "s95");
    __builtin_unreachable();
}
DEV void qw_role(const MegaArgs& A, const int L, const int h, const int rep) {
    ROLE_HEAD;
    const u32x4 s0_ = {RFL(rx[0]), RFL(rx[1]), RFL(rx[2]), RFL(rx[3])};
    const u32x4 s4_ = {RFL(lo(A.Wq + h * 192 + 64)), RFL(hi(A.Wq + h * 192 + 64)), RFL(WBYTES - h * 384 - 128), RFL(0x20000u)};
    const u32x4 s8_ = {RFL(lo(A.Wv + h * 192 + 128)), RFL(hi(A.Wv + h * 192 + 128)), RFL(WBYTES - h * 384 - 256), RFL(0x20000u)};
    const u32x4 s12_ = {RFL(w), RFL(REC0 + w * 2048), RFL(QT0 + h * 32768), RFL((A.layers & 2) ? 64 : 0)};
    const u32x4 s16_ = {RFL(xl(Z_CT + (h & 7) * 256, L)), RFL(xl(Z_XN + rep * 2048, 0)), RFL(DBG0 + ((L * 128 + 112 + (h >> 3) * 4 + (h & 3)) * 4 + w) * 2048), RFL(0u)};
    const u32x4 v0_ = {(unsigned)((8 * w + (l >> 3)) * 12288 + ((l & 7) ^ (((8 * w + (l >> 3)) >> 1) & 6)) * 16), (unsigned)((8 * w + (l >> 3)) * 12288 + ((l & 7) ^ (((8 * w + (l >> 3)) >> 1) & 6)) * 16 + 128), (unsigned)((8 * lq + ((l >> 2) & 1) + ((l >> 1) & 4)) * 128 + (w ^ (((8 * lq + ((l >> 2) & 1) + ((l >> 1) & 4)) >> 2) & 3)) * 32 + (l & 3) * 8), (unsigned)(0u)};
    const u32x4 v64_ = {(unsigned)(l * 16), (unsigned)(l * 16 + 40960), (unsigned)(l * 16 + w * 2048), (unsigned)(l * 16 + w * 1024 + 16384)};
    const u32x4 v68_ = {(unsigned)(l < 20 ? (l >> 2) * 128 + (l & 3) * 4 : 0), (unsigned)(l == 0 ? xl(Z_QW + h * 128 + w * 4, 0) : OOB), (unsigned)(0u), (unsigned)(0u)};
    asm volatile(
    "s_mov_b32 s20, 0\n"
    "s_mov_b64 s[22:23], 0\n"
    "s_mov_b32 s25, 0\n"
    "s_lshl_b32 s32, s12, 10\n"
    "s_mov_b32 s38, 0\n"
    "s_add_u32 m0, s32, 0\n"
    "s_nop 0\n"
    "buffer_load_dwordx4 v0, s[4:7], s38 offen nt lds\n"
    "s_add_u32 m0, s32, 4096\n"
    "s_nop 0\n"
    "buffer_load_dwordx4 v1, s[4:7], s38 offen nt lds\n"
    "s_add_u32 m0, s32, 8192\n"
    "s_nop 0\n"
    "buffer_load_dwordx4 v0, s[8:11], s38 offen nt lds\n"
    "s_mov_b32 s38, 1572864\n"
    "s_add_u32 m0, s32, 12288\n"
    "s_nop 0\n"
    "buffer_load_dwordx4 v0, s[4:7], s38 offen nt lds\n"
    "s_add_u32 m0, s32, 16384\n"
    "s_nop 0\n"
    "buffer_load_dwordx4 v1, s[4:7], s38 offen nt lds\n"
    "s_add_u32 m0, s32, 20480\n"
    "s_nop 0\n"
    "buffer_load_dwordx4 v0, s[8:11], s38 offen nt lds\n"
    "s_mov_b32 s38, 3145728\n"
    "s_add_u32 m0, s32, 24576\n"
    "s_nop 0\n"
    "buffer_load_dwordx4 v0, s[4:7], s38 offen nt lds\n"
    "s_add_u32 m0, s32, 28672\n"
    "s_nop 0\n"
    "buffer_load_dwordx4 v1, s[4:7], s38 offen nt lds\n"
    "s_add_u32 m0, s32, 32768\n"
    "s_nop 0\n"
    "buffer_load_dwordx4 v0, s[8:11], s38 offen nt lds\n"
    "s_mov_b32 s38, 4718592\n"
    "s_add_u32 m0, s32, 36864\n"
    "s_nop 0\n"
    "buffer_load_dwordx4 v0, s[4:7], s38 offen nt lds\n"
    "s_add_u32 m0, s32, 40960\n"
    "s_nop 0\n"
    "buffer_load_dwordx4 v1, s[4:7], s38 offen nt lds\n"
    "s_add_u32 m0, s32, 45056\n"
    "s_nop 0\n"
    "buffer_load_dwordx4 v0, s[8:11], s38 offen nt lds\n"
    "s_mov_b32 s38, 393216\n"
    "s_add_u32 m0, s32, 49152\n"
    "s_nop 0\n"
    "buffer_load_dwordx4 v0, s[4:7], s38 offen nt lds\n"
    "s_add_u32 m0, s32, 53248\n"
    "s_nop 0\n"
    "buffer_load_dwordx4 v1, s[4:7], s38 offen nt lds\n"
    "s_add_u32 m0, s32, 57344\n"
    "s_nop 0\n"
    "buffer_load_dwordx4 v0, s[8:11], s38 offen nt lds\n"
    "s_mov_b32 s38, 1966080\n"
    "s_add_u32 m0, s32, 61440\n"
    "s_nop 0\n"
    "buffer_load_dwordx4 v0, s[4:7], s38 offen nt lds\n"
    "s_add_u32 m0, s32, 65536\n"
    "s_nop 0\n"
    "buffer_load_dwordx4 v1, s[4:7], s38 offen nt lds\n"
    "s_add_u32 m0, s32, 69632\n"
    "s_nop 0\n"
    "buffer_load_dwordx4 v0, s[8:11], s38 offen nt lds\n"
    "s_mov_b32 s38, 3538944\n"
    "s_add_u32 m0, s32, 73728\n"
    "s_nop 0\n"
    "buffer_load_dwordx4 v0, s[4:7], s38 offen nt lds\n"
    "s_add_u32 m0, s32, 77824\n"
    "s_nop 0\n"
    "buffer_load_dwordx4 v1, s[4:7], s38 offen nt lds\n"
    "s_add_u32 m0, s32, 81920\n"
    "s_nop 0\n"
    "buffer_load_dwordx4 v0, s[8:11], s38 offen nt lds\n"
    "s_mov_b32 s38, 5111808\n"
    "s_add_u32 m0, s32, 86016\n"
    "s_nop 0\n"
    "buffer_load_dwordx4 v0, s[4:7], s38 offen nt lds\n"
    "s_add_u32 m0, s32, 90112\n"
    "s_nop 0\n"
    "buffer_load_dwordx4 v1, s[4:7], s38 offen nt lds\n"
    "s_add_u32 m0, s32, 94208\n"
    "s_nop 0\n"
    "buffer_load_dwordx4 v0, s[8:11], s38 offen nt lds\n"
    "s_mov_b32 s38, 786432\n"
    "s_add_u32 m0, s32, 98304\n"
    "s_nop 0\n"
    "buffer_load_dwordx4 v0, s[4:7], s38 offen nt lds\n"
    "s_add_u32 m0, s32, 102400\n"
    "s_nop 0\n"
    "buffer_load_dwordx4 v1, s[4:7], s38 offen nt lds\n"
    "s_add_u32 m0, s32, 106496\n"
    "s_nop 0\n"
    "buffer_load_dwordx4 v0, s[8:11], s38 offen nt lds\n"
    "s_mov_b32 s38, 2359296\n"
    "s_add_u32 m0, s32, 110592\n"
    "s_nop 0\n"
    "buffer_load_dwordx4 v0, s[4:7], s38 offen nt lds\n"
    "s_add_u32 m0, s32, 114688\n"
    "s_nop 0\n"
    "buffer_load_dwordx4 v1, s[4:7], s38 offen nt lds\n"
    "s_add_u32 m0, s32, 118784\n"
    "s_nop 0\n"
    "buffer_load_dwordx4 v0, s[8:11], s38 offen nt lds\n"
    "s_mov_b32 s38, 3932160\n"
    "s_add_u32 m0, s32, 122880\n"
    "s_nop 0\n"
    "buffer_load_dwordx4 v0, s[4:7], s38 offen nt lds\n"
    "s_add_u32 m0, s32, 126976\n"
    "s_nop 0\n"
    "buffer_load_dwordx4 v1, s[4:7], s38 offen nt lds\n"
    "s_add_u32 m0, s32, 131072\n"
    "s_nop 0\n"
    "buffer_load_dwordx4 v0, s[8:11], s38 offen nt lds\n"
    "s_mov_b32 s38, 5505024\n"
    "s_add_u32 m0, s32, 135168\n"
    "s_nop 0\n"
    "buffer_load_dwordx4 v0, s[4:7], s38 offen nt lds\n"
    "s_add_u32 m0, s32, 139264\n"
    "s_nop 0\n"
    "buffer_load_dwordx4 v1, s[4:7], s38 offen nt lds\n"
    "s_add_u32 m0, s32, 143360\n"
    "s_nop 0\n"
    "buffer_load_dwordx4 v0, s[8:11], s38 offen nt lds\n"
    "s_waitcnt vmcnt(24)\n"
    "s_barrier\n"
    "v_add_u32 v8, 0, v2\n"
    "ds_read_b64_tr_b16 a[0:1], v8 offset:0\n"
    "ds_read_b64_tr_b16 a[2:3], v8 offset:256\n"
    "ds_read_b64_tr_b16 a[80:81], v8 offset:4096\n"
    "ds_read_b64_tr_b16 a[82:83], v8 offset:4352\n"
    "ds_read_b64_tr_b16 a[160:161], v8 offset:8192\n"
    "ds_read_b64_tr_b16 a[162:163], v8 offset:8448\n"
    "ds_read_b64_tr_b16 a[16:17], v8 offset:12288\n"
    "ds_read_b64_tr_b16 a[18:19], v8 offset:12544\n"
    "ds_read_b64_tr_b16 a[96:97], v8 offset:16384\n"
    "ds_read_b64_tr_b16 a[98:99], v8 offset:16640\n"
    "ds_read_b64_tr_b16 a[176:177], v8 offset:20480\n"
    "ds_read_b64_tr_b16 a[178:179], v8 offset:20736\n"
    "ds_read_b64_tr_b16 a[32:33], v8 offset:24576\n"
    "ds_read_b64_tr_b16 a[34:35], v8 offset:24832\n"
    "ds_read_b64_tr_b16 a[112:113], v8 offset:28672\n"
    "ds_read_b64_tr_b16 a[114:115], v8 offset:28928\n"
    "ds_read_b64_tr_b16 a[192:193], v8 offset:32768\n"
    "ds_read_b64_tr_b16 a[194:195], v8 offset:33024\n"
    "ds_read_b64_tr_b16 a[48:49], v8 offset:36864\n"
    "ds_read_b64_tr_b16 a[50:51], v8 offset:37120\n"
    "ds_read_b64_tr_b16 a[128:129], v8 offset:40960\n"
    "ds_read_b64_tr_b16 a[130:131], v8 offset:41216\n"
    "ds_read_b64_tr_b16 a[208:209], v8 offset:45056\n"
    "ds_read_b64_tr_b16 a[210:211], v8 offset:45312\n"
    "s_waitcnt lgkmcnt(0)\n"
    "s_barrier\n"
    "s_mov_b32 s38, 1179648\n"
    "s_add_u32 m0, s32, 0\n"
    "s_nop 0\n"
    "buffer_load_dwordx4 v0, s[4:7], s38 offen nt lds\n"
    "s_add_u32 m0, s32, 4096\n"
    "s_nop 0\n"
    "buffer_load_dwordx4 v1, s[4:7], s38 offen nt lds\n"
    "s_add_u32 m0, s32, 8192\n"
    "s_nop 0\n"
    "buffer_load_dwordx4 v0, s[8:11], s38 offen nt lds\n"
    "s_mov_b32 s38, 2752512\n"
    "s_add_u32 m0, s32, 12288\n"
    "s_nop 0\n"
    "buffer_load_dwordx4 v0, s[4:7], s38 offen nt lds\n"
    "s_add_u32 m0, s32, 16384\n"
    "s_nop 0\n"
    "buffer_load_dwordx4 v1, s[4:7], s38 offen nt lds\n"
    "s_add_u32 m0, s32, 20480\n"
    "s_nop 0\n"
    "buffer_load_dwordx4 v0, s[8:11], s38 offen nt lds\n"
    "s_mov_b32 s38, 4325376\n"
    "s_add_u32 m0, s32, 24576\n"
    "s_nop 0\n"
    "buffer_load_dwordx4 v0, s[4:7], s38 offen nt lds\n"
    "s_add_u32 m0, s32, 28672\n"
    "s_nop 0\n"
    "buffer_load_dwordx4 v1, s[4:7], s38 offen nt lds\n"
    "s_add_u32 m0, s32, 32768\n"
    "s_nop 0\n"
    "buffer_load_dwordx4 v0, s[8:11], s38 offen nt lds\n"
    "s_mov_b32 s38, 5898240\n"
    "s_add_u32 m0, s32, 36864\n"
    "s_nop 0\n"
    "buffer_load_dwordx4 v0, s[4:7], s38 offen nt lds\n"
    "s_add_u32 m0, s32, 40960\n"
    "s_nop 0\n"
    "buffer_load_dwordx4 v1, s[4:7], s38 offen nt lds\n"
    "s_add_u32 m0, s32, 45056\n"
    "s_nop 0\n"
    "buffer_load_dwordx4 v0, s[8:11], s38 offen nt lds\n"
    "v_and_b32 v4, 0x70, v64\n"
    "v_lshlrev_b32 v4, 2, v4\n"
    "v_or_b32 v4, 0x8000, v4\n"
    "buffer_load_dword v5, v4, s[0:3], 0 offen sc1\n"
    "s_waitcnt vmcnt(25)\n"
    "s_barrier\n"
    "v_add_u32 v8, 49152, v2\n"
    "ds_read_b64_tr_b16 a[4:5], v8 offset:0\n"
    "ds_read_b64_tr_b16 a[6:7], v8 offset:256\n"
    "ds_read_b64_tr_b16 a[84:85], v8 offset:4096\n"
    "ds_read_b64_tr_b16 a[86:87], v8 offset:4352\n"
    "ds_read_b64_tr_b16 a[164:165], v8 offset:8192\n"
    "ds_read_b64_tr_b16 a[166:167], v8 offset:8448\n"
    "ds_read_b64_tr_b16 a[20:21], v8 offset:12288\n"
    "ds_read_b64_tr_b16 a[22:23], v8 offset:12544\n"
    "ds_read_b64_tr_b16 a[100:101], v8 offset:16384\n"
    "ds_read_b64_tr_b16 a[102:103], v8 offset:16640\n"
    "ds_read_b64_tr_b16 a[180:181], v8 offset:20480\n"
    "ds_read_b64_tr_b16 a[182:183], v8 offset:20736\n"
    "ds_read_b64_tr_b16 a[36:37], v8 offset:24576\n"
    "ds_read_b64_tr_b16 a[38:39], v8 offset:24832\n"
    "ds_read_b64_tr_b16 a[116:117], v8 offset:28672\n"
    "ds_read_b64_tr_b16 a[118:119], v8 offset:28928\n"
    "ds_read_b64_tr_b16 a[196:197], v8 offset:32768\n"
    "ds_read_b64_tr_b16 a[198:199], v8 offset:33024\n"
    "ds_read_b64_tr_b16 a[52:53], v8 offset:36864\n"
    "ds_read_b64_tr_b16 a[54:55], v8 offset:37120\n"
    "ds_read_b64_tr_b16 a[132:133], v8 offset:40960\n"
    "ds_read_b64_tr_b16 a[134:135], v8 offset:41216\n"
    "ds_read_b64_tr_b16 a[212:213], v8 offset:45056\n"
    "ds_read_b64_tr_b16 a[214:215], v8 offset:45312\n"
    "s_waitcnt lgkmcnt(0)\n"
    "s_barrier\n"
    "s_mov_b32 s38, 6291456\n"
    "s_add_u32 m0, s32, 49152\n"
    "s_nop 0\n"
    "buffer_load_dwordx4 v0, s[4:7], s38 offen nt lds\n"
    "s_add_u32 m0, s32, 53248\n"
    "s_nop 0\n"
    "buffer_load_dwordx4 v1, s[4:7], s38 offen nt lds\n"
    "s_add_u32 m0, s32, 57344\n"
    "s_nop 0\n"
    "buffer_load_dwordx4 v0, s[8:11], s38 offen nt lds\n"
    "s_mov_b32 s38, 6684672\n"
    "s_add_u32 m0, s32, 61440\n"
    "s_nop 0\n"
    "buffer_load_dwordx4 v0, s[4:7], s38 offen nt lds\n"
    "s_add_u32 m0, s32, 65536\n"
    "s_nop 0\n"
    "buffer_load_dwordx4 v1, s[4:7], s38 offen nt lds\n"
    "s_add_u32 m0, s32, 69632\n"
    "s_nop 0\n"
    "buffer_load_dwordx4 v0, s[8:11], s38 offen nt lds\n"
    "s_mov_b32 s38, 7077888\n"
    "s_add_u32 m0, s32, 73728\n"
    "s_nop 0\n"
    "buffer_load_dwordx4 v0, s[4:7], s38 offen nt lds\n"
    "s_add_u32 m0, s32, 77824\n"
    "s_nop 0\n"
    "buffer_load_dwordx4 v1, s[4:7], s38 offen nt lds\n"
    "s_add_u32 m0, s32, 81920\n"
    "s_nop 0\n"
    "buffer_load_dwordx4 v0, s[8:11], s38 offen nt lds\n"
    "s_mov_b32 s38, 7471104\n"
    "s_add_u32 m0, s32, 86016\n"
    "s_nop 0\n"
    "buffer_load_dwordx4 v0, s[4:7], s38 offen nt lds\n"
    "s_add_u32 m0, s32, 90112\n"
    "s_nop 0\n"
    "buffer_load_dwordx4 v1, s[4:7], s38 offen nt lds\n"
    "s_add_u32 m0, s32, 94208\n"
    "s_nop 0\n"
    "buffer_load_dwordx4 v0, s[8:11], s38 offen nt lds\n"
    "s_waitcnt vmcnt(25)\n"
    "s_barrier\n"
    "v_add_u32 v8, 98304, v2\n"
    "ds_read_b64_tr_b16 a[8:9], v8 offset:0\n"
    "ds_read_b64_tr_b16 a[10:11], v8 offset:256\n"
    "ds_read_b64_tr_b16 a[88:89], v8 offset:4096\n"
    "ds_read_b64_tr_b16 a[90:91], v8 offset:4352\n"
    "ds_read_b64_tr_b16 a[168:169], v8 offset:8192\n"
    "ds_read_b64_tr_b16 a[170:171], v8 offset:8448\n"
    "ds_read_b64_tr_b16 a[24:25], v8 offset:12288\n"
    "ds_read_b64_tr_b16 a[26:27], v8 offset:12544\n"
    "ds_read_b64_tr_b16 a[104:105], v8 offset:16384\n"
    "ds_read_b64_tr_b16 a[106:107], v8 offset:16640\n"
    "ds_read_b64_tr_b16 a[184:185], v8 offset:20480\n"
    "ds_read_b64_tr_b16 a[186:187], v8 offset:20736\n"
    "ds_read_b64_tr_b16 a[40:41], v8 offset:24576\n"
    "ds_read_b64_tr_b16 a[42:43], v8 offset:24832\n"
    "ds_read_b64_tr_b16 a[120:121], v8 offset:28672\n"
    "ds_read_b64_tr_b16 a[122:123], v8 offset:28928\n"
    "ds_read_b64_tr_b16 a[200:201], v8 offset:32768\n"
    "ds_read_b64_tr_b16 a[202:203], v8 offset:33024\n"
    "ds_read_b64_tr_b16 a[56:57], v8 offset:36864\n"
    "ds_read_b64_tr_b16 a[58:59], v8 offset:37120\n"
    "ds_read_b64_tr_b16 a[136:137], v8 offset:40960\n"
    "ds_read_b64_tr_b16 a[138:139], v8 offset:41216\n"
    "ds_read_b64_tr_b16 a[216:217], v8 offset:45056\n"
    "ds_read_b64_tr_b16 a[218:219], v8 offset:45312\n"
    "s_waitcnt lgkmcnt(0)\n"
    "s_barrier\n"
    "s_waitcnt vmcnt(13)\n"
    "s_barrier\n"
    "v_add_u32 v8, 0, v2\n"
    "ds_read_b64_tr_b16 a[12:13], v8 offset:0\n"
    "ds_read_b64_tr_b16 a[14:15], v8 offset:256\n"
    "ds_read_b64_tr_b16 a[92:93], v8 offset:4096\n"
    "ds_read_b64_tr_b16 a[94:95], v8 offset:4352\n"
    "ds_read_b64_tr_b16 a[172:173], v8 offset:8192\n"
    "ds_read_b64_tr_b16 a[174:175], v8 offset:8448\n"
    "ds_read_b64_tr_b16 a[28:29], v8 offset:12288\n"
    "ds_read_b64_tr_b16 a[30:31], v8 offset:12544\n"
    "ds_read_b64_tr_b16 a[108:109], v8 offset:16384\n"
    "ds_read_b64_tr_b16 a[110:111], v8 offset:16640\n"
    "ds_read_b64_tr_b16 a[188:189], v8 offset:20480\n"
    "ds_read_b64_tr_b16 a[190:191], v8 offset:20736\n"
    "ds_read_b64_tr_b16 a[44:45], v8 offset:24576\n"
    "ds_read_b64_tr_b16 a[46:47], v8 offset:24832\n"
    "ds_read_b64_tr_b16 a[124:125], v8 offset:28672\n"
    "ds_read_b64_tr_b16 a[126:127], v8 offset:28928\n"
    "ds_read_b64_tr_b16 a[204:205], v8 offset:32768\n"
    "ds_read_b64_tr_b16 a[206:207], v8 offset:33024\n"
    "ds_read_b64_tr_b16 a[60:61], v8 offset:36864\n"
    "ds_read_b64_tr_b16 a[62:63], v8 offset:37120\n"
    "ds_read_b64_tr_b16 a[140:141], v8 offset:40960\n"
    "ds_read_b64_tr_b16 a[142:143], v8 offset:41216\n"
    "ds_read_b64_tr_b16 a[220:221], v8 offset:45056\n"
    "ds_read_b64_tr_b16 a[222:223], v8 offset:45312\n"
    "s_waitcnt lgkmcnt(0)\n"
    "s_barrier\n"
    "s_waitcnt vmcnt(0)\n"
    "s_barrier\n"
    "v_add_u32 v8, 49152, v2\n"
    "ds_read_b64_tr_b16 a[64:65], v8 offset:0\n"
    "ds_read_b64_tr_b16 a[66:67], v8 offset:256\n"
    "ds_read_b64_tr_b16 a[144:145], v8 offset:4096\n"
    "ds_read_b64_tr_b16 a[146:147], v8 offset:4352\n"
    "ds_read_b64_tr_b16 a[224:225], v8 offset:8192\n"
    "ds_read_b64_tr_b16 a[226:227], v8 offset:8448\n"
    "ds_read_b64_tr_b16 a[68:69], v8 offset:12288\n"
    "ds_read_b64_tr_b16 a[70:71], v8 offset:12544\n"
    "ds_read_b64_tr_b16 a[148:149], v8 offset:16384\n"
    "ds_read_b64_tr_b16 a[150:151], v8 offset:16640\n"
    "ds_read_b64_tr_b16 a[228:229], v8 offset:20480\n"
    "ds_read_b64_tr_b16 a[230:231], v8 offset:20736\n"
    "ds_read_b64_tr_b16 a[72:73], v8 offset:24576\n"
    "ds_read_b64_tr_b16 a[74:75], v8 offset:24832\n"
    "ds_read_b64_tr_b16 a[152:153], v8 offset:28672\n"
    "ds_read_b64_tr_b16 a[154:155], v8 offset:28928\n"
    "ds_read_b64_tr_b16 a[232:233], v8 offset:32768\n"
    "ds_read_b64_tr_b16 a[234:235], v8 offset:33024\n"
    "ds_read_b64_tr_b16 a[76:77], v8 offset:36864\n"
    "ds_read_b64_tr_b16 a[78:79], v8 offset:37120\n"
    "ds_read_b64_tr_b16 a[156:157], v8 offset:40960\n"
    "ds_read_b64_tr_b16 a[158:159], v8 offset:41216\n"
    "ds_read_b64_tr_b16 a[236:237], v8 offset:45056\n"
    "ds_read_b64_tr_b16 a[238:239], v8 offset:45312\n"
    "s_waitcnt lgkmcnt(0)\n"
    "s_barrier\n"
    "s_waitcnt vmcnt(12)\n"
    "v_cmp_gt_i32 vcc, 32, v5\n"
    "s_nop 0\n"
    "s_cbranch_vccnz .Lqw_tk0_%=\n"
    ".Lqw_tk1_%=:\n"
    "s_mov_b32 s26, -2\n"
    "v_lshrrev_b32 v4, 2, v64\n"
    ".Lqw_po4_%=:\n"
    "buffer_load_dword v8, v4, s[0:3], s16 offen sc1\n"
    "s_waitcnt vmcnt(0)\n"
    "v_cmp_gt_i32 vcc, 1, v8\n"
    "s_nop 0\n"
    "s_cbranch_vccz .Lqw_pd5_%=\n"
    "s_add_u32 s20, s20, 1\n"
    "s_cmp_lt_u32 s20, 0x40000\n"
    "s_cbranch_scc1 .Lqw_sp6_%=\n"
    "v_mov_b32 v8, 50\n"
    "s_mov_b32 s24, 33728\n"
    "buffer_store_dword v8, off, s[0:3], s24 sc1\n"
    "s_waitcnt vmcnt(0)\n"
    "s_branch .Lqw_pd5_%=\n"
    ".Lqw_sp6_%=:\n"
    "s_sleep 4\n"
    "s_branch .Lqw_po4_%=\n"
    ".Lqw_pd5_%=:\n"
    "s_mov_b32 s26, -1\n"
    "s_mov_b32 s35, 0\n"
    "s_lshl_b32 s37, s12, 11\n"
    "s_mov_b32 s45, 0\n"
    "v_mov_b32 v60, 0\n"
    "s_mov_b32 s43, 2130706432\n"
    "s_mov_b32 s41, 2130706432\n"
    "s_mov_b32 s46, 2130706432\n"
    "s_mov_b32 s34, 2130706432\n"
    "s_add_u32 s26, s26, 1\n"
    "s_sub_u32 s27, s26, 14\n"
    "s_ashr_i32 s28, s27, 1\n"
    "s_and_b32 s29, s27, 1\n"
    "s_add_u32 s30, s28, 14\n"
    "s_cmp_lt_i32 s27, 0\n"
    "s_cselect_b32 s29, 0, s29\n"
    "s_cselect_b32 s30, s26, s30\n"
    "s_cmp_eq_u32 s29, 1\n"
    "s_cselect_b32 s30, s28, s30\n"
    "s_cselect_b32 s27, s15, 64\n"
    "s_lshl_b32 s31, s29, 13\n"
    "s_add_u32 s40, s30, 1\n"
    "s_lshl_b32 s32, s30, 19\n"
    "s_lshl_b32 s25, s30, 11\n"
    "s_xor_b32 s25, s25, s31\n"
    "s_and_b32 s25, s25, 0x2000\n"
    "s_add_u32 s32, s32, s25\n"
    "s_add_u32 s32, s32, s13\n"
    "s_lshl_b32 s33, s30, 20\n"
    "s_lshl_b32 s25, s30, 12\n"
    "s_xor_b32 s25, s25, s31\n"
    "s_and_b32 s25, s25, 0x2000\n"
    "s_add_u32 s33, s33, s25\n"
    "s_add_u32 s33, s33, s14\n"
    "s_mul_i32 s47, s40, s29\n"
    "s_cmp_lt_u32 s30, s27\n"
    "s_cselect_b32 s36, 1, 0\n"
    "s_cselect_b32 s32, s32, 2130706432\n"
    "s_cmp_lt_u32 s30, s27\n"
    "s_cselect_b32 s39, s31, 2130706432\n"
    "s_cselect_b32 s47, s47, 0\n"
    "s_cmp_gt_u32 s47, 0\n"
    "s_cselect_b32 s38, s17, 2130706432\n"
    ".Lqw_top_%=:\n"
    "ds_read_b128 v[24:27], v65 offset:0\n"
    "ds_read_b128 v[28:31], v65 offset:1024\n"
    "ds_read_b128 v[32:35], v65 offset:2048\n"
    "ds_read_b128 v[36:39], v65 offset:3072\n"
    "ds_read_b128 v[40:43], v65 offset:4096\n"
    "ds_read_b128 v[44:47], v65 offset:5120\n"
    "v_cmp_gt_i32 vcc, s47, v60\n"
    "s_nop 0\n"
    "s_cbranch_vccnz .Lqw_wx_%=\n"
    ".Lqw_wxb_%=:\n"
    "s_cmp_eq_u32 s35, 0\n"
    "s_cbranch_scc1 .Lqw_skip_%=\n"
    ".Lqw_mk0x6_%=:\n"
    "s_waitcnt lgkmcnt(4)\n"
    "v_mfma_f32_16x16x32_bf16 v[0:3], a[0:3], v[24:27], 0\n"
    "buffer_store_dwordx4 v[48:51], v66, s[0:3], s46 offen offset:0 sc1\n"
    "v_mfma_f32_16x16x32_bf16 v[4:7], a[0:3], v[28:31], 0\n"
    "v_mfma_f32_16x16x32_bf16 v[8:11], a[80:83], v[24:27], 0\n"
    "v_mfma_f32_16x16x32_bf16 v[12:15], a[80:83], v[28:31], 0\n"
    "v_mfma_f32_16x16x32_bf16 v[16:19], a[160:163], v[24:27], 0\n"
    "ds_read_b128 v[24:27], v65 offset:6144\n"
    "v_mfma_f32_16x16x32_bf16 v[20:23], a[160:163], v[28:31], 0\n"
    "ds_read_b128 v[28:31], v65 offset:7168\n"
    "s_waitcnt lgkmcnt(4)\n"
    "v_mfma_f32_16x16x32_bf16 v[0:3], a[4:7], v[32:35], v[0:3]\n"
    "v_mfma_f32_16x16x32_bf16 v[4:7], a[4:7], v[36:39], v[4:7]\n"
    "buffer_store_dwordx4 v[52:55], v66, s[0:3], s46 offen offset:1024 sc1\n"
    "v_mfma_f32_16x16x32_bf16 v[8:11], a[84:87], v[32:35], v[8:11]\n"
    "v_mfma_f32_16x16x32_bf16 v[12:15], a[84:87], v[36:39], v[12:15]\n"
    "v_mfma_f32_16x16x32_bf16 v[16:19], a[164:167], v[32:35], v[16:19]\n"
    "ds_read_b128 v[32:35], v65 offset:8192\n"
    "v_mfma_f32_16x16x32_bf16 v[20:23], a[164:167], v[36:39], v[20:23]\n"
    "ds_read_b128 v[36:39], v65 offset:9216\n"
    "s_waitcnt lgkmcnt(4)\n"
    "v_mfma_f32_16x16x32_bf16 v[0:3], a[8:11], v[40:43], v[0:3]\n"
    "v_mfma_f32_16x16x32_bf16 v[4:7], a[8:11], v[44:47], v[4:7]\n"
    "v_mfma_f32_16x16x32_bf16 v[8:11], a[88:91], v[40:43], v[8:11]\n"
    "buffer_store_dwordx4 v[56:59], v67, s[0:3], s46 offen sc1\n"
    "s_add_u32 m0, s37, 0\n"
    "v_mfma_f32_16x16x32_bf16 v[12:15], a[88:91], v[44:47], v[12:15]\n"
    "v_mfma_f32_16x16x32_bf16 v[16:19], a[168:171], v[40:43], v[16:19]\n"
    "ds_read_b128 v[40:43], v65 offset:10240\n"
    "v_mfma_f32_16x16x32_bf16 v[20:23], a[168:171], v[44:47], v[20:23]\n"
    "ds_read_b128 v[44:47], v65 offset:11264\n"
    "s_waitcnt lgkmcnt(4)\n"
    "v_mfma_f32_16x16x32_bf16 v[0:3], a[12:15], v[24:27], v[0:3]\n"
    "v_mfma_f32_16x16x32_bf16 v[4:7], a[12:15], v[28:31], v[4:7]\n"
    "v_mfma_f32_16x16x32_bf16 v[8:11], a[92:95], v[24:27], v[8:11]\n"
    "v_mfma_f32_16x16x32_bf16 v[12:15], a[92:95], v[28:31], v[12:15]\n"
    "buffer_load_dwordx4 v64, s[0:3], s32 offen sc1 lds\n"
    "v_mfma_f32_16x16x32_bf16 v[16:19], a[172:175], v[24:27], v[16:19]\n"
    "ds_read_b128 v[24:27], v65 offset:12288\n"
    "v_mfma_f32_16x16x32_bf16 v[20:23], a[172:175], v[28:31], v[20:23]\n"
    "ds_read_b128 v[28:31], v65 offset:13312\n"
    "s_waitcnt lgkmcnt(4)\n"
    "v_mfma_f32_16x16x32_bf16 v[0:3], a[16:19], v[32:35], v[0:3]\n"
    "v_mfma_f32_16x16x32_bf16 v[4:7], a[16:19], v[36:39], v[4:7]\n"
    "buffer_load_dwordx4 v64, s[0:3], s32 offen offset:1024 sc1 lds\n"
    "v_mfma_f32_16x16x32_bf16 v[8:11], a[96:99], v[32:35], v[8:11]\n"
    "s_add_u32 m0, s37, 8192\n"
    "s_add_u32 s32, s32, 0x4000\n"
    "v_mfma_f32_16x16x32_bf16 v[12:15], a[96:99], v[36:39], v[12:15]\n"
    "v_mfma_f32_16x16x32_bf16 v[16:19], a[176:179], v[32:35], v[16:19]\n"
    "ds_read_b128 v[32:35], v65 offset:14336\n"
    "v_mfma_f32_16x16x32_bf16 v[20:23], a[176:179], v[36:39], v[20:23]\n"
    "ds_read_b128 v[36:39], v65 offset:15360\n"
    "s_waitcnt lgkmcnt(4)\n"
    "v_mfma_f32_16x16x32_bf16 v[0:3], a[20:23], v[40:43], v[0:3]\n"
    "buffer_load_dwordx4 v64, s[0:3], s32 offen sc1 lds\n"
    "v_mfma_f32_16x16x32_bf16 v[4:7], a[20:23], v[44:47], v[4:7]\n"
    "v_mfma_f32_16x16x32_bf16 v[8:11], a[100:103], v[40:43], v[8:11]\n"
    "v_mfma_f32_16x16x32_bf16 v[12:15], a[100:103], v[44:47], v[12:15]\n"
    "v_mfma_f32_16x16x32_bf16 v[16:19], a[180:183], v[40:43], v[16:19]\n"
    "ds_read_b128 v[40:43], v65 offset:16384\n"
    "v_mfma_f32_16x16x32_bf16 v[20:23], a[180:183], v[44:47], v[20:23]\n"
    "ds_read_b128 v[44:47], v65 offset:17408\n"
    "s_waitcnt lgkmcnt(4)\n"
    "v_mfma_f32_16x16x32_bf16 v[0:3], a[24:27], v[24:27], v[0:3]\n"
    "buffer_load_dwordx4 v64, s[0:3], s32 offen offset:1024 sc1 lds\n"
    "v_mfma_f32_16x16x32_bf16 v[4:7], a[24:27], v[28:31], v[4:7]\n"
    "s_add_u32 m0, s37, 16384\n"
    "s_add_u32 s32, s32, 0x4000\n"
    "v_mfma_f32_16x16x32_bf16 v[8:11], a[104:107], v[24:27], v[8:11]\n"
    "v_mfma_f32_16x16x32_bf16 v[12:15], a[104:107], v[28:31], v[12:15]\n"
    "v_mfma_f32_16x16x32_bf16 v[16:19], a[184:187], v[24:27], v[16:19]\n"
    "ds_read_b128 v[24:27], v65 offset:18432\n"
    "v_mfma_f32_16x16x32_bf16 v[20:23], a[184:187], v[28:31], v[20:23]\n"
    "ds_read_b128 v[28:31], v65 offset:19456\n"
    "s_waitcnt lgkmcnt(4)\n"
    "v_mfma_f32_16x16x32_bf16 v[0:3], a[28:31], v[32:35], v[0:3]\n"
    "buffer_load_dwordx4 v64, s[0:3], s32 offen sc1 lds\n"
    "v_mfma_f32_16x16x32_bf16 v[4:7], a[28:31], v[36:39], v[4:7]\n"
    "v_mfma_f32_16x16x32_bf16 v[8:11], a[108:111], v[32:35], v[8:11]\n"
    "v_mfma_f32_16x16x32_bf16 v[12:15], a[108:111], v[36:39], v[12:15]\n"
    "v_mfma_f32_16x16x32_bf16 v[16:19], a[188:191], v[32:35], v[16:19]\n"
    "ds_read_b128 v[32:35], v65 offset:20480\n"
    "v_mfma_f32_16x16x32_bf16 v[20:23], a[188:191], v[36:39], v[20:23]\n"
    "ds_read_b128 v[36:39], v65 offset:21504\n"
    "s_waitcnt lgkmcnt(4)\n"
    "v_mfma_f32_16x16x32_bf16 v[0:3], a[32:35], v[40:43], v[0:3]\n"
    "buffer_load_dwordx4 v64, s[0:3], s32 offen offset:1024 sc1 lds\n"
    "v_mfma_f32_16x16x32_bf16 v[4:7], a[32:35], v[44:47], v[4:7]\n"
    "s_add_u32 m0, s37, 24576\n"
    "s_add_u32 s32, s32, 0x4000\n"
    "v_mfma_f32_16x16x32_bf16 v[8:11], a[112:115], v[40:43], v[8:11]\n"
    "v_mfma_f32_16x16x32_bf16 v[12:15], a[112:115], v[44:47], v[12:15]\n"
    "v_mfma_f32_16x16x32_bf16 v[16:19], a[192:195], v[40:43], v[16:19]\n"
    "ds_read_b128 v[40:43], v65 offset:22528\n"
    "v_mfma_f32_16x16x32_bf16 v[20:23], a[192:195], v[44:47], v[20:23]\n"
    "ds_read_b128 v[44:47], v65 offset:23552\n"
    "s_waitcnt lgkmcnt(4)\n"
    "v_mfma_f32_16x16x32_bf16 v[0:3], a[36:39], v[24:27], v[0:3]\n"
    "buffer_load_dwordx4 v64, s[0:3], s32 offen sc1 lds\n"
    "v_mfma_f32_16x16x32_bf16 v[4:7], a[36:39], v[28:31], v[4:7]\n"
    "v_mfma_f32_16x16x32_bf16 v[8:11], a[116:119], v[24:27], v[8:11]\n"
    "v_mfma_f32_16x16x32_bf16 v[12:15], a[116:119], v[28:31], v[12:15]\n"
    "v_mfma_f32_16x16x32_bf16 v[16:19], a[196:199], v[24:27], v[16:19]\n"
    "ds_read_b128 v[24:27], v65 offset:24576\n"
    "v_mfma_f32_16x16x32_bf16 v[20:23], a[196:199], v[28:31], v[20:23]\n"
    "ds_read_b128 v[28:31], v65 offset:25600\n"
    "s_waitcnt lgkmcnt(4)\n"
    "v_mfma_f32_16x16x32_bf16 v[0:3], a[40:43], v[32:35], v[0:3]\n"
    "buffer_load_dwordx4 v64, s[0:3], s32 offen offset:1024 sc1 lds\n"
    "v_mfma_f32_16x16x32_bf16 v[4:7], a[40:43], v[36:39], v[4:7]\n"
    "s_add_u32 m0, s37, 32768\n"
    "s_add_u32 s32, s32, 0x4000\n"
    "v_mfma_f32_16x16x32_bf16 v[8:11], a[120:123], v[32:35], v[8:11]\n"
    "v_mfma_f32_16x16x32_bf16 v[12:15], a[120:123], v[36:39], v[12:15]\n"
    "v_mfma_f32_16x16x32_bf16 v[16:19], a[200:203], v[32:35], v[16:19]\n"
    "ds_read_b128 v[32:35], v65 offset:26624\n"
    "v_mfma_f32_16x16x32_bf16 v[20:23], a[200:203], v[36:39], v[20:23]\n"
    "ds_read_b128 v[36:39], v65 offset:27648\n"
    "s_waitcnt lgkmcnt(4)\n"
    "v_mfma_f32_16x16x32_bf16 v[0:3], a[44:47], v[40:43], v[0:3]\n"
    "buffer_load_dwordx4 v64, s[0:3], s32 offen sc1 lds\n"
    "v_mfma_f32_16x16x32_bf16 v[4:7], a[44:47], v[44:47], v[4:7]\n"
    "v_mfma_f32_16x16x32_bf16 v[8:11], a[124:127], v[40:43], v[8:11]\n"
    "v_mfma_f32_16x16x32_bf16 v[12:15], a[124:127], v[44:47], v[12:15]\n"
    "v_mfma_f32_16x16x32_bf16 v[16:19], a[204:207], v[40:43], v[16:19]\n"
    "ds_read_b128 v[40:43], v65 offset:28672\n"
    "v_mfma_f32_16x16x32_bf16 v[20:23], a[204:207], v[44:47], v[20:23]\n"
    "ds_read_b128 v[44:47], v65 offset:29696\n"
    "s_waitcnt lgkmcnt(4)\n"
    "v_mfma_f32_16x16x32_bf16 v[0:3], a[48:51], v[24:27], v[0:3]\n"
    "buffer_load_dwordx4 v64, s[0:3], s32 offen offset:1024 sc1 lds\n"
    "v_mfma_f32_16x16x32_bf16 v[4:7], a[48:51], v[28:31], v[4:7]\n"
    "s_waitcnt vmcnt(10)\n"
    "v_mov_b32 v61, s44\n"
    "v_mfma_f32_16x16x32_bf16 v[8:11], a[128:131], v[24:27], v[8:11]\n"
    "v_mfma_f32_16x16x32_bf16 v[12:15], a[128:131], v[28:31], v[12:15]\n"
    "v_mfma_f32_16x16x32_bf16 v[16:19], a[208:211], v[24:27], v[16:19]\n"
    "buffer_store_dword v61, v69, s[0:3], s43 offen sc1\n"
    "ds_read_b128 v[24:27], v65 offset:30720\n"
    "v_mfma_f32_16x16x32_bf16 v[20:23], a[208:211], v[28:31], v[20:23]\n"
    "s_mov_b32 s46, s34\n"
    "ds_read_b128 v[28:31], v65 offset:31744\n"
    "s_waitcnt lgkmcnt(4)\n"
    "v_mfma_f32_16x16x32_bf16 v[0:3], a[52:55], v[32:35], v[0:3]\n"
    "s_mov_b32 s43, s41\n"
    "s_mov_b32 s44, s42\n"
    "s_mov_b32 s41, s39\n"
    "v_mfma_f32_16x16x32_bf16 v[4:7], a[52:55], v[36:39], v[4:7]\n"
    "s_mov_b32 s42, s40\n"
    "s_mov_b32 s34, s33\n"
    "s_mov_b32 s35, s36\n"
    "v_mfma_f32_16x16x32_bf16 v[8:11], a[132:135], v[32:35], v[8:11]\n"
    "s_xor_b32 s37, s37, 40960\n"
    "s_add_u32 s26, s26, 1\n"
    "s_sub_u32 s27, s26, 14\n"
    "v_mfma_f32_16x16x32_bf16 v[12:15], a[132:135], v[36:39], v[12:15]\n"
    "s_ashr_i32 s28, s27, 1\n"
    "s_and_b32 s29, s27, 1\n"
    "s_add_u32 s30, s28, 14\n"
    "v_mfma_f32_16x16x32_bf16 v[16:19], a[212:215], v[32:35], v[16:19]\n"
    "ds_read_b128 v[32:35], v65 offset:32768\n"
    "v_mfma_f32_16x16x32_bf16 v[20:23], a[212:215], v[36:39], v[20:23]\n"
    "ds_read_b128 v[36:39], v65 offset:33792\n"
    "s_waitcnt lgkmcnt(4)\n"
    "v_mfma_f32_16x16x32_bf16 v[0:3], a[56:59], v[40:43], v[0:3]\n"
    "s_cmp_lt_i32 s27, 0\n"
    "s_cselect_b32 s29, 0, s29\n"
    "s_cselect_b32 s30, s26, s30\n"
    "v_mfma_f32_16x16x32_bf16 v[4:7], a[56:59], v[44:47], v[4:7]\n"
    "s_cmp_eq_u32 s29, 1\n"
    "s_cselect_b32 s30, s28, s30\n"
    "s_cselect_b32 s27, s15, 64\n"
    "v_mfma_f32_16x16x32_bf16 v[8:11], a[136:139], v[40:43], v[8:11]\n"
    "s_lshl_b32 s31, s29, 13\n"
    "s_add_u32 s40, s30, 1\n"
    "s_lshl_b32 s32, s30, 19\n"
    "v_mfma_f32_16x16x32_bf16 v[12:15], a[136:139], v[44:47], v[12:15]\n"
    "s_lshl_b32 s25, s30, 11\n"
    "s_xor_b32 s25, s25, s31\n"
    "s_and_b32 s25, s25, 0x2000\n"
    "v_mfma_f32_16x16x32_bf16 v[16:19], a[216:219], v[40:43], v[16:19]\n"
    "ds_read_b128 v[40:43], v65 offset:34816\n"
    "s_add_u32 s32, s32, s25\n"
    "v_mfma_f32_16x16x32_bf16 v[20:23], a[216:219], v[44:47], v[20:23]\n"
    "s_add_u32 s32, s32, s13\n"
    "ds_read_b128 v[44:47], v65 offset:35840\n"
    "s_waitcnt lgkmcnt(4)\n"
    "v_mfma_f32_16x16x32_bf16 v[0:3], a[60:63], v[24:27], v[0:3]\n"
    "s_lshl_b32 s33, s30, 20\n"
    "s_lshl_b32 s25, s30, 12\n"
    "s_xor_b32 s25, s25, s31\n"
    "v_mfma_f32_16x16x32_bf16 v[4:7], a[60:63], v[28:31], v[4:7]\n"
    "s_and_b32 s25, s25, 0x2000\n"
    "s_add_u32 s33, s33, s25\n"
    "s_add_u32 s33, s33, s14\n"
    "v_mfma_f32_16x16x32_bf16 v[8:11], a[140:143], v[24:27], v[8:11]\n"
    "s_mul_i32 s47, s40, s29\n"
    "v_mfma_f32_16x16x32_bf16 v[12:15], a[140:143], v[28:31], v[12:15]\n"
    "s_cmp_lt_u32 s30, s27\n"
    "s_cselect_b32 s36, 1, 0\n"
    "s_cselect_b32 s32, s32, 2130706432\n"
    "v_mfma_f32_16x16x32_bf16 v[16:19], a[220:223], v[24:27], v[16:19]\n"
    "ds_read_b128 v[24:27], v65 offset:36864\n"
    "v_mfma_f32_16x16x32_bf16 v[20:23], a[220:223], v[28:31], v[20:23]\n"
    "ds_read_b128 v[28:31], v65 offset:37888\n"
    "s_waitcnt lgkmcnt(4)\n"
    "v_mfma_f32_16x16x32_bf16 v[0:3], a[64:67], v[32:35], v[0:3]\n"
    "s_cmp_lt_u32 s30, s27\n"
    "s_cselect_b32 s39, s31, 2130706432\n"
    "s_cselect_b32 s47, s47, 0\n"
    "v_mfma_f32_16x16x32_bf16 v[4:7], a[64:67], v[36:39], v[4:7]\n"
    "s_cmp_gt_u32 s47, 0\n"
    "s_cselect_b32 s38, s17, 2130706432\n"
    "v_mfma_f32_16x16x32_bf16 v[8:11], a[144:147], v[32:35], v[8:11]\n"
    "buffer_load_dword v60, v68, s[0:3], s38 offen sc1\n"
    "v_mfma_f32_16x16x32_bf16 v[12:15], a[144:147], v[36:39], v[12:15]\n"
    "v_mfma_f32_16x16x32_bf16 v[16:19], a[224:227], v[32:35], v[16:19]\n"
    "ds_read_b128 v[32:35], v65 offset:38912\n"
    "v_mfma_f32_16x16x32_bf16 v[20:23], a[224:227], v[36:39], v[20:23]\n"
    "ds_read_b128 v[36:39], v65 offset:39936\n"
    "s_waitcnt lgkmcnt(4)\n"
    "v_mfma_f32_16x16x32_bf16 v[0:3], a[68:71], v[40:43], v[0:3]\n"
    "v_mfma_f32_16x16x32_bf16 v[4:7], a[68:71], v[44:47], v[4:7]\n"
    "v_mfma_f32_16x16x32_bf16 v[8:11], a[148:151], v[40:43], v[8:11]\n"
    "v_mfma_f32_16x16x32_bf16 v[12:15], a[148:151], v[44:47], v[12:15]\n"
    "v_mfma_f32_16x16x32_bf16 v[16:19], a[228:231], v[40:43], v[16:19]\n"
    "v_mfma_f32_16x16x32_bf16 v[20:23], a[228:231], v[44:47], v[20:23]\n"
    "s_waitcnt lgkmcnt(2)\n"
    "v_mfma_f32_16x16x32_bf16 v[0:3], a[72:75], v[24:27], v[0:3]\n"
    "v_mfma_f32_16x16x32_bf16 v[4:7], a[72:75], v[28:31], v[4:7]\n"
    "v_mfma_f32_16x16x32_bf16 v[8:11], a[152:155], v[24:27], v[8:11]\n"
    "v_mfma_f32_16x16x32_bf16 v[12:15], a[152:155], v[28:31], v[12:15]\n"
    "v_mfma_f32_16x16x32_bf16 v[16:19], a[232:235], v[24:27], v[16:19]\n"
    "v_mfma_f32_16x16x32_bf16 v[20:23], a[232:235], v[28:31], v[20:23]\n"
    "s_waitcnt lgkmcnt(0)\n"
    "v_mfma_f32_16x16x32_bf16 v[0:3], a[76:79], v[32:35], v[0:3]\n"
    "v_mfma_f32_16x16x32_bf16 v[4:7], a[76:79], v[36:39], v[4:7]\n"
    "v_mfma_f32_16x16x32_bf16 v[8:11], a[156:159], v[32:35], v[8:11]\n"
    "v_mfma_f32_16x16x32_bf16 v[12:15], a[156:159], v[36:39], v[12:15]\n"
    "v_mfma_f32_16x16x32_bf16 v[16:19], a[236:239], v[32:35], v[16:19]\n"
    "v_mfma_f32_16x16x32_bf16 v[20:23], a[236:239], v[36:39], v[20:23]\n"
    "v_cvt_pk_bf16_f32 v48, v0, v1\n"
    "v_cvt_pk_bf16_f32 v49, v2, v3\n"
    "v_cvt_pk_bf16_f32 v50, v8, v9\n"
    "v_cvt_pk_bf16_f32 v51, v10, v11\n"
    "v_cvt_pk_bf16_f32 v52, v4, v5\n"
    "v_cvt_pk_bf16_f32 v53, v6, v7\n"
    "v_cvt_pk_bf16_f32 v54, v12, v13\n"
    "v_cvt_pk_bf16_f32 v55, v14, v15\n"
    "v_cvt_pk_bf16_f32 v56, v16, v17\n"
    "v_cvt_pk_bf16_f32 v57, v18, v19\n"
    "v_cvt_pk_bf16_f32 v58, v20, v21\n"
    "v_cvt_pk_bf16_f32 v59, v22, v23\n"
    ".Lqw_mk1x7_%=:\n"
    ".Lqw_join_%=:\n"
    "v_xor_b32 v65, 40960, v65\n"
    "s_waitcnt vmcnt(0) lgkmcnt(0)\n"
    "s_barrier\n"
    ".Lqw_mk2x8_%=:\n"
    "s_cmp_le_u32 s26, 142\n"
    "s_cbranch_scc1 .Lqw_top_%=\n"
    "buffer_store_dwordx4 v[48:51], v66, s[0:3], s46 offen offset:0 sc1\n"
    "buffer_store_dwordx4 v[52:55], v66, s[0:3], s46 offen offset:1024 sc1\n"
    "buffer_store_dwordx4 v[56:59], v67, s[0:3], s46 offen sc1\n"
    "s_waitcnt vmcnt(0)\n"
    "v_mov_b32 v61, s44\n"
    "buffer_store_dword v61, v69, s[0:3], s43 offen sc1\n"
    "s_waitcnt vmcnt(0) lgkmcnt(0)\n"
    "s_endpgm\n"
    ".Lqw_tk0_%=:\n"
    ".Lqw_po1_%=:\n"
    "buffer_load_dword v5, v4, s[0:3], 0 offen sc1\n"
    "s_waitcnt vmcnt(0)\n"
    "v_cmp_gt_i32 vcc, 32, v5\n"
    "s_nop 0\n"
    "s_cbranch_vccz .Lqw_pd2_%=\n"
    "s_add_u32 s20, s20, 1\n"
    "s_cmp_lt_u32 s20, 0x40000\n"
    "s_cbranch_scc1 .Lqw_sp3_%=\n"
    "v_mov_b32 v5, 90\n"
    "s_mov_b32 s24, 33728\n"
    "buffer_store_dword v5, off, s[0:3], s24 sc1\n"
    "s_waitcnt vmcnt(0)\n"
    "s_branch .Lqw_pd2_%=\n"
    ".Lqw_sp3_%=:\n"
    "s_sleep 8\n"
    "s_branch .Lqw_po1_%=\n"
    ".Lqw_pd2_%=:\n"
    "s_branch .Lqw_tk1_%=\n"
    ".Lqw_wx_%=:\n"
    "buffer_store_dwordx4 v[48:51], v66, s[0:3], s46 offen offset:0 sc1\n"
    "buffer_store_dwordx4 v[52:55], v66, s[0:3], s46 offen offset:1024 sc1\n"
    "buffer_store_dwordx4 v[56:59], v67, s[0:3], s46 offen sc1\n"
    "s_waitcnt vmcnt(0)\n"
    "v_mov_b32 v61, s44\n"
    "buffer_store_dword v61, v69, s[0:3], s43 offen sc1\n"
    "s_mov_b32 s43, 2130706432\n"
    "s_mov_b32 s46, 2130706432\n"
    ".Lqw_rt10_%=:\n"
    "s_memrealtime s[22:23]\n"
    "s_waitcnt lgkmcnt(0)\n"
    "s_sub_u32 s24, s45, s22\n"
    "s_cmp_lt_u32 s24, 600\n"
    "s_cbranch_scc1 .Lqw_rt10_%=\n"
    ".Lqw_po11_%=:\n"
    "buffer_load_dword v60, v68, s[0:3], s17 offen sc1\n"
    "s_waitcnt vmcnt(0)\n"
    "s_memrealtime s[22:23]\n"
    "s_waitcnt lgkmcnt(0)\n"
    "s_add_u32 s45, s22, 236\n"
    "v_cmp_gt_i32 vcc, s47, v60\n"
    "s_nop 0\n"
    "s_cbranch_vccz .Lqw_pd12_%=\n"
    "s_add_u32 s20, s20, 1\n"
    "s_cmp_lt_u32 s20, 0x40000\n"
    "s_cbranch_scc1 .Lqw_sp13_%=\n"
    "v_mov_b32 v60, 51\n"
    "s_mov_b32 s24, 33728\n"
    "buffer_store_dword v60, off, s[0:3], s24 sc1\n"
    "s_waitcnt vmcnt(0)\n"
    "s_branch .Lqw_pd12_%=\n"
    ".Lqw_sp13_%=:\n"
    "s_sleep 10\n"
    "s_branch .Lqw_po11_%=\n"
    ".Lqw_pd12_%=:\n"
    "s_branch .Lqw_wxb_%=\n"
    ".Lqw_skip_%=:\n"
    "buffer_store_dwordx4 v[48:51], v66, s[0:3], s46 offen offset:0 sc1\n"
    "buffer_store_dwordx4 v[52:55], v66, s[0:3], s46 offen offset:1024 sc1\n"
    "buffer_store_dwordx4 v[56:59], v67, s[0:3], s46 offen sc1\n"
    "s_add_u32 m0, s37, 0\n"
    "s_nop 0\n"
    "buffer_load_dwordx4 v64, s[0:3], s32 offen sc1 lds\n"
    "buffer_load_dwordx4 v64, s[0:3], s32 offen offset:1024 sc1 lds\n"
    "s_add_u32 m0, s37, 8192\n"
    "s_add_u32 s32, s32, 0x4000\n"
    "buffer_load_dwordx4 v64, s[0:3], s32 offen sc1 lds\n"
    "buffer_load_dwordx4 v64, s[0:3], s32 offen offset:1024 sc1 lds\n"
    "s_add_u32 m0, s37, 16384\n"
    "s_add_u32 s32, s32, 0x4000\n"
    "buffer_load_dwordx4 v64, s[0:3], s32 offen sc1 lds\n"
    "buffer_load_dwordx4 v64, s[0:3], s32 offen offset:1024 sc1 lds\n"
    "s_add_u32 m0, s37, 24576\n"
    "s_add_u32 s32, s32, 0x4000\n"
    "buffer_load_dwordx4 v64, s[0:3], s32 offen sc1 lds\n"
    "buffer_load_dwordx4 v64, s[0:3], s32 offen offset:1024 sc1 lds\n"
    "s_add_u32 m0, s37, 32768\n"
    "s_add_u32 s32, s32, 0x4000\n"
    "buffer_load_dwordx4 v64, s[0:3], s32 offen sc1 lds\n"
    "buffer_load_dwordx4 v64, s[0:3], s32 offen offset:1024 sc1 lds\n"
    "s_waitcnt vmcnt(0)\n"
    "v_mov_b32 v61, s44\n"
    "buffer_store_dword v61, v69, s[0:3], s43 offen sc1\n"
    "s_mov_b32 s34, 2130706432\n"
    "s_mov_b32 s46, s34\n"
    "s_mov_b32 s43, s41\n"
    "s_mov_b32 s44, s42\n"
    "s_mov_b32 s41, s39\n"
    "s_mov_b32 s42, s40\n"
    "s_mov_b32 s34, s33\n"
    "s_mov_b32 s35, s36\n"
    "s_xor_b32 s37, s37, 40960\n"
    "s_add_u32 s26, s26, 1\n"
    "s_sub_u32 s27, s26, 14\n"
    "s_ashr_i32 s28, s27, 1\n"
    "s_and_b32 s29, s27, 1\n"
    "s_add_u32 s30, s28, 14\n"
    "s_cmp_lt_i32 s27, 0\n"
    "s_cselect_b32 s29, 0, s29\n"
    "s_cselect_b32 s30, s26, s30\n"
    "s_cmp_eq_u32 s29, 1\n"
    "s_cselect_b32 s30, s28, s30\n"
    "s_cselect_b32 s27, s15, 64\n"
    "s_lshl_b32 s31, s29, 13\n"
    "s_add_u32 s40, s30, 1\n"
    "s_lshl_b32 s32, s30, 19\n"
    "s_lshl_b32 s25, s30, 11\n"
    "s_xor_b32 s25, s25, s31\n"
    "s_and_b32 s25, s25, 0x2000\n"
    "s_add_u32 s32, s32, s25\n"
    "s_add_u32 s32, s32, s13\n"
    "s_lshl_b32 s33, s30, 20\n"
    "s_lshl_b32 s25, s30, 12\n"
    "s_xor_b32 s25, s25, s31\n"
    "s_and_b32 s25, s25, 0x2000\n"
    "s_add_u32 s33, s33, s25\n"
    "s_add_u32 s33, s33, s14\n"
    "s_mul_i32 s47, s40, s29\n"
    "s_cmp_lt_u32 s30, s27\n"
    "s_cselect_b32 s36, 1, 0\n"
    "s_cselect_b32 s32, s32, 2130706432\n"
    "s_cmp_lt_u32 s30, s27\n"
    "s_cselect_b32 s39, s31, 2130706432\n"
    "s_cselect_b32 s47, s47, 0\n"
    "s_cmp_gt_u32 s47, 0\n"
    "s_cselect_b32 s38, s17, 2130706432\n"
    "buffer_load_dword v60, v68, s[0:3], s38 offen sc1\n"
    "s_branch .Lqw_join_%=\n"
    :: "{s[0:3]}"(s0_), "{s[4:7]}"(s4_), "{s[8:11]}"(s8_), "{s[12:15]}"(s12_), "{s[16:19]}"(s16_), "{v[0:3]}"(v0_), "{v[64:67]}"(v64_), "{v[68:71]}"(v68_) : "memory", "v255", "a255", "s95");
    __builtin_unreachable();
}
DEV void b1_role(const MegaArgs& A, const int L, const int i, const int rep) {
    ROLE_HEAD;
    const int qd = 4 * w + lq, sw = (l16 & 6) << 3, hw = w < 3 ? w : 2, hd = 3 * i + w;
    const bool live = w < 3 && hd < 32;
    const unsigned om = live ? RC_O - RC_X1 - w * 2048 + (hd >> 3) * 16384 + ((hd >> 1) & 3) * 2048 + (lq & 1) * 1024 + (2 * (hd & 1) + (lq >> 1)) * 256 + l16 * 16 : OOB;
    const unsigned ox = live && l < 16 ? RC_O - RC_X1 - w * 2048 + 65536 + hd * 256 + l16 * 16 : OOB;
    u64 wm[4];
#pragma unroll
    for (int e = 0; e < 4; ++e) { const int d = l16 + 16 - 16 * (e >> 1) - 4 * lq - 2 * (e & 1) - (l16 & 1); wm[e] = __ballot(d >= 0 && d <= 14); }
    const u32x4 s0_ = {RFL(rx[0]), RFL(rx[1]), RFL(rx[2]), RFL(rx[3])};
    const u32x4 s4_ = {RFL(lo(A.wqkv + 60 * i + 16 * w)), RFL(hi(A.wqkv + 60 * i + 16 * w)), RFL((640 * 1920 - 60 * i - 16 * w) * 2), RFL(0x20000u)};
    const u32x4 s8_ = {RFL((unsigned)wm[0]), RFL((unsigned)(wm[0] >> 32)), RFL((unsigned)wm[1]), RFL((unsigned)(wm[1] >> 32))};
    const u32x4 s12_ = {RFL((unsigned)wm[2]), RFL((unsigned)(wm[2] >> 32)), RFL((unsigned)wm[3]), RFL((unsigned)(wm[3] >> 32))};
    const u32x4 s16_ = {RFL(w == 3 ? 0xf000f000u : 0u), RFL(w == 3 ? 0xf000f000u : 0u), RFL(w), RFL(L << 13)};
    const u32x4 s20_ = {RFL(REC0 + RC_X1 + w * 2048), RFL(xl(Z_X1 + rep * 4096, L)), RFL(DBG0 + ((L * 128 + 96 + i) * 4 + w) * 2048), RFL(0u)};
    const u32x4 v0_ = {(unsigned)((l >> 1) * 3840 + (l & 1) * 16 + 0), (unsigned)((l >> 1) * 3840 + (l & 1) * 16 + 256), (unsigned)((l >> 1) * 3840 + (l & 1) * 16 + 512), (unsigned)(w * 40960 + (8 * lq + (l16 >> 2)) * 32 + (l & 3) * 8)};
    const u32x4 v224_ = {(unsigned)(l * 16 + ((w + 0) & 3) * 2048), (unsigned)(l * 16 + ((w + 1) & 3) * 2048), (unsigned)(l * 16 + ((w + 2) & 3) * 2048), (unsigned)(l * 16 + ((w + 3) & 3) * 2048)};
    const u32x4 v228_ = {(unsigned)(l * 16), (unsigned)((l & 31) * 128), (unsigned)(l < 4 && w == 3 ? xl(Z_O + l * 2048 + i * 128, L) : OOB), (unsigned)(qd == 15 ? 86016 : 40960 + (qd / 5) * 2048 + l16 * 64 + (((qd % 5) * 8) ^ sw))};
    const u32x4 v232_ = {(unsigned)(qd == 15 ? 86016 : 49152 + (qd / 5) * 4096 + l16 * 64 + (((qd % 5) * 8) ^ sw)), (unsigned)(qd == 15 ? 86016 : 61440 + (qd / 5) * 4096 + l16 * 64 + (((qd % 5) * 8) ^ sw)), (unsigned)(40960 + hw * 2048 + l16 * 64 + ((lq * 16) ^ sw)), (unsigned)(49152 + hw * 4096 + l16 * 64 + ((lq * 16) ^ sw))};
    const u32x4 v236_ = {(unsigned)(51200 + hw * 4096 + l16 * 64 + ((lq * 16) ^ sw)), (unsigned)(61440 + hw * 4096 + (l >> 2) * 64 + ((0 + (l & 3) * 8) ^ (((l >> 2) & 6) << 3))), (unsigned)(63488 + hw * 4096 + (l >> 2) * 64 + ((0 + (l & 3) * 8) ^ (((l >> 2) & 6) << 3))), (unsigned)(61440 + hw * 4096 + (l >> 2) * 64 + ((32 + (l & 3) * 8) ^ (((l >> 2) & 6) << 3)))};
    const u32x4 v240_ = {(unsigned)(63488 + hw * 4096 + (l >> 2) * 64 + ((32 + (l & 3) * 8) ^ (((l >> 2) & 6) << 3))), (unsigned)(73728 + w * 2048 + l * 16), (unsigned)(73728 + (l >> 5) * 1024 + ((l16 >> 2) * 16 + l16) * 16 + (l & 3) * 4), (unsigned)(81920 + w * 512 + l * 4)};
    const u32x4 v244_ = {(unsigned)(81920 + 3 * 512 + (l >> 5) * 256 + (48 + l16) * 4), (unsigned)(om), (unsigned)(ox), (unsigned)((l & 1) ? 0x01000c0cu : 0x0c0c0100u)};
    const u32x4 v248_ = {(unsigned)((l & 1) ? 0x03020c0cu : 0x0c0c0302u), (unsigned)(0xff800000u), (unsigned)(0u), (unsigned)(0u)};
    asm volatile(
    "s_mov_b32 s24, 0\n"
    "s_mov_b64 s[26:27], 0\n"
    "s_mov_b32 s29, 0\n"
    "s_cmp_eq_u32 s19, 0\n"
    "s_cselect_b32 s28, 16, 50\n"
    ".Lb1_ho1_%=:\n"
    "s_sub_u32 s28, s28, 1\n"
    "s_cbranch_scc1 .Lb1_hd2_%=\n"
    "s_sleep 17\n"
    "s_branch .Lb1_ho1_%=\n"
    ".Lb1_hd2_%=:\n"
    "v_and_b32 v30, 0x70, v228\n"
    "v_lshlrev_b32 v30, 2, v30\n"
    "v_or_b32 v30, 0x8000, v30\n"
    "buffer_load_dword v31, v30, s[0:3], 0 offen sc1\n"
    "s_waitcnt vmcnt(0)\n"
    "v_cmp_gt_i32 vcc, 32, v31\n"
    "s_nop 0\n"
    "s_cbranch_vccnz .Lb1_tk0_%=\n"
    ".Lb1_tk1_%=:\n"
    "s_mul_i32 s32, s18, 40960\n"
    "s_add_u32 s35, s18, 0\n"
    "s_and_b32 s35, s35, 3\n"
    "s_add_u32 s35, s35, 0\n"
    "s_mul_i32 s35, s35, 122880\n"
    "s_add_u32 m0, s32, 0\n"
    "s_nop 0\n"
    "buffer_load_dwordx4 v0, s[4:7], s35 offen offset:0 lds\n"
    "buffer_load_dwordx4 v1, s[4:7], s35 offen offset:1024 lds\n"
    "buffer_load_dwordx4 v2, s[4:7], s35 offen offset:2048 lds\n"
    "s_add_u32 s35, s18, 1\n"
    "s_and_b32 s35, s35, 3\n"
    "s_add_u32 s35, s35, 0\n"
    "s_mul_i32 s35, s35, 122880\n"
    "s_add_u32 m0, s32, 3072\n"
    "s_nop 0\n"
    "buffer_load_dwordx4 v0, s[4:7], s35 offen offset:0 lds\n"
    "buffer_load_dwordx4 v1, s[4:7], s35 offen offset:1024 lds\n"
    "buffer_load_dwordx4 v2, s[4:7], s35 offen offset:2048 lds\n"
    "s_add_u32 s35, s18, 2\n"
    "s_and_b32 s35, s35, 3\n"
    "s_add_u32 s35, s35, 0\n"
    "s_mul_i32 s35, s35, 122880\n"
    "s_add_u32 m0, s32, 6144\n"
    "s_nop 0\n"
    "buffer_load_dwordx4 v0, s[4:7], s35 offen offset:0 lds\n"
    "buffer_load_dwordx4 v1, s[4:7], s35 offen offset:1024 lds\n"
    "buffer_load_dwordx4 v2, s[4:7], s35 offen offset:2048 lds\n"
    "s_add_u32 s35, s18, 3\n"
    "s_and_b32 s35, s35, 3\n"
    "s_add_u32 s35, s35, 0\n"
    "s_mul_i32 s35, s35, 122880\n"
    "s_add_u32 m0, s32, 9216\n"
    "s_nop 0\n"
    "buffer_load_dwordx4 v0, s[4:7], s35 offen offset:0 lds\n"
    "buffer_load_dwordx4 v1, s[4:7], s35 offen offset:1024 lds\n"
    "buffer_load_dwordx4 v2, s[4:7], s35 offen offset:2048 lds\n"
    "s_add_u32 s35, s18, 0\n"
    "s_and_b32 s35, s35, 3\n"
    "s_add_u32 s35, s35, 4\n"
    "s_mul_i32 s35, s35, 122880\n"
    "s_add_u32 m0, s32, 12288\n"
    "s_nop 0\n"
    "buffer_load_dwordx4 v0, s[4:7], s35 offen offset:0 lds\n"
    "buffer_load_dwordx4 v1, s[4:7], s35 offen offset:1024 lds\n"
    "buffer_load_dwordx4 v2, s[4:7], s35 offen offset:2048 lds\n"
    "s_add_u32 s35, s18, 1\n"
    "s_and_b32 s35, s35, 3\n"
    "s_add_u32 s35, s35, 4\n"
    "s_mul_i32 s35, s35, 122880\n"
    "s_add_u32 m0, s32, 15360\n"
    "s_nop 0\n"
    "buffer_load_dwordx4 v0, s[4:7], s35 offen offset:0 lds\n"
    "buffer_load_dwordx4 v1, s[4:7], s35 offen offset:1024 lds\n"
    "buffer_load_dwordx4 v2, s[4:7], s35 offen offset:2048 lds\n"
    "s_add_u32 s35, s18, 2\n"
    "s_and_b32 s35, s35, 3\n"
    "s_add_u32 s35, s35, 4\n"
    "s_mul_i32 s35, s35, 122880\n"
    "s_add_u32 m0, s32, 18432\n"
    "s_nop 0\n"
    "buffer_load_dwordx4 v0, s[4:7], s35 offen offset:0 lds\n"
    "buffer_load_dwordx4 v1, s[4:7], s35 offen offset:1024 lds\n"
    "buffer_load_dwordx4 v2, s[4:7], s35 offen offset:2048 lds\n"
    "s_add_u32 s35, s18, 3\n"
    "s_and_b32 s35, s35, 3\n"
    "s_add_u32 s35, s35, 4\n"
    "s_mul_i32 s35, s35, 122880\n"
    "s_add_u32 m0, s32, 21504\n"
    "s_nop 0\n"
    "buffer_load_dwordx4 v0, s[4:7], s35 offen offset:0 lds\n"
    "buffer_load_dwordx4 v1, s[4:7], s35 offen offset:1024 lds\n"
    "buffer_load_dwordx4 v2, s[4:7], s35 offen offset:2048 lds\n"
    "s_add_u32 s35, s18, 0\n"
    "s_and_b32 s35, s35, 3\n"
    "s_add_u32 s35, s35, 8\n"
    "s_mul_i32 s35, s35, 122880\n"
    "s_add_u32 m0, s32, 24576\n"
    "s_nop 0\n"
    "buffer_load_dwordx4 v0, s[4:7], s35 offen offset:0 lds\n"
    "buffer_load_dwordx4 v1, s[4:7], s35 offen offset:1024 lds\n"
    "buffer_load_dwordx4 v2, s[4:7], s35 offen offset:2048 lds\n"
    "s_add_u32 s35, s18, 1\n"
    "s_and_b32 s35, s35, 3\n"
    "s_add_u32 s35, s35, 8\n"
    "s_mul_i32 s35, s35, 122880\n"
    "s_add_u32 m0, s32, 27648\n"
    "s_nop 0\n"
    "buffer_load_dwordx4 v0, s[4:7], s35 offen offset:0 lds\n"
    "buffer_load_dwordx4 v1, s[4:7], s35 offen offset:1024 lds\n"
    "buffer_load_dwordx4 v2, s[4:7], s35 offen offset:2048 lds\n"
    "s_add_u32 s35, s18, 2\n"
    "s_and_b32 s35, s35, 3\n"
    "s_add_u32 s35, s35, 8\n"
    "s_mul_i32 s35, s35, 122880\n"
    "s_add_u32 m0, s32, 30720\n"
    "s_nop 0\n"
    "buffer_load_dwordx4 v0, s[4:7], s35 offen offset:0 lds\n"
    "buffer_load_dwordx4 v1, s[4:7], s35 offen offset:1024 lds\n"
    "buffer_load_dwordx4 v2, s[4:7], s35 offen offset:2048 lds\n"
    "s_add_u32 s35, s18, 3\n"
    "s_and_b32 s35, s35, 3\n"
    "s_add_u32 s35, s35, 8\n"
    "s_mul_i32 s35, s35, 122880\n"
    "s_add_u32 m0, s32, 33792\n"
    "s_nop 0\n"
    "buffer_load_dwordx4 v0, s[4:7], s35 offen offset:0 lds\n"
    "buffer_load_dwordx4 v1, s[4:7], s35 offen offset:1024 lds\n"
    "buffer_load_dwordx4 v2, s[4:7], s35 offen offset:2048 lds\n"
    "s_add_u32 s35, s18, 0\n"
    "s_and_b32 s35, s35, 3\n"
    "s_add_u32 s35, s35, 12\n"
    "s_mul_i32 s35, s35, 122880\n"
    "s_add_u32 m0, s32, 36864\n"
    "s_nop 0\n"
    "buffer_load_dwordx4 v0, s[4:7], s35 offen offset:0 lds\n"
    "buffer_load_dwordx4 v1, s[4:7], s35 offen offset:1024 lds\n"
    "buffer_load_dwordx4 v2, s[4:7], s35 offen offset:2048 lds\n"
    "s_waitcnt vmcnt(36)\n"
    "ds_read_b64_tr_b16 a[0:1], v3 offset:0\n"
    "ds_read_b64_tr_b16 a[2:3], v3 offset:128\n"
    "ds_read_b64_tr_b16 a[80:81], v3 offset:1024\n"
    "ds_read_b64_tr_b16 a[82:83], v3 offset:1152\n"
    "ds_read_b64_tr_b16 a[160:161], v3 offset:2048\n"
    "ds_read_b64_tr_b16 a[162:163], v3 offset:2176\n"
    "s_waitcnt lgkmcnt(0)\n"
    "s_add_u32 s35, s18, 1\n"
    "s_and_b32 s35, s35, 3\n"
    "s_add_u32 s35, s35, 12\n"
    "s_mul_i32 s35, s35, 122880\n"
    "s_add_u32 m0, s32, 0\n"
    "s_nop 0\n"
    "buffer_load_dwordx4 v0, s[4:7], s35 offen offset:0 lds\n"
    "buffer_load_dwordx4 v1, s[4:7], s35 offen offset:1024 lds\n"
    "buffer_load_dwordx4 v2, s[4:7], s35 offen offset:2048 lds\n"
    "s_waitcnt vmcnt(36)\n"
    "ds_read_b64_tr_b16 a[4:5], v3 offset:3072\n"
    "ds_read_b64_tr_b16 a[6:7], v3 offset:3200\n"
    "ds_read_b64_tr_b16 a[84:85], v3 offset:4096\n"
    "ds_read_b64_tr_b16 a[86:87], v3 offset:4224\n"
    "ds_read_b64_tr_b16 a[164:165], v3 offset:5120\n"
    "ds_read_b64_tr_b16 a[166:167], v3 offset:5248\n"
    "s_waitcnt lgkmcnt(0)\n"
    "s_add_u32 s35, s18, 2\n"
    "s_and_b32 s35, s35, 3\n"
    "s_add_u32 s35, s35, 12\n"
    "s_mul_i32 s35, s35, 122880\n"
    "s_add_u32 m0, s32, 3072\n"
    "s_nop 0\n"
    "buffer_load_dwordx4 v0, s[4:7], s35 offen offset:0 lds\n"
    "buffer_load_dwordx4 v1, s[4:7], s35 offen offset:1024 lds\n"
    "buffer_load_dwordx4 v2, s[4:7], s35 offen offset:2048 lds\n"
    "s_waitcnt vmcnt(36)\n"
    "ds_read_b64_tr_b16 a[8:9], v3 offset:6144\n"
    "ds_read_b64_tr_b16 a[10:11], v3 offset:6272\n"
    "ds_read_b64_tr_b16 a[88:89], v3 offset:7168\n"
    "ds_read_b64_tr_b16 a[90:91], v3 offset:7296\n"
    "ds_read_b64_tr_b16 a[168:169], v3 offset:8192\n"
    "ds_read_b64_tr_b16 a[170:171], v3 offset:8320\n"
    "s_waitcnt lgkmcnt(0)\n"
    "s_add_u32 s35, s18, 3\n"
    "s_and_b32 s35, s35, 3\n"
    "s_add_u32 s35, s35, 12\n"
    "s_mul_i32 s35, s35, 122880\n"
    "s_add_u32 m0, s32, 6144\n"
    "s_nop 0\n"
    "buffer_load_dwordx4 v0, s[4:7], s35 offen offset:0 lds\n"
    "buffer_load_dwordx4 v1, s[4:7], s35 offen offset:1024 lds\n"
    "buffer_load_dwordx4 v2, s[4:7], s35 offen offset:2048 lds\n"
    "s_waitcnt vmcnt(36)\n"
    "ds_read_b64_tr_b16 a[12:13], v3 offset:9216\n"
    "ds_read_b64_tr_b16 a[14:15], v3 offset:9344\n"
    "ds_read_b64_tr_b16 a[92:93], v3 offset:10240\n"
    "ds_read_b64_tr_b16 a[94:95], v3 offset:10368\n"
    "ds_read_b64_tr_b16 a[172:173], v3 offset:11264\n"
    "ds_read_b64_tr_b16 a[174:175], v3 offset:11392\n"
    "s_waitcnt lgkmcnt(0)\n"
    "s_add_u32 s35, s18, 0\n"
    "s_and_b32 s35, s35, 3\n"
    "s_add_u32 s35, s35, 16\n"
    "s_mul_i32 s35, s35, 122880\n"
    "s_add_u32 m0, s32, 9216\n"
    "s_nop 0\n"
    "buffer_load_dwordx4 v0, s[4:7], s35 offen offset:0 lds\n"
    "buffer_load_dwordx4 v1, s[4:7], s35 offen offset:1024 lds\n"
    "buffer_load_dwordx4 v2, s[4:7], s35 offen offset:2048 lds\n"
    "s_waitcnt vmcnt(36)\n"
    "ds_read_b64_tr_b16 a[16:17], v3 offset:12288\n"
    "ds_read_b64_tr_b16 a[18:19], v3 offset:12416\n"
    "ds_read_b64_tr_b16 a[96:97], v3 offset:13312\n"
    "ds_read_b64_tr_b16 a[98:99], v3 offset:13440\n"
    "ds_read_b64_tr_b16 a[176:177], v3 offset:14336\n"
    "ds_read_b64_tr_b16 a[178:179], v3 offset:14464\n"
    "s_waitcnt lgkmcnt(0)\n"
    "s_add_u32 s35, s18, 1\n"
    "s_and_b32 s35, s35, 3\n"
    "s_add_u32 s35, s35, 16\n"
    "s_mul_i32 s35, s35, 122880\n"
    "s_add_u32 m0, s32, 12288\n"
    "s_nop 0\n"
    "buffer_load_dwordx4 v0, s[4:7], s35 offen offset:0 lds\n"
    "buffer_load_dwordx4 v1, s[4:7], s35 offen offset:1024 lds\n"
    "buffer_load_dwordx4 v2, s[4:7], s35 offen offset:2048 lds\n"
    "s_waitcnt vmcnt(36)\n"
    "ds_read_b64_tr_b16 a[20:21], v3 offset:15360\n"
    "ds_read_b64_tr_b16 a[22:23], v3 offset:15488\n"
    "ds_read_b64_tr_b16 a[100:101], v3 offset:16384\n"
    "ds_read_b64_tr_b16 a[102:103], v3 offset:16512\n"
    "ds_read_b64_tr_b16 a[180:181], v3 offset:17408\n"
    "ds_read_b64_tr_b16 a[182:183], v3 offset:17536\n"
    "s_waitcnt lgkmcnt(0)\n"
    "s_add_u32 s35, s18, 2\n"
    "s_and_b32 s35, s35, 3\n"
    "s_add_u32 s35, s35, 16\n"
    "s_mul_i32 s35, s35, 122880\n"
    "s_add_u32 m0, s32, 15360\n"
    "s_nop 0\n"
    "buffer_load_dwordx4 v0, s[4:7], s35 offen offset:0 lds\n"
    "buffer_load_dwordx4 v1, s[4:7], s35 offen offset:1024 lds\n"
    "buffer_load_dwordx4 v2, s[4:7], s35 offen offset:2048 lds\n"
    "s_waitcnt vmcnt(36)\n"
    "ds_read_b64_tr_b16 a[24:25], v3 offset:18432\n"
    "ds_read_b64_tr_b16 a[26:27], v3 offset:18560\n"
    "ds_read_b64_tr_b16 a[104:105], v3 offset:19456\n"
    "ds_read_b64_tr_b16 a[106:107], v3 offset:19584\n"
    "ds_read_b64_tr_b16 a[184:185], v3 offset:20480\n"
    "ds_read_b64_tr_b16 a[186:187], v3 offset:20608\n"
    "s_waitcnt lgkmcnt(0)\n"
    "s_add_u32 s35, s18, 3\n"
    "s_and_b32 s35, s35, 3\n"
    "s_add_u32 s35, s35, 16\n"
    "s_mul_i32 s35, s35, 122880\n"
    "s_add_u32 m0, s32, 18432\n"
    "s_nop 0\n"
    "buffer_load_dwordx4 v0, s[4:7], s35 offen offset:0 lds\n"
    "buffer_load_dwordx4 v1, s[4:7], s35 offen offset:1024 lds\n"
    "buffer_load_dwordx4 v2, s[4:7], s35 offen offset:2048 lds\n"
    "s_waitcnt vmcnt(36)\n"
    "ds_read_b64_tr_b16 a[28:29], v3 offset:21504\n"
    "ds_read_b64_tr_b16 a[30:31], v3 offset:21632\n"
    "ds_read_b64_tr_b16 a[108:109], v3 offset:22528\n"
    "ds_read_b64_tr_b16 a[110:111], v3 offset:22656\n"
    "ds_read_b64_tr_b16 a[188:189], v3 offset:23552\n"
    "ds_read_b64_tr_b16 a[190:191], v3 offset:23680\n"
    "s_waitcnt lgkmcnt(0)\n"
    "s_waitcnt vmcnt(33)\n"
    "ds_read_b64_tr_b16 a[32:33], v3 offset:24576\n"
    "ds_read_b64_tr_b16 a[34:35], v3 offset:24704\n"
    "ds_read_b64_tr_b16 a[112:113], v3 offset:25600\n"
    "ds_read_b64_tr_b16 a[114:115], v3 offset:25728\n"
    "ds_read_b64_tr_b16 a[192:193], v3 offset:26624\n"
    "ds_read_b64_tr_b16 a[194:195], v3 offset:26752\n"
    "s_waitcnt lgkmcnt(0)\n"
    "s_waitcnt vmcnt(30)\n"
    "ds_read_b64_tr_b16 a[36:37], v3 offset:27648\n"
    "ds_read_b64_tr_b16 a[38:39], v3 offset:27776\n"
    "ds_read_b64_tr_b16 a[116:117], v3 offset:28672\n"
    "ds_read_b64_tr_b16 a[118:119], v3 offset:28800\n"
    "ds_read_b64_tr_b16 a[196:197], v3 offset:29696\n"
    "ds_read_b64_tr_b16 a[198:199], v3 offset:29824\n"
    "s_waitcnt lgkmcnt(0)\n"
    "s_waitcnt vmcnt(27)\n"
    "ds_read_b64_tr_b16 a[40:41], v3 offset:30720\n"
    "ds_read_b64_tr_b16 a[42:43], v3 offset:30848\n"
    "ds_read_b64_tr_b16 a[120:121], v3 offset:31744\n"
    "ds_read_b64_tr_b16 a[122:123], v3 offset:31872\n"
    "ds_read_b64_tr_b16 a[200:201], v3 offset:32768\n"
    "ds_read_b64_tr_b16 a[202:203], v3 offset:32896\n"
    "s_waitcnt lgkmcnt(0)\n"
    "s_waitcnt vmcnt(24)\n"
    "ds_read_b64_tr_b16 a[44:45], v3 offset:33792\n"
    "ds_read_b64_tr_b16 a[46:47], v3 offset:33920\n"
    "ds_read_b64_tr_b16 a[124:125], v3 offset:34816\n"
    "ds_read_b64_tr_b16 a[126:127], v3 offset:34944\n"
    "ds_read_b64_tr_b16 a[204:205], v3 offset:35840\n"
    "ds_read_b64_tr_b16 a[206:207], v3 offset:35968\n"
    "s_waitcnt lgkmcnt(0)\n"
    "s_waitcnt vmcnt(21)\n"
    "ds_read_b64_tr_b16 a[48:49], v3 offset:36864\n"
    "ds_read_b64_tr_b16 a[50:51], v3 offset:36992\n"
    "ds_read_b64_tr_b16 a[128:129], v3 offset:37888\n"
    "ds_read_b64_tr_b16 a[130:131], v3 offset:38016\n"
    "ds_read_b64_tr_b16 a[208:209], v3 offset:38912\n"
    "ds_read_b64_tr_b16 a[210:211], v3 offset:39040\n"
    "s_waitcnt lgkmcnt(0)\n"
    "s_waitcnt vmcnt(18)\n"
    "ds_read_b64_tr_b16 a[52:53], v3 offset:0\n"
    "ds_read_b64_tr_b16 a[54:55], v3 offset:128\n"
    "ds_read_b64_tr_b16 a[132:133], v3 offset:1024\n"
    "ds_read_b64_tr_b16 a[134:135], v3 offset:1152\n"
    "ds_read_b64_tr_b16 a[212:213], v3 offset:2048\n"
    "ds_read_b64_tr_b16 a[214:215], v3 offset:2176\n"
    "s_waitcnt lgkmcnt(0)\n"
    "s_waitcnt vmcnt(15)\n"
    "ds_read_b64_tr_b16 a[56:57], v3 offset:3072\n"
    "ds_read_b64_tr_b16 a[58:59], v3 offset:3200\n"
    "ds_read_b64_tr_b16 a[136:137], v3 offset:4096\n"
    "ds_read_b64_tr_b16 a[138:139], v3 offset:4224\n"
    "ds_read_b64_tr_b16 a[216:217], v3 offset:5120\n"
    "ds_read_b64_tr_b16 a[218:219], v3 offset:5248\n"
    "s_waitcnt lgkmcnt(0)\n"
    "s_waitcnt vmcnt(12)\n"
    "ds_read_b64_tr_b16 a[60:61], v3 offset:6144\n"
    "ds_read_b64_tr_b16 a[62:63], v3 offset:6272\n"
    "ds_read_b64_tr_b16 a[140:141], v3 offset:7168\n"
    "ds_read_b64_tr_b16 a[142:143], v3 offset:7296\n"
    "ds_read_b64_tr_b16 a[220:221], v3 offset:8192\n"
    "ds_read_b64_tr_b16 a[222:223], v3 offset:8320\n"
    "s_waitcnt lgkmcnt(0)\n"
    "s_waitcnt vmcnt(9)\n"
    "ds_read_b64_tr_b16 a[64:65], v3 offset:9216\n"
    "ds_read_b64_tr_b16 a[66:67], v3 offset:9344\n"
    "ds_read_b64_tr_b16 a[144:145], v3 offset:10240\n"
    "ds_read_b64_tr_b16 a[146:147], v3 offset:10368\n"
    "ds_read_b64_tr_b16 a[224:225], v3 offset:11264\n"
    "ds_read_b64_tr_b16 a[226:227], v3 offset:11392\n"
    "s_waitcnt lgkmcnt(0)\n"
    "s_waitcnt vmcnt(6)\n"
    "ds_read_b64_tr_b16 a[68:69], v3 offset:12288\n"
    "ds_read_b64_tr_b16 a[70:71], v3 offset:12416\n"
    "ds_read_b64_tr_b16 a[148:149], v3 offset:13312\n"
    "ds_read_b64_tr_b16 a[150:151], v3 offset:13440\n"
    "ds_read_b64_tr_b16 a[228:229], v3 offset:14336\n"
    "ds_read_b64_tr_b16 a[230:231], v3 offset:14464\n"
    "s_waitcnt lgkmcnt(0)\n"
    "s_waitcnt vmcnt(3)\n"
    "ds_read_b64_tr_b16 a[72:73], v3 offset:15360\n"
    "ds_read_b64_tr_b16 a[74:75], v3 offset:15488\n"
    "ds_read_b64_tr_b16 a[152:153], v3 offset:16384\n"
    "ds_read_b64_tr_b16 a[154:155], v3 offset:16512\n"
    "ds_read_b64_tr_b16 a[232:233], v3 offset:17408\n"
    "ds_read_b64_tr_b16 a[234:235], v3 offset:17536\n"
    "s_waitcnt lgkmcnt(0)\n"
    "s_waitcnt vmcnt(0)\n"
    "ds_read_b64_tr_b16 a[76:77], v3 offset:18432\n"
    "ds_read_b64_tr_b16 a[78:79], v3 offset:18560\n"
    "ds_read_b64_tr_b16 a[156:157], v3 offset:19456\n"
    "ds_read_b64_tr_b16 a[158:159], v3 offset:19584\n"
    "ds_read_b64_tr_b16 a[236:237], v3 offset:20480\n"
    "ds_read_b64_tr_b16 a[238:239], v3 offset:20608\n"
    "s_waitcnt lgkmcnt(0)\n"
    "s_waitcnt lgkmcnt(0)\n"
    "s_barrier\n"
    "v_mov_b32 v32, 0x3f803f80\n"
    "v_mov_b32 v36, 0\n"
    "v_mov_b32 v33, 0x3f803f80\n"
    "v_mov_b32 v37, 0\n"
    "v_mov_b32 v34, 0x3f803f80\n"
    "v_mov_b32 v38, 0\n"
    "v_mov_b32 v35, 0x3f803f80\n"
    "v_mov_b32 v39, 0\n"
    "s_nop 0\n"
    "v_mfma_f32_16x16x32_bf16 v[96:99], a[0:3], v[32:35], 0\n"
    "v_mfma_f32_16x16x32_bf16 v[100:103], a[80:83], v[32:35], 0\n"
    "v_mfma_f32_16x16x32_bf16 v[104:107], a[160:163], v[32:35], 0\n"
    "v_mfma_f32_16x16x32_bf16 v[96:99], a[4:7], v[32:35], v[96:99]\n"
    "v_mfma_f32_16x16x32_bf16 v[100:103], a[84:87], v[32:35], v[100:103]\n"
    "v_mfma_f32_16x16x32_bf16 v[104:107], a[164:167], v[32:35], v[104:107]\n"
    "v_mfma_f32_16x16x32_bf16 v[96:99], a[8:11], v[32:35], v[96:99]\n"
    "v_mfma_f32_16x16x32_bf16 v[100:103], a[88:91], v[32:35], v[100:103]\n"
    "v_mfma_f32_16x16x32_bf16 v[104:107], a[168:171], v[32:35], v[104:107]\n"
    "v_mfma_f32_16x16x32_bf16 v[96:99], a[12:15], v[32:35], v[96:99]\n"
    "v_mfma_f32_16x16x32_bf16 v[100:103], a[92:95], v[32:35], v[100:103]\n"
    "v_mfma_f32_16x16x32_bf16 v[104:107], a[172:175], v[32:35], v[104:107]\n"
    "v_mfma_f32_16x16x32_bf16 v[96:99], a[16:19], v[32:35], v[96:99]\n"
    "v_mfma_f32_16x16x32_bf16 v[100:103], a[96:99], v[32:35], v[100:103]\n"
    "v_mfma_f32_16x16x32_bf16 v[104:107], a[176:179], v[32:35], v[104:107]\n"
    "v_mfma_f32_16x16x32_bf16 v[96:99], a[20:23], v[32:35], v[96:99]\n"
    "v_mfma_f32_16x16x32_bf16 v[100:103], a[100:103], v[32:35], v[100:103]\n"
    "v_mfma_f32_16x16x32_bf16 v[104:107], a[180:183], v[32:35], v[104:107]\n"
    "v_mfma_f32_16x16x32_bf16 v[96:99], a[24:27], v[32:35], v[96:99]\n"
    "v_mfma_f32_16x16x32_bf16 v[100:103], a[104:107], v[32:35], v[100:103]\n"
    "v_mfma_f32_16x16x32_bf16 v[104:107], a[184:187], v[32:35], v[104:107]\n"
    "v_mfma_f32_16x16x32_bf16 v[96:99], a[28:31], v[32:35], v[96:99]\n"
    "v_mfma_f32_16x16x32_bf16 v[100:103], a[108:111], v[32:35], v[100:103]\n"
    "v_mfma_f32_16x16x32_bf16 v[104:107], a[188:191], v[32:35], v[104:107]\n"
    "v_mfma_f32_16x16x32_bf16 v[96:99], a[32:35], v[32:35], v[96:99]\n"
    "v_mfma_f32_16x16x32_bf16 v[100:103], a[112:115], v[32:35], v[100:103]\n"
    "v_mfma_f32_16x16x32_bf16 v[104:107], a[192:195], v[32:35], v[104:107]\n"
    "v_mfma_f32_16x16x32_bf16 v[96:99], a[36:39], v[32:35], v[96:99]\n"
    "v_mfma_f32_16x16x32_bf16 v[100:103], a[116:119], v[32:35], v[100:103]\n"
    "v_mfma_f32_16x16x32_bf16 v[104:107], a[196:199], v[32:35], v[104:107]\n"
    "v_mfma_f32_16x16x32_bf16 v[96:99], a[40:43], v[32:35], v[96:99]\n"
    "v_mfma_f32_16x16x32_bf16 v[100:103], a[120:123], v[32:35], v[100:103]\n"
    "v_mfma_f32_16x16x32_bf16 v[104:107], a[200:203], v[32:35], v[104:107]\n"
    "v_mfma_f32_16x16x32_bf16 v[96:99], a[44:47], v[32:35], v[96:99]\n"
    "v_mfma_f32_16x16x32_bf16 v[100:103], a[124:127], v[32:35], v[100:103]\n"
    "v_mfma_f32_16x16x32_bf16 v[104:107], a[204:207], v[32:35], v[104:107]\n"
    "v_mfma_f32_16x16x32_bf16 v[96:99], a[48:51], v[32:35], v[96:99]\n"
    "v_mfma_f32_16x16x32_bf16 v[100:103], a[128:131], v[32:35], v[100:103]\n"
    "v_mfma_f32_16x16x32_bf16 v[104:107], a[208:211], v[32:35], v[104:107]\n"
    "v_mfma_f32_16x16x32_bf16 v[96:99], a[52:55], v[32:35], v[96:99]\n"
    "v_mfma_f32_16x16x32_bf16 v[100:103], a[132:135], v[32:35], v[100:103]\n"
    "v_mfma_f32_16x16x32_bf16 v[104:107], a[212:215], v[32:35], v[104:107]\n"
    "v_mfma_f32_16x16x32_bf16 v[96:99], a[56:59], v[32:35], v[96:99]\n"
    "v_mfma_f32_16x16x32_bf16 v[100:103], a[136:139], v[32:35], v[100:103]\n"
    "v_mfma_f32_16x16x32_bf16 v[104:107], a[216:219], v[32:35], v[104:107]\n"
    "v_mfma_f32_16x16x32_bf16 v[96:99], a[60:63], v[32:35], v[96:99]\n"
    "v_mfma_f32_16x16x32_bf16 v[100:103], a[140:143], v[32:35], v[100:103]\n"
    "v_mfma_f32_16x16x32_bf16 v[104:107], a[220:223], v[32:35], v[104:107]\n"
    "v_mfma_f32_16x16x32_bf16 v[96:99], a[64:67], v[32:35], v[96:99]\n"
    "v_mfma_f32_16x16x32_bf16 v[100:103], a[144:147], v[32:35], v[100:103]\n"
    "v_mfma_f32_16x16x32_bf16 v[104:107], a[224:227], v[32:35], v[104:107]\n"
    "v_mfma_f32_16x16x32_bf16 v[96:99], a[68:71], v[32:35], v[96:99]\n"
    "v_mfma_f32_16x16x32_bf16 v[100:103], a[148:151], v[32:35], v[100:103]\n"
    "v_mfma_f32_16x16x32_bf16 v[104:107], a[228:231], v[32:35], v[104:107]\n"
    "v_mfma_f32_16x16x32_bf16 v[96:99], a[72:75], v[32:35], v[96:99]\n"
    "v_mfma_f32_16x16x32_bf16 v[100:103], a[152:155], v[32:35], v[100:103]\n"
    "v_mfma_f32_16x16x32_bf16 v[104:107], a[232:235], v[32:35], v[104:107]\n"
    "v_mfma_f32_16x16x32_bf16 v[96:99], a[76:79], v[32:35], v[96:99]\n"
    "v_mfma_f32_16x16x32_bf16 v[100:103], a[156:159], v[32:35], v[100:103]\n"
    "v_mfma_f32_16x16x32_bf16 v[104:107], a[236:239], v[32:35], v[104:107]\n"
    "s_lshl_b32 s35, s18, 13\n"
    "v_add_u32 v8, s35, v228\n"
    "ds_write_b128 v8, v[36:39] offset:40960\n"
    "ds_write_b128 v8, v[36:39] offset:41984\n"
    "ds_write_b128 v8, v[36:39] offset:43008\n"
    "ds_write_b128 v8, v[36:39] offset:44032\n"
    "ds_write_b128 v8, v[36:39] offset:45056\n"
    "ds_write_b128 v8, v[36:39] offset:46080\n"
    "ds_write_b128 v8, v[36:39] offset:47104\n"
    "ds_write_b128 v8, v[36:39] offset:48128\n"
    "s_nop 7\n"
    "s_nop 7\n"
    "s_mov_b64 exec, s[16:17]\n"
    "v_accvgpr_write_b32 a0, v32\n"
    "v_accvgpr_write_b32 a1, v32\n"
    "v_accvgpr_write_b32 a2, v32\n"
    "v_accvgpr_write_b32 a3, v32\n"
    "v_accvgpr_write_b32 a4, v32\n"
    "v_accvgpr_write_b32 a5, v32\n"
    "v_accvgpr_write_b32 a6, v32\n"
    "v_accvgpr_write_b32 a7, v32\n"
    "v_accvgpr_write_b32 a8, v32\n"
    "v_accvgpr_write_b32 a9, v32\n"
    "v_accvgpr_write_b32 a10, v32\n"
    "v_accvgpr_write_b32 a11, v32\n"
    "v_accvgpr_write_b32 a12, v32\n"
    "v_accvgpr_write_b32 a13, v32\n"
    "v_accvgpr_write_b32 a14, v32\n"
    "v_accvgpr_write_b32 a15, v32\n"
    "v_accvgpr_write_b32 a16, v32\n"
    "v_accvgpr_write_b32 a17, v32\n"
    "v_accvgpr_write_b32 a18, v32\n"
    "v_accvgpr_write_b32 a19, v32\n"
    "v_accvgpr_write_b32 a20, v32\n"
    "v_accvgpr_write_b32 a21, v32\n"
    "v_accvgpr_write_b32 a22, v32\n"
    "v_accvgpr_write_b32 a23, v32\n"
    "v_accvgpr_write_b32 a24, v32\n"
    "v_accvgpr_write_b32 a25, v32\n"
    "v_accvgpr_write_b32 a26, v32\n"
    "v_accvgpr_write_b32 a27, v32\n"
    "v_accvgpr_write_b32 a28, v32\n"
    "v_accvgpr_write_b32 a29, v32\n"
    "v_accvgpr_write_b32 a30, v32\n"
    "v_accvgpr_write_b32 a31, v32\n"
    "v_accvgpr_write_b32 a32, v32\n"
    "v_accvgpr_write_b32 a33, v32\n"
    "v_accvgpr_write_b32 a34, v32\n"
    "v_accvgpr_write_b32 a35, v32\n"
    "v_accvgpr_write_b32 a36, v32\n"
    "v_accvgpr_write_b32 a37, v32\n"
    "v_accvgpr_write_b32 a38, v32\n"
    "v_accvgpr_write_b32 a39, v32\n"
    "v_accvgpr_write_b32 a40, v32\n"
    "v_accvgpr_write_b32 a41, v32\n"
    "v_accvgpr_write_b32 a42, v32\n"
    "v_accvgpr_write_b32 a43, v32\n"
    "v_accvgpr_write_b32 a44, v32\n"
    "v_accvgpr_write_b32 a45, v32\n"
    "v_accvgpr_write_b32 a46, v32\n"
    "v_accvgpr_write_b32 a47, v32\n"
    "v_accvgpr_write_b32 a48, v32\n"
    "v_accvgpr_write_b32 a49, v32\n"
    "v_accvgpr_write_b32 a50, v32\n"
    "v_accvgpr_write_b32 a51, v32\n"
    "v_accvgpr_write_b32 a52, v32\n"
    "v_accvgpr_write_b32 a53, v32\n"
    "v_accvgpr_write_b32 a54, v32\n"
    "v_accvgpr_write_b32 a55, v32\n"
    "v_accvgpr_write_b32 a56, v32\n"
    "v_accvgpr_write_b32 a57, v32\n"
    "v_accvgpr_write_b32 a58, v32\n"
    "v_accvgpr_write_b32 a59, v32\n"
    "v_accvgpr_write_b32 a60, v32\n"
    "v_accvgpr_write_b32 a61, v32\n"
    "v_accvgpr_write_b32 a62, v32\n"
    "v_accvgpr_write_b32 a63, v32\n"
    "v_accvgpr_write_b32 a64, v32\n"
    "v_accvgpr_write_b32 a65, v32\n"
    "v_accvgpr_write_b32 a66, v32\n"
    "v_accvgpr_write_b32 a67, v32\n"
    "v_accvgpr_write_b32 a68, v32\n"
    "v_accvgpr_write_b32 a69, v32\n"
    "v_accvgpr_write_b32 a70, v32\n"
    "v_accvgpr_write_b32 a71, v32\n"
    "v_accvgpr_write_b32 a72, v32\n"
    "v_accvgpr_write_b32 a73, v32\n"
    "v_accvgpr_write_b32 a74, v32\n"
    "v_accvgpr_write_b32 a75, v32\n"
    "v_accvgpr_write_b32 a76, v32\n"
    "v_accvgpr_write_b32 a77, v32\n"
    "v_accvgpr_write_b32 a78, v32\n"
    "v_accvgpr_write_b32 a79, v32\n"
    "s_mov_b64 exec, -1\n"
    "s_mov_b32 s30, -2\n"
    "s_mov_b32 s30, 0\n"
    "s_mov_b32 s31, 1\n"
    "s_lshl_b32 s34, s18, 11\n"
    "s_add_u32 s32, s20, s19\n"
    "s_mov_b32 s37, 0x3ea52b5c\n"
    "s_mov_b64 s[38:39], 0\n"
    "s_mov_b64 s[40:41], 0\n"
    "s_mov_b32 s36, 0\n"
    "v_mov_b32 v108, 0\n"
    "s_mov_b32 s42, 0xaaaaaaaa\n"
    "s_mov_b32 s43, 0xaaaaaaaa\n"
    "s_waitcnt lgkmcnt(0)\n"
    ".Lb1_top_%=:\n"
    "s_cmp_lg_u32 s18, 0\n"
    "s_cbranch_scc1 .Lb1_pd7_%=\n"
    "s_add_u32 s29, s26, 252\n"
    "v_min3_i32 v108, v108, v109, v110\n"
    "v_min_i32 v108, v108, v111\n"
    "v_cmp_gt_i32 vcc, s31, v108\n"
    "s_nop 0\n"
    "s_cbranch_vccz .Lb1_pd7_%=\n"
    ".Lb1_rt8_%=:\n"
    "s_memrealtime s[26:27]\n"
    "s_waitcnt lgkmcnt(0)\n"
    "s_sub_u32 s28, s36, s26\n"
    "s_cmp_lt_u32 s28, 600\n"
    "s_cbranch_scc1 .Lb1_rt8_%=\n"
    ".Lb1_po6_%=:\n"
    "buffer_load_dwordx4 v[108:111], v229, s[0:3], s21 offen sc1\n"
    "s_memrealtime s[26:27]\n"
    "s_waitcnt vmcnt(0) lgkmcnt(0)\n"
    "s_add_u32 s29, s26, 252\n"
    "v_min3_i32 v108, v108, v109, v110\n"
    "v_min_i32 v108, v108, v111\n"
    "v_cmp_gt_i32 vcc, s31, v108\n"
    "s_nop 0\n"
    "s_cbranch_vccz .Lb1_pd7_%=\n"
    "s_add_u32 s24, s24, 1\n"
    "s_cmp_lt_u32 s24, 0x40000\n"
    "s_cbranch_scc1 .Lb1_sp9_%=\n"
    "v_mov_b32 v108, 60\n"
    "s_mov_b32 s28, 33728\n"
    "buffer_store_dword v108, off, s[0:3], s28 sc1\n"
    "s_waitcnt vmcnt(0)\n"
    "s_branch .Lb1_pd7_%=\n"
    ".Lb1_sp9_%=:\n"
    "s_sleep 6\n"
    "s_branch .Lb1_po6_%=\n"
    ".Lb1_pd7_%=:\n"
    "s_barrier\n"
    "s_mov_b32 s36, s29\n"
    ".Lb1_mk0x9_%=:\n"
    "s_mov_b32 s33, s32\n"
    "s_add_u32 m0, s34, 0\n"
    "s_nop 0\n"
    "buffer_load_dwordx4 v228, s[0:3], s32 offen sc1 lds\n"
    "buffer_load_dwordx4 v228, s[0:3], s32 offen offset:1024 sc1 lds\n"
    "s_add_u32 m0, s34, 8192\n"
    "s_add_u32 s32, s32, 0x4000\n"
    "buffer_load_dwordx4 v228, s[0:3], s32 offen sc1 lds\n"
    "buffer_load_dwordx4 v228, s[0:3], s32 offen offset:1024 sc1 lds\n"
    "s_add_u32 m0, s34, 16384\n"
    "s_add_u32 s32, s32, 0x4000\n"
    "buffer_load_dwordx4 v228, s[0:3], s32 offen sc1 lds\n"
    "buffer_load_dwordx4 v228, s[0:3], s32 offen offset:1024 sc1 lds\n"
    "s_add_u32 m0, s34, 24576\n"
    "s_add_u32 s32, s32, 0x4000\n"
    "buffer_load_dwordx4 v228, s[0:3], s32 offen sc1 lds\n"
    "buffer_load_dwordx4 v228, s[0:3], s32 offen offset:1024 sc1 lds\n"
    "s_add_u32 m0, s34, 32768\n"
    "s_add_u32 s32, s32, 0x4000\n"
    "buffer_load_dwordx4 v228, s[0:3], s32 offen sc1 lds\n"
    "buffer_load_dwordx4 v228, s[0:3], s32 offen offset:1024 sc1 lds\n"
    "s_waitcnt vmcnt(8)\n"
    "s_barrier\n"
    ".Lb1_mk1x10_%=:\n"
    "ds_read_b128 v[32:35], v224 offset:0\n"
    "ds_read_b128 v[36:39], v224 offset:1024\n"
    "ds_read_b128 v[40:43], v225 offset:0\n"
    "ds_read_b128 v[44:47], v225 offset:1024\n"
    "s_waitcnt lgkmcnt(2)\n"
    "v_mfma_f32_16x16x32_bf16 v[24:27], v[32:35], v[32:35], 0\n"
    "ds_read_b128 v[48:51], v226 offset:0\n"
    "ds_read_b128 v[52:55], v226 offset:1024\n"
    "v_mfma_f32_16x16x32_bf16 v[28:31], v[36:39], v[36:39], 0\n"
    "v_mfma_f32_16x16x32_bf16 v[0:3], a[0:3], v[32:35], 0\n"
    "v_mfma_f32_16x16x32_bf16 v[4:7], a[0:3], v[36:39], 0\n"
    "v_mfma_f32_16x16x32_bf16 v[8:11], a[80:83], v[32:35], 0\n"
    "s_add_u32 s35, s30, 1\n"
    "s_lshl_b32 s32, s35, 19\n"
    "s_lshl_b32 s29, s35, 11\n"
    "v_mfma_f32_16x16x32_bf16 v[12:15], a[80:83], v[36:39], 0\n"
    "s_xor_b32 s29, s29, s19\n"
    "s_and_b32 s29, s29, 0x2000\n"
    "s_add_u32 s32, s32, s29\n"
    "v_mfma_f32_16x16x32_bf16 v[16:19], a[160:163], v[32:35], 0\n"
    "s_add_u32 s32, s32, s20\n"
    "v_xor_b32 v232, 0x800, v232\n"
    "v_xor_b32 v233, 0x800, v233\n"
    "v_mfma_f32_16x16x32_bf16 v[20:23], a[160:163], v[36:39], 0\n"
    "s_waitcnt lgkmcnt(2)\n"
    "v_xor_b32 v235, 0x800, v235\n"
    "v_xor_b32 v236, 0x800, v236\n"
    "v_mfma_f32_16x16x32_bf16 v[0:3], a[4:7], v[40:43], v[0:3]\n"
    "ds_read_b128 v[56:59], v227 offset:0\n"
    "ds_read_b128 v[60:63], v227 offset:1024\n"
    "v_mfma_f32_16x16x32_bf16 v[4:7], a[4:7], v[44:47], v[4:7]\n"
    "v_xor_b32 v237, 0x800, v237\n"
    "v_xor_b32 v238, 0x800, v238\n"
    "v_mfma_f32_16x16x32_bf16 v[8:11], a[84:87], v[40:43], v[8:11]\n"
    "v_xor_b32 v239, 0x800, v239\n"
    "v_xor_b32 v240, 0x800, v240\n"
    "v_mfma_f32_16x16x32_bf16 v[12:15], a[84:87], v[44:47], v[12:15]\n"
    "v_mov_b32 v116, s31\n"
    "v_mov_b32 v117, s31\n"
    "v_mfma_f32_16x16x32_bf16 v[16:19], a[164:167], v[40:43], v[16:19]\n"
    "v_mov_b32 v118, s31\n"
    "v_mov_b32 v119, s31\n"
    "v_mfma_f32_16x16x32_bf16 v[20:23], a[164:167], v[44:47], v[20:23]\n"
    "s_waitcnt lgkmcnt(2)\n"
    "v_mfma_f32_16x16x32_bf16 v[0:3], a[8:11], v[48:51], v[0:3]\n"
    "s_waitcnt vmcnt(6)\n"
    "s_barrier\n"
    "ds_read_b128 v[64:67], v224 offset:8192\n"
    "v_mfma_f32_16x16x32_bf16 v[4:7], a[8:11], v[52:55], v[4:7]\n"
    "ds_read_b128 v[68:71], v224 offset:9216\n"
    "v_mfma_f32_16x16x32_bf16 v[8:11], a[88:91], v[48:51], v[8:11]\n"
    "v_mfma_f32_16x16x32_bf16 v[12:15], a[88:91], v[52:55], v[12:15]\n"
    "v_mfma_f32_16x16x32_bf16 v[16:19], a[168:171], v[48:51], v[16:19]\n"
    "v_mfma_f32_16x16x32_bf16 v[20:23], a[168:171], v[52:55], v[20:23]\n"
    "s_waitcnt lgkmcnt(2)\n"
    "v_mfma_f32_16x16x32_bf16 v[0:3], a[12:15], v[56:59], v[0:3]\n"
    "ds_read_b128 v[72:75], v225 offset:8192\n"
    "ds_read_b128 v[76:79], v225 offset:9216\n"
    "v_mfma_f32_16x16x32_bf16 v[4:7], a[12:15], v[60:63], v[4:7]\n"
    "v_mfma_f32_16x16x32_bf16 v[8:11], a[92:95], v[56:59], v[8:11]\n"
    "v_mfma_f32_16x16x32_bf16 v[12:15], a[92:95], v[60:63], v[12:15]\n"
    "v_mfma_f32_16x16x32_bf16 v[16:19], a[172:175], v[56:59], v[16:19]\n"
    "v_mfma_f32_16x16x32_bf16 v[20:23], a[172:175], v[60:63], v[20:23]\n"
    "s_waitcnt lgkmcnt(2)\n"
    "v_mfma_f32_16x16x32_bf16 v[24:27], v[64:67], v[64:67], v[24:27]\n"
    "ds_read_b128 v[80:83], v226 offset:8192\n"
    "ds_read_b128 v[84:87], v226 offset:9216\n"
    "v_mfma_f32_16x16x32_bf16 v[28:31], v[68:71], v[68:71], v[28:31]\n"
    "v_mfma_f32_16x16x32_bf16 v[0:3], a[16:19], v[64:67], v[0:3]\n"
    "v_mfma_f32_16x16x32_bf16 v[4:7], a[16:19], v[68:71], v[4:7]\n"
    "v_mfma_f32_16x16x32_bf16 v[8:11], a[96:99], v[64:67], v[8:11]\n"
    "v_mfma_f32_16x16x32_bf16 v[12:15], a[96:99], v[68:71], v[12:15]\n"
    "v_mfma_f32_16x16x32_bf16 v[16:19], a[176:179], v[64:67], v[16:19]\n"
    "v_mfma_f32_16x16x32_bf16 v[20:23], a[176:179], v[68:71], v[20:23]\n"
    "s_waitcnt lgkmcnt(2)\n"
    "v_mfma_f32_16x16x32_bf16 v[0:3], a[20:23], v[72:75], v[0:3]\n"
    "ds_read_b128 v[88:91], v227 offset:8192\n"
    "ds_read_b128 v[92:95], v227 offset:9216\n"
    "v_mfma_f32_16x16x32_bf16 v[4:7], a[20:23], v[76:79], v[4:7]\n"
    "v_mfma_f32_16x16x32_bf16 v[8:11], a[100:103], v[72:75], v[8:11]\n"
    "v_mfma_f32_16x16x32_bf16 v[12:15], a[100:103], v[76:79], v[12:15]\n"
    "v_mfma_f32_16x16x32_bf16 v[16:19], a[180:183], v[72:75], v[16:19]\n"
    "v_mfma_f32_16x16x32_bf16 v[20:23], a[180:183], v[76:79], v[20:23]\n"
    "s_waitcnt lgkmcnt(2)\n"
    "v_mfma_f32_16x16x32_bf16 v[0:3], a[24:27], v[80:83], v[0:3]\n"
    "s_waitcnt vmcnt(4)\n"
    "s_barrier\n"
    "ds_read_b128 v[32:35], v224 offset:16384\n"
    "v_mfma_f32_16x16x32_bf16 v[4:7], a[24:27], v[84:87], v[4:7]\n"
    "ds_read_b128 v[36:39], v224 offset:17408\n"
    "v_mfma_f32_16x16x32_bf16 v[8:11], a[104:107], v[80:83], v[8:11]\n"
    "v_mfma_f32_16x16x32_bf16 v[12:15], a[104:107], v[84:87], v[12:15]\n"
    "v_mfma_f32_16x16x32_bf16 v[16:19], a[184:187], v[80:83], v[16:19]\n"
    "v_mfma_f32_16x16x32_bf16 v[20:23], a[184:187], v[84:87], v[20:23]\n"
    "s_waitcnt lgkmcnt(2)\n"
    "v_mfma_f32_16x16x32_bf16 v[0:3], a[28:31], v[88:91], v[0:3]\n"
    "ds_read_b128 v[40:43], v225 offset:16384\n"
    "ds_read_b128 v[44:47], v225 offset:17408\n"
    "v_mfma_f32_16x16x32_bf16 v[4:7], a[28:31], v[92:95], v[4:7]\n"
    "v_mfma_f32_16x16x32_bf16 v[8:11], a[108:111], v[88:91], v[8:11]\n"
    "v_mfma_f32_16x16x32_bf16 v[12:15], a[108:111], v[92:95], v[12:15]\n"
    "v_mfma_f32_16x16x32_bf16 v[16:19], a[188:191], v[88:91], v[16:19]\n"
    "v_mfma_f32_16x16x32_bf16 v[20:23], a[188:191], v[92:95], v[20:23]\n"
    "s_waitcnt lgkmcnt(2)\n"
    "v_mfma_f32_16x16x32_bf16 v[24:27], v[32:35], v[32:35], v[24:27]\n"
    "ds_read_b128 v[48:51], v226 offset:16384\n"
    "ds_read_b128 v[52:55], v226 offset:17408\n"
    "v_mfma_f32_16x16x32_bf16 v[28:31], v[36:39], v[36:39], v[28:31]\n"
    "v_mfma_f32_16x16x32_bf16 v[0:3], a[32:35], v[32:35], v[0:3]\n"
    "v_mfma_f32_16x16x32_bf16 v[4:7], a[32:35], v[36:39], v[4:7]\n"
    "v_mfma_f32_16x16x32_bf16 v[8:11], a[112:115], v[32:35], v[8:11]\n"
    "v_mfma_f32_16x16x32_bf16 v[12:15], a[112:115], v[36:39], v[12:15]\n"
    "v_mfma_f32_16x16x32_bf16 v[16:19], a[192:195], v[32:35], v[16:19]\n"
    "v_mfma_f32_16x16x32_bf16 v[20:23], a[192:195], v[36:39], v[20:23]\n"
    "s_waitcnt lgkmcnt(2)\n"
    "v_mfma_f32_16x16x32_bf16 v[0:3], a[36:39], v[40:43], v[0:3]\n"
    "ds_read_b128 v[56:59], v227 offset:16384\n"
    "ds_read_b128 v[60:63], v227 offset:17408\n"
    "v_mfma_f32_16x16x32_bf16 v[4:7], a[36:39], v[44:47], v[4:7]\n"
    "v_mfma_f32_16x16x32_bf16 v[8:11], a[116:119], v[40:43], v[8:11]\n"
    "v_mfma_f32_16x16x32_bf16 v[12:15], a[116:119], v[44:47], v[12:15]\n"
    "v_mfma_f32_16x16x32_bf16 v[16:19], a[196:199], v[40:43], v[16:19]\n"
    "v_mfma_f32_16x16x32_bf16 v[20:23], a[196:199], v[44:47], v[20:23]\n"
    "s_waitcnt lgkmcnt(2)\n"
    "v_mfma_f32_16x16x32_bf16 v[0:3], a[40:43], v[48:51], v[0:3]\n"
    "s_waitcnt vmcnt(2)\n"
    "s_barrier\n"
    "ds_read_b128 v[64:67], v224 offset:24576\n"
    "v_mfma_f32_16x16x32_bf16 v[4:7], a[40:43], v[52:55], v[4:7]\n"
    "ds_read_b128 v[68:71], v224 offset:25600\n"
    "ds_read_b128 v[72:75], v225 offset:24576\n"
    "v_mfma_f32_16x16x32_bf16 v[8:11], a[120:123], v[48:51], v[8:11]\n"
    "v_mfma_f32_16x16x32_bf16 v[12:15], a[120:123], v[52:55], v[12:15]\n"
    "ds_read_b128 v[76:79], v225 offset:25600\n"
    "v_mfma_f32_16x16x32_bf16 v[16:19], a[200:203], v[48:51], v[16:19]\n"
    "ds_read_b128 v[80:83], v226 offset:24576\n"
    "ds_read_b128 v[84:87], v226 offset:25600\n"
    "v_mfma_f32_16x16x32_bf16 v[20:23], a[200:203], v[52:55], v[20:23]\n"
    "s_waitcnt lgkmcnt(6)\n"
    "v_mfma_f32_16x16x32_bf16 v[0:3], a[44:47], v[56:59], v[0:3]\n"
    "ds_read_b128 v[88:91], v227 offset:24576\n"
    "v_mfma_f32_16x16x32_bf16 v[4:7], a[44:47], v[60:63], v[4:7]\n"
    "ds_read_b128 v[92:95], v227 offset:25600\n"
    "s_waitcnt vmcnt(0)\n"
    "v_mfma_f32_16x16x32_bf16 v[8:11], a[124:127], v[56:59], v[8:11]\n"
    "s_barrier\n"
    "ds_read_b128 v[32:35], v224 offset:32768\n"
    "v_mfma_f32_16x16x32_bf16 v[12:15], a[124:127], v[60:63], v[12:15]\n"
    "ds_read_b128 v[36:39], v224 offset:33792\n"
    "v_mfma_f32_16x16x32_bf16 v[16:19], a[204:207], v[56:59], v[16:19]\n"
    "ds_read_b128 v[40:43], v225 offset:32768\n"
    "v_mfma_f32_16x16x32_bf16 v[20:23], a[204:207], v[60:63], v[20:23]\n"
    "s_waitcnt lgkmcnt(9)\n"
    "ds_read_b128 v[44:47], v225 offset:33792\n"
    "v_mfma_f32_16x16x32_bf16 v[24:27], v[64:67], v[64:67], v[24:27]\n"
    "ds_read_b128 v[48:51], v226 offset:32768\n"
    "v_mfma_f32_16x16x32_bf16 v[28:31], v[68:71], v[68:71], v[28:31]\n"
    "ds_read_b128 v[52:55], v226 offset:33792\n"
    "v_mfma_f32_16x16x32_bf16 v[0:3], a[48:51], v[64:67], v[0:3]\n"
    "ds_read_b128 v[56:59], v227 offset:32768\n"
    "v_mfma_f32_16x16x32_bf16 v[4:7], a[48:51], v[68:71], v[4:7]\n"
    "s_waitcnt lgkmcnt(11)\n"
    "ds_read_b128 v[60:63], v227 offset:33792\n"
    "v_mfma_f32_16x16x32_bf16 v[0:3], a[52:55], v[72:75], v[0:3]\n"
    "v_mfma_f32_16x16x32_bf16 v[4:7], a[52:55], v[76:79], v[4:7]\n"
    "s_waitcnt lgkmcnt(10)\n"
    "v_mfma_f32_16x16x32_bf16 v[0:3], a[56:59], v[80:83], v[0:3]\n"
    "v_mfma_f32_16x16x32_bf16 v[4:7], a[56:59], v[84:87], v[4:7]\n"
    "s_waitcnt lgkmcnt(8)\n"
    "v_mfma_f32_16x16x32_bf16 v[0:3], a[60:63], v[88:91], v[0:3]\n"
    "v_mfma_f32_16x16x32_bf16 v[4:7], a[60:63], v[92:95], v[4:7]\n"
    "s_waitcnt lgkmcnt(6)\n"
    "v_mfma_f32_16x16x32_bf16 v[24:27], v[32:35], v[32:35], v[24:27]\n"
    "v_mfma_f32_16x16x32_bf16 v[28:31], v[36:39], v[36:39], v[28:31]\n"
    "v_mfma_f32_16x16x32_bf16 v[0:3], a[64:67], v[32:35], v[0:3]\n"
    "v_mfma_f32_16x16x32_bf16 v[4:7], a[64:67], v[36:39], v[4:7]\n"
    "s_waitcnt lgkmcnt(4)\n"
    "v_mfma_f32_16x16x32_bf16 v[0:3], a[68:71], v[40:43], v[0:3]\n"
    "v_mfma_f32_16x16x32_bf16 v[4:7], a[68:71], v[44:47], v[4:7]\n"
    "s_waitcnt lgkmcnt(2)\n"
    "v_mfma_f32_16x16x32_bf16 v[0:3], a[72:75], v[48:51], v[0:3]\n"
    "v_mfma_f32_16x16x32_bf16 v[4:7], a[72:75], v[52:55], v[4:7]\n"
    "s_waitcnt lgkmcnt(0)\n"
    "v_mfma_f32_16x16x32_bf16 v[0:3], a[76:79], v[56:59], v[0:3]\n"
    "v_mfma_f32_16x16x32_bf16 v[4:7], a[76:79], v[60:63], v[4:7]\n"
    "ds_write_b128 v241, v[24:27]\n"
    "v_mfma_f32_16x16x32_bf16 v[8:11], a[128:131], v[64:67], v[8:11]\n"
    "ds_write_b128 v241, v[28:31] offset:1024\n"
    "v_mfma_f32_16x16x32_bf16 v[12:15], a[128:131], v[68:71], v[12:15]\n"
    "ds_write2st64_b32 v243, v0, v4 offset1:1\n"
    "v_mfma_f32_16x16x32_bf16 v[8:11], a[132:135], v[72:75], v[8:11]\n"
    "s_waitcnt lgkmcnt(0)\n"
    "s_barrier\n"
    "ds_read2st64_b32 v[24:25], v242 offset1:8\n"
    "v_mfma_f32_16x16x32_bf16 v[12:15], a[132:135], v[76:79], v[12:15]\n"
    "ds_read2st64_b32 v[26:27], v242 offset0:16 offset1:24\n"
    "ds_read_b32 v108, v244\n"
    "v_mfma_f32_16x16x32_bf16 v[8:11], a[136:139], v[80:83], v[8:11]\n"
    "v_mfma_f32_16x16x32_bf16 v[12:15], a[136:139], v[84:87], v[12:15]\n"
    "v_mfma_f32_16x16x32_bf16 v[8:11], a[140:143], v[88:91], v[8:11]\n"
    "v_mfma_f32_16x16x32_bf16 v[12:15], a[140:143], v[92:95], v[12:15]\n"
    "v_mfma_f32_16x16x32_bf16 v[8:11], a[144:147], v[32:35], v[8:11]\n"
    "s_waitcnt lgkmcnt(0)\n"
    "v_add_f32 v24, v24, v26\n"
    "v_add_f32 v25, v25, v27\n"
    "v_mfma_f32_16x16x32_bf16 v[12:15], a[144:147], v[36:39], v[12:15]\n"
    "v_add_f32 v112, v24, v25\n"
    "v_mul_f32 v108, 0x3acccccd, v108\n"
    "v_mfma_f32_16x16x32_bf16 v[8:11], a[148:151], v[40:43], v[8:11]\n"
    "v_mul_f32 v112, 0x3acccccd, v112\n"
    "v_fma_f32 v112, -v108, v108, v112\n"
    "v_mfma_f32_16x16x32_bf16 v[12:15], a[148:151], v[44:47], v[12:15]\n"
    "v_max_f32 v112, 0, v112\n"
    "v_cvt_pk_bf16_f32 v108, v108, v112\n"
    "v_mfma_f32_16x16x32_bf16 v[8:11], a[152:155], v[48:51], v[8:11]\n"
    "v_and_b32 v112, 0xffff0000, v108\n"
    "v_lshlrev_b32 v108, 16, v108\n"
    "v_mfma_f32_16x16x32_bf16 v[12:15], a[152:155], v[52:55], v[12:15]\n"
    "v_add_f32 v112, 0x3727c5ac, v112\n"
    "v_mov_b32 v110, v108\n"
    "v_mfma_f32_16x16x32_bf16 v[8:11], a[156:159], v[56:59], v[8:11]\n"
    "v_cvt_pk_bf16_f32 v112, v112, v112\n"
    "v_lshlrev_b32 v112, 16, v112\n"
    "v_mfma_f32_16x16x32_bf16 v[12:15], a[156:159], v[60:63], v[12:15]\n"
    "v_sqrt_f32 v112, v112\n"
    "v_mfma_f32_16x16x32_bf16 v[16:19], a[208:211], v[64:67], v[16:19]\n"
    "v_permlane32_swap_b32 v108, v110\n"
    "v_mfma_f32_16x16x32_bf16 v[20:23], a[208:211], v[68:71], v[20:23]\n"
    "v_cvt_pk_bf16_f32 v112, v112, v112\n"
    "v_lshlrev_b32 v112, 16, v112\n"
    "v_mfma_f32_16x16x32_bf16 v[16:19], a[212:215], v[72:75], v[16:19]\n"
    "v_rcp_f32 v112, v112\n"
    "v_mfma_f32_16x16x32_bf16 v[20:23], a[212:215], v[76:79], v[20:23]\n"
    "v_mov_b32 v114, v112\n"
    "v_mfma_f32_16x16x32_bf16 v[16:19], a[216:219], v[80:83], v[16:19]\n"
    "s_nop 0\n"
    "v_permlane32_swap_b32 v112, v114\n"
    "v_mfma_f32_16x16x32_bf16 v[20:23], a[216:219], v[84:87], v[20:23]\n"
    "v_fma_f32 v0, v96, -v108, v0\n"
    "v_fma_f32 v1, v97, -v108, v1\n"
    "v_mfma_f32_16x16x32_bf16 v[16:19], a[220:223], v[88:91], v[16:19]\n"
    "v_fma_f32 v2, v98, -v108, v2\n"
    "v_fma_f32 v3, v99, -v108, v3\n"
    "v_mfma_f32_16x16x32_bf16 v[20:23], a[220:223], v[92:95], v[20:23]\n"
    "v_fma_f32 v4, v96, -v110, v4\n"
    "v_fma_f32 v5, v97, -v110, v5\n"
    "v_mfma_f32_16x16x32_bf16 v[16:19], a[224:227], v[32:35], v[16:19]\n"
    "v_fma_f32 v6, v98, -v110, v6\n"
    "v_fma_f32 v7, v99, -v110, v7\n"
    "v_mfma_f32_16x16x32_bf16 v[20:23], a[224:227], v[36:39], v[20:23]\n"
    "v_mul_f32 v0, v0, v112\n"
    "v_mul_f32 v1, v1, v112\n"
    "v_mfma_f32_16x16x32_bf16 v[16:19], a[228:231], v[40:43], v[16:19]\n"
    "v_mul_f32 v2, v2, v112\n"
    "v_mul_f32 v3, v3, v112\n"
    "v_mfma_f32_16x16x32_bf16 v[20:23], a[228:231], v[44:47], v[20:23]\n"
    "v_mul_f32 v4, v4, v114\n"
    "v_mul_f32 v5, v5, v114\n"
    "v_mfma_f32_16x16x32_bf16 v[16:19], a[232:235], v[48:51], v[16:19]\n"
    "v_mul_f32 v6, v6, v114\n"
    "v_mul_f32 v7, v7, v114\n"
    "v_mfma_f32_16x16x32_bf16 v[20:23], a[232:235], v[52:55], v[20:23]\n"
    "v_cvt_pk_bf16_f32 v0, v0, v1\n"
    "v_cvt_pk_bf16_f32 v1, v2, v3\n"
    "v_mfma_f32_16x16x32_bf16 v[16:19], a[236:239], v[56:59], v[16:19]\n"
    "ds_write_b64 v231, v[0:1] offset:0\n"
    "v_mfma_f32_16x16x32_bf16 v[20:23], a[236:239], v[60:63], v[20:23]\n"
    "v_cvt_pk_bf16_f32 v4, v4, v5\n"
    "v_cvt_pk_bf16_f32 v5, v6, v7\n"
    ".Lb1_mk2x11_%=:\n"
    "ds_write_b64 v231, v[4:5] offset:1024\n"
    "v_pk_fma_f32 v[8:9], v[100:101], v[108:109], v[8:9] op_sel_hi:[1,0,1] neg_lo:[0,1,0] neg_hi:[0,1,0]\n"
    "v_pk_fma_f32 v[10:11], v[102:103], v[108:109], v[10:11] op_sel_hi:[1,0,1] neg_lo:[0,1,0] neg_hi:[0,1,0]\n"
    "v_pk_fma_f32 v[12:13], v[100:101], v[110:111], v[12:13] op_sel_hi:[1,0,1] neg_lo:[0,1,0] neg_hi:[0,1,0]\n"
    "v_pk_fma_f32 v[14:15], v[102:103], v[110:111], v[14:15] op_sel_hi:[1,0,1] neg_lo:[0,1,0] neg_hi:[0,1,0]\n"
    "v_pk_mul_f32 v[8:9], v[8:9], v[112:113] op_sel_hi:[1,0]\n"
    "v_pk_mul_f32 v[10:11], v[10:11], v[112:113] op_sel_hi:[1,0]\n"
    "v_pk_mul_f32 v[12:13], v[12:13], v[114:115] op_sel_hi:[1,0]\n"
    "v_pk_mul_f32 v[14:15], v[14:15], v[114:115] op_sel_hi:[1,0]\n"
    "v_cvt_pk_bf16_f32 v8, v8, v9\n"
    "v_cvt_pk_bf16_f32 v9, v10, v11\n"
    "ds_write_b64 v232, v[8:9] offset:0\n"
    "v_cvt_pk_bf16_f32 v12, v12, v13\n"
    "v_cvt_pk_bf16_f32 v13, v14, v15\n"
    "ds_write_b64 v232, v[12:13] offset:1024\n"
    "v_pk_fma_f32 v[16:17], v[104:105], v[108:109], v[16:17] op_sel_hi:[1,0,1] neg_lo:[0,1,0] neg_hi:[0,1,0]\n"
    "v_pk_fma_f32 v[18:19], v[106:107], v[108:109], v[18:19] op_sel_hi:[1,0,1] neg_lo:[0,1,0] neg_hi:[0,1,0]\n"
    "v_pk_fma_f32 v[20:21], v[104:105], v[110:111], v[20:21] op_sel_hi:[1,0,1] neg_lo:[0,1,0] neg_hi:[0,1,0]\n"
    "v_pk_fma_f32 v[22:23], v[106:107], v[110:111], v[22:23] op_sel_hi:[1,0,1] neg_lo:[0,1,0] neg_hi:[0,1,0]\n"
    "v_pk_mul_f32 v[16:17], v[16:17], v[112:113] op_sel_hi:[1,0]\n"
    "v_pk_mul_f32 v[18:19], v[18:19], v[112:113] op_sel_hi:[1,0]\n"
    "v_pk_mul_f32 v[20:21], v[20:21], v[114:115] op_sel_hi:[1,0]\n"
    "v_pk_mul_f32 v[22:23], v[22:23], v[114:115] op_sel_hi:[1,0]\n"
    "v_cvt_pk_bf16_f32 v16, v16, v17\n"
    "v_cvt_pk_bf16_f32 v17, v18, v19\n"
    "ds_write_b64 v233, v[16:17] offset:0\n"
    "v_cvt_pk_bf16_f32 v20, v20, v21\n"
    "v_cvt_pk_bf16_f32 v21, v22, v23\n"
    "ds_write_b64 v233, v[20:21] offset:1024\n"
    "s_waitcnt lgkmcnt(0)\n"
    "s_barrier\n"
    ".Lb1_mk3x12_%=:\n"
    "ds_read_b128 v[32:35], v236 offset:1024\n"
    "ds_read_b128 v[44:47], v234 offset:0\n"
    "ds_read_b128 v[36:39], v235 offset:0\n"
    "ds_read_b128 v[48:51], v234 offset:1024\n"
    "ds_read_b128 v[40:43], v235 offset:1024\n"
    "ds_read_b64_tr_b16 v[52:53], v238 offset:1024\n"
    "ds_read_b64_tr_b16 v[54:55], v237 offset:0\n"
    "ds_read_b64_tr_b16 v[56:57], v237 offset:1024\n"
    "ds_read_b64_tr_b16 v[58:59], v240 offset:1024\n"
    "ds_read_b64_tr_b16 v[60:61], v239 offset:0\n"
    "ds_read_b64_tr_b16 v[62:63], v239 offset:1024\n"
    "s_waitcnt lgkmcnt(9)\n"
    "v_mfma_f32_16x16x32_bf16 v[64:67], v[32:35], v[44:47], 0\n"
    "s_waitcnt lgkmcnt(8)\n"
    "v_mfma_f32_16x16x32_bf16 v[68:71], v[36:39], v[44:47], 0\n"
    "s_waitcnt lgkmcnt(7)\n"
    "v_mfma_f32_16x16x32_bf16 v[72:75], v[36:39], v[48:51], 0\n"
    "s_waitcnt lgkmcnt(6)\n"
    "v_mfma_f32_16x16x32_bf16 v[76:79], v[40:43], v[48:51], 0\n"
    "v_cndmask_b32 v64, v64, v65, s[42:43]\n"
    "v_cndmask_b32 v65, v66, v67, s[42:43]\n"
    "v_cndmask_b32 v66, v68, v69, s[42:43]\n"
    "v_cndmask_b32 v67, v70, v71, s[42:43]\n"
    "s_nop 0\n"
    "v_cndmask_b32 v68, v72, v73, s[42:43]\n"
    "v_cndmask_b32 v69, v74, v75, s[42:43]\n"
    "s_nop 0\n"
    "v_cndmask_b32 v70, v76, v77, s[42:43]\n"
    "v_cndmask_b32 v71, v78, v79, s[42:43]\n"
    "v_cndmask_b32 v64, v249, v64, s[38:39]\n"
    "v_cndmask_b32 v65, v249, v65, s[40:41]\n"
    "v_cndmask_b32 v66, v249, v66, s[12:13]\n"
    "v_cndmask_b32 v67, v249, v67, s[14:15]\n"
    "v_cndmask_b32 v68, v249, v68, s[8:9]\n"
    "v_cndmask_b32 v69, v249, v69, s[10:11]\n"
    "v_cndmask_b32 v70, v249, v70, s[12:13]\n"
    "v_cndmask_b32 v71, v249, v71, s[14:15]\n"
    "v_max3_f32 v72, v64, v65, v66\n"
    "v_max3_f32 v74, v68, v69, v70\n"
    "v_max_f32 v72, v72, v67\n"
    "v_max_f32 v74, v74, v71\n"
    "s_nop 1\n"
    "v_permlane16_swap_b32 v72, v74\n"
    "v_max_f32 v72, v72, v74\n"
    "v_mov_b32 v74, v72\n"
    "s_nop 1\n"
    "v_permlane32_swap_b32 v72, v74\n"
    "v_max_f32 v72, v72, v74\n"
    "v_mul_f32 v72, s37, v72\n"
    "v_mov_b32 v74, v72\n"
    "s_nop 1\n"
    "v_permlane16_swap_b32 v72, v74\n"
    "v_fma_f32 v64, v64, s37, -v72\n"
    "v_fma_f32 v65, v65, s37, -v72\n"
    "v_fma_f32 v66, v66, s37, -v72\n"
    "v_fma_f32 v67, v67, s37, -v72\n"
    "v_fma_f32 v68, v68, s37, -v74\n"
    "v_fma_f32 v69, v69, s37, -v74\n"
    "v_fma_f32 v70, v70, s37, -v74\n"
    "v_fma_f32 v71, v71, s37, -v74\n"
    "v_exp_f32 v64, v64\n"
    "v_exp_f32 v65, v65\n"
    "v_exp_f32 v66, v66\n"
    "v_exp_f32 v67, v67\n"
    "v_exp_f32 v68, v68\n"
    "v_exp_f32 v69, v69\n"
    "v_exp_f32 v70, v70\n"
    "v_exp_f32 v71, v71\n"
    "v_pk_add_f32 v[72:73], v[64:65], v[66:67]\n"
    "v_pk_add_f32 v[74:75], v[68:69], v[70:71]\n"
    "v_add_f32 v72, v72, v73\n"
    "v_add_f32 v74, v74, v75\n"
    "s_nop 1\n"
    "v_permlane16_swap_b32 v72, v74\n"
    "v_add_f32 v72, v72, v74\n"
    "v_mov_b32 v74, v72\n"
    "s_nop 1\n"
    "v_permlane32_swap_b32 v72, v74\n"
    "v_add_f32 v72, v72, v74\n"
    "v_rcp_f32 v72, v72\n"
    "s_nop 0\n"
    "v_mov_b32 v74, v72\n"
    "s_nop 1\n"
    "v_permlane16_swap_b32 v72, v74\n"
    "v_pk_mul_f32 v[64:65], v[64:65], v[72:73] op_sel_hi:[1,0]\n"
    "v_pk_mul_f32 v[66:67], v[66:67], v[72:73] op_sel_hi:[1,0]\n"
    "v_pk_mul_f32 v[68:69], v[68:69], v[74:75] op_sel_hi:[1,0]\n"
    "v_pk_mul_f32 v[70:71], v[70:71], v[74:75] op_sel_hi:[1,0]\n"
    "v_cvt_pk_bf16_f32 v64, v64, v65\n"
    "v_cvt_pk_bf16_f32 v66, v66, v67\n"
    "v_cvt_pk_bf16_f32 v68, v68, v69\n"
    "v_cvt_pk_bf16_f32 v70, v70, v71\n"
    "v_perm_b32 v80, v64, v64, v247\n"
    "v_perm_b32 v81, v64, v64, v248\n"
    "v_perm_b32 v82, v66, v66, v247\n"
    "v_perm_b32 v83, v66, v66, v248\n"
    "v_perm_b32 v84, v68, v68, v247\n"
    "v_perm_b32 v85, v68, v68, v248\n"
    "v_perm_b32 v86, v70, v70, v247\n"
    "v_perm_b32 v87, v70, v70, v248\n"
    "s_waitcnt lgkmcnt(0)\n"
    "v_mfma_f32_16x16x32_bf16 v[0:3], v[52:55], v[80:83], 0\n"
    "v_mfma_f32_16x16x32_bf16 v[4:7], v[54:57], v[84:87], 0\n"
    "v_mfma_f32_16x16x32_bf16 v[8:11], v[58:61], v[80:83], 0\n"
    "v_mfma_f32_16x16x32_bf16 v[12:15], v[60:63], v[84:87], 0\n"
    "s_mov_b64 s[38:39], s[8:9]\n"
    "s_mov_b64 s[40:41], s[10:11]\n"
    "v_cvt_pk_bf16_f32 v16, v0, v1\n"
    "v_cvt_pk_bf16_f32 v17, v2, v3\n"
    "v_cvt_pk_bf16_f32 v18, v4, v5\n"
    "v_cvt_pk_bf16_f32 v19, v6, v7\n"
    "s_nop 0\n"
    "v_permlane16_swap_b32 v16, v18\n"
    "v_permlane16_swap_b32 v17, v19\n"
    "buffer_store_dwordx4 v[16:19], v245, s[0:3], s33 offen sc1\n"
    "v_cvt_pk_bf16_f32 v20, v8, v9\n"
    "v_cvt_pk_bf16_f32 v21, v10, v11\n"
    "v_cvt_pk_bf16_f32 v22, v12, v13\n"
    "v_cvt_pk_bf16_f32 v23, v14, v15\n"
    "buffer_store_dwordx4 v[20:23], v246, s[0:3], s33 offen sc1\n"
    ".Lb1_mk4x13_%=:\n"
    "v_mov_b32 v108, 0\n"
    "s_memrealtime s[26:27]\n"
    "s_cmp_lg_u32 s18, 0\n"
    "s_waitcnt lgkmcnt(0)\n"
    "s_cbranch_scc1 .Lb1_pl15_%=\n"
    "s_sub_u32 s28, s36, s26\n"
    "s_cmp_gt_i32 s28, 40\n"
    "s_cbranch_scc1 .Lb1_pl15_%=\n"
    "buffer_load_dwordx4 v[108:111], v229, s[0:3], s21 offen sc1\n"
    ".Lb1_pl15_%=:\n"
    "s_waitcnt vmcnt(0)\n"
    "s_barrier\n"
    "buffer_store_dwordx4 v[116:119], v230, s[0:3], 0 offen sc1\n"
    ".Lb1_mk5x15_%=:\n"
    "s_mov_b32 s30, s31\n"
    "s_add_u32 s31, s31, 1\n"
    "s_cmp_lt_u32 s30, 64\n"
    "s_cbranch_scc1 .Lb1_top_%=\n"
    "s_waitcnt vmcnt(0) lgkmcnt(0)\n"
    "s_endpgm\n"
    ".Lb1_tk0_%=:\n"
    ".Lb1_po3_%=:\n"
    "buffer_load_dword v31, v30, s[0:3], 0 offen sc1\n"
    "s_waitcnt vmcnt(0)\n"
    "v_cmp_gt_i32 vcc, 32, v31\n"
    "s_nop 0\n"
    "s_cbranch_vccz .Lb1_pd4_%=\n"
    "s_add_u32 s24, s24, 1\n"
    "s_cmp_lt_u32 s24, 0x40000\n"
    "s_cbranch_scc1 .Lb1_sp5_%=\n"
    "v_mov_b32 v31, 90\n"
    "s_mov_b32 s28, 33728\n"
    "buffer_store_dword v31, off, s[0:3], s28 sc1\n"
    "s_waitcnt vmcnt(0)\n"
    "s_branch .Lb1_pd4_%=\n"
    ".Lb1_sp5_%=:\n"
    "s_sleep 8\n"
    "s_branch .Lb1_po3_%=\n"
    ".Lb1_pd4_%=:\n"
    "s_branch .Lb1_tk1_%=\n"
    :: "{s[0:3]}"(s0_), "{s[4:7]}"(s4_), "{s[8:11]}"(s8_), "{s[12:15]}"(s12_), "{s[16:19]}"(s16_), "{s[20:23]}"(s20_), "{v[0:3]}"(v0_), "{v[224:227]}"(v224_), "{v[228:231]}"(v228_), "{v[232:235]}"(v232_), "{v[236:239]}"(v236_), "{v[240:243]}"(v240_), "{v[244:247]}"(v244_), "{v[248:251]}"(v248_) : "memory", "v255", "a255", "s95");
    __builtin_unreachable();
}
DEV void b2_role(const MegaArgs& A, const int L, const int c, const int rep) {
    ROLE_HEAD;
    const int b = 4 * c + w;
    const u32x4 ro = L ? u32x4{lo(A.out), hi(A.out), 2048u * 1280u, 0x20000u} : rx;
    if (L == 0) {
#pragma unroll
        for (int k = 0; k < 16; ++k) bst16(rr, ST0 + b * 32768 + (k >> 3) * 16384 + (k & 7) * 1024 + l * 16, ones);
        DRAIN();
        wait_drawn(A.ar);
        DRAIN_BARRIER();
        bst4(rr, threadIdx.x == 0 ? xl(Z_ST + c * 128, 0) : OOB, 1u);
    }
    const u32x4 s0_ = {RFL(rx[0]), RFL(rx[1]), RFL(rx[2]), RFL(rx[3])};
    const u32x4 s4_ = {RFL(lo(A.wo + 32 * b)), RFL(hi(A.wo + 32 * b)), RFL((640 * 640 - 32 * b) * 2), RFL(0x20000u)};
    const u32x4 s8_ = {RFL(ro[0]), RFL(ro[1]), RFL(ro[2]), RFL(ro[3])};
    const u32x4 s12_ = {RFL(w), RFL(L << 13), RFL(REC0 + RC_O + w * 2048), RFL(xl(Z_O + rep * 2048, L))};
    const u32x4 s16_ = {RFL(xl(Z_ST, 0)), RFL(ST0), RFL(DBG0 + ((L * 128 + 107 + c) * 4 + w) * 2048), RFL(0u)};
    const u32x4 v0_ = {(unsigned)((0 + (l >> 2)) * 1280 + (l & 3) * 16 - 0), (unsigned)((16 + (l >> 2)) * 1280 + (l & 3) * 16 - 1024), (unsigned)((32 + (l >> 2)) * 1280 + (l & 3) * 16 - 2048), (unsigned)((48 + (l >> 2)) * 1280 + (l & 3) * 16 - 3072)};
    const u32x4 v4_ = {(unsigned)(w * 40960 + (8 * (lq & 1) + 20 * (lq >> 1) + (l16 >> 2)) * 64 + (l & 3) * 16), (unsigned)(w * 40960 + (20 * lq + 16 + (l16 >> 2)) * 64 + (l & 3) * 16), (unsigned)((l < 5 ? l : 0) * 128), (unsigned)(0u)};
    const u32x4 v224_ = {(unsigned)(l * 16), (unsigned)((l < 11 ? l : 0) * 128), (unsigned)(l < 4 && w == 1 ? xl(Z_XN + l * 2048 + c * 128, 0) : OOB), (unsigned)(c * 16384 + l * 16)};
    const u32x4 v228_ = {(unsigned)(L ? (l16 + 0) * 1280 + b * 64 + lq * 16 : RC_X2 + c * 16384 + 0 + l * 16), (unsigned)(L ? (l16 + 16) * 1280 + b * 64 + lq * 16 : RC_X2 + c * 16384 + 1024 + l * 16), (unsigned)(l < 16 ? b * 32768 + l * 16 : OOB), (unsigned)((0 + lq) * 32768 + l16 * 16)};
    const u32x4 v232_ = {(unsigned)((4 + lq) * 32768 + l16 * 16), (unsigned)((8 + lq) * 32768 + l16 * 16), (unsigned)((12 + lq) * 32768 + l16 * 16), (unsigned)((16 + lq) * 32768 + l16 * 16)};
    asm volatile(
    "s_mov_b32 s20, 0\n"
    "s_mov_b64 s[22:23], 0\n"
    "s_mov_b32 s25, 0\n"
    "s_cmp_eq_u32 s13, 0\n"
    "s_cselect_b32 s24, 34, 60\n"
    ".Lb2_ho1_%=:\n"
    "s_sub_u32 s24, s24, 1\n"
    "s_cbranch_scc1 .Lb2_hd2_%=\n"
    "s_sleep 17\n"
    "s_branch .Lb2_ho1_%=\n"
    ".Lb2_hd2_%=:\n"
    "v_and_b32 v30, 0x70, v224\n"
    "v_lshlrev_b32 v30, 2, v30\n"
    "v_or_b32 v30, 0x8000, v30\n"
    "buffer_load_dword v31, v30, s[0:3], 0 offen sc1\n"
    "s_waitcnt vmcnt(0)\n"
    "v_cmp_gt_i32 vcc, 32, v31\n"
    "s_nop 0\n"
    "s_cbranch_vccnz .Lb2_tk0_%=\n"
    ".Lb2_tk1_%=:\n"
    "s_mul_i32 s28, s12, 40960\n"
    "s_mov_b32 s34, 0\n"
    "s_add_u32 m0, s28, 0\n"
    "s_nop 0\n"
    "buffer_load_dwordx4 v0, s[4:7], s34 offen offset:0 lds\n"
    "buffer_load_dwordx4 v1, s[4:7], s34 offen offset:1024 lds\n"
    "buffer_load_dwordx4 v2, s[4:7], s34 offen offset:2048 lds\n"
    "buffer_load_dwordx4 v3, s[4:7], s34 offen offset:3072 lds\n"
    "s_mov_b32 s34, 81920\n"
    "s_add_u32 m0, s28, 4096\n"
    "s_nop 0\n"
    "buffer_load_dwordx4 v0, s[4:7], s34 offen offset:0 lds\n"
    "buffer_load_dwordx4 v1, s[4:7], s34 offen offset:1024 lds\n"
    "buffer_load_dwordx4 v2, s[4:7], s34 offen offset:2048 lds\n"
    "buffer_load_dwordx4 v3, s[4:7], s34 offen offset:3072 lds\n"
    "s_mov_b32 s34, 163840\n"
    "s_add_u32 m0, s28, 8192\n"
    "s_nop 0\n"
    "buffer_load_dwordx4 v0, s[4:7], s34 offen offset:0 lds\n"
    "buffer_load_dwordx4 v1, s[4:7], s34 offen offset:1024 lds\n"
    "buffer_load_dwordx4 v2, s[4:7], s34 offen offset:2048 lds\n"
    "buffer_load_dwordx4 v3, s[4:7], s34 offen offset:3072 lds\n"
    "s_mov_b32 s34, 245760\n"
    "s_add_u32 m0, s28, 12288\n"
    "s_nop 0\n"
    "buffer_load_dwordx4 v0, s[4:7], s34 offen offset:0 lds\n"
    "buffer_load_dwordx4 v1, s[4:7], s34 offen offset:1024 lds\n"
    "buffer_load_dwordx4 v2, s[4:7], s34 offen offset:2048 lds\n"
    "buffer_load_dwordx4 v3, s[4:7], s34 offen offset:3072 lds\n"
    "s_mov_b32 s34, 327680\n"
    "s_add_u32 m0, s28, 16384\n"
    "s_nop 0\n"
    "buffer_load_dwordx4 v0, s[4:7], s34 offen offset:0 lds\n"
    "buffer_load_dwordx4 v1, s[4:7], s34 offen offset:1024 lds\n"
    "buffer_load_dwordx4 v2, s[4:7], s34 offen offset:2048 lds\n"
    "buffer_load_dwordx4 v3, s[4:7], s34 offen offset:3072 lds\n"
    "s_mov_b32 s34, 409600\n"
    "s_add_u32 m0, s28, 20480\n"
    "s_nop 0\n"
    "buffer_load_dwordx4 v0, s[4:7], s34 offen offset:0 lds\n"
    "buffer_load_dwordx4 v1, s[4:7], s34 offen offset:1024 lds\n"
    "buffer_load_dwordx4 v2, s[4:7], s34 offen offset:2048 lds\n"
    "buffer_load_dwordx4 v3, s[4:7], s34 offen offset:3072 lds\n"
    "s_mov_b32 s34, 491520\n"
    "s_add_u32 m0, s28, 24576\n"
    "s_nop 0\n"
    "buffer_load_dwordx4 v0, s[4:7], s34 offen offset:0 lds\n"
    "buffer_load_dwordx4 v1, s[4:7], s34 offen offset:1024 lds\n"
    "buffer_load_dwordx4 v2, s[4:7], s34 offen offset:2048 lds\n"
    "buffer_load_dwordx4 v3, s[4:7], s34 offen offset:3072 lds\n"
    "s_mov_b32 s34, 573440\n"
    "s_add_u32 m0, s28, 28672\n"
    "s_nop 0\n"
    "buffer_load_dwordx4 v0, s[4:7], s34 offen offset:0 lds\n"
    "buffer_load_dwordx4 v1, s[4:7], s34 offen offset:1024 lds\n"
    "buffer_load_dwordx4 v2, s[4:7], s34 offen offset:2048 lds\n"
    "buffer_load_dwordx4 v3, s[4:7], s34 offen offset:3072 lds\n"
    "s_mov_b32 s34, 655360\n"
    "s_add_u32 m0, s28, 32768\n"
    "s_nop 0\n"
    "buffer_load_dwordx4 v0, s[4:7], s34 offen offset:0 lds\n"
    "buffer_load_dwordx4 v1, s[4:7], s34 offen offset:1024 lds\n"
    "buffer_load_dwordx4 v2, s[4:7], s34 offen offset:2048 lds\n"
    "buffer_load_dwordx4 v3, s[4:7], s34 offen offset:3072 lds\n"
    "s_mov_b32 s34, 737280\n"
    "s_add_u32 m0, s28, 36864\n"
    "s_nop 0\n"
    "buffer_load_dwordx4 v0, s[4:7], s34 offen offset:0 lds\n"
    "buffer_load_dwordx4 v1, s[4:7], s34 offen offset:1024 lds\n"
    "buffer_load_dwordx4 v2, s[4:7], s34 offen offset:2048 lds\n"
    "buffer_load_dwordx4 v3, s[4:7], s34 offen offset:3072 lds\n"
    "s_waitcnt vmcnt(0)\n"
    "ds_read_b64_tr_b16 a[0:1], v4 offset:0\n"
    "ds_read_b64_tr_b16 a[2:3], v4 offset:256\n"
    "ds_read_b64_tr_b16 a[80:81], v4 offset:8\n"
    "ds_read_b64_tr_b16 a[82:83], v4 offset:264\n"
    "s_waitcnt lgkmcnt(0)\n"
    "ds_read_b64_tr_b16 a[4:5], v4 offset:2560\n"
    "ds_read_b64_tr_b16 a[6:7], v4 offset:2816\n"
    "ds_read_b64_tr_b16 a[84:85], v4 offset:2568\n"
    "ds_read_b64_tr_b16 a[86:87], v4 offset:2824\n"
    "s_waitcnt lgkmcnt(0)\n"
    "ds_read_b64_tr_b16 a[8:9], v4 offset:5120\n"
    "ds_read_b64_tr_b16 a[10:11], v4 offset:5376\n"
    "ds_read_b64_tr_b16 a[88:89], v4 offset:5128\n"
    "ds_read_b64_tr_b16 a[90:91], v4 offset:5384\n"
    "s_waitcnt lgkmcnt(0)\n"
    "ds_read_b64_tr_b16 a[12:13], v4 offset:7680\n"
    "ds_read_b64_tr_b16 a[14:15], v4 offset:7936\n"
    "ds_read_b64_tr_b16 a[92:93], v4 offset:7688\n"
    "ds_read_b64_tr_b16 a[94:95], v4 offset:7944\n"
    "s_waitcnt lgkmcnt(0)\n"
    "ds_read_b64_tr_b16 a[16:17], v4 offset:10240\n"
    "ds_read_b64_tr_b16 a[18:19], v4 offset:10496\n"
    "ds_read_b64_tr_b16 a[96:97], v4 offset:10248\n"
    "ds_read_b64_tr_b16 a[98:99], v4 offset:10504\n"
    "s_waitcnt lgkmcnt(0)\n"
    "ds_read_b64_tr_b16 a[20:21], v4 offset:12800\n"
    "ds_read_b64_tr_b16 a[22:23], v4 offset:13056\n"
    "ds_read_b64_tr_b16 a[100:101], v4 offset:12808\n"
    "ds_read_b64_tr_b16 a[102:103], v4 offset:13064\n"
    "s_waitcnt lgkmcnt(0)\n"
    "ds_read_b64_tr_b16 a[24:25], v4 offset:15360\n"
    "ds_read_b64_tr_b16 a[26:27], v4 offset:15616\n"
    "ds_read_b64_tr_b16 a[104:105], v4 offset:15368\n"
    "ds_read_b64_tr_b16 a[106:107], v4 offset:15624\n"
    "s_waitcnt lgkmcnt(0)\n"
    "ds_read_b64_tr_b16 a[28:29], v4 offset:17920\n"
    "ds_read_b64_tr_b16 a[30:31], v4 offset:18176\n"
    "ds_read_b64_tr_b16 a[108:109], v4 offset:17928\n"
    "ds_read_b64_tr_b16 a[110:111], v4 offset:18184\n"
    "s_waitcnt lgkmcnt(0)\n"
    "ds_read_b64_tr_b16 a[32:33], v4 offset:20480\n"
    "ds_read_b64_tr_b16 a[34:35], v4 offset:20736\n"
    "ds_read_b64_tr_b16 a[112:113], v4 offset:20488\n"
    "ds_read_b64_tr_b16 a[114:115], v4 offset:20744\n"
    "s_waitcnt lgkmcnt(0)\n"
    "ds_read_b64_tr_b16 a[36:37], v4 offset:23040\n"
    "ds_read_b64_tr_b16 a[38:39], v4 offset:23296\n"
    "ds_read_b64_tr_b16 a[116:117], v4 offset:23048\n"
    "ds_read_b64_tr_b16 a[118:119], v4 offset:23304\n"
    "s_waitcnt lgkmcnt(0)\n"
    "ds_read_b64_tr_b16 a[40:41], v4 offset:25600\n"
    "ds_read_b64_tr_b16 a[42:43], v4 offset:25856\n"
    "ds_read_b64_tr_b16 a[120:121], v4 offset:25608\n"
    "ds_read_b64_tr_b16 a[122:123], v4 offset:25864\n"
    "s_waitcnt lgkmcnt(0)\n"
    "ds_read_b64_tr_b16 a[44:45], v4 offset:28160\n"
    "ds_read_b64_tr_b16 a[46:47], v4 offset:28416\n"
    "ds_read_b64_tr_b16 a[124:125], v4 offset:28168\n"
    "ds_read_b64_tr_b16 a[126:127], v4 offset:28424\n"
    "s_waitcnt lgkmcnt(0)\n"
    "ds_read_b64_tr_b16 a[48:49], v4 offset:30720\n"
    "ds_read_b64_tr_b16 a[50:51], v4 offset:30976\n"
    "ds_read_b64_tr_b16 a[128:129], v4 offset:30728\n"
    "ds_read_b64_tr_b16 a[130:131], v4 offset:30984\n"
    "s_waitcnt lgkmcnt(0)\n"
    "ds_read_b64_tr_b16 a[52:53], v4 offset:33280\n"
    "ds_read_b64_tr_b16 a[54:55], v4 offset:33536\n"
    "ds_read_b64_tr_b16 a[132:133], v4 offset:33288\n"
    "ds_read_b64_tr_b16 a[134:135], v4 offset:33544\n"
    "s_waitcnt lgkmcnt(0)\n"
    "ds_read_b64_tr_b16 a[56:57], v4 offset:35840\n"
    "ds_read_b64_tr_b16 a[58:59], v4 offset:36096\n"
    "ds_read_b64_tr_b16 a[136:137], v4 offset:35848\n"
    "ds_read_b64_tr_b16 a[138:139], v4 offset:36104\n"
    "s_waitcnt lgkmcnt(0)\n"
    "ds_read_b64_tr_b16 a[60:61], v4 offset:38400\n"
    "ds_read_b64_tr_b16 a[62:63], v4 offset:38656\n"
    "ds_read_b64_tr_b16 a[140:141], v4 offset:38408\n"
    "ds_read_b64_tr_b16 a[142:143], v4 offset:38664\n"
    "s_waitcnt lgkmcnt(0)\n"
    "ds_read_b64_tr_b16 a[64:65], v5 offset:0\n"
    "ds_read_b64_tr_b16 a[66:67], v5 offset:5120\n"
    "ds_read_b64_tr_b16 a[144:145], v5 offset:8\n"
    "ds_read_b64_tr_b16 a[146:147], v5 offset:5128\n"
    "s_waitcnt lgkmcnt(0)\n"
    "ds_read_b64_tr_b16 a[68:69], v5 offset:10240\n"
    "ds_read_b64_tr_b16 a[70:71], v5 offset:15360\n"
    "ds_read_b64_tr_b16 a[148:149], v5 offset:10248\n"
    "ds_read_b64_tr_b16 a[150:151], v5 offset:15368\n"
    "s_waitcnt lgkmcnt(0)\n"
    "ds_read_b64_tr_b16 a[72:73], v5 offset:20480\n"
    "ds_read_b64_tr_b16 a[74:75], v5 offset:25600\n"
    "ds_read_b64_tr_b16 a[152:153], v5 offset:20488\n"
    "ds_read_b64_tr_b16 a[154:155], v5 offset:25608\n"
    "s_waitcnt lgkmcnt(0)\n"
    "ds_read_b64_tr_b16 a[76:77], v5 offset:30720\n"
    "ds_read_b64_tr_b16 a[78:79], v5 offset:35840\n"
    "ds_read_b64_tr_b16 a[156:157], v5 offset:30728\n"
    "ds_read_b64_tr_b16 a[158:159], v5 offset:35848\n"
    "s_waitcnt lgkmcnt(0)\n"
    "s_mov_b32 s26, -2\n"
    ".Lb2_po6_%=:\n"
    "buffer_load_dword v8, v6, s[0:3], s16 offen sc1\n"
    "s_waitcnt vmcnt(0)\n"
    "v_cmp_gt_i32 vcc, 1, v8\n"
    "s_nop 0\n"
    "s_cbranch_vccz .Lb2_pd7_%=\n"
    "s_add_u32 s20, s20, 1\n"
    "s_cmp_lt_u32 s20, 0x40000\n"
    "s_cbranch_scc1 .Lb2_sp8_%=\n"
    "v_mov_b32 v8, 70\n"
    "s_mov_b32 s24, 33728\n"
    "buffer_store_dword v8, off, s[0:3], s24 sc1\n"
    "s_waitcnt vmcnt(0)\n"
    "s_branch .Lb2_pd7_%=\n"
    ".Lb2_sp8_%=:\n"
    "s_sleep 4\n"
    "s_branch .Lb2_po6_%=\n"
    ".Lb2_pd7_%=:\n"
    "s_mov_b32 s26, 0\n"
    "s_lshl_b32 s33, s12, 11\n"
    "s_mov_b32 s35, 0\n"
    "v_mov_b32 v84, 0\n"
    ".Lb2_top_%=:\n"
    "s_lshl_b32 s28, s26, 19\n"
    "s_lshl_b32 s25, s26, 11\n"
    "s_xor_b32 s25, s25, s13\n"
    "s_and_b32 s25, s25, 0x2000\n"
    "s_add_u32 s28, s28, s25\n"
    "s_add_u32 s28, s28, s14\n"
    "s_add_u32 s27, s26, 1\n"
    "s_sub_u32 s29, s28, 81920\n"
    "s_sub_u32 s31, s28, 163840\n"
    "s_xor_b32 s31, s31, 0x2000\n"
    "s_mul_i32 s30, s26, 40960\n"
    "s_cmp_eq_u32 s13, 0\n"
    "s_cselect_b32 s30, s31, s30\n"
    "s_and_b32 s32, s26, 32\n"
    "s_add_u32 s32, s32, s26\n"
    "s_lshl_b32 s32, s32, 8\n"
    "s_add_u32 s32, s32, s17\n"
    "s_cmp_lg_u32 s12, 0\n"
    "s_cbranch_scc1 .Lb2_pd10_%=\n"
    "s_add_u32 s25, s22, 252\n"
    "v_min3_i32 v84, v84, v85, v86\n"
    "v_min_i32 v84, v84, v87\n"
    "v_cmp_gt_i32 vcc, s27, v84\n"
    "s_nop 0\n"
    "s_cbranch_vccz .Lb2_pd10_%=\n"
    ".Lb2_rt11_%=:\n"
    "s_memrealtime s[22:23]\n"
    "s_waitcnt lgkmcnt(0)\n"
    "s_sub_u32 s24, s35, s22\n"
    "s_cmp_lt_u32 s24, 600\n"
    "s_cbranch_scc1 .Lb2_rt11_%=\n"
    ".Lb2_po9_%=:\n"
    "buffer_load_dwordx4 v[84:87], v225, s[0:3], s15 offen sc1\n"
    "s_memrealtime s[22:23]\n"
    "s_waitcnt vmcnt(0) lgkmcnt(0)\n"
    "s_add_u32 s25, s22, 252\n"
    "v_min3_i32 v84, v84, v85, v86\n"
    "v_min_i32 v84, v84, v87\n"
    "v_cmp_gt_i32 vcc, s27, v84\n"
    "s_nop 0\n"
    "s_cbranch_vccz .Lb2_pd10_%=\n"
    "s_add_u32 s20, s20, 1\n"
    "s_cmp_lt_u32 s20, 0x40000\n"
    "s_cbranch_scc1 .Lb2_sp12_%=\n"
    "v_mov_b32 v84, 71\n"
    "s_mov_b32 s24, 33728\n"
    "buffer_store_dword v84, off, s[0:3], s24 sc1\n"
    "s_waitcnt vmcnt(0)\n"
    "s_branch .Lb2_pd10_%=\n"
    ".Lb2_sp12_%=:\n"
    "s_sleep 6\n"
    "s_branch .Lb2_po9_%=\n"
    ".Lb2_pd10_%=:\n"
    "s_barrier\n"
    "s_mov_b32 s35, s25\n"
    ".Lb2_mk0x12_%=:\n"
    "s_add_u32 m0, s33, 0\n"
    "s_nop 0\n"
    "buffer_load_dwordx4 v224, s[0:3], s28 offen sc1 lds\n"
    "buffer_load_dwordx4 v224, s[0:3], s28 offen offset:1024 sc1 lds\n"
    "s_add_u32 m0, s33, 8192\n"
    "s_add_u32 s28, s28, 0x4000\n"
    "buffer_load_dwordx4 v224, s[0:3], s28 offen sc1 lds\n"
    "buffer_load_dwordx4 v224, s[0:3], s28 offen offset:1024 sc1 lds\n"
    "s_add_u32 m0, s33, 16384\n"
    "s_add_u32 s28, s28, 0x4000\n"
    "buffer_load_dwordx4 v224, s[0:3], s28 offen sc1 lds\n"
    "buffer_load_dwordx4 v224, s[0:3], s28 offen offset:1024 sc1 lds\n"
    "s_add_u32 m0, s33, 24576\n"
    "s_add_u32 s28, s28, 0x4000\n"
    "buffer_load_dwordx4 v224, s[0:3], s28 offen sc1 lds\n"
    "buffer_load_dwordx4 v224, s[0:3], s28 offen offset:1024 sc1 lds\n"
    "s_add_u32 m0, s33, 32768\n"
    "s_add_u32 s28, s28, 0x4000\n"
    "buffer_load_dwordx4 v224, s[0:3], s28 offen sc1 lds\n"
    "buffer_load_dwordx4 v224, s[0:3], s28 offen offset:1024 sc1 lds\n"
    "buffer_load_dwordx4 v[48:51], v227, s[0:3], s29 offen offset:0 sc1\n"
    "buffer_load_dwordx4 v[52:55], v227, s[0:3], s29 offen offset:1024 sc1\n"
    "s_waitcnt vmcnt(10)\n"
    "s_barrier\n"
    ".Lb2_mk1x13_%=:\n"
    "ds_read_b128 v[16:19], v224 offset:0\n"
    "ds_read_b128 v[20:23], v224 offset:1024\n"
    "ds_read_b128 v[24:27], v224 offset:2048\n"
    "ds_read_b128 v[28:31], v224 offset:3072\n"
    "ds_read_b128 v[32:35], v224 offset:4096\n"
    "ds_read_b128 v[36:39], v224 offset:5120\n"
    "s_waitcnt lgkmcnt(4)\n"
    "v_mfma_f32_16x16x32_bf16 v[0:3], a[0:3], v[16:19], 0\n"
    "ds_read_b128 v[40:43], v224 offset:6144\n"
    "ds_read_b128 v[44:47], v224 offset:7168\n"
    "v_mfma_f32_16x16x32_bf16 v[8:11], a[0:3], v[20:23], 0\n"
    "v_mfma_f32_16x16x32_bf16 v[4:7], a[80:83], v[16:19], 0\n"
    "v_mfma_f32_16x16x32_bf16 v[12:15], a[80:83], v[20:23], 0\n"
    "s_waitcnt lgkmcnt(4)\n"
    "v_mfma_f32_16x16x32_bf16 v[0:3], a[4:7], v[24:27], v[0:3]\n"
    "s_waitcnt vmcnt(8)\n"
    "s_barrier\n"
    "ds_read_b128 v[16:19], v224 offset:8192\n"
    "v_mfma_f32_16x16x32_bf16 v[8:11], a[4:7], v[28:31], v[8:11]\n"
    "ds_read_b128 v[20:23], v224 offset:9216\n"
    "v_mfma_f32_16x16x32_bf16 v[4:7], a[84:87], v[24:27], v[4:7]\n"
    "v_mfma_f32_16x16x32_bf16 v[12:15], a[84:87], v[28:31], v[12:15]\n"
    "s_waitcnt lgkmcnt(4)\n"
    "v_mfma_f32_16x16x32_bf16 v[0:3], a[8:11], v[32:35], v[0:3]\n"
    "ds_read_b128 v[24:27], v224 offset:10240\n"
    "ds_read_b128 v[28:31], v224 offset:11264\n"
    "v_mfma_f32_16x16x32_bf16 v[8:11], a[8:11], v[36:39], v[8:11]\n"
    "v_mfma_f32_16x16x32_bf16 v[4:7], a[88:91], v[32:35], v[4:7]\n"
    "v_mfma_f32_16x16x32_bf16 v[12:15], a[88:91], v[36:39], v[12:15]\n"
    "s_waitcnt lgkmcnt(4)\n"
    "v_mfma_f32_16x16x32_bf16 v[0:3], a[12:15], v[40:43], v[0:3]\n"
    "ds_read_b128 v[32:35], v224 offset:12288\n"
    "ds_read_b128 v[36:39], v224 offset:13312\n"
    "v_mfma_f32_16x16x32_bf16 v[8:11], a[12:15], v[44:47], v[8:11]\n"
    "v_mfma_f32_16x16x32_bf16 v[4:7], a[92:95], v[40:43], v[4:7]\n"
    "v_mfma_f32_16x16x32_bf16 v[12:15], a[92:95], v[44:47], v[12:15]\n"
    "s_waitcnt lgkmcnt(4)\n"
    "v_mfma_f32_16x16x32_bf16 v[0:3], a[16:19], v[16:19], v[0:3]\n"
    "ds_read_b128 v[40:43], v224 offset:14336\n"
    "ds_read_b128 v[44:47], v224 offset:15360\n"
    "v_mfma_f32_16x16x32_bf16 v[8:11], a[16:19], v[20:23], v[8:11]\n"
    "v_mfma_f32_16x16x32_bf16 v[4:7], a[96:99], v[16:19], v[4:7]\n"
    "v_mfma_f32_16x16x32_bf16 v[12:15], a[96:99], v[20:23], v[12:15]\n"
    "s_waitcnt lgkmcnt(4)\n"
    "v_mfma_f32_16x16x32_bf16 v[0:3], a[20:23], v[24:27], v[0:3]\n"
    "s_waitcnt vmcnt(6)\n"
    "s_barrier\n"
    "ds_read_b128 v[16:19], v224 offset:16384\n"
    "v_mfma_f32_16x16x32_bf16 v[8:11], a[20:23], v[28:31], v[8:11]\n"
    "ds_read_b128 v[20:23], v224 offset:17408\n"
    "v_mfma_f32_16x16x32_bf16 v[4:7], a[100:103], v[24:27], v[4:7]\n"
    "v_mfma_f32_16x16x32_bf16 v[12:15], a[100:103], v[28:31], v[12:15]\n"
    "s_waitcnt lgkmcnt(4)\n"
    "v_mfma_f32_16x16x32_bf16 v[0:3], a[24:27], v[32:35], v[0:3]\n"
    "ds_read_b128 v[24:27], v224 offset:18432\n"
    "ds_read_b128 v[28:31], v224 offset:19456\n"
    "v_mfma_f32_16x16x32_bf16 v[8:11], a[24:27], v[36:39], v[8:11]\n"
    "v_mfma_f32_16x16x32_bf16 v[4:7], a[104:107], v[32:35], v[4:7]\n"
    "v_mfma_f32_16x16x32_bf16 v[12:15], a[104:107], v[36:39], v[12:15]\n"
    "s_waitcnt lgkmcnt(4)\n"
    "v_mfma_f32_16x16x32_bf16 v[0:3], a[28:31], v[40:43], v[0:3]\n"
    "ds_read_b128 v[32:35], v224 offset:20480\n"
    "ds_read_b128 v[36:39], v224 offset:21504\n"
    "v_mfma_f32_16x16x32_bf16 v[8:11], a[28:31], v[44:47], v[8:11]\n"
    "v_mfma_f32_16x16x32_bf16 v[4:7], a[108:111], v[40:43], v[4:7]\n"
    "v_mfma_f32_16x16x32_bf16 v[12:15], a[108:111], v[44:47], v[12:15]\n"
    "s_waitcnt lgkmcnt(4)\n"
    "v_mfma_f32_16x16x32_bf16 v[0:3], a[32:35], v[16:19], v[0:3]\n"
    "ds_read_b128 v[40:43], v224 offset:22528\n"
    "ds_read_b128 v[44:47], v224 offset:23552\n"
    "v_mfma_f32_16x16x32_bf16 v[8:11], a[32:35], v[20:23], v[8:11]\n"
    "v_mfma_f32_16x16x32_bf16 v[4:7], a[112:115], v[16:19], v[4:7]\n"
    "v_mfma_f32_16x16x32_bf16 v[12:15], a[112:115], v[20:23], v[12:15]\n"
    "s_waitcnt lgkmcnt(4)\n"
    "v_mfma_f32_16x16x32_bf16 v[0:3], a[36:39], v[24:27], v[0:3]\n"
    "s_waitcnt vmcnt(4)\n"
    "s_barrier\n"
    "ds_read_b128 v[16:19], v224 offset:24576\n"
    "v_mfma_f32_16x16x32_bf16 v[8:11], a[36:39], v[28:31], v[8:11]\n"
    "ds_read_b128 v[20:23], v224 offset:25600\n"
    "v_mfma_f32_16x16x32_bf16 v[4:7], a[116:119], v[24:27], v[4:7]\n"
    "v_mfma_f32_16x16x32_bf16 v[12:15], a[116:119], v[28:31], v[12:15]\n"
    "s_waitcnt lgkmcnt(4)\n"
    "v_mfma_f32_16x16x32_bf16 v[0:3], a[40:43], v[32:35], v[0:3]\n"
    "ds_read_b128 v[24:27], v224 offset:26624\n"
    "ds_read_b128 v[28:31], v224 offset:27648\n"
    "v_mfma_f32_16x16x32_bf16 v[8:11], a[40:43], v[36:39], v[8:11]\n"
    "v_mfma_f32_16x16x32_bf16 v[4:7], a[120:123], v[32:35], v[4:7]\n"
    "v_mfma_f32_16x16x32_bf16 v[12:15], a[120:123], v[36:39], v[12:15]\n"
    "s_waitcnt lgkmcnt(4)\n"
    "v_mfma_f32_16x16x32_bf16 v[0:3], a[44:47], v[40:43], v[0:3]\n"
    "ds_read_b128 v[32:35], v224 offset:28672\n"
    "ds_read_b128 v[36:39], v224 offset:29696\n"
    "v_mfma_f32_16x16x32_bf16 v[8:11], a[44:47], v[44:47], v[8:11]\n"
    "v_mfma_f32_16x16x32_bf16 v[4:7], a[124:127], v[40:43], v[4:7]\n"
    "v_mfma_f32_16x16x32_bf16 v[12:15], a[124:127], v[44:47], v[12:15]\n"
    "s_waitcnt lgkmcnt(4)\n"
    "v_mfma_f32_16x16x32_bf16 v[0:3], a[48:51], v[16:19], v[0:3]\n"
    "ds_read_b128 v[40:43], v224 offset:30720\n"
    "ds_read_b128 v[44:47], v224 offset:31744\n"
    "v_mfma_f32_16x16x32_bf16 v[8:11], a[48:51], v[20:23], v[8:11]\n"
    "v_mfma_f32_16x16x32_bf16 v[4:7], a[128:131], v[16:19], v[4:7]\n"
    "v_mfma_f32_16x16x32_bf16 v[12:15], a[128:131], v[20:23], v[12:15]\n"
    "s_waitcnt lgkmcnt(4)\n"
    "v_mfma_f32_16x16x32_bf16 v[0:3], a[52:55], v[24:27], v[0:3]\n"
    "s_waitcnt vmcnt(2)\n"
    "s_barrier\n"
    "ds_read_b128 v[16:19], v224 offset:32768\n"
    "v_mfma_f32_16x16x32_bf16 v[8:11], a[52:55], v[28:31], v[8:11]\n"
    "ds_read_b128 v[20:23], v224 offset:33792\n"
    "v_mfma_f32_16x16x32_bf16 v[4:7], a[132:135], v[24:27], v[4:7]\n"
    "v_mfma_f32_16x16x32_bf16 v[12:15], a[132:135], v[28:31], v[12:15]\n"
    "s_waitcnt lgkmcnt(4)\n"
    "v_mfma_f32_16x16x32_bf16 v[0:3], a[56:59], v[32:35], v[0:3]\n"
    "ds_read_b128 v[24:27], v224 offset:34816\n"
    "ds_read_b128 v[28:31], v224 offset:35840\n"
    "v_mfma_f32_16x16x32_bf16 v[8:11], a[56:59], v[36:39], v[8:11]\n"
    "v_mfma_f32_16x16x32_bf16 v[4:7], a[136:139], v[32:35], v[4:7]\n"
    "s_waitcnt vmcnt(0)\n"
    "v_lshlrev_b32 v96, 16, v48\n"
    "v_and_b32 v97, 0xffff0000, v48\n"
    "v_mfma_f32_16x16x32_bf16 v[12:15], a[136:139], v[36:39], v[12:15]\n"
    "s_waitcnt lgkmcnt(4)\n"
    "v_lshlrev_b32 v98, 16, v49\n"
    "v_and_b32 v99, 0xffff0000, v49\n"
    "v_mfma_f32_16x16x32_bf16 v[0:3], a[60:63], v[40:43], v[0:3]\n"
    "s_waitcnt lgkmcnt(2)\n"
    "ds_read_b128 v[32:35], v224 offset:36864\n"
    "ds_read_b128 v[36:39], v224 offset:37888\n"
    "v_mfma_f32_16x16x32_bf16 v[8:11], a[60:63], v[44:47], v[8:11]\n"
    "v_swap_b32 v18, v20\n"
    "v_mfma_f32_16x16x32_bf16 v[4:7], a[140:143], v[40:43], v[4:7]\n"
    "v_swap_b32 v19, v21\n"
    "v_mfma_f32_16x16x32_bf16 v[12:15], a[140:143], v[44:47], v[12:15]\n"
    "v_lshlrev_b32 v100, 16, v50\n"
    "v_and_b32 v101, 0xffff0000, v50\n"
    "v_mfma_f32_16x16x32_bf16 v[0:3], a[64:67], v[16:19], v[0:3]\n"
    "s_waitcnt lgkmcnt(2)\n"
    "ds_read_b128 v[40:43], v224 offset:38912\n"
    "ds_read_b128 v[44:47], v224 offset:39936\n"
    "v_mfma_f32_16x16x32_bf16 v[8:11], a[64:67], v[20:23], v[8:11]\n"
    "v_swap_b32 v26, v28\n"
    "v_mfma_f32_16x16x32_bf16 v[4:7], a[144:147], v[16:19], v[4:7]\n"
    "v_swap_b32 v27, v29\n"
    "v_mfma_f32_16x16x32_bf16 v[12:15], a[144:147], v[20:23], v[12:15]\n"
    "v_lshlrev_b32 v102, 16, v51\n"
    "v_and_b32 v103, 0xffff0000, v51\n"
    "v_mfma_f32_16x16x32_bf16 v[0:3], a[68:71], v[24:27], v[0:3]\n"
    "s_waitcnt lgkmcnt(2)\n"
    "v_lshlrev_b32 v104, 16, v52\n"
    "v_and_b32 v105, 0xffff0000, v52\n"
    "v_mfma_f32_16x16x32_bf16 v[8:11], a[68:71], v[28:31], v[8:11]\n"
    "v_swap_b32 v34, v36\n"
    "v_mfma_f32_16x16x32_bf16 v[4:7], a[148:151], v[24:27], v[4:7]\n"
    "v_swap_b32 v35, v37\n"
    "v_mfma_f32_16x16x32_bf16 v[12:15], a[148:151], v[28:31], v[12:15]\n"
    "v_lshlrev_b32 v106, 16, v53\n"
    "v_and_b32 v107, 0xffff0000, v53\n"
    "v_mfma_f32_16x16x32_bf16 v[0:3], a[72:75], v[32:35], v[0:3]\n"
    "s_waitcnt lgkmcnt(0)\n"
    "v_lshlrev_b32 v108, 16, v54\n"
    "v_and_b32 v109, 0xffff0000, v54\n"
    "v_mfma_f32_16x16x32_bf16 v[8:11], a[72:75], v[36:39], v[8:11]\n"
    "v_swap_b32 v42, v44\n"
    "v_mfma_f32_16x16x32_bf16 v[4:7], a[152:155], v[32:35], v[4:7]\n"
    "v_swap_b32 v43, v45\n"
    "v_mfma_f32_16x16x32_bf16 v[12:15], a[152:155], v[36:39], v[12:15]\n"
    "v_lshlrev_b32 v110, 16, v55\n"
    "v_and_b32 v111, 0xffff0000, v55\n"
    "v_mfma_f32_16x16x32_bf16 v[0:3], a[76:79], v[40:43], v[0:3]\n"
    "v_mfma_f32_16x16x32_bf16 v[8:11], a[76:79], v[44:47], v[8:11]\n"
    "v_mfma_f32_16x16x32_bf16 v[4:7], a[156:159], v[40:43], v[4:7]\n"
    "v_mfma_f32_16x16x32_bf16 v[12:15], a[156:159], v[44:47], v[12:15]\n"
    ".Lb2_mk2x14_%=:\n"
    "v_pk_add_f32 v[0:1], v[0:1], v[96:97]\n"
    "v_pk_add_f32 v[2:3], v[2:3], v[98:99]\n"
    "v_pk_add_f32 v[8:9], v[8:9], v[104:105]\n"
    "v_pk_add_f32 v[10:11], v[10:11], v[106:107]\n"
    "s_nop 0\n"
    "v_pk_add_f32 v[4:5], v[4:5], v[100:101]\n"
    "v_pk_add_f32 v[6:7], v[6:7], v[102:103]\n"
    "s_nop 0\n"
    "v_pk_add_f32 v[12:13], v[12:13], v[108:109]\n"
    "v_pk_add_f32 v[14:15], v[14:15], v[110:111]\n"
    "s_cmp_lg_u32 s13, 0\n"
    "s_cbranch_scc1 .Lb2_out_%=\n"
    "v_pk_add_f32 v[64:65], v[0:1], v[2:3]\n"
    "v_pk_add_f32 v[68:69], v[8:9], v[10:11]\n"
    "v_pk_mul_f32 v[66:67], v[0:1], v[0:1]\n"
    "v_pk_mul_f32 v[70:71], v[8:9], v[8:9]\n"
    "v_pk_add_f32 v[96:97], v[4:5], v[6:7]\n"
    "v_pk_add_f32 v[98:99], v[12:13], v[14:15]\n"
    "v_pk_fma_f32 v[66:67], v[2:3], v[2:3], v[66:67]\n"
    "v_pk_fma_f32 v[70:71], v[10:11], v[10:11], v[70:71]\n"
    "v_pk_add_f32 v[64:65], v[64:65], v[96:97]\n"
    "v_pk_add_f32 v[68:69], v[68:69], v[98:99]\n"
    "v_pk_fma_f32 v[66:67], v[4:5], v[4:5], v[66:67]\n"
    "v_pk_fma_f32 v[70:71], v[12:13], v[12:13], v[70:71]\n"
    "v_add_f32 v64, v64, v65\n"
    "v_add_f32 v68, v68, v69\n"
    "v_pk_fma_f32 v[66:67], v[6:7], v[6:7], v[66:67]\n"
    "v_pk_fma_f32 v[70:71], v[14:15], v[14:15], v[70:71]\n"
    "v_add_f32 v66, v66, v67\n"
    "v_add_f32 v70, v70, v71\n"
    "s_nop 0\n"
    "v_permlane16_swap_b32 v64, v66\n"
    "v_permlane16_swap_b32 v68, v70\n"
    "v_add_f32 v64, v64, v66\n"
    "v_add_f32 v68, v68, v70\n"
    "s_nop 1\n"
    "v_permlane32_swap_b32 v64, v68\n"
    "v_add_f32 v64, v64, v68\n"
    "v_min_u32 v64, -2, v64\n"
    "v_mov_b32 v65, v64\n"
    "v_cvt_pk_bf16_f32 v56, v0, v1\n"
    "v_cvt_pk_bf16_f32 v57, v2, v3\n"
    "v_permlane16_swap_b32 v64, v65\n"
    "v_mov_b32 v66, v64\n"
    "v_mov_b32 v67, v65\n"
    "v_cvt_pk_bf16_f32 v58, v4, v5\n"
    "v_cvt_pk_bf16_f32 v59, v6, v7\n"
    "v_permlane32_swap_b32 v64, v66\n"
    "v_permlane32_swap_b32 v65, v67\n"
    "buffer_store_dwordx4 v[64:67], v230, s[0:3], s32 offen sc1\n"
    "v_cvt_pk_bf16_f32 v60, v8, v9\n"
    "v_cvt_pk_bf16_f32 v61, v10, v11\n"
    "v_cvt_pk_bf16_f32 v62, v12, v13\n"
    "v_cvt_pk_bf16_f32 v63, v14, v15\n"
    ".Lb2_mk3x15_%=:\n"
    "v_lshlrev_b32 v0, 16, v56\n"
    "v_and_b32 v1, 0xffff0000, v56\n"
    "v_lshlrev_b32 v2, 16, v57\n"
    "v_and_b32 v3, 0xffff0000, v57\n"
    "v_lshlrev_b32 v4, 16, v58\n"
    "v_and_b32 v5, 0xffff0000, v58\n"
    "v_lshlrev_b32 v6, 16, v59\n"
    "v_and_b32 v7, 0xffff0000, v59\n"
    "v_lshlrev_b32 v8, 16, v60\n"
    "v_and_b32 v9, 0xffff0000, v60\n"
    "v_lshlrev_b32 v10, 16, v61\n"
    "v_and_b32 v11, 0xffff0000, v61\n"
    "v_lshlrev_b32 v12, 16, v62\n"
    "v_and_b32 v13, 0xffff0000, v62\n"
    "v_lshlrev_b32 v14, 16, v63\n"
    "v_and_b32 v15, 0xffff0000, v63\n"
    "s_sleep 6\n"
    "buffer_load_dwordx4 v[64:67], v231, s[0:3], s32 offen sc1\n"
    "buffer_load_dwordx4 v[68:71], v232, s[0:3], s32 offen sc1\n"
    "buffer_load_dwordx4 v[72:75], v233, s[0:3], s32 offen sc1\n"
    "buffer_load_dwordx4 v[76:79], v234, s[0:3], s32 offen sc1\n"
    "buffer_load_dwordx4 v[80:83], v235, s[0:3], s32 offen sc1\n"
    "buffer_store_dwordx4 v[56:59], v228, s[8:11], s30 offen sc1\n"
    "buffer_store_dwordx4 v[60:63], v229, s[8:11], s30 offen sc1\n"
    ".Lb2_sl_%=:\n"
    "s_waitcnt vmcnt(6)\n"
    "v_max_u32 v84, v64, v64\n"
    "s_waitcnt vmcnt(5)\n"
    "v_max_u32 v84, v68, v84\n"
    "v_pk_add_f32 v[64:65], v[64:65], v[68:69]\n"
    "v_pk_add_f32 v[66:67], v[66:67], v[70:71]\n"
    "s_waitcnt vmcnt(4)\n"
    "v_max_u32 v84, v72, v84\n"
    "v_pk_add_f32 v[64:65], v[64:65], v[72:73]\n"
    "v_pk_add_f32 v[66:67], v[66:67], v[74:75]\n"
    "s_waitcnt vmcnt(3)\n"
    "v_max_u32 v84, v76, v84\n"
    "v_pk_add_f32 v[64:65], v[64:65], v[76:77]\n"
    "v_pk_add_f32 v[66:67], v[66:67], v[78:79]\n"
    "s_waitcnt vmcnt(2)\n"
    "v_max_u32 v84, v80, v84\n"
    "v_pk_add_f32 v[64:65], v[64:65], v[80:81]\n"
    "v_pk_add_f32 v[66:67], v[66:67], v[82:83]\n"
    "v_cmp_eq_u32 vcc, -1, v84\n"
    "s_nop 0\n"
    "s_cbranch_vccnz .Lb2_sr_%=\n"
    ".Lb2_sg_%=:\n"
    ".Lb2_mk4x16_%=:\n"
    "v_permlane16_swap_b32 v64, v65\n"
    "v_permlane16_swap_b32 v66, v67\n"
    "v_add_f32 v64, v64, v65\n"
    "v_add_f32 v66, v66, v67\n"
    "s_nop 1\n"
    "v_permlane32_swap_b32 v64, v66\n"
    "v_add_f32 v64, v64, v66\n"
    "v_mov_b32 v66, v64\n"
    "s_nop 1\n"
    "v_permlane16_swap_b32 v64, v66\n"
    "v_mul_f32 v64, 0x3acccccd, v64\n"
    "v_mul_f32 v66, 0x3acccccd, v66\n"
    "v_fma_f32 v66, -v64, v64, v66\n"
    "v_max_f32 v66, 0, v66\n"
    "v_cvt_pk_bf16_f32 v64, v64, v66\n"
    "v_and_b32 v66, 0xffff0000, v64\n"
    "v_lshlrev_b32 v64, 16, v64\n"
    "v_add_f32 v66, 0x3727c5ac, v66\n"
    "v_mov_b32 v68, v64\n"
    "v_cvt_pk_bf16_f32 v66, v66, v66\n"
    "v_lshlrev_b32 v66, 16, v66\n"
    "v_sqrt_f32 v66, v66\n"
    "v_permlane32_swap_b32 v64, v68\n"
    "v_cvt_pk_bf16_f32 v66, v66, v66\n"
    "v_lshlrev_b32 v66, 16, v66\n"
    "v_rcp_f32 v66, v66\n"
    "s_nop 0\n"
    "v_mov_b32 v70, v66\n"
    "s_nop 1\n"
    "v_permlane32_swap_b32 v66, v70\n"
    "v_mul_f32 v64, v64, v66\n"
    "v_mul_f32 v68, v68, v70\n"
    "v_pk_fma_f32 v[0:1], v[0:1], v[66:67], v[64:65] op_sel_hi:[1,0,0] neg_lo:[0,0,1] neg_hi:[0,0,1]\n"
    "v_pk_fma_f32 v[2:3], v[2:3], v[66:67], v[64:65] op_sel_hi:[1,0,0] neg_lo:[0,0,1] neg_hi:[0,0,1]\n"
    "v_pk_fma_f32 v[4:5], v[4:5], v[66:67], v[64:65] op_sel_hi:[1,0,0] neg_lo:[0,0,1] neg_hi:[0,0,1]\n"
    "v_pk_fma_f32 v[6:7], v[6:7], v[66:67], v[64:65] op_sel_hi:[1,0,0] neg_lo:[0,0,1] neg_hi:[0,0,1]\n"
    "v_cvt_pk_bf16_f32 v96, v0, v1\n"
    "v_cvt_pk_bf16_f32 v98, v2, v3\n"
    "v_cvt_pk_bf16_f32 v97, v4, v5\n"
    "v_cvt_pk_bf16_f32 v99, v6, v7\n"
    "buffer_store_dwordx4 v[96:99], v227, s[0:3], s31 offen offset:0 sc1\n"
    "v_pk_fma_f32 v[8:9], v[8:9], v[70:71], v[68:69] op_sel_hi:[1,0,0] neg_lo:[0,0,1] neg_hi:[0,0,1]\n"
    "v_pk_fma_f32 v[10:11], v[10:11], v[70:71], v[68:69] op_sel_hi:[1,0,0] neg_lo:[0,0,1] neg_hi:[0,0,1]\n"
    "v_pk_fma_f32 v[12:13], v[12:13], v[70:71], v[68:69] op_sel_hi:[1,0,0] neg_lo:[0,0,1] neg_hi:[0,0,1]\n"
    "v_pk_fma_f32 v[14:15], v[14:15], v[70:71], v[68:69] op_sel_hi:[1,0,0] neg_lo:[0,0,1] neg_hi:[0,0,1]\n"
    "v_cvt_pk_bf16_f32 v100, v8, v9\n"
    "v_cvt_pk_bf16_f32 v102, v10, v11\n"
    "v_cvt_pk_bf16_f32 v101, v12, v13\n"
    "v_cvt_pk_bf16_f32 v103, v14, v15\n"
    "buffer_store_dwordx4 v[100:103], v227, s[0:3], s31 offen offset:1024 sc1\n"
    "v_mov_b32 v88, s27\n"
    "v_mov_b32 v89, s27\n"
    "v_mov_b32 v90, s27\n"
    "v_mov_b32 v91, s27\n"
    ".Lb2_mk5x17_%=:\n"
    "v_mov_b32 v84, 0\n"
    "s_memrealtime s[22:23]\n"
    "s_cmp_lg_u32 s12, 0\n"
    "s_waitcnt lgkmcnt(0)\n"
    "s_cbranch_scc1 .Lb2_pl19_%=\n"
    "s_sub_u32 s24, s35, s22\n"
    "s_cmp_gt_i32 s24, 40\n"
    "s_cbranch_scc1 .Lb2_pl19_%=\n"
    "buffer_load_dwordx4 v[84:87], v225, s[0:3], s15 offen sc1\n"
    ".Lb2_pl19_%=:\n"
    "s_waitcnt vmcnt(0)\n"
    "s_barrier\n"
    "buffer_store_dwordx4 v[88:91], v226, s[0:3], 0 offen sc1\n"
    ".Lb2_mk6x19_%=:\n"
    ".Lb2_nx_%=:\n"
    "s_mov_b32 s26, s27\n"
    "s_cmp_lt_u32 s26, 64\n"
    "s_cbranch_scc1 .Lb2_top_%=\n"
    "s_waitcnt vmcnt(0) lgkmcnt(0)\n"
    "s_endpgm\n"
    ".Lb2_tk0_%=:\n"
    ".Lb2_po3_%=:\n"
    "buffer_load_dword v31, v30, s[0:3], 0 offen sc1\n"
    "s_waitcnt vmcnt(0)\n"
    "v_cmp_gt_i32 vcc, 32, v31\n"
    "s_nop 0\n"
    "s_cbranch_vccz .Lb2_pd4_%=\n"
    "s_add_u32 s20, s20, 1\n"
    "s_cmp_lt_u32 s20, 0x40000\n"
    "s_cbranch_scc1 .Lb2_sp5_%=\n"
    "v_mov_b32 v31, 90\n"
    "s_mov_b32 s24, 33728\n"
    "buffer_store_dword v31, off, s[0:3], s24 sc1\n"
    "s_waitcnt vmcnt(0)\n"
    "s_branch .Lb2_pd4_%=\n"
    ".Lb2_sp5_%=:\n"
    "s_sleep 8\n"
    "s_branch .Lb2_po3_%=\n"
    ".Lb2_pd4_%=:\n"
    "s_branch .Lb2_tk1_%=\n"
    ".Lb2_out_%=:\n"
    "v_cvt_pk_bf16_f32 v56, v0, v1\n"
    "v_cvt_pk_bf16_f32 v57, v2, v3\n"
    "v_cvt_pk_bf16_f32 v58, v4, v5\n"
    "v_cvt_pk_bf16_f32 v59, v6, v7\n"
    "v_cvt_pk_bf16_f32 v60, v8, v9\n"
    "v_cvt_pk_bf16_f32 v61, v10, v11\n"
    "v_cvt_pk_bf16_f32 v62, v12, v13\n"
    "v_cvt_pk_bf16_f32 v63, v14, v15\n"
    "buffer_store_dwordx4 v[56:59], v228, s[8:11], s30 offen\n"
    "buffer_store_dwordx4 v[60:63], v229, s[8:11], s30 offen\n"
    "v_mov_b32 v84, 0\n"
    "s_branch .Lb2_nx_%=\n"
    ".Lb2_sr_%=:\n"
    "s_add_u32 s20, s20, 1\n"
    "s_cmp_lt_u32 s20, 0x40000\n"
    "s_cbranch_scc1 .Lb2_sp21_%=\n"
    "v_mov_b32 v84, 72\n"
    "s_mov_b32 s24, 33728\n"
    "buffer_store_dword v84, off, s[0:3], s24 sc1\n"
    "s_waitcnt vmcnt(0)\n"
    "s_branch .Lb2_sg_%=\n"
    ".Lb2_sp21_%=:\n"
    "s_sleep 2\n"
    "buffer_load_dwordx4 v[64:67], v231, s[0:3], s32 offen sc1\n"
    "buffer_load_dwordx4 v[68:71], v232, s[0:3], s32 offen sc1\n"
    "buffer_load_dwordx4 v[72:75], v233, s[0:3], s32 offen sc1\n"
    "buffer_load_dwordx4 v[76:79], v234, s[0:3], s32 offen sc1\n"
    "buffer_load_dwordx4 v[80:83], v235, s[0:3], s32 offen sc1\n"
    "s_waitcnt vmcnt(0)\n"
    "s_branch .Lb2_sl_%=\n"
    :: "{s[0:3]}"(s0_), "{s[4:7]}"(s4_), "{s[8:11]}"(s8_), "{s[12:15]}"(s12_), "{s[16:19]}"(s16_), "{v[0:3]}"(v0_), "{v[4:7]}"(v4_), "{v[224:227]}"(v224_), "{v[228:231]}"(v228_), "{v[232:235]}"(v232_) : "memory", "v255", "a255", "s95");
    __builtin_unreachable();
}
#define SWP 112 - ((k - 24) >> 2) * 16
#define BOX A.ar - 262144
#define NP 4
constexpr int MEGA_LDS = 163840;
__global__ void __launch_bounds__(256, 1) mega_kernel(MegaArgs A) {
    extern __shared__ __attribute__((aligned(16))) char smem[];
    int* sRole = (int*)smem;
    const int x4 = (int)(__builtin_amdgcn_s_getreg((3 << 11) | 20) & 3u);
    const rsrc_t rf = mk_rsrc(arena_base(A.ar), ARENA);
    unsigned* const cnt = (unsigned*)(arena_base(A.ar) + xl(Z_TK, 0));
    u64* const box = (u64*)(BOX);
    const int ln = threadIdx.x & 63;
    static_assert(2 * (Z_END - Z_TK) == 256 * 640, "");
    if (threadIdx.x < 64) {
        bst16(rf, ln < 40 ? 2 * Z_TK + blockIdx.x * 640 + ln * 16 : OOB, u32x4{0u, 0u, 0u, 0u});
        int want;
        if (blockIdx.x == 0) {
            DRAIN();
            u64 sent[4];
            for (int n = 0;; ++n) {
                u64 v[4];
#pragma unroll
                for (int k = 0; k < 4; ++k) v[k] = __hip_atomic_load(box + 8 * (((64 * k + ln) & 31) * 8 + ((64 * k + ln) >> 5)), RLX_AGENT);
                const unsigned c = n < 2 ? 0u : __hip_atomic_load(cnt + 16 * (ln & 7), RLX_AGENT);
                const unsigned open = (unsigned)__ballot(c < 32u) & 255u;
                want = __ffs(open) - 1;
                if ((open & (open - 1)) == 0 && __all(c >= 31u)) break;
#pragma unroll
                for (int k = 0; k < 4; ++k) { if (n == 0 || v[k] != sent[k]) __hip_atomic_store(box + 2048 + 64 * k + ln, v[k], RLX_AGENT); sent[k] = v[k]; }
                if (n > 0) __builtin_amdgcn_s_sleep(16);
            }
        } else {
            const u64 mine = __builtin_amdgcn_s_memrealtime();
            if (threadIdx.x == 0) __hip_atomic_store(box + 8 * blockIdx.x, mine, RLX_AGENT);
            unsigned tm[2] = {~0u, ~0u};
            const unsigned pr = (blockIdx.x >> 7) * 16384 + (blockIdx.x & 127) * 64;
            if (threadIdx.x < 2) bst4(rf, pr + threadIdx.x * 8192, 0u);
            DRAIN();
#pragma unroll
            for (int k = 0; k < NP; ++k) {
                const unsigned long long t0 = __builtin_amdgcn_s_memtime();
                if (threadIdx.x == 0) bst4(rf, pr + (k & 1) * 8192, 0u);
                DRAIN();
                tm[k & 1] = min(tm[k & 1], (unsigned)(__builtin_amdgcn_s_memtime() - t0));
            }
            while (__hip_atomic_load(box + 2048 + (blockIdx.x & 7) * 32 + (blockIdx.x >> 3), RLX_AGENT) != mine) __builtin_amdgcn_s_sleep(4);
            want = (tm[1] < tm[0] ? 4 : 0) + x4;
        }
        asm volatile("" ::: "memory");
        if (threadIdx.x == 0) {
            int role = -1;
            for (int p = -1; role < 0; ++p) {
                const int pool = p < 0 ? want : p, x = pool & 3;
                const int k = (int)__hip_atomic_fetch_add(cnt + 16 * pool, 1u, RLX_AGENT);
                if (k < 32) role = (pool >> 2) * 128 + (k < 24 ? (k >> 3) * 32 + 8 * x + (k & 7) : SWP + 4 * x + (k & 3));
            }
            *sRole = role;
        }
    }
    __syncthreads();
    const int role = RFL(*sRole), L = role >> 7, r = role & 127;
    __syncthreads();
    const bool on = (A.layers >> L) & 1;
    if (r < 32) { if (on) qk_role(A, L, r, x4); }
    else if (r < 64) { if (on) vo_role(A, L, r - 32, x4); }
    else if (r < 96) ct_role(A, L, r - 64, smem);
    else if (r < 112) { if (on) { if (r < 107) b1_role(A, L, r - 96, x4); else b2_role(A, L, r - 107, x4); } }
    else qw_role(A, L, 8 * ((r - 112) >> 2) + 4 * L + (r & 3), x4);
}

#define LAYERS 3
extern "C" void kernel_launch(void* const* d_in, const int* in_sizes, int n_in, void* d_out, int out_size, void* d_ws, size_t ws_size, hipStream_t stream) {
    MegaArgs A;
    A.x0 = (const bf16_t*)d_in[0]; A.mem0 = (const bf16_t*)d_in[1]; A.Wq = (const bf16_t*)d_in[2]; A.Wk = (const bf16_t*)d_in[3]; A.Wv = (const bf16_t*)d_in[4];
    A.Wo = (const bf16_t*)d_in[5]; A.wqkv = (const bf16_t*)d_in[8]; A.wo = (const bf16_t*)d_in[9];
    A.ar = (char*)d_ws + 266240;
    A.out = (bf16_t*)d_out; A.layers = LAYERS; A.pad = 0;
    (void)hipFuncSetAttribute((const void*)mega_kernel, hipFuncAttributeMaxDynamicSharedMemorySize, MEGA_LDS);
    mega_kernel<<<256, 256, MEGA_LDS, stream>>>(A);
}
```

```cpp
#include <hip/hip_runtime.h>
#include <hip/hip_bf16.h>
#include <stdint.h>

typedef unsigned short bf16_t;
typedef short bf16x8 __attribute__((ext_vector_type(8)));
typedef short bf16x4 __attribute__((ext_vector_type(4)));
typedef float f32x4 __attribute__((ext_vector_type(4)));
typedef unsigned u32x4 __attribute__((ext_vector_type(4)));
typedef unsigned u32x2 __attribute__((ext_vector_type(2)));
typedef unsigned long long u64;
typedef __bf16 bf16v2 __attribute__((ext_vector_type(2)));
typedef float f32v2 __attribute__((ext_vector_type(2)));
#define DEV __device__ __forceinline__
DEV unsigned pk2(float a, float b) { f32v2 f = {a, b}; bf16v2 r = __builtin_convertvector(f, bf16v2); return __builtin_bit_cast(unsigned, r); }
DEV float rb(float a) { return __uint_as_float((pk2(a, 0.f) & 0xffffu) << 16); }
DEV float lo16(unsigned u) { return __uint_as_float(u << 16); }
DEV float hi16(unsigned u) { return __uint_as_float(u & 0xffff0000u); }
constexpr int D_ = 640;
#define RLX_AGENT __ATOMIC_RELAXED, __HIP_MEMORY_SCOPE_AGENT
#define AUX_SC1 16
typedef __amdgpu_buffer_rsrc_t rsrc_t;
DEV rsrc_t mk_rsrc(const void* p, unsigned bytes) { return __builtin_amdgcn_make_buffer_rsrc((void*)p, 0, (int)bytes, 0x00020000); }
DEV void bst16(rsrc_t r, unsigned off, u32x4 v) { __builtin_amdgcn_raw_buffer_store_b128(v, r, off, 0, AUX_SC1); }
DEV void bst8(rsrc_t r, unsigned off, u32x2 v) { __builtin_amdgcn_raw_buffer_store_b64(v, r, off, 0, AUX_SC1); }
DEV void bst4(rsrc_t r, unsigned off, unsigned v) { __builtin_amdgcn_raw_buffer_store_b32(v, r, off, 0, AUX_SC1); }
#define DRAIN() asm volatile("s_waitcnt vmcnt(0)" ::: "memory")
#define DRAIN_BARRIER() asm volatile("s_waitcnt vmcnt(0) lgkmcnt(0)\n\ts_barrier" ::: "memory")
#define RFL(x) ((unsigned)__builtin_amdgcn_readfirstlane((int)(x)))
DEV unsigned lo(const void* p) { return (unsigned)(size_t)p; }
DEV unsigned hi(const void* p) { return (unsigned)((size_t)p >> 32) & 0xffffu; }
DEV unsigned nosent(unsigned x) { return min(x, 0xfffffffeu); }
DEV int sw2(int x) { return ((x & 1) << 1) | (x >> 1); }

DEV unsigned xl(unsigned o, int side) { const unsigned ph = ((o >> 13) << 14) | (o & 8191u); return ph | ((((ph >> 21) ^ side) & 1u) << 13); }
constexpr unsigned Z_TK = 16384, Z_QV = Z_TK + 1024, Z_CT = Z_QV + 1024, Z_QW = Z_CT + 2048, Z_X = 3 * 8192, Z_X1 = 7 * 8192, Z_XN = 9 * 8192, Z_O = 10 * 8192, Z_ST = 11 * 8192, Z_END = 12 * 8192;
constexpr unsigned RG_UT = 1u << 21, RG_UTX = RG_UT + (1u << 20), RG_UM = RG_UTX + 262144, RG_UMX = RG_UM + 262144, RG_PT = RG_UMX + 65536, RG_PM = RG_PT + 65536, RG_MEM = RG_PM + 32768;
constexpr unsigned SLOT = 1u << 22;
constexpr unsigned REC0 = 20u << 20, RC_X1 = 5 * 16384, RC_O = 10 * 16384, RC_X2 = 15 * 16384, ST0 = 1u << 20, QT0 = 52u << 20, DBG0 = 116u << 20, ARENA = 120u << 20;
DEV unsigned rec(int s, int L) { return REC0 + (s << 19) + ((((s >> 2) ^ L) & 1) << 13); }
constexpr unsigned OOB = 0x7f000000u, WBYTES = 640u * 6144u * 2u;
DEV char* arena_base(char* p) { return p + ((0 - (size_t)p) & 0x3fffff); }

struct MegaArgs {
    const bf16_t* x0; const bf16_t* mem0; const bf16_t* Wq; const bf16_t* Wk; const bf16_t* Wv; const bf16_t* Wo; const bf16_t* wqkv; const bf16_t* wo;
    char* ar; bf16_t* out; int layers, pad;
};
DEV bool all_drawn(char* ar) { return __all(__hip_atomic_load((unsigned*)(arena_base(ar) + xl(Z_TK, 0)) + 16 * (threadIdx.x & 7), RLX_AGENT) >= 32u); }
DEV void wait_drawn(char* ar) { while (!all_drawn(ar)) __builtin_amdgcn_s_sleep(8); asm volatile("" ::: "memory"); }
#define ROLE_HEAD const int l = threadIdx.x & 63, l16 = l & 15, lq = l >> 4, w = RFL(threadIdx.x >> 6); char* const ab = arena_base(A.ar); \
    const rsrc_t rr = mk_rsrc(ab, ARENA); const u32x4 rx = {lo(ab), hi(ab), ARENA, 0x20000u}; const u32x4 ones = {~0u, ~0u, ~0u, ~0u}; const u32x2 ones2 = {~0u, ~0u}
DEV void qk_role(const MegaArgs& A, const int L, const int h, const int rep) {
    ROLE_HEAD;
#define XP 10
    const int i = 4 * (w & 1) + lq, p = l16 < 8 ? l16 >> 2 : l16 & 1, rho = 4 * (i >> 1) + 2 * p + (i & 1), tp = 4 * w + lq;
    const unsigned pm = (w >= 2 || l16 >= XP) ? OOB : xl(RG_PM + (h >> 3) * 8192 +
        (l16 < 8 ? ((h & 7) >> 1) * 1024 + ((2 * (h & 1) + ((l16 >> 1) & 1)) * 16 + rho) * 16 + (l16 & 1) * 8 : 4096 + (((h & 7) >> 1) * 16 + rho) * 16 + (h & 1) * 8), L);
    const unsigned pt = xl(RG_PT + h * 2048 + tp * 16 + (l16 < 8 ? (l16 & 3) * 256 + (l16 >> 2) * 8 : (l16 & 1) * 8 + (l16 < 10 ? 1792 : (l16 < 14 ? 1024 + ((l16 - 10) >> 1) * 256 : 1536))), L);
    u64 cm[4];
    const int k0 = (l16 >= 4 && l16 < 8) ? 4 * (l16 - 4) : ((l16 >= 10 && l16 < 14) ? 4 * (l16 - 10) : -4);
#pragma unroll
    for (int r = 0; r < 4; ++r) cm[r] = __ballot(k0 + r <= tp);
    const u32x4 s0_ = {RFL(rx[0]), RFL(rx[1]), RFL(rx[2]), RFL(rx[3])};
    const u32x4 s4_ = {RFL(lo(A.Wk + h * 192)), RFL(hi(A.Wk + h * 192)), RFL(WBYTES - h * 384), RFL(0x20000u)};
    const u32x4 s8_ = {RFL(lo(A.Wq + h * 192)), RFL(hi(A.Wq + h * 192)), RFL(WBYTES - h * 384), RFL(0x20000u)};
    const u32x4 s12_ = {RFL((unsigned)cm[0]), RFL((unsigned)(cm[0] >> 32)), RFL((unsigned)cm[1]), RFL((unsigned)(cm[1] >> 32))};
    const u32x4 s16_ = {RFL((unsigned)cm[2]), RFL((unsigned)(cm[2] >> 32)), RFL((unsigned)cm[3]), RFL((unsigned)(cm[3] >> 32))};
    const u32x4 s20_ = {RFL(w < 2 ? ~0u : 1u), RFL(w < 2 ? ~0u : 0u), RFL(w), RFL(L << 13)};
    const u32x4 s24_ = {RFL(xl(RG_MEM + rep * 16384, L) + w * 2048), RFL(w < 2 ? xl(RG_MEM + rep * 16384 + 8192, L) + w * 1024 - 2048 : OOB), RFL(w * 11264 + 8192), RFL(REC0 + w * 16384)};
    const u32x4 s28_ = {RFL(65536 - 4096 - w * 14336), RFL(QT0 + h * 32768 + w * 2048), RFL(h == 0 && w == 0 ? xl(Z_X, L) : OOB), RFL(xl(Z_X, L))};
    const u32x4 s32_ = {RFL(xl(Z_CT + (h & 7) * 256, L)), RFL(xl(Z_XN + rep * 2048, 0)), RFL(xl(Z_QW + h * 128, L)), RFL(w == 0 ? xl(Z_QV + h * 4, L) : OOB)};
    const u32x4 s36_ = {RFL(DBG0 + ((L * 128 + h) * 4 + w) * 2048), RFL(0u), RFL(0u), RFL(0u)};
    const u32x4 v0_ = {(unsigned)(((3 * w + 0) * 64 + l) / 24 * 12288 + ((((3 * w + 0) * 64 + l) % 24) ^ ((((3 * w + 0) * 64 + l) / 48) & 6)) * 16 - 0), (unsigned)(((3 * w + 1) * 64 + l) / 24 * 12288 + ((((3 * w + 1) * 64 + l) % 24) ^ ((((3 * w + 1) * 64 + l) / 48) & 6)) * 16 - 1024), (unsigned)(((3 * w + 2) * 64 + l) / 24 * 12288 + ((((3 * w + 2) * 64 + l) % 24) ^ ((((3 * w + 2) * 64 + l) / 48) & 6)) * 16 - 2048), (unsigned)((8 * lq + ((l >> 2) & 1) + ((l >> 1) & 4)) * 384 + (w ^ (((8 * lq + ((l >> 2) & 1) + ((l >> 1) & 4)) >> 2) & 3)) * 32 + (l & 3) * 8 + ((w + 0) & 3) * 12288)};
    const u32x4 v4_ = {(unsigned)((8 * lq + ((l >> 2) & 1) + ((l >> 1) & 4)) * 384 + (w ^ (((8 * lq + ((l >> 2) & 1) + ((l >> 1) & 4)) >> 2) & 3)) * 32 + (l & 3) * 8 + ((w + 1) & 3) * 12288), (unsigned)((8 * lq + ((l >> 2) & 1) + ((l >> 1) & 4)) * 384 + (w ^ (((8 * lq + ((l >> 2) & 1) + ((l >> 1) & 4)) >> 2) & 3)) * 32 + (l & 3) * 8 + ((w + 2) & 3) * 12288), (unsigned)((8 * lq + ((l >> 2) & 1) + ((l >> 1) & 4)) * 384 + (w ^ (((8 * lq + ((l >> 2) & 1) + ((l >> 1) & 4)) >> 2) & 3)) * 32 + (l & 3) * 8 + ((w + 3) & 3) * 12288), (unsigned)((8 * lq + ((l >> 2) & 1) + ((l >> 1) & 4)) * 384 + (w ^ (((8 * lq + ((l >> 2) & 1) + ((l >> 1) & 4)) >> 2) & 3)) * 32 + (l & 3) * 8)};
    const u32x4 v92_ = {(unsigned)(l * 16 + ((w + 0) & 3) * 11264), (unsigned)(l * 16 + ((w + 1) & 3) * 11264), (unsigned)(l * 16 + ((w + 2) & 3) * 11264), (unsigned)(l * 16 + ((w + 3) & 3) * 11264)};
    const u32x4 v96_ = {(unsigned)(l * 16), (unsigned)(l * 16 + 53248 + w * 20480), (unsigned)((l & 2) ? 0xeeeeu : 0x4444u), (unsigned)(0xff800000u)};
    const u32x4 v100_ = {(unsigned)(l16 == 0 + 4 * lq ? 0x3f80u : (l16 == 0 + 4 * lq + 1 ? 0x3f800000u : 0u)), (unsigned)(l16 == 2 + 4 * lq ? 0x3f80u : (l16 == 2 + 4 * lq + 1 ? 0x3f800000u : 0u)), (unsigned)(135168 + w * 1936 + (2 * (l16 >> 2) + (l & 1)) * 240 + (l & 2) * 32 + lq * 16), (unsigned)(135168 + (l16 < 10 ? (4 * (w & 1) + lq) * 240 + l16 * 16 : 1920))};
    const u32x4 v104_ = {(unsigned)(142912 + w * 4352 + l16 * 272 + lq * 16), (unsigned)(142912 + (4 * w + lq) * 272 + l16 * 16), (unsigned)(pm), (unsigned)(pt)};
    asm volatile(
    "s_mov_b32 s40, 0\n"
    "s_mov_b64 s[42:43], 0\n"
    "s_mov_b32 s45, 0\n"
    "s_cmp_eq_u32 s23, 0\n"
    "s_cselect_b32 s44, 0, 28\n"
    "s_sub_u32 s44, s44, 0\n"
    ".Lqk_ho1_%=:\n"
    "s_sub_u32 s44, s44, 1\n"
    "s_cbranch_scc1 .Lqk_hd2_%=\n"
    "s_sleep 17\n"
    "s_branch .Lqk_ho1_%=\n"
    ".Lqk_hd2_%=:\n"
    "s_mul_i32 s51, s22, 3072\n"
    "s_mov_b32 s60, 0\n"
    "s_add_u32 m0, s51, 0\n"
    "s_nop 0\n"
    "buffer_load_dwordx4 v0, s[8:11], s60 offen offset:0 nt lds\n"
    "buffer_load_dwordx4 v1, s[8:11], s60 offen offset:1024 nt lds\n"
    "buffer_load_dwordx4 v2, s[8:11], s60 offen offset:2048 nt lds\n"
    "s_mov_b32 s60, 1572864\n"
    "s_add_u32 m0, s51, 12288\n"
    "s_nop 0\n"
    "buffer_load_dwordx4 v0, s[8:11], s60 offen offset:0 nt lds\n"
    "buffer_load_dwordx4 v1, s[8:11], s60 offen offset:1024 nt lds\n"
    "buffer_load_dwordx4 v2, s[8:11], s60 offen offset:2048 nt lds\n"
    "s_mov_b32 s60, 3145728\n"
    "s_add_u32 m0, s51, 24576\n"
    "s_nop 0\n"
    "buffer_load_dwordx4 v0, s[8:11], s60 offen offset:0 nt lds\n"
    "buffer_load_dwordx4 v1, s[8:11], s60 offen offset:1024 nt lds\n"
    "buffer_load_dwordx4 v2, s[8:11], s60 offen offset:2048 nt lds\n"
    "s_mov_b32 s60, 4718592\n"
    "s_add_u32 m0, s51, 36864\n"
    "s_nop 0\n"
    "buffer_load_dwordx4 v0, s[8:11], s60 offen offset:0 nt lds\n"
    "buffer_load_dwordx4 v1, s[8:11], s60 offen offset:1024 nt lds\n"
    "buffer_load_dwordx4 v2, s[8:11], s60 offen offset:2048 nt lds\n"
    "s_mov_b32 s60, 393216\n"
    "s_add_u32 m0, s51, 49152\n"
    "s_nop 0\n"
    "buffer_load_dwordx4 v0, s[8:11], s60 offen offset:0 nt lds\n"
    "buffer_load_dwordx4 v1, s[8:11], s60 offen offset:1024 nt lds\n"
    "buffer_load_dwordx4 v2, s[8:11], s60 offen offset:2048 nt lds\n"
    "s_mov_b32 s60, 1966080\n"
    "s_add_u32 m0, s51, 61440\n"
    "s_nop 0\n"
    "buffer_load_dwordx4 v0, s[8:11], s60 offen offset:0 nt lds\n"
    "buffer_load_dwordx4 v1, s[8:11], s60 offen offset:1024 nt lds\n"
    "buffer_load_dwordx4 v2, s[8:11], s60 offen offset:2048 nt lds\n"
    "s_mov_b32 s60, 3538944\n"
    "s_add_u32 m0, s51, 73728\n"
    "s_nop 0\n"
    "buffer_load_dwordx4 v0, s[8:11], s60 offen offset:0 nt lds\n"
    "buffer_load_dwordx4 v1, s[8:11], s60 offen offset:1024 nt lds\n"
    "buffer_load_dwordx4 v2, s[8:11], s60 offen offset:2048 nt lds\n"
    "s_mov_b32 s60, 5111808\n"
    "s_add_u32 m0, s51, 86016\n"
    "s_nop 0\n"
    "buffer_load_dwordx4 v0, s[8:11], s60 offen offset:0 nt lds\n"
    "buffer_load_dwordx4 v1, s[8:11], s60 offen offset:1024 nt lds\n"
    "buffer_load_dwordx4 v2, s[8:11], s60 offen offset:2048 nt lds\n"
    "s_mov_b32 s60, 786432\n"
    "s_add_u32 m0, s51, 98304\n"
    "s_nop 0\n"
    "buffer_load_dwordx4 v0, s[8:11], s60 offen offset:0 nt lds\n"
    "buffer_load_dwordx4 v1, s[8:11], s60 offen offset:1024 nt lds\n"
    "buffer_load_dwordx4 v2, s[8:11], s60 offen offset:2048 nt lds\n"
    "s_mov_b32 s60, 2359296\n"
    "s_add_u32 m0, s51, 110592\n"
    "s_nop 0\n"
    "buffer_load_dwordx4 v0, s[8:11], s60 offen offset:0 nt lds\n"
    "buffer_load_dwordx4 v1, s[8:11], s60 offen offset:1024 nt lds\n"
    "buffer_load_dwordx4 v2, s[8:11], s60 offen offset:2048 nt lds\n"
    "s_mov_b32 s60, 3932160\n"
    "s_add_u32 m0, s51, 122880\n"
    "s_nop 0\n"
    "buffer_load_dwordx4 v0, s[8:11], s60 offen offset:0 nt lds\n"
    "buffer_load_dwordx4 v1, s[8:11], s60 offen offset:1024 nt lds\n"
    "buffer_load_dwordx4 v2, s[8:11], s60 offen offset:2048 nt lds\n"
    "s_mov_b32 s60, 5505024\n"
    "s_add_u32 m0, s51, 135168\n"
    "s_nop 0\n"
    "buffer_load_dwordx4 v0, s[8:11], s60 offen offset:0 nt lds\n"
    "buffer_load_dwordx4 v1, s[8:11], s60 offen offset:1024 nt lds\n"
    "buffer_load_dwordx4 v2, s[8:11], s60 offen offset:2048 nt lds\n"
    "v_mov_b64 v[108:109], -1\n"
    "buffer_store_dwordx2 v[108:109], v106, s[0:3], 0 offen sc1\n"
    "buffer_store_dwordx2 v[108:109], v107, s[0:3], 0 offen sc1\n"
    "s_waitcnt vmcnt(26)\n"
    "s_barrier\n"
    "v_add_u32 v8, 0, v3\n"
    "ds_read_b64_tr_b16 a[240:241], v8 offset:0\n"
    "ds_read_b64_tr_b16 a[242:243], v8 offset:768\n"
    "ds_read_b64_tr_b16 v[176:177], v8 offset:128\n"
    "ds_read_b64_tr_b16 v[178:179], v8 offset:896\n"
    "ds_read_b64_tr_b16 v[12:13], v8 offset:256\n"
    "ds_read_b64_tr_b16 v[14:15], v8 offset:1024\n"
    "v_add_u32 v9, 0, v4\n"
    "ds_read_b64_tr_b16 v[112:113], v9 offset:0\n"
    "ds_read_b64_tr_b16 v[114:115], v9 offset:768\n"
    "ds_read_b64_tr_b16 v[192:193], v9 offset:128\n"
    "ds_read_b64_tr_b16 v[194:195], v9 offset:896\n"
    "ds_read_b64_tr_b16 v[28:29], v9 offset:256\n"
    "ds_read_b64_tr_b16 v[30:31], v9 offset:1024\n"
    "v_add_u32 v10, 0, v5\n"
    "ds_read_b64_tr_b16 v[128:129], v10 offset:0\n"
    "ds_read_b64_tr_b16 v[130:131], v10 offset:768\n"
    "ds_read_b64_tr_b16 v[208:209], v10 offset:128\n"
    "ds_read_b64_tr_b16 v[210:211], v10 offset:896\n"
    "ds_read_b64_tr_b16 v[44:45], v10 offset:256\n"
    "ds_read_b64_tr_b16 v[46:47], v10 offset:1024\n"
    "v_add_u32 v11, 0, v6\n"
    "ds_read_b64_tr_b16 v[144:145], v11 offset:0\n"
    "ds_read_b64_tr_b16 v[146:147], v11 offset:768\n"
    "ds_read_b64_tr_b16 v[224:225], v11 offset:128\n"
    "ds_read_b64_tr_b16 v[226:227], v11 offset:896\n"
    "ds_read_b64_tr_b16 v[60:61], v11 offset:256\n"
    "ds_read_b64_tr_b16 v[62:63], v11 offset:1024\n"
    "s_waitcnt lgkmcnt(0)\n"
    "s_barrier\n"
    "s_mov_b32 s60, 1179648\n"
    "s_add_u32 m0, s51, 0\n"
    "s_nop 0\n"
    "buffer_load_dwordx4 v0, s[8:11], s60 offen offset:0 nt lds\n"
    "buffer_load_dwordx4 v1, s[8:11], s60 offen offset:1024 nt lds\n"
    "buffer_load_dwordx4 v2, s[8:11], s60 offen offset:2048 nt lds\n"
    "s_mov_b32 s60, 2752512\n"
    "s_add_u32 m0, s51, 12288\n"
    "s_nop 0\n"
    "buffer_load_dwordx4 v0, s[8:11], s60 offen offset:0 nt lds\n"
    "buffer_load_dwordx4 v1, s[8:11], s60 offen offset:1024 nt lds\n"
    "buffer_load_dwordx4 v2, s[8:11], s60 offen offset:2048 nt lds\n"
    "s_mov_b32 s60, 4325376\n"
    "s_add_u32 m0, s51, 24576\n"
    "s_nop 0\n"
    "buffer_load_dwordx4 v0, s[8:11], s60 offen offset:0 nt lds\n"
    "buffer_load_dwordx4 v1, s[8:11], s60 offen offset:1024 nt lds\n"
    "buffer_load_dwordx4 v2, s[8:11], s60 offen offset:2048 nt lds\n"
    "s_mov_b32 s60, 5898240\n"
    "s_add_u32 m0, s51, 36864\n"
    "s_nop 0\n"
    "buffer_load_dwordx4 v0, s[8:11], s60 offen offset:0 nt lds\n"
    "buffer_load_dwordx4 v1, s[8:11], s60 offen offset:1024 nt lds\n"
    "buffer_load_dwordx4 v2, s[8:11], s60 offen offset:2048 nt lds\n"
    "v_and_b32 v108, 0x70, v96\n"
    "v_lshlrev_b32 v108, 2, v108\n"
    "v_or_b32 v108, 0x8000, v108\n"
    "buffer_load_dword v109, v108, s[0:3], 0 offen sc1\n"
    "s_waitcnt vmcnt(27)\n"
    "s_barrier\n"
    "v_add_u32 v8, 49152, v3\n"
    "ds_read_b64_tr_b16 a[244:245], v8 offset:0\n"
    "ds_read_b64_tr_b16 a[246:247], v8 offset:768\n"
    "ds_read_b64_tr_b16 v[180:181], v8 offset:128\n"
    "ds_read_b64_tr_b16 v[182:183], v8 offset:896\n"
    "ds_read_b64_tr_b16 v[16:17], v8 offset:256\n"
    "ds_read_b64_tr_b16 v[18:19], v8 offset:1024\n"
    "v_add_u32 v9, 49152, v4\n"
    "ds_read_b64_tr_b16 v[116:117], v9 offset:0\n"
    "ds_read_b64_tr_b16 v[118:119], v9 offset:768\n"
    "ds_read_b64_tr_b16 v[196:197], v9 offset:128\n"
    "ds_read_b64_tr_b16 v[198:199], v9 offset:896\n"
    "ds_read_b64_tr_b16 v[32:33], v9 offset:256\n"
    "ds_read_b64_tr_b16 v[34:35], v9 offset:1024\n"
    "v_add_u32 v10, 49152, v5\n"
    "ds_read_b64_tr_b16 v[132:133], v10 offset:0\n"
    "ds_read_b64_tr_b16 v[134:135], v10 offset:768\n"
    "ds_read_b64_tr_b16 v[212:213], v10 offset:128\n"
    "ds_read_b64_tr_b16 v[214:215], v10 offset:896\n"
    "ds_read_b64_tr_b16 v[48:49], v10 offset:256\n"
    "ds_read_b64_tr_b16 v[50:51], v10 offset:1024\n"
    "v_add_u32 v11, 49152, v6\n"
    "ds_read_b64_tr_b16 v[148:149], v11 offset:0\n"
    "ds_read_b64_tr_b16 v[150:151], v11 offset:768\n"
    "ds_read_b64_tr_b16 v[228:229], v11 offset:128\n"
    "ds_read_b64_tr_b16 v[230:231], v11 offset:896\n"
    "ds_read_b64_tr_b16 v[64:65], v11 offset:256\n"
    "ds_read_b64_tr_b16 v[66:67], v11 offset:1024\n"
    "s_waitcnt lgkmcnt(0)\n"
    "s_barrier\n"
    "s_mov_b32 s60, 6291456\n"
    "s_add_u32 m0, s51, 49152\n"
    "s_nop 0\n"
    "buffer_load_dwordx4 v0, s[8:11], s60 offen offset:0 nt lds\n"
    "buffer_load_dwordx4 v1, s[8:11], s60 offen offset:1024 nt lds\n"
    "buffer_load_dwordx4 v2, s[8:11], s60 offen offset:2048 nt lds\n"
    "s_mov_b32 s60, 6684672\n"
    "s_add_u32 m0, s51, 61440\n"
    "s_nop 0\n"
    "buffer_load_dwordx4 v0, s[8:11], s60 offen offset:0 nt lds\n"
    "buffer_load_dwordx4 v1, s[8:11], s60 offen offset:1024 nt lds\n"
    "buffer_load_dwordx4 v2, s[8:11], s60 offen offset:2048 nt lds\n"
    "s_mov_b32 s60, 7077888\n"
    "s_add_u32 m0, s51, 73728\n"
    "s_nop 0\n"
    "buffer_load_dwordx4 v0, s[8:11], s60 offen offset:0 nt lds\n"
    "buffer_load_dwordx4 v1, s[8:11], s60 offen offset:1024 nt lds\n"
    "buffer_load_dwordx4 v2, s[8:11], s60 offen offset:2048 nt lds\n"
    "s_mov_b32 s60, 7471104\n"
    "s_add_u32 m0, s51, 86016\n"
    "s_nop 0\n"
    "buffer_load_dwordx4 v0, s[8:11], s60 offen offset:0 nt lds\n"
    "buffer_load_dwordx4 v1, s[8:11], s60 offen offset:1024 nt lds\n"
    "buffer_load_dwordx4 v2, s[8:11], s60 offen offset:2048 nt lds\n"
    "s_waitcnt vmcnt(27)\n"
    "s_barrier\n"
    "v_add_u32 v8, 98304, v3\n"
    "ds_read_b64_tr_b16 a[248:249], v8 offset:0\n"
    "ds_read_b64_tr_b16 a[250:251], v8 offset:768\n"
    "ds_read_b64_tr_b16 v[184:185], v8 offset:128\n"
    "ds_read_b64_tr_b16 v[186:187], v8 offset:896\n"
    "ds_read_b64_tr_b16 v[20:21], v8 offset:256\n"
    "ds_read_b64_tr_b16 v[22:23], v8 offset:1024\n"
    "v_add_u32 v9, 98304, v4\n"
    "ds_read_b64_tr_b16 v[120:121], v9 offset:0\n"
    "ds_read_b64_tr_b16 v[122:123], v9 offset:768\n"
    "ds_read_b64_tr_b16 v[200:201], v9 offset:128\n"
    "ds_read_b64_tr_b16 v[202:203], v9 offset:896\n"
    "ds_read_b64_tr_b16 v[36:37], v9 offset:256\n"
    "ds_read_b64_tr_b16 v[38:39], v9 offset:1024\n"
    "v_add_u32 v10, 98304, v5\n"
    "ds_read_b64_tr_b16 v[136:137], v10 offset:0\n"
    "ds_read_b64_tr_b16 v[138:139], v10 offset:768\n"
    "ds_read_b64_tr_b16 v[216:217], v10 offset:128\n"
    "ds_read_b64_tr_b16 v[218:219], v10 offset:896\n"
    "ds_read_b64_tr_b16 v[52:53], v10 offset:256\n"
    "ds_read_b64_tr_b16 v[54:55], v10 offset:1024\n"
    "v_add_u32 v11, 98304, v6\n"
    "ds_read_b64_tr_b16 v[152:153], v11 offset:0\n"
    "ds_read_b64_tr_b16 v[154:155], v11 offset:768\n"
    "ds_read_b64_tr_b16 v[232:233], v11 offset:128\n"
    "ds_read_b64_tr_b16 v[234:235], v11 offset:896\n"
    "ds_read_b64_tr_b16 v[68:69], v11 offset:256\n"
    "ds_read_b64_tr_b16 v[70:71], v11 offset:1024\n"
    "s_waitcnt lgkmcnt(0)\n"
    "s_barrier\n"
    "s_mov_b32 s60, 0\n"
    "s_add_u32 m0, s51, 98304\n"
    "s_nop 0\n"
    "buffer_load_dwordx4 v0, s[4:7], s60 offen offset:0 nt lds\n"
    "buffer_load_dwordx4 v1, s[4:7], s60 offen offset:1024 nt lds\n"
    "buffer_load_dwordx4 v2, s[4:7], s60 offen offset:2048 nt lds\n"
    "s_mov_b32 s60, 1572864\n"
    "s_add_u32 m0, s51, 110592\n"
    "s_nop 0\n"
    "buffer_load_dwordx4 v0, s[4:7], s60 offen offset:0 nt lds\n"
    "buffer_load_dwordx4 v1, s[4:7], s60 offen offset:1024 nt lds\n"
    "buffer_load_dwordx4 v2, s[4:7], s60 offen offset:2048 nt lds\n"
    "s_mov_b32 s60, 3145728\n"
    "s_add_u32 m0, s51, 122880\n"
    "s_nop 0\n"
    "buffer_load_dwordx4 v0, s[4:7], s60 offen offset:0 nt lds\n"
    "buffer_load_dwordx4 v1, s[4:7], s60 offen offset:1024 nt lds\n"
    "buffer_load_dwordx4 v2, s[4:7], s60 offen offset:2048 nt lds\n"
    "s_mov_b32 s60, 4718592\n"
    "s_add_u32 m0, s51, 135168\n"
    "s_nop 0\n"
    "buffer_load_dwordx4 v0, s[4:7], s60 offen offset:0 nt lds\n"
    "buffer_load_dwordx4 v1, s[4:7], s60 offen offset:1024 nt lds\n"
    "buffer_load_dwordx4 v2, s[4:7], s60 offen offset:2048 nt lds\n"
    "s_waitcnt vmcnt(25)\n"
    "s_barrier\n"
    "v_add_u32 v8, 0, v3\n"
    "ds_read_b64_tr_b16 a[252:253], v8 offset:0\n"
    "ds_read_b64_tr_b16 a[254:255], v8 offset:768\n"
    "ds_read_b64_tr_b16 v[188:189], v8 offset:128\n"
    "ds_read_b64_tr_b16 v[190:191], v8 offset:896\n"
    "ds_read_b64_tr_b16 v[24:25], v8 offset:256\n"
    "ds_read_b64_tr_b16 v[26:27], v8 offset:1024\n"
    "v_add_u32 v9, 0, v4\n"
    "ds_read_b64_tr_b16 v[124:125], v9 offset:0\n"
    "ds_read_b64_tr_b16 v[126:127], v9 offset:768\n"
    "ds_read_b64_tr_b16 v[204:205], v9 offset:128\n"
    "ds_read_b64_tr_b16 v[206:207], v9 offset:896\n"
    "ds_read_b64_tr_b16 v[40:41], v9 offset:256\n"
    "ds_read_b64_tr_b16 v[42:43], v9 offset:1024\n"
    "v_add_u32 v10, 0, v5\n"
    "ds_read_b64_tr_b16 v[140:141], v10 offset:0\n"
    "ds_read_b64_tr_b16 v[142:143], v10 offset:768\n"
    "ds_read_b64_tr_b16 v[220:221], v10 offset:128\n"
    "ds_read_b64_tr_b16 v[222:223], v10 offset:896\n"
    "ds_read_b64_tr_b16 v[56:57], v10 offset:256\n"
    "ds_read_b64_tr_b16 v[58:59], v10 offset:1024\n"
    "v_add_u32 v11, 0, v6\n"
    "ds_read_b64_tr_b16 v[156:157], v11 offset:0\n"
    "ds_read_b64_tr_b16 v[158:159], v11 offset:768\n"
    "ds_read_b64_tr_b16 v[236:237], v11 offset:128\n"
    "ds_read_b64_tr_b16 v[238:239], v11 offset:896\n"
    "ds_read_b64_tr_b16 v[72:73], v11 offset:256\n"
    "ds_read_b64_tr_b16 v[74:75], v11 offset:1024\n"
    "s_waitcnt lgkmcnt(0)\n"
    "s_barrier\n"
    "s_mov_b32 s60, 393216\n"
    "s_add_u32 m0, s51, 0\n"
    "s_nop 0\n"
    "buffer_load_dwordx4 v0, s[4:7], s60 offen offset:0 nt lds\n"
    "buffer_load_dwordx4 v1, s[4:7], s60 offen offset:1024 nt lds\n"
    "buffer_load_dwordx4 v2, s[4:7], s60 offen offset:2048 nt lds\n"
    "s_mov_b32 s60, 1966080\n"
    "s_add_u32 m0, s51, 12288\n"
    "s_nop 0\n"
    "buffer_load_dwordx4 v0, s[4:7], s60 offen offset:0 nt lds\n"
    "buffer_load_dwordx4 v1, s[4:7], s60 offen offset:1024 nt lds\n"
    "buffer_load_dwordx4 v2, s[4:7], s60 offen offset:2048 nt lds\n"
    "s_mov_b32 s60, 3538944\n"
    "s_add_u32 m0, s51, 24576\n"
    "s_nop 0\n"
    "buffer_load_dwordx4 v0, s[4:7], s60 offen offset:0 nt lds\n"
    "buffer_load_dwordx4 v1, s[4:7], s60 offen offset:1024 nt lds\n"
    "buffer_load_dwordx4 v2, s[4:7], s60 offen offset:2048 nt lds\n"
    "s_mov_b32 s60, 5111808\n"
    "s_add_u32 m0, s51, 36864\n"
    "s_nop 0\n"
    "buffer_load_dwordx4 v0, s[4:7], s60 offen offset:0 nt lds\n"
    "buffer_load_dwordx4 v1, s[4:7], s60 offen offset:1024 nt lds\n"
    "buffer_load_dwordx4 v2, s[4:7], s60 offen offset:2048 nt lds\n"
    "s_waitcnt vmcnt(24)\n"
    "s_barrier\n"
    "v_add_u32 v8, 49152, v7\n"
    "ds_read_b64_tr_b16 v[160:161], v8 offset:0\n"
    "ds_read_b64_tr_b16 v[162:163], v8 offset:768\n"
    "ds_read_b64_tr_b16 v[240:241], v8 offset:128\n"
    "ds_read_b64_tr_b16 v[242:243], v8 offset:896\n"
    "ds_read_b64_tr_b16 v[76:77], v8 offset:256\n"
    "ds_read_b64_tr_b16 v[78:79], v8 offset:1024\n"
    "v_add_u32 v9, 61440, v7\n"
    "ds_read_b64_tr_b16 v[164:165], v9 offset:0\n"
    "ds_read_b64_tr_b16 v[166:167], v9 offset:768\n"
    "ds_read_b64_tr_b16 v[244:245], v9 offset:128\n"
    "ds_read_b64_tr_b16 v[246:247], v9 offset:896\n"
    "ds_read_b64_tr_b16 v[80:81], v9 offset:256\n"
    "ds_read_b64_tr_b16 v[82:83], v9 offset:1024\n"
    "v_add_u32 v10, 73728, v7\n"
    "ds_read_b64_tr_b16 v[168:169], v10 offset:0\n"
    "ds_read_b64_tr_b16 v[170:171], v10 offset:768\n"
    "ds_read_b64_tr_b16 v[248:249], v10 offset:128\n"
    "ds_read_b64_tr_b16 v[250:251], v10 offset:896\n"
    "ds_read_b64_tr_b16 v[84:85], v10 offset:256\n"
    "ds_read_b64_tr_b16 v[86:87], v10 offset:1024\n"
    "v_add_u32 v11, 86016, v7\n"
    "ds_read_b64_tr_b16 v[172:173], v11 offset:0\n"
    "ds_read_b64_tr_b16 v[174:175], v11 offset:768\n"
    "ds_read_b64_tr_b16 v[252:253], v11 offset:128\n"
    "ds_read_b64_tr_b16 v[254:255], v11 offset:896\n"
    "ds_read_b64_tr_b16 v[88:89], v11 offset:256\n"
    "ds_read_b64_tr_b16 v[90:91], v11 offset:1024\n"
    "s_waitcnt lgkmcnt(0)\n"
    "s_barrier\n"
    "s_waitcnt vmcnt(36)\n"
    "v_cmp_gt_i32 vcc, 32, v109\n"
    "s_nop 0\n"
    "s_cbranch_vccnz .Lqk_tk0_%=\n"
    ".Lqk_tk1_%=:\n"
    "s_mov_b32 s60, 786432\n"
    "s_add_u32 m0, s51, 49152\n"
    "s_nop 0\n"
    "buffer_load_dwordx4 v0, s[4:7], s60 offen offset:0 nt lds\n"
    "buffer_load_dwordx4 v1, s[4:7], s60 offen offset:1024 nt lds\n"
    "buffer_load_dwordx4 v2, s[4:7], s60 offen offset:2048 nt lds\n"
    "s_mov_b32 s60, 2359296\n"
    "s_add_u32 m0, s51, 61440\n"
    "s_nop 0\n"
    "buffer_load_dwordx4 v0, s[4:7], s60 offen offset:0 nt lds\n"
    "buffer_load_dwordx4 v1, s[4:7], s60 offen offset:1024 nt lds\n"
    "buffer_load_dwordx4 v2, s[4:7], s60 offen offset:2048 nt lds\n"
    "s_mov_b32 s60, 3932160\n"
    "s_add_u32 m0, s51, 73728\n"
    "s_nop 0\n"
    "buffer_load_dwordx4 v0, s[4:7], s60 offen offset:0 nt lds\n"
    "buffer_load_dwordx4 v1, s[4:7], s60 offen offset:1024 nt lds\n"
    "buffer_load_dwordx4 v2, s[4:7], s60 offen offset:2048 nt lds\n"
    "s_mov_b32 s60, 5505024\n"
    "s_add_u32 m0, s51, 86016\n"
    "s_nop 0\n"
    "buffer_load_dwordx4 v0, s[4:7], s60 offen offset:0 nt lds\n"
    "buffer_load_dwordx4 v1, s[4:7], s60 offen offset:1024 nt lds\n"
    "buffer_load_dwordx4 v2, s[4:7], s60 offen offset:2048 nt lds\n"
    "v_lshlrev_b32 v108, 4, v96\n"
    "v_mov_b32 v109, 1\n"
    "s_mov_b64 exec, 15\n"
    "buffer_store_dword v109, v108, s[0:3], s35 offen sc1\n"
    "s_mov_b64 exec, -1\n"
    "s_waitcnt vmcnt(25)\n"
    "s_barrier\n"
    "v_add_u32 v8, 98304, v3\n"
    "ds_read_b64_tr_b16 a[0:1], v8 offset:0\n"
    "ds_read_b64_tr_b16 a[2:3], v8 offset:768\n"
    "ds_read_b64_tr_b16 a[80:81], v8 offset:128\n"
    "ds_read_b64_tr_b16 a[82:83], v8 offset:896\n"
    "ds_read_b64_tr_b16 a[160:161], v8 offset:256\n"
    "ds_read_b64_tr_b16 a[162:163], v8 offset:1024\n"
    "v_add_u32 v9, 98304, v4\n"
    "ds_read_b64_tr_b16 a[16:17], v9 offset:0\n"
    "ds_read_b64_tr_b16 a[18:19], v9 offset:768\n"
    "ds_read_b64_tr_b16 a[96:97], v9 offset:128\n"
    "ds_read_b64_tr_b16 a[98:99], v9 offset:896\n"
    "ds_read_b64_tr_b16 a[176:177], v9 offset:256\n"
    "ds_read_b64_tr_b16 a[178:179], v9 offset:1024\n"
    "v_add_u32 v10, 98304, v5\n"
    "ds_read_b64_tr_b16 a[32:33], v10 offset:0\n"
    "ds_read_b64_tr_b16 a[34:35], v10 offset:768\n"
    "ds_read_b64_tr_b16 a[112:113], v10 offset:128\n"
    "ds_read_b64_tr_b16 a[114:115], v10 offset:896\n"
    "ds_read_b64_tr_b16 a[192:193], v10 offset:256\n"
    "ds_read_b64_tr_b16 a[194:195], v10 offset:1024\n"
    "v_add_u32 v11, 98304, v6\n"
    "ds_read_b64_tr_b16 a[48:49], v11 offset:0\n"
    "ds_read_b64_tr_b16 a[50:51], v11 offset:768\n"
    "ds_read_b64_tr_b16 a[128:129], v11 offset:128\n"
    "ds_read_b64_tr_b16 a[130:131], v11 offset:896\n"
    "ds_read_b64_tr_b16 a[208:209], v11 offset:256\n"
    "ds_read_b64_tr_b16 a[210:211], v11 offset:1024\n"
    "s_waitcnt lgkmcnt(0)\n"
    "s_barrier\n"
    "s_mov_b32 s60, 1179648\n"
    "s_add_u32 m0, s51, 98304\n"
    "s_nop 0\n"
    "buffer_load_dwordx4 v0, s[4:7], s60 offen offset:0 nt lds\n"
    "buffer_load_dwordx4 v1, s[4:7], s60 offen offset:1024 nt lds\n"
    "buffer_load_dwordx4 v2, s[4:7], s60 offen offset:2048 nt lds\n"
    "s_mov_b32 s60, 2752512\n"
    "s_add_u32 m0, s51, 110592\n"
    "s_nop 0\n"
    "buffer_load_dwordx4 v0, s[4:7], s60 offen offset:0 nt lds\n"
    "buffer_load_dwordx4 v1, s[4:7], s60 offen offset:1024 nt lds\n"
    "buffer_load_dwordx4 v2, s[4:7], s60 offen offset:2048 nt lds\n"
    "s_mov_b32 s60, 4325376\n"
    "s_add_u32 m0, s51, 122880\n"
    "s_nop 0\n"
    "buffer_load_dwordx4 v0, s[4:7], s60 offen offset:0 nt lds\n"
    "buffer_load_dwordx4 v1, s[4:7], s60 offen offset:1024 nt lds\n"
    "buffer_load_dwordx4 v2, s[4:7], s60 offen offset:2048 nt lds\n"
    "s_mov_b32 s60, 5898240\n"
    "s_add_u32 m0, s51, 135168\n"
    "s_nop 0\n"
    "buffer_load_dwordx4 v0, s[4:7], s60 offen offset:0 nt lds\n"
    "buffer_load_dwordx4 v1, s[4:7], s60 offen offset:1024 nt lds\n"
    "buffer_load_dwordx4 v2, s[4:7], s60 offen offset:2048 nt lds\n"
    "s_waitcnt vmcnt(25)\n"
    "s_barrier\n"
    "v_add_u32 v8, 0, v3\n"
    "ds_read_b64_tr_b16 a[4:5], v8 offset:0\n"
    "ds_read_b64_tr_b16 a[6:7], v8 offset:768\n"
    "ds_read_b64_tr_b16 a[84:85], v8 offset:128\n"
    "ds_read_b64_tr_b16 a[86:87], v8 offset:896\n"
    "ds_read_b64_tr_b16 a[164:165], v8 offset:256\n"
    "ds_read_b64_tr_b16 a[166:167], v8 offset:1024\n"
    "v_add_u32 v9, 0, v4\n"
    "ds_read_b64_tr_b16 a[20:21], v9 offset:0\n"
    "ds_read_b64_tr_b16 a[22:23], v9 offset:768\n"
    "ds_read_b64_tr_b16 a[100:101], v9 offset:128\n"
    "ds_read_b64_tr_b16 a[102:103], v9 offset:896\n"
    "ds_read_b64_tr_b16 a[180:181], v9 offset:256\n"
    "ds_read_b64_tr_b16 a[182:183], v9 offset:1024\n"
    "v_add_u32 v10, 0, v5\n"
    "ds_read_b64_tr_b16 a[36:37], v10 offset:0\n"
    "ds_read_b64_tr_b16 a[38:39], v10 offset:768\n"
    "ds_read_b64_tr_b16 a[116:117], v10 offset:128\n"
    "ds_read_b64_tr_b16 a[118:119], v10 offset:896\n"
    "ds_read_b64_tr_b16 a[196:197], v10 offset:256\n"
    "ds_read_b64_tr_b16 a[198:199], v10 offset:1024\n"
    "v_add_u32 v11, 0, v6\n"
    "ds_read_b64_tr_b16 a[52:53], v11 offset:0\n"
    "ds_read_b64_tr_b16 a[54:55], v11 offset:768\n"
    "ds_read_b64_tr_b16 a[132:133], v11 offset:128\n"
    "ds_read_b64_tr_b16 a[134:135], v11 offset:896\n"
    "ds_read_b64_tr_b16 a[212:213], v11 offset:256\n"
    "ds_read_b64_tr_b16 a[214:215], v11 offset:1024\n"
    "s_waitcnt lgkmcnt(0)\n"
    "s_barrier\n"
    "s_mov_b32 s60, 6291456\n"
    "s_add_u32 m0, s51, 0\n"
    "s_nop 0\n"
    "buffer_load_dwordx4 v0, s[4:7], s60 offen offset:0 nt lds\n"
    "buffer_load_dwordx4 v1, s[4:7], s60 offen offset:1024 nt lds\n"
    "buffer_load_dwordx4 v2, s[4:7], s60 offen offset:2048 nt lds\n"
    "s_mov_b32 s60, 6684672\n"
    "s_add_u32 m0, s51, 12288\n"
    "s_nop 0\n"
    "buffer_load_dwordx4 v0, s[4:7], s60 offen offset:0 nt lds\n"
    "buffer_load_dwordx4 v1, s[4:7], s60 offen offset:1024 nt lds\n"
    "buffer_load_dwordx4 v2, s[4:7], s60 offen offset:2048 nt lds\n"
    "s_mov_b32 s60, 7077888\n"
    "s_add_u32 m0, s51, 24576\n"
    "s_nop 0\n"
    "buffer_load_dwordx4 v0, s[4:7], s60 offen offset:0 nt lds\n"
    "buffer_load_dwordx4 v1, s[4:7], s60 offen offset:1024 nt lds\n"
    "buffer_load_dwordx4 v2, s[4:7], s60 offen offset:2048 nt lds\n"
    "s_mov_b32 s60, 7471104\n"
    "s_add_u32 m0, s51, 36864\n"
    "s_nop 0\n"
    "buffer_load_dwordx4 v0, s[4:7], s60 offen offset:0 nt lds\n"
    "buffer_load_dwordx4 v1, s[4:7], s60 offen offset:1024 nt lds\n"
    "buffer_load_dwordx4 v2, s[4:7], s60 offen offset:2048 nt lds\n"
    "s_waitcnt vmcnt(25)\n"
    "s_barrier\n"
    "v_add_u32 v8, 49152, v3\n"
    "ds_read_b64_tr_b16 a[8:9], v8 offset:0\n"
    "ds_read_b64_tr_b16 a[10:11], v8 offset:768\n"
    "ds_read_b64_tr_b16 a[88:89], v8 offset:128\n"
    "ds_read_b64_tr_b16 a[90:91], v8 offset:896\n"
    "ds_read_b64_tr_b16 a[168:169], v8 offset:256\n"
    "ds_read_b64_tr_b16 a[170:171], v8 offset:1024\n"
    "v_add_u32 v9, 49152, v4\n"
    "ds_read_b64_tr_b16 a[24:25], v9 offset:0\n"
    "ds_read_b64_tr_b16 a[26:27], v9 offset:768\n"
    "ds_read_b64_tr_b16 a[104:105], v9 offset:128\n"
    "ds_read_b64_tr_b16 a[106:107], v9 offset:896\n"
    "ds_read_b64_tr_b16 a[184:185], v9 offset:256\n"
    "ds_read_b64_tr_b16 a[186:187], v9 offset:1024\n"
    "v_add_u32 v10, 49152, v5\n"
    "ds_read_b64_tr_b16 a[40:41], v10 offset:0\n"
    "ds_read_b64_tr_b16 a[42:43], v10 offset:768\n"
    "ds_read_b64_tr_b16 a[120:121], v10 offset:128\n"
    "ds_read_b64_tr_b16 a[122:123], v10 offset:896\n"
    "ds_read_b64_tr_b16 a[200:201], v10 offset:256\n"
    "ds_read_b64_tr_b16 a[202:203], v10 offset:1024\n"
    "v_add_u32 v11, 49152, v6\n"
    "ds_read_b64_tr_b16 a[56:57], v11 offset:0\n"
    "ds_read_b64_tr_b16 a[58:59], v11 offset:768\n"
    "ds_read_b64_tr_b16 a[136:137], v11 offset:128\n"
    "ds_read_b64_tr_b16 a[138:139], v11 offset:896\n"
    "ds_read_b64_tr_b16 a[216:217], v11 offset:256\n"
    "ds_read_b64_tr_b16 a[218:219], v11 offset:1024\n"
    "s_waitcnt lgkmcnt(0)\n"
    "s_barrier\n"
    "s_waitcnt vmcnt(12)\n"
    "s_barrier\n"
    "v_add_u32 v8, 98304, v3\n"
    "ds_read_b64_tr_b16 a[12:13], v8 offset:0\n"
    "ds_read_b64_tr_b16 a[14:15], v8 offset:768\n"
    "ds_read_b64_tr_b16 a[92:93], v8 offset:128\n"
    "ds_read_b64_tr_b16 a[94:95], v8 offset:896\n"
    "ds_read_b64_tr_b16 a[172:173], v8 offset:256\n"
    "ds_read_b64_tr_b16 a[174:175], v8 offset:1024\n"
    "v_add_u32 v9, 98304, v4\n"
    "ds_read_b64_tr_b16 a[28:29], v9 offset:0\n"
    "ds_read_b64_tr_b16 a[30:31], v9 offset:768\n"
    "ds_read_b64_tr_b16 a[108:109], v9 offset:128\n"
    "ds_read_b64_tr_b16 a[110:111], v9 offset:896\n"
    "ds_read_b64_tr_b16 a[188:189], v9 offset:256\n"
    "ds_read_b64_tr_b16 a[190:191], v9 offset:1024\n"
    "v_add_u32 v10, 98304, v5\n"
    "ds_read_b64_tr_b16 a[44:45], v10 offset:0\n"
    "ds_read_b64_tr_b16 a[46:47], v10 offset:768\n"
    "ds_read_b64_tr_b16 a[124:125], v10 offset:128\n"
    "ds_read_b64_tr_b16 a[126:127], v10 offset:896\n"
    "ds_read_b64_tr_b16 a[204:205], v10 offset:256\n"
    "ds_read_b64_tr_b16 a[206:207], v10 offset:1024\n"
    "v_add_u32 v11, 98304, v6\n"
    "ds_read_b64_tr_b16 a[60:61], v11 offset:0\n"
    "ds_read_b64_tr_b16 a[62:63], v11 offset:768\n"
    "ds_read_b64_tr_b16 a[140:141], v11 offset:128\n"
    "ds_read_b64_tr_b16 a[142:143], v11 offset:896\n"
    "ds_read_b64_tr_b16 a[220:221], v11 offset:256\n"
    "ds_read_b64_tr_b16 a[222:223], v11 offset:1024\n"
    "s_waitcnt lgkmcnt(0)\n"
    "s_barrier\n"
    "s_waitcnt vmcnt(0)\n"
    "s_barrier\n"
    "v_add_u32 v8, 0, v7\n"
    "ds_read_b64_tr_b16 a[64:65], v8 offset:0\n"
    "ds_read_b64_tr_b16 a[66:67], v8 offset:768\n"
    "ds_read_b64_tr_b16 a[144:145], v8 offset:128\n"
    "ds_read_b64_tr_b16 a[146:147], v8 offset:896\n"
    "ds_read_b64_tr_b16 a[224:225], v8 offset:256\n"
    "ds_read_b64_tr_b16 a[226:227], v8 offset:1024\n"
    "v_add_u32 v9, 12288, v7\n"
    "ds_read_b64_tr_b16 a[68:69], v9 offset:0\n"
    "ds_read_b64_tr_b16 a[70:71], v9 offset:768\n"
    "ds_read_b64_tr_b16 a[148:149], v9 offset:128\n"
    "ds_read_b64_tr_b16 a[150:151], v9 offset:896\n"
    "ds_read_b64_tr_b16 a[228:229], v9 offset:256\n"
    "ds_read_b64_tr_b16 a[230:231], v9 offset:1024\n"
    "v_add_u32 v10, 24576, v7\n"
    "ds_read_b64_tr_b16 a[72:73], v10 offset:0\n"
    "ds_read_b64_tr_b16 a[74:75], v10 offset:768\n"
    "ds_read_b64_tr_b16 a[152:153], v10 offset:128\n"
    "ds_read_b64_tr_b16 a[154:155], v10 offset:896\n"
    "ds_read_b64_tr_b16 a[232:233], v10 offset:256\n"
    "ds_read_b64_tr_b16 a[234:235], v10 offset:1024\n"
    "v_add_u32 v11, 36864, v7\n"
    "ds_read_b64_tr_b16 a[76:77], v11 offset:0\n"
    "ds_read_b64_tr_b16 a[78:79], v11 offset:768\n"
    "ds_read_b64_tr_b16 a[156:157], v11 offset:128\n"
    "ds_read_b64_tr_b16 a[158:159], v11 offset:896\n"
    "ds_read_b64_tr_b16 a[236:237], v11 offset:256\n"
    "ds_read_b64_tr_b16 a[238:239], v11 offset:1024\n"
    "s_waitcnt lgkmcnt(0)\n"
    "s_barrier\n"
    "ds_write_b128 v97, v[12:15] offset:0\n"
    "ds_write_b128 v97, v[16:19] offset:1024\n"
    "ds_write_b128 v97, v[20:23] offset:2048\n"
    "ds_write_b128 v97, v[24:27] offset:3072\n"
    "ds_write_b128 v97, v[28:31] offset:4096\n"
    "ds_write_b128 v97, v[32:35] offset:5120\n"
    "ds_write_b128 v97, v[36:39] offset:6144\n"
    "ds_write_b128 v97, v[40:43] offset:7168\n"
    "ds_write_b128 v97, v[44:47] offset:8192\n"
    "ds_write_b128 v97, v[48:51] offset:9216\n"
    "ds_write_b128 v97, v[52:55] offset:10240\n"
    "ds_write_b128 v97, v[56:59] offset:11264\n"
    "ds_write_b128 v97, v[60:63] offset:12288\n"
    "ds_write_b128 v97, v[64:67] offset:13312\n"
    "ds_write_b128 v97, v[68:71] offset:14336\n"
    "ds_write_b128 v97, v[72:75] offset:15360\n"
    "ds_write_b128 v97, v[76:79] offset:16384\n"
    "ds_write_b128 v97, v[80:83] offset:17408\n"
    "ds_write_b128 v97, v[84:87] offset:18432\n"
    "ds_write_b128 v97, v[88:91] offset:19456\n"
    "s_mov_b32 s46, -2\n"
    "s_mov_b32 s62, 1037384597\n"
    "s_mov_b32 s63, 1037384597\n"
    "s_mov_b32 s64, 858993459\n"
    "s_mov_b32 s65, 858993459\n"
    "s_mov_b32 s66, 67044351\n"
    "s_mov_b32 s67, 67044351\n"
    "v_mov_b32 v8, v99\n"
    "v_mov_b32 v9, v99\n"
    "v_mov_b32 v10, 137088\n"
    "ds_write_b64 v10, v[8:9] offset:0\n"
    "ds_write_b64 v10, v[8:9] offset:8\n"
    "ds_write_b64 v10, v[8:9] offset:1936\n"
    "ds_write_b64 v10, v[8:9] offset:1944\n"
    "ds_write_b64 v10, v[8:9] offset:3872\n"
    "ds_write_b64 v10, v[8:9] offset:3880\n"
    "ds_write_b64 v10, v[8:9] offset:5808\n"
    "ds_write_b64 v10, v[8:9] offset:5816\n"
    "s_lshl_b32 s59, s22, 11\n"
    "s_add_u32 s59, s59, 45056\n"
    "v_lshrrev_b32 v4, 2, v96\n"
    "v_and_b32 v6, 12, v4\n"
    "v_lshlrev_b32 v5, 1, v96\n"
    "v_and_b32 v5, 0x380, v5\n"
    "v_min_u32 v5, 0x200, v5\n"
    "v_add_u32 v5, v5, v6\n"
    "s_lshr_b32 s60, s23, 13\n"
    "s_mul_i32 s60, s60, 3\n"
    ".Lqk_po6_%=:\n"
    "buffer_load_dword v8, v4, s[0:3], s32 offen sc1\n"
    "s_waitcnt vmcnt(0)\n"
    "v_cmp_gt_i32 vcc, 1, v8\n"
    "s_nop 0\n"
    "s_cbranch_vccz .Lqk_pd7_%=\n"
    "s_add_u32 s40, s40, 1\n"
    "s_cmp_lt_u32 s40, 0x40000\n"
    "s_cbranch_scc1 .Lqk_sp8_%=\n"
    "v_mov_b32 v8, 20\n"
    "s_mov_b32 s44, 33728\n"
    "buffer_store_dword v8, off, s[0:3], s44 sc1\n"
    "s_waitcnt vmcnt(0)\n"
    "s_branch .Lqk_pd7_%=\n"
    ".Lqk_sp8_%=:\n"
    "s_sleep 4\n"
    "s_branch .Lqk_po6_%=\n"
    ".Lqk_pd7_%=:\n"
    ".Lqk_po9_%=:\n"
    "buffer_load_dword v8, v5, s[0:3], s33 offen sc1\n"
    "s_waitcnt vmcnt(0)\n"
    "v_cmp_gt_i32 vcc, s60, v8\n"
    "s_nop 0\n"
    "s_cbranch_vccz .Lqk_pd10_%=\n"
    "s_add_u32 s40, s40, 1\n"
    "s_cmp_lt_u32 s40, 0x40000\n"
    "s_cbranch_scc1 .Lqk_sp11_%=\n"
    "v_mov_b32 v8, 21\n"
    "s_mov_b32 s44, 33728\n"
    "buffer_store_dword v8, off, s[0:3], s44 sc1\n"
    "s_waitcnt vmcnt(0)\n"
    "s_branch .Lqk_pd10_%=\n"
    ".Lqk_sp11_%=:\n"
    "s_sleep 4\n"
    "s_branch .Lqk_po9_%=\n"
    ".Lqk_pd10_%=:\n"
    "s_add_u32 s53, s27, s23\n"
    "s_sub_u32 m0, s26, 8192\n"
    "s_nop 0\n"
    "buffer_load_dwordx4 v96, s[0:3], s53 offen offset:0 sc1 lds\n"
    "buffer_load_dwordx4 v96, s[0:3], s53 offen offset:1024 sc1 lds\n"
    "buffer_load_dwordx4 v96, s[0:3], s53 offen offset:2048 sc1 lds\n"
    "buffer_load_dwordx4 v96, s[0:3], s53 offen offset:3072 sc1 lds\n"
    "s_sub_u32 m0, s26, 4096\n"
    "s_add_u32 s53, s53, 4096\n"
    "buffer_load_dwordx4 v96, s[0:3], s53 offen offset:0 sc1 lds\n"
    "buffer_load_dwordx4 v96, s[0:3], s53 offen offset:1024 sc1 lds\n"
    ".Lqk_po12_%=:\n"
    "buffer_load_dword v8, v6, s[0:3], s34 offen sc1\n"
    "s_waitcnt vmcnt(0)\n"
    "v_cmp_gt_i32 vcc, 2, v8\n"
    "s_nop 0\n"
    "s_cbranch_vccz .Lqk_pd13_%=\n"
    "s_add_u32 s40, s40, 1\n"
    "s_cmp_lt_u32 s40, 0x40000\n"
    "s_cbranch_scc1 .Lqk_sp14_%=\n"
    "v_mov_b32 v8, 22\n"
    "s_mov_b32 s44, 33728\n"
    "buffer_store_dword v8, off, s[0:3], s44 sc1\n"
    "s_waitcnt vmcnt(0)\n"
    "s_branch .Lqk_pd13_%=\n"
    ".Lqk_sp14_%=:\n"
    "s_sleep 4\n"
    "s_branch .Lqk_po12_%=\n"
    ".Lqk_pd13_%=:\n"
    "s_memrealtime s[42:43]\n"
    "s_waitcnt lgkmcnt(0)\n"
    "s_sub_u32 s57, s42, 792\n"
    "s_mov_b32 s46, -1\n"
    "s_mov_b32 s47, -5\n"
    "s_mov_b32 s50, 2130706432\n"
    "s_mov_b32 s48, 2130706432\n"
    "s_mov_b32 s49, 4194304\n"
    "s_mov_b32 s55, s31\n"
    "s_add_u32 s54, s29, s23\n"
    "s_barrier\n"
    "v_mov_b32 v110, v104\n"
    "ds_read_b128 v[32:35], v92 offset:0\n"
    "ds_read_b128 v[36:39], v92 offset:1024\n"
    "ds_read_b128 v[40:43], v93 offset:0\n"
    "ds_read_b128 v[44:47], v93 offset:1024\n"
    "ds_read_b128 v[48:51], v94 offset:0\n"
    "ds_read_b128 v[52:55], v94 offset:1024\n"
    "s_waitcnt lgkmcnt(0)\n"
    "s_branch .Lqk_bulk_%=\n"
    ".Lqk_take_%=:\n"
    "s_waitcnt vmcnt(2)\n"
    ".Lqk_mk0x14_%=:\n"
    "ds_read_b128 v[56:59], v92 offset:8192\n"
    "s_waitcnt vmcnt(1)\n"
    "ds_read_b128 v[60:63], v92 offset:9216\n"
    "s_waitcnt lgkmcnt(1)\n"
    "v_smfmac_f32_16x16x64_bf16 v[8:11], v[56:59], v[32:39], v98\n"
    "v_max_u32 v108, v56, v58\n"
    "v_smfmac_f32_16x16x64_bf16 v[12:15], v[56:59], a[160:167], v98\n"
    "s_waitcnt lgkmcnt(0)\n"
    "ds_read_b128 v[32:35], v97 offset:6144\n"
    "v_max3_u32 v108, v108, v60, v62\n"
    "v_smfmac_f32_16x16x64_bf16 v[0:3], v[56:59], v[176:183], v98\n"
    "ds_read_b128 v[36:39], v97 offset:7168\n"
    "v_cmp_eq_u32 vcc, -1, v108\n"
    "v_smfmac_f32_16x16x64_bf16 v[4:7], v[56:59], a[80:87], v98\n"
    "s_cbranch_vccnz .Lqk_retry_%=\n"
    ".Lqk_go_%=:\n"
    "s_barrier\n"
    ".Lqk_mk1x15_%=:\n"
    "v_smfmac_f32_16x16x64_bf16 v[16:19], v[56:59], a[240:247], v98\n"
    "ds_read_b128 v[24:27], v93 offset:8192\n"
    "ds_read_b128 v[28:31], v93 offset:9216\n"
    "v_smfmac_f32_16x16x64_bf16 v[20:23], v[56:59], a[0:7], v98\n"
    "s_waitcnt vmcnt(0)\n"
    "ds_read_b128 v[64:67], v92 offset:10240\n"
    "v_smfmac_f32_16x16x64_bf16 v[8:11], v[60:63], v[40:47], v98\n"
    "ds_read_b128 v[56:59], v94 offset:9216\n"
    "v_smfmac_f32_16x16x64_bf16 v[12:15], v[60:63], a[168:175], v98\n"
    "ds_read_b128 v[40:43], v97 offset:8192\n"
    "ds_read_b128 v[44:47], v97 offset:9216\n"
    "v_smfmac_f32_16x16x64_bf16 v[0:3], v[60:63], v[184:191], v98\n"
    "v_smfmac_f32_16x16x64_bf16 v[4:7], v[60:63], a[88:95], v98\n"
    "v_smfmac_f32_16x16x64_bf16 v[16:19], v[60:63], a[248:255], v98\n"
    "s_waitcnt lgkmcnt(3)\n"
    "v_max_u32 v108, v64, v66\n"
    "v_smfmac_f32_16x16x64_bf16 v[20:23], v[60:63], a[8:15], v98\n"
    "v_cmp_eq_u32 vcc, -1, v108\n"
    "v_smfmac_f32_16x16x64_bf16 v[8:11], v[24:27], v[48:55], v98\n"
    "s_cbranch_vccnz .Lqk_r2_%=\n"
    ".Lqk_go2_%=:\n"
    "s_barrier\n"
    "v_smfmac_f32_16x16x64_bf16 v[12:15], v[24:27], a[176:183], v98\n"
    "s_memrealtime s[68:69]\n"
    "ds_read_b128 v[64:67], v94 offset:8192\n"
    "ds_read_b128 v[60:63], v95 offset:8192\n"
    "v_smfmac_f32_16x16x64_bf16 v[0:3], v[24:27], v[192:199], v98\n"
    "ds_read_b128 v[48:51], v97 offset:10240\n"
    "ds_read_b128 v[52:55], v97 offset:11264\n"
    "v_smfmac_f32_16x16x64_bf16 v[4:7], v[24:27], a[96:103], v98\n"
    "v_mov_b32 v109, s47\n"
    "v_smfmac_f32_16x16x64_bf16 v[16:19], v[24:27], v[112:119], v98\n"
    "v_lshlrev_b32 v110, 10, v96\n"
    "v_smfmac_f32_16x16x64_bf16 v[20:23], v[24:27], a[16:23], v98\n"
    "ds_read_b128 v[24:27], v95 offset:9216\n"
    "s_mov_b64 vcc, s[64:65]\n"
    "v_smfmac_f32_16x16x64_bf16 v[8:11], v[28:31], v[32:39], v98\n"
    "ds_read_b128 v[32:35], v97 offset:12288\n"
    "ds_read_b128 v[36:39], v97 offset:13312\n"
    "v_smfmac_f32_16x16x64_bf16 v[12:15], v[28:31], a[184:191], v98\n"
    "v_smfmac_f32_16x16x64_bf16 v[0:3], v[28:31], v[200:207], v98\n"
    "v_smfmac_f32_16x16x64_bf16 v[4:7], v[28:31], a[104:111], v98\n"
    "v_smfmac_f32_16x16x64_bf16 v[16:19], v[28:31], v[120:127], v98\n"
    "v_smfmac_f32_16x16x64_bf16 v[20:23], v[28:31], a[24:31], v98\n"
    "s_waitcnt lgkmcnt(0)\n"
    "v_smfmac_f32_16x16x64_bf16 v[8:11], v[64:67], v[40:47], v98\n"
    "s_sub_u32 s44, s68, s57\n"
    "ds_read_b128 v[40:43], v97 offset:14336\n"
    "ds_read_b128 v[44:47], v97 offset:15360\n"
    "v_smfmac_f32_16x16x64_bf16 v[12:15], v[64:67], a[192:199], v98\n"
    "ds_read_b128 v[28:31], v96 offset:10240\n"
    "s_add_u32 s45, s68, 10\n"
    "v_smfmac_f32_16x16x64_bf16 v[0:3], v[64:67], v[208:215], v98\n"
    "s_cmp_gt_i32 s44, 6\n"
    "s_cselect_b32 s57, s45, s57\n"
    "v_mov_b32 v108, s57\n"
    "v_smfmac_f32_16x16x64_bf16 v[4:7], v[64:67], a[112:119], v98\n"
    "s_mov_b64 exec, 15\n"
    "buffer_store_dwordx2 v[108:109], v110, s[0:3], s56 offen sc1\n"
    "s_mov_b64 exec, -1\n"
    "v_smfmac_f32_16x16x64_bf16 v[16:19], v[64:67], v[128:135], v98\n"
    "v_smfmac_f32_16x16x64_bf16 v[20:23], v[64:67], a[32:39], v98\n"
    "v_bfe_u32 v111, v96, 8, 2\n"
    "v_smfmac_f32_16x16x64_bf16 v[8:11], v[56:59], v[48:55], v98\n"
    "ds_read_b128 v[48:51], v97 offset:16384\n"
    "ds_read_b128 v[52:55], v97 offset:17408\n"
    "v_smfmac_f32_16x16x64_bf16 v[12:15], v[56:59], a[200:207], v98\n"
    "ds_read_b128 v[64:67], v96 offset:21504\n"
    "v_mad_i32_i24 v110, v111, -8, v104\n"
    "v_smfmac_f32_16x16x64_bf16 v[8:11], v[60:63], v[32:39], v98\n"
    "ds_read_b128 v[32:35], v97 offset:18432\n"
    "ds_read_b128 v[36:39], v97 offset:19456\n"
    "v_smfmac_f32_16x16x64_bf16 v[12:15], v[60:63], a[208:215], v98\n"
    "s_waitcnt lgkmcnt(5)\n"
    "v_mad_i32_i24 v111, v111, -8, v102\n"
    "v_smfmac_f32_16x16x64_bf16 v[8:11], v[24:27], v[40:47], v98\n"
    "v_smfmac_f32_16x16x64_bf16 v[12:15], v[24:27], a[216:223], v98\n"
    "v_smfmac_f32_16x16x64_bf16 v[0:3], v[56:59], v[216:223], v98\n"
    "v_smfmac_f32_16x16x64_bf16 v[4:7], v[56:59], a[120:127], v98\n"
    "s_waitcnt lgkmcnt(3)\n"
    "v_smfmac_f32_16x16x64_bf16 v[8:11], v[28:31], v[48:55], v98\n"
    "v_smfmac_f32_16x16x64_bf16 v[12:15], v[28:31], a[224:231], v98\n"
    "s_waitcnt lgkmcnt(0)\n"
    "v_smfmac_f32_16x16x64_bf16 v[8:11], v[64:67], v[32:39], v98\n"
    "v_smfmac_f32_16x16x64_bf16 v[12:15], v[64:67], a[232:239], v98\n"
    "ds_read_b128 v[32:35], v92 offset:0\n"
    "ds_read_b128 v[36:39], v92 offset:1024\n"
    "v_smfmac_f32_16x16x64_bf16 v[0:3], v[60:63], v[224:231], v98\n"
    "s_nop 1\n"
    "v_add_f32 v8, v8, v10\n"
    "v_smfmac_f32_16x16x64_bf16 v[4:7], v[60:63], a[128:135], v98\n"
    "ds_read_b128 v[40:43], v93 offset:0\n"
    "v_add_f32 v9, v9, v11\n"
    "v_smfmac_f32_16x16x64_bf16 v[0:3], v[24:27], v[232:239], v98\n"
    "ds_read_b128 v[44:47], v93 offset:1024\n"
    "v_cvt_pk_bf16_f32 v8, v8, v9\n"
    "v_smfmac_f32_16x16x64_bf16 v[0:3], v[28:31], v[240:247], v98\n"
    "ds_read_b128 v[48:51], v94 offset:0\n"
    "v_add_f32 v12, v12, v14\n"
    "v_smfmac_f32_16x16x64_bf16 v[0:3], v[64:67], v[248:255], v98\n"
    "ds_read_b128 v[52:55], v94 offset:1024\n"
    "v_add_f32 v13, v13, v15\n"
    "v_smfmac_f32_16x16x64_bf16 v[4:7], v[24:27], a[136:143], v98\n"
    "v_cvt_pk_bf16_f32 v9, v12, v13\n"
    "v_smfmac_f32_16x16x64_bf16 v[4:7], v[28:31], a[144:151], v98\n"
    "s_nop 0\n"
    "v_mfma_f32_16x16x16_bf16 v[12:15], v[8:9], v[100:101], 0\n"
    "v_add_f32 v0, v0, v2\n"
    "v_add_f32 v1, v1, v3\n"
    "v_smfmac_f32_16x16x64_bf16 v[4:7], v[64:67], a[152:159], v98\n"
    "v_cvt_pk_bf16_f32 v0, v0, v1\n"
    "v_smfmac_f32_16x16x64_bf16 v[16:19], v[56:59], v[136:143], v98\n"
    "s_nop 1\n"
    "v_cvt_pk_bf16_f32 v15, v14, v15\n"
    "v_smfmac_f32_16x16x64_bf16 v[20:23], v[56:59], a[40:47], v98\n"
    "s_nop 0\n"
    "v_add_f32 v4, v4, v6\n"
    "v_smfmac_f32_16x16x64_bf16 v[16:19], v[60:63], v[144:151], v98\n"
    "v_add_f32 v5, v5, v7\n"
    "v_smfmac_f32_16x16x64_bf16 v[20:23], v[60:63], a[48:55], v98\n"
    "v_cvt_pk_bf16_f32 v1, v4, v5\n"
    "v_smfmac_f32_16x16x64_bf16 v[16:19], v[24:27], v[152:159], v98\n"
    "s_nop 0\n"
    "v_mfma_f32_16x16x16_bf16 v[4:7], v[0:1], v[100:101], 0\n"
    "v_cvt_pk_bf16_f32 v14, v12, v13\n"
    "v_smfmac_f32_16x16x64_bf16 v[20:23], v[24:27], a[56:63], v98\n"
    "v_smfmac_f32_16x16x64_bf16 v[16:19], v[28:31], v[160:167], v98\n"
    "s_nop 1\n"
    "v_cvt_pk_bf16_f32 v13, v6, v7\n"
    "v_smfmac_f32_16x16x64_bf16 v[20:23], v[28:31], a[64:71], v98\n"
    "v_cvt_pk_bf16_f32 v12, v4, v5\n"
    "v_smfmac_f32_16x16x64_bf16 v[16:19], v[64:67], v[168:175], v98\n"
    "v_smfmac_f32_16x16x64_bf16 v[20:23], v[64:67], a[72:79], v98\n"
    ".Lqk_mk2x16_%=:\n"
    "v_mfma_f32_16x16x32_bf16 v[24:27], v[12:15], v[12:15], 0\n"
    "s_nop 1\n"
    "v_add_f32 v16, v16, v18\n"
    "v_add_f32 v17, v17, v19\n"
    "v_cvt_pk_bf16_f32 v16, v16, v17\n"
    "v_add_f32 v20, v20, v22\n"
    "v_add_f32 v21, v21, v23\n"
    "v_cvt_pk_bf16_f32 v17, v20, v21\n"
    "s_nop 1\n"
    "v_mfma_f32_16x16x16_bf16 v[20:23], v[16:17], v[100:101], 0\n"
    "v_mfma_f32_16x16x32_bf16 v[0:3], v[72:75], v[12:15], 0\n"
    "v_mfma_f32_16x16x32_bf16 v[8:11], v[68:71], v[12:15], 0\n"
    "s_nop 1\n"
    "v_cvt_pk_bf16_f32 v17, v22, v23\n"
    "v_cvt_pk_bf16_f32 v16, v20, v21\n"
    "s_nop 1\n"
    "v_mfma_f32_16x16x16_bf16 v[24:27], v[16:17], v[16:17], v[24:27]\n"
    "v_mfma_f32_16x16x16_bf16 v[0:3], v[78:79], v[16:17], v[0:3]\n"
    "v_mfma_f32_16x16x16_bf16 v[8:11], v[76:77], v[16:17], v[8:11]\n"
    "s_nop 1\n"
    "ds_write_b64 v111, v[26:27] offset:128\n"
    "v_mfma_f32_16x16x32_bf16 v[56:59], v[12:15], v[80:83], 0\n"
    "s_nop 3\n"
    "v_cndmask_b32_dpp v8, v0, v8, vcc quad_perm:[0,1,0,1] row_mask:0xf bank_mask:0xf\n"
    "v_cndmask_b32_dpp v9, v1, v9, vcc quad_perm:[0,1,0,1] row_mask:0xf bank_mask:0xf\n"
    "v_cndmask_b32_dpp v10, v2, v10, vcc quad_perm:[0,1,0,1] row_mask:0xf bank_mask:0xf\n"
    "v_cndmask_b32_dpp v11, v3, v11, vcc quad_perm:[0,1,0,1] row_mask:0xf bank_mask:0xf\n"
    "ds_write_b128 v102, v[8:11]\n"
    "v_mfma_f32_16x16x32_bf16 v[60:63], v[12:15], v[84:87], 0\n"
    "s_mov_b64 exec, s[20:21]\n"
    "s_waitcnt lgkmcnt(0)\n"
    "s_barrier\n"
    ".Lqk_mk3x17_%=:\n"
    "ds_read_b128 v[72:75], v103 offset:0\n"
    "ds_read_b128 v[76:79], v103 offset:1936\n"
    "ds_read_b128 v[80:83], v103 offset:3872\n"
    "ds_read_b128 v[84:87], v103 offset:5808\n"
    "s_mov_b64 exec, -1\n"
    "v_mfma_f32_16x16x16_bf16 v[56:59], v[16:17], v[88:89], v[56:59]\n"
    "v_mfma_f32_16x16x16_bf16 v[60:63], v[16:17], v[90:91], v[60:63]\n"
    "s_waitcnt lgkmcnt(2)\n"
    "v_pk_add_f32 v[72:73], v[72:73], v[76:77]\n"
    "v_pk_add_f32 v[74:75], v[74:75], v[78:79]\n"
    "s_waitcnt lgkmcnt(1)\n"
    "v_pk_add_f32 v[72:73], v[72:73], v[80:81]\n"
    "v_pk_add_f32 v[74:75], v[74:75], v[82:83]\n"
    "s_waitcnt lgkmcnt(0)\n"
    "v_pk_add_f32 v[72:73], v[72:73], v[84:85]\n"
    "v_pk_add_f32 v[74:75], v[74:75], v[86:87]\n"
    "v_max3_f32 v76, v72, v73, v74\n"
    "v_max_f32 v76, v76, v75\n"
    "v_pk_mul_f32 v[72:73], v[72:73], s[62:63]\n"
    "v_pk_mul_f32 v[74:75], v[74:75], s[62:63]\n"
    "v_max_f32_dpp v76, v76, v76 row_ror:8 row_mask:0xf bank_mask:0xf\n"
    "s_nop 1\n"
    "v_max_f32_dpp v76, v76, v76 row_ror:4 row_mask:0xf bank_mask:0xf\n"
    "s_nop 1\n"
    "v_max_f32_dpp v76, v76, v76 row_ror:2 row_mask:0xf bank_mask:0xf\n"
    "s_nop 1\n"
    "v_max_f32_dpp v76, v76, v76 row_ror:1 row_mask:0xf bank_mask:0xf\n"
    "v_pk_fma_f32 v[72:73], v[76:77], s[62:63], v[72:73] op_sel_hi:[0,1,1] neg_lo:[1,0,0] neg_hi:[1,0,0]\n"
    "v_pk_fma_f32 v[74:75], v[76:77], s[62:63], v[74:75] op_sel_hi:[0,1,1] neg_lo:[1,0,0] neg_hi:[1,0,0]\n"
    "v_exp_f32 v72, v72\n"
    "v_exp_f32 v74, v74\n"
    "v_exp_f32 v73, v73\n"
    "v_exp_f32 v75, v75\n"
    "v_add_f32 v76, v72, v74\n"
    "v_add_f32 v77, v73, v75\n"
    "v_add_f32 v76, v76, v77\n"
    "s_nop 1\n"
    "v_add_f32_dpp v76, v76, v76 row_ror:8 row_mask:0xf bank_mask:0xf\n"
    "s_nop 1\n"
    "v_add_f32_dpp v76, v76, v76 row_ror:4 row_mask:0xf bank_mask:0xf\n"
    "s_nop 1\n"
    "v_add_f32_dpp v76, v76, v76 row_ror:2 row_mask:0xf bank_mask:0xf\n"
    "s_nop 1\n"
    "v_add_f32_dpp v76, v76, v76 row_ror:1 row_mask:0xf bank_mask:0xf\n"
    "v_rcp_f32 v76, v76\n"
    "s_nop 0\n"
    "v_pk_mul_f32 v[72:73], v[72:73], v[76:77] op_sel_hi:[1,0] clamp\n"
    "v_pk_mul_f32 v[74:75], v[74:75], v[76:77] op_sel_hi:[1,0] clamp\n"
    "v_cvt_pk_bf16_f32 v72, v72, v73\n"
    "v_cvt_pk_bf16_f32 v73, v74, v75\n"
    "buffer_store_dwordx2 v[72:73], v106, s[0:3], s48 offen sc1\n"
    ".Lqk_mk4x18_%=:\n"
    ".Lqk_bulk_%=:\n"
    "v_mfma_f32_16x16x32_bf16 v[0:3], a[80:83], v[32:35], 0\n"
    "ds_write_b64 v110, v[58:59] offset:224\n"
    "v_mfma_f32_16x16x32_bf16 v[4:7], a[80:83], v[36:39], 0\n"
    "ds_write_b64 v110, v[62:63] offset:128\n"
    ".Lqk_mkL0_%=:\n"
    "v_mfma_f32_16x16x32_bf16 v[8:11], a[160:163], v[32:35], 0\n"
    "v_mfma_f32_16x16x32_bf16 v[12:15], a[160:163], v[36:39], 0\n"
    "v_mfma_f32_16x16x32_bf16 v[16:19], a[0:3], v[32:35], 0\n"
    "v_mfma_f32_16x16x32_bf16 v[20:23], a[0:3], v[36:39], 0\n"
    "s_waitcnt lgkmcnt(0)\n"
    "s_barrier\n"
    "ds_read_b128 v[56:59], v105 offset:0\n"
    "v_mfma_f32_16x16x32_bf16 v[24:27], a[240:243], v[32:35], 0\n"
    "ds_read_b128 v[60:63], v105 offset:4352\n"
    "v_mfma_f32_16x16x32_bf16 v[28:31], a[240:243], v[36:39], 0\n"
    "v_mfma_f32_16x16x32_bf16 v[0:3], a[96:99], v[40:43], v[0:3]\n"
    "s_sub_u32 m0, s26, 4096\n"
    "ds_read_b128 v[32:35], v95 offset:0\n"
    "ds_read_b128 v[36:39], v95 offset:1024\n"
    "v_mfma_f32_16x16x32_bf16 v[4:7], a[96:99], v[44:47], v[4:7]\n"
    "ds_read_b128 v[64:67], v105 offset:8704\n"
    "v_mfma_f32_16x16x32_bf16 v[8:11], a[176:179], v[40:43], v[8:11]\n"
    "buffer_load_dwordx4 v96, s[0:3], s53 offen offset:2048 sc1 lds\n"
    "v_mfma_f32_16x16x32_bf16 v[12:15], a[176:179], v[44:47], v[12:15]\n"
    "ds_read_b128 v[68:71], v105 offset:13056\n"
    "v_mfma_f32_16x16x32_bf16 v[16:19], a[16:19], v[40:43], v[16:19]\n"
    "v_mfma_f32_16x16x32_bf16 v[20:23], a[16:19], v[44:47], v[20:23]\n"
    "v_mfma_f32_16x16x32_bf16 v[24:27], v[112:115], v[40:43], v[24:27]\n"
    "buffer_load_dwordx4 v96, s[0:3], s53 offen offset:3072 sc1 lds\n"
    "v_mfma_f32_16x16x32_bf16 v[28:31], v[112:115], v[44:47], v[28:31]\n"
    "s_mov_b32 m0, s59\n"
    "s_add_u32 s53, s53, s28\n"
    "s_waitcnt lgkmcnt(4)\n"
    "v_mfma_f32_16x16x32_bf16 v[0:3], a[112:115], v[48:51], v[0:3]\n"
    "ds_read_b128 v[40:43], v92 offset:2048\n"
    "ds_read_b128 v[44:47], v92 offset:3072\n"
    "v_mfma_f32_16x16x32_bf16 v[4:7], a[112:115], v[52:55], v[4:7]\n"
    "v_add_f32 v56, v56, v60\n"
    "v_mfma_f32_16x16x32_bf16 v[8:11], a[192:195], v[48:51], v[8:11]\n"
    "buffer_load_dwordx4 v96, s[0:3], s53 offen offset:0 sc1 lds\n"
    "v_mfma_f32_16x16x32_bf16 v[12:15], a[192:195], v[52:55], v[12:15]\n"
    "v_add_f32 v57, v57, v61\n"
    "v_add_f32 v58, v58, v62\n"
    "v_mfma_f32_16x16x32_bf16 v[16:19], a[32:35], v[48:51], v[16:19]\n"
    "v_add_f32 v59, v59, v63\n"
    "s_waitcnt lgkmcnt(3)\n"
    "v_mfma_f32_16x16x32_bf16 v[20:23], a[32:35], v[52:55], v[20:23]\n"
    "v_add_f32 v56, v56, v64\n"
    ".Lqk_mkL1_%=:\n"
    "v_mfma_f32_16x16x32_bf16 v[24:27], v[128:131], v[48:51], v[24:27]\n"
    "buffer_load_dwordx4 v96, s[0:3], s53 offen offset:1024 sc1 lds\n"
    "v_mfma_f32_16x16x32_bf16 v[28:31], v[128:131], v[52:55], v[28:31]\n"
    "s_waitcnt lgkmcnt(4)\n"
    "v_mov_b64 v[110:111], -1\n"
    "v_add_f32 v57, v57, v65\n"
    "v_mfma_f32_16x16x32_bf16 v[0:3], a[128:131], v[32:35], v[0:3]\n"
    "ds_read_b128 v[48:51], v93 offset:2048\n"
    "ds_read_b128 v[52:55], v93 offset:3072\n"
    "v_mfma_f32_16x16x32_bf16 v[4:7], a[128:131], v[36:39], v[4:7]\n"
    "v_add_f32 v58, v58, v66\n"
    "v_mfma_f32_16x16x32_bf16 v[8:11], a[208:211], v[32:35], v[8:11]\n"
    "buffer_load_dwordx4 v[80:83], v96, s[0:3], s54 offen sc1\n"
    "v_mfma_f32_16x16x32_bf16 v[12:15], a[208:211], v[36:39], v[12:15]\n"
    "v_add_f32 v59, v59, v67\n"
    "s_waitcnt lgkmcnt(4)\n"
    "v_mfma_f32_16x16x32_bf16 v[16:19], a[48:51], v[32:35], v[16:19]\n"
    "v_add_f32 v56, v56, v68\n"
    "v_add_f32 v57, v57, v69\n"
    "v_mfma_f32_16x16x32_bf16 v[20:23], a[48:51], v[36:39], v[20:23]\n"
    "v_add_f32 v58, v58, v70\n"
    "v_mfma_f32_16x16x32_bf16 v[24:27], v[144:147], v[32:35], v[24:27]\n"
    "buffer_load_dwordx4 v[84:87], v96, s[0:3], s54 offen offset:1024 sc1\n"
    "v_mfma_f32_16x16x32_bf16 v[28:31], v[144:147], v[36:39], v[28:31]\n"
    "s_waitcnt lgkmcnt(2)\n"
    "v_add_f32 v59, v59, v71\n"
    "s_barrier\n"
    "v_mfma_f32_16x16x32_bf16 v[0:3], a[84:87], v[40:43], v[0:3]\n"
    "ds_read_b128 v[32:35], v94 offset:2048\n"
    "ds_read_b128 v[36:39], v94 offset:3072\n"
    "v_mfma_f32_16x16x32_bf16 v[4:7], a[84:87], v[44:47], v[4:7]\n"
    "v_cndmask_b32 v56, v99, v56, s[12:13]\n"
    "v_cndmask_b32 v57, v99, v57, s[14:15]\n"
    "v_mfma_f32_16x16x32_bf16 v[8:11], a[164:167], v[40:43], v[8:11]\n"
    "buffer_store_dwordx2 v[110:111], v106, s[0:3], s49 offen sc1\n"
    "v_cndmask_b32 v58, v99, v58, s[16:17]\n"
    "v_mfma_f32_16x16x32_bf16 v[12:15], a[164:167], v[44:47], v[12:15]\n"
    "v_cndmask_b32 v59, v99, v59, s[18:19]\n"
    "v_max3_f32 v72, v56, v57, v58\n"
    "v_mfma_f32_16x16x32_bf16 v[16:19], a[4:7], v[40:43], v[16:19]\n"
    "v_max_f32 v72, v72, v59\n"
    "v_cndmask_b32 v74, v99, v72, s[66:67]\n"
    "v_mfma_f32_16x16x32_bf16 v[20:23], a[4:7], v[44:47], v[20:23]\n"
    "v_cndmask_b32 v75, v72, v99, s[66:67]\n"
    "v_max_f32_dpp v74, v74, v74 row_ror:8 row_mask:0xf bank_mask:0xf\n"
    "v_mfma_f32_16x16x32_bf16 v[24:27], a[244:247], v[40:43], v[24:27]\n"
    "v_max_f32_dpp v75, v75, v75 row_ror:8 row_mask:0xf bank_mask:0xf\n"
    "v_max_f32_dpp v74, v74, v74 row_ror:4 row_mask:0xf bank_mask:0xf\n"
    "v_mfma_f32_16x16x32_bf16 v[28:31], a[244:247], v[44:47], v[28:31]\n"
    "s_waitcnt lgkmcnt(2)\n"
    "v_max_f32_dpp v75, v75, v75 row_ror:4 row_mask:0xf bank_mask:0xf\n"
    "v_max_f32_dpp v74, v74, v74 row_ror:2 row_mask:0xf bank_mask:0xf\n"
    "v_mfma_f32_16x16x32_bf16 v[0:3], a[100:103], v[48:51], v[0:3]\n"
    "ds_read_b128 v[40:43], v95 offset:2048\n"
    "ds_read_b128 v[44:47], v95 offset:3072\n"
    "v_mfma_f32_16x16x32_bf16 v[4:7], a[100:103], v[52:55], v[4:7]\n"
    "v_max_f32_dpp v75, v75, v75 row_ror:2 row_mask:0xf bank_mask:0xf\n"
    "v_max_f32_dpp v74, v74, v74 row_ror:1 row_mask:0xf bank_mask:0xf\n"
    "v_mfma_f32_16x16x32_bf16 v[8:11], a[180:183], v[48:51], v[8:11]\n"
    "buffer_store_dwordx2 v[110:111], v107, s[0:3], s49 offen sc1\n"
    "v_max_f32_dpp v75, v75, v75 row_ror:1 row_mask:0xf bank_mask:0xf\n"
    ".Lqk_mkL2_%=:\n"
    "v_mfma_f32_16x16x32_bf16 v[12:15], a[180:183], v[52:55], v[12:15]\n"
    "v_cndmask_b32 v72, v75, v74, s[66:67]\n"
    "v_mul_f32 v72, s62, v72\n"
    "v_mfma_f32_16x16x32_bf16 v[16:19], a[20:23], v[48:51], v[16:19]\n"
    "v_fma_f32 v56, v56, s62, -v72\n"
    "v_fma_f32 v57, v57, s62, -v72\n"
    "v_mfma_f32_16x16x32_bf16 v[20:23], a[20:23], v[52:55], v[20:23]\n"
    "v_fma_f32 v58, v58, s62, -v72\n"
    "v_fma_f32 v59, v59, s62, -v72\n"
    "v_mfma_f32_16x16x32_bf16 v[24:27], v[116:119], v[48:51], v[24:27]\n"
    "v_exp_f32 v56, v56\n"
    "v_mfma_f32_16x16x32_bf16 v[28:31], v[116:119], v[52:55], v[28:31]\n"
    "s_waitcnt lgkmcnt(2)\n"
    "v_exp_f32 v58, v58\n"
    "v_mfma_f32_16x16x32_bf16 v[0:3], a[116:119], v[32:35], v[0:3]\n"
    "ds_read_b128 v[48:51], v92 offset:4096\n"
    "ds_read_b128 v[52:55], v92 offset:5120\n"
    "v_mfma_f32_16x16x32_bf16 v[4:7], a[116:119], v[36:39], v[4:7]\n"
    "v_mfma_f32_16x16x32_bf16 v[8:11], a[196:199], v[32:35], v[8:11]\n"
    "buffer_load_dwordx2 v[108:109], off, s[0:3], s55 sc1\n"
    "v_mfma_f32_16x16x32_bf16 v[12:15], a[196:199], v[36:39], v[12:15]\n"
    "v_exp_f32 v57, v57\n"
    "v_mfma_f32_16x16x32_bf16 v[16:19], a[36:39], v[32:35], v[16:19]\n"
    "v_exp_f32 v59, v59\n"
    "v_mfma_f32_16x16x32_bf16 v[20:23], a[36:39], v[36:39], v[20:23]\n"
    "v_add_f32 v72, v56, v58\n"
    "v_add_f32 v73, v57, v59\n"
    "v_mfma_f32_16x16x32_bf16 v[24:27], v[132:135], v[32:35], v[24:27]\n"
    "v_add_f32 v72, v72, v73\n"
    "v_cndmask_b32 v74, 0, v72, s[66:67]\n"
    "v_mfma_f32_16x16x32_bf16 v[28:31], v[132:135], v[36:39], v[28:31]\n"
    "s_waitcnt lgkmcnt(2)\n"
    "v_cndmask_b32 v75, v72, 0, s[66:67]\n"
    "v_add_f32_dpp v74, v74, v74 row_ror:8 row_mask:0xf bank_mask:0xf\n"
    "v_mfma_f32_16x16x32_bf16 v[0:3], a[132:135], v[40:43], v[0:3]\n"
    "ds_read_b128 v[32:35], v93 offset:4096\n"
    "ds_read_b128 v[36:39], v93 offset:5120\n"
    "v_mfma_f32_16x16x32_bf16 v[4:7], a[132:135], v[44:47], v[4:7]\n"
    "v_add_f32_dpp v75, v75, v75 row_ror:8 row_mask:0xf bank_mask:0xf\n"
    "v_add_f32_dpp v74, v74, v74 row_ror:4 row_mask:0xf bank_mask:0xf\n"
    "v_mfma_f32_16x16x32_bf16 v[8:11], a[212:215], v[40:43], v[8:11]\n"
    "v_add_f32_dpp v75, v75, v75 row_ror:4 row_mask:0xf bank_mask:0xf\n"
    "v_add_f32_dpp v74, v74, v74 row_ror:2 row_mask:0xf bank_mask:0xf\n"
    "v_mfma_f32_16x16x32_bf16 v[12:15], a[212:215], v[44:47], v[12:15]\n"
    "v_add_f32_dpp v75, v75, v75 row_ror:2 row_mask:0xf bank_mask:0xf\n"
    "v_add_f32_dpp v74, v74, v74 row_ror:1 row_mask:0xf bank_mask:0xf\n"
    "v_mfma_f32_16x16x32_bf16 v[16:19], a[52:55], v[40:43], v[16:19]\n"
    "v_add_f32_dpp v75, v75, v75 row_ror:1 row_mask:0xf bank_mask:0xf\n"
    "v_cndmask_b32 v72, v75, v74, s[66:67]\n"
    "v_mfma_f32_16x16x32_bf16 v[20:23], a[52:55], v[44:47], v[20:23]\n"
    "v_rcp_f32 v72, v72\n"
    "v_mfma_f32_16x16x32_bf16 v[24:27], v[148:151], v[40:43], v[24:27]\n"
    "v_mul_f32 v56, v56, v72 clamp\n"
    "v_mul_f32 v57, v57, v72 clamp\n"
    "v_mfma_f32_16x16x32_bf16 v[28:31], v[148:151], v[44:47], v[28:31]\n"
    "s_waitcnt lgkmcnt(2)\n"
    "v_mul_f32 v58, v58, v72 clamp\n"
    "v_mul_f32 v59, v59, v72 clamp\n"
    "v_mfma_f32_16x16x32_bf16 v[0:3], a[88:91], v[48:51], v[0:3]\n"
    "ds_read_b128 v[40:43], v94 offset:4096\n"
    "ds_read_b128 v[44:47], v94 offset:5120\n"
    "v_mfma_f32_16x16x32_bf16 v[4:7], a[88:91], v[52:55], v[4:7]\n"
    "v_cvt_pk_bf16_f32 v56, v56, v57\n"
    "v_cvt_pk_bf16_f32 v57, v58, v59\n"
    "v_mfma_f32_16x16x32_bf16 v[8:11], a[168:171], v[48:51], v[8:11]\n"
    "buffer_store_dwordx2 v[56:57], v107, s[0:3], s50 offen sc1\n"
    ".Lqk_mkL3_%=:\n"
    "v_mfma_f32_16x16x32_bf16 v[12:15], a[168:171], v[52:55], v[12:15]\n"
    "s_cmp_eq_u32 s46, 63\n"
    "s_cbranch_scc1 .Lqk_end_%=\n"
    "v_mfma_f32_16x16x32_bf16 v[16:19], a[8:11], v[48:51], v[16:19]\n"
    "s_add_u32 s46, s46, 1\n"
    "s_add_u32 s60, s46, 1\n"
    "s_lshl_b32 s53, s60, 19\n"
    "v_mfma_f32_16x16x32_bf16 v[20:23], a[8:11], v[52:55], v[20:23]\n"
    "s_lshl_b32 s45, s60, 11\n"
    "s_xor_b32 s45, s45, s23\n"
    "s_and_b32 s45, s45, 0x2000\n"
    "v_mfma_f32_16x16x32_bf16 v[24:27], a[248:251], v[48:51], v[24:27]\n"
    "s_add_u32 s53, s53, s45\n"
    "s_add_u32 s53, s53, s27\n"
    "s_add_u32 s48, s46, 0\n"
    "v_mfma_f32_16x16x32_bf16 v[28:31], a[248:251], v[52:55], v[28:31]\n"
    "s_waitcnt lgkmcnt(2)\n"
    "s_and_b32 s48, s48, 3\n"
    "s_lshl_b32 s48, s48, 22\n"
    "v_mfma_f32_16x16x32_bf16 v[0:3], a[104:107], v[32:35], v[0:3]\n"
    "s_add_u32 s49, s46, 2\n"
    "ds_read_b128 v[48:51], v95 offset:4096\n"
    "ds_read_b128 v[52:55], v95 offset:5120\n"
    "v_mfma_f32_16x16x32_bf16 v[4:7], a[104:107], v[36:39], v[4:7]\n"
    "s_and_b32 s49, s49, 3\n"
    "s_lshl_b32 s49, s49, 22\n"
    "s_and_b32 s55, s46, 63\n"
    "v_mfma_f32_16x16x32_bf16 v[8:11], a[184:187], v[32:35], v[8:11]\n"
    "s_lshl_b32 s55, s55, 7\n"
    "s_mov_b32 s50, s48\n"
    "s_add_u32 s51, s24, s48\n"
    "v_mfma_f32_16x16x32_bf16 v[12:15], a[184:187], v[36:39], v[12:15]\n"
    "s_add_u32 s52, s25, s48\n"
    "s_add_u32 s56, s30, s55\n"
    "s_add_u32 s55, s31, s55\n"
    "v_mfma_f32_16x16x32_bf16 v[16:19], a[24:27], v[32:35], v[16:19]\n"
    "s_lshl_b32 s54, s60, 20\n"
    "s_lshl_b32 s45, s60, 12\n"
    "s_xor_b32 s45, s45, s23\n"
    "v_mfma_f32_16x16x32_bf16 v[20:23], a[24:27], v[36:39], v[20:23]\n"
    "s_and_b32 s45, s45, 0x2000\n"
    "s_add_u32 s54, s54, s45\n"
    "s_add_u32 s54, s54, s29\n"
    "v_mfma_f32_16x16x32_bf16 v[24:27], v[120:123], v[32:35], v[24:27]\n"
    "v_mfma_f32_16x16x32_bf16 v[28:31], v[120:123], v[36:39], v[28:31]\n"
    "s_waitcnt lgkmcnt(2)\n"
    "s_waitcnt vmcnt(6) lgkmcnt(0)\n"
    "s_barrier\n"
    "v_mfma_f32_16x16x32_bf16 v[0:3], a[120:123], v[40:43], v[0:3]\n"
    "s_sub_u32 m0, s26, 8192\n"
    "ds_read_b128 v[32:35], v92 offset:6144\n"
    "ds_read_b128 v[36:39], v92 offset:7168\n"
    "v_mfma_f32_16x16x32_bf16 v[4:7], a[120:123], v[44:47], v[4:7]\n"
    "buffer_load_dwordx4 v96, s[0:3], s53 offen offset:0 sc1 lds\n"
    "v_mfma_f32_16x16x32_bf16 v[8:11], a[200:203], v[40:43], v[8:11]\n"
    ".Lqk_mkL4_%=:\n"
    "v_mfma_f32_16x16x32_bf16 v[12:15], a[200:203], v[44:47], v[12:15]\n"
    "v_mfma_f32_16x16x32_bf16 v[16:19], a[40:43], v[40:43], v[16:19]\n"
    "v_mfma_f32_16x16x32_bf16 v[20:23], a[40:43], v[44:47], v[20:23]\n"
    "buffer_load_dwordx4 v96, s[0:3], s53 offen offset:1024 sc1 lds\n"
    "v_mfma_f32_16x16x32_bf16 v[24:27], v[136:139], v[40:43], v[24:27]\n"
    "v_mfma_f32_16x16x32_bf16 v[28:31], v[136:139], v[44:47], v[28:31]\n"
    "s_waitcnt lgkmcnt(2)\n"
    "v_mfma_f32_16x16x32_bf16 v[0:3], a[136:139], v[48:51], v[0:3]\n"
    "ds_read_b128 v[40:43], v93 offset:6144\n"
    "ds_read_b128 v[44:47], v93 offset:7168\n"
    "v_mfma_f32_16x16x32_bf16 v[4:7], a[136:139], v[52:55], v[4:7]\n"
    "buffer_load_dwordx4 v96, s[0:3], s53 offen offset:2048 sc1 lds\n"
    "v_mfma_f32_16x16x32_bf16 v[8:11], a[216:219], v[48:51], v[8:11]\n"
    "v_mfma_f32_16x16x32_bf16 v[12:15], a[216:219], v[52:55], v[12:15]\n"
    "v_mfma_f32_16x16x32_bf16 v[16:19], a[56:59], v[48:51], v[16:19]\n"
    "v_mfma_f32_16x16x32_bf16 v[20:23], a[56:59], v[52:55], v[20:23]\n"
    "buffer_load_dwordx4 v96, s[0:3], s53 offen offset:3072 sc1 lds\n"
    "v_mfma_f32_16x16x32_bf16 v[24:27], v[152:155], v[48:51], v[24:27]\n"
    "s_sub_u32 m0, s26, 4096\n"
    "s_add_u32 s53, s53, 4096\n"
    "v_mfma_f32_16x16x32_bf16 v[28:31], v[152:155], v[52:55], v[28:31]\n"
    "s_waitcnt lgkmcnt(2)\n"
    "v_mfma_f32_16x16x32_bf16 v[0:3], a[92:95], v[32:35], v[0:3]\n"
    "ds_read_b128 v[48:51], v94 offset:6144\n"
    "ds_read_b128 v[52:55], v94 offset:7168\n"
    "v_mfma_f32_16x16x32_bf16 v[4:7], a[92:95], v[36:39], v[4:7]\n"
    "buffer_load_dwordx4 v96, s[0:3], s53 offen offset:0 sc1 lds\n"
    "v_mfma_f32_16x16x32_bf16 v[8:11], a[172:175], v[32:35], v[8:11]\n"
    "v_mfma_f32_16x16x32_bf16 v[12:15], a[172:175], v[36:39], v[12:15]\n"
    "v_mfma_f32_16x16x32_bf16 v[16:19], a[12:15], v[32:35], v[16:19]\n"
    "v_mfma_f32_16x16x32_bf16 v[20:23], a[12:15], v[36:39], v[20:23]\n"
    "buffer_load_dwordx4 v96, s[0:3], s53 offen offset:1024 sc1 lds\n"
    "v_mfma_f32_16x16x32_bf16 v[24:27], a[252:255], v[32:35], v[24:27]\n"
    "v_mfma_f32_16x16x32_bf16 v[28:31], a[252:255], v[36:39], v[28:31]\n"
    "s_waitcnt lgkmcnt(2)\n"
    "v_mfma_f32_16x16x32_bf16 v[0:3], a[108:111], v[40:43], v[0:3]\n"
    "ds_read_b128 v[32:35], v95 offset:6144\n"
    "ds_read_b128 v[36:39], v95 offset:7168\n"
    "v_mfma_f32_16x16x32_bf16 v[4:7], a[108:111], v[44:47], v[4:7]\n"
    "v_mfma_f32_16x16x32_bf16 v[8:11], a[188:191], v[40:43], v[8:11]\n"
    ".Lqk_mkL5_%=:\n"
    "v_mfma_f32_16x16x32_bf16 v[12:15], a[188:191], v[44:47], v[12:15]\n"
    "v_mfma_f32_16x16x32_bf16 v[16:19], a[28:31], v[40:43], v[16:19]\n"
    "v_mfma_f32_16x16x32_bf16 v[20:23], a[28:31], v[44:47], v[20:23]\n"
    "v_mfma_f32_16x16x32_bf16 v[24:27], v[124:127], v[40:43], v[24:27]\n"
    "v_mfma_f32_16x16x32_bf16 v[28:31], v[124:127], v[44:47], v[28:31]\n"
    "s_waitcnt lgkmcnt(2)\n"
    "v_mfma_f32_16x16x32_bf16 v[0:3], a[124:127], v[48:51], v[0:3]\n"
    "ds_read_b128 v[40:43], v96 offset:45056\n"
    "ds_read_b128 v[44:47], v96 offset:46080\n"
    "v_mfma_f32_16x16x32_bf16 v[4:7], a[124:127], v[52:55], v[4:7]\n"
    "v_mfma_f32_16x16x32_bf16 v[8:11], a[204:207], v[48:51], v[8:11]\n"
    "v_mfma_f32_16x16x32_bf16 v[12:15], a[204:207], v[52:55], v[12:15]\n"
    "v_mfma_f32_16x16x32_bf16 v[16:19], a[44:47], v[48:51], v[16:19]\n"
    "v_mfma_f32_16x16x32_bf16 v[20:23], a[44:47], v[52:55], v[20:23]\n"
    "v_mfma_f32_16x16x32_bf16 v[24:27], v[140:143], v[48:51], v[24:27]\n"
    "v_mfma_f32_16x16x32_bf16 v[28:31], v[140:143], v[52:55], v[28:31]\n"
    "s_waitcnt lgkmcnt(2)\n"
    "v_mfma_f32_16x16x32_bf16 v[0:3], a[140:143], v[32:35], v[0:3]\n"
    "ds_read_b128 v[48:51], v96 offset:47104\n"
    "ds_read_b128 v[52:55], v96 offset:48128\n"
    "v_mfma_f32_16x16x32_bf16 v[4:7], a[140:143], v[36:39], v[4:7]\n"
    "v_mfma_f32_16x16x32_bf16 v[8:11], a[220:223], v[32:35], v[8:11]\n"
    "v_mfma_f32_16x16x32_bf16 v[12:15], a[220:223], v[36:39], v[12:15]\n"
    "v_mfma_f32_16x16x32_bf16 v[16:19], a[60:63], v[32:35], v[16:19]\n"
    "v_mfma_f32_16x16x32_bf16 v[20:23], a[60:63], v[36:39], v[20:23]\n"
    "v_mfma_f32_16x16x32_bf16 v[24:27], v[156:159], v[32:35], v[24:27]\n"
    "v_mfma_f32_16x16x32_bf16 v[28:31], v[156:159], v[36:39], v[28:31]\n"
    "s_waitcnt lgkmcnt(2)\n"
    "v_mfma_f32_16x16x32_bf16 v[0:3], a[144:147], v[40:43], v[0:3]\n"
    "s_waitcnt vmcnt(7)\n"
    "ds_read_b128 v[32:35], v96 offset:49152\n"
    "ds_read_b128 v[36:39], v96 offset:50176\n"
    "s_memrealtime s[70:71]\n"
    "v_mfma_f32_16x16x32_bf16 v[4:7], a[144:147], v[44:47], v[4:7]\n"
    "v_readfirstlane_b32 s44, v108\n"
    "v_readfirstlane_b32 s45, v109\n"
    "v_mfma_f32_16x16x32_bf16 v[8:11], a[224:227], v[40:43], v[8:11]\n"
    "v_mfma_f32_16x16x32_bf16 v[12:15], a[224:227], v[44:47], v[12:15]\n"
    "s_cmp_eq_u32 s45, s47\n"
    "s_cselect_b32 s57, s44, s57\n"
    "s_add_u32 s57, s57, 264\n"
    "v_mfma_f32_16x16x32_bf16 v[16:19], a[64:67], v[40:43], v[16:19]\n"
    "s_sub_u32 s58, s57, 59\n"
    "s_mov_b32 s47, s60\n"
    "s_mov_b32 m0, s26\n"
    "v_mfma_f32_16x16x32_bf16 v[20:23], a[64:67], v[44:47], v[20:23]\n"
    ".Lqk_mkL6_%=:\n"
    "v_mfma_f32_16x16x32_bf16 v[24:27], v[160:163], v[40:43], v[24:27]\n"
    "s_sub_u32 s72, s58, s70\n"
    "v_mfma_f32_16x16x32_bf16 v[28:31], v[160:163], v[44:47], v[28:31]\n"
    "s_waitcnt lgkmcnt(2)\n"
    ".Lqk_mk6x19_%=:\n"
    "s_cmp_gt_i32 s72, 4\n"
    "s_cbranch_scc1 .Lqk_early_%=\n"
    "buffer_load_dwordx4 v96, s[0:3], s51 offen sc1 lds\n"
    "buffer_load_dwordx4 v96, s[0:3], s51 offen offset:1024 sc1 lds\n"
    ".Lqk_asked_%=:\n"
    ".Lqk_mk7x21_%=:\n"
    "v_mfma_f32_16x16x32_bf16 v[0:3], a[148:151], v[48:51], v[0:3]\n"
    "ds_read_b128 v[40:43], v96 offset:51200\n"
    "ds_read_b128 v[44:47], v96 offset:52224\n"
    "v_mfma_f32_16x16x32_bf16 v[4:7], a[148:151], v[52:55], v[4:7]\n"
    ".Lqk_mkM0_%=:\n"
    "v_mfma_f32_16x16x32_bf16 v[8:11], a[228:231], v[48:51], v[8:11]\n"
    "v_mfma_f32_16x16x32_bf16 v[12:15], a[228:231], v[52:55], v[12:15]\n"
    "v_mfma_f32_16x16x32_bf16 v[16:19], a[68:71], v[48:51], v[16:19]\n"
    "v_mfma_f32_16x16x32_bf16 v[20:23], a[68:71], v[52:55], v[20:23]\n"
    ".Lqk_mkM1_%=:\n"
    "v_mfma_f32_16x16x32_bf16 v[24:27], v[164:167], v[48:51], v[24:27]\n"
    "v_mfma_f32_16x16x32_bf16 v[28:31], v[164:167], v[52:55], v[28:31]\n"
    "s_waitcnt lgkmcnt(2)\n"
    "v_mfma_f32_16x16x32_bf16 v[0:3], a[152:155], v[32:35], v[0:3]\n"
    "buffer_load_dwordx4 v96, s[0:3], s52 offen offset:2048 sc1 lds\n"
    "v_mfma_f32_16x16x32_bf16 v[4:7], a[152:155], v[36:39], v[4:7]\n"
    "v_mfma_f32_16x16x32_bf16 v[8:11], a[232:235], v[32:35], v[8:11]\n"
    ".Lqk_mkM2_%=:\n"
    "v_mfma_f32_16x16x32_bf16 v[12:15], a[232:235], v[36:39], v[12:15]\n"
    "v_mfma_f32_16x16x32_bf16 v[16:19], a[72:75], v[32:35], v[16:19]\n"
    "v_mfma_f32_16x16x32_bf16 v[20:23], a[72:75], v[36:39], v[20:23]\n"
    "v_mfma_f32_16x16x32_bf16 v[24:27], v[168:171], v[32:35], v[24:27]\n"
    ".Lqk_mkM3_%=:\n"
    "v_mfma_f32_16x16x32_bf16 v[28:31], v[168:171], v[36:39], v[28:31]\n"
    "s_waitcnt lgkmcnt(0)\n"
    "v_mfma_f32_16x16x32_bf16 v[0:3], a[156:159], v[40:43], v[0:3]\n"
    "v_mfma_f32_16x16x32_bf16 v[4:7], a[156:159], v[44:47], v[4:7]\n"
    "v_mfma_f32_16x16x32_bf16 v[8:11], a[236:239], v[40:43], v[8:11]\n"
    "v_mfma_f32_16x16x32_bf16 v[12:15], a[236:239], v[44:47], v[12:15]\n"
    "v_mfma_f32_16x16x32_bf16 v[16:19], a[76:79], v[40:43], v[16:19]\n"
    "v_mfma_f32_16x16x32_bf16 v[20:23], a[76:79], v[44:47], v[20:23]\n"
    "v_mfma_f32_16x16x32_bf16 v[24:27], v[172:175], v[40:43], v[24:27]\n"
    ".Lqk_mkM4_%=:\n"
    "v_mfma_f32_16x16x32_bf16 v[28:31], v[172:175], v[44:47], v[28:31]\n"
    "ds_read_b128 v[32:35], v97 offset:0\n"
    "ds_read_b128 v[36:39], v97 offset:1024\n"
    "ds_read_b128 v[40:43], v97 offset:2048\n"
    "ds_read_b128 v[44:47], v97 offset:3072\n"
    "ds_read_b128 v[48:51], v97 offset:4096\n"
    "ds_read_b128 v[52:55], v97 offset:5120\n"
    "v_cvt_pk_bf16_f32 v68, v0, v1\n"
    "v_cvt_pk_bf16_f32 v69, v2, v3\n"
    "v_cvt_pk_bf16_f32 v70, v8, v9\n"
    "v_cvt_pk_bf16_f32 v71, v10, v11\n"
    "v_cvt_pk_bf16_f32 v72, v4, v5\n"
    "v_cvt_pk_bf16_f32 v73, v6, v7\n"
    "v_cvt_pk_bf16_f32 v74, v12, v13\n"
    "v_cvt_pk_bf16_f32 v75, v14, v15\n"
    "v_mfma_f32_16x16x32_bf16 v[108:111], v[68:71], v[84:87], 0\n"
    "v_cvt_pk_bf16_f32 v88, v24, v25\n"
    "v_cvt_pk_bf16_f32 v89, v26, v27\n"
    "v_cvt_pk_bf16_f32 v90, v28, v29\n"
    "v_cvt_pk_bf16_f32 v91, v30, v31\n"
    "v_mfma_f32_16x16x32_bf16 v[24:27], v[72:75], v[84:87], 0\n"
    "v_cvt_pk_bf16_f32 v76, v16, v17\n"
    "v_cvt_pk_bf16_f32 v77, v18, v19\n"
    "v_cvt_pk_bf16_f32 v78, v20, v21\n"
    "v_cvt_pk_bf16_f32 v79, v22, v23\n"
    "v_mfma_f32_16x16x32_bf16 v[28:31], v[68:71], v[80:83], 0\n"
    "v_mov_b64 v[0:1], 0\n"
    "v_mov_b64 v[2:3], 0\n"
    "v_mfma_f32_16x16x16_bf16 v[108:111], v[76:77], v[90:91], v[108:111]\n"
    "v_mov_b64 v[4:5], 0\n"
    "v_mov_b64 v[6:7], 0\n"
    "v_mfma_f32_16x16x16_bf16 v[24:27], v[78:79], v[90:91], v[24:27]\n"
    "v_mov_b64 v[8:9], 0\n"
    "v_mov_b64 v[10:11], 0\n"
    "v_mfma_f32_16x16x16_bf16 v[28:31], v[76:77], v[88:89], v[28:31]\n"
    "v_mov_b64 v[12:13], 0\n"
    "v_mov_b64 v[14:15], 0\n"
    "v_mov_b64 v[16:17], 0\n"
    "v_mov_b64 v[18:19], 0\n"
    "v_mov_b64 v[20:21], 0\n"
    "v_mov_b64 v[22:23], 0\n"
    "ds_write_b128 v104, v[108:111]\n"
    "ds_write_b128 v104, v[24:27] offset:64\n"
    "ds_write_b128 v104, v[28:31] offset:160\n"
    ".Lqk_mk8x22_%=:\n"
    "s_branch .Lqk_take_%=\n"
    ".Lqk_tk0_%=:\n"
    ".Lqk_po3_%=:\n"
    "buffer_load_dword v109, v108, s[0:3], 0 offen sc1\n"
    "s_waitcnt vmcnt(0)\n"
    "v_cmp_gt_i32 vcc, 32, v109\n"
    "s_nop 0\n"
    "s_cbranch_vccz .Lqk_pd4_%=\n"
    "s_add_u32 s40, s40, 1\n"
    "s_cmp_lt_u32 s40, 0x40000\n"
    "s_cbranch_scc1 .Lqk_sp5_%=\n"
    "v_mov_b32 v109, 90\n"
    "s_mov_b32 s44, 33728\n"
    "buffer_store_dword v109, off, s[0:3], s44 sc1\n"
    "s_waitcnt vmcnt(0)\n"
    "s_branch .Lqk_pd4_%=\n"
    ".Lqk_sp5_%=:\n"
    "s_sleep 8\n"
    "s_branch .Lqk_po3_%=\n"
    ".Lqk_pd4_%=:\n"
    "s_branch .Lqk_tk1_%=\n"
    ".Lqk_early_%=:\n"
    ".Lqk_rt21_%=:\n"
    "s_memrealtime s[42:43]\n"
    "s_waitcnt lgkmcnt(0)\n"
    "s_sub_u32 s44, s58, s42\n"
    "s_cmp_lt_u32 s44, 600\n"
    "s_cbranch_scc1 .Lqk_rt21_%=\n"
    "buffer_load_dwordx4 v96, s[0:3], s51 offen sc1 lds\n"
    "buffer_load_dwordx4 v96, s[0:3], s51 offen offset:1024 sc1 lds\n"
    "s_branch .Lqk_asked_%=\n"
    ".Lqk_retry_%=:\n"
    "s_add_u32 s40, s40, 1\n"
    "s_cmp_lt_u32 s40, 0x40000\n"
    "s_cbranch_scc1 .Lqk_sp24_%=\n"
    "v_mov_b32 v108, 1\n"
    "s_mov_b32 s44, 33728\n"
    "buffer_store_dword v108, off, s[0:3], s44 sc1\n"
    "s_waitcnt vmcnt(0)\n"
    "s_branch .Lqk_go_%=\n"
    ".Lqk_sp24_%=:\n"
    "s_waitcnt lgkmcnt(0)\n"
    "s_nop 7\n"
    "ds_read_b128 v[32:35], v97 offset:0\n"
    "ds_read_b128 v[36:39], v97 offset:1024\n"
    "v_mov_b64 v[0:1], 0\n"
    "v_mov_b64 v[2:3], 0\n"
    "v_mov_b64 v[4:5], 0\n"
    "v_mov_b64 v[6:7], 0\n"
    "v_mov_b64 v[8:9], 0\n"
    "v_mov_b64 v[10:11], 0\n"
    "v_mov_b64 v[12:13], 0\n"
    "v_mov_b64 v[14:15], 0\n"
    "v_mov_b64 v[16:17], 0\n"
    "v_mov_b64 v[18:19], 0\n"
    "v_mov_b64 v[20:21], 0\n"
    "v_mov_b64 v[22:23], 0\n"
    "buffer_load_dwordx4 v96, s[0:3], s51 offen sc1 lds\n"
    "buffer_load_dwordx4 v96, s[0:3], s51 offen offset:1024 sc1 lds\n"
    "buffer_load_dwordx4 v96, s[0:3], s52 offen offset:2048 sc1 lds\n"
    ".Lqk_r1_%=:\n"
    "s_waitcnt vmcnt(0)\n"
    "s_branch .Lqk_take_%=\n"
    ".Lqk_r2_%=:\n"
    "s_add_u32 s40, s40, 1\n"
    "s_cmp_lt_u32 s40, 0x40000\n"
    "s_cbranch_scc1 .Lqk_sp25_%=\n"
    "v_mov_b32 v108, 5\n"
    "s_mov_b32 s44, 33728\n"
    "buffer_store_dword v108, off, s[0:3], s44 sc1\n"
    "s_waitcnt vmcnt(0)\n"
    "s_branch .Lqk_go2_%=\n"
    ".Lqk_sp25_%=:\n"
    "buffer_load_dwordx4 v96, s[0:3], s52 offen offset:2048 sc1 lds\n"
    ".Lqk_r3_%=:\n"
    "s_waitcnt vmcnt(0)\n"
    "ds_read_b128 v[64:67], v92 offset:10240\n"
    "s_waitcnt lgkmcnt(0)\n"
    "v_max_u32 v108, v64, v66\n"
    "v_cmp_eq_u32 vcc, -1, v108\n"
    "s_nop 0\n"
    "s_cbranch_vccnz .Lqk_r2_%=\n"
    "s_branch .Lqk_go2_%=\n"
    ".Lqk_end_%=:\n"
    "s_waitcnt vmcnt(0) lgkmcnt(0)\n"
    "s_endpgm\n"
    :: "{s[0:3]}"(s0_), "{s[4:7]}"(s4_), "{s[8:11]}"(s8_), "{s[12:15]}"(s12_), "{s[16:19]}"(s16_), "{s[20:23]}"(s20_), "{s[24:27]}"(s24_), "{s[28:31]}"(s28_), "{s[32:35]}"(s32_), "{s[36:39]}"(s36_), "{v[0:3]}"(v0_), "{v[4:7]}"(v4_), "{v[92:95]}"(v92_), "{v[96:99]}"(v96_), "{v[100:103]}"(v100_), "{v[104:107]}"(v104_) : "memory", "v255", "a255", "s95");
    __builtin_unreachable();
}
DEV void vo_role(const MegaArgs& A, const int L, const int h, const int rep) {
    ROLE_HEAD;
    const unsigned um = xl(RG_UM + ((8 * w + lq) * 4 + (h >> 3)) * 2048 + (h & 7) * 256 + l16 * 16, L);
#define XU 0
    const unsigned umx = XU ? OOB : xl(RG_UMX + ((4 * (2 * w + (lq & 1)) + (l16 >> 2)) * 4 + (h >> 3)) * 512 + (h & 7) * 64 + (l & 3) * 16 + (lq >> 1) * 8, L);
    const u32x4 s0_ = {RFL(rx[0]), RFL(rx[1]), RFL(rx[2]), RFL(rx[3])};
    const u32x4 s4_ = {RFL(lo(A.Wo + h * 192 * 640)), RFL(hi(A.Wo + h * 192 * 640)), RFL(192u * 1280u), RFL(0x20000u)};
    const u32x4 s8_ = {RFL(lo(A.Wv + h * 192)), RFL(hi(A.Wv + h * 192)), RFL(WBYTES - h * 384), RFL(0x20000u)};
    const u32x4 s12_ = {RFL(w), RFL(L << 13), RFL(L), RFL(xl(RG_MEM + rep * 16384, L) + w * 2048)};
    const u32x4 s16_ = {RFL(w < 2 ? xl(RG_MEM + rep * 16384 + 8192, L) + w * 1024 - 2048 : OOB), RFL(w * 13312 + 8192), RFL(REC0 + w * 16384), RFL(65536 - 4096 - w * 14336)};
    const u32x4 s20_ = {RFL(QT0 + h * 32768 + 16384 + w * 1024), RFL(146432 + w * 1024), RFL(xl(RG_UT + (32 * w + (h >> 3)) * 8192 + (h & 7) * 1024, L)), RFL(xl(Z_X + 8192, L))};
    const u32x4 s24_ = {RFL(xl(Z_CT + (h & 7) * 256, L)), RFL(w == 0 ? xl(Z_QV + (32 + h) * 4, L) : OOB), RFL(DBG0 + ((L * 128 + 32 + h) * 4 + w) * 2048), RFL(0u)};
    const u32x4 v0_ = {(unsigned)(((3 * w + 0) * 64 + l) / 24 * 12288 + ((((3 * w + 0) * 64 + l) % 24) ^ ((((3 * w + 0) * 64 + l) / 48) & 6)) * 16 - 0), (unsigned)(((3 * w + 1) * 64 + l) / 24 * 12288 + ((((3 * w + 1) * 64 + l) % 24) ^ ((((3 * w + 1) * 64 + l) / 48) & 6)) * 16 - 1024), (unsigned)(((3 * w + 2) * 64 + l) / 24 * 12288 + ((((3 * w + 2) * 64 + l) % 24) ^ ((((3 * w + 2) * 64 + l) / 48) & 6)) * 16 - 2048), (unsigned)((8 * lq + ((l >> 2) & 1) + ((l >> 1) & 4)) * 384 + (w ^ (((8 * lq + ((l >> 2) & 1) + ((l >> 1) & 4)) >> 2) & 3)) * 32 + (l & 3) * 8 + ((w + 0) & 3) * 12288)};
    const u32x4 v4_ = {(unsigned)((8 * lq + ((l >> 2) & 1) + ((l >> 1) & 4)) * 384 + (w ^ (((8 * lq + ((l >> 2) & 1) + ((l >> 1) & 4)) >> 2) & 3)) * 32 + (l & 3) * 8 + ((w + 1) & 3) * 12288), (unsigned)((8 * lq + ((l >> 2) & 1) + ((l >> 1) & 4)) * 384 + (w ^ (((8 * lq + ((l >> 2) & 1) + ((l >> 1) & 4)) >> 2) & 3)) * 32 + (l & 3) * 8 + ((w + 2) & 3) * 12288), (unsigned)((8 * lq + ((l >> 2) & 1) + ((l >> 1) & 4)) * 384 + (w ^ (((8 * lq + ((l >> 2) & 1) + ((l >> 1) & 4)) >> 2) & 3)) * 32 + (l & 3) * 8 + ((w + 3) & 3) * 12288), (unsigned)((8 * lq + ((l >> 2) & 1) + ((l >> 1) & 4)) * 384 + (w ^ (((8 * lq + ((l >> 2) & 1) + ((l >> 1) & 4)) >> 2) & 3)) * 32 + (l & 3) * 8)};
    const u32x4 v8_ = {(unsigned)(0), (unsigned)(0), (unsigned)(0), (unsigned)(0)};
    const u32x4 v12_ = {(unsigned)((4 * 0 + lq) * 1280 + 256 * w + ((l16 ^ (2 * ((4 * 0 + lq) & 7))) * 16) - 0), (unsigned)((4 * 1 + lq) * 1280 + 256 * w + ((l16 ^ (2 * ((4 * 1 + lq) & 7))) * 16) - 1024), (unsigned)((4 * 2 + lq) * 1280 + 256 * w + ((l16 ^ (2 * ((4 * 2 + lq) & 7))) * 16) - 2048), (unsigned)((4 * 3 + lq) * 1280 + 256 * w + ((l16 ^ (2 * ((4 * 3 + lq) & 7))) * 16) - 3072)};
    const u32x4 v16_ = {(unsigned)((4 * w + lq) * 1280 + 1024 + ((l16 ^ (2 * ((4 * w + lq) & 7))) * 16)), (unsigned)(w * 8192 + (l >> 2) * 256 + ((l >> 2) & 7) * 32 + (l & 3) * 8), (unsigned)(32768 + (l >> 2) * 256 + (((l >> 2) & 7) ^ (2 * w)) * 32 + (l & 3) * 8), (unsigned)(0u)};
    const u32x4 v92_ = {(unsigned)(l * 16 + ((w + 0) & 3) * 13312), (unsigned)(l * 16 + ((w + 1) & 3) * 13312), (unsigned)(l * 16 + ((w + 2) & 3) * 13312), (unsigned)(l * 16 + ((w + 3) & 3) * 13312)};
    const u32x4 v96_ = {(unsigned)(l * 16), (unsigned)(l * 16 + 64512 + w * 20480), (unsigned)((l & 2) ? 0xeeeeu : 0x4444u), (unsigned)(61440 + 512 * w + (l16 >> 2) * 64 + (l & 1) * 32 + sw2(lq) * 8 + (l & 2) * 2)};
    const u32x4 v100_ = {(unsigned)(61440 + 2048 + 256 * w + (l16 >> 2) * 64 + (l & 1) * 32 + sw2(lq) * 8 + (l & 2) * 2), (unsigned)(61440 + 512 * (lq >> 1) + 128 * (lq & 1) + (l & 4) * 8 + (l & 8) * 32 + (l & 3) * 8), (unsigned)(um), (unsigned)(umx)};
    const u32x4 v104_ = {(unsigned)(xl(RG_UTX + ((8 * w + (l16 >> 2)) * 4 + (h >> 3)) * 2048 + (h & 1) * 1024 + ((h & 7) >> 1) * 256 + lq * 64 + (l & 3) * 16, L)), (unsigned)(l < 4 || l > 8 ? xl(Z_QW + h * 128 + (l & 3) * 4, L) : (L ? xl(Z_XN + rep * 2048 + (l - 4) * 128, 0) : OOB)), (unsigned)(146432 + l * 16), (unsigned)(148480 + l * 16)};
    asm volatile(
    "s_mov_b32 s28, 0\n"
    "s_mov_b64 s[30:31], 0\n"
    "s_mov_b32 s33, 0\n"
    "s_cmp_eq_u32 s13, 0\n"
    "s_cselect_b32 s32, 0, 28\n"
    "s_sub_u32 s32, s32, 0\n"
    ".Lvo_ho1_%=:\n"
    "s_sub_u32 s32, s32, 1\n"
    "s_cbranch_scc1 .Lvo_hd2_%=\n"
    "s_sleep 17\n"
    "s_branch .Lvo_ho1_%=\n"
    ".Lvo_hd2_%=:\n"
    "s_mul_i32 s41, s12, 3072\n"
    "s_lshl_b32 s42, s12, 13\n"
    "s_lshl_b32 s43, s12, 10\n"
    "s_mov_b32 s49, 0\n"
    "s_add_u32 m0, s42, 0\n"
    "s_nop 0\n"
    "buffer_load_dwordx4 v12, s[4:7], s49 offen offset:0 nt lds\n"
    "buffer_load_dwordx4 v13, s[4:7], s49 offen offset:1024 nt lds\n"
    "buffer_load_dwordx4 v14, s[4:7], s49 offen offset:2048 nt lds\n"
    "buffer_load_dwordx4 v15, s[4:7], s49 offen offset:3072 nt lds\n"
    "s_add_u32 m0, s43, 32768\n"
    "s_nop 0\n"
    "buffer_load_dwordx4 v16, s[4:7], s49 offen nt lds\n"
    "s_mov_b32 s49, 81920\n"
    "s_add_u32 m0, s42, 4096\n"
    "s_nop 0\n"
    "buffer_load_dwordx4 v12, s[4:7], s49 offen offset:0 nt lds\n"
    "buffer_load_dwordx4 v13, s[4:7], s49 offen offset:1024 nt lds\n"
    "buffer_load_dwordx4 v14, s[4:7], s49 offen offset:2048 nt lds\n"
    "buffer_load_dwordx4 v15, s[4:7], s49 offen offset:3072 nt lds\n"
    "s_add_u32 m0, s43, 36864\n"
    "s_nop 0\n"
    "buffer_load_dwordx4 v16, s[4:7], s49 offen nt lds\n"
    "s_mov_b32 s49, 20480\n"
    "s_add_u32 m0, s42, 49152\n"
    "s_nop 0\n"
    "buffer_load_dwordx4 v12, s[4:7], s49 offen offset:0 nt lds\n"
    "buffer_load_dwordx4 v13, s[4:7], s49 offen offset:1024 nt lds\n"
    "buffer_load_dwordx4 v14, s[4:7], s49 offen offset:2048 nt lds\n"
    "buffer_load_dwordx4 v15, s[4:7], s49 offen offset:3072 nt lds\n"
    "s_add_u32 m0, s43, 81920\n"
    "s_nop 0\n"
    "buffer_load_dwordx4 v16, s[4:7], s49 offen nt lds\n"
    "s_mov_b32 s49, 102400\n"
    "s_add_u32 m0, s42, 53248\n"
    "s_nop 0\n"
    "buffer_load_dwordx4 v12, s[4:7], s49 offen offset:0 nt lds\n"
    "buffer_load_dwordx4 v13, s[4:7], s49 offen offset:1024 nt lds\n"
    "buffer_load_dwordx4 v14, s[4:7], s49 offen offset:2048 nt lds\n"
    "buffer_load_dwordx4 v15, s[4:7], s49 offen offset:3072 nt lds\n"
    "s_add_u32 m0, s43, 86016\n"
    "s_nop 0\n"
    "buffer_load_dwordx4 v16, s[4:7], s49 offen nt lds\n"
    "s_mov_b32 s49, 40960\n"
    "s_add_u32 m0, s42, 98304\n"
    "s_nop 0\n"
    "buffer_load_dwordx4 v12, s[4:7], s49 offen offset:0 nt lds\n"
    "buffer_load_dwordx4 v13, s[4:7], s49 offen offset:1024 nt lds\n"
    "buffer_load_dwordx4 v14, s[4:7], s49 offen offset:2048 nt lds\n"
    "buffer_load_dwordx4 v15, s[4:7], s49 offen offset:3072 nt lds\n"
    "s_add_u32 m0, s43, 131072\n"
    "s_nop 0\n"
    "buffer_load_dwordx4 v16, s[4:7], s49 offen nt lds\n"
    "s_mov_b32 s49, 122880\n"
    "s_add_u32 m0, s42, 102400\n"
    "s_nop 0\n"
    "buffer_load_dwordx4 v12, s[4:7], s49 offen offset:0 nt lds\n"
    "buffer_load_dwordx4 v13, s[4:7], s49 offen offset:1024 nt lds\n"
    "buffer_load_dwordx4 v14, s[4:7], s49 offen offset:2048 nt lds\n"
    "buffer_load_dwordx4 v15, s[4:7], s49 offen offset:3072 nt lds\n"
    "s_add_u32 m0, s43, 135168\n"
    "s_nop 0\n"
    "buffer_load_dwordx4 v16, s[4:7], s49 offen nt lds\n"
    "v_mov_b64 v[108:109], -1\n"
    "v_mov_b64 v[110:111], -1\n"
    "s_mov_b32 s49, 0x10000\n"
    "buffer_store_dwordx4 v[108:111], v102, s[0:3], 0 offen sc1\n"
    "buffer_store_dwordx4 v[108:111], v102, s[0:3], s49 offen sc1\n"
    "buffer_store_dwordx2 v[108:109], v103, s[0:3], 0 offen sc1\n"
    "s_waitcnt vmcnt(23)\n"
    "s_barrier\n"
    "v_add_u32 v24, 0, v17\n"
    "v_add_u32 v32, 0, v18\n"
    "v_xor_b32 v33, 32, v32\n"
    "v_xor_b32 v25, 32, v24\n"
    "v_xor_b32 v26, 64, v24\n"
    "v_xor_b32 v27, 96, v24\n"
    "v_xor_b32 v28, 128, v24\n"
    "v_xor_b32 v29, 160, v24\n"
    "v_xor_b32 v30, 192, v24\n"
    "v_xor_b32 v31, 224, v24\n"
    "ds_read_b64_tr_b16 a[0:1], v24 offset:0\n"
    "ds_read_b64_tr_b16 a[2:3], v24 offset:4096\n"
    "ds_read_b64_tr_b16 a[24:25], v25 offset:0\n"
    "ds_read_b64_tr_b16 a[26:27], v25 offset:4096\n"
    "ds_read_b64_tr_b16 a[48:49], v26 offset:0\n"
    "ds_read_b64_tr_b16 a[50:51], v26 offset:4096\n"
    "ds_read_b64_tr_b16 a[72:73], v27 offset:0\n"
    "ds_read_b64_tr_b16 a[74:75], v27 offset:4096\n"
    "ds_read_b64_tr_b16 a[96:97], v28 offset:0\n"
    "ds_read_b64_tr_b16 a[98:99], v28 offset:4096\n"
    "ds_read_b64_tr_b16 a[120:121], v29 offset:0\n"
    "ds_read_b64_tr_b16 a[122:123], v29 offset:4096\n"
    "ds_read_b64_tr_b16 a[144:145], v30 offset:0\n"
    "ds_read_b64_tr_b16 a[146:147], v30 offset:4096\n"
    "ds_read_b64_tr_b16 a[168:169], v31 offset:0\n"
    "ds_read_b64_tr_b16 a[170:171], v31 offset:4096\n"
    "ds_read_b64_tr_b16 a[192:193], v32 offset:0\n"
    "ds_read_b64_tr_b16 a[194:195], v32 offset:4096\n"
    "ds_read_b64_tr_b16 a[216:217], v33 offset:0\n"
    "ds_read_b64_tr_b16 a[218:219], v33 offset:4096\n"
    "s_waitcnt lgkmcnt(0)\n"
    "s_barrier\n"
    "s_mov_b32 s49, 61440\n"
    "s_add_u32 m0, s42, 0\n"
    "s_nop 0\n"
    "buffer_load_dwordx4 v12, s[4:7], s49 offen offset:0 nt lds\n"
    "buffer_load_dwordx4 v13, s[4:7], s49 offen offset:1024 nt lds\n"
    "buffer_load_dwordx4 v14, s[4:7], s49 offen offset:2048 nt lds\n"
    "buffer_load_dwordx4 v15, s[4:7], s49 offen offset:3072 nt lds\n"
    "s_add_u32 m0, s43, 32768\n"
    "s_nop 0\n"
    "buffer_load_dwordx4 v16, s[4:7], s49 offen nt lds\n"
    "s_mov_b32 s49, 143360\n"
    "s_add_u32 m0, s42, 4096\n"
    "s_nop 0\n"
    "buffer_load_dwordx4 v12, s[4:7], s49 offen offset:0 nt lds\n"
    "buffer_load_dwordx4 v13, s[4:7], s49 offen offset:1024 nt lds\n"
    "buffer_load_dwordx4 v14, s[4:7], s49 offen offset:2048 nt lds\n"
    "buffer_load_dwordx4 v15, s[4:7], s49 offen offset:3072 nt lds\n"
    "s_add_u32 m0, s43, 36864\n"
    "s_nop 0\n"
    "buffer_load_dwordx4 v16, s[4:7], s49 offen nt lds\n"
    "s_waitcnt vmcnt(23)\n"
    "s_barrier\n"
    "v_add_u32 v24, 49152, v17\n"
    "v_add_u32 v32, 49152, v18\n"
    "v_xor_b32 v33, 32, v32\n"
    "v_xor_b32 v25, 32, v24\n"
    "v_xor_b32 v26, 64, v24\n"
    "v_xor_b32 v27, 96, v24\n"
    "v_xor_b32 v28, 128, v24\n"
    "v_xor_b32 v29, 160, v24\n"
    "v_xor_b32 v30, 192, v24\n"
    "v_xor_b32 v31, 224, v24\n"
    "ds_read_b64_tr_b16 a[4:5], v24 offset:0\n"
    "ds_read_b64_tr_b16 a[6:7], v24 offset:4096\n"
    "ds_read_b64_tr_b16 a[28:29], v25 offset:0\n"
    "ds_read_b64_tr_b16 a[30:31], v25 offset:4096\n"
    "ds_read_b64_tr_b16 a[52:53], v26 offset:0\n"
    "ds_read_b64_tr_b16 a[54:55], v26 offset:4096\n"
    "ds_read_b64_tr_b16 a[76:77], v27 offset:0\n"
    "ds_read_b64_tr_b16 a[78:79], v27 offset:4096\n"
    "ds_read_b64_tr_b16 a[100:101], v28 offset:0\n"
    "ds_read_b64_tr_b16 a[102:103], v28 offset:4096\n"
    "ds_read_b64_tr_b16 a[124:125], v29 offset:0\n"
    "ds_read_b64_tr_b16 a[126:127], v29 offset:4096\n"
    "ds_read_b64_tr_b16 a[148:149], v30 offset:0\n"
    "ds_read_b64_tr_b16 a[150:151], v30 offset:4096\n"
    "ds_read_b64_tr_b16 a[172:173], v31 offset:0\n"
    "ds_read_b64_tr_b16 a[174:175], v31 offset:4096\n"
    "ds_read_b64_tr_b16 a[196:197], v32 offset:0\n"
    "ds_read_b64_tr_b16 a[198:199], v32 offset:4096\n"
    "ds_read_b64_tr_b16 a[220:221], v33 offset:0\n"
    "ds_read_b64_tr_b16 a[222:223], v33 offset:4096\n"
    "s_waitcnt lgkmcnt(0)\n"
    "s_barrier\n"
    "s_mov_b32 s49, 163840\n"
    "s_add_u32 m0, s42, 49152\n"
    "s_nop 0\n"
    "buffer_load_dwordx4 v12, s[4:7], s49 offen offset:0 nt lds\n"
    "buffer_load_dwordx4 v13, s[4:7], s49 offen offset:1024 nt lds\n"
    "buffer_load_dwordx4 v14, s[4:7], s49 offen offset:2048 nt lds\n"
    "buffer_load_dwordx4 v15, s[4:7], s49 offen offset:3072 nt lds\n"
    "s_add_u32 m0, s43, 81920\n"
    "s_nop 0\n"
    "buffer_load_dwordx4 v16, s[4:7], s49 offen nt lds\n"
    "s_mov_b32 s49, 184320\n"
    "s_add_u32 m0, s42, 53248\n"
    "s_nop 0\n"
    "buffer_load_dwordx4 v12, s[4:7], s49 offen offset:0 nt lds\n"
    "buffer_load_dwordx4 v13, s[4:7], s49 offen offset:1024 nt lds\n"
    "buffer_load_dwordx4 v14, s[4:7], s49 offen offset:2048 nt lds\n"
    "buffer_load_dwordx4 v15, s[4:7], s49 offen offset:3072 nt lds\n"
    "s_add_u32 m0, s43, 86016\n"
    "s_nop 0\n"
    "buffer_load_dwordx4 v16, s[4:7], s49 offen nt lds\n"
    "v_and_b32 v108, 0x70, v96\n"
    "v_lshlrev_b32 v108, 2, v108\n"
    "v_or_b32 v108, 0x8000, v108\n"
    "buffer_load_dword v109, v108, s[0:3], 0 offen sc1\n"
    "s_waitcnt vmcnt(24)\n"
    "s_barrier\n"
    "v_add_u32 v24, 98304, v17\n"
    "v_add_u32 v32, 98304, v18\n"
    "v_xor_b32 v33, 32, v32\n"
    "v_xor_b32 v25, 32, v24\n"
    "v_xor_b32 v26, 64, v24\n"
    "v_xor_b32 v27, 96, v24\n"
    "v_xor_b32 v28, 128, v24\n"
    "v_xor_b32 v29, 160, v24\n"
    "v_xor_b32 v30, 192, v24\n"
    "v_xor_b32 v31, 224, v24\n"
    "ds_read_b64_tr_b16 a[8:9], v24 offset:0\n"
    "ds_read_b64_tr_b16 a[10:11], v24 offset:4096\n"
    "ds_read_b64_tr_b16 a[32:33], v25 offset:0\n"
    "ds_read_b64_tr_b16 a[34:35], v25 offset:4096\n"
    "ds_read_b64_tr_b16 a[56:57], v26 offset:0\n"
    "ds_read_b64_tr_b16 a[58:59], v26 offset:4096\n"
    "ds_read_b64_tr_b16 a[80:81], v27 offset:0\n"
    "ds_read_b64_tr_b16 a[82:83], v27 offset:4096\n"
    "ds_read_b64_tr_b16 a[104:105], v28 offset:0\n"
    "ds_read_b64_tr_b16 a[106:107], v28 offset:4096\n"
    "ds_read_b64_tr_b16 a[128:129], v29 offset:0\n"
    "ds_read_b64_tr_b16 a[130:131], v29 offset:4096\n"
    "ds_read_b64_tr_b16 a[152:153], v30 offset:0\n"
    "ds_read_b64_tr_b16 a[154:155], v30 offset:4096\n"
    "ds_read_b64_tr_b16 a[176:177], v31 offset:0\n"
    "ds_read_b64_tr_b16 a[178:179], v31 offset:4096\n"
    "ds_read_b64_tr_b16 a[200:201], v32 offset:0\n"
    "ds_read_b64_tr_b16 a[202:203], v32 offset:4096\n"
    "ds_read_b64_tr_b16 a[224:225], v33 offset:0\n"
    "ds_read_b64_tr_b16 a[226:227], v33 offset:4096\n"
    "s_waitcnt lgkmcnt(0)\n"
    "s_barrier\n"
    "s_mov_b32 s49, 204800\n"
    "s_add_u32 m0, s42, 98304\n"
    "s_nop 0\n"
    "buffer_load_dwordx4 v12, s[4:7], s49 offen offset:0 nt lds\n"
    "buffer_load_dwordx4 v13, s[4:7], s49 offen offset:1024 nt lds\n"
    "buffer_load_dwordx4 v14, s[4:7], s49 offen offset:2048 nt lds\n"
    "buffer_load_dwordx4 v15, s[4:7], s49 offen offset:3072 nt lds\n"
    "s_add_u32 m0, s43, 131072\n"
    "s_nop 0\n"
    "buffer_load_dwordx4 v16, s[4:7], s49 offen nt lds\n"
    "s_mov_b32 s49, 225280\n"
    "s_add_u32 m0, s42, 102400\n"
    "s_nop 0\n"
    "buffer_load_dwordx4 v12, s[4:7], s49 offen offset:0 nt lds\n"
    "buffer_load_dwordx4 v13, s[4:7], s49 offen offset:1024 nt lds\n"
    "buffer_load_dwordx4 v14, s[4:7], s49 offen offset:2048 nt lds\n"
    "buffer_load_dwordx4 v15, s[4:7], s49 offen offset:3072 nt lds\n"
    "s_add_u32 m0, s43, 135168\n"
    "s_nop 0\n"
    "buffer_load_dwordx4 v16, s[4:7], s49 offen nt lds\n"
    "s_waitcnt vmcnt(21)\n"
    "s_barrier\n"
    "v_add_u32 v24, 0, v17\n"
    "v_add_u32 v32, 0, v18\n"
    "v_xor_b32 v33, 32, v32\n"
    "v_xor_b32 v25, 32, v24\n"
    "v_xor_b32 v26, 64, v24\n"
    "v_xor_b32 v27, 96, v24\n"
    "v_xor_b32 v28, 128, v24\n"
    "v_xor_b32 v29, 160, v24\n"
    "v_xor_b32 v30, 192, v24\n"
    "v_xor_b32 v31, 224, v24\n"
    "ds_read_b64_tr_b16 a[12:13], v24 offset:0\n"
    "ds_read_b64_tr_b16 a[14:15], v24 offset:4096\n"
    "ds_read_b64_tr_b16 a[36:37], v25 offset:0\n"
    "ds_read_b64_tr_b16 a[38:39], v25 offset:4096\n"
    "ds_read_b64_tr_b16 a[60:61], v26 offset:0\n"
    "ds_read_b64_tr_b16 a[62:63], v26 offset:4096\n"
    "ds_read_b64_tr_b16 a[84:85], v27 offset:0\n"
    "ds_read_b64_tr_b16 a[86:87], v27 offset:4096\n"
    "ds_read_b64_tr_b16 a[108:109], v28 offset:0\n"
    "ds_read_b64_tr_b16 a[110:111], v28 offset:4096\n"
    "ds_read_b64_tr_b16 a[132:133], v29 offset:0\n"
    "ds_read_b64_tr_b16 a[134:135], v29 offset:4096\n"
    "ds_read_b64_tr_b16 a[156:157], v30 offset:0\n"
    "ds_read_b64_tr_b16 a[158:159], v30 offset:4096\n"
    "ds_read_b64_tr_b16 a[180:181], v31 offset:0\n"
    "ds_read_b64_tr_b16 a[182:183], v31 offset:4096\n"
    "ds_read_b64_tr_b16 a[204:205], v32 offset:0\n"
    "ds_read_b64_tr_b16 a[206:207], v32 offset:4096\n"
    "ds_read_b64_tr_b16 a[228:229], v33 offset:0\n"
    "ds_read_b64_tr_b16 a[230:231], v33 offset:4096\n"
    "s_waitcnt lgkmcnt(0)\n"
    "s_barrier\n"
    "s_mov_b32 s49, 0\n"
    "s_add_u32 m0, s41, 0\n"
    "s_nop 0\n"
    "buffer_load_dwordx4 v0, s[8:11], s49 offen offset:0 nt lds\n"
    "buffer_load_dwordx4 v1, s[8:11], s49 offen offset:1024 nt lds\n"
    "buffer_load_dwordx4 v2, s[8:11], s49 offen offset:2048 nt lds\n"
    "s_mov_b32 s49, 1572864\n"
    "s_add_u32 m0, s41, 12288\n"
    "s_nop 0\n"
    "buffer_load_dwordx4 v0, s[8:11], s49 offen offset:0 nt lds\n"
    "buffer_load_dwordx4 v1, s[8:11], s49 offen offset:1024 nt lds\n"
    "buffer_load_dwordx4 v2, s[8:11], s49 offen offset:2048 nt lds\n"
    "s_mov_b32 s49, 3145728\n"
    "s_add_u32 m0, s41, 24576\n"
    "s_nop 0\n"
    "buffer_load_dwordx4 v0, s[8:11], s49 offen offset:0 nt lds\n"
    "buffer_load_dwordx4 v1, s[8:11], s49 offen offset:1024 nt lds\n"
    "buffer_load_dwordx4 v2, s[8:11], s49 offen offset:2048 nt lds\n"
    "s_mov_b32 s49, 4718592\n"
    "s_add_u32 m0, s41, 36864\n"
    "s_nop 0\n"
    "buffer_load_dwordx4 v0, s[8:11], s49 offen offset:0 nt lds\n"
    "buffer_load_dwordx4 v1, s[8:11], s49 offen offset:1024 nt lds\n"
    "buffer_load_dwordx4 v2, s[8:11], s49 offen offset:2048 nt lds\n"
    "s_waitcnt vmcnt(23)\n"
    "s_barrier\n"
    "v_add_u32 v24, 49152, v17\n"
    "v_add_u32 v32, 49152, v18\n"
    "v_xor_b32 v33, 32, v32\n"
    "v_xor_b32 v25, 32, v24\n"
    "v_xor_b32 v26, 64, v24\n"
    "v_xor_b32 v27, 96, v24\n"
    "v_xor_b32 v28, 128, v24\n"
    "v_xor_b32 v29, 160, v24\n"
    "v_xor_b32 v30, 192, v24\n"
    "v_xor_b32 v31, 224, v24\n"
    "ds_read_b64_tr_b16 a[16:17], v24 offset:0\n"
    "ds_read_b64_tr_b16 a[18:19], v24 offset:4096\n"
    "ds_read_b64_tr_b16 a[40:41], v25 offset:0\n"
    "ds_read_b64_tr_b16 a[42:43], v25 offset:4096\n"
    "ds_read_b64_tr_b16 a[64:65], v26 offset:0\n"
    "ds_read_b64_tr_b16 a[66:67], v26 offset:4096\n"
    "ds_read_b64_tr_b16 a[88:89], v27 offset:0\n"
    "ds_read_b64_tr_b16 a[90:91], v27 offset:4096\n"
    "ds_read_b64_tr_b16 a[112:113], v28 offset:0\n"
    "ds_read_b64_tr_b16 a[114:115], v28 offset:4096\n"
    "ds_read_b64_tr_b16 a[136:137], v29 offset:0\n"
    "ds_read_b64_tr_b16 a[138:139], v29 offset:4096\n"
    "ds_read_b64_tr_b16 a[160:161], v30 offset:0\n"
    "ds_read_b64_tr_b16 a[162:163], v30 offset:4096\n"
    "ds_read_b64_tr_b16 a[184:185], v31 offset:0\n"
    "ds_read_b64_tr_b16 a[186:187], v31 offset:4096\n"
    "ds_read_b64_tr_b16 a[208:209], v32 offset:0\n"
    "ds_read_b64_tr_b16 a[210:211], v32 offset:4096\n"
    "ds_read_b64_tr_b16 a[232:233], v33 offset:0\n"
    "ds_read_b64_tr_b16 a[234:235], v33 offset:4096\n"
    "s_waitcnt lgkmcnt(0)\n"
    "s_barrier\n"
    "s_mov_b32 s49, 393216\n"
    "s_add_u32 m0, s41, 49152\n"
    "s_nop 0\n"
    "buffer_load_dwordx4 v0, s[8:11], s49 offen offset:0 nt lds\n"
    "buffer_load_dwordx4 v1, s[8:11], s49 offen offset:1024 nt lds\n"
    "buffer_load_dwordx4 v2, s[8:11], s49 offen offset:2048 nt lds\n"
    "s_mov_b32 s49, 1966080\n"
    "s_add_u32 m0, s41, 61440\n"
    "s_nop 0\n"
    "buffer_load_dwordx4 v0, s[8:11], s49 offen offset:0 nt lds\n"
    "buffer_load_dwordx4 v1, s[8:11], s49 offen offset:1024 nt lds\n"
    "buffer_load_dwordx4 v2, s[8:11], s49 offen offset:2048 nt lds\n"
    "s_mov_b32 s49, 3538944\n"
    "s_add_u32 m0, s41, 73728\n"
    "s_nop 0\n"
    "buffer_load_dwordx4 v0, s[8:11], s49 offen offset:0 nt lds\n"
    "buffer_load_dwordx4 v1, s[8:11], s49 offen offset:1024 nt lds\n"
    "buffer_load_dwordx4 v2, s[8:11], s49 offen offset:2048 nt lds\n"
    "s_mov_b32 s49, 5111808\n"
    "s_add_u32 m0, s41, 86016\n"
    "s_nop 0\n"
    "buffer_load_dwordx4 v0, s[8:11], s49 offen offset:0 nt lds\n"
    "buffer_load_dwordx4 v1, s[8:11], s49 offen offset:1024 nt lds\n"
    "buffer_load_dwordx4 v2, s[8:11], s49 offen offset:2048 nt lds\n"
    "s_waitcnt vmcnt(24)\n"
    "s_barrier\n"
    "v_add_u32 v24, 98304, v17\n"
    "v_add_u32 v32, 98304, v18\n"
    "v_xor_b32 v33, 32, v32\n"
    "v_xor_b32 v25, 32, v24\n"
    "v_xor_b32 v26, 64, v24\n"
    "v_xor_b32 v27, 96, v24\n"
    "v_xor_b32 v28, 128, v24\n"
    "v_xor_b32 v29, 160, v24\n"
    "v_xor_b32 v30, 192, v24\n"
    "v_xor_b32 v31, 224, v24\n"
    "ds_read_b64_tr_b16 a[20:21], v24 offset:0\n"
    "ds_read_b64_tr_b16 a[22:23], v24 offset:4096\n"
    "ds_read_b64_tr_b16 a[44:45], v25 offset:0\n"
    "ds_read_b64_tr_b16 a[46:47], v25 offset:4096\n"
    "ds_read_b64_tr_b16 a[68:69], v26 offset:0\n"
    "ds_read_b64_tr_b16 a[70:71], v26 offset:4096\n"
    "ds_read_b64_tr_b16 a[92:93], v27 offset:0\n"
    "ds_read_b64_tr_b16 a[94:95], v27 offset:4096\n"
    "ds_read_b64_tr_b16 a[116:117], v28 offset:0\n"
    "ds_read_b64_tr_b16 a[118:119], v28 offset:4096\n"
    "ds_read_b64_tr_b16 a[140:141], v29 offset:0\n"
    "ds_read_b64_tr_b16 a[142:143], v29 offset:4096\n"
    "ds_read_b64_tr_b16 a[164:165], v30 offset:0\n"
    "ds_read_b64_tr_b16 a[166:167], v30 offset:4096\n"
    "ds_read_b64_tr_b16 a[188:189], v31 offset:0\n"
    "ds_read_b64_tr_b16 a[190:191], v31 offset:4096\n"
    "ds_read_b64_tr_b16 a[212:213], v32 offset:0\n"
    "ds_read_b64_tr_b16 a[214:215], v32 offset:4096\n"
    "ds_read_b64_tr_b16 a[236:237], v33 offset:0\n"
    "ds_read_b64_tr_b16 a[238:239], v33 offset:4096\n"
    "s_waitcnt lgkmcnt(0)\n"
    "s_barrier\n"
    "s_mov_b32 s49, 786432\n"
    "s_add_u32 m0, s41, 98304\n"
    "s_nop 0\n"
    "buffer_load_dwordx4 v0, s[8:11], s49 offen offset:0 nt lds\n"
    "buffer_load_dwordx4 v1, s[8:11], s49 offen offset:1024 nt lds\n"
    "buffer_load_dwordx4 v2, s[8:11], s49 offen offset:2048 nt lds\n"
    "s_mov_b32 s49, 2359296\n"
    "s_add_u32 m0, s41, 110592\n"
    "s_nop 0\n"
    "buffer_load_dwordx4 v0, s[8:11], s49 offen offset:0 nt lds\n"
    "buffer_load_dwordx4 v1, s[8:11], s49 offen offset:1024 nt lds\n"
    "buffer_load_dwordx4 v2, s[8:11], s49 offen offset:2048 nt lds\n"
    "s_mov_b32 s49, 3932160\n"
    "s_add_u32 m0, s41, 122880\n"
    "s_nop 0\n"
    "buffer_load_dwordx4 v0, s[8:11], s49 offen offset:0 nt lds\n"
    "buffer_load_dwordx4 v1, s[8:11], s49 offen offset:1024 nt lds\n"
    "buffer_load_dwordx4 v2, s[8:11], s49 offen offset:2048 nt lds\n"
    "s_mov_b32 s49, 5505024\n"
    "s_add_u32 m0, s41, 135168\n"
    "s_nop 0\n"
    "buffer_load_dwordx4 v0, s[8:11], s49 offen offset:0 nt lds\n"
    "buffer_load_dwordx4 v1, s[8:11], s49 offen offset:1024 nt lds\n"
    "buffer_load_dwordx4 v2, s[8:11], s49 offen offset:2048 nt lds\n"
    "s_waitcnt vmcnt(24)\n"
    "s_barrier\n"
    "v_add_u32 v8, 0, v3\n"
    "ds_read_b64_tr_b16 a[240:241], v8 offset:0\n"
    "ds_read_b64_tr_b16 a[242:243], v8 offset:768\n"
    "ds_read_b64_tr_b16 v[176:177], v8 offset:128\n"
    "ds_read_b64_tr_b16 v[178:179], v8 offset:896\n"
    "ds_read_b64_tr_b16 v[12:13], v8 offset:256\n"
    "ds_read_b64_tr_b16 v[14:15], v8 offset:1024\n"
    "v_add_u32 v9, 0, v4\n"
    "ds_read_b64_tr_b16 v[112:113], v9 offset:0\n"
    "ds_read_b64_tr_b16 v[114:115], v9 offset:768\n"
    "ds_read_b64_tr_b16 v[192:193], v9 offset:128\n"
    "ds_read_b64_tr_b16 v[194:195], v9 offset:896\n"
    "ds_read_b64_tr_b16 v[28:29], v9 offset:256\n"
    "ds_read_b64_tr_b16 v[30:31], v9 offset:1024\n"
    "v_add_u32 v10, 0, v5\n"
    "ds_read_b64_tr_b16 v[128:129], v10 offset:0\n"
    "ds_read_b64_tr_b16 v[130:131], v10 offset:768\n"
    "ds_read_b64_tr_b16 v[208:209], v10 offset:128\n"
    "ds_read_b64_tr_b16 v[210:211], v10 offset:896\n"
    "ds_read_b64_tr_b16 v[44:45], v10 offset:256\n"
    "ds_read_b64_tr_b16 v[46:47], v10 offset:1024\n"
    "v_add_u32 v11, 0, v6\n"
    "ds_read_b64_tr_b16 v[144:145], v11 offset:0\n"
    "ds_read_b64_tr_b16 v[146:147], v11 offset:768\n"
    "ds_read_b64_tr_b16 v[224:225], v11 offset:128\n"
    "ds_read_b64_tr_b16 v[226:227], v11 offset:896\n"
    "ds_read_b64_tr_b16 v[60:61], v11 offset:256\n"
    "ds_read_b64_tr_b16 v[62:63], v11 offset:1024\n"
    "s_waitcnt lgkmcnt(0)\n"
    "s_barrier\n"
    "s_mov_b32 s49, 1179648\n"
    "s_add_u32 m0, s41, 0\n"
    "s_nop 0\n"
    "buffer_load_dwordx4 v0, s[8:11], s49 offen offset:0 nt lds\n"
    "buffer_load_dwordx4 v1, s[8:11], s49 offen offset:1024 nt lds\n"
    "buffer_load_dwordx4 v2, s[8:11], s49 offen offset:2048 nt lds\n"
    "s_mov_b32 s49, 2752512\n"
    "s_add_u32 m0, s41, 12288\n"
    "s_nop 0\n"
    "buffer_load_dwordx4 v0, s[8:11], s49 offen offset:0 nt lds\n"
    "buffer_load_dwordx4 v1, s[8:11], s49 offen offset:1024 nt lds\n"
    "buffer_load_dwordx4 v2, s[8:11], s49 offen offset:2048 nt lds\n"
    "s_mov_b32 s49, 4325376\n"
    "s_add_u32 m0, s41, 24576\n"
    "s_nop 0\n"
    "buffer_load_dwordx4 v0, s[8:11], s49 offen offset:0 nt lds\n"
    "buffer_load_dwordx4 v1, s[8:11], s49 offen offset:1024 nt lds\n"
    "buffer_load_dwordx4 v2, s[8:11], s49 offen offset:2048 nt lds\n"
    "s_mov_b32 s49, 5898240\n"
    "s_add_u32 m0, s41, 36864\n"
    "s_nop 0\n"
    "buffer_load_dwordx4 v0, s[8:11], s49 offen offset:0 nt lds\n"
    "buffer_load_dwordx4 v1, s[8:11], s49 offen offset:1024 nt lds\n"
    "buffer_load_dwordx4 v2, s[8:11], s49 offen offset:2048 nt lds\n"
    "s_waitcnt vmcnt(24)\n"
    "s_barrier\n"
    "v_add_u32 v8, 49152, v3\n"
    "ds_read_b64_tr_b16 a[244:245], v8 offset:0\n"
    "ds_read_b64_tr_b16 a[246:247], v8 offset:768\n"
    "ds_read_b64_tr_b16 v[180:181], v8 offset:128\n"
    "ds_read_b64_tr_b16 v[182:183], v8 offset:896\n"
    "ds_read_b64_tr_b16 v[16:17], v8 offset:256\n"
    "ds_read_b64_tr_b16 v[18:19], v8 offset:1024\n"
    "v_add_u32 v9, 49152, v4\n"
    "ds_read_b64_tr_b16 v[116:117], v9 offset:0\n"
    "ds_read_b64_tr_b16 v[118:119], v9 offset:768\n"
    "ds_read_b64_tr_b16 v[196:197], v9 offset:128\n"
    "ds_read_b64_tr_b16 v[198:199], v9 offset:896\n"
    "ds_read_b64_tr_b16 v[32:33], v9 offset:256\n"
    "ds_read_b64_tr_b16 v[34:35], v9 offset:1024\n"
    "v_add_u32 v10, 49152, v5\n"
    "ds_read_b64_tr_b16 v[132:133], v10 offset:0\n"
    "ds_read_b64_tr_b16 v[134:135], v10 offset:768\n"
    "ds_read_b64_tr_b16 v[212:213], v10 offset:128\n"
    "ds_read_b64_tr_b16 v[214:215], v10 offset:896\n"
    "ds_read_b64_tr_b16 v[48:49], v10 offset:256\n"
    "ds_read_b64_tr_b16 v[50:51], v10 offset:1024\n"
    "v_add_u32 v11, 49152, v6\n"
    "ds_read_b64_tr_b16 v[148:149], v11 offset:0\n"
    "ds_read_b64_tr_b16 v[150:151], v11 offset:768\n"
    "ds_read_b64_tr_b16 v[228:229], v11 offset:128\n"
    "ds_read_b64_tr_b16 v[230:231], v11 offset:896\n"
    "ds_read_b64_tr_b16 v[64:65], v11 offset:256\n"
    "ds_read_b64_tr_b16 v[66:67], v11 offset:1024\n"
    "s_waitcnt lgkmcnt(0)\n"
    "s_barrier\n"
    "s_waitcnt vmcnt(58)\n"
    "v_cmp_gt_i32 vcc, 32, v109\n"
    "s_nop 0\n"
    "s_cbranch_vccnz .Lvo_tk0_%=\n"
    ".Lvo_tk1_%=:\n"
    "s_lshl_b32 s48, s12, 11\n"
    "s_add_u32 s48, s48, 53248\n"
    "v_lshrrev_b32 v108, 2, v96\n"
    "s_mov_b32 s50, 0\n"
    "s_mov_b32 s51, s14\n"
    "s_add_u32 s43, s18, s13\n"
    "v_mov_b32 v111, s50\n"
    "s_mov_b64 exec, 0x1f0\n"
    "v_mov_b32 v111, s51\n"
    "s_mov_b64 exec, -1\n"
    "buffer_load_dword v109, v108, s[0:3], s24 offen sc1\n"
    "buffer_load_dword v110, v105, s[0:3], 0 offen sc1\n"
    "s_mov_b32 s49, 6291456\n"
    "s_add_u32 m0, s41, 49152\n"
    "s_nop 0\n"
    "buffer_load_dwordx4 v0, s[8:11], s49 offen offset:0 nt lds\n"
    "buffer_load_dwordx4 v1, s[8:11], s49 offen offset:1024 nt lds\n"
    "buffer_load_dwordx4 v2, s[8:11], s49 offen offset:2048 nt lds\n"
    "s_mov_b32 s49, 6684672\n"
    "s_add_u32 m0, s41, 61440\n"
    "s_nop 0\n"
    "buffer_load_dwordx4 v0, s[8:11], s49 offen offset:0 nt lds\n"
    "buffer_load_dwordx4 v1, s[8:11], s49 offen offset:1024 nt lds\n"
    "buffer_load_dwordx4 v2, s[8:11], s49 offen offset:2048 nt lds\n"
    "s_mov_b32 s49, 7077888\n"
    "s_add_u32 m0, s41, 73728\n"
    "s_nop 0\n"
    "buffer_load_dwordx4 v0, s[8:11], s49 offen offset:0 nt lds\n"
    "buffer_load_dwordx4 v1, s[8:11], s49 offen offset:1024 nt lds\n"
    "buffer_load_dwordx4 v2, s[8:11], s49 offen offset:2048 nt lds\n"
    "s_mov_b32 s49, 7471104\n"
    "s_add_u32 m0, s41, 86016\n"
    "s_nop 0\n"
    "buffer_load_dwordx4 v0, s[8:11], s49 offen offset:0 nt lds\n"
    "buffer_load_dwordx4 v1, s[8:11], s49 offen offset:1024 nt lds\n"
    "buffer_load_dwordx4 v2, s[8:11], s49 offen offset:2048 nt lds\n"
    "s_waitcnt vmcnt(26)\n"
    "s_barrier\n"
    "v_add_u32 v8, 98304, v3\n"
    "ds_read_b64_tr_b16 a[248:249], v8 offset:0\n"
    "ds_read_b64_tr_b16 a[250:251], v8 offset:768\n"
    "ds_read_b64_tr_b16 v[184:185], v8 offset:128\n"
    "ds_read_b64_tr_b16 v[186:187], v8 offset:896\n"
    "ds_read_b64_tr_b16 v[20:21], v8 offset:256\n"
    "ds_read_b64_tr_b16 v[22:23], v8 offset:1024\n"
    "v_add_u32 v9, 98304, v4\n"
    "ds_read_b64_tr_b16 v[120:121], v9 offset:0\n"
    "ds_read_b64_tr_b16 v[122:123], v9 offset:768\n"
    "ds_read_b64_tr_b16 v[200:201], v9 offset:128\n"
    "ds_read_b64_tr_b16 v[202:203], v9 offset:896\n"
    "ds_read_b64_tr_b16 v[36:37], v9 offset:256\n"
    "ds_read_b64_tr_b16 v[38:39], v9 offset:1024\n"
    "v_add_u32 v10, 98304, v5\n"
    "ds_read_b64_tr_b16 v[136:137], v10 offset:0\n"
    "ds_read_b64_tr_b16 v[138:139], v10 offset:768\n"
    "ds_read_b64_tr_b16 v[216:217], v10 offset:128\n"
    "ds_read_b64_tr_b16 v[218:219], v10 offset:896\n"
    "ds_read_b64_tr_b16 v[52:53], v10 offset:256\n"
    "ds_read_b64_tr_b16 v[54:55], v10 offset:1024\n"
    "v_add_u32 v11, 98304, v6\n"
    "ds_read_b64_tr_b16 v[152:153], v11 offset:0\n"
    "ds_read_b64_tr_b16 v[154:155], v11 offset:768\n"
    "ds_read_b64_tr_b16 v[232:233], v11 offset:128\n"
    "ds_read_b64_tr_b16 v[234:235], v11 offset:896\n"
    "ds_read_b64_tr_b16 v[68:69], v11 offset:256\n"
    "ds_read_b64_tr_b16 v[70:71], v11 offset:1024\n"
    "s_waitcnt lgkmcnt(0)\n"
    "s_barrier\n"
    "s_waitcnt vmcnt(14)\n"
    "s_barrier\n"
    "v_add_u32 v8, 0, v3\n"
    "ds_read_b64_tr_b16 a[252:253], v8 offset:0\n"
    "ds_read_b64_tr_b16 a[254:255], v8 offset:768\n"
    "ds_read_b64_tr_b16 v[188:189], v8 offset:128\n"
    "ds_read_b64_tr_b16 v[190:191], v8 offset:896\n"
    "ds_read_b64_tr_b16 v[24:25], v8 offset:256\n"
    "ds_read_b64_tr_b16 v[26:27], v8 offset:1024\n"
    "v_add_u32 v9, 0, v4\n"
    "ds_read_b64_tr_b16 v[124:125], v9 offset:0\n"
    "ds_read_b64_tr_b16 v[126:127], v9 offset:768\n"
    "ds_read_b64_tr_b16 v[204:205], v9 offset:128\n"
    "ds_read_b64_tr_b16 v[206:207], v9 offset:896\n"
    "ds_read_b64_tr_b16 v[40:41], v9 offset:256\n"
    "ds_read_b64_tr_b16 v[42:43], v9 offset:1024\n"
    "v_add_u32 v10, 0, v5\n"
    "ds_read_b64_tr_b16 v[140:141], v10 offset:0\n"
    "ds_read_b64_tr_b16 v[142:143], v10 offset:768\n"
    "ds_read_b64_tr_b16 v[220:221], v10 offset:128\n"
    "ds_read_b64_tr_b16 v[222:223], v10 offset:896\n"
    "ds_read_b64_tr_b16 v[56:57], v10 offset:256\n"
    "ds_read_b64_tr_b16 v[58:59], v10 offset:1024\n"
    "v_add_u32 v11, 0, v6\n"
    "ds_read_b64_tr_b16 v[156:157], v11 offset:0\n"
    "ds_read_b64_tr_b16 v[158:159], v11 offset:768\n"
    "ds_read_b64_tr_b16 v[236:237], v11 offset:128\n"
    "ds_read_b64_tr_b16 v[238:239], v11 offset:896\n"
    "ds_read_b64_tr_b16 v[72:73], v11 offset:256\n"
    "ds_read_b64_tr_b16 v[74:75], v11 offset:1024\n"
    "s_waitcnt lgkmcnt(0)\n"
    "s_barrier\n"
    "s_waitcnt vmcnt(12)\n"
    "v_cmp_gt_i32 vcc, 1, v109\n"
    "s_nop 0\n"
    "s_cbranch_vccnz .Lvo_e0_%=\n"
    "v_cmp_gt_i32 vcc, v111, v110\n"
    "s_nop 0\n"
    "s_cbranch_vccnz .Lvo_e0_%=\n"
    ".Lvo_e1_%=:\n"
    "s_sub_u32 m0, s17, 8192\n"
    "s_nop 0\n"
    "buffer_load_dwordx4 v96, s[0:3], s43 offen offset:0 sc1 lds\n"
    "buffer_load_dwordx4 v96, s[0:3], s43 offen offset:1024 sc1 lds\n"
    "buffer_load_dwordx4 v96, s[0:3], s43 offen offset:2048 sc1 lds\n"
    "buffer_load_dwordx4 v96, s[0:3], s43 offen offset:3072 sc1 lds\n"
    "s_sub_u32 m0, s17, 4096\n"
    "s_add_u32 s43, s43, 4096\n"
    "buffer_load_dwordx4 v96, s[0:3], s43 offen offset:0 sc1 lds\n"
    "buffer_load_dwordx4 v96, s[0:3], s43 offen offset:1024 sc1 lds\n"
    "buffer_load_dwordx4 v96, s[0:3], s43 offen offset:2048 sc1 lds\n"
    "buffer_load_dwordx4 v96, s[0:3], s43 offen offset:3072 sc1 lds\n"
    "s_waitcnt vmcnt(8)\n"
    "s_barrier\n"
    "v_add_u32 v8, 49152, v7\n"
    "ds_read_b64_tr_b16 v[160:161], v8 offset:0\n"
    "ds_read_b64_tr_b16 v[162:163], v8 offset:768\n"
    "ds_read_b64_tr_b16 v[240:241], v8 offset:128\n"
    "ds_read_b64_tr_b16 v[242:243], v8 offset:896\n"
    "ds_read_b64_tr_b16 v[76:77], v8 offset:256\n"
    "ds_read_b64_tr_b16 v[78:79], v8 offset:1024\n"
    "v_add_u32 v9, 61440, v7\n"
    "ds_read_b64_tr_b16 v[164:165], v9 offset:0\n"
    "ds_read_b64_tr_b16 v[166:167], v9 offset:768\n"
    "ds_read_b64_tr_b16 v[244:245], v9 offset:128\n"
    "ds_read_b64_tr_b16 v[246:247], v9 offset:896\n"
    "ds_read_b64_tr_b16 v[80:81], v9 offset:256\n"
    "ds_read_b64_tr_b16 v[82:83], v9 offset:1024\n"
    "v_add_u32 v10, 73728, v7\n"
    "ds_read_b64_tr_b16 v[168:169], v10 offset:0\n"
    "ds_read_b64_tr_b16 v[170:171], v10 offset:768\n"
    "ds_read_b64_tr_b16 v[248:249], v10 offset:128\n"
    "ds_read_b64_tr_b16 v[250:251], v10 offset:896\n"
    "ds_read_b64_tr_b16 v[84:85], v10 offset:256\n"
    "ds_read_b64_tr_b16 v[86:87], v10 offset:1024\n"
    "v_add_u32 v11, 86016, v7\n"
    "ds_read_b64_tr_b16 v[172:173], v11 offset:0\n"
    "ds_read_b64_tr_b16 v[174:175], v11 offset:768\n"
    "ds_read_b64_tr_b16 v[252:253], v11 offset:128\n"
    "ds_read_b64_tr_b16 v[254:255], v11 offset:896\n"
    "ds_read_b64_tr_b16 v[88:89], v11 offset:256\n"
    "ds_read_b64_tr_b16 v[90:91], v11 offset:1024\n"
    "s_waitcnt lgkmcnt(0)\n"
    "s_barrier\n"
    "s_mov_b32 m0, s48\n"
    "s_add_u32 s43, s43, s19\n"
    "buffer_load_dwordx4 v96, s[0:3], s43 offen offset:0 sc1 lds\n"
    "buffer_load_dwordx4 v96, s[0:3], s43 offen offset:1024 sc1 lds\n"
    "ds_write_b128 v97, v[12:15] offset:0\n"
    "ds_write_b128 v97, v[16:19] offset:1024\n"
    "ds_write_b128 v97, v[20:23] offset:2048\n"
    "ds_write_b128 v97, v[24:27] offset:3072\n"
    "ds_write_b128 v97, v[28:31] offset:4096\n"
    "ds_write_b128 v97, v[32:35] offset:5120\n"
    "ds_write_b128 v97, v[36:39] offset:6144\n"
    "ds_write_b128 v97, v[40:43] offset:7168\n"
    "ds_write_b128 v97, v[44:47] offset:8192\n"
    "ds_write_b128 v97, v[48:51] offset:9216\n"
    "ds_write_b128 v97, v[52:55] offset:10240\n"
    "ds_write_b128 v97, v[56:59] offset:11264\n"
    "ds_write_b128 v97, v[60:63] offset:12288\n"
    "ds_write_b128 v97, v[64:67] offset:13312\n"
    "ds_write_b128 v97, v[68:71] offset:14336\n"
    "ds_write_b128 v97, v[72:75] offset:15360\n"
    "ds_write_b128 v97, v[76:79] offset:16384\n"
    "ds_write_b128 v97, v[80:83] offset:17408\n"
    "ds_write_b128 v97, v[84:87] offset:18432\n"
    "ds_write_b128 v97, v[88:91] offset:19456\n"
    "s_mov_b32 s34, -4\n"
    "s_memrealtime s[30:31]\n"
    "s_waitcnt vmcnt(0) lgkmcnt(0)\n"
    "s_barrier\n"
    "ds_read_b128 v[24:27], v92 offset:0\n"
    "ds_read_b128 v[28:31], v92 offset:1024\n"
    "ds_read_b128 v[32:35], v93 offset:0\n"
    "ds_read_b128 v[36:39], v93 offset:1024\n"
    "ds_read_b128 v[40:43], v94 offset:0\n"
    "ds_read_b128 v[44:47], v94 offset:1024\n"
    "s_sub_u32 s46, s30, 1320\n"
    "s_mov_b32 s34, -3\n"
    "s_mov_b32 s35, -5\n"
    "s_mov_b32 s45, s23\n"
    "s_mov_b32 s38, 2130706432\n"
    "s_mov_b32 s39, 2130706432\n"
    "s_mov_b32 s40, 2130706432\n"
    "s_waitcnt lgkmcnt(0)\n"
    "s_branch .Lvo_vph_%=\n"
    ".Lvo_take_%=:\n"
    "v_mfma_f32_16x16x32_bf16 v[84:87], v[248:251], v[24:27], v[84:87]\n"
    ".Lvo_mk8x5_%=:\n"
    "s_waitcnt vmcnt(3)\n"
    ".Lvo_mk0x6_%=:\n"
    "ds_read_b128 v[72:75], v92 offset:8192\n"
    "v_cvt_pk_bf16_f32 v40, v64, v65\n"
    "v_mfma_f32_16x16x32_bf16 v[84:87], v[252:255], v[32:35], v[84:87]\n"
    "s_waitcnt vmcnt(2)\n"
    "ds_read_b128 v[76:79], v92 offset:9216\n"
    "v_cvt_pk_bf16_f32 v41, v66, v67\n"
    "v_mfma_f32_16x16x32_bf16 v[68:71], v[164:167], v[44:47], v[68:71]\n"
    "v_mov_b64 v[64:65], 0\n"
    "v_mov_b64 v[66:67], 0\n"
    "v_mfma_f32_16x16x32_bf16 v[68:71], v[168:171], v[28:31], v[68:71]\n"
    "s_waitcnt lgkmcnt(2)\n"
    "ds_read_b128 v[24:27], v97 offset:6144\n"
    "v_cvt_pk_bf16_f32 v42, v84, v85\n"
    "v_mfma_f32_16x16x32_bf16 v[68:71], v[172:175], v[36:39], v[68:71]\n"
    "v_cvt_pk_bf16_f32 v43, v86, v87\n"
    "v_mov_b64 v[84:85], 0\n"
    "v_mfma_f32_16x16x32_bf16 v[88:91], v[244:247], v[44:47], v[88:91]\n"
    "ds_write_b128 v92, v[40:43] offset:11264\n"
    "v_mfma_f32_16x16x32_bf16 v[88:91], v[248:251], v[28:31], v[88:91]\n"
    "s_waitcnt lgkmcnt(2)\n"
    "v_max_u32 v108, v72, v74\n"
    "v_max3_u32 v108, v108, v76, v78\n"
    "v_mfma_f32_16x16x32_bf16 v[88:91], v[252:255], v[36:39], v[88:91]\n"
    "v_cmp_le_u32 vcc, s52, v108\n"
    "v_mov_b64 v[86:87], 0\n"
    "v_smfmac_f32_16x16x64_bf16 v[64:67], v[72:75], a[240:247], v98\n"
    "s_cbranch_vccnz .Lvo_retry_%=\n"
    ".Lvo_go_%=:\n"
    "s_barrier\n"
    ".Lvo_mk1x7_%=:\n"
    "ds_read_b128 v[32:35], v93 offset:8192\n"
    "v_cvt_pk_bf16_f32 v44, v68, v69\n"
    "v_smfmac_f32_16x16x64_bf16 v[64:67], v[76:79], a[248:255], v98\n"
    "ds_read_b128 v[36:39], v93 offset:9216\n"
    "v_cvt_pk_bf16_f32 v45, v70, v71\n"
    "v_smfmac_f32_16x16x64_bf16 v[84:87], v[72:75], v[0:7], v98\n"
    "s_waitcnt vmcnt(1)\n"
    "ds_read_b128 v[80:83], v92 offset:10240\n"
    "v_mov_b64 v[68:69], 0\n"
    "v_mov_b64 v[70:71], 0\n"
    "v_smfmac_f32_16x16x64_bf16 v[84:87], v[76:79], v[8:15], v98\n"
    "ds_read_b128 v[28:31], v97 offset:7168\n"
    "v_cvt_pk_bf16_f32 v46, v88, v89\n"
    "v_smfmac_f32_16x16x64_bf16 v[68:71], v[72:75], v[176:183], v98\n"
    "ds_read_b128 v[0:3], v97 offset:12288\n"
    "v_cvt_pk_bf16_f32 v47, v90, v91\n"
    "v_smfmac_f32_16x16x64_bf16 v[68:71], v[76:79], v[184:191], v98\n"
    "s_waitcnt lgkmcnt(4)\n"
    "ds_read_b128 v[88:91], v94 offset:8192\n"
    "ds_write_b128 v92, v[44:47] offset:12288\n"
    "v_smfmac_f32_16x16x64_bf16 v[84:87], v[32:35], v[16:23], v98\n"
    "ds_read_b128 v[40:43], v94 offset:9216\n"
    "v_smfmac_f32_16x16x64_bf16 v[64:67], v[32:35], v[112:119], v98\n"
    "s_waitcnt lgkmcnt(5)\n"
    "ds_read_b128 v[4:7], v97 offset:13312\n"
    "v_max_u32 v108, v80, v82\n"
    "v_smfmac_f32_16x16x64_bf16 v[68:71], v[32:35], v[192:199], v98\n"
    "s_waitcnt lgkmcnt(5)\n"
    "ds_read_b128 v[44:47], v95 offset:8192\n"
    "v_cmp_eq_u32 vcc, -1, v108\n"
    "v_smfmac_f32_16x16x64_bf16 v[84:87], v[36:39], v[24:31], v98\n"
    "ds_read_b128 v[8:11], v97 offset:14336\n"
    "ds_read_b128 v[12:15], v97 offset:15360\n"
    "v_smfmac_f32_16x16x64_bf16 v[64:67], v[36:39], v[120:127], v98\n"
    "ds_read_b128 v[16:19], v97 offset:16384\n"
    "v_smfmac_f32_16x16x64_bf16 v[68:71], v[36:39], v[200:207], v98\n"
    "s_waitcnt lgkmcnt(6)\n"
    "s_cbranch_vccnz .Lvo_r2_%=\n"
    ".Lvo_go2_%=:\n"
    "s_barrier\n"
    "ds_read_b128 v[72:75], v96 offset:10240\n"
    "v_smfmac_f32_16x16x64_bf16 v[84:87], v[88:91], v[48:55], v98\n"
    "s_mov_b32 m0, s21\n"
    "ds_read_b128 v[80:83], v95 offset:9216\n"
    "ds_read_b128 v[76:79], v96 offset:23552\n"
    "v_smfmac_f32_16x16x64_bf16 v[64:67], v[88:91], v[128:135], v98\n"
    "s_waitcnt vmcnt(0)\n"
    "ds_read_b128 v[20:23], v97 offset:17408\n"
    "v_cmp_gt_i32 vcc, v111, v110\n"
    "v_smfmac_f32_16x16x64_bf16 v[68:71], v[88:91], v[208:215], v98\n"
    "s_waitcnt lgkmcnt(7)\n"
    "s_cbranch_vccnz .Lvo_wf_%=\n"
    ".Lvo_wfb_%=:\n"
    "v_smfmac_f32_16x16x64_bf16 v[64:67], v[40:43], v[136:143], v98\n"
    "buffer_load_dwordx4 v96, s[0:3], s44 offen sc1 lds\n"
    "v_smfmac_f32_16x16x64_bf16 v[64:67], v[44:47], v[144:151], v98\n"
    "s_waitcnt lgkmcnt(3)\n"
    "ds_read_b128 v[24:27], v97 offset:18432\n"
    "v_mov_b64 v[32:33], 0\n"
    "v_smfmac_f32_16x16x64_bf16 v[64:67], v[72:75], v[160:167], v98\n"
    "s_waitcnt lgkmcnt(3)\n"
    "ds_read_b128 v[28:31], v97 offset:19456\n"
    "v_mov_b64 v[34:35], 0\n"
    "v_smfmac_f32_16x16x64_bf16 v[64:67], v[80:83], v[152:159], v98\n"
    "s_waitcnt lgkmcnt(3)\n"
    "v_mov_b64 v[36:37], 0\n"
    "v_smfmac_f32_16x16x64_bf16 v[64:67], v[76:79], v[168:175], v98\n"
    "s_sub_u32 m0, s17, 8192\n"
    "v_mov_b64 v[38:39], 0\n"
    "v_smfmac_f32_16x16x64_bf16 v[84:87], v[40:43], v[56:63], v98\n"
    "v_mov_b64 v[48:49], 0\n"
    "v_smfmac_f32_16x16x64_bf16 v[84:87], v[44:47], v[0:7], v98\n"
    "ds_read_b128 v[0:3], v96 offset:11264\n"
    "s_nop 0\n"
    "v_add_f32 v64, v64, v66\n"
    "v_smfmac_f32_16x16x64_bf16 v[84:87], v[80:83], v[8:15], v98\n"
    "s_waitcnt lgkmcnt(3)\n"
    "ds_read_b128 v[4:7], v96 offset:12288\n"
    "v_add_f32 v65, v65, v67\n"
    "v_smfmac_f32_16x16x64_bf16 v[84:87], v[72:75], v[16:23], v98\n"
    "s_waitcnt lgkmcnt(2)\n"
    "ds_read_b128 v[8:11], v96 offset:24576\n"
    "v_cvt_pk_bf16_f32 v64, v64, v65\n"
    "v_smfmac_f32_16x16x64_bf16 v[84:87], v[76:79], v[24:31], v98\n"
    "ds_write_b32 v99, v64 offset:0\n"
    "v_smfmac_f32_16x16x64_bf16 v[68:71], v[40:43], v[216:223], v98\n"
    "ds_read_b128 v[12:15], v96 offset:25600\n"
    "v_mov_b64 v[50:51], 0\n"
    "v_smfmac_f32_16x16x64_bf16 v[68:71], v[44:47], v[224:231], v98\n"
    "ds_read_b128 v[16:19], v96 offset:37888\n"
    "s_nop 0\n"
    "v_add_f32 v84, v84, v86\n"
    "v_smfmac_f32_16x16x64_bf16 v[68:71], v[80:83], v[232:239], v98\n"
    "ds_read_b128 v[20:23], v96 offset:38912\n"
    "v_add_f32 v85, v85, v87\n"
    "v_smfmac_f32_16x16x64_bf16 v[68:71], v[72:75], v[240:247], v98\n"
    "ds_read_b128 v[24:27], v96 offset:51200\n"
    "v_cvt_pk_bf16_f32 v84, v84, v85\n"
    "v_smfmac_f32_16x16x64_bf16 v[68:71], v[76:79], v[248:255], v98\n"
    "ds_write_b32 v100, v84 offset:0\n"
    "s_waitcnt lgkmcnt(7)\n"
    "v_mfma_f32_16x16x32_bf16 v[40:43], v[0:3], a[0:3], 0\n"
    "ds_read_b128 v[28:31], v96 offset:52224\n"
    "v_mov_b64 v[52:53], 0\n"
    "v_mfma_f32_16x16x32_bf16 v[44:47], v[4:7], a[0:3], 0\n"
    "s_nop 1\n"
    "v_add_f32 v68, v68, v70\n"
    "v_add_f32 v69, v69, v71\n"
    "v_cvt_pk_bf16_f32 v68, v68, v69\n"
    "ds_write_b32 v99, v68 offset:256\n"
    "s_waitcnt lgkmcnt(4)\n"
    "v_mfma_f32_16x16x32_bf16 v[40:43], v[8:11], a[4:7], v[40:43]\n"
    "v_mov_b64 v[54:55], 0\n"
    "v_mov_b64 v[56:57], 0\n"
    "v_mfma_f32_16x16x32_bf16 v[44:47], v[12:15], a[4:7], v[44:47]\n"
    "v_mov_b64 v[58:59], 0\n"
    "v_mov_b64 v[60:61], 0\n"
    "v_mfma_f32_16x16x32_bf16 v[40:43], v[16:19], a[8:11], v[40:43]\n"
    "v_mov_b64 v[62:63], 0\n"
    "v_mov_b64 v[64:65], 0\n"
    "v_mfma_f32_16x16x32_bf16 v[44:47], v[20:23], a[8:11], v[44:47]\n"
    "v_mov_b64 v[66:67], 0\n"
    "v_mov_b64 v[68:69], 0\n"
    "s_waitcnt lgkmcnt(0)\n"
    "s_barrier\n"
    ".Lvo_mk2x8_%=:\n"
    "ds_read_b64_tr_b16 v[72:73], v101 offset:0\n"
    "ds_read_b64_tr_b16 v[74:75], v101 offset:64\n"
    "v_mfma_f32_16x16x32_bf16 v[40:43], v[24:27], a[12:15], v[40:43]\n"
    "ds_read_b64_tr_b16 v[76:77], v101 offset:1024\n"
    "ds_read_b64_tr_b16 v[78:79], v101 offset:1088\n"
    "v_mfma_f32_16x16x32_bf16 v[44:47], v[28:31], a[12:15], v[44:47]\n"
    "ds_read_b64_tr_b16 v[80:81], v101 offset:2048\n"
    "ds_read_b64_tr_b16 v[82:83], v101 offset:2112\n"
    "v_mov_b64 v[70:71], 0\n"
    "v_mov_b64 v[84:85], 0\n"
    "v_mov_b64 v[86:87], 0\n"
    "v_mov_b64 v[88:89], 0\n"
    "v_mov_b64 v[90:91], 0\n"
    "s_waitcnt lgkmcnt(4)\n"
    "v_smfmac_f32_16x16x64_bf16 v[32:35], v[72:75], a[192:199], v98\n"
    "buffer_load_dwordx4 v96, s[0:3], s43 offen offset:0 sc1 lds\n"
    "s_waitcnt lgkmcnt(2)\n"
    "v_smfmac_f32_16x16x64_bf16 v[32:35], v[76:79], a[200:207], v98\n"
    "s_waitcnt lgkmcnt(0)\n"
    "v_smfmac_f32_16x16x64_bf16 v[32:35], v[80:83], a[208:215], v98\n"
    "v_smfmac_f32_16x16x64_bf16 v[36:39], v[72:75], a[216:223], v98\n"
    "buffer_load_dwordx4 v96, s[0:3], s43 offen offset:1024 sc1 lds\n"
    "v_smfmac_f32_16x16x64_bf16 v[36:39], v[76:79], a[224:231], v98\n"
    "s_nop 1\n"
    "v_add_f32 v32, v32, v34\n"
    "v_smfmac_f32_16x16x64_bf16 v[36:39], v[80:83], a[232:239], v98\n"
    "v_add_f32 v33, v33, v35\n"
    "v_smfmac_f32_16x16x64_bf16 v[48:51], v[72:75], a[0:7], v98\n"
    "v_cvt_pk_bf16_f32 v108, v32, v33\n"
    "v_smfmac_f32_16x16x64_bf16 v[48:51], v[76:79], a[8:15], v98\n"
    "s_nop 1\n"
    "v_add_f32 v36, v36, v38\n"
    "v_smfmac_f32_16x16x64_bf16 v[48:51], v[80:83], a[16:23], v98\n"
    "v_add_f32 v37, v37, v39\n"
    "v_smfmac_f32_16x16x64_bf16 v[52:55], v[72:75], a[24:31], v98\n"
    "v_cvt_pk_bf16_f32 v109, v36, v37\n"
    "v_smfmac_f32_16x16x64_bf16 v[52:55], v[76:79], a[32:39], v98\n"
    "s_nop 1\n"
    "v_add_f32 v48, v48, v50\n"
    "v_permlane16_swap_b32 v108, v109\n"
    "v_min_u32 v108, -2, v108\n"
    "v_smfmac_f32_16x16x64_bf16 v[52:55], v[80:83], a[40:47], v98\n"
    "buffer_store_dwordx2 v[108:109], v103, s[0:3], s36 offen sc1\n"
    "v_add_f32 v49, v49, v51\n"
    "v_smfmac_f32_16x16x64_bf16 v[56:59], v[72:75], a[48:55], v98\n"
    "v_cvt_pk_bf16_f32 v48, v48, v49\n"
    "v_smfmac_f32_16x16x64_bf16 v[56:59], v[76:79], a[56:63], v98\n"
    "s_nop 1\n"
    "v_add_f32 v52, v52, v54\n"
    "v_smfmac_f32_16x16x64_bf16 v[56:59], v[80:83], a[64:71], v98\n"
    "v_add_f32 v53, v53, v55\n"
    "v_smfmac_f32_16x16x64_bf16 v[60:63], v[72:75], a[72:79], v98\n"
    "v_cvt_pk_bf16_f32 v49, v52, v53\n"
    "v_smfmac_f32_16x16x64_bf16 v[60:63], v[76:79], a[80:87], v98\n"
    "s_waitcnt vmcnt(3)\n"
    "s_barrier\n"
    "v_add_f32 v56, v56, v58\n"
    "v_smfmac_f32_16x16x64_bf16 v[60:63], v[80:83], a[88:95], v98\n"
    "ds_read2_b64 v[32:35], v106 offset0:0 offset1:128\n"
    "v_add_f32 v57, v57, v59\n"
    "v_smfmac_f32_16x16x64_bf16 v[64:67], v[72:75], a[96:103], v98\n"
    "ds_read2_b64 v[36:39], v106 offset0:1 offset1:129\n"
    "v_cvt_pk_bf16_f32 v50, v56, v57\n"
    "v_smfmac_f32_16x16x64_bf16 v[64:67], v[76:79], a[104:111], v98\n"
    "s_nop 1\n"
    "v_add_f32 v60, v60, v62\n"
    "v_permlane32_swap_b32 v48, v50\n"
    "v_smfmac_f32_16x16x64_bf16 v[64:67], v[80:83], a[112:119], v98\n"
    "v_add_f32 v61, v61, v63\n"
    "v_smfmac_f32_16x16x64_bf16 v[68:71], v[72:75], a[120:127], v98\n"
    "v_cvt_pk_bf16_f32 v51, v60, v61\n"
    "v_smfmac_f32_16x16x64_bf16 v[68:71], v[76:79], a[128:135], v98\n"
    "s_nop 0\n"
    "v_permlane32_swap_b32 v49, v51\n"
    "v_smfmac_f32_16x16x64_bf16 v[68:71], v[80:83], a[136:143], v98\n"
    "s_nop 0\n"
    "v_permlane16_swap_b32 v50, v51\n"
    "v_permlane16_swap_b32 v48, v49\n"
    "v_min_u32 v48, -2, v48\n"
    "v_smfmac_f32_16x16x64_bf16 v[84:87], v[72:75], a[144:151], v98\n"
    "buffer_store_dwordx4 v[48:51], v102, s[0:3], s36 offen sc1\n"
    "v_add_f32 v64, v64, v66\n"
    "v_add_f32 v65, v65, v67\n"
    "v_cvt_pk_bf16_f32 v64, v64, v65\n"
    "v_smfmac_f32_16x16x64_bf16 v[84:87], v[76:79], a[152:159], v98\n"
    "v_add_f32 v68, v68, v70\n"
    "v_smfmac_f32_16x16x64_bf16 v[84:87], v[80:83], a[160:167], v98\n"
    "v_add_f32 v69, v69, v71\n"
    "v_smfmac_f32_16x16x64_bf16 v[88:91], v[72:75], a[168:175], v98\n"
    "v_cvt_pk_bf16_f32 v65, v68, v69\n"
    "v_smfmac_f32_16x16x64_bf16 v[88:91], v[76:79], a[176:183], v98\n"
    "s_nop 1\n"
    "v_add_f32 v84, v84, v86\n"
    "v_smfmac_f32_16x16x64_bf16 v[88:91], v[80:83], a[184:191], v98\n"
    ".Lvo_mk3x9_%=:\n"
    ".Lvo_uph_%=:\n"
    "s_waitcnt lgkmcnt(0)\n"
    "v_add_f32 v85, v85, v87\n"
    "v_mfma_f32_16x16x32_bf16 v[56:59], v[32:35], a[16:19], v[40:43]\n"
    "v_cvt_pk_bf16_f32 v66, v84, v85\n"
    "v_mfma_f32_16x16x32_bf16 v[60:63], v[36:39], a[16:19], v[44:47]\n"
    "s_nop 0\n"
    "v_permlane32_swap_b32 v64, v66\n"
    "v_add_f32 v88, v88, v90\n"
    "v_add_f32 v89, v89, v91\n"
    "v_cvt_pk_bf16_f32 v67, v88, v89\n"
    "s_nop 1\n"
    "v_permlane32_swap_b32 v65, v67\n"
    "s_nop 1\n"
    "v_permlane16_swap_b32 v66, v67\n"
    "v_permlane16_swap_b32 v64, v65\n"
    "v_min_u32 v64, -2, v64\n"
    "buffer_store_dwordx4 v[64:67], v102, s[0:3], s37 offen sc1\n"
    ".Lvo_mkum_%=:\n"
    "s_cmp_eq_u32 s34, 63\n"
    "s_cbranch_scc1 .Lvo_end_%=\n"
    "ds_read2_b64 v[40:43], v107 offset0:0 offset1:128\n"
    "ds_read2_b64 v[44:47], v107 offset0:1 offset1:129\n"
    "v_mfma_f32_16x16x32_bf16 v[48:51], v[0:3], a[24:27], 0\n"
    "v_mfma_f32_16x16x32_bf16 v[52:55], v[4:7], a[24:27], 0\n"
    ".Lvo_mkL0_%=:\n"
    "v_mfma_f32_16x16x32_bf16 v[48:51], v[8:11], a[28:31], v[48:51]\n"
    "v_mfma_f32_16x16x32_bf16 v[52:55], v[12:15], a[28:31], v[52:55]\n"
    "v_mfma_f32_16x16x32_bf16 v[48:51], v[16:19], a[32:35], v[48:51]\n"
    "v_mov_b64 v[88:89], -1\n"
    "v_mov_b64 v[90:91], -1\n"
    "v_mfma_f32_16x16x32_bf16 v[52:55], v[20:23], a[32:35], v[52:55]\n"
    "s_waitcnt lgkmcnt(0)\n"
    "v_mfma_f32_16x16x32_bf16 v[56:59], v[40:43], a[20:23], v[56:59]\n"
    "buffer_load_dwordx4 v96, s[0:3], s43 offen offset:2048 sc1 lds\n"
    "v_mfma_f32_16x16x32_bf16 v[60:63], v[44:47], a[20:23], v[60:63]\n"
    "v_mfma_f32_16x16x32_bf16 v[48:51], v[24:27], a[36:39], v[48:51]\n"
    "s_nop 1\n"
    "v_cvt_pk_bf16_f32 v72, v56, v57\n"
    "v_mfma_f32_16x16x32_bf16 v[52:55], v[28:31], a[36:39], v[52:55]\n"
    "v_cvt_pk_bf16_f32 v74, v58, v59\n"
    "v_cvt_pk_bf16_f32 v73, v60, v61\n"
    "v_mfma_f32_16x16x32_bf16 v[48:51], v[32:35], a[40:43], v[48:51]\n"
    "v_cvt_pk_bf16_f32 v75, v62, v63\n"
    "v_mfma_f32_16x16x32_bf16 v[52:55], v[36:39], a[40:43], v[52:55]\n"
    "buffer_load_dwordx4 v96, s[0:3], s43 offen offset:3072 sc1 lds\n"
    "v_mfma_f32_16x16x32_bf16 v[48:51], v[40:43], a[44:47], v[48:51]\n"
    "s_sub_u32 m0, s17, 4096\n"
    "s_add_u32 s43, s43, 4096\n"
    "v_mfma_f32_16x16x32_bf16 v[52:55], v[44:47], a[44:47], v[52:55]\n"
    "v_mfma_f32_16x16x32_bf16 v[56:59], v[0:3], a[48:51], 0\n"
    "s_nop 1\n"
    "v_cvt_pk_bf16_f32 v76, v48, v49\n"
    "v_mfma_f32_16x16x32_bf16 v[60:63], v[4:7], a[48:51], 0\n"
    "v_cvt_pk_bf16_f32 v78, v50, v51\n"
    "v_cvt_pk_bf16_f32 v77, v52, v53\n"
    "v_mfma_f32_16x16x32_bf16 v[56:59], v[8:11], a[52:55], v[56:59]\n"
    "v_cvt_pk_bf16_f32 v79, v54, v55\n"
    "v_mfma_f32_16x16x32_bf16 v[60:63], v[12:15], a[52:55], v[60:63]\n"
    "buffer_load_dwordx4 v96, s[0:3], s43 offen offset:0 sc1 lds\n"
    "v_mfma_f32_16x16x32_bf16 v[56:59], v[16:19], a[56:59], v[56:59]\n"
    "v_mfma_f32_16x16x32_bf16 v[60:63], v[20:23], a[56:59], v[60:63]\n"
    "v_mfma_f32_16x16x32_bf16 v[56:59], v[24:27], a[60:63], v[56:59]\n"
    "v_mfma_f32_16x16x32_bf16 v[60:63], v[28:31], a[60:63], v[60:63]\n"
    "buffer_load_dwordx4 v96, s[0:3], s43 offen offset:1024 sc1 lds\n"
    "v_mfma_f32_16x16x32_bf16 v[56:59], v[32:35], a[64:67], v[56:59]\n"
    "v_mfma_f32_16x16x32_bf16 v[60:63], v[36:39], a[64:67], v[60:63]\n"
    "v_mfma_f32_16x16x32_bf16 v[56:59], v[40:43], a[68:71], v[56:59]\n"
    "v_mfma_f32_16x16x32_bf16 v[60:63], v[44:47], a[68:71], v[60:63]\n"
    "buffer_load_dwordx4 v96, s[0:3], s43 offen offset:2048 sc1 lds\n"
    "v_mfma_f32_16x16x32_bf16 v[48:51], v[0:3], a[72:75], 0\n"
    "s_nop 1\n"
    "v_cvt_pk_bf16_f32 v80, v56, v57\n"
    "v_mfma_f32_16x16x32_bf16 v[52:55], v[4:7], a[72:75], 0\n"
    "v_cvt_pk_bf16_f32 v82, v58, v59\n"
    "v_cvt_pk_bf16_f32 v81, v60, v61\n"
    "v_mfma_f32_16x16x32_bf16 v[48:51], v[8:11], a[76:79], v[48:51]\n"
    "v_cvt_pk_bf16_f32 v83, v62, v63\n"
    "v_mfma_f32_16x16x32_bf16 v[52:55], v[12:15], a[76:79], v[52:55]\n"
    "buffer_store_dwordx4 v[72:75], v96, s[0:3], s40 offen sc1\n"
    "s_add_u32 s40, s40, 0x10000\n"
    "v_mfma_f32_16x16x32_bf16 v[48:51], v[16:19], a[80:83], v[48:51]\n"
    ".Lvo_mkL1_%=:\n"
    "v_mfma_f32_16x16x32_bf16 v[52:55], v[20:23], a[80:83], v[52:55]\n"
    "v_mfma_f32_16x16x32_bf16 v[48:51], v[24:27], a[84:87], v[48:51]\n"
    "v_mfma_f32_16x16x32_bf16 v[52:55], v[28:31], a[84:87], v[52:55]\n"
    "v_mfma_f32_16x16x32_bf16 v[48:51], v[32:35], a[88:91], v[48:51]\n"
    "v_mfma_f32_16x16x32_bf16 v[52:55], v[36:39], a[88:91], v[52:55]\n"
    "v_mfma_f32_16x16x32_bf16 v[48:51], v[40:43], a[92:95], v[48:51]\n"
    "buffer_load_dwordx4 v96, s[0:3], s43 offen offset:3072 sc1 lds\n"
    "v_mfma_f32_16x16x32_bf16 v[52:55], v[44:47], a[92:95], v[52:55]\n"
    "v_mfma_f32_16x16x32_bf16 v[56:59], v[0:3], a[96:99], 0\n"
    "s_nop 1\n"
    "v_cvt_pk_bf16_f32 v72, v48, v49\n"
    "v_mfma_f32_16x16x32_bf16 v[60:63], v[4:7], a[96:99], 0\n"
    "v_cvt_pk_bf16_f32 v74, v50, v51\n"
    "v_cvt_pk_bf16_f32 v73, v52, v53\n"
    "v_mfma_f32_16x16x32_bf16 v[56:59], v[8:11], a[100:103], v[56:59]\n"
    "buffer_store_dwordx4 v[76:79], v96, s[0:3], s40 offen sc1\n"
    "v_cvt_pk_bf16_f32 v75, v54, v55\n"
    "s_add_u32 s40, s40, 0x10000\n"
    "s_mov_b32 m0, s48\n"
    "v_mfma_f32_16x16x32_bf16 v[60:63], v[12:15], a[100:103], v[60:63]\n"
    "s_add_u32 s43, s43, s19\n"
    "v_mfma_f32_16x16x32_bf16 v[56:59], v[16:19], a[104:107], v[56:59]\n"
    "v_mfma_f32_16x16x32_bf16 v[60:63], v[20:23], a[104:107], v[60:63]\n"
    "v_mfma_f32_16x16x32_bf16 v[56:59], v[24:27], a[108:111], v[56:59]\n"
    "v_mfma_f32_16x16x32_bf16 v[60:63], v[28:31], a[108:111], v[60:63]\n"
    "v_mfma_f32_16x16x32_bf16 v[56:59], v[32:35], a[112:115], v[56:59]\n"
    "v_mfma_f32_16x16x32_bf16 v[60:63], v[36:39], a[112:115], v[60:63]\n"
    "buffer_load_dwordx4 v96, s[0:3], s43 offen offset:0 sc1 lds\n"
    "v_mfma_f32_16x16x32_bf16 v[56:59], v[40:43], a[116:119], v[56:59]\n"
    "v_mfma_f32_16x16x32_bf16 v[60:63], v[44:47], a[116:119], v[60:63]\n"
    "v_mfma_f32_16x16x32_bf16 v[48:51], v[0:3], a[120:123], 0\n"
    "s_nop 1\n"
    "v_cvt_pk_bf16_f32 v76, v56, v57\n"
    "v_mfma_f32_16x16x32_bf16 v[52:55], v[4:7], a[120:123], 0\n"
    "buffer_store_dwordx4 v[80:83], v96, s[0:3], s40 offen sc1\n"
    "v_cvt_pk_bf16_f32 v78, v58, v59\n"
    "v_cvt_pk_bf16_f32 v77, v60, v61\n"
    "s_add_u32 s40, s40, 0x10000\n"
    "v_mfma_f32_16x16x32_bf16 v[48:51], v[8:11], a[124:127], v[48:51]\n"
    "v_cvt_pk_bf16_f32 v79, v62, v63\n"
    "v_mfma_f32_16x16x32_bf16 v[52:55], v[12:15], a[124:127], v[52:55]\n"
    "v_mfma_f32_16x16x32_bf16 v[48:51], v[16:19], a[128:131], v[48:51]\n"
    "v_mfma_f32_16x16x32_bf16 v[52:55], v[20:23], a[128:131], v[52:55]\n"
    "v_mfma_f32_16x16x32_bf16 v[48:51], v[24:27], a[132:135], v[48:51]\n"
    "v_mfma_f32_16x16x32_bf16 v[52:55], v[28:31], a[132:135], v[52:55]\n"
    "v_mfma_f32_16x16x32_bf16 v[48:51], v[32:35], a[136:139], v[48:51]\n"
    "buffer_load_dwordx4 v96, s[0:3], s43 offen offset:1024 sc1 lds\n"
    "v_mfma_f32_16x16x32_bf16 v[52:55], v[36:39], a[136:139], v[52:55]\n"
    "v_mfma_f32_16x16x32_bf16 v[48:51], v[40:43], a[140:143], v[48:51]\n"
    ".Lvo_mkL2_%=:\n"
    "v_mfma_f32_16x16x32_bf16 v[52:55], v[44:47], a[140:143], v[52:55]\n"
    "v_mfma_f32_16x16x32_bf16 v[56:59], v[0:3], a[144:147], 0\n"
    "buffer_store_dwordx4 v[72:75], v96, s[0:3], s40 offen sc1\n"
    "s_nop 0\n"
    "v_cvt_pk_bf16_f32 v80, v48, v49\n"
    "s_add_u32 s40, s40, 0x10000\n"
    "v_mfma_f32_16x16x32_bf16 v[60:63], v[4:7], a[144:147], 0\n"
    "v_cvt_pk_bf16_f32 v82, v50, v51\n"
    "v_cvt_pk_bf16_f32 v81, v52, v53\n"
    "v_mfma_f32_16x16x32_bf16 v[56:59], v[8:11], a[148:151], v[56:59]\n"
    "v_cvt_pk_bf16_f32 v83, v54, v55\n"
    "v_mfma_f32_16x16x32_bf16 v[60:63], v[12:15], a[148:151], v[60:63]\n"
    "v_mfma_f32_16x16x32_bf16 v[56:59], v[16:19], a[152:155], v[56:59]\n"
    "v_mfma_f32_16x16x32_bf16 v[60:63], v[20:23], a[152:155], v[60:63]\n"
    "v_mfma_f32_16x16x32_bf16 v[56:59], v[24:27], a[156:159], v[56:59]\n"
    "v_mfma_f32_16x16x32_bf16 v[60:63], v[28:31], a[156:159], v[60:63]\n"
    "buffer_load_dwordx2 v[108:109], off, s[0:3], s45 sc1\n"
    "v_mfma_f32_16x16x32_bf16 v[56:59], v[32:35], a[160:163], v[56:59]\n"
    "v_mfma_f32_16x16x32_bf16 v[60:63], v[36:39], a[160:163], v[60:63]\n"
    "v_mfma_f32_16x16x32_bf16 v[56:59], v[40:43], a[164:167], v[56:59]\n"
    "v_mfma_f32_16x16x32_bf16 v[60:63], v[44:47], a[164:167], v[60:63]\n"
    "buffer_store_dwordx4 v[76:79], v96, s[0:3], s40 offen sc1\n"
    "s_add_u32 s40, s40, 0x10000\n"
    "v_mfma_f32_16x16x32_bf16 v[48:51], v[0:3], a[168:171], 0\n"
    "s_nop 1\n"
    "v_cvt_pk_bf16_f32 v72, v56, v57\n"
    "v_mfma_f32_16x16x32_bf16 v[52:55], v[4:7], a[168:171], 0\n"
    "v_cvt_pk_bf16_f32 v74, v58, v59\n"
    "v_cvt_pk_bf16_f32 v73, v60, v61\n"
    "v_mfma_f32_16x16x32_bf16 v[48:51], v[8:11], a[172:175], v[48:51]\n"
    "v_cvt_pk_bf16_f32 v75, v62, v63\n"
    "v_mfma_f32_16x16x32_bf16 v[52:55], v[12:15], a[172:175], v[52:55]\n"
    "v_mfma_f32_16x16x32_bf16 v[48:51], v[16:19], a[176:179], v[48:51]\n"
    "v_mfma_f32_16x16x32_bf16 v[52:55], v[20:23], a[176:179], v[52:55]\n"
    "v_mfma_f32_16x16x32_bf16 v[48:51], v[24:27], a[180:183], v[48:51]\n"
    "buffer_store_dwordx4 v[88:91], v102, s[0:3], s38 offen sc1\n"
    "v_mfma_f32_16x16x32_bf16 v[52:55], v[28:31], a[180:183], v[52:55]\n"
    "v_mfma_f32_16x16x32_bf16 v[48:51], v[32:35], a[184:187], v[48:51]\n"
    "v_mfma_f32_16x16x32_bf16 v[52:55], v[36:39], a[184:187], v[52:55]\n"
    "v_mfma_f32_16x16x32_bf16 v[48:51], v[40:43], a[188:191], v[48:51]\n"
    "v_mfma_f32_16x16x32_bf16 v[52:55], v[44:47], a[188:191], v[52:55]\n"
    "v_mfma_f32_16x16x32_bf16 v[56:59], v[0:3], a[192:195], 0\n"
    "s_nop 1\n"
    "v_cvt_pk_bf16_f32 v76, v48, v49\n"
    "v_mfma_f32_16x16x32_bf16 v[60:63], v[4:7], a[192:195], 0\n"
    "buffer_store_dwordx4 v[80:83], v96, s[0:3], s40 offen sc1\n"
    "v_cvt_pk_bf16_f32 v78, v50, v51\n"
    "v_cvt_pk_bf16_f32 v77, v52, v53\n"
    "s_add_u32 s40, s40, 0x10000\n"
    "v_mfma_f32_16x16x32_bf16 v[56:59], v[8:11], a[196:199], v[56:59]\n"
    "v_cvt_pk_bf16_f32 v79, v54, v55\n"
    "v_mfma_f32_16x16x32_bf16 v[60:63], v[12:15], a[196:199], v[60:63]\n"
    "v_mfma_f32_16x16x32_bf16 v[56:59], v[16:19], a[200:203], v[56:59]\n"
    ".Lvo_mkL3_%=:\n"
    "v_mfma_f32_16x16x32_bf16 v[60:63], v[20:23], a[200:203], v[60:63]\n"
    "v_mfma_f32_16x16x32_bf16 v[56:59], v[24:27], a[204:207], v[56:59]\n"
    "v_mfma_f32_16x16x32_bf16 v[60:63], v[28:31], a[204:207], v[60:63]\n"
    "v_mfma_f32_16x16x32_bf16 v[56:59], v[32:35], a[208:211], v[56:59]\n"
    "buffer_store_dwordx4 v[88:91], v102, s[0:3], s39 offen sc1\n"
    "v_mfma_f32_16x16x32_bf16 v[60:63], v[36:39], a[208:211], v[60:63]\n"
    "v_mfma_f32_16x16x32_bf16 v[56:59], v[40:43], a[212:215], v[56:59]\n"
    "v_mfma_f32_16x16x32_bf16 v[60:63], v[44:47], a[212:215], v[60:63]\n"
    "s_waitcnt vmcnt(15)\n"
    "s_barrier\n"
    "v_mfma_f32_16x16x32_bf16 v[48:51], v[24:27], a[228:231], 0\n"
    "s_nop 1\n"
    "v_cvt_pk_bf16_f32 v80, v56, v57\n"
    "v_mfma_f32_16x16x32_bf16 v[52:55], v[28:31], a[228:231], 0\n"
    "v_cvt_pk_bf16_f32 v82, v58, v59\n"
    "v_cvt_pk_bf16_f32 v81, v60, v61\n"
    "v_mfma_f32_16x16x32_bf16 v[48:51], v[32:35], a[232:235], v[48:51]\n"
    "ds_read_b128 v[24:27], v92 offset:0\n"
    "v_cvt_pk_bf16_f32 v83, v62, v63\n"
    "v_mfma_f32_16x16x32_bf16 v[52:55], v[36:39], a[232:235], v[52:55]\n"
    "buffer_store_dwordx4 v[72:75], v96, s[0:3], s40 offen sc1\n"
    "ds_read_b128 v[28:31], v92 offset:1024\n"
    "s_add_u32 s40, s40, 0x10000\n"
    "v_mfma_f32_16x16x32_bf16 v[48:51], v[40:43], a[236:239], v[48:51]\n"
    "ds_read_b128 v[32:35], v93 offset:0\n"
    "v_mfma_f32_16x16x32_bf16 v[52:55], v[44:47], a[236:239], v[52:55]\n"
    "ds_read_b128 v[36:39], v93 offset:1024\n"
    "v_mfma_f32_16x16x32_bf16 v[48:51], v[0:3], a[216:219], v[48:51]\n"
    "ds_read_b128 v[40:43], v94 offset:0\n"
    "v_mfma_f32_16x16x32_bf16 v[52:55], v[4:7], a[216:219], v[52:55]\n"
    "ds_read_b128 v[44:47], v94 offset:1024\n"
    "v_mfma_f32_16x16x32_bf16 v[48:51], v[8:11], a[220:223], v[48:51]\n"
    ".Lvo_mkL4_%=:\n"
    "v_mfma_f32_16x16x32_bf16 v[52:55], v[12:15], a[220:223], v[52:55]\n"
    "v_mfma_f32_16x16x32_bf16 v[48:51], v[16:19], a[224:227], v[48:51]\n"
    "buffer_store_dwordx2 v[88:89], v103, s[0:3], s38 offen sc1\n"
    "v_mfma_f32_16x16x32_bf16 v[52:55], v[20:23], a[224:227], v[52:55]\n"
    ".Lvo_mk4x10_%=:\n"
    ".Lvo_vph_%=:\n"
    "s_waitcnt lgkmcnt(4)\n"
    "v_mfma_f32_16x16x32_bf16 v[64:67], a[240:243], v[24:27], 0\n"
    "s_nop 1\n"
    "v_cvt_pk_bf16_f32 v72, v48, v49\n"
    "v_mfma_f32_16x16x32_bf16 v[84:87], v[176:179], v[24:27], 0\n"
    "s_add_u32 s34, s34, 1\n"
    "v_cvt_pk_bf16_f32 v74, v50, v51\n"
    "v_cvt_pk_bf16_f32 v73, v52, v53\n"
    "v_mfma_f32_16x16x32_bf16 v[68:71], a[240:243], v[28:31], 0\n"
    "s_add_u32 s36, s34, 0\n"
    "ds_read_b128 v[24:27], v95 offset:0\n"
    "v_cvt_pk_bf16_f32 v75, v54, v55\n"
    "v_mfma_f32_16x16x32_bf16 v[88:91], v[176:179], v[28:31], 0\n"
    "s_waitcnt lgkmcnt(3)\n"
    "ds_read_b128 v[28:31], v95 offset:1024\n"
    "s_and_b32 s36, s36, 3\n"
    "v_mfma_f32_16x16x32_bf16 v[64:67], v[112:115], v[32:35], v[64:67]\n"
    "s_lshl_b32 s36, s36, 22\n"
    "s_add_u32 s41, s15, s36\n"
    "s_add_u32 s42, s16, s36\n"
    "v_mfma_f32_16x16x32_bf16 v[84:87], v[192:195], v[32:35], v[84:87]\n"
    "s_add_u32 s37, s36, 0x10000\n"
    "s_add_u32 s50, s34, 3\n"
    "ds_read_b128 v[0:3], v97 offset:0\n"
    "v_mfma_f32_16x16x32_bf16 v[68:71], v[112:115], v[36:39], v[68:71]\n"
    "buffer_store_dwordx4 v[76:79], v96, s[0:3], s40 offen sc1\n"
    "ds_read_b128 v[32:35], v92 offset:2048\n"
    "s_add_u32 s40, s40, 0x10000\n"
    "s_cmp_gt_i32 s50, 64\n"
    "s_cselect_b32 s50, 0, s50\n"
    "v_mfma_f32_16x16x32_bf16 v[88:91], v[192:195], v[36:39], v[88:91]\n"
    "s_waitcnt lgkmcnt(4)\n"
    "ds_read_b128 v[36:39], v92 offset:3072\n"
    "s_add_u32 s51, s34, 4\n"
    "v_mfma_f32_16x16x32_bf16 v[64:67], v[128:131], v[40:43], v[64:67]\n"
    "s_cmp_gt_i32 s51, 64\n"
    "s_cselect_b32 s51, 0, s51\n"
    "s_mul_i32 s51, s51, s14\n"
    "v_mfma_f32_16x16x32_bf16 v[84:87], v[208:211], v[40:43], v[84:87]\n"
    "ds_read_b128 v[4:7], v97 offset:1024\n"
    "v_mov_b32 v111, s50\n"
    "v_mfma_f32_16x16x32_bf16 v[68:71], v[128:131], v[44:47], v[68:71]\n"
    "ds_read_b128 v[40:43], v93 offset:2048\n"
    "v_mfma_f32_16x16x32_bf16 v[88:91], v[208:211], v[44:47], v[88:91]\n"
    "s_waitcnt lgkmcnt(5)\n"
    "ds_read_b128 v[44:47], v93 offset:3072\n"
    "v_mfma_f32_16x16x32_bf16 v[64:67], v[144:147], v[24:27], v[64:67]\n"
    "s_mov_b64 exec, 0x1f0\n"
    "v_mov_b32 v111, s51\n"
    "s_mov_b64 exec, -1\n"
    "v_mfma_f32_16x16x32_bf16 v[84:87], v[224:227], v[24:27], v[84:87]\n"
    "buffer_store_dwordx4 v[80:83], v104, s[0:3], s38 offen sc1\n"
    "ds_read_b128 v[8:11], v97 offset:2048\n"
    "v_mfma_f32_16x16x32_bf16 v[68:71], v[144:147], v[28:31], v[68:71]\n"
    "ds_read_b128 v[24:27], v94 offset:2048\n"
    "v_mfma_f32_16x16x32_bf16 v[88:91], v[224:227], v[28:31], v[88:91]\n"
    "s_waitcnt lgkmcnt(5)\n"
    "ds_read_b128 v[28:31], v94 offset:3072\n"
    "v_mfma_f32_16x16x32_bf16 v[64:67], a[244:247], v[32:35], v[64:67]\n"
    "ds_read_b128 v[12:15], v97 offset:3072\n"
    "v_mfma_f32_16x16x32_bf16 v[84:87], v[180:183], v[32:35], v[84:87]\n"
    "ds_read_b128 v[16:19], v97 offset:4096\n"
    "v_mfma_f32_16x16x32_bf16 v[68:71], a[244:247], v[36:39], v[68:71]\n"
    "ds_read_b128 v[32:35], v95 offset:2048\n"
    "v_mfma_f32_16x16x32_bf16 v[88:91], v[180:183], v[36:39], v[88:91]\n"
    "s_waitcnt lgkmcnt(6)\n"
    "ds_read_b128 v[36:39], v95 offset:3072\n"
    "v_mfma_f32_16x16x32_bf16 v[64:67], v[116:119], v[40:43], v[64:67]\n"
    "buffer_store_dwordx4 v[72:75], v104, s[0:3], s39 offen sc1\n"
    "s_waitcnt vmcnt(11)\n"
    "ds_read_b128 v[20:23], v97 offset:5120\n"
    "v_mfma_f32_16x16x32_bf16 v[84:87], v[196:199], v[40:43], v[84:87]\n"
    "s_barrier\n"
    "ds_read_b128 v[48:51], v97 offset:8192\n"
    "v_mfma_f32_16x16x32_bf16 v[68:71], v[116:119], v[44:47], v[68:71]\n"
    "ds_read_b128 v[40:43], v92 offset:4096\n"
    "v_mfma_f32_16x16x32_bf16 v[88:91], v[196:199], v[44:47], v[88:91]\n"
    "s_waitcnt lgkmcnt(7)\n"
    "ds_read_b128 v[44:47], v92 offset:5120\n"
    "s_waitcnt vmcnt(9)\n"
    "v_mfma_f32_16x16x32_bf16 v[64:67], v[132:135], v[24:27], v[64:67]\n"
    "s_memrealtime s[54:55]\n"
    "v_readfirstlane_b32 s32, v108\n"
    "v_readfirstlane_b32 s33, v109\n"
    ".Lvo_mkL5_%=:\n"
    "v_mfma_f32_16x16x32_bf16 v[84:87], v[212:215], v[24:27], v[84:87]\n"
    "ds_read_b128 v[52:55], v97 offset:9216\n"
    "v_mfma_f32_16x16x32_bf16 v[68:71], v[132:135], v[28:31], v[68:71]\n"
    "s_cmp_eq_u32 s33, s35\n"
    "s_cselect_b32 s46, s32, s46\n"
    "ds_read_b128 v[24:27], v93 offset:4096\n"
    "v_mfma_f32_16x16x32_bf16 v[88:91], v[212:215], v[28:31], v[88:91]\n"
    "s_waitcnt lgkmcnt(6)\n"
    "ds_read_b128 v[28:31], v93 offset:5120\n"
    "s_add_u32 s46, s46, 264\n"
    "v_mfma_f32_16x16x32_bf16 v[64:67], v[148:151], v[32:35], v[64:67]\n"
    "s_sub_u32 s47, s46, 59\n"
    "s_mov_b32 m0, s17\n"
    "ds_read_b128 v[56:59], v97 offset:10240\n"
    "v_mfma_f32_16x16x32_bf16 v[84:87], v[228:231], v[32:35], v[84:87]\n"
    "ds_read_b128 v[60:63], v97 offset:11264\n"
    ".Lvo_mkL6_%=:\n"
    "v_mfma_f32_16x16x32_bf16 v[68:71], v[148:151], v[36:39], v[68:71]\n"
    "ds_read_b128 v[32:35], v94 offset:4096\n"
    "s_sub_u32 s56, s47, s54\n"
    "v_mfma_f32_16x16x32_bf16 v[88:91], v[228:231], v[36:39], v[88:91]\n"
    "s_waitcnt lgkmcnt(6)\n"
    "ds_read_b128 v[36:39], v94 offset:5120\n"
    ".Lvo_mk6x11_%=:\n"
    "s_cmp_gt_i32 s56, 4\n"
    "s_cbranch_scc1 .Lvo_early_%=\n"
    "buffer_load_dwordx4 v96, s[0:3], s41 offen sc1 lds\n"
    "buffer_load_dwordx4 v96, s[0:3], s41 offen offset:1024 sc1 lds\n"
    ".Lvo_asked_%=:\n"
    ".Lvo_mk7x13_%=:\n"
    "v_mfma_f32_16x16x32_bf16 v[64:67], a[248:251], v[40:43], v[64:67]\n"
    "s_add_u32 s49, s34, 3\n"
    "s_lshl_b32 s43, s49, 19\n"
    "s_lshl_b32 s33, s49, 11\n"
    "v_mfma_f32_16x16x32_bf16 v[84:87], v[184:187], v[40:43], v[84:87]\n"
    "s_xor_b32 s33, s33, s13\n"
    "s_and_b32 s33, s33, 0x2000\n"
    "s_add_u32 s43, s43, s33\n"
    ".Lvo_mkM0_%=:\n"
    "v_mfma_f32_16x16x32_bf16 v[68:71], a[248:251], v[44:47], v[68:71]\n"
    "ds_read_b128 v[40:43], v95 offset:4096\n"
    "s_add_u32 s43, s43, s18\n"
    "v_mfma_f32_16x16x32_bf16 v[88:91], v[184:187], v[44:47], v[88:91]\n"
    "s_waitcnt lgkmcnt(5)\n"
    "ds_read_b128 v[44:47], v95 offset:5120\n"
    "s_add_u32 s49, s34, 2\n"
    "v_mfma_f32_16x16x32_bf16 v[64:67], v[120:123], v[24:27], v[64:67]\n"
    "s_lshl_b32 s44, s49, 20\n"
    "s_lshl_b32 s33, s49, 12\n"
    "s_xor_b32 s33, s33, s13\n"
    "v_mfma_f32_16x16x32_bf16 v[84:87], v[200:203], v[24:27], v[84:87]\n"
    "s_and_b32 s33, s33, 0x2000\n"
    "s_add_u32 s44, s44, s33\n"
    "s_add_u32 s44, s44, s20\n"
    "v_mfma_f32_16x16x32_bf16 v[68:71], v[120:123], v[28:31], v[68:71]\n"
    "ds_read_b128 v[24:27], v92 offset:6144\n"
    "s_add_u32 s38, s34, 2\n"
    "v_mfma_f32_16x16x32_bf16 v[88:91], v[200:203], v[28:31], v[88:91]\n"
    "s_waitcnt lgkmcnt(3)\n"
    "ds_read_b128 v[28:31], v92 offset:7168\n"
    "s_and_b32 s38, s38, 3\n"
    "v_mfma_f32_16x16x32_bf16 v[64:67], v[136:139], v[32:35], v[64:67]\n"
    "buffer_load_dwordx4 v96, s[0:3], s42 offen offset:2048 sc1 lds\n"
    "v_mfma_f32_16x16x32_bf16 v[84:87], v[216:219], v[32:35], v[84:87]\n"
    "s_lshl_b32 s38, s38, 22\n"
    "s_add_u32 s39, s38, 0x10000\n"
    "s_add_u32 s40, s22, s38\n"
    "v_mfma_f32_16x16x32_bf16 v[68:71], v[136:139], v[36:39], v[68:71]\n"
    "ds_read_b128 v[32:35], v93 offset:6144\n"
    "s_and_b32 s45, s34, 63\n"
    ".Lvo_mkM1_%=:\n"
    "v_mfma_f32_16x16x32_bf16 v[88:91], v[216:219], v[36:39], v[88:91]\n"
    "s_waitcnt lgkmcnt(3)\n"
    "ds_read_b128 v[36:39], v93 offset:7168\n"
    "s_lshl_b32 s45, s45, 7\n"
    "v_mfma_f32_16x16x32_bf16 v[64:67], v[152:155], v[40:43], v[64:67]\n"
    "s_add_u32 s45, s23, s45\n"
    "s_add_u32 s35, s34, 1\n"
    "v_mfma_f32_16x16x32_bf16 v[84:87], v[232:235], v[40:43], v[84:87]\n"
    "s_cmp_lt_i32 s34, 0\n"
    "s_cselect_b32 s35, -5, s35\n"
    "s_cselect_b32 s52, 0, -1\n"
    "v_mfma_f32_16x16x32_bf16 v[68:71], v[152:155], v[44:47], v[68:71]\n"
    "ds_read_b128 v[40:43], v94 offset:6144\n"
    "v_mfma_f32_16x16x32_bf16 v[88:91], v[232:235], v[44:47], v[88:91]\n"
    "s_waitcnt lgkmcnt(3)\n"
    "ds_read_b128 v[44:47], v94 offset:7168\n"
    "v_mfma_f32_16x16x32_bf16 v[64:67], a[252:255], v[24:27], v[64:67]\n"
    "buffer_load_dword v110, v105, s[0:3], 0 offen sc1\n"
    "v_mfma_f32_16x16x32_bf16 v[84:87], v[188:191], v[24:27], v[84:87]\n"
    "v_mfma_f32_16x16x32_bf16 v[68:71], a[252:255], v[28:31], v[68:71]\n"
    "ds_read_b128 v[24:27], v95 offset:6144\n"
    "v_mfma_f32_16x16x32_bf16 v[88:91], v[188:191], v[28:31], v[88:91]\n"
    "s_waitcnt lgkmcnt(3)\n"
    "ds_read_b128 v[28:31], v95 offset:7168\n"
    "v_mfma_f32_16x16x32_bf16 v[64:67], v[124:127], v[32:35], v[64:67]\n"
    ".Lvo_mkM2_%=:\n"
    "v_mfma_f32_16x16x32_bf16 v[84:87], v[204:207], v[32:35], v[84:87]\n"
    "v_mfma_f32_16x16x32_bf16 v[68:71], v[124:127], v[36:39], v[68:71]\n"
    "ds_read_b128 v[32:35], v96 offset:53248\n"
    "v_mfma_f32_16x16x32_bf16 v[88:91], v[204:207], v[36:39], v[88:91]\n"
    "s_waitcnt lgkmcnt(3)\n"
    "ds_read_b128 v[36:39], v96 offset:54272\n"
    "v_mfma_f32_16x16x32_bf16 v[64:67], v[140:143], v[40:43], v[64:67]\n"
    "v_mfma_f32_16x16x32_bf16 v[84:87], v[220:223], v[40:43], v[84:87]\n"
    "v_mfma_f32_16x16x32_bf16 v[68:71], v[140:143], v[44:47], v[68:71]\n"
    "ds_read_b128 v[40:43], v96 offset:55296\n"
    ".Lvo_mkM3_%=:\n"
    "v_mfma_f32_16x16x32_bf16 v[88:91], v[220:223], v[44:47], v[88:91]\n"
    "s_waitcnt lgkmcnt(3)\n"
    "ds_read_b128 v[44:47], v96 offset:56320\n"
    "v_mfma_f32_16x16x32_bf16 v[64:67], v[156:159], v[24:27], v[64:67]\n"
    "v_mfma_f32_16x16x32_bf16 v[84:87], v[236:239], v[24:27], v[84:87]\n"
    "v_mfma_f32_16x16x32_bf16 v[68:71], v[156:159], v[28:31], v[68:71]\n"
    "ds_read_b128 v[24:27], v96 offset:57344\n"
    "v_mfma_f32_16x16x32_bf16 v[88:91], v[236:239], v[28:31], v[88:91]\n"
    "s_waitcnt lgkmcnt(3)\n"
    "ds_read_b128 v[28:31], v96 offset:58368\n"
    "v_mfma_f32_16x16x32_bf16 v[64:67], v[160:163], v[32:35], v[64:67]\n"
    "v_mfma_f32_16x16x32_bf16 v[84:87], v[240:243], v[32:35], v[84:87]\n"
    "ds_read_b128 v[32:35], v96 offset:59392\n"
    "v_mfma_f32_16x16x32_bf16 v[68:71], v[160:163], v[36:39], v[68:71]\n"
    "v_mfma_f32_16x16x32_bf16 v[88:91], v[240:243], v[36:39], v[88:91]\n"
    "s_waitcnt lgkmcnt(3)\n"
    "ds_read_b128 v[36:39], v96 offset:60416\n"
    "v_mfma_f32_16x16x32_bf16 v[64:67], v[164:167], v[40:43], v[64:67]\n"
    "s_waitcnt lgkmcnt(2)\n"
    "v_mfma_f32_16x16x32_bf16 v[64:67], v[168:171], v[24:27], v[64:67]\n"
    "s_waitcnt lgkmcnt(1)\n"
    "v_mfma_f32_16x16x32_bf16 v[64:67], v[172:175], v[32:35], v[64:67]\n"
    ".Lvo_mkM4_%=:\n"
    "v_mfma_f32_16x16x32_bf16 v[84:87], v[244:247], v[40:43], v[84:87]\n"
    "s_branch .Lvo_take_%=\n"
    ".Lvo_tk0_%=:\n"
    ".Lvo_po3_%=:\n"
    "buffer_load_dword v109, v108, s[0:3], 0 offen sc1\n"
    "s_waitcnt vmcnt(0)\n"
    "v_cmp_gt_i32 vcc, 32, v109\n"
    "s_nop 0\n"
    "s_cbranch_vccz .Lvo_pd4_%=\n"
    "s_add_u32 s28, s28, 1\n"
    "s_cmp_lt_u32 s28, 0x40000\n"
    "s_cbranch_scc1 .Lvo_sp5_%=\n"
    "v_mov_b32 v109, 90\n"
    "s_mov_b32 s32, 33728\n"
    "buffer_store_dword v109, off, s[0:3], s32 sc1\n"
    "s_waitcnt vmcnt(0)\n"
    "s_branch .Lvo_pd4_%=\n"
    ".Lvo_sp5_%=:\n"
    "s_sleep 8\n"
    "s_branch .Lvo_po3_%=\n"
    ".Lvo_pd4_%=:\n"
    "s_branch .Lvo_tk1_%=\n"
    ".Lvo_early_%=:\n"
    ".Lvo_rt13_%=:\n"
    "s_memrealtime s[30:31]\n"
    "s_waitcnt lgkmcnt(0)\n"
    "s_sub_u32 s32, s47, s30\n"
    "s_cmp_lt_u32 s32, 600\n"
    "s_cbranch_scc1 .Lvo_rt13_%=\n"
    "buffer_load_dwordx4 v96, s[0:3], s41 offen sc1 lds\n"
    "buffer_load_dwordx4 v96, s[0:3], s41 offen offset:1024 sc1 lds\n"
    "s_branch .Lvo_asked_%=\n"
    ".Lvo_retry_%=:\n"
    "s_nop 7\n"
    "s_nop 7\n"
    "s_cmp_lt_i32 s34, 0\n"
    "s_cbranch_scc0 .Lvo_again_%=\n"
    "v_cvt_pk_bf16_f32 v44, v68, v69\n"
    "v_cvt_pk_bf16_f32 v45, v70, v71\n"
    "v_cvt_pk_bf16_f32 v46, v88, v89\n"
    "v_cvt_pk_bf16_f32 v47, v90, v91\n"
    "ds_write_b128 v92, v[44:47] offset:12288\n"
    "s_waitcnt vmcnt(0) lgkmcnt(0)\n"
    "s_barrier\n"
    "v_cmp_gt_i32 vcc, v111, v110\n"
    "s_nop 0\n"
    "s_cbranch_vccz .Lvo_e2_%=\n"
    ".Lvo_po15_%=:\n"
    "buffer_load_dword v110, v105, s[0:3], 0 offen sc1\n"
    "s_waitcnt vmcnt(0)\n"
    "v_cmp_gt_i32 vcc, v111, v110\n"
    "s_nop 0\n"
    "s_cbranch_vccz .Lvo_pd16_%=\n"
    "s_add_u32 s28, s28, 1\n"
    "s_cmp_lt_u32 s28, 0x40000\n"
    "s_cbranch_scc1 .Lvo_sp17_%=\n"
    "v_mov_b32 v110, 32\n"
    "s_mov_b32 s32, 33728\n"
    "buffer_store_dword v110, off, s[0:3], s32 sc1\n"
    "s_waitcnt vmcnt(0)\n"
    "s_branch .Lvo_pd16_%=\n"
    ".Lvo_sp17_%=:\n"
    "s_sleep 4\n"
    "s_branch .Lvo_po15_%=\n"
    ".Lvo_pd16_%=:\n"
    ".Lvo_e2_%=:\n"
    "s_mov_b32 m0, s21\n"
    "s_nop 0\n"
    "buffer_load_dwordx4 v96, s[0:3], s44 offen sc1 lds\n"
    "s_sub_u32 m0, s17, 8192\n"
    "s_nop 0\n"
    "buffer_load_dwordx4 v96, s[0:3], s43 offen offset:0 sc1 lds\n"
    "buffer_load_dwordx4 v96, s[0:3], s43 offen offset:1024 sc1 lds\n"
    "s_add_u32 s49, s34, 3\n"
    "v_lshlrev_b32 v108, 4, v96\n"
    "v_mov_b32 v109, s49\n"
    "s_mov_b64 exec, 15\n"
    "buffer_store_dword v109, v108, s[0:3], s25 offen sc1\n"
    "s_mov_b64 exec, -1\n"
    "ds_read_b128 v[0:3], v96 offset:11264\n"
    "ds_read_b128 v[4:7], v96 offset:12288\n"
    "ds_read_b128 v[8:11], v96 offset:24576\n"
    "ds_read_b128 v[12:15], v96 offset:25600\n"
    "ds_read_b128 v[16:19], v96 offset:37888\n"
    "ds_read_b128 v[20:23], v96 offset:38912\n"
    "ds_read_b128 v[24:27], v96 offset:51200\n"
    "ds_read_b128 v[28:31], v96 offset:52224\n"
    "s_waitcnt vmcnt(3) lgkmcnt(0)\n"
    "s_barrier\n"
    "ds_read2_b64 v[32:35], v106 offset0:0 offset1:128\n"
    "ds_read2_b64 v[36:39], v106 offset0:1 offset1:129\n"
    "v_mfma_f32_16x16x32_bf16 v[40:43], v[0:3], a[0:3], 0\n"
    "v_mfma_f32_16x16x32_bf16 v[44:47], v[4:7], a[0:3], 0\n"
    "v_mfma_f32_16x16x32_bf16 v[40:43], v[8:11], a[4:7], v[40:43]\n"
    "v_mfma_f32_16x16x32_bf16 v[44:47], v[12:15], a[4:7], v[44:47]\n"
    "v_mfma_f32_16x16x32_bf16 v[40:43], v[16:19], a[8:11], v[40:43]\n"
    "v_mfma_f32_16x16x32_bf16 v[44:47], v[20:23], a[8:11], v[44:47]\n"
    "v_mfma_f32_16x16x32_bf16 v[40:43], v[24:27], a[12:15], v[40:43]\n"
    "v_mfma_f32_16x16x32_bf16 v[44:47], v[28:31], a[12:15], v[44:47]\n"
    "s_waitcnt lgkmcnt(0)\n"
    "s_mov_b32 s37, 2130706432\n"
    "s_mov_b32 s36, 2130706432\n"
    "s_nop 7\n"
    "s_branch .Lvo_uph_%=\n"
    ".Lvo_e0_%=:\n"
    ".Lvo_po18_%=:\n"
    "buffer_load_dword v109, v108, s[0:3], s24 offen sc1\n"
    "s_waitcnt vmcnt(0)\n"
    "v_cmp_gt_i32 vcc, 1, v109\n"
    "s_nop 0\n"
    "s_cbranch_vccz .Lvo_pd19_%=\n"
    "s_add_u32 s28, s28, 1\n"
    "s_cmp_lt_u32 s28, 0x40000\n"
    "s_cbranch_scc1 .Lvo_sp20_%=\n"
    "v_mov_b32 v109, 30\n"
    "s_mov_b32 s32, 33728\n"
    "buffer_store_dword v109, off, s[0:3], s32 sc1\n"
    "s_waitcnt vmcnt(0)\n"
    "s_branch .Lvo_pd19_%=\n"
    ".Lvo_sp20_%=:\n"
    "s_sleep 4\n"
    "s_branch .Lvo_po18_%=\n"
    ".Lvo_pd19_%=:\n"
    ".Lvo_po21_%=:\n"
    "buffer_load_dword v110, v105, s[0:3], 0 offen sc1\n"
    "s_waitcnt vmcnt(0)\n"
    "v_cmp_gt_i32 vcc, v111, v110\n"
    "s_nop 0\n"
    "s_cbranch_vccz .Lvo_pd22_%=\n"
    "s_add_u32 s28, s28, 1\n"
    "s_cmp_lt_u32 s28, 0x40000\n"
    "s_cbranch_scc1 .Lvo_sp23_%=\n"
    "v_mov_b32 v110, 31\n"
    "s_mov_b32 s32, 33728\n"
    "buffer_store_dword v110, off, s[0:3], s32 sc1\n"
    "s_waitcnt vmcnt(0)\n"
    "s_branch .Lvo_pd22_%=\n"
    ".Lvo_sp23_%=:\n"
    "s_sleep 4\n"
    "s_branch .Lvo_po21_%=\n"
    ".Lvo_pd22_%=:\n"
    "s_branch .Lvo_e1_%=\n"
    ".Lvo_again_%=:\n"
    "s_add_u32 s28, s28, 1\n"
    "s_cmp_lt_u32 s28, 0x40000\n"
    "s_cbranch_scc1 .Lvo_sp24_%=\n"
    "v_mov_b32 v108, 2\n"
    "s_mov_b32 s32, 33728\n"
    "buffer_store_dword v108, off, s[0:3], s32 sc1\n"
    "s_waitcnt vmcnt(0)\n"
    "s_branch .Lvo_go_%=\n"
    ".Lvo_sp24_%=:\n"
    "buffer_load_dwordx4 v96, s[0:3], s41 offen sc1 lds\n"
    "buffer_load_dwordx4 v96, s[0:3], s41 offen offset:1024 sc1 lds\n"
    "buffer_load_dwordx4 v96, s[0:3], s42 offen offset:2048 sc1 lds\n"
    ".Lvo_ag1_%=:\n"
    "s_waitcnt vmcnt(0)\n"
    "ds_read_b128 v[72:75], v92 offset:8192\n"
    "ds_read_b128 v[76:79], v92 offset:9216\n"
    "s_waitcnt lgkmcnt(0)\n"
    "v_mov_b64 v[64:65], 0\n"
    "v_mov_b64 v[66:67], 0\n"
    "v_max_u32 v108, v72, v74\n"
    "v_max3_u32 v108, v108, v76, v78\n"
    "v_cmp_le_u32 vcc, s52, v108\n"
    "s_nop 0\n"
    "s_cbranch_vccnz .Lvo_again_%=\n"
    "v_smfmac_f32_16x16x64_bf16 v[64:67], v[72:75], a[240:247], v98\n"
    "s_branch .Lvo_go_%=\n"
    ".Lvo_r2_%=:\n"
    "s_add_u32 s28, s28, 1\n"
    "s_cmp_lt_u32 s28, 0x40000\n"
    "s_cbranch_scc1 .Lvo_sp25_%=\n"
    "v_mov_b32 v108, 6\n"
    "s_mov_b32 s32, 33728\n"
    "buffer_store_dword v108, off, s[0:3], s32 sc1\n"
    "s_waitcnt vmcnt(0)\n"
    "s_branch .Lvo_go2_%=\n"
    ".Lvo_sp25_%=:\n"
    "buffer_load_dwordx4 v96, s[0:3], s42 offen offset:2048 sc1 lds\n"
    ".Lvo_r3_%=:\n"
    "s_waitcnt vmcnt(0)\n"
    "ds_read_b128 v[80:83], v92 offset:10240\n"
    "s_waitcnt lgkmcnt(0)\n"
    "v_max_u32 v108, v80, v82\n"
    "v_cmp_eq_u32 vcc, -1, v108\n"
    "s_nop 0\n"
    "s_cbranch_vccnz .Lvo_r2_%=\n"
    "s_branch .Lvo_go2_%=\n"
    ".Lvo_wf_%=:\n"
    ".Lvo_po26_%=:\n"
    "buffer_load_dword v110, v105, s[0:3], 0 offen sc1\n"
    "s_waitcnt vmcnt(0)\n"
    "v_cmp_gt_i32 vcc, v111, v110\n"
    "s_nop 0\n"
    "s_cbranch_vccz .Lvo_pd27_%=\n"
    "s_add_u32 s28, s28, 1\n"
    "s_cmp_lt_u32 s28, 0x40000\n"
    "s_cbranch_scc1 .Lvo_sp28_%=\n"
    "v_mov_b32 v110, 33\n"
    "s_mov_b32 s32, 33728\n"
    "buffer_store_dword v110, off, s[0:3], s32 sc1\n"
    "s_waitcnt vmcnt(0)\n"
    "s_branch .Lvo_pd27_%=\n"
    ".Lvo_sp28_%=:\n"
    "s_sleep 4\n"
    "s_branch .Lvo_po26_%=\n"
    ".Lvo_pd27_%=:\n"
    "s_branch .Lvo_wfb_%=\n"
    ".Lvo_end_%=:\n"
    "s_waitcnt vmcnt(0) lgkmcnt(0)\n"
    "s_endpgm\n"
    :: "{s[0:3]}"(s0_), "{s[4:7]}"(s4_), "{s[8:11]}"(s8_), "{s[12:15]}"(s12_), "{s[16:19]}"(s16_), "{s[20:23]}"(s20_), "{s[24:27]}"(s24_), "{v[0:3]}"(v0_), "{v[4:7]}"(v4_), "{v[8:11]}"(v8_), "{v[12:15]}"(v12_), "{v[16:19]}"(v16_), "{v[92:95]}"(v92_), "{v[96:99]}"(v96_), "{v[100:103]}"(v100_), "{v[104:107]}"(v104_) : "memory", "v255", "a255", "s95");
    __builtin_unreachable();
}
DEV void ct_role(const MegaArgs& A, const int L, const int u, char* smem) {
    ROLE_HEAD;
    const int r5 = l / 5, q5 = l - 5 * r5, c0 = q5 < 4 ? 16 * u + 4 * q5 : 512 + 4 * u, tk = 8 * w + r5;
    u32x2 m0;
    {
        const int sg = 2 * u + L, rt = w & 1, j0 = 10 * (w >> 1);
        float* ex = (float*)(smem + 1024);
        u32x4 xa[10];
        const unsigned xo = ((32 * sg + 16 * rt + l16) * D_ + 32 * j0 + 8 * lq) * 2;
#pragma unroll
        for (int j = 0; j < 10; ++j) asm volatile("global_load_dwordx4 %0, %1, %2 offset:%3" : "=&v"(xa[j]) : "v"(xo), "s"(A.x0), "n"(64 * j) : "memory");
        asm volatile("global_load_dwordx2 %0, %1, %2" : "=&v"(m0) : "v"(((r5 & 7) * D_ + c0) * 2), "s"(A.mem0) : "memory");
        asm volatile("s_waitcnt vmcnt(0)" ::: "memory");
#pragma unroll
        for (int j = 0; j < 10; ++j) asm volatile("" : "+v"(xa[j]));
        asm volatile("" : "+v"(m0));
        float s = 0.f, q = 0.f;
#pragma unroll
        for (int j = 0; j < 10; ++j)
#pragma unroll
            for (int e = 0; e < 4; ++e) s += lo16(xa[j][e]) + hi16(xa[j][e]);
        s += __shfl_xor(s, 16); s += __shfl_xor(s, 32);
        ex[threadIdx.x] = s;
        asm volatile("s_waitcnt lgkmcnt(0)\n\ts_barrier" ::: "memory");
        const float mean = (ex[64 * rt + l16] + ex[128 + 64 * rt + l16]) * (1.0f / D_);
#pragma unroll
        for (int j = 0; j < 10; ++j)
#pragma unroll
            for (int e = 0; e < 4; ++e) { const float d = lo16(xa[j][e]) - mean, d2 = hi16(xa[j][e]) - mean; q += d * d; q += d2 * d2; }
        q += __shfl_xor(q, 16); q += __shfl_xor(q, 32);
        ex[256 + threadIdx.x] = q;
        asm volatile("s_waitcnt lgkmcnt(0)\n\ts_barrier" ::: "memory");
        q = ex[256 + 64 * rt + l16] + ex[384 + 64 * rt + l16];
        const float mb = rb(mean), rd = __builtin_amdgcn_rcpf(rb(sqrtf(rb(rb(q * (1.0f / D_)) + 1e-5f))));
#pragma unroll
        for (int j = 0; j < 10; ++j) {
            u32x4 o;
#pragma unroll
            for (int e = 0; e < 4; ++e) { const unsigned d = pk2(lo16(xa[j][e]) - mb, hi16(xa[j][e]) - mb); o[sw2(e)] = pk2(lo16(d) * rd, hi16(d) * rd); }
            bst16(rr, rec(sg, 0) + ((j0 + j) >> 2) * 16384 + ((j0 + j) & 3) * 2048 + rt * 1024 + l * 16, o);
        }
    }
#define XM 5
    const unsigned mpos = l < 40 && q5 < XM ? xl(RG_MEM + w * 16384 + (c0 >> 6) * 1024 + (((c0 >> 4) & 3) * 16 + 4 * (r5 >> 1) + ((c0 >> 1) & 2) + (r5 & 1)) * 16 + (c0 & 8), L) : OOB;
    const unsigned xpos = l < 40 ? (c0 >> 7) * 16384 + ((c0 >> 5) & 3) * 2048 + (tk >> 4) * 1024 + (((c0 >> 3) & 3) * 16 + (tk & 15)) * 16 + (c0 & 4) * 2 : OOB;
    const unsigned rpos = (L || l >= 40) ? xpos + RC_X2 : tk * 1280 + c0 * 2;
    const u32x4 rs = L ? rx : u32x4{lo(A.x0), hi(A.x0), 2048u * 1280u, 0x20000u};
#pragma unroll
    for (int k = 0; k < 4; ++k) bst16(rr, rec(2 * u + (k >> 1), L) + RC_X1 + 65536 + w * 2048 + (k & 1) * 1024 + l * 16, ones);
    bst8(rr, mpos, u32x2{nosent(m0[0]), m0[1]}); bst8(rr, mpos + SLOT, ones2); bst8(rr, mpos + 2 * SLOT, ones2);
    wait_drawn(A.ar);
    DRAIN_BARRIER();
    bst4(rr, threadIdx.x < 16 ? xl(Z_CT + (l & 7) * 256 + (32 * L + u) * 4, l >> 3) : OOB, 1u);
    if (!((A.layers >> L) & 1)) return;
    const u32x4 s0_ = {RFL(rx[0]), RFL(rx[1]), RFL(rx[2]), RFL(rx[3])};
    const u32x4 s4_ = {RFL(rs[0]), RFL(rs[1]), RFL(rs[2]), RFL(rs[3])};
    const u32x4 s8_ = {RFL(L << 13), RFL(REC0), RFL(xl(RG_UT + (u * 4 + w) * 8192, L)), RFL(xl(RG_UTX + (u * 4 + w) * 2048, L))};
    const u32x4 s12_ = {RFL(xl(RG_UM + (u * 4 + w) * 2048, L)), RFL(xl(RG_UMX + (u * 4 + w) * 512, L)), RFL(xl(RG_PT + w * 16384, L)), RFL(xl(RG_PM + w * 8192, L))};
    const u32x4 s16_ = {RFL(xl(Z_X + 16384 + (u & 1) * 8192, L)), RFL(xl(Z_QV + (u & 3) * 256, L)), RFL(DBG0 + ((L * 128 + 64 + u) * 4 + w) * 2048), RFL(DBG0 + (2 << 20) + ((L * 32 + u) * 4 + w) * 8192)};
    const u32x4 v192_ = {(unsigned)(l * 16), (unsigned)(l * 16 + 4096), (unsigned)((l & 2) ? 0xeeeeu : 0x4444u), (unsigned)(0x4444u)};
    const u32x4 v196_ = {(unsigned)((l16 >> 2) * 256 + lq * 64 + (l & 3) * 16), (unsigned)(lq * 64 + (l & 3) * 16), (unsigned)((lq >> 1) * 2048 + 1024 + (lq & 1) * 256 + l16 * 16), (unsigned)(lq * 2048 + 1536 + l16 * 16)};
    const u32x4 v200_ = {(unsigned)(0 + w * 640 + lq * 160 + l16 * 4), (unsigned)(l16 < 4 ? 0 + w * 640 + lq * 160 + 64 + l16 * 4 : 12800), (unsigned)(0 + (l < 40 ? l : 0) * 16), (unsigned)(mpos)};
    const u32x4 v204_ = {(unsigned)(2560 + w * 2560 + lq * 320 + l16 * 4), (unsigned)(l16 < 4 ? 2560 + w * 2560 + lq * 320 + 64 + l16 * 4 : 12800), (unsigned)(3840 + w * 2560 + lq * 320 + l16 * 4), (unsigned)(l16 < 4 ? 3840 + w * 2560 + lq * 320 + 64 + l16 * 4 : 12800)};
    const u32x4 v208_ = {(unsigned)(2560 + w * 640 + (l < 40 ? l : 0) * 16), (unsigned)(xpos + RC_X1), (unsigned)(rpos), (unsigned)(l < 4 && w == 1 ? xl(Z_X1 + l * 4096 + u * 128, L) : OOB)};
    const u32x4 v212_ = {(unsigned)(l < 16 ? ((u >> 2) & 1) * 16384 + (u & 3) * 2048 + l * 128 : OOB), (unsigned)(0u), (unsigned)(0u), (unsigned)(0u)};
    asm volatile(
    "s_mov_b32 s20, 0\n"
    "s_mov_b64 s[22:23], 0\n"
    "s_mov_b32 s25, 0\n"
    "s_mov_b32 s28, -2\n"
    "v_lshrrev_b32 v4, 2, v192\n"
    "v_lshrrev_b32 v5, 9, v192\n"
    "v_add_u32 v5, 1, v5\n"
    ".Lct_po1_%=:\n"
    "buffer_load_dword v8, v4, s[0:3], s17 offen sc1\n"
    "s_waitcnt vmcnt(0)\n"
    "v_cmp_gt_i32 vcc, v5, v8\n"
    "s_nop 0\n"
    "s_cbranch_vccz .Lct_pd2_%=\n"
    "s_add_u32 s20, s20, 1\n"
    "s_cmp_lt_u32 s20, 0x40000\n"
    "s_cbranch_scc1 .Lct_sp3_%=\n"
    "v_mov_b32 v8, 40\n"
    "s_mov_b32 s24, 33728\n"
    "buffer_store_dword v8, off, s[0:3], s24 sc1\n"
    "s_waitcnt vmcnt(0)\n"
    "s_branch .Lct_pd2_%=\n"
    ".Lct_sp3_%=:\n"
    "s_sleep 16\n"
    "s_branch .Lct_po1_%=\n"
    ".Lct_pd2_%=:\n"
    "s_memrealtime s[22:23]\n"
    "s_waitcnt lgkmcnt(0)\n"
    "s_sub_u32 s26, s22, 792\n"
    "s_mov_b32 s28, -1\n"
    "s_add_u32 s28, s28, 1\n"
    "s_add_u32 s27, s28, 1\n"
    "s_add_u32 s29, s28, 0\n"
    "s_and_b32 s29, s29, 3\n"
    "s_lshl_b32 s29, s29, 22\n"
    "s_add_u32 s30, s28, 1\n"
    "s_and_b32 s30, s30, 3\n"
    "s_lshl_b32 s30, s30, 22\n"
    "s_add_u32 s31, s28, 3\n"
    "s_and_b32 s31, s31, 3\n"
    "s_lshl_b32 s31, s31, 22\n"
    "s_and_b32 s41, s28, 63\n"
    "s_lshl_b32 s41, s41, 7\n"
    "s_add_u32 s32, s12, s29\n"
    "s_add_u32 s33, s13, s29\n"
    "s_add_u32 s34, s15, s29\n"
    "s_add_u32 s35, s14, s29\n"
    "s_add_u32 s39, s10, s30\n"
    "s_add_u32 s40, s11, s30\n"
    "s_add_u32 s41, s16, s41\n"
    "s_add_u32 s36, s35, 4096\n"
    "s_add_u32 s37, s35, 16384\n"
    "s_add_u32 s38, s35, 20480\n"
    "s_add_u32 s43, s26, 304\n"
    "s_lshl_b32 s45, s28, 19\n"
    "s_lshl_b32 s25, s28, 11\n"
    "s_xor_b32 s25, s25, s8\n"
    "s_and_b32 s25, s25, 0x2000\n"
    "s_add_u32 s45, s45, s25\n"
    "s_add_u32 s45, s45, s9\n"
    "s_mul_i32 s46, s28, 40960\n"
    "s_cmp_eq_u32 s8, 0\n"
    "s_cselect_b32 s46, s46, s45\n"
    "s_cmp_eq_u32 s28, 63\n"
    "s_cselect_b32 s39, 2130706432, s39\n"
    "s_cselect_b32 s40, 2130706432, s40\n"
    "v_mov_b64 v[76:77], 0\n"
    "v_mov_b64 v[78:79], 0\n"
    "v_mov_b64 v[80:81], 0\n"
    "v_mov_b64 v[82:83], 0\n"
    "s_mov_b32 s39, s10\n"
    "s_mov_b32 s40, s11\n"
    "buffer_load_dwordx4 a[0:3], v192, s[0:3], s39 offen offset:0 sc1\n"
    "buffer_load_dwordx4 a[4:7], v192, s[0:3], s39 offen offset:1024 sc1\n"
    "buffer_load_dwordx4 a[8:11], v192, s[0:3], s39 offen offset:2048 sc1\n"
    "buffer_load_dwordx4 a[12:15], v192, s[0:3], s39 offen offset:3072 sc1\n"
    "buffer_load_dwordx4 a[16:19], v193, s[0:3], s39 offen offset:0 sc1\n"
    "buffer_load_dwordx4 a[20:23], v193, s[0:3], s39 offen offset:1024 sc1\n"
    "buffer_load_dwordx4 a[24:27], v193, s[0:3], s39 offen offset:2048 sc1\n"
    "buffer_load_dwordx4 a[28:31], v193, s[0:3], s39 offen offset:3072 sc1\n"
    "buffer_load_dwordx4 v[0:3], v196, s[0:3], s40 offen offset:0 sc1\n"
    "buffer_load_dwordx4 v[4:7], v196, s[0:3], s40 offen offset:1024 sc1\n"
    "s_add_u32 s39, s10, s30\n"
    "s_add_u32 s40, s11, s30\n"
    "buffer_load_dwordx2 v[190:191], off, s[0:3], s41 sc1\n"
    "s_waitcnt vmcnt(0)\n"
    ".Lct_top_%=:\n"
    "s_waitcnt vmcnt(2)\n"
    "v_mov_b64 v[8:9], v[0:1]\n"
    "v_mov_b64 v[10:11], v[2:3]\n"
    "v_mov_b64 v[12:13], v[4:5]\n"
    "v_mov_b64 v[14:15], v[6:7]\n"
    "v_mov_b32_dpp v16, v0 row_shl:4 row_mask:0xf bank_mask:0xf bound_ctrl:1\n"
    "v_mov_b32_dpp v17, v1 row_shl:4 row_mask:0xf bank_mask:0xf bound_ctrl:1\n"
    "v_mov_b32_dpp v18, v2 row_shl:4 row_mask:0xf bank_mask:0xf bound_ctrl:1\n"
    "v_mov_b32_dpp v19, v3 row_shl:4 row_mask:0xf bank_mask:0xf bound_ctrl:1\n"
    "v_mov_b32_dpp v20, v4 row_shl:4 row_mask:0xf bank_mask:0xf bound_ctrl:1\n"
    "v_mov_b32_dpp v21, v5 row_shl:4 row_mask:0xf bank_mask:0xf bound_ctrl:1\n"
    "v_mov_b32_dpp v22, v6 row_shl:4 row_mask:0xf bank_mask:0xf bound_ctrl:1\n"
    "v_mov_b32_dpp v23, v7 row_shl:4 row_mask:0xf bank_mask:0xf bound_ctrl:1\n"
    "v_mov_b32_dpp v24, v0 row_shl:8 row_mask:0xf bank_mask:0xf bound_ctrl:1\n"
    "v_mov_b32_dpp v25, v1 row_shl:8 row_mask:0xf bank_mask:0xf bound_ctrl:1\n"
    "v_mov_b32_dpp v26, v2 row_shl:8 row_mask:0xf bank_mask:0xf bound_ctrl:1\n"
    "v_mov_b32_dpp v27, v3 row_shl:8 row_mask:0xf bank_mask:0xf bound_ctrl:1\n"
    "v_mov_b32_dpp v28, v4 row_shl:8 row_mask:0xf bank_mask:0xf bound_ctrl:1\n"
    "v_mov_b32_dpp v29, v5 row_shl:8 row_mask:0xf bank_mask:0xf bound_ctrl:1\n"
    "v_mov_b32_dpp v30, v6 row_shl:8 row_mask:0xf bank_mask:0xf bound_ctrl:1\n"
    "v_mov_b32_dpp v31, v7 row_shl:8 row_mask:0xf bank_mask:0xf bound_ctrl:1\n"
    "v_mov_b32_dpp v32, v0 row_shl:12 row_mask:0xf bank_mask:0xf bound_ctrl:1\n"
    "v_mov_b32_dpp v33, v1 row_shl:12 row_mask:0xf bank_mask:0xf bound_ctrl:1\n"
    "v_mov_b32_dpp v34, v2 row_shl:12 row_mask:0xf bank_mask:0xf bound_ctrl:1\n"
    "v_mov_b32_dpp v35, v3 row_shl:12 row_mask:0xf bank_mask:0xf bound_ctrl:1\n"
    "v_mov_b32_dpp v36, v4 row_shl:12 row_mask:0xf bank_mask:0xf bound_ctrl:1\n"
    "v_mov_b32_dpp v37, v5 row_shl:12 row_mask:0xf bank_mask:0xf bound_ctrl:1\n"
    "v_mov_b32_dpp v38, v6 row_shl:12 row_mask:0xf bank_mask:0xf bound_ctrl:1\n"
    "v_mov_b32_dpp v39, v7 row_shl:12 row_mask:0xf bank_mask:0xf bound_ctrl:1\n"
    "s_waitcnt vmcnt(1)\n"
    ".Lct_x0_%=:\n"
    "v_readfirstlane_b32 s25, v191\n"
    "v_readfirstlane_b32 s24, v190\n"
    "s_cmp_eq_u32 s25, s27\n"
    "s_cselect_b32 s26, s24, s26\n"
    "s_cbranch_scc0 .Lct_xr0_%=\n"
    ".Lct_xg0_%=:\n"
    "s_add_u32 s43, s26, 97\n"
    "s_add_u32 s44, s26, 105\n"
    ".Lct_mk0x3_%=:\n"
    ".Lct_rt5_%=:\n"
    "s_memrealtime s[22:23]\n"
    "s_waitcnt lgkmcnt(0)\n"
    "s_sub_u32 s24, s43, s22\n"
    "s_cmp_lt_u32 s24, 600\n"
    "s_cbranch_scc1 .Lct_rt5_%=\n"
    "buffer_load_dwordx4 v[40:43], v192, s[0:3], s32 offen offset:0 sc1\n"
    "buffer_load_dwordx4 v[44:47], v192, s[0:3], s32 offen offset:1024 sc1\n"
    "buffer_load_dwordx4 v[48:51], v197, s[0:3], s33 offen offset:0 sc1\n"
    "buffer_load_dwordx4 v[52:55], v197, s[0:3], s33 offen offset:256 sc1\n"
    ".Lct_rt6_%=:\n"
    "s_memrealtime s[22:23]\n"
    "s_waitcnt lgkmcnt(0)\n"
    "s_sub_u32 s24, s44, s22\n"
    "s_cmp_lt_u32 s24, 600\n"
    "s_cbranch_scc1 .Lct_rt6_%=\n"
    "buffer_load_dwordx4 v[72:75], v193, s[0:3], s34 offen sc1\n"
    "buffer_load_dwordx4 v[56:59], v192, s[0:3], s34 offen offset:0 sc1\n"
    "buffer_load_dwordx4 v[60:63], v192, s[0:3], s34 offen offset:1024 sc1\n"
    "buffer_load_dwordx4 v[64:67], v192, s[0:3], s34 offen offset:2048 sc1\n"
    "buffer_load_dwordx4 v[68:71], v192, s[0:3], s34 offen offset:3072 sc1\n"
    "v_mov_b32 v148, s28\n"
    "v_mov_b32 v149, s28\n"
    "v_mov_b32 v150, s28\n"
    "v_mov_b32 v151, s28\n"
    "s_waitcnt vmcnt(9)\n"
    "s_barrier\n"
    "buffer_store_dwordx4 v[148:151], v211, s[0:3], 0 offen sc1\n"
    ".Lct_mk1x6_%=:\n"
    ".Lct_take0_%=:\n"
    "s_waitcnt vmcnt(6)\n"
    "v_max_u32 v184, v40, v44\n"
    "v_max3_u32 v184, v48, v50, v184\n"
    "v_max3_u32 v184, v52, v54, v184\n"
    "s_waitcnt vmcnt(5)\n"
    "v_smfmac_f32_16x16x64_bf16 v[76:79], v[72:75], v[40:47], v194\n"
    "v_max3_u32 v184, v72, v74, v184\n"
    "v_smfmac_f32_16x16x64_bf16 v[80:83], v[72:75], v[48:55], v194\n"
    "s_waitcnt vmcnt(4)\n"
    "v_smfmac_f32_16x16x64_bf16 v[80:83], v[56:59], v[8:15], v194\n"
    "v_max3_u32 v184, v56, v58, v184\n"
    "v_smfmac_f32_16x16x64_bf16 v[76:79], v[56:59], a[0:7], v194\n"
    "s_waitcnt vmcnt(3)\n"
    "v_smfmac_f32_16x16x64_bf16 v[80:83], v[60:63], v[16:23], v194\n"
    "v_max3_u32 v184, v60, v62, v184\n"
    "v_smfmac_f32_16x16x64_bf16 v[76:79], v[60:63], a[8:15], v194\n"
    "s_waitcnt vmcnt(2)\n"
    "v_smfmac_f32_16x16x64_bf16 v[80:83], v[64:67], v[24:31], v194\n"
    "v_max3_u32 v184, v64, v66, v184\n"
    "v_smfmac_f32_16x16x64_bf16 v[76:79], v[64:67], a[16:23], v194\n"
    "s_waitcnt vmcnt(1)\n"
    "v_smfmac_f32_16x16x64_bf16 v[80:83], v[68:71], v[32:39], v194\n"
    "v_max3_u32 v184, v68, v70, v184\n"
    "v_smfmac_f32_16x16x64_bf16 v[76:79], v[68:71], a[24:31], v194\n"
    ".Lct_mk2x7_%=:\n"
    "s_nop 1\n"
    "v_cmp_eq_u32 vcc, -1, v184\n"
    "s_nop 1\n"
    "v_add_f32 v80, v80, v82\n"
    "v_add_f32 v81, v81, v83\n"
    "s_nop 0\n"
    "v_pk_add_f32 v[76:77], v[76:77], v[78:79]\n"
    "ds_write2_b32 v201, v80, v81 offset1:20\n"
    "ds_write2_b32 v200, v76, v77 offset1:20\n"
    "s_cbranch_vccnz .Lct_retry0_%=\n"
    ".Lct_go0_%=:\n"
    "s_waitcnt lgkmcnt(0)\n"
    "s_barrier\n"
    "ds_read_b128 v[164:167], v202 offset:0\n"
    "ds_read_b128 v[168:171], v202 offset:640\n"
    "ds_read_b128 v[172:175], v202 offset:1280\n"
    "ds_read_b128 v[176:179], v202 offset:1920\n"
    "s_waitcnt lgkmcnt(2)\n"
    "v_pk_add_f32 v[164:165], v[164:165], v[168:169]\n"
    "v_pk_add_f32 v[166:167], v[166:167], v[170:171]\n"
    "s_waitcnt lgkmcnt(1)\n"
    "v_pk_add_f32 v[164:165], v[164:165], v[172:173]\n"
    "v_pk_add_f32 v[166:167], v[166:167], v[174:175]\n"
    "s_waitcnt lgkmcnt(0)\n"
    "v_pk_add_f32 v[164:165], v[164:165], v[176:177]\n"
    "v_pk_add_f32 v[166:167], v[166:167], v[178:179]\n"
    "v_cvt_pk_bf16_f32 v164, v164, v165\n"
    "v_cvt_pk_bf16_f32 v165, v166, v167\n"
    "v_min_u32 v164, -2, v164\n"
    "buffer_store_dwordx2 v[164:165], v203, s[0:3], s30 offen sc1\n"
    ".Lct_mk3x8_%=:\n"
    "s_sleep 2\n"
    "v_mov_b64 v[166:167], -1\n"
    "buffer_load_dwordx4 v[132:135], v199, s[0:3], s35 offen offset:0 sc1\n"
    "buffer_load_dwordx4 v[136:139], v199, s[0:3], s37 offen offset:0 sc1\n"
    "buffer_load_dwordx4 v[140:143], v199, s[0:3], s35 offen offset:256 sc1\n"
    "buffer_load_dwordx4 v[144:147], v199, s[0:3], s37 offen offset:256 sc1\n"
    "buffer_load_dwordx4 v[116:119], v198, s[0:3], s35 offen sc1\n"
    "buffer_load_dwordx4 v[84:87], v192, s[0:3], s35 offen offset:0 sc1\n"
    "buffer_load_dwordx4 v[88:91], v192, s[0:3], s35 offen offset:2048 sc1\n"
    "buffer_load_dwordx4 v[120:123], v198, s[0:3], s36 offen sc1\n"
    "buffer_load_dwordx4 v[92:95], v192, s[0:3], s36 offen offset:0 sc1\n"
    "buffer_load_dwordx4 v[96:99], v192, s[0:3], s36 offen offset:2048 sc1\n"
    ".Lct_mk7x9_%=:\n"
    "buffer_load_dwordx4 v[124:127], v198, s[0:3], s37 offen sc1\n"
    "s_waitcnt vmcnt(10)\n"
    "v_max3_u32 v184, v132, v134, v132\n"
    "v_swap_b32 v133, v134\n"
    "s_nop 1\n"
    "v_mfma_f32_16x16x32_bf16 v[148:151], v[132:135], v[40:43], 0\n"
    "v_mfma_f32_16x16x32_bf16 v[152:155], v[132:135], v[48:51], 0\n"
    "buffer_load_dwordx4 v[100:103], v192, s[0:3], s37 offen offset:0 sc1\n"
    "s_waitcnt vmcnt(10)\n"
    "v_max3_u32 v184, v136, v138, v184\n"
    "v_swap_b32 v137, v138\n"
    "s_nop 1\n"
    "v_mfma_f32_16x16x32_bf16 v[148:151], v[136:139], v[44:47], v[148:151]\n"
    "v_mfma_f32_16x16x32_bf16 v[152:155], v[136:139], v[52:55], v[152:155]\n"
    "buffer_load_dwordx4 v[104:107], v192, s[0:3], s37 offen offset:2048 sc1\n"
    "s_waitcnt vmcnt(10)\n"
    "v_max3_u32 v184, v140, v142, v184\n"
    "v_swap_b32 v141, v142\n"
    "s_nop 1\n"
    "v_mfma_f32_16x16x32_bf16 v[156:159], v[140:143], v[40:43], 0\n"
    "v_mfma_f32_16x16x32_bf16 v[160:163], v[140:143], v[48:51], 0\n"
    "buffer_load_dwordx4 v[128:131], v198, s[0:3], s38 offen sc1\n"
    "s_waitcnt vmcnt(10)\n"
    "v_max3_u32 v184, v144, v146, v184\n"
    "v_swap_b32 v145, v146\n"
    "s_nop 1\n"
    "v_mfma_f32_16x16x32_bf16 v[156:159], v[144:147], v[44:47], v[156:159]\n"
    "v_mfma_f32_16x16x32_bf16 v[160:163], v[144:147], v[52:55], v[160:163]\n"
    "buffer_load_dwordx4 v[108:111], v192, s[0:3], s38 offen offset:0 sc1\n"
    "s_waitcnt vmcnt(10)\n"
    "v_max3_u32 v184, v116, v118, v184\n"
    "v_smfmac_f32_16x16x64_bf16 v[148:151], v[116:119], a[0:7], v195\n"
    "v_smfmac_f32_16x16x64_bf16 v[152:155], v[116:119], v[8:15], v195\n"
    "buffer_load_dwordx4 v[112:115], v192, s[0:3], s38 offen offset:2048 sc1\n"
    "s_waitcnt vmcnt(10)\n"
    "v_max3_u32 v184, v84, v86, v184\n"
    "v_swap_b32 v85, v86\n"
    "s_nop 1\n"
    "v_mfma_f32_16x16x32_bf16 v[156:159], v[84:87], a[0:3], v[156:159]\n"
    "v_mfma_f32_16x16x32_bf16 v[160:163], v[84:87], v[8:11], v[160:163]\n"
    "buffer_load_dwordx2 v[186:187], v210, s[4:7], s46 offen sc1\n"
    "s_waitcnt vmcnt(10)\n"
    "v_max3_u32 v184, v88, v90, v184\n"
    "v_swap_b32 v89, v90\n"
    "s_nop 1\n"
    "v_mfma_f32_16x16x32_bf16 v[156:159], v[88:91], a[4:7], v[156:159]\n"
    "v_mfma_f32_16x16x32_bf16 v[160:163], v[88:91], v[12:15], v[160:163]\n"
    "buffer_store_dwordx2 v[166:167], v203, s[0:3], s31 offen sc1\n"
    "s_waitcnt vmcnt(10)\n"
    "v_max3_u32 v184, v120, v122, v184\n"
    "v_smfmac_f32_16x16x64_bf16 v[148:151], v[120:123], a[8:15], v195\n"
    "v_smfmac_f32_16x16x64_bf16 v[152:155], v[120:123], v[16:23], v195\n"
    "buffer_load_dwordx4 a[32:35], v192, s[0:3], s39 offen offset:0 sc1\n"
    "s_waitcnt vmcnt(10)\n"
    "v_max3_u32 v184, v92, v94, v184\n"
    "v_swap_b32 v93, v94\n"
    "s_nop 1\n"
    "v_mfma_f32_16x16x32_bf16 v[156:159], v[92:95], a[8:11], v[156:159]\n"
    "v_mfma_f32_16x16x32_bf16 v[160:163], v[92:95], v[16:19], v[160:163]\n"
    "buffer_load_dwordx4 a[36:39], v192, s[0:3], s39 offen offset:1024 sc1\n"
    "s_waitcnt vmcnt(10)\n"
    "v_max3_u32 v184, v96, v98, v184\n"
    "v_swap_b32 v97, v98\n"
    "s_nop 1\n"
    "v_mfma_f32_16x16x32_bf16 v[156:159], v[96:99], a[12:15], v[156:159]\n"
    "v_mfma_f32_16x16x32_bf16 v[160:163], v[96:99], v[20:23], v[160:163]\n"
    "buffer_load_dwordx4 a[40:43], v192, s[0:3], s39 offen offset:2048 sc1\n"
    "s_waitcnt vmcnt(10)\n"
    "v_max3_u32 v184, v124, v126, v184\n"
    "v_smfmac_f32_16x16x64_bf16 v[148:151], v[124:127], a[16:23], v195\n"
    "v_smfmac_f32_16x16x64_bf16 v[152:155], v[124:127], v[24:31], v195\n"
    "buffer_load_dwordx4 a[44:47], v192, s[0:3], s39 offen offset:3072 sc1\n"
    "s_waitcnt vmcnt(10)\n"
    "v_max3_u32 v184, v100, v102, v184\n"
    "v_swap_b32 v101, v102\n"
    "s_nop 1\n"
    "v_mfma_f32_16x16x32_bf16 v[156:159], v[100:103], a[16:19], v[156:159]\n"
    "v_mfma_f32_16x16x32_bf16 v[160:163], v[100:103], v[24:27], v[160:163]\n"
    "buffer_load_dwordx4 a[48:51], v193, s[0:3], s39 offen offset:0 sc1\n"
    "s_waitcnt vmcnt(10)\n"
    "v_max3_u32 v184, v104, v106, v184\n"
    "v_swap_b32 v105, v106\n"
    "s_nop 1\n"
    "v_mfma_f32_16x16x32_bf16 v[156:159], v[104:107], a[20:23], v[156:159]\n"
    "v_mfma_f32_16x16x32_bf16 v[160:163], v[104:107], v[28:31], v[160:163]\n"
    "buffer_load_dwordx4 a[52:55], v193, s[0:3], s39 offen offset:1024 sc1\n"
    "s_waitcnt vmcnt(10)\n"
    "v_max3_u32 v184, v128, v130, v184\n"
    "v_smfmac_f32_16x16x64_bf16 v[148:151], v[128:131], a[24:31], v195\n"
    "v_smfmac_f32_16x16x64_bf16 v[152:155], v[128:131], v[32:39], v195\n"
    "buffer_load_dwordx4 a[56:59], v193, s[0:3], s39 offen offset:2048 sc1\n"
    "s_waitcnt vmcnt(10)\n"
    "v_max3_u32 v184, v108, v110, v184\n"
    "v_swap_b32 v109, v110\n"
    "s_nop 1\n"
    "v_mfma_f32_16x16x32_bf16 v[156:159], v[108:111], a[24:27], v[156:159]\n"
    "v_mfma_f32_16x16x32_bf16 v[160:163], v[108:111], v[32:35], v[160:163]\n"
    "buffer_load_dwordx4 a[60:63], v193, s[0:3], s39 offen offset:3072 sc1\n"
    "s_waitcnt vmcnt(10)\n"
    "v_max3_u32 v184, v112, v114, v184\n"
    "v_swap_b32 v113, v114\n"
    "s_nop 1\n"
    "v_mfma_f32_16x16x32_bf16 v[156:159], v[112:115], a[28:31], v[156:159]\n"
    "v_mfma_f32_16x16x32_bf16 v[160:163], v[112:115], v[36:39], v[160:163]\n"
    "v_cmp_eq_u32 vcc, -1, v184\n"
    "s_nop 0\n"
    "s_cbranch_vccnz .Lct_tr0_%=\n"
    ".Lct_tg0_%=:\n"
    ".Lct_mk4x10_%=:\n"
    "ds_write2_b32 v204, v148, v149 offset0:0 offset1:20\n"
    "ds_write2_b32 v204, v150, v151 offset0:40 offset1:60\n"
    "ds_write2_b32 v205, v152, v153 offset0:0 offset1:20\n"
    "ds_write2_b32 v205, v154, v155 offset0:40 offset1:60\n"
    "ds_write2_b32 v206, v156, v157 offset0:0 offset1:20\n"
    "ds_write2_b32 v206, v158, v159 offset0:40 offset1:60\n"
    "ds_write2_b32 v207, v160, v161 offset0:0 offset1:20\n"
    "ds_write2_b32 v207, v162, v163 offset0:40 offset1:60\n"
    "s_mov_b32 s42, s45\n"
    "buffer_load_dwordx4 v[0:3], v196, s[0:3], s40 offen offset:0 sc1\n"
    "buffer_load_dwordx4 v[4:7], v196, s[0:3], s40 offen offset:1024 sc1\n"
    "s_add_u32 s28, s28, 1\n"
    "s_add_u32 s27, s28, 1\n"
    "s_add_u32 s29, s28, 0\n"
    "s_and_b32 s29, s29, 3\n"
    "s_lshl_b32 s29, s29, 22\n"
    "s_add_u32 s30, s28, 1\n"
    "s_and_b32 s30, s30, 3\n"
    "s_lshl_b32 s30, s30, 22\n"
    "s_add_u32 s31, s28, 3\n"
    "s_and_b32 s31, s31, 3\n"
    "s_lshl_b32 s31, s31, 22\n"
    "s_and_b32 s41, s28, 63\n"
    "s_lshl_b32 s41, s41, 7\n"
    "s_add_u32 s32, s12, s29\n"
    "s_add_u32 s33, s13, s29\n"
    "s_add_u32 s34, s15, s29\n"
    "s_add_u32 s35, s14, s29\n"
    "s_add_u32 s39, s10, s30\n"
    "s_add_u32 s40, s11, s30\n"
    "s_add_u32 s41, s16, s41\n"
    "s_add_u32 s36, s35, 4096\n"
    "s_add_u32 s37, s35, 16384\n"
    "s_add_u32 s38, s35, 20480\n"
    "s_add_u32 s43, s26, 304\n"
    "s_lshl_b32 s45, s28, 19\n"
    "s_lshl_b32 s25, s28, 11\n"
    "s_xor_b32 s25, s25, s8\n"
    "s_and_b32 s25, s25, 0x2000\n"
    "s_add_u32 s45, s45, s25\n"
    "s_add_u32 s45, s45, s9\n"
    "s_mul_i32 s46, s28, 40960\n"
    "s_cmp_eq_u32 s8, 0\n"
    "s_cselect_b32 s46, s46, s45\n"
    "s_cmp_eq_u32 s28, 63\n"
    "s_cselect_b32 s39, 2130706432, s39\n"
    "s_cselect_b32 s40, 2130706432, s40\n"
    "v_mov_b64 v[76:77], 0\n"
    "v_mov_b64 v[78:79], 0\n"
    "v_mov_b64 v[80:81], 0\n"
    "v_mov_b64 v[82:83], 0\n"
    "s_waitcnt vmcnt(11)\n"
    "v_lshlrev_b32 v148, 16, v186\n"
    "v_and_b32 v149, 0xffff0000, v186\n"
    "v_lshlrev_b32 v150, 16, v187\n"
    "v_and_b32 v151, 0xffff0000, v187\n"
    "s_memrealtime s[22:23]\n"
    "s_waitcnt lgkmcnt(0)\n"
    "s_barrier\n"
    "s_sub_u32 s24, s43, s22\n"
    "s_cmp_gt_i32 s24, 10\n"
    "s_cselect_b32 s24, 2130706432, s41\n"
    "buffer_load_dwordx2 v[190:191], off, s[0:3], s24 sc1\n"
    ".Lct_mk6x11_%=:\n"
    "ds_read_b128 v[164:167], v208 offset:0\n"
    "ds_read_b128 v[168:171], v208 offset:2560\n"
    "ds_read_b128 v[172:175], v208 offset:5120\n"
    "ds_read_b128 v[176:179], v208 offset:7680\n"
    "s_waitcnt lgkmcnt(2)\n"
    "v_pk_add_f32 v[164:165], v[164:165], v[168:169]\n"
    "v_pk_add_f32 v[166:167], v[166:167], v[170:171]\n"
    "s_waitcnt lgkmcnt(1)\n"
    "v_pk_add_f32 v[164:165], v[164:165], v[172:173]\n"
    "v_pk_add_f32 v[166:167], v[166:167], v[174:175]\n"
    "s_waitcnt lgkmcnt(0)\n"
    "v_pk_add_f32 v[164:165], v[164:165], v[176:177]\n"
    "v_pk_add_f32 v[166:167], v[166:167], v[178:179]\n"
    "v_cvt_pk_bf16_f32 v164, v164, v165\n"
    "v_cvt_pk_bf16_f32 v165, v166, v167\n"
    "v_lshlrev_b32 v168, 16, v164\n"
    "v_and_b32 v169, 0xffff0000, v164\n"
    "v_lshlrev_b32 v170, 16, v165\n"
    "v_and_b32 v171, 0xffff0000, v165\n"
    "v_pk_add_f32 v[168:169], v[168:169], v[148:149]\n"
    "v_pk_add_f32 v[170:171], v[170:171], v[150:151]\n"
    "v_cvt_pk_bf16_f32 v164, v168, v169\n"
    "v_cvt_pk_bf16_f32 v165, v170, v171\n"
    "buffer_store_dwordx2 v[164:165], v209, s[0:3], s42 offen sc1\n"
    ".Lct_mk5x12_%=:\n"
    "s_waitcnt vmcnt(2)\n"
    "v_mov_b64 v[8:9], v[0:1]\n"
    "v_mov_b64 v[10:11], v[2:3]\n"
    "v_mov_b64 v[12:13], v[4:5]\n"
    "v_mov_b64 v[14:15], v[6:7]\n"
    "v_mov_b32_dpp v16, v0 row_shl:4 row_mask:0xf bank_mask:0xf bound_ctrl:1\n"
    "v_mov_b32_dpp v17, v1 row_shl:4 row_mask:0xf bank_mask:0xf bound_ctrl:1\n"
    "v_mov_b32_dpp v18, v2 row_shl:4 row_mask:0xf bank_mask:0xf bound_ctrl:1\n"
    "v_mov_b32_dpp v19, v3 row_shl:4 row_mask:0xf bank_mask:0xf bound_ctrl:1\n"
    "v_mov_b32_dpp v20, v4 row_shl:4 row_mask:0xf bank_mask:0xf bound_ctrl:1\n"
    "v_mov_b32_dpp v21, v5 row_shl:4 row_mask:0xf bank_mask:0xf bound_ctrl:1\n"
    "v_mov_b32_dpp v22, v6 row_shl:4 row_mask:0xf bank_mask:0xf bound_ctrl:1\n"
    "v_mov_b32_dpp v23, v7 row_shl:4 row_mask:0xf bank_mask:0xf bound_ctrl:1\n"
    "v_mov_b32_dpp v24, v0 row_shl:8 row_mask:0xf bank_mask:0xf bound_ctrl:1\n"
    "v_mov_b32_dpp v25, v1 row_shl:8 row_mask:0xf bank_mask:0xf bound_ctrl:1\n"
    "v_mov_b32_dpp v26, v2 row_shl:8 row_mask:0xf bank_mask:0xf bound_ctrl:1\n"
    "v_mov_b32_dpp v27, v3 row_shl:8 row_mask:0xf bank_mask:0xf bound_ctrl:1\n"
    "v_mov_b32_dpp v28, v4 row_shl:8 row_mask:0xf bank_mask:0xf bound_ctrl:1\n"
    "v_mov_b32_dpp v29, v5 row_shl:8 row_mask:0xf bank_mask:0xf bound_ctrl:1\n"
    "v_mov_b32_dpp v30, v6 row_shl:8 row_mask:0xf bank_mask:0xf bound_ctrl:1\n"
    "v_mov_b32_dpp v31, v7 row_shl:8 row_mask:0xf bank_mask:0xf bound_ctrl:1\n"
    "v_mov_b32_dpp v32, v0 row_shl:12 row_mask:0xf bank_mask:0xf bound_ctrl:1\n"
    "v_mov_b32_dpp v33, v1 row_shl:12 row_mask:0xf bank_mask:0xf bound_ctrl:1\n"
    "v_mov_b32_dpp v34, v2 row_shl:12 row_mask:0xf bank_mask:0xf bound_ctrl:1\n"
    "v_mov_b32_dpp v35, v3 row_shl:12 row_mask:0xf bank_mask:0xf bound_ctrl:1\n"
    "v_mov_b32_dpp v36, v4 row_shl:12 row_mask:0xf bank_mask:0xf bound_ctrl:1\n"
    "v_mov_b32_dpp v37, v5 row_shl:12 row_mask:0xf bank_mask:0xf bound_ctrl:1\n"
    "v_mov_b32_dpp v38, v6 row_shl:12 row_mask:0xf bank_mask:0xf bound_ctrl:1\n"
    "v_mov_b32_dpp v39, v7 row_shl:12 row_mask:0xf bank_mask:0xf bound_ctrl:1\n"
    "s_waitcnt vmcnt(1)\n"
    ".Lct_x1_%=:\n"
    "v_readfirstlane_b32 s25, v191\n"
    "v_readfirstlane_b32 s24, v190\n"
    "s_cmp_eq_u32 s25, s27\n"
    "s_cselect_b32 s26, s24, s26\n"
    "s_cbranch_scc0 .Lct_xr1_%=\n"
    ".Lct_xg1_%=:\n"
    "s_add_u32 s43, s26, 97\n"
    "s_add_u32 s44, s26, 105\n"
    ".Lct_mk0x17_%=:\n"
    ".Lct_rt19_%=:\n"
    "s_memrealtime s[22:23]\n"
    "s_waitcnt lgkmcnt(0)\n"
    "s_sub_u32 s24, s43, s22\n"
    "s_cmp_lt_u32 s24, 600\n"
    "s_cbranch_scc1 .Lct_rt19_%=\n"
    "buffer_load_dwordx4 v[40:43], v192, s[0:3], s32 offen offset:0 sc1\n"
    "buffer_load_dwordx4 v[44:47], v192, s[0:3], s32 offen offset:1024 sc1\n"
    "buffer_load_dwordx4 v[48:51], v197, s[0:3], s33 offen offset:0 sc1\n"
    "buffer_load_dwordx4 v[52:55], v197, s[0:3], s33 offen offset:256 sc1\n"
    ".Lct_rt20_%=:\n"
    "s_memrealtime s[22:23]\n"
    "s_waitcnt lgkmcnt(0)\n"
    "s_sub_u32 s24, s44, s22\n"
    "s_cmp_lt_u32 s24, 600\n"
    "s_cbranch_scc1 .Lct_rt20_%=\n"
    "buffer_load_dwordx4 v[72:75], v193, s[0:3], s34 offen sc1\n"
    "buffer_load_dwordx4 v[56:59], v192, s[0:3], s34 offen offset:0 sc1\n"
    "buffer_load_dwordx4 v[60:63], v192, s[0:3], s34 offen offset:1024 sc1\n"
    "buffer_load_dwordx4 v[64:67], v192, s[0:3], s34 offen offset:2048 sc1\n"
    "buffer_load_dwordx4 v[68:71], v192, s[0:3], s34 offen offset:3072 sc1\n"
    "v_mov_b32 v148, s28\n"
    "v_mov_b32 v149, s28\n"
    "v_mov_b32 v150, s28\n"
    "v_mov_b32 v151, s28\n"
    "s_waitcnt vmcnt(9)\n"
    "s_barrier\n"
    "buffer_store_dwordx4 v[148:151], v211, s[0:3], 0 offen sc1\n"
    ".Lct_mk1x20_%=:\n"
    ".Lct_take1_%=:\n"
    "s_waitcnt vmcnt(6)\n"
    "v_max_u32 v184, v40, v44\n"
    "v_max3_u32 v184, v48, v50, v184\n"
    "v_max3_u32 v184, v52, v54, v184\n"
    "s_waitcnt vmcnt(5)\n"
    "v_smfmac_f32_16x16x64_bf16 v[76:79], v[72:75], v[40:47], v194\n"
    "v_max3_u32 v184, v72, v74, v184\n"
    "v_smfmac_f32_16x16x64_bf16 v[80:83], v[72:75], v[48:55], v194\n"
    "s_waitcnt vmcnt(4)\n"
    "v_smfmac_f32_16x16x64_bf16 v[80:83], v[56:59], v[8:15], v194\n"
    "v_max3_u32 v184, v56, v58, v184\n"
    "v_smfmac_f32_16x16x64_bf16 v[76:79], v[56:59], a[32:39], v194\n"
    "s_waitcnt vmcnt(3)\n"
    "v_smfmac_f32_16x16x64_bf16 v[80:83], v[60:63], v[16:23], v194\n"
    "v_max3_u32 v184, v60, v62, v184\n"
    "v_smfmac_f32_16x16x64_bf16 v[76:79], v[60:63], a[40:47], v194\n"
    "s_waitcnt vmcnt(2)\n"
    "v_smfmac_f32_16x16x64_bf16 v[80:83], v[64:67], v[24:31], v194\n"
    "v_max3_u32 v184, v64, v66, v184\n"
    "v_smfmac_f32_16x16x64_bf16 v[76:79], v[64:67], a[48:55], v194\n"
    "s_waitcnt vmcnt(1)\n"
    "v_smfmac_f32_16x16x64_bf16 v[80:83], v[68:71], v[32:39], v194\n"
    "v_max3_u32 v184, v68, v70, v184\n"
    "v_smfmac_f32_16x16x64_bf16 v[76:79], v[68:71], a[56:63], v194\n"
    ".Lct_mk2x21_%=:\n"
    "s_nop 1\n"
    "v_cmp_eq_u32 vcc, -1, v184\n"
    "s_nop 1\n"
    "v_add_f32 v80, v80, v82\n"
    "v_add_f32 v81, v81, v83\n"
    "s_nop 0\n"
    "v_pk_add_f32 v[76:77], v[76:77], v[78:79]\n"
    "ds_write2_b32 v201, v80, v81 offset1:20\n"
    "ds_write2_b32 v200, v76, v77 offset1:20\n"
    "s_cbranch_vccnz .Lct_retry1_%=\n"
    ".Lct_go1_%=:\n"
    "s_waitcnt lgkmcnt(0)\n"
    "s_barrier\n"
    "ds_read_b128 v[164:167], v202 offset:0\n"
    "ds_read_b128 v[168:171], v202 offset:640\n"
    "ds_read_b128 v[172:175], v202 offset:1280\n"
    "ds_read_b128 v[176:179], v202 offset:1920\n"
    "s_waitcnt lgkmcnt(2)\n"
    "v_pk_add_f32 v[164:165], v[164:165], v[168:169]\n"
    "v_pk_add_f32 v[166:167], v[166:167], v[170:171]\n"
    "s_waitcnt lgkmcnt(1)\n"
    "v_pk_add_f32 v[164:165], v[164:165], v[172:173]\n"
    "v_pk_add_f32 v[166:167], v[166:167], v[174:175]\n"
    "s_waitcnt lgkmcnt(0)\n"
    "v_pk_add_f32 v[164:165], v[164:165], v[176:177]\n"
    "v_pk_add_f32 v[166:167], v[166:167], v[178:179]\n"
    "v_cvt_pk_bf16_f32 v164, v164, v165\n"
    "v_cvt_pk_bf16_f32 v165, v166, v167\n"
    "v_min_u32 v164, -2, v164\n"
    "buffer_store_dwordx2 v[164:165], v203, s[0:3], s30 offen sc1\n"
    ".Lct_mk3x22_%=:\n"
    "s_sleep 2\n"
    "v_mov_b64 v[166:167], -1\n"
    "buffer_load_dwordx4 v[132:135], v199, s[0:3], s35 offen offset:0 sc1\n"
    "buffer_load_dwordx4 v[136:139], v199, s[0:3], s37 offen offset:0 sc1\n"
    "buffer_load_dwordx4 v[140:143], v199, s[0:3], s35 offen offset:256 sc1\n"
    "buffer_load_dwordx4 v[144:147], v199, s[0:3], s37 offen offset:256 sc1\n"
    "buffer_load_dwordx4 v[116:119], v198, s[0:3], s35 offen sc1\n"
    "buffer_load_dwordx4 v[84:87], v192, s[0:3], s35 offen offset:0 sc1\n"
    "buffer_load_dwordx4 v[88:91], v192, s[0:3], s35 offen offset:2048 sc1\n"
    "buffer_load_dwordx4 v[120:123], v198, s[0:3], s36 offen sc1\n"
    "buffer_load_dwordx4 v[92:95], v192, s[0:3], s36 offen offset:0 sc1\n"
    "buffer_load_dwordx4 v[96:99], v192, s[0:3], s36 offen offset:2048 sc1\n"
    ".Lct_mk7x23_%=:\n"
    "buffer_load_dwordx4 v[124:127], v198, s[0:3], s37 offen sc1\n"
    "s_waitcnt vmcnt(10)\n"
    "v_max3_u32 v184, v132, v134, v132\n"
    "v_swap_b32 v133, v134\n"
    "s_nop 1\n"
    "v_mfma_f32_16x16x32_bf16 v[148:151], v[132:135], v[40:43], 0\n"
    "v_mfma_f32_16x16x32_bf16 v[152:155], v[132:135], v[48:51], 0\n"
    "buffer_load_dwordx4 v[100:103], v192, s[0:3], s37 offen offset:0 sc1\n"
    "s_waitcnt vmcnt(10)\n"
    "v_max3_u32 v184, v136, v138, v184\n"
    "v_swap_b32 v137, v138\n"
    "s_nop 1\n"
    "v_mfma_f32_16x16x32_bf16 v[148:151], v[136:139], v[44:47], v[148:151]\n"
    "v_mfma_f32_16x16x32_bf16 v[152:155], v[136:139], v[52:55], v[152:155]\n"
    "buffer_load_dwordx4 v[104:107], v192, s[0:3], s37 offen offset:2048 sc1\n"
    "s_waitcnt vmcnt(10)\n"
    "v_max3_u32 v184, v140, v142, v184\n"
    "v_swap_b32 v141, v142\n"
    "s_nop 1\n"
    "v_mfma_f32_16x16x32_bf16 v[156:159], v[140:143], v[40:43], 0\n"
    "v_mfma_f32_16x16x32_bf16 v[160:163], v[140:143], v[48:51], 0\n"
    "buffer_load_dwordx4 v[128:131], v198, s[0:3], s38 offen sc1\n"
    "s_waitcnt vmcnt(10)\n"
    "v_max3_u32 v184, v144, v146, v184\n"
    "v_swap_b32 v145, v146\n"
    "s_nop 1\n"
    "v_mfma_f32_16x16x32_bf16 v[156:159], v[144:147], v[44:47], v[156:159]\n"
    "v_mfma_f32_16x16x32_bf16 v[160:163], v[144:147], v[52:55], v[160:163]\n"
    "buffer_load_dwordx4 v[108:111], v192, s[0:3], s38 offen offset:0 sc1\n"
    "s_waitcnt vmcnt(10)\n"
    "v_max3_u32 v184, v116, v118, v184\n"
    "v_smfmac_f32_16x16x64_bf16 v[148:151], v[116:119], a[32:39], v195\n"
    "v_smfmac_f32_16x16x64_bf16 v[152:155], v[116:119], v[8:15], v195\n"
    "buffer_load_dwordx4 v[112:115], v192, s[0:3], s38 offen offset:2048 sc1\n"
    "s_waitcnt vmcnt(10)\n"
    "v_max3_u32 v184, v84, v86, v184\n"
    "v_swap_b32 v85, v86\n"
    "s_nop 1\n"
    "v_mfma_f32_16x16x32_bf16 v[156:159], v[84:87], a[32:35], v[156:159]\n"
    "v_mfma_f32_16x16x32_bf16 v[160:163], v[84:87], v[8:11], v[160:163]\n"
    "buffer_load_dwordx2 v[186:187], v210, s[4:7], s46 offen sc1\n"
    "s_waitcnt vmcnt(10)\n"
    "v_max3_u32 v184, v88, v90, v184\n"
    "v_swap_b32 v89, v90\n"
    "s_nop 1\n"
    "v_mfma_f32_16x16x32_bf16 v[156:159], v[88:91], a[36:39], v[156:159]\n"
    "v_mfma_f32_16x16x32_bf16 v[160:163], v[88:91], v[12:15], v[160:163]\n"
    "buffer_store_dwordx2 v[166:167], v203, s[0:3], s31 offen sc1\n"
    "s_waitcnt vmcnt(10)\n"
    "v_max3_u32 v184, v120, v122, v184\n"
    "v_smfmac_f32_16x16x64_bf16 v[148:151], v[120:123], a[40:47], v195\n"
    "v_smfmac_f32_16x16x64_bf16 v[152:155], v[120:123], v[16:23], v195\n"
    "buffer_load_dwordx4 a[0:3], v192, s[0:3], s39 offen offset:0 sc1\n"
    "s_waitcnt vmcnt(10)\n"
    "v_max3_u32 v184, v92, v94, v184\n"
    "v_swap_b32 v93, v94\n"
    "s_nop 1\n"
    "v_mfma_f32_16x16x32_bf16 v[156:159], v[92:95], a[40:43], v[156:159]\n"
    "v_mfma_f32_16x16x32_bf16 v[160:163], v[92:95], v[16:19], v[160:163]\n"
    "buffer_load_dwordx4 a[4:7], v192, s[0:3], s39 offen offset:1024 sc1\n"
    "s_waitcnt vmcnt(10)\n"
    "v_max3_u32 v184, v96, v98, v184\n"
    "v_swap_b32 v97, v98\n"
    "s_nop 1\n"
    "v_mfma_f32_16x16x32_bf16 v[156:159], v[96:99], a[44:47], v[156:159]\n"
    "v_mfma_f32_16x16x32_bf16 v[160:163], v[96:99], v[20:23], v[160:163]\n"
    "buffer_load_dwordx4 a[8:11], v192, s[0:3], s39 offen offset:2048 sc1\n"
    "s_waitcnt vmcnt(10)\n"
    "v_max3_u32 v184, v124, v126, v184\n"
    "v_smfmac_f32_16x16x64_bf16 v[148:151], v[124:127], a[48:55], v195\n"
    "v_smfmac_f32_16x16x64_bf16 v[152:155], v[124:127], v[24:31], v195\n"
    "buffer_load_dwordx4 a[12:15], v192, s[0:3], s39 offen offset:3072 sc1\n"
    "s_waitcnt vmcnt(10)\n"
    "v_max3_u32 v184, v100, v102, v184\n"
    "v_swap_b32 v101, v102\n"
    "s_nop 1\n"
    "v_mfma_f32_16x16x32_bf16 v[156:159], v[100:103], a[48:51], v[156:159]\n"
    "v_mfma_f32_16x16x32_bf16 v[160:163], v[100:103], v[24:27], v[160:163]\n"
    "buffer_load_dwordx4 a[16:19], v193, s[0:3], s39 offen offset:0 sc1\n"
    "s_waitcnt vmcnt(10)\n"
    "v_max3_u32 v184, v104, v106, v184\n"
    "v_swap_b32 v105, v106\n"
    "s_nop 1\n"
    "v_mfma_f32_16x16x32_bf16 v[156:159], v[104:107], a[52:55], v[156:159]\n"
    "v_mfma_f32_16x16x32_bf16 v[160:163], v[104:107], v[28:31], v[160:163]\n"
    "buffer_load_dwordx4 a[20:23], v193, s[0:3], s39 offen offset:1024 sc1\n"
    "s_waitcnt vmcnt(10)\n"
    "v_max3_u32 v184, v128, v130, v184\n"
    "v_smfmac_f32_16x16x64_bf16 v[148:151], v[128:131], a[56:63], v195\n"
    "v_smfmac_f32_16x16x64_bf16 v[152:155], v[128:131], v[32:39], v195\n"
    "buffer_load_dwordx4 a[24:27], v193, s[0:3], s39 offen offset:2048 sc1\n"
    "s_waitcnt vmcnt(10)\n"
    "v_max3_u32 v184, v108, v110, v184\n"
    "v_swap_b32 v109, v110\n"
    "s_nop 1\n"
    "v_mfma_f32_16x16x32_bf16 v[156:159], v[108:111], a[56:59], v[156:159]\n"
    "v_mfma_f32_16x16x32_bf16 v[160:163], v[108:111], v[32:35], v[160:163]\n"
    "buffer_load_dwordx4 a[28:31], v193, s[0:3], s39 offen offset:3072 sc1\n"
    "s_waitcnt vmcnt(10)\n"
    "v_max3_u32 v184, v112, v114, v184\n"
    "v_swap_b32 v113, v114\n"
    "s_nop 1\n"
    "v_mfma_f32_16x16x32_bf16 v[156:159], v[112:115], a[60:63], v[156:159]\n"
    "v_mfma_f32_16x16x32_bf16 v[160:163], v[112:115], v[36:39], v[160:163]\n"
    "v_cmp_eq_u32 vcc, -1, v184\n"
    "s_nop 0\n"
    "s_cbranch_vccnz .Lct_tr1_%=\n"
    ".Lct_tg1_%=:\n"
    ".Lct_mk4x24_%=:\n"
    "ds_write2_b32 v204, v148, v149 offset0:0 offset1:20\n"
    "ds_write2_b32 v204, v150, v151 offset0:40 offset1:60\n"
    "ds_write2_b32 v205, v152, v153 offset0:0 offset1:20\n"
    "ds_write2_b32 v205, v154, v155 offset0:40 offset1:60\n"
    "ds_write2_b32 v206, v156, v157 offset0:0 offset1:20\n"
    "ds_write2_b32 v206, v158, v159 offset0:40 offset1:60\n"
    "ds_write2_b32 v207, v160, v161 offset0:0 offset1:20\n"
    "ds_write2_b32 v207, v162, v163 offset0:40 offset1:60\n"
    "s_mov_b32 s42, s45\n"
    "buffer_load_dwordx4 v[0:3], v196, s[0:3], s40 offen offset:0 sc1\n"
    "buffer_load_dwordx4 v[4:7], v196, s[0:3], s40 offen offset:1024 sc1\n"
    "s_add_u32 s28, s28, 1\n"
    "s_add_u32 s27, s28, 1\n"
    "s_add_u32 s29, s28, 0\n"
    "s_and_b32 s29, s29, 3\n"
    "s_lshl_b32 s29, s29, 22\n"
    "s_add_u32 s30, s28, 1\n"
    "s_and_b32 s30, s30, 3\n"
    "s_lshl_b32 s30, s30, 22\n"
    "s_add_u32 s31, s28, 3\n"
    "s_and_b32 s31, s31, 3\n"
    "s_lshl_b32 s31, s31, 22\n"
    "s_and_b32 s41, s28, 63\n"
    "s_lshl_b32 s41, s41, 7\n"
    "s_add_u32 s32, s12, s29\n"
    "s_add_u32 s33, s13, s29\n"
    "s_add_u32 s34, s15, s29\n"
    "s_add_u32 s35, s14, s29\n"
    "s_add_u32 s39, s10, s30\n"
    "s_add_u32 s40, s11, s30\n"
    "s_add_u32 s41, s16, s41\n"
    "s_add_u32 s36, s35, 4096\n"
    "s_add_u32 s37, s35, 16384\n"
    "s_add_u32 s38, s35, 20480\n"
    "s_add_u32 s43, s26, 304\n"
    "s_lshl_b32 s45, s28, 19\n"
    "s_lshl_b32 s25, s28, 11\n"
    "s_xor_b32 s25, s25, s8\n"
    "s_and_b32 s25, s25, 0x2000\n"
    "s_add_u32 s45, s45, s25\n"
    "s_add_u32 s45, s45, s9\n"
    "s_mul_i32 s46, s28, 40960\n"
    "s_cmp_eq_u32 s8, 0\n"
    "s_cselect_b32 s46, s46, s45\n"
    "s_cmp_eq_u32 s28, 63\n"
    "s_cselect_b32 s39, 2130706432, s39\n"
    "s_cselect_b32 s40, 2130706432, s40\n"
    "v_mov_b64 v[76:77], 0\n"
    "v_mov_b64 v[78:79], 0\n"
    "v_mov_b64 v[80:81], 0\n"
    "v_mov_b64 v[82:83], 0\n"
    "s_waitcnt vmcnt(11)\n"
    "v_lshlrev_b32 v148, 16, v186\n"
    "v_and_b32 v149, 0xffff0000, v186\n"
    "v_lshlrev_b32 v150, 16, v187\n"
    "v_and_b32 v151, 0xffff0000, v187\n"
    "s_memrealtime s[22:23]\n"
    "s_waitcnt lgkmcnt(0)\n"
    "s_barrier\n"
    "s_sub_u32 s24, s43, s22\n"
    "s_cmp_gt_i32 s24, 10\n"
    "s_cselect_b32 s24, 2130706432, s41\n"
    "buffer_load_dwordx2 v[190:191], off, s[0:3], s24 sc1\n"
    ".Lct_mk6x25_%=:\n"
    "ds_read_b128 v[164:167], v208 offset:0\n"
    "ds_read_b128 v[168:171], v208 offset:2560\n"
    "ds_read_b128 v[172:175], v208 offset:5120\n"
    "ds_read_b128 v[176:179], v208 offset:7680\n"
    "s_waitcnt lgkmcnt(2)\n"
    "v_pk_add_f32 v[164:165], v[164:165], v[168:169]\n"
    "v_pk_add_f32 v[166:167], v[166:167], v[170:171]\n"
    "s_waitcnt lgkmcnt(1)\n"
    "v_pk_add_f32 v[164:165], v[164:165], v[172:173]\n"
    "v_pk_add_f32 v[166:167], v[166:167], v[174:175]\n"
    "s_waitcnt lgkmcnt(0)\n"
    "v_pk_add_f32 v[164:165], v[164:165], v[176:177]\n"
    "v_pk_add_f32 v[166:167], v[166:167], v[178:179]\n"
    "v_cvt_pk_bf16_f32 v164, v164, v165\n"
    "v_cvt_pk_bf16_f32 v165, v166, v167\n"
    "v_lshlrev_b32 v168, 16, v164\n"
    "v_and_b32 v169, 0xffff0000, v164\n"
    "v_lshlrev_b32 v170, 16, v165\n"
    "v_and_b32 v171, 0xffff0000, v165\n"
    "v_pk_add_f32 v[168:169], v[168:169], v[148:149]\n"
    "v_pk_add_f32 v[170:171], v[170:171], v[150:151]\n"
    "v_cvt_pk_bf16_f32 v164, v168, v169\n"
    "v_cvt_pk_bf16_f32 v165, v170, v171\n"
    "buffer_store_dwordx2 v[164:165], v209, s[0:3], s42 offen sc1\n"
    ".Lct_mk5x26_%=:\n"
    "s_cmp_lt_u32 s28, 64\n"
    "s_cbranch_scc1 .Lct_top_%=\n"
    "v_mov_b32 v148, s28\n"
    "v_mov_b32 v149, s28\n"
    "v_mov_b32 v150, s28\n"
    "v_mov_b32 v151, s28\n"
    "s_waitcnt vmcnt(0)\n"
    "s_barrier\n"
    "buffer_store_dwordx4 v[148:151], v211, s[0:3], 0 offen sc1\n"
    "s_waitcnt vmcnt(0) lgkmcnt(0)\n"
    "s_endpgm\n"
    ".Lct_xr0_%=:\n"
    "s_add_u32 s20, s20, 1\n"
    "s_cmp_lt_u32 s20, 0x40000\n"
    "s_cbranch_scc1 .Lct_sp14_%=\n"
    "v_mov_b32 v184, 42\n"
    "s_mov_b32 s24, 33728\n"
    "buffer_store_dword v184, off, s[0:3], s24 sc1\n"
    "s_waitcnt vmcnt(0)\n"
    "s_branch .Lct_xg0_%=\n"
    ".Lct_sp14_%=:\n"
    ".Lct_rt15_%=:\n"
    "s_memrealtime s[22:23]\n"
    "s_waitcnt lgkmcnt(0)\n"
    "s_sub_u32 s24, s43, s22\n"
    "s_cmp_lt_u32 s24, 600\n"
    "s_cbranch_scc1 .Lct_rt15_%=\n"
    "s_add_u32 s43, s22, 44\n"
    "buffer_load_dwordx2 v[190:191], off, s[0:3], s41 sc1\n"
    "s_waitcnt vmcnt(0)\n"
    "s_branch .Lct_x0_%=\n"
    ".Lct_retry0_%=:\n"
    "s_add_u32 s20, s20, 1\n"
    "s_cmp_lt_u32 s20, 0x40000\n"
    "s_cbranch_scc1 .Lct_sp16_%=\n"
    "v_mov_b32 v184, 3\n"
    "s_mov_b32 s24, 33728\n"
    "buffer_store_dword v184, off, s[0:3], s24 sc1\n"
    "s_waitcnt vmcnt(0)\n"
    "s_branch .Lct_go0_%=\n"
    ".Lct_sp16_%=:\n"
    "s_waitcnt vmcnt(0) lgkmcnt(0)\n"
    "v_mov_b64 v[76:77], 0\n"
    "v_mov_b64 v[78:79], 0\n"
    "v_mov_b64 v[80:81], 0\n"
    "v_mov_b64 v[82:83], 0\n"
    "buffer_load_dwordx4 v[40:43], v192, s[0:3], s32 offen offset:0 sc1\n"
    "buffer_load_dwordx4 v[44:47], v192, s[0:3], s32 offen offset:1024 sc1\n"
    "buffer_load_dwordx4 v[48:51], v197, s[0:3], s33 offen offset:0 sc1\n"
    "buffer_load_dwordx4 v[52:55], v197, s[0:3], s33 offen offset:256 sc1\n"
    "buffer_load_dwordx4 v[72:75], v193, s[0:3], s34 offen sc1\n"
    "buffer_load_dwordx4 v[56:59], v192, s[0:3], s34 offen offset:0 sc1\n"
    "buffer_load_dwordx4 v[60:63], v192, s[0:3], s34 offen offset:1024 sc1\n"
    "buffer_load_dwordx4 v[64:67], v192, s[0:3], s34 offen offset:2048 sc1\n"
    "buffer_load_dwordx4 v[68:71], v192, s[0:3], s34 offen offset:3072 sc1\n"
    "buffer_store_dwordx4 v[148:151], v211, s[0:3], 0 offen sc1\n"
    "s_waitcnt vmcnt(0)\n"
    "s_branch .Lct_take0_%=\n"
    ".Lct_tr0_%=:\n"
    "s_add_u32 s20, s20, 1\n"
    "s_cmp_lt_u32 s20, 0x40000\n"
    "s_cbranch_scc1 .Lct_sp17_%=\n"
    "v_mov_b32 v184, 4\n"
    "s_mov_b32 s24, 33728\n"
    "buffer_store_dword v184, off, s[0:3], s24 sc1\n"
    "s_waitcnt vmcnt(0)\n"
    "s_branch .Lct_tg0_%=\n"
    ".Lct_sp17_%=:\n"
    "buffer_load_dwordx4 v[132:135], v199, s[0:3], s35 offen offset:0 sc1\n"
    "buffer_load_dwordx4 v[136:139], v199, s[0:3], s37 offen offset:0 sc1\n"
    "buffer_load_dwordx4 v[140:143], v199, s[0:3], s35 offen offset:256 sc1\n"
    "buffer_load_dwordx4 v[144:147], v199, s[0:3], s37 offen offset:256 sc1\n"
    "buffer_load_dwordx4 v[116:119], v198, s[0:3], s35 offen sc1\n"
    "buffer_load_dwordx4 v[84:87], v192, s[0:3], s35 offen offset:0 sc1\n"
    "buffer_load_dwordx4 v[88:91], v192, s[0:3], s35 offen offset:2048 sc1\n"
    "buffer_load_dwordx4 v[120:123], v198, s[0:3], s36 offen sc1\n"
    "buffer_load_dwordx4 v[92:95], v192, s[0:3], s36 offen offset:0 sc1\n"
    "buffer_load_dwordx4 v[96:99], v192, s[0:3], s36 offen offset:2048 sc1\n"
    "buffer_load_dwordx4 v[124:127], v198, s[0:3], s37 offen sc1\n"
    "buffer_load_dwordx4 v[100:103], v192, s[0:3], s37 offen offset:0 sc1\n"
    "buffer_load_dwordx4 v[104:107], v192, s[0:3], s37 offen offset:2048 sc1\n"
    "buffer_load_dwordx4 v[128:131], v198, s[0:3], s38 offen sc1\n"
    "buffer_load_dwordx4 v[108:111], v192, s[0:3], s38 offen offset:0 sc1\n"
    "buffer_load_dwordx4 v[112:115], v192, s[0:3], s38 offen offset:2048 sc1\n"
    "s_waitcnt vmcnt(0)\n"
    "v_max3_u32 v184, v132, v134, v132\n"
    "v_swap_b32 v133, v134\n"
    "s_nop 1\n"
    "v_mfma_f32_16x16x32_bf16 v[148:151], v[132:135], v[40:43], 0\n"
    "v_mfma_f32_16x16x32_bf16 v[152:155], v[132:135], v[48:51], 0\n"
    "v_max3_u32 v184, v136, v138, v184\n"
    "v_swap_b32 v137, v138\n"
    "s_nop 1\n"
    "v_mfma_f32_16x16x32_bf16 v[148:151], v[136:139], v[44:47], v[148:151]\n"
    "v_mfma_f32_16x16x32_bf16 v[152:155], v[136:139], v[52:55], v[152:155]\n"
    "v_max3_u32 v184, v140, v142, v184\n"
    "v_swap_b32 v141, v142\n"
    "s_nop 1\n"
    "v_mfma_f32_16x16x32_bf16 v[156:159], v[140:143], v[40:43], 0\n"
    "v_mfma_f32_16x16x32_bf16 v[160:163], v[140:143], v[48:51], 0\n"
    "v_max3_u32 v184, v144, v146, v184\n"
    "v_swap_b32 v145, v146\n"
    "s_nop 1\n"
    "v_mfma_f32_16x16x32_bf16 v[156:159], v[144:147], v[44:47], v[156:159]\n"
    "v_mfma_f32_16x16x32_bf16 v[160:163], v[144:147], v[52:55], v[160:163]\n"
    "v_max3_u32 v184, v116, v118, v184\n"
    "v_smfmac_f32_16x16x64_bf16 v[148:151], v[116:119], a[0:7], v195\n"
    "v_smfmac_f32_16x16x64_bf16 v[152:155], v[116:119], v[8:15], v195\n"
    "v_max3_u32 v184, v84, v86, v184\n"
    "v_swap_b32 v85, v86\n"
    "s_nop 1\n"
    "v_mfma_f32_16x16x32_bf16 v[156:159], v[84:87], a[0:3], v[156:159]\n"
    "v_mfma_f32_16x16x32_bf16 v[160:163], v[84:87], v[8:11], v[160:163]\n"
    "v_max3_u32 v184, v88, v90, v184\n"
    "v_swap_b32 v89, v90\n"
    "s_nop 1\n"
    "v_mfma_f32_16x16x32_bf16 v[156:159], v[88:91], a[4:7], v[156:159]\n"
    "v_mfma_f32_16x16x32_bf16 v[160:163], v[88:91], v[12:15], v[160:163]\n"
    "v_max3_u32 v184, v120, v122, v184\n"
    "v_smfmac_f32_16x16x64_bf16 v[148:151], v[120:123], a[8:15], v195\n"
    "v_smfmac_f32_16x16x64_bf16 v[152:155], v[120:123], v[16:23], v195\n"
    "v_max3_u32 v184, v92, v94, v184\n"
    "v_swap_b32 v93, v94\n"
    "s_nop 1\n"
    "v_mfma_f32_16x16x32_bf16 v[156:159], v[92:95], a[8:11], v[156:159]\n"
    "v_mfma_f32_16x16x32_bf16 v[160:163], v[92:95], v[16:19], v[160:163]\n"
    "v_max3_u32 v184, v96, v98, v184\n"
    "v_swap_b32 v97, v98\n"
    "s_nop 1\n"
    "v_mfma_f32_16x16x32_bf16 v[156:159], v[96:99], a[12:15], v[156:159]\n"
    "v_mfma_f32_16x16x32_bf16 v[160:163], v[96:99], v[20:23], v[160:163]\n"
    "v_max3_u32 v184, v124, v126, v184\n"
    "v_smfmac_f32_16x16x64_bf16 v[148:151], v[124:127], a[16:23], v195\n"
    "v_smfmac_f32_16x16x64_bf16 v[152:155], v[124:127], v[24:31], v195\n"
    "v_max3_u32 v184, v100, v102, v184\n"
    "v_swap_b32 v101, v102\n"
    "s_nop 1\n"
    "v_mfma_f32_16x16x32_bf16 v[156:159], v[100:103], a[16:19], v[156:159]\n"
    "v_mfma_f32_16x16x32_bf16 v[160:163], v[100:103], v[24:27], v[160:163]\n"
    "v_max3_u32 v184, v104, v106, v184\n"
    "v_swap_b32 v105, v106\n"
    "s_nop 1\n"
    "v_mfma_f32_16x16x32_bf16 v[156:159], v[104:107], a[20:23], v[156:159]\n"
    "v_mfma_f32_16x16x32_bf16 v[160:163], v[104:107], v[28:31], v[160:163]\n"
    "v_max3_u32 v184, v128, v130, v184\n"
    "v_smfmac_f32_16x16x64_bf16 v[148:151], v[128:131], a[24:31], v195\n"
    "v_smfmac_f32_16x16x64_bf16 v[152:155], v[128:131], v[32:39], v195\n"
    "v_max3_u32 v184, v108, v110, v184\n"
    "v_swap_b32 v109, v110\n"
    "s_nop 1\n"
    "v_mfma_f32_16x16x32_bf16 v[156:159], v[108:111], a[24:27], v[156:159]\n"
    "v_mfma_f32_16x16x32_bf16 v[160:163], v[108:111], v[32:35], v[160:163]\n"
    "v_max3_u32 v184, v112, v114, v184\n"
    "v_swap_b32 v113, v114\n"
    "s_nop 1\n"
    "v_mfma_f32_16x16x32_bf16 v[156:159], v[112:115], a[28:31], v[156:159]\n"
    "v_mfma_f32_16x16x32_bf16 v[160:163], v[112:115], v[36:39], v[160:163]\n"
    "v_cmp_eq_u32 vcc, -1, v184\n"
    "s_nop 0\n"
    "s_cbranch_vccnz .Lct_tr0_%=\n"
    "s_nop 7\n"
    "s_branch .Lct_tg0_%=\n"
    ".Lct_xr1_%=:\n"
    "s_add_u32 s20, s20, 1\n"
    "s_cmp_lt_u32 s20, 0x40000\n"
    "s_cbranch_scc1 .Lct_sp28_%=\n"
    "v_mov_b32 v184, 42\n"
    "s_mov_b32 s24, 33728\n"
    "buffer_store_dword v184, off, s[0:3], s24 sc1\n"
    "s_waitcnt vmcnt(0)\n"
    "s_branch .Lct_xg1_%=\n"
    ".Lct_sp28_%=:\n"
    ".Lct_rt29_%=:\n"
    "s_memrealtime s[22:23]\n"
    "s_waitcnt lgkmcnt(0)\n"
    "s_sub_u32 s24, s43, s22\n"
    "s_cmp_lt_u32 s24, 600\n"
    "s_cbranch_scc1 .Lct_rt29_%=\n"
    "s_add_u32 s43, s22, 44\n"
    "buffer_load_dwordx2 v[190:191], off, s[0:3], s41 sc1\n"
    "s_waitcnt vmcnt(0)\n"
    "s_branch .Lct_x1_%=\n"
    ".Lct_retry1_%=:\n"
    "s_add_u32 s20, s20, 1\n"
    "s_cmp_lt_u32 s20, 0x40000\n"
    "s_cbranch_scc1 .Lct_sp30_%=\n"
    "v_mov_b32 v184, 3\n"
    "s_mov_b32 s24, 33728\n"
    "buffer_store_dword v184, off, s[0:3], s24 sc1\n"
    "s_waitcnt vmcnt(0)\n"
    "s_branch .Lct_go1_%=\n"
    ".Lct_sp30_%=:\n"
    "s_waitcnt vmcnt(0) lgkmcnt(0)\n"
    "v_mov_b64 v[76:77], 0\n"
    "v_mov_b64 v[78:79], 0\n"
    "v_mov_b64 v[80:81], 0\n"
    "v_mov_b64 v[82:83], 0\n"
    "buffer_load_dwordx4 v[40:43], v192, s[0:3], s32 offen offset:0 sc1\n"
    "buffer_load_dwordx4 v[44:47], v192, s[0:3], s32 offen offset:1024 sc1\n"
    "buffer_load_dwordx4 v[48:51], v197, s[0:3], s33 offen offset:0 sc1\n"
    "buffer_load_dwordx4 v[52:55], v197, s[0:3], s33 offen offset:256 sc1\n"
    "buffer_load_dwordx4 v[72:75], v193, s[0:3], s34 offen sc1\n"
    "buffer_load_dwordx4 v[56:59], v192, s[0:3], s34 offen offset:0 sc1\n"
    "buffer_load_dwordx4 v[60:63], v192, s[0:3], s34 offen offset:1024 sc1\n"
    "buffer_load_dwordx4 v[64:67], v192, s[0:3], s34 offen offset:2048 sc1\n"
    "buffer_load_dwordx4 v[68:71], v192, s[0:3], s34 offen offset:3072 sc1\n"
    "buffer_store_dwordx4 v[148:151], v211, s[0:3], 0 offen sc1\n"
    "s_waitcnt vmcnt(0)\n"
    "s_branch .Lct_take1_%=\n"
    ".Lct_tr1_%=:\n"
    "s_add_u32 s20, s20, 1\n"
    "s_cmp_lt_u32 s20, 0x40000\n"
    "s_cbranch_scc1 .Lct_sp31_%=\n"
    "v_mov_b32 v184, 4\n"
    "s_mov_b32 s24, 33728\n"
    "buffer_store_dword v184, off, s[0:3], s24 sc1\n"
    "s_waitcnt vmcnt(0)\n"
    "s_branch .Lct_tg1_%=\n"
    ".Lct_sp31_%=:\n"
    "buffer_load_dwordx4 v[132:135], v199, s[0:3], s35 offen offset:0 sc1\n"
    "buffer_load_dwordx4 v[136:139], v199, s[0:3], s37 offen offset:0 sc1\n"
    "buffer_load_dwordx4 v[140:143], v199, s[0:3], s35 offen offset:256 sc1\n"
    "buffer_load_dwordx4 v[144:147], v199, s[0:3], s37 offen offset:256 sc1\n"
    "buffer_load_dwordx4 v[116:119], v198, s[0:3], s35 offen sc1\n"
    "buffer_load_dwordx4 v[84:87], v192, s[0:3], s35 offen offset:0 sc1\n"
    "buffer_load_dwordx4 v[88:91], v192, s[0:3], s35 offen offset:2048 sc1\n"
    "buffer_load_dwordx4 v[120:123], v198, s[0:3], s36 offen sc1\n"
    "buffer_load_dwordx4 v[92:95], v192, s[0:3], s36 offen offset:0 sc1\n"
    "buffer_load_dwordx4 v[96:99], v192, s[0:3], s36 offen offset:2048 sc1\n"
    "buffer_load_dwordx4 v[124:127], v198, s[0:3], s37 offen sc1\n"
    "buffer_load_dwordx4 v[100:103], v192, s[0:3], s37 offen offset:0 sc1\n"
    "buffer_load_dwordx4 v[104:107], v192, s[0:3], s37 offen offset:2048 sc1\n"
    "buffer_load_dwordx4 v[128:131], v198, s[0:3], s38 offen sc1\n"
    "buffer_load_dwordx4 v[108:111], v192, s[0:3], s38 offen offset:0 sc1\n"
    "buffer_load_dwordx4 v[112:115], v192, s[0:3], s38 offen offset:2048 sc1\n"
    "s_waitcnt vmcnt(0)\n"
    "v_max3_u32 v184, v132, v134, v132\n"
    "v_swap_b32 v133, v134\n"
    "s_nop 1\n"
    "v_mfma_f32_16x16x32_bf16 v[148:151], v[132:135], v[40:43], 0\n"
    "v_mfma_f32_16x16x32_bf16 v[152:155], v[132:135], v[48:51], 0\n"
    "v_max3_u32 v184, v136, v138, v184\n"
    "v_swap_b32 v137, v138\n"
    "s_nop 1\n"
    "v_mfma_f32_16x16x32_bf16 v[148:151], v[136:139], v[44:47], v[148:151]\n"
    "v_mfma_f32_16x16x32_bf16 v[152:155], v[136:139], v[52:55], v[152:155]\n"
    "v_max3_u32 v184, v140, v142, v184\n"
    "v_swap_b32 v141, v142\n"
    "s_nop 1\n"
    "v_mfma_f32_16x16x32_bf16 v[156:159], v[140:143], v[40:43], 0\n"
    "v_mfma_f32_16x16x32_bf16 v[160:163], v[140:143], v[48:51], 0\n"
    "v_max3_u32 v184, v144, v146, v184\n"
    "v_swap_b32 v145, v146\n"
    "s_nop 1\n"
    "v_mfma_f32_16x16x32_bf16 v[156:159], v[144:147], v[44:47], v[156:159]\n"
    "v_mfma_f32_16x16x32_bf16 v[160:163], v[144:147], v[52:55], v[160:163]\n"
    "v_max3_u32 v184, v116, v118, v184\n"
    "v_smfmac_f32_16x16x64_bf16 v[148:151], v[116:119], a[32:39], v195\n"
    "v_smfmac_f32_16x16x64_bf16 v[152:155], v[116:119], v[8:15], v195\n"
    "v_max3_u32 v184, v84, v86, v184\n"
    "v_swap_b32 v85, v86\n"
    "s_nop 1\n"
    "v_mfma_f32_16x16x32_bf16 v[156:159], v[84:87], a[32:35], v[156:159]\n"
    "v_mfma_f32_16x16x32_bf16 v[160:163], v[84:87], v[8:11], v[160:163]\n"
    "v_max3_u32 v184, v88, v90, v184\n"
    "v_swap_b32 v89, v90\n"
    "s_nop 1\n"
    "v_mfma_f32_16x16x32_bf16 v[156:159], v[88:91], a[36:39], v[156:159]\n"
    "v_mfma_f32_16x16x32_bf16 v[160:163], v[88:91], v[12:15], v[160:163]\n"
    "v_max3_u32 v184, v120, v122, v184\n"
    "v_smfmac_f32_16x16x64_bf16 v[148:151], v[120:123], a[40:47], v195\n"
    "v_smfmac_f32_16x16x64_bf16 v[152:155], v[120:123], v[16:23], v195\n"
    "v_max3_u32 v184, v92, v94, v184\n"
    "v_swap_b32 v93, v94\n"
    "s_nop 1\n"
    "v_mfma_f32_16x16x32_bf16 v[156:159], v[92:95], a[40:43], v[156:159]\n"
    "v_mfma_f32_16x16x32_bf16 v[160:163], v[92:95], v[16:19], v[160:163]\n"
    "v_max3_u32 v184, v96, v98, v184\n"
    "v_swap_b32 v97, v98\n"
    "s_nop 1\n"
    "v_mfma_f32_16x16x32_bf16 v[156:159], v[96:99], a[44:47], v[156:159]\n"
    "v_mfma_f32_16x16x32_bf16 v[160:163], v[96:99], v[20:23], v[160:163]\n"
    "v_max3_u32 v184, v124, v126, v184\n"
    "v_smfmac_f32_16x16x64_bf16 v[148:151], v[124:127], a[48:55], v195\n"
    "v_smfmac_f32_16x16x64_bf16 v[152:155], v[124:127], v[24:31], v195\n"
    "v_max3_u32 v184, v100, v102, v184\n"
    "v_swap_b32 v101, v102\n"
    "s_nop 1\n"
    "v_mfma_f32_16x16x32_bf16 v[156:159], v[100:103], a[48:51], v[156:159]\n"
    "v_mfma_f32_16x16x32_bf16 v[160:163], v[100:103], v[24:27], v[160:163]\n"
    "v_max3_u32 v184, v104, v106, v184\n"
    "v_swap_b32 v105, v106\n"
    "s_nop 1\n"
    "v_mfma_f32_16x16x32_bf16 v[156:159], v[104:107], a[52:55], v[156:159]\n"
    "v_mfma_f32_16x16x32_bf16 v[160:163], v[104:107], v[28:31], v[160:163]\n"
    "v_max3_u32 v184, v128, v130, v184\n"
    "v_smfmac_f32_16x16x64_bf16 v[148:151], v[128:131], a[56:63], v195\n"
    "v_smfmac_f32_16x16x64_bf16 v[152:155], v[128:131], v[32:39], v195\n"
    "v_max3_u32 v184, v108, v110, v184\n"
    "v_swap_b32 v109, v110\n"
    "s_nop 1\n"
    "v_mfma_f32_16x16x32_bf16 v[156:159], v[108:111], a[56:59], v[156:159]\n"
    "v_mfma_f32_16x16x32_bf16 v[160:163], v[108:111], v[32:35], v[160:163]\n"
    "v_max3_u32 v184, v112, v114, v184\n"
    "v_swap_b32 v113, v114\n"
    "s_nop 1\n"
    "v_mfma_f32_16x16x32_bf16 v[156:159], v[112:115], a[60:63], v[156:159]\n"
    "v_mfma_f32_16x16x32_bf16 v[160:163], v[112:115], v[36:39], v[160:163]\n"
    "v_cmp_eq_u32 vcc, -1, v184\n"
    "s_nop 0\n"
    "s_cbranch_vccnz .Lct_tr1_%=\n"
    "s_nop 7\n"
    "s_branch .Lct_tg1_%=\n"
    :: "{s[0:3]}"(s0_), "{s[4:7]}"(s4_), "{s[8:11]}"(s8_), "{s[12:15]}"(s12_), "{s[16:19]}"(s16_), "{v[192:195]}"(v192_), "{v[196:199]}"(v196_), "{v[200:203]}"(v200_), "{v[204:207]}"(v204_), "{v[208:211]}"(v208_), "{v[212:215]}"(v212_) : "memory", "v255", "a255", "s95");
    __builtin_unreachable();
}
DEV void qw_role(const MegaArgs& A, const int L, const int h, const int rep) {
    ROLE_HEAD;
    const u32x4 s0_ = {RFL(rx[0]), RFL(rx[1]), RFL(rx[2]), RFL(rx[3])};
    const u32x4 s4_ = {RFL(lo(A.Wq + h * 192 + 64)), RFL(hi(A.Wq + h * 192 + 64)), RFL(WBYTES - h * 384 - 128), RFL(0x20000u)};
    const u32x4 s8_ = {RFL(lo(A.Wv + h * 192 + 128)), RFL(hi(A.Wv + h * 192 + 128)), RFL(WBYTES - h * 384 - 256), RFL(0x20000u)};
    const u32x4 s12_ = {RFL(w), RFL(REC0 + w * 2048), RFL(QT0 + h * 32768), RFL((A.layers & 2) ? 64 : 0)};
    const u32x4 s16_ = {RFL(xl(Z_CT + (h & 7) * 256, L)), RFL(xl(Z_XN + rep * 2048, 0)), RFL(DBG0 + ((L * 128 + 112 + (h >> 3) * 4 + (h & 3)) * 4 + w) * 2048), RFL(0u)};
    const u32x4 v0_ = {(unsigned)((8 * w + (l >> 3)) * 12288 + ((l & 7) ^ (((8 * w + (l >> 3)) >> 1) & 6)) * 16), (unsigned)((8 * w + (l >> 3)) * 12288 + ((l & 7) ^ (((8 * w + (l >> 3)) >> 1) & 6)) * 16 + 128), (unsigned)((8 * lq + ((l >> 2) & 1) + ((l >> 1) & 4)) * 128 + (w ^ (((8 * lq + ((l >> 2) & 1) + ((l >> 1) & 4)) >> 2) & 3)) * 32 + (l & 3) * 8), (unsigned)(0u)};
    const u32x4 v64_ = {(unsigned)(l * 16), (unsigned)(l * 16 + 40960), (unsigned)(l * 16 + w * 2048), (unsigned)(l * 16 + w * 1024 + 16384)};
    const u32x4 v68_ = {(unsigned)(l < 20 ? (l >> 2) * 128 + (l & 3) * 4 : 0), (unsigned)(l == 0 ? xl(Z_QW + h * 128 + w * 4, 0) : OOB), (unsigned)(0u), (unsigned)(0u)};
    asm volatile(
    "s_mov_b32 s20, 0\n"
    "s_mov_b64 s[22:23], 0\n"
    "s_mov_b32 s25, 0\n"
    "s_lshl_b32 s32, s12, 10\n"
    "s_mov_b32 s38, 0\n"
    "s_add_u32 m0, s32, 0\n"
    "s_nop 0\n"
    "buffer_load_dwordx4 v0, s[4:7], s38 offen nt lds\n"
    "s_add_u32 m0, s32, 4096\n"
    "s_nop 0\n"
    "buffer_load_dwordx4 v1, s[4:7], s38 offen nt lds\n"
    "s_add_u32 m0, s32, 8192\n"
    "s_nop 0\n"
    "buffer_load_dwordx4 v0, s[8:11], s38 offen nt lds\n"
    "s_mov_b32 s38, 1572864\n"
    "s_add_u32 m0, s32, 12288\n"
    "s_nop 0\n"
    "buffer_load_dwordx4 v0, s[4:7], s38 offen nt lds\n"
    "s_add_u32 m0, s32, 16384\n"
    "s_nop 0\n"
    "buffer_load_dwordx4 v1, s[4:7], s38 offen nt lds\n"
    "s_add_u32 m0, s32, 20480\n"
    "s_nop 0\n"
    "buffer_load_dwordx4 v0, s[8:11], s38 offen nt lds\n"
    "s_mov_b32 s38, 3145728\n"
    "s_add_u32 m0, s32, 24576\n"
    "s_nop 0\n"
    "buffer_load_dwordx4 v0, s[4:7], s38 offen nt lds\n"
    "s_add_u32 m0, s32, 28672\n"
    "s_nop 0\n"
    "buffer_load_dwordx4 v1, s[4:7], s38 offen nt lds\n"
    "s_add_u32 m0, s32, 32768\n"
    "s_nop 0\n"
    "buffer_load_dwordx4 v0, s[8:11], s38 offen nt lds\n"
    "s_mov_b32 s38, 4718592\n"
    "s_add_u32 m0, s32, 36864\n"
    "s_nop 0\n"
    "buffer_load_dwordx4 v0, s[4:7], s38 offen nt lds\n"
    "s_add_u32 m0, s32, 40960\n"
    "s_nop 0\n"
    "buffer_load_dwordx4 v1, s[4:7], s38 offen nt lds\n"
    "s_add_u32 m0, s32, 45056\n"
    "s_nop 0\n"
    "buffer_load_dwordx4 v0, s[8:11], s38 offen nt lds\n"
    "s_mov_b32 s38, 393216\n"
    "s_add_u32 m0, s32, 49152\n"
    "s_nop 0\n"
    "buffer_load_dwordx4 v0, s[4:7], s38 offen nt lds\n"
    "s_add_u32 m0, s32, 53248\n"
    "s_nop 0\n"
    "buffer_load_dwordx4 v1, s[4:7], s38 offen nt lds\n"
    "s_add_u32 m0, s32, 57344\n"
    "s_nop 0\n"
    "buffer_load_dwordx4 v0, s[8:11], s38 offen nt lds\n"
    "s_mov_b32 s38, 1966080\n"
    "s_add_u32 m0, s32, 61440\n"
    "s_nop 0\n"
    "buffer_load_dwordx4 v0, s[4:7], s38 offen nt lds\n"
    "s_add_u32 m0, s32, 65536\n"
    "s_nop 0\n"
    "buffer_load_dwordx4 v1, s[4:7], s38 offen nt lds\n"
    "s_add_u32 m0, s32, 69632\n"
    "s_nop 0\n"
    "buffer_load_dwordx4 v0, s[8:11], s38 offen nt lds\n"
    "s_mov_b32 s38, 3538944\n"
    "s_add_u32 m0, s32, 73728\n"
    "s_nop 0\n"
    "buffer_load_dwordx4 v0, s[4:7], s38 offen nt lds\n"
    "s_add_u32 m0, s32, 77824\n"
    "s_nop 0\n"
    "buffer_load_dwordx4 v1, s[4:7], s38 offen nt lds\n"
    "s_add_u32 m0, s32, 81920\n"
    "s_nop 0\n"
    "buffer_load_dwordx4 v0, s[8:11], s38 offen nt lds\n"
    "s_mov_b32 s38, 5111808\n"
    "s_add_u32 m0, s32, 86016\n"
    "s_nop 0\n"
    "buffer_load_dwordx4 v0, s[4:7], s38 offen nt lds\n"
    "s_add_u32 m0, s32, 90112\n"
    "s_nop 0\n"
    "buffer_load_dwordx4 v1, s[4:7], s38 offen nt lds\n"
    "s_add_u32 m0, s32, 94208\n"
    "s_nop 0\n"
    "buffer_load_dwordx4 v0, s[8:11], s38 offen nt lds\n"
    "s_mov_b32 s38, 786432\n"
    "s_add_u32 m0, s32, 98304\n"
    "s_nop 0\n"
    "buffer_load_dwordx4 v0, s[4:7], s38 offen nt lds\n"
    "s_add_u32 m0, s32, 102400\n"
    "s_nop 0\n"
    "buffer_load_dwordx4 v1, s[4:7], s38 offen nt lds\n"
    "s_add_u32 m0, s32, 106496\n"
    "s_nop 0\n"
    "buffer_load_dwordx4 v0, s[8:11], s38 offen nt lds\n"
    "s_mov_b32 s38, 2359296\n"
    "s_add_u32 m0, s32, 110592\n"
    "s_nop 0\n"
    "buffer_load_dwordx4 v0, s[4:7], s38 offen nt lds\n"
    "s_add_u32 m0, s32, 114688\n"
    "s_nop 0\n"
    "buffer_load_dwordx4 v1, s[4:7], s38 offen nt lds\n"
    "s_add_u32 m0, s32, 118784\n"
    "s_nop 0\n"
    "buffer_load_dwordx4 v0, s[8:11], s38 offen nt lds\n"
    "s_mov_b32 s38, 3932160\n"
    "s_add_u32 m0, s32, 122880\n"
    "s_nop 0\n"
    "buffer_load_dwordx4 v0, s[4:7], s38 offen nt lds\n"
    "s_add_u32 m0, s32, 126976\n"
    "s_nop 0\n"
    "buffer_load_dwordx4 v1, s[4:7], s38 offen nt lds\n"
    "s_add_u32 m0, s32, 131072\n"
    "s_nop 0\n"
    "buffer_load_dwordx4 v0, s[8:11], s38 offen nt lds\n"
    "s_mov_b32 s38, 5505024\n"
    "s_add_u32 m0, s32, 135168\n"
    "s_nop 0\n"
    "buffer_load_dwordx4 v0, s[4:7], s38 offen nt lds\n"
    "s_add_u32 m0, s32, 139264\n"
    "s_nop 0\n"
    "buffer_load_dwordx4 v1, s[4:7], s38 offen nt lds\n"
    "s_add_u32 m0, s32, 143360\n"
    "s_nop 0\n"
    "buffer_load_dwordx4 v0, s[8:11], s38 offen nt lds\n"
    "s_waitcnt vmcnt(24)\n"
    "s_barrier\n"
    "v_add_u32 v8, 0, v2\n"
    "ds_read_b64_tr_b16 a[0:1], v8 offset:0\n"
    "ds_read_b64_tr_b16 a[2:3], v8 offset:256\n"
    "ds_read_b64_tr_b16 a[80:81], v8 offset:4096\n"
    "ds_read_b64_tr_b16 a[82:83], v8 offset:4352\n"
    "ds_read_b64_tr_b16 a[160:161], v8 offset:8192\n"
    "ds_read_b64_tr_b16 a[162:163], v8 offset:8448\n"
    "ds_read_b64_tr_b16 a[16:17], v8 offset:12288\n"
    "ds_read_b64_tr_b16 a[18:19], v8 offset:12544\n"
    "ds_read_b64_tr_b16 a[96:97], v8 offset:16384\n"
    "ds_read_b64_tr_b16 a[98:99], v8 offset:16640\n"
    "ds_read_b64_tr_b16 a[176:177], v8 offset:20480\n"
    "ds_read_b64_tr_b16 a[178:179], v8 offset:20736\n"
    "ds_read_b64_tr_b16 a[32:33], v8 offset:24576\n"
    "ds_read_b64_tr_b16 a[34:35], v8 offset:24832\n"
    "ds_read_b64_tr_b16 a[112:113], v8 offset:28672\n"
    "ds_read_b64_tr_b16 a[114:115], v8 offset:28928\n"
    "ds_read_b64_tr_b16 a[192:193], v8 offset:32768\n"
    "ds_read_b64_tr_b16 a[194:195], v8 offset:33024\n"
    "ds_read_b64_tr_b16 a[48:49], v8 offset:36864\n"
    "ds_read_b64_tr_b16 a[50:51], v8 offset:37120\n"
    "ds_read_b64_tr_b16 a[128:129], v8 offset:40960\n"
    "ds_read_b64_tr_b16 a[130:131], v8 offset:41216\n"
    "ds_read_b64_tr_b16 a[208:209], v8 offset:45056\n"
    "ds_read_b64_tr_b16 a[210:211], v8 offset:45312\n"
    "s_waitcnt lgkmcnt(0)\n"
    "s_barrier\n"
    "s_mov_b32 s38, 1179648\n"
    "s_add_u32 m0, s32, 0\n"
    "s_nop 0\n"
    "buffer_load_dwordx4 v0, s[4:7], s38 offen nt lds\n"
    "s_add_u32 m0, s32, 4096\n"
    "s_nop 0\n"
    "buffer_load_dwordx4 v1, s[4:7], s38 offen nt lds\n"
    "s_add_u32 m0, s32, 8192\n"
    "s_nop 0\n"
    "buffer_load_dwordx4 v0, s[8:11], s38 offen nt lds\n"
    "s_mov_b32 s38, 2752512\n"
    "s_add_u32 m0, s32, 12288\n"
    "s_nop 0\n"
    "buffer_load_dwordx4 v0, s[4:7], s38 offen nt lds\n"
    "s_add_u32 m0, s32, 16384\n"
    "s_nop 0\n"
    "buffer_load_dwordx4 v1, s[4:7], s38 offen nt lds\n"
    "s_add_u32 m0, s32, 20480\n"
    "s_nop 0\n"
    "buffer_load_dwordx4 v0, s[8:11], s38 offen nt lds\n"
    "s_mov_b32 s38, 4325376\n"
    "s_add_u32 m0, s32, 24576\n"
    "s_nop 0\n"
    "buffer_load_dwordx4 v0, s[4:7], s38 offen nt lds\n"
    "s_add_u32 m0, s32, 28672\n"
    "s_nop 0\n"
    "buffer_load_dwordx4 v1, s[4:7], s38 offen nt lds\n"
    "s_add_u32 m0, s32, 32768\n"
    "s_nop 0\n"
    "buffer_load_dwordx4 v0, s[8:11], s38 offen nt lds\n"
    "s_mov_b32 s38, 5898240\n"
    "s_add_u32 m0, s32, 36864\n"
    "s_nop 0\n"
    "buffer_load_dwordx4 v0, s[4:7], s38 offen nt lds\n"
    "s_add_u32 m0, s32, 40960\n"
    "s_nop 0\n"
    "buffer_load_dwordx4 v1, s[4:7], s38 offen nt lds\n"
    "s_add_u32 m0, s32, 45056\n"
    "s_nop 0\n"
    "buffer_load_dwordx4 v0, s[8:11], s38 offen nt lds\n"
    "v_and_b32 v4, 0x70, v64\n"
    "v_lshlrev_b32 v4, 2, v4\n"
    "v_or_b32 v4, 0x8000, v4\n"
    "buffer_load_dword v5, v4, s[0:3], 0 offen sc1\n"
    "s_waitcnt vmcnt(25)\n"
    "s_barrier\n"
    "v_add_u32 v8, 49152, v2\n"
    "ds_read_b64_tr_b16 a[4:5], v8 offset:0\n"
    "ds_read_b64_tr_b16 a[6:7], v8 offset:256\n"
    "ds_read_b64_tr_b16 a[84:85], v8 offset:4096\n"
    "ds_read_b64_tr_b16 a[86:87], v8 offset:4352\n"
    "ds_read_b64_tr_b16 a[164:165], v8 offset:8192\n"
    "ds_read_b64_tr_b16 a[166:167], v8 offset:8448\n"
    "ds_read_b64_tr_b16 a[20:21], v8 offset:12288\n"
    "ds_read_b64_tr_b16 a[22:23], v8 offset:12544\n"
    "ds_read_b64_tr_b16 a[100:101], v8 offset:16384\n"
    "ds_read_b64_tr_b16 a[102:103], v8 offset:16640\n"
    "ds_read_b64_tr_b16 a[180:181], v8 offset:20480\n"
    "ds_read_b64_tr_b16 a[182:183], v8 offset:20736\n"
    "ds_read_b64_tr_b16 a[36:37], v8 offset:24576\n"
    "ds_read_b64_tr_b16 a[38:39], v8 offset:24832\n"
    "ds_read_b64_tr_b16 a[116:117], v8 offset:28672\n"
    "ds_read_b64_tr_b16 a[118:119], v8 offset:28928\n"
    "ds_read_b64_tr_b16 a[196:197], v8 offset:32768\n"
    "ds_read_b64_tr_b16 a[198:199], v8 offset:33024\n"
    "ds_read_b64_tr_b16 a[52:53], v8 offset:36864\n"
    "ds_read_b64_tr_b16 a[54:55], v8 offset:37120\n"
    "ds_read_b64_tr_b16 a[132:133], v8 offset:40960\n"
    "ds_read_b64_tr_b16 a[134:135], v8 offset:41216\n"
    "ds_read_b64_tr_b16 a[212:213], v8 offset:45056\n"
    "ds_read_b64_tr_b16 a[214:215], v8 offset:45312\n"
    "s_waitcnt lgkmcnt(0)\n"
    "s_barrier\n"
    "s_mov_b32 s38, 6291456\n"
    "s_add_u32 m0, s32, 49152\n"
    "s_nop 0\n"
    "buffer_load_dwordx4 v0, s[4:7], s38 offen nt lds\n"
    "s_add_u32 m0, s32, 53248\n"
    "s_nop 0\n"
    "buffer_load_dwordx4 v1, s[4:7], s38 offen nt lds\n"
    "s_add_u32 m0, s32, 57344\n"
    "s_nop 0\n"
    "buffer_load_dwordx4 v0, s[8:11], s38 offen nt lds\n"
    "s_mov_b32 s38, 6684672\n"
    "s_add_u32 m0, s32, 61440\n"
    "s_nop 0\n"
    "buffer_load_dwordx4 v0, s[4:7], s38 offen nt lds\n"
    "s_add_u32 m0, s32, 65536\n"
    "s_nop 0\n"
    "buffer_load_dwordx4 v1, s[4:7], s38 offen nt lds\n"
    "s_add_u32 m0, s32, 69632\n"
    "s_nop 0\n"
    "buffer_load_dwordx4 v0, s[8:11], s38 offen nt lds\n"
    "s_mov_b32 s38, 7077888\n"
    "s_add_u32 m0, s32, 73728\n"
    "s_nop 0\n"
    "buffer_load_dwordx4 v0, s[4:7], s38 offen nt lds\n"
    "s_add_u32 m0, s32, 77824\n"
    "s_nop 0\n"
    "buffer_load_dwordx4 v1, s[4:7], s38 offen nt lds\n"
    "s_add_u32 m0, s32, 81920\n"
    "s_nop 0\n"
    "buffer_load_dwordx4 v0, s[8:11], s38 offen nt lds\n"
    "s_mov_b32 s38, 7471104\n"
    "s_add_u32 m0, s32, 86016\n"
    "s_nop 0\n"
    "buffer_load_dwordx4 v0, s[4:7], s38 offen nt lds\n"
    "s_add_u32 m0, s32, 90112\n"
    "s_nop 0\n"
    "buffer_load_dwordx4 v1, s[4:7], s38 offen nt lds\n"
    "s_add_u32 m0, s32, 94208\n"
    "s_nop 0\n"
    "buffer_load_dwordx4 v0, s[8:11], s38 offen nt lds\n"
    "s_waitcnt vmcnt(25)\n"
    "s_barrier\n"
    "v_add_u32 v8, 98304, v2\n"
    "ds_read_b64_tr_b16 a[8:9], v8 offset:0\n"
    "ds_read_b64_tr_b16 a[10:11], v8 offset:256\n"
    "ds_read_b64_tr_b16 a[88:89], v8 offset:4096\n"
    "ds_read_b64_tr_b16 a[90:91], v8 offset:4352\n"
    "ds_read_b64_tr_b16 a[168:169], v8 offset:8192\n"
    "ds_read_b64_tr_b16 a[170:171], v8 offset:8448\n"
    "ds_read_b64_tr_b16 a[24:25], v8 offset:12288\n"
    "ds_read_b64_tr_b16 a[26:27], v8 offset:12544\n"
    "ds_read_b64_tr_b16 a[104:105], v8 offset:16384\n"
    "ds_read_b64_tr_b16 a[106:107], v8 offset:16640\n"
    "ds_read_b64_tr_b16 a[184:185], v8 offset:20480\n"
    "ds_read_b64_tr_b16 a[186:187], v8 offset:20736\n"
    "ds_read_b64_tr_b16 a[40:41], v8 offset:24576\n"
    "ds_read_b64_tr_b16 a[42:43], v8 offset:24832\n"
    "ds_read_b64_tr_b16 a[120:121], v8 offset:28672\n"
    "ds_read_b64_tr_b16 a[122:123], v8 offset:28928\n"
    "ds_read_b64_tr_b16 a[200:201], v8 offset:32768\n"
    "ds_read_b64_tr_b16 a[202:203], v8 offset:33024\n"
    "ds_read_b64_tr_b16 a[56:57], v8 offset:36864\n"
    "ds_read_b64_tr_b16 a[58:59], v8 offset:37120\n"
    "ds_read_b64_tr_b16 a[136:137], v8 offset:40960\n"
    "ds_read_b64_tr_b16 a[138:139], v8 offset:41216\n"
    "ds_read_b64_tr_b16 a[216:217], v8 offset:45056\n"
    "ds_read_b64_tr_b16 a[218:219], v8 offset:45312\n"
    "s_waitcnt lgkmcnt(0)\n"
    "s_barrier\n"
    "s_waitcnt vmcnt(13)\n"
    "s_barrier\n"
    "v_add_u32 v8, 0, v2\n"
    "ds_read_b64_tr_b16 a[12:13], v8 offset:0\n"
    "ds_read_b64_tr_b16 a[14:15], v8 offset:256\n"
    "ds_read_b64_tr_b16 a[92:93], v8 offset:4096\n"
    "ds_read_b64_tr_b16 a[94:95], v8 offset:4352\n"
    "ds_read_b64_tr_b16 a[172:173], v8 offset:8192\n"
    "ds_read_b64_tr_b16 a[174:175], v8 offset:8448\n"
    "ds_read_b64_tr_b16 a[28:29], v8 offset:12288\n"
    "ds_read_b64_tr_b16 a[30:31], v8 offset:12544\n"
    "ds_read_b64_tr_b16 a[108:109], v8 offset:16384\n"
    "ds_read_b64_tr_b16 a[110:111], v8 offset:16640\n"
    "ds_read_b64_tr_b16 a[188:189], v8 offset:20480\n"
    "ds_read_b64_tr_b16 a[190:191], v8 offset:20736\n"
    "ds_read_b64_tr_b16 a[44:45], v8 offset:24576\n"
    "ds_read_b64_tr_b16 a[46:47], v8 offset:24832\n"
    "ds_read_b64_tr_b16 a[124:125], v8 offset:28672\n"
    "ds_read_b64_tr_b16 a[126:127], v8 offset:28928\n"
    "ds_read_b64_tr_b16 a[204:205], v8 offset:32768\n"
    "ds_read_b64_tr_b16 a[206:207], v8 offset:33024\n"
    "ds_read_b64_tr_b16 a[60:61], v8 offset:36864\n"
    "ds_read_b64_tr_b16 a[62:63], v8 offset:37120\n"
    "ds_read_b64_tr_b16 a[140:141], v8 offset:40960\n"
    "ds_read_b64_tr_b16 a[142:143], v8 offset:41216\n"
    "ds_read_b64_tr_b16 a[220:221], v8 offset:45056\n"
    "ds_read_b64_tr_b16 a[222:223], v8 offset:45312\n"
    "s_waitcnt lgkmcnt(0)\n"
    "s_barrier\n"
    "s_waitcnt vmcnt(0)\n"
    "s_barrier\n"
    "v_add_u32 v8, 49152, v2\n"
    "ds_read_b64_tr_b16 a[64:65], v8 offset:0\n"
    "ds_read_b64_tr_b16 a[66:67], v8 offset:256\n"
    "ds_read_b64_tr_b16 a[144:145], v8 offset:4096\n"
    "ds_read_b64_tr_b16 a[146:147], v8 offset:4352\n"
    "ds_read_b64_tr_b16 a[224:225], v8 offset:8192\n"
    "ds_read_b64_tr_b16 a[226:227], v8 offset:8448\n"
    "ds_read_b64_tr_b16 a[68:69], v8 offset:12288\n"
    "ds_read_b64_tr_b16 a[70:71], v8 offset:12544\n"
    "ds_read_b64_tr_b16 a[148:149], v8 offset:16384\n"
    "ds_read_b64_tr_b16 a[150:151], v8 offset:16640\n"
    "ds_read_b64_tr_b16 a[228:229], v8 offset:20480\n"
    "ds_read_b64_tr_b16 a[230:231], v8 offset:20736\n"
    "ds_read_b64_tr_b16 a[72:73], v8 offset:24576\n"
    "ds_read_b64_tr_b16 a[74:75], v8 offset:24832\n"
    "ds_read_b64_tr_b16 a[152:153], v8 offset:28672\n"
    "ds_read_b64_tr_b16 a[154:155], v8 offset:28928\n"
    "ds_read_b64_tr_b16 a[232:233], v8 offset:32768\n"
    "ds_read_b64_tr_b16 a[234:235], v8 offset:33024\n"
    "ds_read_b64_tr_b16 a[76:77], v8 offset:36864\n"
    "ds_read_b64_tr_b16 a[78:79], v8 offset:37120\n"
    "ds_read_b64_tr_b16 a[156:157], v8 offset:40960\n"
    "ds_read_b64_tr_b16 a[158:159], v8 offset:41216\n"
    "ds_read_b64_tr_b16 a[236:237], v8 offset:45056\n"
    "ds_read_b64_tr_b16 a[238:239], v8 offset:45312\n"
    "s_waitcnt lgkmcnt(0)\n"
    "s_barrier\n"
    "s_waitcnt vmcnt(12)\n"
    "v_cmp_gt_i32 vcc, 32, v5\n"
    "s_nop 0\n"
    "s_cbranch_vccnz .Lqw_tk0_%=\n"
    ".Lqw_tk1_%=:\n"
    "s_mov_b32 s26, -2\n"
    "v_lshrrev_b32 v4, 2, v64\n"
    ".Lqw_po4_%=:\n"
    "buffer_load_dword v8, v4, s[0:3], s16 offen sc1\n"
    "s_waitcnt vmcnt(0)\n"
    "v_cmp_gt_i32 vcc, 1, v8\n"
    "s_nop 0\n"
    "s_cbranch_vccz .Lqw_pd5_%=\n"
    "s_add_u32 s20, s20, 1\n"
    "s_cmp_lt_u32 s20, 0x40000\n"
    "s_cbranch_scc1 .Lqw_sp6_%=\n"
    "v_mov_b32 v8, 50\n"
    "s_mov_b32 s24, 33728\n"
    "buffer_store_dword v8, off, s[0:3], s24 sc1\n"
    "s_waitcnt vmcnt(0)\n"
    "s_branch .Lqw_pd5_%=\n"
    ".Lqw_sp6_%=:\n"
    "s_sleep 4\n"
    "s_branch .Lqw_po4_%=\n"
    ".Lqw_pd5_%=:\n"
    "s_mov_b32 s26, -1\n"
    "s_mov_b32 s35, 0\n"
    "s_lshl_b32 s37, s12, 11\n"
    "s_mov_b32 s45, 0\n"
    "v_mov_b32 v60, 0\n"
    "s_mov_b32 s43, 2130706432\n"
    "s_mov_b32 s41, 2130706432\n"
    "s_mov_b32 s46, 2130706432\n"
    "s_mov_b32 s34, 2130706432\n"
    "s_add_u32 s26, s26, 1\n"
    "s_sub_u32 s27, s26, 14\n"
    "s_ashr_i32 s28, s27, 1\n"
    "s_and_b32 s29, s27, 1\n"
    "s_add_u32 s30, s28, 14\n"
    "s_cmp_lt_i32 s27, 0\n"
    "s_cselect_b32 s29, 0, s29\n"
    "s_cselect_b32 s30, s26, s30\n"
    "s_cmp_eq_u32 s29, 1\n"
    "s_cselect_b32 s30, s28, s30\n"
    "s_cselect_b32 s27, s15, 64\n"
    "s_lshl_b32 s31, s29, 13\n"
    "s_add_u32 s40, s30, 1\n"
    "s_lshl_b32 s32, s30, 19\n"
    "s_lshl_b32 s25, s30, 11\n"
    "s_xor_b32 s25, s25, s31\n"
    "s_and_b32 s25, s25, 0x2000\n"
    "s_add_u32 s32, s32, s25\n"
    "s_add_u32 s32, s32, s13\n"
    "s_lshl_b32 s33, s30, 20\n"
    "s_lshl_b32 s25, s30, 12\n"
    "s_xor_b32 s25, s25, s31\n"
    "s_and_b32 s25, s25, 0x2000\n"
    "s_add_u32 s33, s33, s25\n"
    "s_add_u32 s33, s33, s14\n"
    "s_mul_i32 s47, s40, s29\n"
    "s_cmp_lt_u32 s30, s27\n"
    "s_cselect_b32 s36, 1, 0\n"
    "s_cselect_b32 s32, s32, 2130706432\n"
    "s_cmp_lt_u32 s30, s27\n"
    "s_cselect_b32 s39, s31, 2130706432\n"
    "s_cselect_b32 s47, s47, 0\n"
    "s_cmp_gt_u32 s47, 0\n"
    "s_cselect_b32 s38, s17, 2130706432\n"
    ".Lqw_top_%=:\n"
    "ds_read_b128 v[24:27], v65 offset:0\n"
    "ds_read_b128 v[28:31], v65 offset:1024\n"
    "ds_read_b128 v[32:35], v65 offset:2048\n"
    "ds_read_b128 v[36:39], v65 offset:3072\n"
    "ds_read_b128 v[40:43], v65 offset:4096\n"
    "ds_read_b128 v[44:47], v65 offset:5120\n"
    "v_cmp_gt_i32 vcc, s47, v60\n"
    "s_nop 0\n"
    "s_cbranch_vccnz .Lqw_wx_%=\n"
    ".Lqw_wxb_%=:\n"
    "s_cmp_eq_u32 s35, 0\n"
    "s_cbranch_scc1 .Lqw_skip_%=\n"
    ".Lqw_mk0x6_%=:\n"
    "s_waitcnt lgkmcnt(4)\n"
    "v_mfma_f32_16x16x32_bf16 v[0:3], a[0:3], v[24:27], 0\n"
    "buffer_store_dwordx4 v[48:51], v66, s[0:3], s46 offen offset:0 sc1\n"
    "v_mfma_f32_16x16x32_bf16 v[4:7], a[0:3], v[28:31], 0\n"
    "v_mfma_f32_16x16x32_bf16 v[8:11], a[80:83], v[24:27], 0\n"
    "v_mfma_f32_16x16x32_bf16 v[12:15], a[80:83], v[28:31], 0\n"
    "v_mfma_f32_16x16x32_bf16 v[16:19], a[160:163], v[24:27], 0\n"
    "ds_read_b128 v[24:27], v65 offset:6144\n"
    "v_mfma_f32_16x16x32_bf16 v[20:23], a[160:163], v[28:31], 0\n"
    "ds_read_b128 v[28:31], v65 offset:7168\n"
    "s_waitcnt lgkmcnt(4)\n"
    "v_mfma_f32_16x16x32_bf16 v[0:3], a[4:7], v[32:35], v[0:3]\n"
    "v_mfma_f32_16x16x32_bf16 v[4:7], a[4:7], v[36:39], v[4:7]\n"
    "buffer_store_dwordx4 v[52:55], v66, s[0:3], s46 offen offset:1024 sc1\n"
    "v_mfma_f32_16x16x32_bf16 v[8:11], a[84:87], v[32:35], v[8:11]\n"
    "v_mfma_f32_16x16x32_bf16 v[12:15], a[84:87], v[36:39], v[12:15]\n"
    "v_mfma_f32_16x16x32_bf16 v[16:19], a[164:167], v[32:35], v[16:19]\n"
    "ds_read_b128 v[32:35], v65 offset:8192\n"
    "v_mfma_f32_16x16x32_bf16 v[20:23], a[164:167], v[36:39], v[20:23]\n"
    "ds_read_b128 v[36:39], v65 offset:9216\n"
    "s_waitcnt lgkmcnt(4)\n"
    "v_mfma_f32_16x16x32_bf16 v[0:3], a[8:11], v[40:43], v[0:3]\n"
    "v_mfma_f32_16x16x32_bf16 v[4:7], a[8:11], v[44:47], v[4:7]\n"
    "v_mfma_f32_16x16x32_bf16 v[8:11], a[88:91], v[40:43], v[8:11]\n"
    "buffer_store_dwordx4 v[56:59], v67, s[0:3], s46 offen sc1\n"
    "s_add_u32 m0, s37, 0\n"
    "v_mfma_f32_16x16x32_bf16 v[12:15], a[88:91], v[44:47], v[12:15]\n"
    "v_mfma_f32_16x16x32_bf16 v[16:19], a[168:171], v[40:43], v[16:19]\n"
    "ds_read_b128 v[40:43], v65 offset:10240\n"
    "v_mfma_f32_16x16x32_bf16 v[20:23], a[168:171], v[44:47], v[20:23]\n"
    "ds_read_b128 v[44:47], v65 offset:11264\n"
    "s_waitcnt lgkmcnt(4)\n"
    "v_mfma_f32_16x16x32_bf16 v[0:3], a[12:15], v[24:27], v[0:3]\n"
    "v_mfma_f32_16x16x32_bf16 v[4:7], a[12:15], v[28:31], v[4:7]\n"
    "v_mfma_f32_16x16x32_bf16 v[8:11], a[92:95], v[24:27], v[8:11]\n"
    "v_mfma_f32_16x16x32_bf16 v[12:15], a[92:95], v[28:31], v[12:15]\n"
    "buffer_load_dwordx4 v64, s[0:3], s32 offen sc1 lds\n"
    "v_mfma_f32_16x16x32_bf16 v[16:19], a[172:175], v[24:27], v[16:19]\n"
    "ds_read_b128 v[24:27], v65 offset:12288\n"
    "v_mfma_f32_16x16x32_bf16 v[20:23], a[172:175], v[28:31], v[20:23]\n"
    "ds_read_b128 v[28:31], v65 offset:13312\n"
    "s_waitcnt lgkmcnt(4)\n"
    "v_mfma_f32_16x16x32_bf16 v[0:3], a[16:19], v[32:35], v[0:3]\n"
    "v_mfma_f32_16x16x32_bf16 v[4:7], a[16:19], v[36:39], v[4:7]\n"
    "buffer_load_dwordx4 v64, s[0:3], s32 offen offset:1024 sc1 lds\n"
    "v_mfma_f32_16x16x32_bf16 v[8:11], a[96:99], v[32:35], v[8:11]\n"
    "s_add_u32 m0, s37, 8192\n"
    "s_add_u32 s32, s32, 0x4000\n"
    "v_mfma_f32_16x16x32_bf16 v[12:15], a[96:99], v[36:39], v[12:15]\n"
    "v_mfma_f32_16x16x32_bf16 v[16:19], a[176:179], v[32:35], v[16:19]\n"
    "ds_read_b128 v[32:35], v65 offset:14336\n"
    "v_mfma_f32_16x16x32_bf16 v[20:23], a[176:179], v[36:39], v[20:23]\n"
    "ds_read_b128 v[36:39], v65 offset:15360\n"
    "s_waitcnt lgkmcnt(4)\n"
    "v_mfma_f32_16x16x32_bf16 v[0:3], a[20:23], v[40:43], v[0:3]\n"
    "buffer_load_dwordx4 v64, s[0:3], s32 offen sc1 lds\n"
    "v_mfma_f32_16x16x32_bf16 v[4:7], a[20:23], v[44:47], v[4:7]\n"
    "v_mfma_f32_16x16x32_bf16 v[8:11], a[100:103], v[40:43], v[8:11]\n"
    "v_mfma_f32_16x16x32_bf16 v[12:15], a[100:103], v[44:47], v[12:15]\n"
    "v_mfma_f32_16x16x32_bf16 v[16:19], a[180:183], v[40:43], v[16:19]\n"
    "ds_read_b128 v[40:43], v65 offset:16384\n"
    "v_mfma_f32_16x16x32_bf16 v[20:23], a[180:183], v[44:47], v[20:23]\n"
    "ds_read_b128 v[44:47], v65 offset:17408\n"
    "s_waitcnt lgkmcnt(4)\n"
    "v_mfma_f32_16x16x32_bf16 v[0:3], a[24:27], v[24:27], v[0:3]\n"
    "buffer_load_dwordx4 v64, s[0:3], s32 offen offset:1024 sc1 lds\n"
    "v_mfma_f32_16x16x32_bf16 v[4:7], a[24:27], v[28:31], v[4:7]\n"
    "s_add_u32 m0, s37, 16384\n"
    "s_add_u32 s32, s32, 0x4000\n"
    "v_mfma_f32_16x16x32_bf16 v[8:11], a[104:107], v[24:27], v[8:11]\n"
    "v_mfma_f32_16x16x32_bf16 v[12:15], a[104:107], v[28:31], v[12:15]\n"
    "v_mfma_f32_16x16x32_bf16 v[16:19], a[184:187], v[24:27], v[16:19]\n"
    "ds_read_b128 v[24:27], v65 offset:18432\n"
    "v_mfma_f32_16x16x32_bf16 v[20:23], a[184:187], v[28:31], v[20:23]\n"
    "ds_read_b128 v[28:31], v65 offset:19456\n"
    "s_waitcnt lgkmcnt(4)\n"
    "v_mfma_f32_16x16x32_bf16 v[0:3], a[28:31], v[32:35], v[0:3]\n"
    "buffer_load_dwordx4 v64, s[0:3], s32 offen sc1 lds\n"
    "v_mfma_f32_16x16x32_bf16 v[4:7], a[28:31], v[36:39], v[4:7]\n"
    "v_mfma_f32_16x16x32_bf16 v[8:11], a[108:111], v[32:35], v[8:11]\n"
    "v_mfma_f32_16x16x32_bf16 v[12:15], a[108:111], v[36:39], v[12:15]\n"
    "v_mfma_f32_16x16x32_bf16 v[16:19], a[188:191], v[32:35], v[16:19]\n"
    "ds_read_b128 v[32:35], v65 offset:20480\n"
    "v_mfma_f32_16x16x32_bf16 v[20:23], a[188:191], v[36:39], v[20:23]\n"
    "ds_read_b128 v[36:39], v65 offset:21504\n"
    "s_waitcnt lgkmcnt(4)\n"
    "v_mfma_f32_16x16x32_bf16 v[0:3], a[32:35], v[40:43], v[0:3]\n"
    "buffer_load_dwordx4 v64, s[0:3], s32 offen offset:1024 sc1 lds\n"
    "v_mfma_f32_16x16x32_bf16 v[4:7], a[32:35], v[44:47], v[4:7]\n"
    "s_add_u32 m0, s37, 24576\n"
    "s_add_u32 s32, s32, 0x4000\n"
    "v_mfma_f32_16x16x32_bf16 v[8:11], a[112:115], v[40:43], v[8:11]\n"
    "v_mfma_f32_16x16x32_bf16 v[12:15], a[112:115], v[44:47], v[12:15]\n"
    "v_mfma_f32_16x16x32_bf16 v[16:19], a[192:195], v[40:43], v[16:19]\n"
    "ds_read_b128 v[40:43], v65 offset:22528\n"
    "v_mfma_f32_16x16x32_bf16 v[20:23], a[192:195], v[44:47], v[20:23]\n"
    "ds_read_b128 v[44:47], v65 offset:23552\n"
    "s_waitcnt lgkmcnt(4)\n"
    "v_mfma_f32_16x16x32_bf16 v[0:3], a[36:39], v[24:27], v[0:3]\n"
    "buffer_load_dwordx4 v64, s[0:3], s32 offen sc1 lds\n"
    "v_mfma_f32_16x16x32_bf16 v[4:7], a[36:39], v[28:31], v[4:7]\n"
    "v_mfma_f32_16x16x32_bf16 v[8:11], a[116:119], v[24:27], v[8:11]\n"
    "v_mfma_f32_16x16x32_bf16 v[12:15], a[116:119], v[28:31], v[12:15]\n"
    "v_mfma_f32_16x16x32_bf16 v[16:19], a[196:199], v[24:27], v[16:19]\n"
    "ds_read_b128 v[24:27], v65 offset:24576\n"
    "v_mfma_f32_16x16x32_bf16 v[20:23], a[196:199], v[28:31], v[20:23]\n"
    "ds_read_b128 v[28:31], v65 offset:25600\n"
    "s_waitcnt lgkmcnt(4)\n"
    "v_mfma_f32_16x16x32_bf16 v[0:3], a[40:43], v[32:35], v[0:3]\n"
    "buffer_load_dwordx4 v64, s[0:3], s32 offen offset:1024 sc1 lds\n"
    "v_mfma_f32_16x16x32_bf16 v[4:7], a[40:43], v[36:39], v[4:7]\n"
    "s_add_u32 m0, s37, 32768\n"
    "s_add_u32 s32, s32, 0x4000\n"
    "v_mfma_f32_16x16x32_bf16 v[8:11], a[120:123], v[32:35], v[8:11]\n"
    "v_mfma_f32_16x16x32_bf16 v[12:15], a[120:123], v[36:39], v[12:15]\n"
    "v_mfma_f32_16x16x32_bf16 v[16:19], a[200:203], v[32:35], v[16:19]\n"
    "ds_read_b128 v[32:35], v65 offset:26624\n"
    "v_mfma_f32_16x16x32_bf16 v[20:23], a[200:203], v[36:39], v[20:23]\n"
    "ds_read_b128 v[36:39], v65 offset:27648\n"
    "s_waitcnt lgkmcnt(4)\n"
    "v_mfma_f32_16x16x32_bf16 v[0:3], a[44:47], v[40:43], v[0:3]\n"
    "buffer_load_dwordx4 v64, s[0:3], s32 offen sc1 lds\n"
    "v_mfma_f32_16x16x32_bf16 v[4:7], a[44:47], v[44:47], v[4:7]\n"
    "v_mfma_f32_16x16x32_bf16 v[8:11], a[124:127], v[40:43], v[8:11]\n"
    "v_mfma_f32_16x16x32_bf16 v[12:15], a[124:127], v[44:47], v[12:15]\n"
    "v_mfma_f32_16x16x32_bf16 v[16:19], a[204:207], v[40:43], v[16:19]\n"
    "ds_read_b128 v[40:43], v65 offset:28672\n"
    "v_mfma_f32_16x16x32_bf16 v[20:23], a[204:207], v[44:47], v[20:23]\n"
    "ds_read_b128 v[44:47], v65 offset:29696\n"
    "s_waitcnt lgkmcnt(4)\n"
    "v_mfma_f32_16x16x32_bf16 v[0:3], a[48:51], v[24:27], v[0:3]\n"
    "buffer_load_dwordx4 v64, s[0:3], s32 offen offset:1024 sc1 lds\n"
    "v_mfma_f32_16x16x32_bf16 v[4:7], a[48:51], v[28:31], v[4:7]\n"
    "s_waitcnt vmcnt(10)\n"
    "v_mov_b32 v61, s44\n"
    "v_mfma_f32_16x16x32_bf16 v[8:11], a[128:131], v[24:27], v[8:11]\n"
    "v_mfma_f32_16x16x32_bf16 v[12:15], a[128:131], v[28:31], v[12:15]\n"
    "v_mfma_f32_16x16x32_bf16 v[16:19], a[208:211], v[24:27], v[16:19]\n"
    "buffer_store_dword v61, v69, s[0:3], s43 offen sc1\n"
    "ds_read_b128 v[24:27], v65 offset:30720\n"
    "v_mfma_f32_16x16x32_bf16 v[20:23], a[208:211], v[28:31], v[20:23]\n"
    "s_mov_b32 s46, s34\n"
    "ds_read_b128 v[28:31], v65 offset:31744\n"
    "s_waitcnt lgkmcnt(4)\n"
    "v_mfma_f32_16x16x32_bf16 v[0:3], a[52:55], v[32:35], v[0:3]\n"
    "s_mov_b32 s43, s41\n"
    "s_mov_b32 s44, s42\n"
    "s_mov_b32 s41, s39\n"
    "v_mfma_f32_16x16x32_bf16 v[4:7], a[52:55], v[36:39], v[4:7]\n"
    "s_mov_b32 s42, s40\n"
    "s_mov_b32 s34, s33\n"
    "s_mov_b32 s35, s36\n"
    "v_mfma_f32_16x16x32_bf16 v[8:11], a[132:135], v[32:35], v[8:11]\n"
    "s_xor_b32 s37, s37, 40960\n"
    "s_add_u32 s26, s26, 1\n"
    "s_sub_u32 s27, s26, 14\n"
    "v_mfma_f32_16x16x32_bf16 v[12:15], a[132:135], v[36:39], v[12:15]\n"
    "s_ashr_i32 s28, s27, 1\n"
    "s_and_b32 s29, s27, 1\n"
    "s_add_u32 s30, s28, 14\n"
    "v_mfma_f32_16x16x32_bf16 v[16:19], a[212:215], v[32:35], v[16:19]\n"
    "ds_read_b128 v[32:35], v65 offset:32768\n"
    "v_mfma_f32_16x16x32_bf16 v[20:23], a[212:215], v[36:39], v[20:23]\n"
    "ds_read_b128 v[36:39], v65 offset:33792\n"
    "s_waitcnt lgkmcnt(4)\n"
    "v_mfma_f32_16x16x32_bf16 v[0:3], a[56:59], v[40:43], v[0:3]\n"
    "s_cmp_lt_i32 s27, 0\n"
    "s_cselect_b32 s29, 0, s29\n"
    "s_cselect_b32 s30, s26, s30\n"
    "v_mfma_f32_16x16x32_bf16 v[4:7], a[56:59], v[44:47], v[4:7]\n"
    "s_cmp_eq_u32 s29, 1\n"
    "s_cselect_b32 s30, s28, s30\n"
    "s_cselect_b32 s27, s15, 64\n"
    "v_mfma_f32_16x16x32_bf16 v[8:11], a[136:139], v[40:43], v[8:11]\n"
    "s_lshl_b32 s31, s29, 13\n"
    "s_add_u32 s40, s30, 1\n"
    "s_lshl_b32 s32, s30, 19\n"
    "v_mfma_f32_16x16x32_bf16 v[12:15], a[136:139], v[44:47], v[12:15]\n"
    "s_lshl_b32 s25, s30, 11\n"
    "s_xor_b32 s25, s25, s31\n"
    "s_and_b32 s25, s25, 0x2000\n"
    "v_mfma_f32_16x16x32_bf16 v[16:19], a[216:219], v[40:43], v[16:19]\n"
    "ds_read_b128 v[40:43], v65 offset:34816\n"
    "s_add_u32 s32, s32, s25\n"
    "v_mfma_f32_16x16x32_bf16 v[20:23], a[216:219], v[44:47], v[20:23]\n"
    "s_add_u32 s32, s32, s13\n"
    "ds_read_b128 v[44:47], v65 offset:35840\n"
    "s_waitcnt lgkmcnt(4)\n"
    "v_mfma_f32_16x16x32_bf16 v[0:3], a[60:63], v[24:27], v[0:3]\n"
    "s_lshl_b32 s33, s30, 20\n"
    "s_lshl_b32 s25, s30, 12\n"
    "s_xor_b32 s25, s25, s31\n"
    "v_mfma_f32_16x16x32_bf16 v[4:7], a[60:63], v[28:31], v[4:7]\n"
    "s_and_b32 s25, s25, 0x2000\n"
    "s_add_u32 s33, s33, s25\n"
    "s_add_u32 s33, s33, s14\n"
    "v_mfma_f32_16x16x32_bf16 v[8:11], a[140:143], v[24:27], v[8:11]\n"
    "s_mul_i32 s47, s40, s29\n"
    "v_mfma_f32_16x16x32_bf16 v[12:15], a[140:143], v[28:31], v[12:15]\n"
    "s_cmp_lt_u32 s30, s27\n"
    "s_cselect_b32 s36, 1, 0\n"
    "s_cselect_b32 s32, s32, 2130706432\n"
    "v_mfma_f32_16x16x32_bf16 v[16:19], a[220:223], v[24:27], v[16:19]\n"
    "ds_read_b128 v[24:27], v65 offset:36864\n"
    "v_mfma_f32_16x16x32_bf16 v[20:23], a[220:223], v[28:31], v[20:23]\n"
    "ds_read_b128 v[28:31], v65 offset:37888\n"
    "s_waitcnt lgkmcnt(4)\n"
    "v_mfma_f32_16x16x32_bf16 v[0:3], a[64:67], v[32:35], v[0:3]\n"
    "s_cmp_lt_u32 s30, s27\n"
    "s_cselect_b32 s39, s31, 2130706432\n"
    "s_cselect_b32 s47, s47, 0\n"
    "v_mfma_f32_16x16x32_bf16 v[4:7], a[64:67], v[36:39], v[4:7]\n"
    "s_cmp_gt_u32 s47, 0\n"
    "s_cselect_b32 s38, s17, 2130706432\n"
    "v_mfma_f32_16x16x32_bf16 v[8:11], a[144:147], v[32:35], v[8:11]\n"
    "buffer_load_dword v60, v68, s[0:3], s38 offen sc1\n"
    "v_mfma_f32_16x16x32_bf16 v[12:15], a[144:147], v[36:39], v[12:15]\n"
    "v_mfma_f32_16x16x32_bf16 v[16:19], a[224:227], v[32:35], v[16:19]\n"
    "ds_read_b128 v[32:35], v65 offset:38912\n"
    "v_mfma_f32_16x16x32_bf16 v[20:23], a[224:227], v[36:39], v[20:23]\n"
    "ds_read_b128 v[36:39], v65 offset:39936\n"
    "s_waitcnt lgkmcnt(4)\n"
    "v_mfma_f32_16x16x32_bf16 v[0:3], a[68:71], v[40:43], v[0:3]\n"
    "v_mfma_f32_16x16x32_bf16 v[4:7], a[68:71], v[44:47], v[4:7]\n"
    "v_mfma_f32_16x16x32_bf16 v[8:11], a[148:151], v[40:43], v[8:11]\n"
    "v_mfma_f32_16x16x32_bf16 v[12:15], a[148:151], v[44:47], v[12:15]\n"
    "v_mfma_f32_16x16x32_bf16 v[16:19], a[228:231], v[40:43], v[16:19]\n"
    "v_mfma_f32_16x16x32_bf16 v[20:23], a[228:231], v[44:47], v[20:23]\n"
    "s_waitcnt lgkmcnt(2)\n"
    "v_mfma_f32_16x16x32_bf16 v[0:3], a[72:75], v[24:27], v[0:3]\n"
    "v_mfma_f32_16x16x32_bf16 v[4:7], a[72:75], v[28:31], v[4:7]\n"
    "v_mfma_f32_16x16x32_bf16 v[8:11], a[152:155], v[24:27], v[8:11]\n"
    "v_mfma_f32_16x16x32_bf16 v[12:15], a[152:155], v[28:31], v[12:15]\n"
    "v_mfma_f32_16x16x32_bf16 v[16:19], a[232:235], v[24:27], v[16:19]\n"
    "v_mfma_f32_16x16x32_bf16 v[20:23], a[232:235], v[28:31], v[20:23]\n"
    "s_waitcnt lgkmcnt(0)\n"
    "v_mfma_f32_16x16x32_bf16 v[0:3], a[76:79], v[32:35], v[0:3]\n"
    "v_mfma_f32_16x16x32_bf16 v[4:7], a[76:79], v[36:39], v[4:7]\n"
    "v_mfma_f32_16x16x32_bf16 v[8:11], a[156:159], v[32:35], v[8:11]\n"
    "v_mfma_f32_16x16x32_bf16 v[12:15], a[156:159], v[36:39], v[12:15]\n"
    "v_mfma_f32_16x16x32_bf16 v[16:19], a[236:239], v[32:35], v[16:19]\n"
    "v_mfma_f32_16x16x32_bf16 v[20:23], a[236:239], v[36:39], v[20:23]\n"
    "v_cvt_pk_bf16_f32 v48, v0, v1\n"
    "v_cvt_pk_bf16_f32 v49, v2, v3\n"
    "v_cvt_pk_bf16_f32 v50, v8, v9\n"
    "v_cvt_pk_bf16_f32 v51, v10, v11\n"
    "v_cvt_pk_bf16_f32 v52, v4, v5\n"
    "v_cvt_pk_bf16_f32 v53, v6, v7\n"
    "v_cvt_pk_bf16_f32 v54, v12, v13\n"
    "v_cvt_pk_bf16_f32 v55, v14, v15\n"
    "v_cvt_pk_bf16_f32 v56, v16, v17\n"
    "v_cvt_pk_bf16_f32 v57, v18, v19\n"
    "v_cvt_pk_bf16_f32 v58, v20, v21\n"
    "v_cvt_pk_bf16_f32 v59, v22, v23\n"
    ".Lqw_mk1x7_%=:\n"
    ".Lqw_join_%=:\n"
    "v_xor_b32 v65, 40960, v65\n"
    "s_waitcnt vmcnt(0) lgkmcnt(0)\n"
    "s_barrier\n"
    ".Lqw_mk2x8_%=:\n"
    "s_cmp_le_u32 s26, 142\n"
    "s_cbranch_scc1 .Lqw_top_%=\n"
    "buffer_store_dwordx4 v[48:51], v66, s[0:3], s46 offen offset:0 sc1\n"
    "buffer_store_dwordx4 v[52:55], v66, s[0:3], s46 offen offset:1024 sc1\n"
    "buffer_store_dwordx4 v[56:59], v67, s[0:3], s46 offen sc1\n"
    "s_waitcnt vmcnt(0)\n"
    "v_mov_b32 v61, s44\n"
    "buffer_store_dword v61, v69, s[0:3], s43 offen sc1\n"
    "s_waitcnt vmcnt(0) lgkmcnt(0)\n"
    "s_endpgm\n"
    ".Lqw_tk0_%=:\n"
    ".Lqw_po1_%=:\n"
    "buffer_load_dword v5, v4, s[0:3], 0 offen sc1\n"
    "s_waitcnt vmcnt(0)\n"
    "v_cmp_gt_i32 vcc, 32, v5\n"
    "s_nop 0\n"
    "s_cbranch_vccz .Lqw_pd2_%=\n"
    "s_add_u32 s20, s20, 1\n"
    "s_cmp_lt_u32 s20, 0x40000\n"
    "s_cbranch_scc1 .Lqw_sp3_%=\n"
    "v_mov_b32 v5, 90\n"
    "s_mov_b32 s24, 33728\n"
    "buffer_store_dword v5, off, s[0:3], s24 sc1\n"
    "s_waitcnt vmcnt(0)\n"
    "s_branch .Lqw_pd2_%=\n"
    ".Lqw_sp3_%=:\n"
    "s_sleep 8\n"
    "s_branch .Lqw_po1_%=\n"
    ".Lqw_pd2_%=:\n"
    "s_branch .Lqw_tk1_%=\n"
    ".Lqw_wx_%=:\n"
    "buffer_store_dwordx4 v[48:51], v66, s[0:3], s46 offen offset:0 sc1\n"
    "buffer_store_dwordx4 v[52:55], v66, s[0:3], s46 offen offset:1024 sc1\n"
    "buffer_store_dwordx4 v[56:59], v67, s[0:3], s46 offen sc1\n"
    "s_waitcnt vmcnt(0)\n"
    "v_mov_b32 v61, s44\n"
    "buffer_store_dword v61, v69, s[0:3], s43 offen sc1\n"
    "s_mov_b32 s43, 2130706432\n"
    "s_mov_b32 s46, 2130706432\n"
    ".Lqw_rt10_%=:\n"
    "s_memrealtime s[22:23]\n"
    "s_waitcnt lgkmcnt(0)\n"
    "s_sub_u32 s24, s45, s22\n"
    "s_cmp_lt_u32 s24, 600\n"
    "s_cbranch_scc1 .Lqw_rt10_%=\n"
    ".Lqw_po11_%=:\n"
    "buffer_load_dword v60, v68, s[0:3], s17 offen sc1\n"
    "s_waitcnt vmcnt(0)\n"
    "s_memrealtime s[22:23]\n"
    "s_waitcnt lgkmcnt(0)\n"
    "s_add_u32 s45, s22, 232\n"
    "v_cmp_gt_i32 vcc, s47, v60\n"
    "s_nop 0\n"
    "s_cbranch_vccz .Lqw_pd12_%=\n"
    "s_add_u32 s20, s20, 1\n"
    "s_cmp_lt_u32 s20, 0x40000\n"
    "s_cbranch_scc1 .Lqw_sp13_%=\n"
    "v_mov_b32 v60, 51\n"
    "s_mov_b32 s24, 33728\n"
    "buffer_store_dword v60, off, s[0:3], s24 sc1\n"
    "s_waitcnt vmcnt(0)\n"
    "s_branch .Lqw_pd12_%=\n"
    ".Lqw_sp13_%=:\n"
    "s_sleep 10\n"
    "s_branch .Lqw_po11_%=\n"
    ".Lqw_pd12_%=:\n"
    "s_branch .Lqw_wxb_%=\n"
    ".Lqw_skip_%=:\n"
    "buffer_store_dwordx4 v[48:51], v66, s[0:3], s46 offen offset:0 sc1\n"
    "buffer_store_dwordx4 v[52:55], v66, s[0:3], s46 offen offset:1024 sc1\n"
    "buffer_store_dwordx4 v[56:59], v67, s[0:3], s46 offen sc1\n"
    "s_add_u32 m0, s37, 0\n"
    "s_nop 0\n"
    "buffer_load_dwordx4 v64, s[0:3], s32 offen sc1 lds\n"
    "buffer_load_dwordx4 v64, s[0:3], s32 offen offset:1024 sc1 lds\n"
    "s_add_u32 m0, s37, 8192\n"
    "s_add_u32 s32, s32, 0x4000\n"
    "buffer_load_dwordx4 v64, s[0:3], s32 offen sc1 lds\n"
    "buffer_load_dwordx4 v64, s[0:3], s32 offen offset:1024 sc1 lds\n"
    "s_add_u32 m0, s37, 16384\n"
    "s_add_u32 s32, s32, 0x4000\n"
    "buffer_load_dwordx4 v64, s[0:3], s32 offen sc1 lds\n"
    "buffer_load_dwordx4 v64, s[0:3], s32 offen offset:1024 sc1 lds\n"
    "s_add_u32 m0, s37, 24576\n"
    "s_add_u32 s32, s32, 0x4000\n"
    "buffer_load_dwordx4 v64, s[0:3], s32 offen sc1 lds\n"
    "buffer_load_dwordx4 v64, s[0:3], s32 offen offset:1024 sc1 lds\n"
    "s_add_u32 m0, s37, 32768\n"
    "s_add_u32 s32, s32, 0x4000\n"
    "buffer_load_dwordx4 v64, s[0:3], s32 offen sc1 lds\n"
    "buffer_load_dwordx4 v64, s[0:3], s32 offen offset:1024 sc1 lds\n"
    "s_waitcnt vmcnt(0)\n"
    "v_mov_b32 v61, s44\n"
    "buffer_store_dword v61, v69, s[0:3], s43 offen sc1\n"
    "s_mov_b32 s34, 2130706432\n"
    "s_mov_b32 s46, s34\n"
    "s_mov_b32 s43, s41\n"
    "s_mov_b32 s44, s42\n"
    "s_mov_b32 s41, s39\n"
    "s_mov_b32 s42, s40\n"
    "s_mov_b32 s34, s33\n"
    "s_mov_b32 s35, s36\n"
    "s_xor_b32 s37, s37, 40960\n"
    "s_add_u32 s26, s26, 1\n"
    "s_sub_u32 s27, s26, 14\n"
    "s_ashr_i32 s28, s27, 1\n"
    "s_and_b32 s29, s27, 1\n"
    "s_add_u32 s30, s28, 14\n"
    "s_cmp_lt_i32 s27, 0\n"
    "s_cselect_b32 s29, 0, s29\n"
    "s_cselect_b32 s30, s26, s30\n"
    "s_cmp_eq_u32 s29, 1\n"
    "s_cselect_b32 s30, s28, s30\n"
    "s_cselect_b32 s27, s15, 64\n"
    "s_lshl_b32 s31, s29, 13\n"
    "s_add_u32 s40, s30, 1\n"
    "s_lshl_b32 s32, s30, 19\n"
    "s_lshl_b32 s25, s30, 11\n"
    "s_xor_b32 s25, s25, s31\n"
    "s_and_b32 s25, s25, 0x2000\n"
    "s_add_u32 s32, s32, s25\n"
    "s_add_u32 s32, s32, s13\n"
    "s_lshl_b32 s33, s30, 20\n"
    "s_lshl_b32 s25, s30, 12\n"
    "s_xor_b32 s25, s25, s31\n"
    "s_and_b32 s25, s25, 0x2000\n"
    "s_add_u32 s33, s33, s25\n"
    "s_add_u32 s33, s33, s14\n"
    "s_mul_i32 s47, s40, s29\n"
    "s_cmp_lt_u32 s30, s27\n"
    "s_cselect_b32 s36, 1, 0\n"
    "s_cselect_b32 s32, s32, 2130706432\n"
    "s_cmp_lt_u32 s30, s27\n"
    "s_cselect_b32 s39, s31, 2130706432\n"
    "s_cselect_b32 s47, s47, 0\n"
    "s_cmp_gt_u32 s47, 0\n"
    "s_cselect_b32 s38, s17, 2130706432\n"
    "buffer_load_dword v60, v68, s[0:3], s38 offen sc1\n"
    "s_branch .Lqw_join_%=\n"
    :: "{s[0:3]}"(s0_), "{s[4:7]}"(s4_), "{s[8:11]}"(s8_), "{s[12:15]}"(s12_), "{s[16:19]}"(s16_), "{v[0:3]}"(v0_), "{v[64:67]}"(v64_), "{v[68:71]}"(v68_) : "memory", "v255", "a255", "s95");
    __builtin_unreachable();
}
DEV void b1_role(const MegaArgs& A, const int L, const int i, const int rep) {
    ROLE_HEAD;
    const int qd = 4 * w + lq, sw = (l16 & 6) << 3, hw = w < 3 ? w : 2, hd = 3 * i + w;
    const bool live = w < 3 && hd < 32;
    const unsigned om = live ? RC_O - RC_X1 - w * 2048 + (hd >> 3) * 16384 + ((hd >> 1) & 3) * 2048 + (lq & 1) * 1024 + (2 * (hd & 1) + (lq >> 1)) * 256 + l16 * 16 : OOB;
    const unsigned ox = live && l < 16 ? RC_O - RC_X1 - w * 2048 + 65536 + hd * 256 + l16 * 16 : OOB;
    u64 wm[4];
#pragma unroll
    for (int e = 0; e < 4; ++e) { const int d = l16 + 16 - 16 * (e >> 1) - 4 * lq - 2 * (e & 1) - (l16 & 1); wm[e] = __ballot(d >= 0 && d <= 14); }
    const u32x4 s0_ = {RFL(rx[0]), RFL(rx[1]), RFL(rx[2]), RFL(rx[3])};
    const u32x4 s4_ = {RFL(lo(A.wqkv + 60 * i + 16 * w)), RFL(hi(A.wqkv + 60 * i + 16 * w)), RFL((640 * 1920 - 60 * i - 16 * w) * 2), RFL(0x20000u)};
    const u32x4 s8_ = {RFL((unsigned)wm[0]), RFL((unsigned)(wm[0] >> 32)), RFL((unsigned)wm[1]), RFL((unsigned)(wm[1] >> 32))};
    const u32x4 s12_ = {RFL((unsigned)wm[2]), RFL((unsigned)(wm[2] >> 32)), RFL((unsigned)wm[3]), RFL((unsigned)(wm[3] >> 32))};
    const u32x4 s16_ = {RFL(w == 3 ? 0xf000f000u : 0u), RFL(w == 3 ? 0xf000f000u : 0u), RFL(w), RFL(L << 13)};
    const u32x4 s20_ = {RFL(REC0 + RC_X1 + w * 2048), RFL(xl(Z_X1 + rep * 4096, L)), RFL(DBG0 + ((L * 128 + 96 + i) * 4 + w) * 2048), RFL(blockIdx.x == 0 ? 6 : 0)};
    const u32x4 v0_ = {(unsigned)((l >> 1) * 3840 + (l & 1) * 16 + 0), (unsigned)((l >> 1) * 3840 + (l & 1) * 16 + 256), (unsigned)((l >> 1) * 3840 + (l & 1) * 16 + 512), (unsigned)(w * 40960 + (8 * lq + (l16 >> 2)) * 32 + (l & 3) * 8)};
    const u32x4 v224_ = {(unsigned)(l * 16 + ((w + 0) & 3) * 2048), (unsigned)(l * 16 + ((w + 1) & 3) * 2048), (unsigned)(l * 16 + ((w + 2) & 3) * 2048), (unsigned)(l * 16 + ((w + 3) & 3) * 2048)};
    const u32x4 v228_ = {(unsigned)(l * 16), (unsigned)((l & 31) * 128), (unsigned)(l < 4 && w == 3 ? xl(Z_O + l * 2048 + i * 128, L) : OOB), (unsigned)(qd == 15 ? 86016 : 40960 + (qd / 5) * 2048 + l16 * 64 + (((qd % 5) * 8) ^ sw))};
    const u32x4 v232_ = {(unsigned)(qd == 15 ? 86016 : 49152 + (qd / 5) * 4096 + l16 * 64 + (((qd % 5) * 8) ^ sw)), (unsigned)(qd == 15 ? 86016 : 61440 + (qd / 5) * 4096 + l16 * 64 + (((qd % 5) * 8) ^ sw)), (unsigned)(40960 + hw * 2048 + l16 * 64 + ((lq * 16) ^ sw)), (unsigned)(49152 + hw * 4096 + l16 * 64 + ((lq * 16) ^ sw))};
    const u32x4 v236_ = {(unsigned)(51200 + hw * 4096 + l16 * 64 + ((lq * 16) ^ sw)), (unsigned)(61440 + hw * 4096 + (l >> 2) * 64 + ((0 + (l & 3) * 8) ^ (((l >> 2) & 6) << 3))), (unsigned)(63488 + hw * 4096 + (l >> 2) * 64 + ((0 + (l & 3) * 8) ^ (((l >> 2) & 6) << 3))), (unsigned)(61440 + hw * 4096 + (l >> 2) * 64 + ((32 + (l & 3) * 8) ^ (((l >> 2) & 6) << 3)))};
    const u32x4 v240_ = {(unsigned)(63488 + hw * 4096 + (l >> 2) * 64 + ((32 + (l & 3) * 8) ^ (((l >> 2) & 6) << 3))), (unsigned)(73728 + w * 2048 + l * 16), (unsigned)(73728 + (l >> 5) * 1024 + ((l16 >> 2) * 16 + l16) * 16 + (l & 3) * 4), (unsigned)(81920 + w * 512 + l * 4)};
    const u32x4 v244_ = {(unsigned)(81920 + 3 * 512 + (l >> 5) * 256 + (48 + l16) * 4), (unsigned)(om), (unsigned)(ox), (unsigned)((l & 1) ? 0x01000c0cu : 0x0c0c0100u)};
    const u32x4 v248_ = {(unsigned)((l & 1) ? 0x03020c0cu : 0x0c0c0302u), (unsigned)(0xff800000u), (unsigned)(0u), (unsigned)(0u)};
    asm volatile(
    "s_mov_b32 s24, 0\n"
    "s_mov_b64 s[26:27], 0\n"
    "s_mov_b32 s29, 0\n"
    "s_cmp_eq_u32 s19, 0\n"
    "s_cselect_b32 s28, 12, 50\n"
    "s_sub_u32 s28, s28, s23\n"
    ".Lb1_ho1_%=:\n"
    "s_sub_u32 s28, s28, 1\n"
    "s_cbranch_scc1 .Lb1_hd2_%=\n"
    "s_sleep 17\n"
    "s_branch .Lb1_ho1_%=\n"
    ".Lb1_hd2_%=:\n"
    "v_and_b32 v30, 0x70, v228\n"
    "v_lshlrev_b32 v30, 2, v30\n"
    "v_or_b32 v30, 0x8000, v30\n"
    "buffer_load_dword v31, v30, s[0:3], 0 offen sc1\n"
    "s_waitcnt vmcnt(0)\n"
    "v_cmp_gt_i32 vcc, 32, v31\n"
    "s_nop 0\n"
    "s_cbranch_vccnz .Lb1_tk0_%=\n"
    ".Lb1_tk1_%=:\n"
    "s_mul_i32 s32, s18, 40960\n"
    "s_add_u32 s35, s18, 0\n"
    "s_and_b32 s35, s35, 3\n"
    "s_add_u32 s35, s35, 0\n"
    "s_mul_i32 s35, s35, 122880\n"
    "s_add_u32 m0, s32, 0\n"
    "s_nop 0\n"
    "buffer_load_dwordx4 v0, s[4:7], s35 offen offset:0 lds\n"
    "buffer_load_dwordx4 v1, s[4:7], s35 offen offset:1024 lds\n"
    "buffer_load_dwordx4 v2, s[4:7], s35 offen offset:2048 lds\n"
    "s_add_u32 s35, s18, 1\n"
    "s_and_b32 s35, s35, 3\n"
    "s_add_u32 s35, s35, 0\n"
    "s_mul_i32 s35, s35, 122880\n"
    "s_add_u32 m0, s32, 3072\n"
    "s_nop 0\n"
    "buffer_load_dwordx4 v0, s[4:7], s35 offen offset:0 lds\n"
    "buffer_load_dwordx4 v1, s[4:7], s35 offen offset:1024 lds\n"
    "buffer_load_dwordx4 v2, s[4:7], s35 offen offset:2048 lds\n"
    "s_add_u32 s35, s18, 2\n"
    "s_and_b32 s35, s35, 3\n"
    "s_add_u32 s35, s35, 0\n"
    "s_mul_i32 s35, s35, 122880\n"
    "s_add_u32 m0, s32, 6144\n"
    "s_nop 0\n"
    "buffer_load_dwordx4 v0, s[4:7], s35 offen offset:0 lds\n"
    "buffer_load_dwordx4 v1, s[4:7], s35 offen offset:1024 lds\n"
    "buffer_load_dwordx4 v2, s[4:7], s35 offen offset:2048 lds\n"
    "s_add_u32 s35, s18, 3\n"
    "s_and_b32 s35, s35, 3\n"
    "s_add_u32 s35, s35, 0\n"
    "s_mul_i32 s35, s35, 122880\n"
    "s_add_u32 m0, s32, 9216\n"
    "s_nop 0\n"
    "buffer_load_dwordx4 v0, s[4:7], s35 offen offset:0 lds\n"
    "buffer_load_dwordx4 v1, s[4:7], s35 offen offset:1024 lds\n"
    "buffer_load_dwordx4 v2, s[4:7], s35 offen offset:2048 lds\n"
    "s_add_u32 s35, s18, 0\n"
    "s_and_b32 s35, s35, 3\n"
    "s_add_u32 s35, s35, 4\n"
    "s_mul_i32 s35, s35, 122880\n"
    "s_add_u32 m0, s32, 12288\n"
    "s_nop 0\n"
    "buffer_load_dwordx4 v0, s[4:7], s35 offen offset:0 lds\n"
    "buffer_load_dwordx4 v1, s[4:7], s35 offen offset:1024 lds\n"
    "buffer_load_dwordx4 v2, s[4:7], s35 offen offset:2048 lds\n"
    "s_add_u32 s35, s18, 1\n"
    "s_and_b32 s35, s35, 3\n"
    "s_add_u32 s35, s35, 4\n"
    "s_mul_i32 s35, s35, 122880\n"
    "s_add_u32 m0, s32, 15360\n"
    "s_nop 0\n"
    "buffer_load_dwordx4 v0, s[4:7], s35 offen offset:0 lds\n"
    "buffer_load_dwordx4 v1, s[4:7], s35 offen offset:1024 lds\n"
    "buffer_load_dwordx4 v2, s[4:7], s35 offen offset:2048 lds\n"
    "s_add_u32 s35, s18, 2\n"
    "s_and_b32 s35, s35, 3\n"
    "s_add_u32 s35, s35, 4\n"
    "s_mul_i32 s35, s35, 122880\n"
    "s_add_u32 m0, s32, 18432\n"
    "s_nop 0\n"
    "buffer_load_dwordx4 v0, s[4:7], s35 offen offset:0 lds\n"
    "buffer_load_dwordx4 v1, s[4:7], s35 offen offset:1024 lds\n"
    "buffer_load_dwordx4 v2, s[4:7], s35 offen offset:2048 lds\n"
    "s_add_u32 s35, s18, 3\n"
    "s_and_b32 s35, s35, 3\n"
    "s_add_u32 s35, s35, 4\n"
    "s_mul_i32 s35, s35, 122880\n"
    "s_add_u32 m0, s32, 21504\n"
    "s_nop 0\n"
    "buffer_load_dwordx4 v0, s[4:7], s35 offen offset:0 lds\n"
    "buffer_load_dwordx4 v1, s[4:7], s35 offen offset:1024 lds\n"
    "buffer_load_dwordx4 v2, s[4:7], s35 offen offset:2048 lds\n"
    "s_add_u32 s35, s18, 0\n"
    "s_and_b32 s35, s35, 3\n"
    "s_add_u32 s35, s35, 8\n"
    "s_mul_i32 s35, s35, 122880\n"
    "s_add_u32 m0, s32, 24576\n"
    "s_nop 0\n"
    "buffer_load_dwordx4 v0, s[4:7], s35 offen offset:0 lds\n"
    "buffer_load_dwordx4 v1, s[4:7], s35 offen offset:1024 lds\n"
    "buffer_load_dwordx4 v2, s[4:7], s35 offen offset:2048 lds\n"
    "s_add_u32 s35, s18, 1\n"
    "s_and_b32 s35, s35, 3\n"
    "s_add_u32 s35, s35, 8\n"
    "s_mul_i32 s35, s35, 122880\n"
    "s_add_u32 m0, s32, 27648\n"
    "s_nop 0\n"
    "buffer_load_dwordx4 v0, s[4:7], s35 offen offset:0 lds\n"
    "buffer_load_dwordx4 v1, s[4:7], s35 offen offset:1024 lds\n"
    "buffer_load_dwordx4 v2, s[4:7], s35 offen offset:2048 lds\n"
    "s_add_u32 s35, s18, 2\n"
    "s_and_b32 s35, s35, 3\n"
    "s_add_u32 s35, s35, 8\n"
    "s_mul_i32 s35, s35, 122880\n"
    "s_add_u32 m0, s32, 30720\n"
    "s_nop 0\n"
    "buffer_load_dwordx4 v0, s[4:7], s35 offen offset:0 lds\n"
    "buffer_load_dwordx4 v1, s[4:7], s35 offen offset:1024 lds\n"
    "buffer_load_dwordx4 v2, s[4:7], s35 offen offset:2048 lds\n"
    "s_add_u32 s35, s18, 3\n"
    "s_and_b32 s35, s35, 3\n"
    "s_add_u32 s35, s35, 8\n"
    "s_mul_i32 s35, s35, 122880\n"
    "s_add_u32 m0, s32, 33792\n"
    "s_nop 0\n"
    "buffer_load_dwordx4 v0, s[4:7], s35 offen offset:0 lds\n"
    "buffer_load_dwordx4 v1, s[4:7], s35 offen offset:1024 lds\n"
    "buffer_load_dwordx4 v2, s[4:7], s35 offen offset:2048 lds\n"
    "s_add_u32 s35, s18, 0\n"
    "s_and_b32 s35, s35, 3\n"
    "s_add_u32 s35, s35, 12\n"
    "s_mul_i32 s35, s35, 122880\n"
    "s_add_u32 m0, s32, 36864\n"
    "s_nop 0\n"
    "buffer_load_dwordx4 v0, s[4:7], s35 offen offset:0 lds\n"
    "buffer_load_dwordx4 v1, s[4:7], s35 offen offset:1024 lds\n"
    "buffer_load_dwordx4 v2, s[4:7], s35 offen offset:2048 lds\n"
    "s_waitcnt vmcnt(36)\n"
    "ds_read_b64_tr_b16 a[0:1], v3 offset:0\n"
    "ds_read_b64_tr_b16 a[2:3], v3 offset:128\n"
    "ds_read_b64_tr_b16 a[80:81], v3 offset:1024\n"
    "ds_read_b64_tr_b16 a[82:83], v3 offset:1152\n"
    "ds_read_b64_tr_b16 a[160:161], v3 offset:2048\n"
    "ds_read_b64_tr_b16 a[162:163], v3 offset:2176\n"
    "s_waitcnt lgkmcnt(0)\n"
    "s_add_u32 s35, s18, 1\n"
    "s_and_b32 s35, s35, 3\n"
    "s_add_u32 s35, s35, 12\n"
    "s_mul_i32 s35, s35, 122880\n"
    "s_add_u32 m0, s32, 0\n"
    "s_nop 0\n"
    "buffer_load_dwordx4 v0, s[4:7], s35 offen offset:0 lds\n"
    "buffer_load_dwordx4 v1, s[4:7], s35 offen offset:1024 lds\n"
    "buffer_load_dwordx4 v2, s[4:7], s35 offen offset:2048 lds\n"
    "s_waitcnt vmcnt(36)\n"
    "ds_read_b64_tr_b16 a[4:5], v3 offset:3072\n"
    "ds_read_b64_tr_b16 a[6:7], v3 offset:3200\n"
    "ds_read_b64_tr_b16 a[84:85], v3 offset:4096\n"
    "ds_read_b64_tr_b16 a[86:87], v3 offset:4224\n"
    "ds_read_b64_tr_b16 a[164:165], v3 offset:5120\n"
    "ds_read_b64_tr_b16 a[166:167], v3 offset:5248\n"
    "s_waitcnt lgkmcnt(0)\n"
    "s_add_u32 s35, s18, 2\n"
    "s_and_b32 s35, s35, 3\n"
    "s_add_u32 s35, s35, 12\n"
    "s_mul_i32 s35, s35, 122880\n"
    "s_add_u32 m0, s32, 3072\n"
    "s_nop 0\n"
    "buffer_load_dwordx4 v0, s[4:7], s35 offen offset:0 lds\n"
    "buffer_load_dwordx4 v1, s[4:7], s35 offen offset:1024 lds\n"
    "buffer_load_dwordx4 v2, s[4:7], s35 offen offset:2048 lds\n"
    "s_waitcnt vmcnt(36)\n"
    "ds_read_b64_tr_b16 a[8:9], v3 offset:6144\n"
    "ds_read_b64_tr_b16 a[10:11], v3 offset:6272\n"
    "ds_read_b64_tr_b16 a[88:89], v3 offset:7168\n"
    "ds_read_b64_tr_b16 a[90:91], v3 offset:7296\n"
    "ds_read_b64_tr_b16 a[168:169], v3 offset:8192\n"
    "ds_read_b64_tr_b16 a[170:171], v3 offset:8320\n"
    "s_waitcnt lgkmcnt(0)\n"
    "s_add_u32 s35, s18, 3\n"
    "s_and_b32 s35, s35, 3\n"
    "s_add_u32 s35, s35, 12\n"
    "s_mul_i32 s35, s35, 122880\n"
    "s_add_u32 m0, s32, 6144\n"
    "s_nop 0\n"
    "buffer_load_dwordx4 v0, s[4:7], s35 offen offset:0 lds\n"
    "buffer_load_dwordx4 v1, s[4:7], s35 offen offset:1024 lds\n"
    "buffer_load_dwordx4 v2, s[4:7], s35 offen offset:2048 lds\n"
    "s_waitcnt vmcnt(36)\n"
    "ds_read_b64_tr_b16 a[12:13], v3 offset:9216\n"
    "ds_read_b64_tr_b16 a[14:15], v3 offset:9344\n"
    "ds_read_b64_tr_b16 a[92:93], v3 offset:10240\n"
    "ds_read_b64_tr_b16 a[94:95], v3 offset:10368\n"
    "ds_read_b64_tr_b16 a[172:173], v3 offset:11264\n"
    "ds_read_b64_tr_b16 a[174:175], v3 offset:11392\n"
    "s_waitcnt lgkmcnt(0)\n"
    "s_add_u32 s35, s18, 0\n"
    "s_and_b32 s35, s35, 3\n"
    "s_add_u32 s35, s35, 16\n"
    "s_mul_i32 s35, s35, 122880\n"
    "s_add_u32 m0, s32, 9216\n"
    "s_nop 0\n"
    "buffer_load_dwordx4 v0, s[4:7], s35 offen offset:0 lds\n"
    "buffer_load_dwordx4 v1, s[4:7], s35 offen offset:1024 lds\n"
    "buffer_load_dwordx4 v2, s[4:7], s35 offen offset:2048 lds\n"
    "s_waitcnt vmcnt(36)\n"
    "ds_read_b64_tr_b16 a[16:17], v3 offset:12288\n"
    "ds_read_b64_tr_b16 a[18:19], v3 offset:12416\n"
    "ds_read_b64_tr_b16 a[96:97], v3 offset:13312\n"
    "ds_read_b64_tr_b16 a[98:99], v3 offset:13440\n"
    "ds_read_b64_tr_b16 a[176:177], v3 offset:14336\n"
    "ds_read_b64_tr_b16 a[178:179], v3 offset:14464\n"
    "s_waitcnt lgkmcnt(0)\n"
    "s_add_u32 s35, s18, 1\n"
    "s_and_b32 s35, s35, 3\n"
    "s_add_u32 s35, s35, 16\n"
    "s_mul_i32 s35, s35, 122880\n"
    "s_add_u32 m0, s32, 12288\n"
    "s_nop 0\n"
    "buffer_load_dwordx4 v0, s[4:7], s35 offen offset:0 lds\n"
    "buffer_load_dwordx4 v1, s[4:7], s35 offen offset:1024 lds\n"
    "buffer_load_dwordx4 v2, s[4:7], s35 offen offset:2048 lds\n"
    "s_waitcnt vmcnt(36)\n"
    "ds_read_b64_tr_b16 a[20:21], v3 offset:15360\n"
    "ds_read_b64_tr_b16 a[22:23], v3 offset:15488\n"
    "ds_read_b64_tr_b16 a[100:101], v3 offset:16384\n"
    "ds_read_b64_tr_b16 a[102:103], v3 offset:16512\n"
    "ds_read_b64_tr_b16 a[180:181], v3 offset:17408\n"
    "ds_read_b64_tr_b16 a[182:183], v3 offset:17536\n"
    "s_waitcnt lgkmcnt(0)\n"
    "s_add_u32 s35, s18, 2\n"
    "s_and_b32 s35, s35, 3\n"
    "s_add_u32 s35, s35, 16\n"
    "s_mul_i32 s35, s35, 122880\n"
    "s_add_u32 m0, s32, 15360\n"
    "s_nop 0\n"
    "buffer_load_dwordx4 v0, s[4:7], s35 offen offset:0 lds\n"
    "buffer_load_dwordx4 v1, s[4:7], s35 offen offset:1024 lds\n"
    "buffer_load_dwordx4 v2, s[4:7], s35 offen offset:2048 lds\n"
    "s_waitcnt vmcnt(36)\n"
    "ds_read_b64_tr_b16 a[24:25], v3 offset:18432\n"
    "ds_read_b64_tr_b16 a[26:27], v3 offset:18560\n"
    "ds_read_b64_tr_b16 a[104:105], v3 offset:19456\n"
    "ds_read_b64_tr_b16 a[106:107], v3 offset:19584\n"
    "ds_read_b64_tr_b16 a[184:185], v3 offset:20480\n"
    "ds_read_b64_tr_b16 a[186:187], v3 offset:20608\n"
    "s_waitcnt lgkmcnt(0)\n"
    "s_add_u32 s35, s18, 3\n"
    "s_and_b32 s35, s35, 3\n"
    "s_add_u32 s35, s35, 16\n"
    "s_mul_i32 s35, s35, 122880\n"
    "s_add_u32 m0, s32, 18432\n"
    "s_nop 0\n"
    "buffer_load_dwordx4 v0, s[4:7], s35 offen offset:0 lds\n"
    "buffer_load_dwordx4 v1, s[4:7], s35 offen offset:1024 lds\n"
    "buffer_load_dwordx4 v2, s[4:7], s35 offen offset:2048 lds\n"
    "s_waitcnt vmcnt(36)\n"
    "ds_read_b64_tr_b16 a[28:29], v3 offset:21504\n"
    "ds_read_b64_tr_b16 a[30:31], v3 offset:21632\n"
    "ds_read_b64_tr_b16 a[108:109], v3 offset:22528\n"
    "ds_read_b64_tr_b16 a[110:111], v3 offset:22656\n"
    "ds_read_b64_tr_b16 a[188:189], v3 offset:23552\n"
    "ds_read_b64_tr_b16 a[190:191], v3 offset:23680\n"
    "s_waitcnt lgkmcnt(0)\n"
    "s_waitcnt vmcnt(33)\n"
    "ds_read_b64_tr_b16 a[32:33], v3 offset:24576\n"
    "ds_read_b64_tr_b16 a[34:35], v3 offset:24704\n"
    "ds_read_b64_tr_b16 a[112:113], v3 offset:25600\n"
    "ds_read_b64_tr_b16 a[114:115], v3 offset:25728\n"
    "ds_read_b64_tr_b16 a[192:193], v3 offset:26624\n"
    "ds_read_b64_tr_b16 a[194:195], v3 offset:26752\n"
    "s_waitcnt lgkmcnt(0)\n"
    "s_waitcnt vmcnt(30)\n"
    "ds_read_b64_tr_b16 a[36:37], v3 offset:27648\n"
    "ds_read_b64_tr_b16 a[38:39], v3 offset:27776\n"
    "ds_read_b64_tr_b16 a[116:117], v3 offset:28672\n"
    "ds_read_b64_tr_b16 a[118:119], v3 offset:28800\n"
    "ds_read_b64_tr_b16 a[196:197], v3 offset:29696\n"
    "ds_read_b64_tr_b16 a[198:199], v3 offset:29824\n"
    "s_waitcnt lgkmcnt(0)\n"
    "s_waitcnt vmcnt(27)\n"
    "ds_read_b64_tr_b16 a[40:41], v3 offset:30720\n"
    "ds_read_b64_tr_b16 a[42:43], v3 offset:30848\n"
    "ds_read_b64_tr_b16 a[120:121], v3 offset:31744\n"
    "ds_read_b64_tr_b16 a[122:123], v3 offset:31872\n"
    "ds_read_b64_tr_b16 a[200:201], v3 offset:32768\n"
    "ds_read_b64_tr_b16 a[202:203], v3 offset:32896\n"
    "s_waitcnt lgkmcnt(0)\n"
    "s_waitcnt vmcnt(24)\n"
    "ds_read_b64_tr_b16 a[44:45], v3 offset:33792\n"
    "ds_read_b64_tr_b16 a[46:47], v3 offset:33920\n"
    "ds_read_b64_tr_b16 a[124:125], v3 offset:34816\n"
    "ds_read_b64_tr_b16 a[126:127], v3 offset:34944\n"
    "ds_read_b64_tr_b16 a[204:205], v3 offset:35840\n"
    "ds_read_b64_tr_b16 a[206:207], v3 offset:35968\n"
    "s_waitcnt lgkmcnt(0)\n"
    "s_waitcnt vmcnt(21)\n"
    "ds_read_b64_tr_b16 a[48:49], v3 offset:36864\n"
    "ds_read_b64_tr_b16 a[50:51], v3 offset:36992\n"
    "ds_read_b64_tr_b16 a[128:129], v3 offset:37888\n"
    "ds_read_b64_tr_b16 a[130:131], v3 offset:38016\n"
    "ds_read_b64_tr_b16 a[208:209], v3 offset:38912\n"
    "ds_read_b64_tr_b16 a[210:211], v3 offset:39040\n"
    "s_waitcnt lgkmcnt(0)\n"
    "s_waitcnt vmcnt(18)\n"
    "ds_read_b64_tr_b16 a[52:53], v3 offset:0\n"
    "ds_read_b64_tr_b16 a[54:55], v3 offset:128\n"
    "ds_read_b64_tr_b16 a[132:133], v3 offset:1024\n"
    "ds_read_b64_tr_b16 a[134:135], v3 offset:1152\n"
    "ds_read_b64_tr_b16 a[212:213], v3 offset:2048\n"
    "ds_read_b64_tr_b16 a[214:215], v3 offset:2176\n"
    "s_waitcnt lgkmcnt(0)\n"
    "s_waitcnt vmcnt(15)\n"
    "ds_read_b64_tr_b16 a[56:57], v3 offset:3072\n"
    "ds_read_b64_tr_b16 a[58:59], v3 offset:3200\n"
    "ds_read_b64_tr_b16 a[136:137], v3 offset:4096\n"
    "ds_read_b64_tr_b16 a[138:139], v3 offset:4224\n"
    "ds_read_b64_tr_b16 a[216:217], v3 offset:5120\n"
    "ds_read_b64_tr_b16 a[218:219], v3 offset:5248\n"
    "s_waitcnt lgkmcnt(0)\n"
    "s_waitcnt vmcnt(12)\n"
    "ds_read_b64_tr_b16 a[60:61], v3 offset:6144\n"
    "ds_read_b64_tr_b16 a[62:63], v3 offset:6272\n"
    "ds_read_b64_tr_b16 a[140:141], v3 offset:7168\n"
    "ds_read_b64_tr_b16 a[142:143], v3 offset:7296\n"
    "ds_read_b64_tr_b16 a[220:221], v3 offset:8192\n"
    "ds_read_b64_tr_b16 a[222:223], v3 offset:8320\n"
    "s_waitcnt lgkmcnt(0)\n"
    "s_waitcnt vmcnt(9)\n"
    "ds_read_b64_tr_b16 a[64:65], v3 offset:9216\n"
    "ds_read_b64_tr_b16 a[66:67], v3 offset:9344\n"
    "ds_read_b64_tr_b16 a[144:145], v3 offset:10240\n"
    "ds_read_b64_tr_b16 a[146:147], v3 offset:10368\n"
    "ds_read_b64_tr_b16 a[224:225], v3 offset:11264\n"
    "ds_read_b64_tr_b16 a[226:227], v3 offset:11392\n"
    "s_waitcnt lgkmcnt(0)\n"
    "s_waitcnt vmcnt(6)\n"
    "ds_read_b64_tr_b16 a[68:69], v3 offset:12288\n"
    "ds_read_b64_tr_b16 a[70:71], v3 offset:12416\n"
    "ds_read_b64_tr_b16 a[148:149], v3 offset:13312\n"
    "ds_read_b64_tr_b16 a[150:151], v3 offset:13440\n"
    "ds_read_b64_tr_b16 a[228:229], v3 offset:14336\n"
    "ds_read_b64_tr_b16 a[230:231], v3 offset:14464\n"
    "s_waitcnt lgkmcnt(0)\n"
    "s_waitcnt vmcnt(3)\n"
    "ds_read_b64_tr_b16 a[72:73], v3 offset:15360\n"
    "ds_read_b64_tr_b16 a[74:75], v3 offset:15488\n"
    "ds_read_b64_tr_b16 a[152:153], v3 offset:16384\n"
    "ds_read_b64_tr_b16 a[154:155], v3 offset:16512\n"
    "ds_read_b64_tr_b16 a[232:233], v3 offset:17408\n"
    "ds_read_b64_tr_b16 a[234:235], v3 offset:17536\n"
    "s_waitcnt lgkmcnt(0)\n"
    "s_waitcnt vmcnt(0)\n"
    "ds_read_b64_tr_b16 a[76:77], v3 offset:18432\n"
    "ds_read_b64_tr_b16 a[78:79], v3 offset:18560\n"
    "ds_read_b64_tr_b16 a[156:157], v3 offset:19456\n"
    "ds_read_b64_tr_b16 a[158:159], v3 offset:19584\n"
    "ds_read_b64_tr_b16 a[236:237], v3 offset:20480\n"
    "ds_read_b64_tr_b16 a[238:239], v3 offset:20608\n"
    "s_waitcnt lgkmcnt(0)\n"
    "s_waitcnt lgkmcnt(0)\n"
    "s_barrier\n"
    "v_mov_b32 v32, 0x3f803f80\n"
    "v_mov_b32 v36, 0\n"
    "v_mov_b32 v33, 0x3f803f80\n"
    "v_mov_b32 v37, 0\n"
    "v_mov_b32 v34, 0x3f803f80\n"
    "v_mov_b32 v38, 0\n"
    "v_mov_b32 v35, 0x3f803f80\n"
    "v_mov_b32 v39, 0\n"
    "s_nop 0\n"
    "v_mfma_f32_16x16x32_bf16 v[96:99], a[0:3], v[32:35], 0\n"
    "v_mfma_f32_16x16x32_bf16 v[100:103], a[80:83], v[32:35], 0\n"
    "v_mfma_f32_16x16x32_bf16 v[104:107], a[160:163], v[32:35], 0\n"
    "v_mfma_f32_16x16x32_bf16 v[96:99], a[4:7], v[32:35], v[96:99]\n"
    "v_mfma_f32_16x16x32_bf16 v[100:103], a[84:87], v[32:35], v[100:103]\n"
    "v_mfma_f32_16x16x32_bf16 v[104:107], a[164:167], v[32:35], v[104:107]\n"
    "v_mfma_f32_16x16x32_bf16 v[96:99], a[8:11], v[32:35], v[96:99]\n"
    "v_mfma_f32_16x16x32_bf16 v[100:103], a[88:91], v[32:35], v[100:103]\n"
    "v_mfma_f32_16x16x32_bf16 v[104:107], a[168:171], v[32:35], v[104:107]\n"
    "v_mfma_f32_16x16x32_bf16 v[96:99], a[12:15], v[32:35], v[96:99]\n"
    "v_mfma_f32_16x16x32_bf16 v[100:103], a[92:95], v[32:35], v[100:103]\n"
    "v_mfma_f32_16x16x32_bf16 v[104:107], a[172:175], v[32:35], v[104:107]\n"
    "v_mfma_f32_16x16x32_bf16 v[96:99], a[16:19], v[32:35], v[96:99]\n"
    "v_mfma_f32_16x16x32_bf16 v[100:103], a[96:99], v[32:35], v[100:103]\n"
    "v_mfma_f32_16x16x32_bf16 v[104:107], a[176:179], v[32:35], v[104:107]\n"
    "v_mfma_f32_16x16x32_bf16 v[96:99], a[20:23], v[32:35], v[96:99]\n"
    "v_mfma_f32_16x16x32_bf16 v[100:103], a[100:103], v[32:35], v[100:103]\n"
    "v_mfma_f32_16x16x32_bf16 v[104:107], a[180:183], v[32:35], v[104:107]\n"
    "v_mfma_f32_16x16x32_bf16 v[96:99], a[24:27], v[32:35], v[96:99]\n"
    "v_mfma_f32_16x16x32_bf16 v[100:103], a[104:107], v[32:35], v[100:103]\n"
    "v_mfma_f32_16x16x32_bf16 v[104:107], a[184:187], v[32:35], v[104:107]\n"
    "v_mfma_f32_16x16x32_bf16 v[96:99], a[28:31], v[32:35], v[96:99]\n"
    "v_mfma_f32_16x16x32_bf16 v[100:103], a[108:111], v[32:35], v[100:103]\n"
    "v_mfma_f32_16x16x32_bf16 v[104:107], a[188:191], v[32:35], v[104:107]\n"
    "v_mfma_f32_16x16x32_bf16 v[96:99], a[32:35], v[32:35], v[96:99]\n"
    "v_mfma_f32_16x16x32_bf16 v[100:103], a[112:115], v[32:35], v[100:103]\n"
    "v_mfma_f32_16x16x32_bf16 v[104:107], a[192:195], v[32:35], v[104:107]\n"
    "v_mfma_f32_16x16x32_bf16 v[96:99], a[36:39], v[32:35], v[96:99]\n"
    "v_mfma_f32_16x16x32_bf16 v[100:103], a[116:119], v[32:35], v[100:103]\n"
    "v_mfma_f32_16x16x32_bf16 v[104:107], a[196:199], v[32:35], v[104:107]\n"
    "v_mfma_f32_16x16x32_bf16 v[96:99], a[40:43], v[32:35], v[96:99]\n"
    "v_mfma_f32_16x16x32_bf16 v[100:103], a[120:123], v[32:35], v[100:103]\n"
    "v_mfma_f32_16x16x32_bf16 v[104:107], a[200:203], v[32:35], v[104:107]\n"
    "v_mfma_f32_16x16x32_bf16 v[96:99], a[44:47], v[32:35], v[96:99]\n"
    "v_mfma_f32_16x16x32_bf16 v[100:103], a[124:127], v[32:35], v[100:103]\n"
    "v_mfma_f32_16x16x32_bf16 v[104:107], a[204:207], v[32:35], v[104:107]\n"
    "v_mfma_f32_16x16x32_bf16 v[96:99], a[48:51], v[32:35], v[96:99]\n"
    "v_mfma_f32_16x16x32_bf16 v[100:103], a[128:131], v[32:35], v[100:103]\n"
    "v_mfma_f32_16x16x32_bf16 v[104:107], a[208:211], v[32:35], v[104:107]\n"
    "v_mfma_f32_16x16x32_bf16 v[96:99], a[52:55], v[32:35], v[96:99]\n"
    "v_mfma_f32_16x16x32_bf16 v[100:103], a[132:135], v[32:35], v[100:103]\n"
    "v_mfma_f32_16x16x32_bf16 v[104:107], a[212:215], v[32:35], v[104:107]\n"
    "v_mfma_f32_16x16x32_bf16 v[96:99], a[56:59], v[32:35], v[96:99]\n"
    "v_mfma_f32_16x16x32_bf16 v[100:103], a[136:139], v[32:35], v[100:103]\n"
    "v_mfma_f32_16x16x32_bf16 v[104:107], a[216:219], v[32:35], v[104:107]\n"
    "v_mfma_f32_16x16x32_bf16 v[96:99], a[60:63], v[32:35], v[96:99]\n"
    "v_mfma_f32_16x16x32_bf16 v[100:103], a[140:143], v[32:35], v[100:103]\n"
    "v_mfma_f32_16x16x32_bf16 v[104:107], a[220:223], v[32:35], v[104:107]\n"
    "v_mfma_f32_16x16x32_bf16 v[96:99], a[64:67], v[32:35], v[96:99]\n"
    "v_mfma_f32_16x16x32_bf16 v[100:103], a[144:147], v[32:35], v[100:103]\n"
    "v_mfma_f32_16x16x32_bf16 v[104:107], a[224:227], v[32:35], v[104:107]\n"
    "v_mfma_f32_16x16x32_bf16 v[96:99], a[68:71], v[32:35], v[96:99]\n"
    "v_mfma_f32_16x16x32_bf16 v[100:103], a[148:151], v[32:35], v[100:103]\n"
    "v_mfma_f32_16x16x32_bf16 v[104:107], a[228:231], v[32:35], v[104:107]\n"
    "v_mfma_f32_16x16x32_bf16 v[96:99], a[72:75], v[32:35], v[96:99]\n"
    "v_mfma_f32_16x16x32_bf16 v[100:103], a[152:155], v[32:35], v[100:103]\n"
    "v_mfma_f32_16x16x32_bf16 v[104:107], a[232:235], v[32:35], v[104:107]\n"
    "v_mfma_f32_16x16x32_bf16 v[96:99], a[76:79], v[32:35], v[96:99]\n"
    "v_mfma_f32_16x16x32_bf16 v[100:103], a[156:159], v[32:35], v[100:103]\n"
    "v_mfma_f32_16x16x32_bf16 v[104:107], a[236:239], v[32:35], v[104:107]\n"
    "s_lshl_b32 s35, s18, 13\n"
    "v_add_u32 v8, s35, v228\n"
    "ds_write_b128 v8, v[36:39] offset:40960\n"
    "ds_write_b128 v8, v[36:39] offset:41984\n"
    "ds_write_b128 v8, v[36:39] offset:43008\n"
    "ds_write_b128 v8, v[36:39] offset:44032\n"
    "ds_write_b128 v8, v[36:39] offset:45056\n"
    "ds_write_b128 v8, v[36:39] offset:46080\n"
    "ds_write_b128 v8, v[36:39] offset:47104\n"
    "ds_write_b128 v8, v[36:39] offset:48128\n"
    "s_nop 7\n"
    "s_nop 7\n"
    "s_mov_b64 exec, s[16:17]\n"
    "v_accvgpr_write_b32 a0, v32\n"
    "v_accvgpr_write_b32 a1, v32\n"
    "v_accvgpr_write_b32 a2, v32\n"
    "v_accvgpr_write_b32 a3, v32\n"
    "v_accvgpr_write_b32 a4, v32\n"
    "v_accvgpr_write_b32 a5, v32\n"
    "v_accvgpr_write_b32 a6, v32\n"
    "v_accvgpr_write_b32 a7, v32\n"
    "v_accvgpr_write_b32 a8, v32\n"
    "v_accvgpr_write_b32 a9, v32\n"
    "v_accvgpr_write_b32 a10, v32\n"
    "v_accvgpr_write_b32 a11, v32\n"
    "v_accvgpr_write_b32 a12, v32\n"
    "v_accvgpr_write_b32 a13, v32\n"
    "v_accvgpr_write_b32 a14, v32\n"
    "v_accvgpr_write_b32 a15, v32\n"
    "v_accvgpr_write_b32 a16, v32\n"
    "v_accvgpr_write_b32 a17, v32\n"
    "v_accvgpr_write_b32 a18, v32\n"
    "v_accvgpr_write_b32 a19, v32\n"
    "v_accvgpr_write_b32 a20, v32\n"
    "v_accvgpr_write_b32 a21, v32\n"
    "v_accvgpr_write_b32 a22, v32\n"
    "v_accvgpr_write_b32 a23, v32\n"
    "v_accvgpr_write_b32 a24, v32\n"
    "v_accvgpr_write_b32 a25, v32\n"
    "v_accvgpr_write_b32 a26, v32\n"
    "v_accvgpr_write_b32 a27, v32\n"
    "v_accvgpr_write_b32 a28, v32\n"
    "v_accvgpr_write_b32 a29, v32\n"
    "v_accvgpr_write_b32 a30, v32\n"
    "v_accvgpr_write_b32 a31, v32\n"
    "v_accvgpr_write_b32 a32, v32\n"
    "v_accvgpr_write_b32 a33, v32\n"
    "v_accvgpr_write_b32 a34, v32\n"
    "v_accvgpr_write_b32 a35, v32\n"
    "v_accvgpr_write_b32 a36, v32\n"
    "v_accvgpr_write_b32 a37, v32\n"
    "v_accvgpr_write_b32 a38, v32\n"
    "v_accvgpr_write_b32 a39, v32\n"
    "v_accvgpr_write_b32 a40, v32\n"
    "v_accvgpr_write_b32 a41, v32\n"
    "v_accvgpr_write_b32 a42, v32\n"
    "v_accvgpr_write_b32 a43, v32\n"
    "v_accvgpr_write_b32 a44, v32\n"
    "v_accvgpr_write_b32 a45, v32\n"
    "v_accvgpr_write_b32 a46, v32\n"
    "v_accvgpr_write_b32 a47, v32\n"
    "v_accvgpr_write_b32 a48, v32\n"
    "v_accvgpr_write_b32 a49, v32\n"
    "v_accvgpr_write_b32 a50, v32\n"
    "v_accvgpr_write_b32 a51, v32\n"
    "v_accvgpr_write_b32 a52, v32\n"
    "v_accvgpr_write_b32 a53, v32\n"
    "v_accvgpr_write_b32 a54, v32\n"
    "v_accvgpr_write_b32 a55, v32\n"
    "v_accvgpr_write_b32 a56, v32\n"
    "v_accvgpr_write_b32 a57, v32\n"
    "v_accvgpr_write_b32 a58, v32\n"
    "v_accvgpr_write_b32 a59, v32\n"
    "v_accvgpr_write_b32 a60, v32\n"
    "v_accvgpr_write_b32 a61, v32\n"
    "v_accvgpr_write_b32 a62, v32\n"
    "v_accvgpr_write_b32 a63, v32\n"
    "v_accvgpr_write_b32 a64, v32\n"
    "v_accvgpr_write_b32 a65, v32\n"
    "v_accvgpr_write_b32 a66, v32\n"
    "v_accvgpr_write_b32 a67, v32\n"
    "v_accvgpr_write_b32 a68, v32\n"
    "v_accvgpr_write_b32 a69, v32\n"
    "v_accvgpr_write_b32 a70, v32\n"
    "v_accvgpr_write_b32 a71, v32\n"
    "v_accvgpr_write_b32 a72, v32\n"
    "v_accvgpr_write_b32 a73, v32\n"
    "v_accvgpr_write_b32 a74, v32\n"
    "v_accvgpr_write_b32 a75, v32\n"
    "v_accvgpr_write_b32 a76, v32\n"
    "v_accvgpr_write_b32 a77, v32\n"
    "v_accvgpr_write_b32 a78, v32\n"
    "v_accvgpr_write_b32 a79, v32\n"
    "s_mov_b64 exec, -1\n"
    "s_mov_b32 s30, -2\n"
    "s_mov_b32 s30, 0\n"
    "s_mov_b32 s31, 1\n"
    "s_lshl_b32 s34, s18, 11\n"
    "s_add_u32 s32, s20, s19\n"
    "s_mov_b32 s37, 0x3ea52b5c\n"
    "s_mov_b64 s[38:39], 0\n"
    "s_mov_b64 s[40:41], 0\n"
    "s_mov_b32 s36, 0\n"
    "v_mov_b32 v108, 0\n"
    "s_mov_b32 s42, 0xaaaaaaaa\n"
    "s_mov_b32 s43, 0xaaaaaaaa\n"
    "s_waitcnt lgkmcnt(0)\n"
    ".Lb1_top_%=:\n"
    "s_cmp_lg_u32 s18, 0\n"
    "s_cbranch_scc1 .Lb1_pd7_%=\n"
    "s_add_u32 s29, s26, 248\n"
    "v_min3_i32 v108, v108, v109, v110\n"
    "v_min_i32 v108, v108, v111\n"
    "v_cmp_gt_i32 vcc, s31, v108\n"
    "s_nop 0\n"
    "s_cbranch_vccz .Lb1_pd7_%=\n"
    ".Lb1_rt8_%=:\n"
    "s_memrealtime s[26:27]\n"
    "s_waitcnt lgkmcnt(0)\n"
    "s_sub_u32 s28, s36, s26\n"
    "s_cmp_lt_u32 s28, 600\n"
    "s_cbranch_scc1 .Lb1_rt8_%=\n"
    ".Lb1_po6_%=:\n"
    "buffer_load_dwordx4 v[108:111], v229, s[0:3], s21 offen sc1\n"
    "s_memrealtime s[26:27]\n"
    "s_waitcnt vmcnt(0) lgkmcnt(0)\n"
    "s_add_u32 s29, s26, 248\n"
    "v_min3_i32 v108, v108, v109, v110\n"
    "v_min_i32 v108, v108, v111\n"
    "v_cmp_gt_i32 vcc, s31, v108\n"
    "s_nop 0\n"
    "s_cbranch_vccz .Lb1_pd7_%=\n"
    "s_add_u32 s24, s24, 1\n"
    "s_cmp_lt_u32 s24, 0x40000\n"
    "s_cbranch_scc1 .Lb1_sp9_%=\n"
    "v_mov_b32 v108, 60\n"
    "s_mov_b32 s28, 33728\n"
    "buffer_store_dword v108, off, s[0:3], s28 sc1\n"
    "s_waitcnt vmcnt(0)\n"
    "s_branch .Lb1_pd7_%=\n"
    ".Lb1_sp9_%=:\n"
    "s_sleep 6\n"
    "s_branch .Lb1_po6_%=\n"
    ".Lb1_pd7_%=:\n"
    "s_barrier\n"
    "s_cmp_eq_u32 s31, 63\n"
    "s_cselect_b32 s36, s26, s29\n"
    ".Lb1_mk0x9_%=:\n"
    "s_mov_b32 s33, s32\n"
    "s_add_u32 m0, s34, 0\n"
    "s_nop 0\n"
    "buffer_load_dwordx4 v228, s[0:3], s32 offen sc1 lds\n"
    "buffer_load_dwordx4 v228, s[0:3], s32 offen offset:1024 sc1 lds\n"
    "s_add_u32 m0, s34, 8192\n"
    "s_add_u32 s32, s32, 0x4000\n"
    "buffer_load_dwordx4 v228, s[0:3], s32 offen sc1 lds\n"
    "buffer_load_dwordx4 v228, s[0:3], s32 offen offset:1024 sc1 lds\n"
    "s_add_u32 m0, s34, 16384\n"
    "s_add_u32 s32, s32, 0x4000\n"
    "buffer_load_dwordx4 v228, s[0:3], s32 offen sc1 lds\n"
    "buffer_load_dwordx4 v228, s[0:3], s32 offen offset:1024 sc1 lds\n"
    "s_add_u32 m0, s34, 24576\n"
    "s_add_u32 s32, s32, 0x4000\n"
    "buffer_load_dwordx4 v228, s[0:3], s32 offen sc1 lds\n"
    "buffer_load_dwordx4 v228, s[0:3], s32 offen offset:1024 sc1 lds\n"
    "s_add_u32 m0, s34, 32768\n"
    "s_add_u32 s32, s32, 0x4000\n"
    "buffer_load_dwordx4 v228, s[0:3], s32 offen sc1 lds\n"
    "buffer_load_dwordx4 v228, s[0:3], s32 offen offset:1024 sc1 lds\n"
    "s_waitcnt vmcnt(8)\n"
    "s_barrier\n"
    ".Lb1_mk1x10_%=:\n"
    "ds_read_b128 v[32:35], v224 offset:0\n"
    "ds_read_b128 v[36:39], v224 offset:1024\n"
    "ds_read_b128 v[40:43], v225 offset:0\n"
    "ds_read_b128 v[44:47], v225 offset:1024\n"
    "s_waitcnt lgkmcnt(2)\n"
    "v_mfma_f32_16x16x32_bf16 v[24:27], v[32:35], v[32:35], 0\n"
    "ds_read_b128 v[48:51], v226 offset:0\n"
    "ds_read_b128 v[52:55], v226 offset:1024\n"
    "v_mfma_f32_16x16x32_bf16 v[28:31], v[36:39], v[36:39], 0\n"
    "v_mfma_f32_16x16x32_bf16 v[0:3], a[0:3], v[32:35], 0\n"
    "v_mfma_f32_16x16x32_bf16 v[4:7], a[0:3], v[36:39], 0\n"
    "v_mfma_f32_16x16x32_bf16 v[8:11], a[80:83], v[32:35], 0\n"
    "s_add_u32 s35, s30, 1\n"
    "s_lshl_b32 s32, s35, 19\n"
    "s_lshl_b32 s29, s35, 11\n"
    "v_mfma_f32_16x16x32_bf16 v[12:15], a[80:83], v[36:39], 0\n"
    "s_xor_b32 s29, s29, s19\n"
    "s_and_b32 s29, s29, 0x2000\n"
    "s_add_u32 s32, s32, s29\n"
    "v_mfma_f32_16x16x32_bf16 v[16:19], a[160:163], v[32:35], 0\n"
    "s_add_u32 s32, s32, s20\n"
    "v_xor_b32 v232, 0x800, v232\n"
    "v_xor_b32 v233, 0x800, v233\n"
    "v_mfma_f32_16x16x32_bf16 v[20:23], a[160:163], v[36:39], 0\n"
    "s_waitcnt lgkmcnt(2)\n"
    "v_xor_b32 v235, 0x800, v235\n"
    "v_xor_b32 v236, 0x800, v236\n"
    "v_mfma_f32_16x16x32_bf16 v[0:3], a[4:7], v[40:43], v[0:3]\n"
    "ds_read_b128 v[56:59], v227 offset:0\n"
    "ds_read_b128 v[60:63], v227 offset:1024\n"
    "v_mfma_f32_16x16x32_bf16 v[4:7], a[4:7], v[44:47], v[4:7]\n"
    "v_xor_b32 v237, 0x800, v237\n"
    "v_xor_b32 v238, 0x800, v238\n"
    "v_mfma_f32_16x16x32_bf16 v[8:11], a[84:87], v[40:43], v[8:11]\n"
    "v_xor_b32 v239, 0x800, v239\n"
    "v_xor_b32 v240, 0x800, v240\n"
    "v_mfma_f32_16x16x32_bf16 v[12:15], a[84:87], v[44:47], v[12:15]\n"
    "v_mov_b32 v116, s31\n"
    "v_mov_b32 v117, s31\n"
    "v_mfma_f32_16x16x32_bf16 v[16:19], a[164:167], v[40:43], v[16:19]\n"
    "v_mov_b32 v118, s31\n"
    "v_mov_b32 v119, s31\n"
    "v_mfma_f32_16x16x32_bf16 v[20:23], a[164:167], v[44:47], v[20:23]\n"
    "s_waitcnt lgkmcnt(2)\n"
    "v_mfma_f32_16x16x32_bf16 v[0:3], a[8:11], v[48:51], v[0:3]\n"
    "s_waitcnt vmcnt(6)\n"
    "s_barrier\n"
    "ds_read_b128 v[64:67], v224 offset:8192\n"
    "v_mfma_f32_16x16x32_bf16 v[4:7], a[8:11], v[52:55], v[4:7]\n"
    "ds_read_b128 v[68:71], v224 offset:9216\n"
    "v_mfma_f32_16x16x32_bf16 v[8:11], a[88:91], v[48:51], v[8:11]\n"
    "v_mfma_f32_16x16x32_bf16 v[12:15], a[88:91], v[52:55], v[12:15]\n"
    "v_mfma_f32_16x16x32_bf16 v[16:19], a[168:171], v[48:51], v[16:19]\n"
    "v_mfma_f32_16x16x32_bf16 v[20:23], a[168:171], v[52:55], v[20:23]\n"
    "s_waitcnt lgkmcnt(2)\n"
    "v_mfma_f32_16x16x32_bf16 v[0:3], a[12:15], v[56:59], v[0:3]\n"
    "ds_read_b128 v[72:75], v225 offset:8192\n"
    "ds_read_b128 v[76:79], v225 offset:9216\n"
    "v_mfma_f32_16x16x32_bf16 v[4:7], a[12:15], v[60:63], v[4:7]\n"
    "v_mfma_f32_16x16x32_bf16 v[8:11], a[92:95], v[56:59], v[8:11]\n"
    "v_mfma_f32_16x16x32_bf16 v[12:15], a[92:95], v[60:63], v[12:15]\n"
    "v_mfma_f32_16x16x32_bf16 v[16:19], a[172:175], v[56:59], v[16:19]\n"
    "v_mfma_f32_16x16x32_bf16 v[20:23], a[172:175], v[60:63], v[20:23]\n"
    "s_waitcnt lgkmcnt(2)\n"
    "v_mfma_f32_16x16x32_bf16 v[24:27], v[64:67], v[64:67], v[24:27]\n"
    "ds_read_b128 v[80:83], v226 offset:8192\n"
    "ds_read_b128 v[84:87], v226 offset:9216\n"
    "v_mfma_f32_16x16x32_bf16 v[28:31], v[68:71], v[68:71], v[28:31]\n"
    "v_mfma_f32_16x16x32_bf16 v[0:3], a[16:19], v[64:67], v[0:3]\n"
    "v_mfma_f32_16x16x32_bf16 v[4:7], a[16:19], v[68:71], v[4:7]\n"
    "v_mfma_f32_16x16x32_bf16 v[8:11], a[96:99], v[64:67], v[8:11]\n"
    "v_mfma_f32_16x16x32_bf16 v[12:15], a[96:99], v[68:71], v[12:15]\n"
    "v_mfma_f32_16x16x32_bf16 v[16:19], a[176:179], v[64:67], v[16:19]\n"
    "v_mfma_f32_16x16x32_bf16 v[20:23], a[176:179], v[68:71], v[20:23]\n"
    "s_waitcnt lgkmcnt(2)\n"
    "v_mfma_f32_16x16x32_bf16 v[0:3], a[20:23], v[72:75], v[0:3]\n"
    "ds_read_b128 v[88:91], v227 offset:8192\n"
    "ds_read_b128 v[92:95], v227 offset:9216\n"
    "v_mfma_f32_16x16x32_bf16 v[4:7], a[20:23], v[76:79], v[4:7]\n"
    "v_mfma_f32_16x16x32_bf16 v[8:11], a[100:103], v[72:75], v[8:11]\n"
    "v_mfma_f32_16x16x32_bf16 v[12:15], a[100:103], v[76:79], v[12:15]\n"
    "v_mfma_f32_16x16x32_bf16 v[16:19], a[180:183], v[72:75], v[16:19]\n"
    "v_mfma_f32_16x16x32_bf16 v[20:23], a[180:183], v[76:79], v[20:23]\n"
    "s_waitcnt lgkmcnt(2)\n"
    "v_mfma_f32_16x16x32_bf16 v[0:3], a[24:27], v[80:83], v[0:3]\n"
    "s_waitcnt vmcnt(4)\n"
    "s_barrier\n"
    "ds_read_b128 v[32:35], v224 offset:16384\n"
    "v_mfma_f32_16x16x32_bf16 v[4:7], a[24:27], v[84:87], v[4:7]\n"
    "ds_read_b128 v[36:39], v224 offset:17408\n"
    "v_mfma_f32_16x16x32_bf16 v[8:11], a[104:107], v[80:83], v[8:11]\n"
    "v_mfma_f32_16x16x32_bf16 v[12:15], a[104:107], v[84:87], v[12:15]\n"
    "v_mfma_f32_16x16x32_bf16 v[16:19], a[184:187], v[80:83], v[16:19]\n"
    "v_mfma_f32_16x16x32_bf16 v[20:23], a[184:187], v[84:87], v[20:23]\n"
    "s_waitcnt lgkmcnt(2)\n"
    "v_mfma_f32_16x16x32_bf16 v[0:3], a[28:31], v[88:91], v[0:3]\n"
    "ds_read_b128 v[40:43], v225 offset:16384\n"
    "ds_read_b128 v[44:47], v225 offset:17408\n"
    "v_mfma_f32_16x16x32_bf16 v[4:7], a[28:31], v[92:95], v[4:7]\n"
    "v_mfma_f32_16x16x32_bf16 v[8:11], a[108:111], v[88:91], v[8:11]\n"
    "v_mfma_f32_16x16x32_bf16 v[12:15], a[108:111], v[92:95], v[12:15]\n"
    "v_mfma_f32_16x16x32_bf16 v[16:19], a[188:191], v[88:91], v[16:19]\n"
    "v_mfma_f32_16x16x32_bf16 v[20:23], a[188:191], v[92:95], v[20:23]\n"
    "s_waitcnt lgkmcnt(2)\n"
    "v_mfma_f32_16x16x32_bf16 v[24:27], v[32:35], v[32:35], v[24:27]\n"
    "ds_read_b128 v[48:51], v226 offset:16384\n"
    "ds_read_b128 v[52:55], v226 offset:17408\n"
    "v_mfma_f32_16x16x32_bf16 v[28:31], v[36:39], v[36:39], v[28:31]\n"
    "v_mfma_f32_16x16x32_bf16 v[0:3], a[32:35], v[32:35], v[0:3]\n"
    "v_mfma_f32_16x16x32_bf16 v[4:7], a[32:35], v[36:39], v[4:7]\n"
    "v_mfma_f32_16x16x32_bf16 v[8:11], a[112:115], v[32:35], v[8:11]\n"
    "v_mfma_f32_16x16x32_bf16 v[12:15], a[112:115], v[36:39], v[12:15]\n"
    "v_mfma_f32_16x16x32_bf16 v[16:19], a[192:195], v[32:35], v[16:19]\n"
    "v_mfma_f32_16x16x32_bf16 v[20:23], a[192:195], v[36:39], v[20:23]\n"
    "s_waitcnt lgkmcnt(2)\n"
    "v_mfma_f32_16x16x32_bf16 v[0:3], a[36:39], v[40:43], v[0:3]\n"
    "ds_read_b128 v[56:59], v227 offset:16384\n"
    "ds_read_b128 v[60:63], v227 offset:17408\n"
    "v_mfma_f32_16x16x32_bf16 v[4:7], a[36:39], v[44:47], v[4:7]\n"
    "v_mfma_f32_16x16x32_bf16 v[8:11], a[116:119], v[40:43], v[8:11]\n"
    "v_mfma_f32_16x16x32_bf16 v[12:15], a[116:119], v[44:47], v[12:15]\n"
    "v_mfma_f32_16x16x32_bf16 v[16:19], a[196:199], v[40:43], v[16:19]\n"
    "v_mfma_f32_16x16x32_bf16 v[20:23], a[196:199], v[44:47], v[20:23]\n"
    "s_waitcnt lgkmcnt(2)\n"
    "v_mfma_f32_16x16x32_bf16 v[0:3], a[40:43], v[48:51], v[0:3]\n"
    "s_waitcnt vmcnt(2)\n"
    "s_barrier\n"
    "ds_read_b128 v[64:67], v224 offset:24576\n"
    "v_mfma_f32_16x16x32_bf16 v[4:7], a[40:43], v[52:55], v[4:7]\n"
    "ds_read_b128 v[68:71], v224 offset:25600\n"
    "ds_read_b128 v[72:75], v225 offset:24576\n"
    "v_mfma_f32_16x16x32_bf16 v[8:11], a[120:123], v[48:51], v[8:11]\n"
    "v_mfma_f32_16x16x32_bf16 v[12:15], a[120:123], v[52:55], v[12:15]\n"
    "ds_read_b128 v[76:79], v225 offset:25600\n"
    "v_mfma_f32_16x16x32_bf16 v[16:19], a[200:203], v[48:51], v[16:19]\n"
    "ds_read_b128 v[80:83], v226 offset:24576\n"
    "ds_read_b128 v[84:87], v226 offset:25600\n"
    "v_mfma_f32_16x16x32_bf16 v[20:23], a[200:203], v[52:55], v[20:23]\n"
    "s_waitcnt lgkmcnt(6)\n"
    "v_mfma_f32_16x16x32_bf16 v[0:3], a[44:47], v[56:59], v[0:3]\n"
    "ds_read_b128 v[88:91], v227 offset:24576\n"
    "v_mfma_f32_16x16x32_bf16 v[4:7], a[44:47], v[60:63], v[4:7]\n"
    "ds_read_b128 v[92:95], v227 offset:25600\n"
    "s_waitcnt vmcnt(0)\n"
    "v_mfma_f32_16x16x32_bf16 v[8:11], a[124:127], v[56:59], v[8:11]\n"
    "s_barrier\n"
    "ds_read_b128 v[32:35], v224 offset:32768\n"
    "v_mfma_f32_16x16x32_bf16 v[12:15], a[124:127], v[60:63], v[12:15]\n"
    "ds_read_b128 v[36:39], v224 offset:33792\n"
    "v_mfma_f32_16x16x32_bf16 v[16:19], a[204:207], v[56:59], v[16:19]\n"
    "ds_read_b128 v[40:43], v225 offset:32768\n"
    "v_mfma_f32_16x16x32_bf16 v[20:23], a[204:207], v[60:63], v[20:23]\n"
    "s_waitcnt lgkmcnt(9)\n"
    "ds_read_b128 v[44:47], v225 offset:33792\n"
    "v_mfma_f32_16x16x32_bf16 v[24:27], v[64:67], v[64:67], v[24:27]\n"
    "ds_read_b128 v[48:51], v226 offset:32768\n"
    "v_mfma_f32_16x16x32_bf16 v[28:31], v[68:71], v[68:71], v[28:31]\n"
    "ds_read_b128 v[52:55], v226 offset:33792\n"
    "v_mfma_f32_16x16x32_bf16 v[0:3], a[48:51], v[64:67], v[0:3]\n"
    "ds_read_b128 v[56:59], v227 offset:32768\n"
    "v_mfma_f32_16x16x32_bf16 v[4:7], a[48:51], v[68:71], v[4:7]\n"
    "s_waitcnt lgkmcnt(11)\n"
    "ds_read_b128 v[60:63], v227 offset:33792\n"
    "v_mfma_f32_16x16x32_bf16 v[0:3], a[52:55], v[72:75], v[0:3]\n"
    "v_mfma_f32_16x16x32_bf16 v[4:7], a[52:55], v[76:79], v[4:7]\n"
    "s_waitcnt lgkmcnt(10)\n"
    "v_mfma_f32_16x16x32_bf16 v[0:3], a[56:59], v[80:83], v[0:3]\n"
    "v_mfma_f32_16x16x32_bf16 v[4:7], a[56:59], v[84:87], v[4:7]\n"
    "s_waitcnt lgkmcnt(8)\n"
    "v_mfma_f32_16x16x32_bf16 v[0:3], a[60:63], v[88:91], v[0:3]\n"
    "v_mfma_f32_16x16x32_bf16 v[4:7], a[60:63], v[92:95], v[4:7]\n"
    "s_waitcnt lgkmcnt(6)\n"
    "v_mfma_f32_16x16x32_bf16 v[24:27], v[32:35], v[32:35], v[24:27]\n"
    "v_mfma_f32_16x16x32_bf16 v[28:31], v[36:39], v[36:39], v[28:31]\n"
    "v_mfma_f32_16x16x32_bf16 v[0:3], a[64:67], v[32:35], v[0:3]\n"
    "v_mfma_f32_16x16x32_bf16 v[4:7], a[64:67], v[36:39], v[4:7]\n"
    "s_waitcnt lgkmcnt(4)\n"
    "v_mfma_f32_16x16x32_bf16 v[0:3], a[68:71], v[40:43], v[0:3]\n"
    "v_mfma_f32_16x16x32_bf16 v[4:7], a[68:71], v[44:47], v[4:7]\n"
    "s_waitcnt lgkmcnt(2)\n"
    "v_mfma_f32_16x16x32_bf16 v[0:3], a[72:75], v[48:51], v[0:3]\n"
    "v_mfma_f32_16x16x32_bf16 v[4:7], a[72:75], v[52:55], v[4:7]\n"
    "s_waitcnt lgkmcnt(0)\n"
    "v_mfma_f32_16x16x32_bf16 v[0:3], a[76:79], v[56:59], v[0:3]\n"
    "v_mfma_f32_16x16x32_bf16 v[4:7], a[76:79], v[60:63], v[4:7]\n"
    "ds_write_b128 v241, v[24:27]\n"
    "v_mfma_f32_16x16x32_bf16 v[8:11], a[128:131], v[64:67], v[8:11]\n"
    "ds_write_b128 v241, v[28:31] offset:1024\n"
    "v_mfma_f32_16x16x32_bf16 v[12:15], a[128:131], v[68:71], v[12:15]\n"
    "ds_write2st64_b32 v243, v0, v4 offset1:1\n"
    "v_mfma_f32_16x16x32_bf16 v[8:11], a[132:135], v[72:75], v[8:11]\n"
    "s_waitcnt lgkmcnt(0)\n"
    "s_barrier\n"
    "ds_read2st64_b32 v[24:25], v242 offset1:8\n"
    "v_mfma_f32_16x16x32_bf16 v[12:15], a[132:135], v[76:79], v[12:15]\n"
    "ds_read2st64_b32 v[26:27], v242 offset0:16 offset1:24\n"
    "ds_read_b32 v108, v244\n"
    "v_mfma_f32_16x16x32_bf16 v[8:11], a[136:139], v[80:83], v[8:11]\n"
    "v_mfma_f32_16x16x32_bf16 v[12:15], a[136:139], v[84:87], v[12:15]\n"
    "v_mfma_f32_16x16x32_bf16 v[8:11], a[140:143], v[88:91], v[8:11]\n"
    "v_mfma_f32_16x16x32_bf16 v[12:15], a[140:143], v[92:95], v[12:15]\n"
    "v_mfma_f32_16x16x32_bf16 v[8:11], a[144:147], v[32:35], v[8:11]\n"
    "s_waitcnt lgkmcnt(0)\n"
    "v_add_f32 v24, v24, v26\n"
    "v_add_f32 v25, v25, v27\n"
    "v_mfma_f32_16x16x32_bf16 v[12:15], a[144:147], v[36:39], v[12:15]\n"
    "v_add_f32 v112, v24, v25\n"
    "v_mul_f32 v108, 0x3acccccd, v108\n"
    "v_mfma_f32_16x16x32_bf16 v[8:11], a[148:151], v[40:43], v[8:11]\n"
    "v_mul_f32 v112, 0x3acccccd, v112\n"
    "v_fma_f32 v112, -v108, v108, v112\n"
    "v_mfma_f32_16x16x32_bf16 v[12:15], a[148:151], v[44:47], v[12:15]\n"
    "v_max_f32 v112, 0, v112\n"
    "v_cvt_pk_bf16_f32 v108, v108, v112\n"
    "v_mfma_f32_16x16x32_bf16 v[8:11], a[152:155], v[48:51], v[8:11]\n"
    "v_and_b32 v112, 0xffff0000, v108\n"
    "v_lshlrev_b32 v108, 16, v108\n"
    "v_mfma_f32_16x16x32_bf16 v[12:15], a[152:155], v[52:55], v[12:15]\n"
    "v_add_f32 v112, 0x3727c5ac, v112\n"
    "v_mov_b32 v110, v108\n"
    "v_mfma_f32_16x16x32_bf16 v[8:11], a[156:159], v[56:59], v[8:11]\n"
    "v_cvt_pk_bf16_f32 v112, v112, v112\n"
    "v_lshlrev_b32 v112, 16, v112\n"
    "v_mfma_f32_16x16x32_bf16 v[12:15], a[156:159], v[60:63], v[12:15]\n"
    "v_sqrt_f32 v112, v112\n"
    "v_mfma_f32_16x16x32_bf16 v[16:19], a[208:211], v[64:67], v[16:19]\n"
    "v_permlane32_swap_b32 v108, v110\n"
    "v_mfma_f32_16x16x32_bf16 v[20:23], a[208:211], v[68:71], v[20:23]\n"
    "v_cvt_pk_bf16_f32 v112, v112, v112\n"
    "v_lshlrev_b32 v112, 16, v112\n"
    "v_mfma_f32_16x16x32_bf16 v[16:19], a[212:215], v[72:75], v[16:19]\n"
    "v_rcp_f32 v112, v112\n"
    "v_mfma_f32_16x16x32_bf16 v[20:23], a[212:215], v[76:79], v[20:23]\n"
    "v_mov_b32 v114, v112\n"
    "v_mfma_f32_16x16x32_bf16 v[16:19], a[216:219], v[80:83], v[16:19]\n"
    "s_nop 0\n"
    "v_permlane32_swap_b32 v112, v114\n"
    "v_mfma_f32_16x16x32_bf16 v[20:23], a[216:219], v[84:87], v[20:23]\n"
    "v_fma_f32 v0, v96, -v108, v0\n"
    "v_fma_f32 v1, v97, -v108, v1\n"
    "v_mfma_f32_16x16x32_bf16 v[16:19], a[220:223], v[88:91], v[16:19]\n"
    "v_fma_f32 v2, v98, -v108, v2\n"
    "v_fma_f32 v3, v99, -v108, v3\n"
    "v_mfma_f32_16x16x32_bf16 v[20:23], a[220:223], v[92:95], v[20:23]\n"
    "v_fma_f32 v4, v96, -v110, v4\n"
    "v_fma_f32 v5, v97, -v110, v5\n"
    "v_mfma_f32_16x16x32_bf16 v[16:19], a[224:227], v[32:35], v[16:19]\n"
    "v_fma_f32 v6, v98, -v110, v6\n"
    "v_fma_f32 v7, v99, -v110, v7\n"
    "v_mfma_f32_16x16x32_bf16 v[20:23], a[224:227], v[36:39], v[20:23]\n"
    "v_mul_f32 v0, v0, v112\n"
    "v_mul_f32 v1, v1, v112\n"
    "v_mfma_f32_16x16x32_bf16 v[16:19], a[228:231], v[40:43], v[16:19]\n"
    "v_mul_f32 v2, v2, v112\n"
    "v_mul_f32 v3, v3, v112\n"
    "v_mfma_f32_16x16x32_bf16 v[20:23], a[228:231], v[44:47], v[20:23]\n"
    "v_mul_f32 v4, v4, v114\n"
    "v_mul_f32 v5, v5, v114\n"
    "v_mfma_f32_16x16x32_bf16 v[16:19], a[232:235], v[48:51], v[16:19]\n"
    "v_mul_f32 v6, v6, v114\n"
    "v_mul_f32 v7, v7, v114\n"
    "v_mfma_f32_16x16x32_bf16 v[20:23], a[232:235], v[52:55], v[20:23]\n"
    "v_cvt_pk_bf16_f32 v0, v0, v1\n"
    "v_cvt_pk_bf16_f32 v1, v2, v3\n"
    "v_mfma_f32_16x16x32_bf16 v[16:19], a[236:239], v[56:59], v[16:19]\n"
    "ds_write_b64 v231, v[0:1] offset:0\n"
    "v_mfma_f32_16x16x32_bf16 v[20:23], a[236:239], v[60:63], v[20:23]\n"
    "v_cvt_pk_bf16_f32 v4, v4, v5\n"
    "v_cvt_pk_bf16_f32 v5, v6, v7\n"
    ".Lb1_mk2x11_%=:\n"
    "ds_write_b64 v231, v[4:5] offset:1024\n"
    "v_pk_fma_f32 v[8:9], v[100:101], v[108:109], v[8:9] op_sel_hi:[1,0,1] neg_lo:[0,1,0] neg_hi:[0,1,0]\n"
    "v_pk_fma_f32 v[10:11], v[102:103], v[108:109], v[10:11] op_sel_hi:[1,0,1] neg_lo:[0,1,0] neg_hi:[0,1,0]\n"
    "v_pk_fma_f32 v[12:13], v[100:101], v[110:111], v[12:13] op_sel_hi:[1,0,1] neg_lo:[0,1,0] neg_hi:[0,1,0]\n"
    "v_pk_fma_f32 v[14:15], v[102:103], v[110:111], v[14:15] op_sel_hi:[1,0,1] neg_lo:[0,1,0] neg_hi:[0,1,0]\n"
    "v_pk_mul_f32 v[8:9], v[8:9], v[112:113] op_sel_hi:[1,0]\n"
    "v_pk_mul_f32 v[10:11], v[10:11], v[112:113] op_sel_hi:[1,0]\n"
    "v_pk_mul_f32 v[12:13], v[12:13], v[114:115] op_sel_hi:[1,0]\n"
    "v_pk_mul_f32 v[14:15], v[14:15], v[114:115] op_sel_hi:[1,0]\n"
    "v_cvt_pk_bf16_f32 v8, v8, v9\n"
    "v_cvt_pk_bf16_f32 v9, v10, v11\n"
    "ds_write_b64 v232, v[8:9] offset:0\n"
    "v_cvt_pk_bf16_f32 v12, v12, v13\n"
    "v_cvt_pk_bf16_f32 v13, v14, v15\n"
    "ds_write_b64 v232, v[12:13] offset:1024\n"
    "v_pk_fma_f32 v[16:17], v[104:105], v[108:109], v[16:17] op_sel_hi:[1,0,1] neg_lo:[0,1,0] neg_hi:[0,1,0]\n"
    "v_pk_fma_f32 v[18:19], v[106:107], v[108:109], v[18:19] op_sel_hi:[1,0,1] neg_lo:[0,1,0] neg_hi:[0,1,0]\n"
    "v_pk_fma_f32 v[20:21], v[104:105], v[110:111], v[20:21] op_sel_hi:[1,0,1] neg_lo:[0,1,0] neg_hi:[0,1,0]\n"
    "v_pk_fma_f32 v[22:23], v[106:107], v[110:111], v[22:23] op_sel_hi:[1,0,1] neg_lo:[0,1,0] neg_hi:[0,1,0]\n"
    "v_pk_mul_f32 v[16:17], v[16:17], v[112:113] op_sel_hi:[1,0]\n"
    "v_pk_mul_f32 v[18:19], v[18:19], v[112:113] op_sel_hi:[1,0]\n"
    "v_pk_mul_f32 v[20:21], v[20:21], v[114:115] op_sel_hi:[1,0]\n"
    "v_pk_mul_f32 v[22:23], v[22:23], v[114:115] op_sel_hi:[1,0]\n"
    "v_cvt_pk_bf16_f32 v16, v16, v17\n"
    "v_cvt_pk_bf16_f32 v17, v18, v19\n"
    "ds_write_b64 v233, v[16:17] offset:0\n"
    "v_cvt_pk_bf16_f32 v20, v20, v21\n"
    "v_cvt_pk_bf16_f32 v21, v22, v23\n"
    "ds_write_b64 v233, v[20:21] offset:1024\n"
    "s_waitcnt lgkmcnt(0)\n"
    "s_barrier\n"
    ".Lb1_mk3x12_%=:\n"
    "ds_read_b128 v[32:35], v236 offset:1024\n"
    "ds_read_b128 v[44:47], v234 offset:0\n"
    "ds_read_b128 v[36:39], v235 offset:0\n"
    "ds_read_b128 v[48:51], v234 offset:1024\n"
    "ds_read_b128 v[40:43], v235 offset:1024\n"
    "ds_read_b64_tr_b16 v[52:53], v238 offset:1024\n"
    "ds_read_b64_tr_b16 v[54:55], v237 offset:0\n"
    "ds_read_b64_tr_b16 v[56:57], v237 offset:1024\n"
    "ds_read_b64_tr_b16 v[58:59], v240 offset:1024\n"
    "ds_read_b64_tr_b16 v[60:61], v239 offset:0\n"
    "ds_read_b64_tr_b16 v[62:63], v239 offset:1024\n"
    "s_waitcnt lgkmcnt(9)\n"
    "v_mfma_f32_16x16x32_bf16 v[64:67], v[32:35], v[44:47], 0\n"
    "s_waitcnt lgkmcnt(8)\n"
    "v_mfma_f32_16x16x32_bf16 v[68:71], v[36:39], v[44:47], 0\n"
    "s_waitcnt lgkmcnt(7)\n"
    "v_mfma_f32_16x16x32_bf16 v[72:75], v[36:39], v[48:51], 0\n"
    "s_waitcnt lgkmcnt(6)\n"
    "v_mfma_f32_16x16x32_bf16 v[76:79], v[40:43], v[48:51], 0\n"
    "v_cndmask_b32 v64, v64, v65, s[42:43]\n"
    "v_cndmask_b32 v65, v66, v67, s[42:43]\n"
    "v_cndmask_b32 v66, v68, v69, s[42:43]\n"
    "v_cndmask_b32 v67, v70, v71, s[42:43]\n"
    "s_nop 0\n"
    "v_cndmask_b32 v68, v72, v73, s[42:43]\n"
    "v_cndmask_b32 v69, v74, v75, s[42:43]\n"
    "s_nop 0\n"
    "v_cndmask_b32 v70, v76, v77, s[42:43]\n"
    "v_cndmask_b32 v71, v78, v79, s[42:43]\n"
    "v_cndmask_b32 v64, v249, v64, s[38:39]\n"
    "v_cndmask_b32 v65, v249, v65, s[40:41]\n"
    "v_cndmask_b32 v66, v249, v66, s[12:13]\n"
    "v_cndmask_b32 v67, v249, v67, s[14:15]\n"
    "v_cndmask_b32 v68, v249, v68, s[8:9]\n"
    "v_cndmask_b32 v69, v249, v69, s[10:11]\n"
    "v_cndmask_b32 v70, v249, v70, s[12:13]\n"
    "v_cndmask_b32 v71, v249, v71, s[14:15]\n"
    "v_max3_f32 v72, v64, v65, v66\n"
    "v_max3_f32 v74, v68, v69, v70\n"
    "v_max_f32 v72, v72, v67\n"
    "v_max_f32 v74, v74, v71\n"
    "s_nop 1\n"
    "v_permlane16_swap_b32 v72, v74\n"
    "v_max_f32 v72, v72, v74\n"
    "v_mov_b32 v74, v72\n"
    "s_nop 1\n"
    "v_permlane32_swap_b32 v72, v74\n"
    "v_max_f32 v72, v72, v74\n"
    "v_mul_f32 v72, s37, v72\n"
    "v_mov_b32 v74, v72\n"
    "s_nop 1\n"
    "v_permlane16_swap_b32 v72, v74\n"
    "v_fma_f32 v64, v64, s37, -v72\n"
    "v_fma_f32 v65, v65, s37, -v72\n"
    "v_fma_f32 v66, v66, s37, -v72\n"
    "v_fma_f32 v67, v67, s37, -v72\n"
    "v_fma_f32 v68, v68, s37, -v74\n"
    "v_fma_f32 v69, v69, s37, -v74\n"
    "v_fma_f32 v70, v70, s37, -v74\n"
    "v_fma_f32 v71, v71, s37, -v74\n"
    "v_exp_f32 v64, v64\n"
    "v_exp_f32 v65, v65\n"
    "v_exp_f32 v66, v66\n"
    "v_exp_f32 v67, v67\n"
    "v_exp_f32 v68, v68\n"
    "v_exp_f32 v69, v69\n"
    "v_exp_f32 v70, v70\n"
    "v_exp_f32 v71, v71\n"
    "v_pk_add_f32 v[72:73], v[64:65], v[66:67]\n"
    "v_pk_add_f32 v[74:75], v[68:69], v[70:71]\n"
    "v_add_f32 v72, v72, v73\n"
    "v_add_f32 v74, v74, v75\n"
    "s_nop 1\n"
    "v_permlane16_swap_b32 v72, v74\n"
    "v_add_f32 v72, v72, v74\n"
    "v_mov_b32 v74, v72\n"
    "s_nop 1\n"
    "v_permlane32_swap_b32 v72, v74\n"
    "v_add_f32 v72, v72, v74\n"
    "v_rcp_f32 v72, v72\n"
    "s_nop 0\n"
    "v_mov_b32 v74, v72\n"
    "s_nop 1\n"
    "v_permlane16_swap_b32 v72, v74\n"
    "v_pk_mul_f32 v[64:65], v[64:65], v[72:73] op_sel_hi:[1,0]\n"
    "v_pk_mul_f32 v[66:67], v[66:67], v[72:73] op_sel_hi:[1,0]\n"
    "v_pk_mul_f32 v[68:69], v[68:69], v[74:75] op_sel_hi:[1,0]\n"
    "v_pk_mul_f32 v[70:71], v[70:71], v[74:75] op_sel_hi:[1,0]\n"
    "v_cvt_pk_bf16_f32 v64, v64, v65\n"
    "v_cvt_pk_bf16_f32 v66, v66, v67\n"
    "v_cvt_pk_bf16_f32 v68, v68, v69\n"
    "v_cvt_pk_bf16_f32 v70, v70, v71\n"
    "v_perm_b32 v80, v64, v64, v247\n"
    "v_perm_b32 v81, v64, v64, v248\n"
    "v_perm_b32 v82, v66, v66, v247\n"
    "v_perm_b32 v83, v66, v66, v248\n"
    "v_perm_b32 v84, v68, v68, v247\n"
    "v_perm_b32 v85, v68, v68, v248\n"
    "v_perm_b32 v86, v70, v70, v247\n"
    "v_perm_b32 v87, v70, v70, v248\n"
    "s_waitcnt lgkmcnt(0)\n"
    "v_mfma_f32_16x16x32_bf16 v[0:3], v[52:55], v[80:83], 0\n"
    "v_mfma_f32_16x16x32_bf16 v[4:7], v[54:57], v[84:87], 0\n"
    "v_mfma_f32_16x16x32_bf16 v[8:11], v[58:61], v[80:83], 0\n"
    "v_mfma_f32_16x16x32_bf16 v[12:15], v[60:63], v[84:87], 0\n"
    "s_mov_b64 s[38:39], s[8:9]\n"
    "s_mov_b64 s[40:41], s[10:11]\n"
    "v_cvt_pk_bf16_f32 v16, v0, v1\n"
    "v_cvt_pk_bf16_f32 v17, v2, v3\n"
    "v_cvt_pk_bf16_f32 v18, v4, v5\n"
    "v_cvt_pk_bf16_f32 v19, v6, v7\n"
    "s_nop 0\n"
    "v_permlane16_swap_b32 v16, v18\n"
    "v_permlane16_swap_b32 v17, v19\n"
    "buffer_store_dwordx4 v[16:19], v245, s[0:3], s33 offen sc1\n"
    "v_cvt_pk_bf16_f32 v20, v8, v9\n"
    "v_cvt_pk_bf16_f32 v21, v10, v11\n"
    "v_cvt_pk_bf16_f32 v22, v12, v13\n"
    "v_cvt_pk_bf16_f32 v23, v14, v15\n"
    "buffer_store_dwordx4 v[20:23], v246, s[0:3], s33 offen sc1\n"
    ".Lb1_mk4x13_%=:\n"
    "v_mov_b32 v108, 0\n"
    "s_memrealtime s[26:27]\n"
    "s_cmp_lg_u32 s18, 0\n"
    "s_waitcnt lgkmcnt(0)\n"
    "s_cbranch_scc1 .Lb1_pl15_%=\n"
    "s_sub_u32 s28, s36, s26\n"
    "s_cmp_gt_i32 s28, 40\n"
    "s_cbranch_scc1 .Lb1_pl15_%=\n"
    "buffer_load_dwordx4 v[108:111], v229, s[0:3], s21 offen sc1\n"
    ".Lb1_pl15_%=:\n"
    "s_waitcnt vmcnt(0)\n"
    "s_barrier\n"
    "buffer_store_dwordx4 v[116:119], v230, s[0:3], 0 offen sc1\n"
    ".Lb1_mk5x15_%=:\n"
    "s_mov_b32 s30, s31\n"
    "s_add_u32 s31, s31, 1\n"
    "s_cmp_lt_u32 s30, 64\n"
    "s_cbranch_scc1 .Lb1_top_%=\n"
    "s_waitcnt vmcnt(0) lgkmcnt(0)\n"
    "s_endpgm\n"
    ".Lb1_tk0_%=:\n"
    ".Lb1_po3_%=:\n"
    "buffer_load_dword v31, v30, s[0:3], 0 offen sc1\n"
    "s_waitcnt vmcnt(0)\n"
    "v_cmp_gt_i32 vcc, 32, v31\n"
    "s_nop 0\n"
    "s_cbranch_vccz .Lb1_pd4_%=\n"
    "s_add_u32 s24, s24, 1\n"
    "s_cmp_lt_u32 s24, 0x40000\n"
    "s_cbranch_scc1 .Lb1_sp5_%=\n"
    "v_mov_b32 v31, 90\n"
    "s_mov_b32 s28, 33728\n"
    "buffer_store_dword v31, off, s[0:3], s28 sc1\n"
    "s_waitcnt vmcnt(0)\n"
    "s_branch .Lb1_pd4_%=\n"
    ".Lb1_sp5_%=:\n"
    "s_sleep 8\n"
    "s_branch .Lb1_po3_%=\n"
    ".Lb1_pd4_%=:\n"
    "s_branch .Lb1_tk1_%=\n"
    :: "{s[0:3]}"(s0_), "{s[4:7]}"(s4_), "{s[8:11]}"(s8_), "{s[12:15]}"(s12_), "{s[16:19]}"(s16_), "{s[20:23]}"(s20_), "{v[0:3]}"(v0_), "{v[224:227]}"(v224_), "{v[228:231]}"(v228_), "{v[232:235]}"(v232_), "{v[236:239]}"(v236_), "{v[240:243]}"(v240_), "{v[244:247]}"(v244_), "{v[248:251]}"(v248_) : "memory", "v255", "a255", "s95");
    __builtin_unreachable();
}
DEV void b2_role(const MegaArgs& A, const int L, const int c, const int rep) {
    ROLE_HEAD;
    const int b = 4 * c + w;
    const u32x4 ro = L ? u32x4{lo(A.out), hi(A.out), 2048u * 1280u, 0x20000u} : rx;
    if (L == 0) {
#pragma unroll
        for (int k = 0; k < 16; ++k) bst16(rr, ST0 + b * 32768 + (k >> 3) * 16384 + (k & 7) * 1024 + l * 16, ones);
        DRAIN();
        wait_drawn(A.ar);
        DRAIN_BARRIER();
        bst4(rr, threadIdx.x == 0 ? xl(Z_ST + c * 128, 0) : OOB, 1u);
    }
    const u32x4 s0_ = {RFL(rx[0]), RFL(rx[1]), RFL(rx[2]), RFL(rx[3])};
    const u32x4 s4_ = {RFL(lo(A.wo + 32 * b)), RFL(hi(A.wo + 32 * b)), RFL((640 * 640 - 32 * b) * 2), RFL(0x20000u)};
    const u32x4 s8_ = {RFL(ro[0]), RFL(ro[1]), RFL(ro[2]), RFL(ro[3])};
    const u32x4 s12_ = {RFL(w), RFL(L << 13), RFL(REC0 + RC_O + w * 2048), RFL(xl(Z_O + rep * 2048, L))};
    const u32x4 s16_ = {RFL(xl(Z_ST, 0)), RFL(ST0), RFL(DBG0 + ((L * 128 + 107 + c) * 4 + w) * 2048), RFL(blockIdx.x == 0 ? 6 : 0)};
    const u32x4 v0_ = {(unsigned)((0 + (l >> 2)) * 1280 + (l & 3) * 16 - 0), (unsigned)((16 + (l >> 2)) * 1280 + (l & 3) * 16 - 1024), (unsigned)((32 + (l >> 2)) * 1280 + (l & 3) * 16 - 2048), (unsigned)((48 + (l >> 2)) * 1280 + (l & 3) * 16 - 3072)};
    const u32x4 v4_ = {(unsigned)(w * 40960 + (8 * (lq & 1) + 20 * (lq >> 1) + (l16 >> 2)) * 64 + (l & 3) * 16), (unsigned)(w * 40960 + (20 * lq + 16 + (l16 >> 2)) * 64 + (l & 3) * 16), (unsigned)((l < 5 ? l : 0) * 128), (unsigned)(0u)};
    const u32x4 v224_ = {(unsigned)(l * 16), (unsigned)((l < 11 ? l : 0) * 128), (unsigned)(l < 4 && w == 1 ? xl(Z_XN + l * 2048 + c * 128, 0) : OOB), (unsigned)(c * 16384 + l * 16)};
    const u32x4 v228_ = {(unsigned)(L ? (l16 + 0) * 1280 + b * 64 + lq * 16 : RC_X2 + c * 16384 + 0 + l * 16), (unsigned)(L ? (l16 + 16) * 1280 + b * 64 + lq * 16 : RC_X2 + c * 16384 + 1024 + l * 16), (unsigned)(l < 16 ? b * 32768 + l * 16 : OOB), (unsigned)((0 + lq) * 32768 + l16 * 16)};
    const u32x4 v232_ = {(unsigned)((4 + lq) * 32768 + l16 * 16), (unsigned)((8 + lq) * 32768 + l16 * 16), (unsigned)((12 + lq) * 32768 + l16 * 16), (unsigned)((16 + lq) * 32768 + l16 * 16)};
    asm volatile(
    "s_mov_b32 s20, 0\n"
    "s_mov_b64 s[22:23], 0\n"
    "s_mov_b32 s25, 0\n"
    "s_cmp_eq_u32 s13, 0\n"
    "s_cselect_b32 s24, 24, 60\n"
    "s_sub_u32 s24, s24, s19\n"
    ".Lb2_ho1_%=:\n"
    "s_sub_u32 s24, s24, 1\n"
    "s_cbranch_scc1 .Lb2_hd2_%=\n"
    "s_sleep 17\n"
    "s_branch .Lb2_ho1_%=\n"
    ".Lb2_hd2_%=:\n"
    "v_and_b32 v30, 0x70, v224\n"
    "v_lshlrev_b32 v30, 2, v30\n"
    "v_or_b32 v30, 0x8000, v30\n"
    "buffer_load_dword v31, v30, s[0:3], 0 offen sc1\n"
    "s_waitcnt vmcnt(0)\n"
    "v_cmp_gt_i32 vcc, 32, v31\n"
    "s_nop 0\n"
    "s_cbranch_vccnz .Lb2_tk0_%=\n"
    ".Lb2_tk1_%=:\n"
    "s_mul_i32 s28, s12, 40960\n"
    "s_mov_b32 s34, 0\n"
    "s_add_u32 m0, s28, 0\n"
    "s_nop 0\n"
    "buffer_load_dwordx4 v0, s[4:7], s34 offen offset:0 lds\n"
    "buffer_load_dwordx4 v1, s[4:7], s34 offen offset:1024 lds\n"
    "buffer_load_dwordx4 v2, s[4:7], s34 offen offset:2048 lds\n"
    "buffer_load_dwordx4 v3, s[4:7], s34 offen offset:3072 lds\n"
    "s_mov_b32 s34, 81920\n"
    "s_add_u32 m0, s28, 4096\n"
    "s_nop 0\n"
    "buffer_load_dwordx4 v0, s[4:7], s34 offen offset:0 lds\n"
    "buffer_load_dwordx4 v1, s[4:7], s34 offen offset:1024 lds\n"
    "buffer_load_dwordx4 v2, s[4:7], s34 offen offset:2048 lds\n"
    "buffer_load_dwordx4 v3, s[4:7], s34 offen offset:3072 lds\n"
    "s_mov_b32 s34, 163840\n"
    "s_add_u32 m0, s28, 8192\n"
    "s_nop 0\n"
    "buffer_load_dwordx4 v0, s[4:7], s34 offen offset:0 lds\n"
    "buffer_load_dwordx4 v1, s[4:7], s34 offen offset:1024 lds\n"
    "buffer_load_dwordx4 v2, s[4:7], s34 offen offset:2048 lds\n"
    "buffer_load_dwordx4 v3, s[4:7], s34 offen offset:3072 lds\n"
    "s_mov_b32 s34, 245760\n"
    "s_add_u32 m0, s28, 12288\n"
    "s_nop 0\n"
    "buffer_load_dwordx4 v0, s[4:7], s34 offen offset:0 lds\n"
    "buffer_load_dwordx4 v1, s[4:7], s34 offen offset:1024 lds\n"
    "buffer_load_dwordx4 v2, s[4:7], s34 offen offset:2048 lds\n"
    "buffer_load_dwordx4 v3, s[4:7], s34 offen offset:3072 lds\n"
    "s_mov_b32 s34, 327680\n"
    "s_add_u32 m0, s28, 16384\n"
    "s_nop 0\n"
    "buffer_load_dwordx4 v0, s[4:7], s34 offen offset:0 lds\n"
    "buffer_load_dwordx4 v1, s[4:7], s34 offen offset:1024 lds\n"
    "buffer_load_dwordx4 v2, s[4:7], s34 offen offset:2048 lds\n"
    "buffer_load_dwordx4 v3, s[4:7], s34 offen offset:3072 lds\n"
    "s_mov_b32 s34, 409600\n"
    "s_add_u32 m0, s28, 20480\n"
    "s_nop 0\n"
    "buffer_load_dwordx4 v0, s[4:7], s34 offen offset:0 lds\n"
    "buffer_load_dwordx4 v1, s[4:7], s34 offen offset:1024 lds\n"
    "buffer_load_dwordx4 v2, s[4:7], s34 offen offset:2048 lds\n"
    "buffer_load_dwordx4 v3, s[4:7], s34 offen offset:3072 lds\n"
    "s_mov_b32 s34, 491520\n"
    "s_add_u32 m0, s28, 24576\n"
    "s_nop 0\n"
    "buffer_load_dwordx4 v0, s[4:7], s34 offen offset:0 lds\n"
    "buffer_load_dwordx4 v1, s[4:7], s34 offen offset:1024 lds\n"
    "buffer_load_dwordx4 v2, s[4:7], s34 offen offset:2048 lds\n"
    "buffer_load_dwordx4 v3, s[4:7], s34 offen offset:3072 lds\n"
    "s_mov_b32 s34, 573440\n"
    "s_add_u32 m0, s28, 28672\n"
    "s_nop 0\n"
    "buffer_load_dwordx4 v0, s[4:7], s34 offen offset:0 lds\n"
    "buffer_load_dwordx4 v1, s[4:7], s34 offen offset:1024 lds\n"
    "buffer_load_dwordx4 v2, s[4:7], s34 offen offset:2048 lds\n"
    "buffer_load_dwordx4 v3, s[4:7], s34 offen offset:3072 lds\n"
    "s_mov_b32 s34, 655360\n"
    "s_add_u32 m0, s28, 32768\n"
    "s_nop 0\n"
    "buffer_load_dwordx4 v0, s[4:7], s34 offen offset:0 lds\n"
    "buffer_load_dwordx4 v1, s[4:7], s34 offen offset:1024 lds\n"
    "buffer_load_dwordx4 v2, s[4:7], s34 offen offset:2048 lds\n"
    "buffer_load_dwordx4 v3, s[4:7], s34 offen offset:3072 lds\n"
    "s_mov_b32 s34, 737280\n"
    "s_add_u32 m0, s28, 36864\n"
    "s_nop 0\n"
    "buffer_load_dwordx4 v0, s[4:7], s34 offen offset:0 lds\n"
    "buffer_load_dwordx4 v1, s[4:7], s34 offen offset:1024 lds\n"
    "buffer_load_dwordx4 v2, s[4:7], s34 offen offset:2048 lds\n"
    "buffer_load_dwordx4 v3, s[4:7], s34 offen offset:3072 lds\n"
    "s_waitcnt vmcnt(0)\n"
    "ds_read_b64_tr_b16 a[0:1], v4 offset:0\n"
    "ds_read_b64_tr_b16 a[2:3], v4 offset:256\n"
    "ds_read_b64_tr_b16 a[80:81], v4 offset:8\n"
    "ds_read_b64_tr_b16 a[82:83], v4 offset:264\n"
    "s_waitcnt lgkmcnt(0)\n"
    "ds_read_b64_tr_b16 a[4:5], v4 offset:2560\n"
    "ds_read_b64_tr_b16 a[6:7], v4 offset:2816\n"
    "ds_read_b64_tr_b16 a[84:85], v4 offset:2568\n"
    "ds_read_b64_tr_b16 a[86:87], v4 offset:2824\n"
    "s_waitcnt lgkmcnt(0)\n"
    "ds_read_b64_tr_b16 a[8:9], v4 offset:5120\n"
    "ds_read_b64_tr_b16 a[10:11], v4 offset:5376\n"
    "ds_read_b64_tr_b16 a[88:89], v4 offset:5128\n"
    "ds_read_b64_tr_b16 a[90:91], v4 offset:5384\n"
    "s_waitcnt lgkmcnt(0)\n"
    "ds_read_b64_tr_b16 a[12:13], v4 offset:7680\n"
    "ds_read_b64_tr_b16 a[14:15], v4 offset:7936\n"
    "ds_read_b64_tr_b16 a[92:93], v4 offset:7688\n"
    "ds_read_b64_tr_b16 a[94:95], v4 offset:7944\n"
    "s_waitcnt lgkmcnt(0)\n"
    "ds_read_b64_tr_b16 a[16:17], v4 offset:10240\n"
    "ds_read_b64_tr_b16 a[18:19], v4 offset:10496\n"
    "ds_read_b64_tr_b16 a[96:97], v4 offset:10248\n"
    "ds_read_b64_tr_b16 a[98:99], v4 offset:10504\n"
    "s_waitcnt lgkmcnt(0)\n"
    "ds_read_b64_tr_b16 a[20:21], v4 offset:12800\n"
    "ds_read_b64_tr_b16 a[22:23], v4 offset:13056\n"
    "ds_read_b64_tr_b16 a[100:101], v4 offset:12808\n"
    "ds_read_b64_tr_b16 a[102:103], v4 offset:13064\n"
    "s_waitcnt lgkmcnt(0)\n"
    "ds_read_b64_tr_b16 a[24:25], v4 offset:15360\n"
    "ds_read_b64_tr_b16 a[26:27], v4 offset:15616\n"
    "ds_read_b64_tr_b16 a[104:105], v4 offset:15368\n"
    "ds_read_b64_tr_b16 a[106:107], v4 offset:15624\n"
    "s_waitcnt lgkmcnt(0)\n"
    "ds_read_b64_tr_b16 a[28:29], v4 offset:17920\n"
    "ds_read_b64_tr_b16 a[30:31], v4 offset:18176\n"
    "ds_read_b64_tr_b16 a[108:109], v4 offset:17928\n"
    "ds_read_b64_tr_b16 a[110:111], v4 offset:18184\n"
    "s_waitcnt lgkmcnt(0)\n"
    "ds_read_b64_tr_b16 a[32:33], v4 offset:20480\n"
    "ds_read_b64_tr_b16 a[34:35], v4 offset:20736\n"
    "ds_read_b64_tr_b16 a[112:113], v4 offset:20488\n"
    "ds_read_b64_tr_b16 a[114:115], v4 offset:20744\n"
    "s_waitcnt lgkmcnt(0)\n"
    "ds_read_b64_tr_b16 a[36:37], v4 offset:23040\n"
    "ds_read_b64_tr_b16 a[38:39], v4 offset:23296\n"
    "ds_read_b64_tr_b16 a[116:117], v4 offset:23048\n"
    "ds_read_b64_tr_b16 a[118:119], v4 offset:23304\n"
    "s_waitcnt lgkmcnt(0)\n"
    "ds_read_b64_tr_b16 a[40:41], v4 offset:25600\n"
    "ds_read_b64_tr_b16 a[42:43], v4 offset:25856\n"
    "ds_read_b64_tr_b16 a[120:121], v4 offset:25608\n"
    "ds_read_b64_tr_b16 a[122:123], v4 offset:25864\n"
    "s_waitcnt lgkmcnt(0)\n"
    "ds_read_b64_tr_b16 a[44:45], v4 offset:28160\n"
    "ds_read_b64_tr_b16 a[46:47], v4 offset:28416\n"
    "ds_read_b64_tr_b16 a[124:125], v4 offset:28168\n"
    "ds_read_b64_tr_b16 a[126:127], v4 offset:28424\n"
    "s_waitcnt lgkmcnt(0)\n"
    "ds_read_b64_tr_b16 a[48:49], v4 offset:30720\n"
    "ds_read_b64_tr_b16 a[50:51], v4 offset:30976\n"
    "ds_read_b64_tr_b16 a[128:129], v4 offset:30728\n"
    "ds_read_b64_tr_b16 a[130:131], v4 offset:30984\n"
    "s_waitcnt lgkmcnt(0)\n"
    "ds_read_b64_tr_b16 a[52:53], v4 offset:33280\n"
    "ds_read_b64_tr_b16 a[54:55], v4 offset:33536\n"
    "ds_read_b64_tr_b16 a[132:133], v4 offset:33288\n"
    "ds_read_b64_tr_b16 a[134:135], v4 offset:33544\n"
    "s_waitcnt lgkmcnt(0)\n"
    "ds_read_b64_tr_b16 a[56:57], v4 offset:35840\n"
    "ds_read_b64_tr_b16 a[58:59], v4 offset:36096\n"
    "ds_read_b64_tr_b16 a[136:137], v4 offset:35848\n"
    "ds_read_b64_tr_b16 a[138:139], v4 offset:36104\n"
    "s_waitcnt lgkmcnt(0)\n"
    "ds_read_b64_tr_b16 a[60:61], v4 offset:38400\n"
    "ds_read_b64_tr_b16 a[62:63], v4 offset:38656\n"
    "ds_read_b64_tr_b16 a[140:141], v4 offset:38408\n"
    "ds_read_b64_tr_b16 a[142:143], v4 offset:38664\n"
    "s_waitcnt lgkmcnt(0)\n"
    "ds_read_b64_tr_b16 a[64:65], v5 offset:0\n"
    "ds_read_b64_tr_b16 a[66:67], v5 offset:5120\n"
    "ds_read_b64_tr_b16 a[144:145], v5 offset:8\n"
    "ds_read_b64_tr_b16 a[146:147], v5 offset:5128\n"
    "s_waitcnt lgkmcnt(0)\n"
    "ds_read_b64_tr_b16 a[68:69], v5 offset:10240\n"
    "ds_read_b64_tr_b16 a[70:71], v5 offset:15360\n"
    "ds_read_b64_tr_b16 a[148:149], v5 offset:10248\n"
    "ds_read_b64_tr_b16 a[150:151], v5 offset:15368\n"
    "s_waitcnt lgkmcnt(0)\n"
    "ds_read_b64_tr_b16 a[72:73], v5 offset:20480\n"
    "ds_read_b64_tr_b16 a[74:75], v5 offset:25600\n"
    "ds_read_b64_tr_b16 a[152:153], v5 offset:20488\n"
    "ds_read_b64_tr_b16 a[154:155], v5 offset:25608\n"
    "s_waitcnt lgkmcnt(0)\n"
    "ds_read_b64_tr_b16 a[76:77], v5 offset:30720\n"
    "ds_read_b64_tr_b16 a[78:79], v5 offset:35840\n"
    "ds_read_b64_tr_b16 a[156:157], v5 offset:30728\n"
    "ds_read_b64_tr_b16 a[158:159], v5 offset:35848\n"
    "s_waitcnt lgkmcnt(0)\n"
    "s_mov_b32 s26, -2\n"
    ".Lb2_po6_%=:\n"
    "buffer_load_dword v8, v6, s[0:3], s16 offen sc1\n"
    "s_waitcnt vmcnt(0)\n"
    "v_cmp_gt_i32 vcc, 1, v8\n"
    "s_nop 0\n"
    "s_cbranch_vccz .Lb2_pd7_%=\n"
    "s_add_u32 s20, s20, 1\n"
    "s_cmp_lt_u32 s20, 0x40000\n"
    "s_cbranch_scc1 .Lb2_sp8_%=\n"
    "v_mov_b32 v8, 70\n"
    "s_mov_b32 s24, 33728\n"
    "buffer_store_dword v8, off, s[0:3], s24 sc1\n"
    "s_waitcnt vmcnt(0)\n"
    "s_branch .Lb2_pd7_%=\n"
    ".Lb2_sp8_%=:\n"
    "s_sleep 4\n"
    "s_branch .Lb2_po6_%=\n"
    ".Lb2_pd7_%=:\n"
    "s_mov_b32 s26, 0\n"
    "s_lshl_b32 s33, s12, 11\n"
    "s_mov_b32 s35, 0\n"
    "v_mov_b32 v84, 0\n"
    ".Lb2_top_%=:\n"
    "s_lshl_b32 s28, s26, 19\n"
    "s_lshl_b32 s25, s26, 11\n"
    "s_xor_b32 s25, s25, s13\n"
    "s_and_b32 s25, s25, 0x2000\n"
    "s_add_u32 s28, s28, s25\n"
    "s_add_u32 s28, s28, s14\n"
    "s_add_u32 s27, s26, 1\n"
    "s_sub_u32 s29, s28, 81920\n"
    "s_sub_u32 s31, s28, 163840\n"
    "s_xor_b32 s31, s31, 0x2000\n"
    "s_mul_i32 s30, s26, 40960\n"
    "s_cmp_eq_u32 s13, 0\n"
    "s_cselect_b32 s30, s31, s30\n"
    "s_and_b32 s32, s26, 32\n"
    "s_add_u32 s32, s32, s26\n"
    "s_lshl_b32 s32, s32, 8\n"
    "s_add_u32 s32, s32, s17\n"
    "s_cmp_lg_u32 s12, 0\n"
    "s_cbranch_scc1 .Lb2_pd10_%=\n"
    "s_add_u32 s25, s22, 248\n"
    "v_min3_i32 v84, v84, v85, v86\n"
    "v_min_i32 v84, v84, v87\n"
    "v_cmp_gt_i32 vcc, s27, v84\n"
    "s_nop 0\n"
    "s_cbranch_vccz .Lb2_pd10_%=\n"
    ".Lb2_rt11_%=:\n"
    "s_memrealtime s[22:23]\n"
    "s_waitcnt lgkmcnt(0)\n"
    "s_sub_u32 s24, s35, s22\n"
    "s_cmp_lt_u32 s24, 600\n"
    "s_cbranch_scc1 .Lb2_rt11_%=\n"
    ".Lb2_po9_%=:\n"
    "buffer_load_dwordx4 v[84:87], v225, s[0:3], s15 offen sc1\n"
    "s_memrealtime s[22:23]\n"
    "s_waitcnt vmcnt(0) lgkmcnt(0)\n"
    "s_add_u32 s25, s22, 248\n"
    "v_min3_i32 v84, v84, v85, v86\n"
    "v_min_i32 v84, v84, v87\n"
    "v_cmp_gt_i32 vcc, s27, v84\n"
    "s_nop 0\n"
    "s_cbranch_vccz .Lb2_pd10_%=\n"
    "s_add_u32 s20, s20, 1\n"
    "s_cmp_lt_u32 s20, 0x40000\n"
    "s_cbranch_scc1 .Lb2_sp12_%=\n"
    "v_mov_b32 v84, 71\n"
    "s_mov_b32 s24, 33728\n"
    "buffer_store_dword v84, off, s[0:3], s24 sc1\n"
    "s_waitcnt vmcnt(0)\n"
    "s_branch .Lb2_pd10_%=\n"
    ".Lb2_sp12_%=:\n"
    "s_sleep 6\n"
    "s_branch .Lb2_po9_%=\n"
    ".Lb2_pd10_%=:\n"
    "s_barrier\n"
    "s_cmp_eq_u32 s27, 63\n"
    "s_cselect_b32 s35, s22, s25\n"
    ".Lb2_mk0x12_%=:\n"
    "s_add_u32 m0, s33, 0\n"
    "s_nop 0\n"
    "buffer_load_dwordx4 v224, s[0:3], s28 offen sc1 lds\n"
    "buffer_load_dwordx4 v224, s[0:3], s28 offen offset:1024 sc1 lds\n"
    "s_add_u32 m0, s33, 8192\n"
    "s_add_u32 s28, s28, 0x4000\n"
    "buffer_load_dwordx4 v224, s[0:3], s28 offen sc1 lds\n"
    "buffer_load_dwordx4 v224, s[0:3], s28 offen offset:1024 sc1 lds\n"
    "s_add_u32 m0, s33, 16384\n"
    "s_add_u32 s28, s28, 0x4000\n"
    "buffer_load_dwordx4 v224, s[0:3], s28 offen sc1 lds\n"
    "buffer_load_dwordx4 v224, s[0:3], s28 offen offset:1024 sc1 lds\n"
    "s_add_u32 m0, s33, 24576\n"
    "s_add_u32 s28, s28, 0x4000\n"
    "buffer_load_dwordx4 v224, s[0:3], s28 offen sc1 lds\n"
    "buffer_load_dwordx4 v224, s[0:3], s28 offen offset:1024 sc1 lds\n"
    "s_add_u32 m0, s33, 32768\n"
    "s_add_u32 s28, s28, 0x4000\n"
    "buffer_load_dwordx4 v224, s[0:3], s28 offen sc1 lds\n"
    "buffer_load_dwordx4 v224, s[0:3], s28 offen offset:1024 sc1 lds\n"
    "buffer_load_dwordx4 v[48:51], v227, s[0:3], s29 offen offset:0 sc1\n"
    "buffer_load_dwordx4 v[52:55], v227, s[0:3], s29 offen offset:1024 sc1\n"
    "s_waitcnt vmcnt(10)\n"
    "s_barrier\n"
    ".Lb2_mk1x13_%=:\n"
    "ds_read_b128 v[16:19], v224 offset:0\n"
    "ds_read_b128 v[20:23], v224 offset:1024\n"
    "ds_read_b128 v[24:27], v224 offset:2048\n"
    "ds_read_b128 v[28:31], v224 offset:3072\n"
    "ds_read_b128 v[32:35], v224 offset:4096\n"
    "ds_read_b128 v[36:39], v224 offset:5120\n"
    "s_waitcnt lgkmcnt(4)\n"
    "v_mfma_f32_16x16x32_bf16 v[0:3], a[0:3], v[16:19], 0\n"
    "ds_read_b128 v[40:43], v224 offset:6144\n"
    "ds_read_b128 v[44:47], v224 offset:7168\n"
    "v_mfma_f32_16x16x32_bf16 v[8:11], a[0:3], v[20:23], 0\n"
    "v_mfma_f32_16x16x32_bf16 v[4:7], a[80:83], v[16:19], 0\n"
    "v_mfma_f32_16x16x32_bf16 v[12:15], a[80:83], v[20:23], 0\n"
    "s_waitcnt lgkmcnt(4)\n"
    "v_mfma_f32_16x16x32_bf16 v[0:3], a[4:7], v[24:27], v[0:3]\n"
    "s_waitcnt vmcnt(8)\n"
    "s_barrier\n"
    "ds_read_b128 v[16:19], v224 offset:8192\n"
    "v_mfma_f32_16x16x32_bf16 v[8:11], a[4:7], v[28:31], v[8:11]\n"
    "ds_read_b128 v[20:23], v224 offset:9216\n"
    "v_mfma_f32_16x16x32_bf16 v[4:7], a[84:87], v[24:27], v[4:7]\n"
    "v_mfma_f32_16x16x32_bf16 v[12:15], a[84:87], v[28:31], v[12:15]\n"
    "s_waitcnt lgkmcnt(4)\n"
    "v_mfma_f32_16x16x32_bf16 v[0:3], a[8:11], v[32:35], v[0:3]\n"
    "ds_read_b128 v[24:27], v224 offset:10240\n"
    "ds_read_b128 v[28:31], v224 offset:11264\n"
    "v_mfma_f32_16x16x32_bf16 v[8:11], a[8:11], v[36:39], v[8:11]\n"
    "v_mfma_f32_16x16x32_bf16 v[4:7], a[88:91], v[32:35], v[4:7]\n"
    "v_mfma_f32_16x16x32_bf16 v[12:15], a[88:91], v[36:39], v[12:15]\n"
    "s_waitcnt lgkmcnt(4)\n"
    "v_mfma_f32_16x16x32_bf16 v[0:3], a[12:15], v[40:43], v[0:3]\n"
    "ds_read_b128 v[32:35], v224 offset:12288\n"
    "ds_read_b128 v[36:39], v224 offset:13312\n"
    "v_mfma_f32_16x16x32_bf16 v[8:11], a[12:15], v[44:47], v[8:11]\n"
    "v_mfma_f32_16x16x32_bf16 v[4:7], a[92:95], v[40:43], v[4:7]\n"
    "v_mfma_f32_16x16x32_bf16 v[12:15], a[92:95], v[44:47], v[12:15]\n"
    "s_waitcnt lgkmcnt(4)\n"
    "v_mfma_f32_16x16x32_bf16 v[0:3], a[16:19], v[16:19], v[0:3]\n"
    "ds_read_b128 v[40:43], v224 offset:14336\n"
    "ds_read_b128 v[44:47], v224 offset:15360\n"
    "v_mfma_f32_16x16x32_bf16 v[8:11], a[16:19], v[20:23], v[8:11]\n"
    "v_mfma_f32_16x16x32_bf16 v[4:7], a[96:99], v[16:19], v[4:7]\n"
    "v_mfma_f32_16x16x32_bf16 v[12:15], a[96:99], v[20:23], v[12:15]\n"
    "s_waitcnt lgkmcnt(4)\n"
    "v_mfma_f32_16x16x32_bf16 v[0:3], a[20:23], v[24:27], v[0:3]\n"
    "s_waitcnt vmcnt(6)\n"
    "s_barrier\n"
    "ds_read_b128 v[16:19], v224 offset:16384\n"
    "v_mfma_f32_16x16x32_bf16 v[8:11], a[20:23], v[28:31], v[8:11]\n"
    "ds_read_b128 v[20:23], v224 offset:17408\n"
    "v_mfma_f32_16x16x32_bf16 v[4:7], a[100:103], v[24:27], v[4:7]\n"
    "v_mfma_f32_16x16x32_bf16 v[12:15], a[100:103], v[28:31], v[12:15]\n"
    "s_waitcnt lgkmcnt(4)\n"
    "v_mfma_f32_16x16x32_bf16 v[0:3], a[24:27], v[32:35], v[0:3]\n"
    "ds_read_b128 v[24:27], v224 offset:18432\n"
    "ds_read_b128 v[28:31], v224 offset:19456\n"
    "v_mfma_f32_16x16x32_bf16 v[8:11], a[24:27], v[36:39], v[8:11]\n"
    "v_mfma_f32_16x16x32_bf16 v[4:7], a[104:107], v[32:35], v[4:7]\n"
    "v_mfma_f32_16x16x32_bf16 v[12:15], a[104:107], v[36:39], v[12:15]\n"
    "s_waitcnt lgkmcnt(4)\n"
    "v_mfma_f32_16x16x32_bf16 v[0:3], a[28:31], v[40:43], v[0:3]\n"
    "ds_read_b128 v[32:35], v224 offset:20480\n"
    "ds_read_b128 v[36:39], v224 offset:21504\n"
    "v_mfma_f32_16x16x32_bf16 v[8:11], a[28:31], v[44:47], v[8:11]\n"
    "v_mfma_f32_16x16x32_bf16 v[4:7], a[108:111], v[40:43], v[4:7]\n"
    "v_mfma_f32_16x16x32_bf16 v[12:15], a[108:111], v[44:47], v[12:15]\n"
    "s_waitcnt lgkmcnt(4)\n"
    "v_mfma_f32_16x16x32_bf16 v[0:3], a[32:35], v[16:19], v[0:3]\n"
    "ds_read_b128 v[40:43], v224 offset:22528\n"
    "ds_read_b128 v[44:47], v224 offset:23552\n"
    "v_mfma_f32_16x16x32_bf16 v[8:11], a[32:35], v[20:23], v[8:11]\n"
    "v_mfma_f32_16x16x32_bf16 v[4:7], a[112:115], v[16:19], v[4:7]\n"
    "v_mfma_f32_16x16x32_bf16 v[12:15], a[112:115], v[20:23], v[12:15]\n"
    "s_waitcnt lgkmcnt(4)\n"
    "v_mfma_f32_16x16x32_bf16 v[0:3], a[36:39], v[24:27], v[0:3]\n"
    "s_waitcnt vmcnt(4)\n"
    "s_barrier\n"
    "ds_read_b128 v[16:19], v224 offset:24576\n"
    "v_mfma_f32_16x16x32_bf16 v[8:11], a[36:39], v[28:31], v[8:11]\n"
    "ds_read_b128 v[20:23], v224 offset:25600\n"
    "v_mfma_f32_16x16x32_bf16 v[4:7], a[116:119], v[24:27], v[4:7]\n"
    "v_mfma_f32_16x16x32_bf16 v[12:15], a[116:119], v[28:31], v[12:15]\n"
    "s_waitcnt lgkmcnt(4)\n"
    "v_mfma_f32_16x16x32_bf16 v[0:3], a[40:43], v[32:35], v[0:3]\n"
    "ds_read_b128 v[24:27], v224 offset:26624\n"
    "ds_read_b128 v[28:31], v224 offset:27648\n"
    "v_mfma_f32_16x16x32_bf16 v[8:11], a[40:43], v[36:39], v[8:11]\n"
    "v_mfma_f32_16x16x32_bf16 v[4:7], a[120:123], v[32:35], v[4:7]\n"
    "v_mfma_f32_16x16x32_bf16 v[12:15], a[120:123], v[36:39], v[12:15]\n"
    "s_waitcnt lgkmcnt(4)\n"
    "v_mfma_f32_16x16x32_bf16 v[0:3], a[44:47], v[40:43], v[0:3]\n"
    "ds_read_b128 v[32:35], v224 offset:28672\n"
    "ds_read_b128 v[36:39], v224 offset:29696\n"
    "v_mfma_f32_16x16x32_bf16 v[8:11], a[44:47], v[44:47], v[8:11]\n"
    "v_mfma_f32_16x16x32_bf16 v[4:7], a[124:127], v[40:43], v[4:7]\n"
    "v_mfma_f32_16x16x32_bf16 v[12:15], a[124:127], v[44:47], v[12:15]\n"
    "s_waitcnt lgkmcnt(4)\n"
    "v_mfma_f32_16x16x32_bf16 v[0:3], a[48:51], v[16:19], v[0:3]\n"
    "ds_read_b128 v[40:43], v224 offset:30720\n"
    "ds_read_b128 v[44:47], v224 offset:31744\n"
    "v_mfma_f32_16x16x32_bf16 v[8:11], a[48:51], v[20:23], v[8:11]\n"
    "v_mfma_f32_16x16x32_bf16 v[4:7], a[128:131], v[16:19], v[4:7]\n"
    "v_mfma_f32_16x16x32_bf16 v[12:15], a[128:131], v[20:23], v[12:15]\n"
    "s_waitcnt lgkmcnt(4)\n"
    "v_mfma_f32_16x16x32_bf16 v[0:3], a[52:55], v[24:27], v[0:3]\n"
    "s_waitcnt vmcnt(2)\n"
    "s_barrier\n"
    "ds_read_b128 v[16:19], v224 offset:32768\n"
    "v_mfma_f32_16x16x32_bf16 v[8:11], a[52:55], v[28:31], v[8:11]\n"
    "ds_read_b128 v[20:23], v224 offset:33792\n"
    "v_mfma_f32_16x16x32_bf16 v[4:7], a[132:135], v[24:27], v[4:7]\n"
    "v_mfma_f32_16x16x32_bf16 v[12:15], a[132:135], v[28:31], v[12:15]\n"
    "s_waitcnt lgkmcnt(4)\n"
    "v_mfma_f32_16x16x32_bf16 v[0:3], a[56:59], v[32:35], v[0:3]\n"
    "ds_read_b128 v[24:27], v224 offset:34816\n"
    "ds_read_b128 v[28:31], v224 offset:35840\n"
    "v_mfma_f32_16x16x32_bf16 v[8:11], a[56:59], v[36:39], v[8:11]\n"
    "v_mfma_f32_16x16x32_bf16 v[4:7], a[136:139], v[32:35], v[4:7]\n"
    "s_waitcnt vmcnt(0)\n"
    "v_lshlrev_b32 v96, 16, v48\n"
    "v_and_b32 v97, 0xffff0000, v48\n"
    "v_mfma_f32_16x16x32_bf16 v[12:15], a[136:139], v[36:39], v[12:15]\n"
    "s_waitcnt lgkmcnt(4)\n"
    "v_lshlrev_b32 v98, 16, v49\n"
    "v_and_b32 v99, 0xffff0000, v49\n"
    "v_mfma_f32_16x16x32_bf16 v[0:3], a[60:63], v[40:43], v[0:3]\n"
    "s_waitcnt lgkmcnt(2)\n"
    "ds_read_b128 v[32:35], v224 offset:36864\n"
    "ds_read_b128 v[36:39], v224 offset:37888\n"
    "v_mfma_f32_16x16x32_bf16 v[8:11], a[60:63], v[44:47], v[8:11]\n"
    "v_swap_b32 v18, v20\n"
    "v_mfma_f32_16x16x32_bf16 v[4:7], a[140:143], v[40:43], v[4:7]\n"
    "v_swap_b32 v19, v21\n"
    "v_mfma_f32_16x16x32_bf16 v[12:15], a[140:143], v[44:47], v[12:15]\n"
    "v_lshlrev_b32 v100, 16, v50\n"
    "v_and_b32 v101, 0xffff0000, v50\n"
    "v_mfma_f32_16x16x32_bf16 v[0:3], a[64:67], v[16:19], v[0:3]\n"
    "s_waitcnt lgkmcnt(2)\n"
    "ds_read_b128 v[40:43], v224 offset:38912\n"
    "ds_read_b128 v[44:47], v224 offset:39936\n"
    "v_mfma_f32_16x16x32_bf16 v[8:11], a[64:67], v[20:23], v[8:11]\n"
    "v_swap_b32 v26, v28\n"
    "v_mfma_f32_16x16x32_bf16 v[4:7], a[144:147], v[16:19], v[4:7]\n"
    "v_swap_b32 v27, v29\n"
    "v_mfma_f32_16x16x32_bf16 v[12:15], a[144:147], v[20:23], v[12:15]\n"
    "v_lshlrev_b32 v102, 16, v51\n"
    "v_and_b32 v103, 0xffff0000, v51\n"
    "v_mfma_f32_16x16x32_bf16 v[0:3], a[68:71], v[24:27], v[0:3]\n"
    "s_waitcnt lgkmcnt(2)\n"
    "v_lshlrev_b32 v104, 16, v52\n"
    "v_and_b32 v105, 0xffff0000, v52\n"
    "v_mfma_f32_16x16x32_bf16 v[8:11], a[68:71], v[28:31], v[8:11]\n"
    "v_swap_b32 v34, v36\n"
    "v_mfma_f32_16x16x32_bf16 v[4:7], a[148:151], v[24:27], v[4:7]\n"
    "v_swap_b32 v35, v37\n"
    "v_mfma_f32_16x16x32_bf16 v[12:15], a[148:151], v[28:31], v[12:15]\n"
    "v_lshlrev_b32 v106, 16, v53\n"
    "v_and_b32 v107, 0xffff0000, v53\n"
    "v_mfma_f32_16x16x32_bf16 v[0:3], a[72:75], v[32:35], v[0:3]\n"
    "s_waitcnt lgkmcnt(0)\n"
    "v_lshlrev_b32 v108, 16, v54\n"
    "v_and_b32 v109, 0xffff0000, v54\n"
    "v_mfma_f32_16x16x32_bf16 v[8:11], a[72:75], v[36:39], v[8:11]\n"
    "v_swap_b32 v42, v44\n"
    "v_mfma_f32_16x16x32_bf16 v[4:7], a[152:155], v[32:35], v[4:7]\n"
    "v_swap_b32 v43, v45\n"
    "v_mfma_f32_16x16x32_bf16 v[12:15], a[152:155], v[36:39], v[12:15]\n"
    "v_lshlrev_b32 v110, 16, v55\n"
    "v_and_b32 v111, 0xffff0000, v55\n"
    "v_mfma_f32_16x16x32_bf16 v[0:3], a[76:79], v[40:43], v[0:3]\n"
    "v_mfma_f32_16x16x32_bf16 v[8:11], a[76:79], v[44:47], v[8:11]\n"
    "v_mfma_f32_16x16x32_bf16 v[4:7], a[156:159], v[40:43], v[4:7]\n"
    "v_mfma_f32_16x16x32_bf16 v[12:15], a[156:159], v[44:47], v[12:15]\n"
    ".Lb2_mk2x14_%=:\n"
    "v_pk_add_f32 v[0:1], v[0:1], v[96:97]\n"
    "v_pk_add_f32 v[2:3], v[2:3], v[98:99]\n"
    "v_pk_add_f32 v[8:9], v[8:9], v[104:105]\n"
    "v_pk_add_f32 v[10:11], v[10:11], v[106:107]\n"
    "s_nop 0\n"
    "v_pk_add_f32 v[4:5], v[4:5], v[100:101]\n"
    "v_pk_add_f32 v[6:7], v[6:7], v[102:103]\n"
    "s_nop 0\n"
    "v_pk_add_f32 v[12:13], v[12:13], v[108:109]\n"
    "v_pk_add_f32 v[14:15], v[14:15], v[110:111]\n"
    "s_cmp_lg_u32 s13, 0\n"
    "s_cbranch_scc1 .Lb2_out_%=\n"
    "v_pk_add_f32 v[64:65], v[0:1], v[2:3]\n"
    "v_pk_add_f32 v[68:69], v[8:9], v[10:11]\n"
    "v_pk_mul_f32 v[66:67], v[0:1], v[0:1]\n"
    "v_pk_mul_f32 v[70:71], v[8:9], v[8:9]\n"
    "v_pk_add_f32 v[96:97], v[4:5], v[6:7]\n"
    "v_pk_add_f32 v[98:99], v[12:13], v[14:15]\n"
    "v_pk_fma_f32 v[66:67], v[2:3], v[2:3], v[66:67]\n"
    "v_pk_fma_f32 v[70:71], v[10:11], v[10:11], v[70:71]\n"
    "v_pk_add_f32 v[64:65], v[64:65], v[96:97]\n"
    "v_pk_add_f32 v[68:69], v[68:69], v[98:99]\n"
    "v_pk_fma_f32 v[66:67], v[4:5], v[4:5], v[66:67]\n"
    "v_pk_fma_f32 v[70:71], v[12:13], v[12:13], v[70:71]\n"
    "v_add_f32 v64, v64, v65\n"
    "v_add_f32 v68, v68, v69\n"
    "v_pk_fma_f32 v[66:67], v[6:7], v[6:7], v[66:67]\n"
    "v_pk_fma_f32 v[70:71], v[14:15], v[14:15], v[70:71]\n"
    "v_add_f32 v66, v66, v67\n"
    "v_add_f32 v70, v70, v71\n"
    "s_nop 0\n"
    "v_permlane16_swap_b32 v64, v66\n"
    "v_permlane16_swap_b32 v68, v70\n"
    "v_add_f32 v64, v64, v66\n"
    "v_add_f32 v68, v68, v70\n"
    "s_nop 1\n"
    "v_permlane32_swap_b32 v64, v68\n"
    "v_add_f32 v64, v64, v68\n"
    "v_min_u32 v64, -2, v64\n"
    "v_mov_b32 v65, v64\n"
    "v_cvt_pk_bf16_f32 v56, v0, v1\n"
    "v_cvt_pk_bf16_f32 v57, v2, v3\n"
    "v_permlane16_swap_b32 v64, v65\n"
    "v_mov_b32 v66, v64\n"
    "v_mov_b32 v67, v65\n"
    "v_cvt_pk_bf16_f32 v58, v4, v5\n"
    "v_cvt_pk_bf16_f32 v59, v6, v7\n"
    "v_permlane32_swap_b32 v64, v66\n"
    "v_permlane32_swap_b32 v65, v67\n"
    "buffer_store_dwordx4 v[64:67], v230, s[0:3], s32 offen sc1\n"
    "v_cvt_pk_bf16_f32 v60, v8, v9\n"
    "v_cvt_pk_bf16_f32 v61, v10, v11\n"
    "v_cvt_pk_bf16_f32 v62, v12, v13\n"
    "v_cvt_pk_bf16_f32 v63, v14, v15\n"
    ".Lb2_mk3x15_%=:\n"
    "v_lshlrev_b32 v0, 16, v56\n"
    "v_and_b32 v1, 0xffff0000, v56\n"
    "v_lshlrev_b32 v2, 16, v57\n"
    "v_and_b32 v3, 0xffff0000, v57\n"
    "v_lshlrev_b32 v4, 16, v58\n"
    "v_and_b32 v5, 0xffff0000, v58\n"
    "v_lshlrev_b32 v6, 16, v59\n"
    "v_and_b32 v7, 0xffff0000, v59\n"
    "v_lshlrev_b32 v8, 16, v60\n"
    "v_and_b32 v9, 0xffff0000, v60\n"
    "v_lshlrev_b32 v10, 16, v61\n"
    "v_and_b32 v11, 0xffff0000, v61\n"
    "v_lshlrev_b32 v12, 16, v62\n"
    "v_and_b32 v13, 0xffff0000, v62\n"
    "v_lshlrev_b32 v14, 16, v63\n"
    "v_and_b32 v15, 0xffff0000, v63\n"
    "s_sleep 6\n"
    "buffer_load_dwordx4 v[64:67], v231, s[0:3], s32 offen sc1\n"
    "buffer_load_dwordx4 v[68:71], v232, s[0:3], s32 offen sc1\n"
    "buffer_load_dwordx4 v[72:75], v233, s[0:3], s32 offen sc1\n"
    "buffer_load_dwordx4 v[76:79], v234, s[0:3], s32 offen sc1\n"
    "buffer_load_dwordx4 v[80:83], v235, s[0:3], s32 offen sc1\n"
    "buffer_store_dwordx4 v[56:59], v228, s[8:11], s30 offen sc1\n"
    "buffer_store_dwordx4 v[60:63], v229, s[8:11], s30 offen sc1\n"
    ".Lb2_sl_%=:\n"
    "s_waitcnt vmcnt(6)\n"
    "v_max_u32 v84, v64, v64\n"
    "s_waitcnt vmcnt(5)\n"
    "v_max_u32 v84, v68, v84\n"
    "v_pk_add_f32 v[64:65], v[64:65], v[68:69]\n"
    "v_pk_add_f32 v[66:67], v[66:67], v[70:71]\n"
    "s_waitcnt vmcnt(4)\n"
    "v_max_u32 v84, v72, v84\n"
    "v_pk_add_f32 v[64:65], v[64:65], v[72:73]\n"
    "v_pk_add_f32 v[66:67], v[66:67], v[74:75]\n"
    "s_waitcnt vmcnt(3)\n"
    "v_max_u32 v84, v76, v84\n"
    "v_pk_add_f32 v[64:65], v[64:65], v[76:77]\n"
    "v_pk_add_f32 v[66:67], v[66:67], v[78:79]\n"
    "s_waitcnt vmcnt(2)\n"
    "v_max_u32 v84, v80, v84\n"
    "v_pk_add_f32 v[64:65], v[64:65], v[80:81]\n"
    "v_pk_add_f32 v[66:67], v[66:67], v[82:83]\n"
    "v_cmp_eq_u32 vcc, -1, v84\n"
    "s_nop 0\n"
    "s_cbranch_vccnz .Lb2_sr_%=\n"
    ".Lb2_sg_%=:\n"
    ".Lb2_mk4x16_%=:\n"
    "v_permlane16_swap_b32 v64, v65\n"
    "v_permlane16_swap_b32 v66, v67\n"
    "v_add_f32 v64, v64, v65\n"
    "v_add_f32 v66, v66, v67\n"
    "s_nop 1\n"
    "v_permlane32_swap_b32 v64, v66\n"
    "v_add_f32 v64, v64, v66\n"
    "v_mov_b32 v66, v64\n"
    "s_nop 1\n"
    "v_permlane16_swap_b32 v64, v66\n"
    "v_mul_f32 v64, 0x3acccccd, v64\n"
    "v_mul_f32 v66, 0x3acccccd, v66\n"
    "v_fma_f32 v66, -v64, v64, v66\n"
    "v_max_f32 v66, 0, v66\n"
    "v_cvt_pk_bf16_f32 v64, v64, v66\n"
    "v_and_b32 v66, 0xffff0000, v64\n"
    "v_lshlrev_b32 v64, 16, v64\n"
    "v_add_f32 v66, 0x3727c5ac, v66\n"
    "v_mov_b32 v68, v64\n"
    "v_cvt_pk_bf16_f32 v66, v66, v66\n"
    "v_lshlrev_b32 v66, 16, v66\n"
    "v_sqrt_f32 v66, v66\n"
    "v_permlane32_swap_b32 v64, v68\n"
    "v_cvt_pk_bf16_f32 v66, v66, v66\n"
    "v_lshlrev_b32 v66, 16, v66\n"
    "v_rcp_f32 v66, v66\n"
    "s_nop 0\n"
    "v_mov_b32 v70, v66\n"
    "s_nop 1\n"
    "v_permlane32_swap_b32 v66, v70\n"
    "v_mul_f32 v64, v64, v66\n"
    "v_mul_f32 v68, v68, v70\n"
    "v_pk_fma_f32 v[0:1], v[0:1], v[66:67], v[64:65] op_sel_hi:[1,0,0] neg_lo:[0,0,1] neg_hi:[0,0,1]\n"
    "v_pk_fma_f32 v[2:3], v[2:3], v[66:67], v[64:65] op_sel_hi:[1,0,0] neg_lo:[0,0,1] neg_hi:[0,0,1]\n"
    "v_pk_fma_f32 v[4:5], v[4:5], v[66:67], v[64:65] op_sel_hi:[1,0,0] neg_lo:[0,0,1] neg_hi:[0,0,1]\n"
    "v_pk_fma_f32 v[6:7], v[6:7], v[66:67], v[64:65] op_sel_hi:[1,0,0] neg_lo:[0,0,1] neg_hi:[0,0,1]\n"
    "v_cvt_pk_bf16_f32 v96, v0, v1\n"
    "v_cvt_pk_bf16_f32 v98, v2, v3\n"
    "v_cvt_pk_bf16_f32 v97, v4, v5\n"
    "v_cvt_pk_bf16_f32 v99, v6, v7\n"
    "buffer_store_dwordx4 v[96:99], v227, s[0:3], s31 offen offset:0 sc1\n"
    "v_pk_fma_f32 v[8:9], v[8:9], v[70:71], v[68:69] op_sel_hi:[1,0,0] neg_lo:[0,0,1] neg_hi:[0,0,1]\n"
    "v_pk_fma_f32 v[10:11], v[10:11], v[70:71], v[68:69] op_sel_hi:[1,0,0] neg_lo:[0,0,1] neg_hi:[0,0,1]\n"
    "v_pk_fma_f32 v[12:13], v[12:13], v[70:71], v[68:69] op_sel_hi:[1,0,0] neg_lo:[0,0,1] neg_hi:[0,0,1]\n"
    "v_pk_fma_f32 v[14:15], v[14:15], v[70:71], v[68:69] op_sel_hi:[1,0,0] neg_lo:[0,0,1] neg_hi:[0,0,1]\n"
    "v_cvt_pk_bf16_f32 v100, v8, v9\n"
    "v_cvt_pk_bf16_f32 v102, v10, v11\n"
    "v_cvt_pk_bf16_f32 v101, v12, v13\n"
    "v_cvt_pk_bf16_f32 v103, v14, v15\n"
    "buffer_store_dwordx4 v[100:103], v227, s[0:3], s31 offen offset:1024 sc1\n"
    "v_mov_b32 v88, s27\n"
    "v_mov_b32 v89, s27\n"
    "v_mov_b32 v90, s27\n"
    "v_mov_b32 v91, s27\n"
    ".Lb2_mk5x17_%=:\n"
    "v_mov_b32 v84, 0\n"
    "s_memrealtime s[22:23]\n"
    "s_cmp_lg_u32 s12, 0\n"
    "s_waitcnt lgkmcnt(0)\n"
    "s_cbranch_scc1 .Lb2_pl19_%=\n"
    "s_sub_u32 s24, s35, s22\n"
    "s_cmp_gt_i32 s24, 40\n"
    "s_cbranch_scc1 .Lb2_pl19_%=\n"
    "buffer_load_dwordx4 v[84:87], v225, s[0:3], s15 offen sc1\n"
    ".Lb2_pl19_%=:\n"
    "s_waitcnt vmcnt(0)\n"
    "s_barrier\n"
    "buffer_store_dwordx4 v[88:91], v226, s[0:3], 0 offen sc1\n"
    ".Lb2_mk6x19_%=:\n"
    ".Lb2_nx_%=:\n"
    "s_mov_b32 s26, s27\n"
    "s_cmp_lt_u32 s26, 64\n"
    "s_cbranch_scc1 .Lb2_top_%=\n"
    "s_waitcnt vmcnt(0) lgkmcnt(0)\n"
    "s_endpgm\n"
    ".Lb2_tk0_%=:\n"
    ".Lb2_po3_%=:\n"
    "buffer_load_dword v31, v30, s[0:3], 0 offen sc1\n"
    "s_waitcnt vmcnt(0)\n"
    "v_cmp_gt_i32 vcc, 32, v31\n"
    "s_nop 0\n"
    "s_cbranch_vccz .Lb2_pd4_%=\n"
    "s_add_u32 s20, s20, 1\n"
    "s_cmp_lt_u32 s20, 0x40000\n"
    "s_cbranch_scc1 .Lb2_sp5_%=\n"
    "v_mov_b32 v31, 90\n"
    "s_mov_b32 s24, 33728\n"
    "buffer_store_dword v31, off, s[0:3], s24 sc1\n"
    "s_waitcnt vmcnt(0)\n"
    "s_branch .Lb2_pd4_%=\n"
    ".Lb2_sp5_%=:\n"
    "s_sleep 8\n"
    "s_branch .Lb2_po3_%=\n"
    ".Lb2_pd4_%=:\n"
    "s_branch .Lb2_tk1_%=\n"
    ".Lb2_out_%=:\n"
    "v_cvt_pk_bf16_f32 v56, v0, v1\n"
    "v_cvt_pk_bf16_f32 v57, v2, v3\n"
    "v_cvt_pk_bf16_f32 v58, v4, v5\n"
    "v_cvt_pk_bf16_f32 v59, v6, v7\n"
    "v_cvt_pk_bf16_f32 v60, v8, v9\n"
    "v_cvt_pk_bf16_f32 v61, v10, v11\n"
    "v_cvt_pk_bf16_f32 v62, v12, v13\n"
    "v_cvt_pk_bf16_f32 v63, v14, v15\n"
    "buffer_store_dwordx4 v[56:59], v228, s[8:11], s30 offen\n"
    "buffer_store_dwordx4 v[60:63], v229, s[8:11], s30 offen\n"
    "v_mov_b32 v84, 0\n"
    "s_branch .Lb2_nx_%=\n"
    ".Lb2_sr_%=:\n"
    "s_add_u32 s20, s20, 1\n"
    "s_cmp_lt_u32 s20, 0x40000\n"
    "s_cbranch_scc1 .Lb2_sp21_%=\n"
    "v_mov_b32 v84, 72\n"
    "s_mov_b32 s24, 33728\n"
    "buffer_store_dword v84, off, s[0:3], s24 sc1\n"
    "s_waitcnt vmcnt(0)\n"
    "s_branch .Lb2_sg_%=\n"
    ".Lb2_sp21_%=:\n"
    "s_sleep 2\n"
    "buffer_load_dwordx4 v[64:67], v231, s[0:3], s32 offen sc1\n"
    "buffer_load_dwordx4 v[68:71], v232, s[0:3], s32 offen sc1\n"
    "buffer_load_dwordx4 v[72:75], v233, s[0:3], s32 offen sc1\n"
    "buffer_load_dwordx4 v[76:79], v234, s[0:3], s32 offen sc1\n"
    "buffer_load_dwordx4 v[80:83], v235, s[0:3], s32 offen sc1\n"
    "s_waitcnt vmcnt(0)\n"
    "s_branch .Lb2_sl_%=\n"
    :: "{s[0:3]}"(s0_), "{s[4:7]}"(s4_), "{s[8:11]}"(s8_), "{s[12:15]}"(s12_), "{s[16:19]}"(s16_), "{v[0:3]}"(v0_), "{v[4:7]}"(v4_), "{v[224:227]}"(v224_), "{v[228:231]}"(v228_), "{v[232:235]}"(v232_) : "memory", "v255", "a255", "s95");
    __builtin_unreachable();
}
#define SWP 112 - ((k - 24) >> 2) * 16
#define BOX A.ar - 262144
#define NP 4
constexpr int MEGA_LDS = 163840;
__global__ void __launch_bounds__(256, 1) mega_kernel(MegaArgs A) {
    extern __shared__ __attribute__((aligned(16))) char smem[];
    int* sRole = (int*)smem;
    asm volatile("" :: "s"(A.x0), "s"(A.mem0), "s"(A.Wq), "s"(A.Wk), "s"(A.Wv), "s"(A.Wo), "s"(A.wqkv), "s"(A.wo));
    const int x4 = (int)(__builtin_amdgcn_s_getreg((3 << 11) | 20) & 3u);
    const rsrc_t rf = mk_rsrc(arena_base(A.ar), ARENA);
    unsigned* const cnt = (unsigned*)(arena_base(A.ar) + xl(Z_TK, 0));
    u64* const box = (u64*)(BOX);
    const int ln = threadIdx.x & 63;
    static_assert(2 * (Z_END - Z_TK) == 256 * 640, "");
    if (threadIdx.x < 64) {
        bst16(rf, ln < 40 ? 2 * Z_TK + blockIdx.x * 640 + ln * 16 : OOB, u32x4{0u, 0u, 0u, 0u});
        int want;
        if (blockIdx.x == 0) {
            DRAIN();
            u64 sent[4];
            for (int n = 0;; ++n) {
                u64 v[4];
#pragma unroll
                for (int k = 0; k < 4; ++k) v[k] = __hip_atomic_load(box + 8 * (((64 * k + ln) & 31) * 8 + ((64 * k + ln) >> 5)), RLX_AGENT);
                const unsigned c = n < 2 ? 0u : __hip_atomic_load(cnt + 16 * (ln & 7), RLX_AGENT);
                const unsigned open = (unsigned)__ballot(c < 32u) & 255u;
                want = __ffs(open) - 1;
                if ((open & (open - 1)) == 0 && __all(c >= 31u)) break;
#pragma unroll
                for (int k = 0; k < 4; ++k) { if (n == 0 || v[k] != sent[k]) __hip_atomic_store(box + 2048 + 64 * k + ln, v[k], RLX_AGENT); sent[k] = v[k]; }
                if (n > 0) __builtin_amdgcn_s_sleep(16);
            }
        } else {
            const u64 mine = __builtin_amdgcn_s_memrealtime();
            if (threadIdx.x == 0) __hip_atomic_store(box + 8 * blockIdx.x, mine, RLX_AGENT);
            unsigned tm[2] = {~0u, ~0u};
            u64* const in = box + 2048 + (blockIdx.x & 7) * 32 + (blockIdx.x >> 3);
            u64 back;
            const unsigned pr = (blockIdx.x >> 7) * 16384 + (blockIdx.x & 127) * 64;
            if (threadIdx.x < 2) bst4(rf, pr + threadIdx.x * 8192, 0u);
            DRAIN();
#pragma unroll
            for (int k = 0; k < NP; ++k) {
                const unsigned long long t0 = __builtin_amdgcn_s_memtime();
                if (threadIdx.x == 0) bst4(rf, pr + (k & 1) * 8192, 0u);
                if (k < NP - 1) DRAIN();
                else asm volatile("global_load_dwordx2 %0, %1, %2 sc1\n\ts_waitcnt vmcnt(1)" : "=&v"(back) : "v"(0u), "s"(in) : "memory");
                tm[k & 1] = min(tm[k & 1], (unsigned)(__builtin_amdgcn_s_memtime() - t0));
            }
            asm volatile("s_waitcnt vmcnt(0)" : "+v"(back) :: "memory");
            while (back != mine) { __builtin_amdgcn_s_sleep(2); back = __hip_atomic_load(in, RLX_AGENT); }
            want = (tm[1] < tm[0] ? 4 : 0) + x4;
        }
        asm volatile("" ::: "memory");
        if (threadIdx.x == 0) {
            int role = -1;
            for (int p = -1; role < 0; ++p) {
                const int pool = p < 0 ? want : p, x = pool & 3;
                const int k = (int)__hip_atomic_fetch_add(cnt + 16 * pool, 1u, RLX_AGENT);
                if (k < 32) role = (pool >> 2) * 128 + (k < 24 ? (k >> 3) * 32 + 8 * x + (k & 7) : SWP + 4 * x + (k & 3));
            }
            *sRole = role;
        }
    }
    __syncthreads();
    const int role = RFL(*sRole), L = role >> 7, r = role & 127;
    __syncthreads();
    const bool on = (A.layers >> L) & 1;
    if (r < 32) { if (on) qk_role(A, L, r, x4); }
    else if (r < 64) { if (on) vo_role(A, L, r - 32, x4); }
    else if (r < 96) ct_role(A, L, r - 64, smem);
    else if (r < 112) { if (on) { if (r < 107) b1_role(A, L, r - 96, x4); else b2_role(A, L, r - 107, x4); } }
    else qw_role(A, L, 8 * ((r - 112) >> 2) + 4 * L + (r & 3), x4);
}

#define LAYERS 3
extern "C" void kernel_launch(void* const* d_in, const int* in_sizes, int n_in, void* d_out, int out_size, void* d_ws, size_t ws_size, hipStream_t stream) {
    MegaArgs A;
    A.x0 = (const bf16_t*)d_in[0]; A.mem0 = (const bf16_t*)d_in[1]; A.Wq = (const bf16_t*)d_in[2]; A.Wk = (const bf16_t*)d_in[3]; A.Wv = (const bf16_t*)d_in[4];
    A.Wo = (const bf16_t*)d_in[5]; A.wqkv = (const bf16_t*)d_in[8]; A.wo = (const bf16_t*)d_in[9];
    A.ar = (char*)d_ws + 266240;
    A.out = (bf16_t*)d_out; A.layers = LAYERS; A.pad = 0;
    (void)hipFuncSetAttribute((const void*)mega_kernel, hipFuncAttributeMaxDynamicSharedMemorySize, MEGA_LDS);
    mega_kernel<<<256, 256, MEGA_LDS, stream>>>(A);
}
```

```cpp
#include <hip/hip_runtime.h>
#include <hip/hip_bf16.h>
#include <stdint.h>

typedef unsigned short bf16_t;
typedef short bf16x8 __attribute__((ext_vector_type(8)));
typedef short bf16x4 __attribute__((ext_vector_type(4)));
typedef float f32x4 __attribute__((ext_vector_type(4)));
typedef unsigned u32x4 __attribute__((ext_vector_type(4)));
typedef unsigned u32x2 __attribute__((ext_vector_type(2)));
typedef unsigned long long u64;
typedef __bf16 bf16v2 __attribute__((ext_vector_type(2)));
typedef float f32v2 __attribute__((ext_vector_type(2)));
#define DEV __device__ __forceinline__
DEV unsigned pk2(float a, float b) { f32v2 f = {a, b}; bf16v2 r = __builtin_convertvector(f, bf16v2); return __builtin_bit_cast(unsigned, r); }
DEV float rb(float a) { return __uint_as_float((pk2(a, 0.f) & 0xffffu) << 16); }
DEV float lo16(unsigned u) { return __uint_as_float(u << 16); }
DEV float hi16(unsigned u) { return __uint_as_float(u & 0xffff0000u); }
constexpr int D_ = 640;
#define RLX_AGENT __ATOMIC_RELAXED, __HIP_MEMORY_SCOPE_AGENT
#define AUX_SC1 16
typedef __amdgpu_buffer_rsrc_t rsrc_t;
DEV rsrc_t mk_rsrc(const void* p, unsigned bytes) { return __builtin_amdgcn_make_buffer_rsrc((void*)p, 0, (int)bytes, 0x00020000); }
DEV void bst16(rsrc_t r, unsigned off, u32x4 v) { __builtin_amdgcn_raw_buffer_store_b128(v, r, off, 0, AUX_SC1); }
DEV void bst8(rsrc_t r, unsigned off, u32x2 v) { __builtin_amdgcn_raw_buffer_store_b64(v, r, off, 0, AUX_SC1); }
DEV void bst4(rsrc_t r, unsigned off, unsigned v) { __builtin_amdgcn_raw_buffer_store_b32(v, r, off, 0, AUX_SC1); }
#define DRAIN() asm volatile("s_waitcnt vmcnt(0)" ::: "memory")
#define DRAIN_BARRIER() asm volatile("s_waitcnt vmcnt(0) lgkmcnt(0)\n\ts_barrier" ::: "memory")
#define RFL(x) ((unsigned)__builtin_amdgcn_readfirstlane((int)(x)))
DEV unsigned lo(const void* p) { return (unsigned)(size_t)p; }
DEV unsigned hi(const void* p) { return (unsigned)((size_t)p >> 32) & 0xffffu; }
DEV unsigned nosent(unsigned x) { return min(x, 0xfffffffeu); }
DEV int sw2(int x) { return ((x & 1) << 1) | (x >> 1); }

DEV unsigned xl(unsigned o, int side) { const unsigned ph = ((o >> 13) << 14) | (o & 8191u); return ph | ((((ph >> 21) ^ side) & 1u) << 13); }
constexpr unsigned Z_TK = 16384, Z_QV = Z_TK + 1024, Z_CT = Z_QV + 1024, Z_QW = Z_CT + 2048, Z_X = 3 * 8192, Z_X1 = 7 * 8192, Z_XN = 9 * 8192, Z_O = 10 * 8192, Z_ST = 11 * 8192, Z_END = 12 * 8192;
constexpr unsigned RG_UT = 1u << 21, RG_UTX = RG_UT + (1u << 20), RG_UM = RG_UTX + 262144, RG_UMX = RG_UM + 262144, RG_PT = RG_UMX + 65536, RG_PM = RG_PT + 65536, RG_MEM = RG_PM + 32768;
constexpr unsigned SLOT = 1u << 22;
constexpr unsigned REC0 = 20u << 20, RC_X1 = 5 * 16384, RC_O = 10 * 16384, RC_X2 = 15 * 16384, ST0 = 1u << 20, QT0 = 52u << 20, DBG0 = 116u << 20, ARENA = 120u << 20;
DEV unsigned rec(int s, int L) { return REC0 + (s << 19) + ((((s >> 2) ^ L) & 1) << 13); }
constexpr unsigned OOB = 0x7f000000u, WBYTES = 640u * 6144u * 2u;
DEV char* arena_base(char* p) { return p + ((0 - (size_t)p) & 0x3fffff); }

struct MegaArgs {
    const bf16_t* x0; const bf16_t* mem0; const bf16_t* Wq; const bf16_t* Wk; const bf16_t* Wv; const bf16_t* Wo; const bf16_t* wqkv; const bf16_t* wo;
    char* ar; bf16_t* out; int layers, pad;
};
DEV bool all_drawn(char* ar) { return __all(__hip_atomic_load((unsigned*)(arena_base(ar) + xl(Z_TK, 0)) + 16 * (threadIdx.x & 7), RLX_AGENT) >= 32u); }
DEV void wait_drawn(char* ar) { while (!all_drawn(ar)) __builtin_amdgcn_s_sleep(8); asm volatile("" ::: "memory"); }
#define ROLE_HEAD const int l = threadIdx.x & 63, l16 = l & 15, lq = l >> 4, w = RFL(threadIdx.x >> 6); char* const ab = arena_base(A.ar); \
    const rsrc_t rr = mk_rsrc(ab, ARENA); const u32x4 rx = {lo(ab), hi(ab), ARENA, 0x20000u}; const u32x4 ones = {~0u, ~0u, ~0u, ~0u}; const u32x2 ones2 = {~0u, ~0u}
DEV void qk_role(const MegaArgs& A, const int L, const int h, const int rep) {
    ROLE_HEAD;
#define XP 10
    const int i = 4 * (w & 1) + lq, p = l16 < 8 ? l16 >> 2 : l16 & 1, rho = 4 * (i >> 1) + 2 * p + (i & 1), tp = 4 * w + lq;
    const unsigned pm = (w >= 2 || l16 >= XP) ? OOB : xl(RG_PM + (h >> 3) * 8192 +
        (l16 < 8 ? ((h & 7) >> 1) * 1024 + ((2 * (h & 1) + ((l16 >> 1) & 1)) * 16 + rho) * 16 + (l16 & 1) * 8 : 4096 + (((h & 7) >> 1) * 16 + rho) * 16 + (h & 1) * 8), L);
    const unsigned pt = xl(RG_PT + h * 2048 + tp * 16 + (l16 < 8 ? (l16 & 3) * 256 + (l16 >> 2) * 8 : (l16 & 1) * 8 + (l16 < 10 ? 1792 : (l16 < 14 ? 1024 + ((l16 - 10) >> 1) * 256 : 1536))), L);
    u64 cm[4];
    const int k0 = (l16 >= 4 && l16 < 8) ? 4 * (l16 - 4) : ((l16 >= 10 && l16 < 14) ? 4 * (l16 - 10) : -4);
#pragma unroll
    for (int r = 0; r < 4; ++r) cm[r] = __ballot(k0 + r <= tp);
    const u32x4 s0_ = {RFL(rx[0]), RFL(rx[1]), RFL(rx[2]), RFL(rx[3])};
    const u32x4 s4_ = {RFL(lo(A.Wk + h * 192)), RFL(hi(A.Wk + h * 192)), RFL(WBYTES - h * 384), RFL(0x20000u)};
    const u32x4 s8_ = {RFL(lo(A.Wq + h * 192)), RFL(hi(A.Wq + h * 192)), RFL(WBYTES - h * 384), RFL(0x20000u)};
    const u32x4 s12_ = {RFL((unsigned)cm[0]), RFL((unsigned)(cm[0] >> 32)), RFL((unsigned)cm[1]), RFL((unsigned)(cm[1] >> 32))};
    const u32x4 s16_ = {RFL((unsigned)cm[2]), RFL((unsigned)(cm[2] >> 32)), RFL((unsigned)cm[3]), RFL((unsigned)(cm[3] >> 32))};
    const u32x4 s20_ = {RFL(w < 2 ? ~0u : 1u), RFL(w < 2 ? ~0u : 0u), RFL(w), RFL(L << 13)};
    const u32x4 s24_ = {RFL(xl(RG_MEM + rep * 16384, L) + w * 2048), RFL(w < 2 ? xl(RG_MEM + rep * 16384 + 8192, L) + w * 1024 - 2048 : OOB), RFL(w * 11264 + 8192), RFL(REC0 + w * 16384)};
    const u32x4 s28_ = {RFL(65536 - 4096 - w * 14336), RFL(QT0 + h * 32768 + w * 2048), RFL(h == 0 && w == 0 ? xl(Z_X, L) : OOB), RFL(xl(Z_X, L))};
    const u32x4 s32_ = {RFL(xl(Z_CT + (h & 7) * 256, L)), RFL(xl(Z_XN + rep * 2048, 0)), RFL(xl(Z_QW + h * 128, L)), RFL(w == 0 ? xl(Z_QV + h * 4, L) : OOB)};
    const u32x4 s36_ = {RFL(DBG0 + ((L * 128 + h) * 4 + w) * 2048), RFL(0u), RFL(0u), RFL(0u)};
    const u32x4 v0_ = {(unsigned)(((3 * w + 0) * 64 + l) / 24 * 12288 + ((((3 * w + 0) * 64 + l) % 24) ^ ((((3 * w + 0) * 64 + l) / 48) & 6)) * 16 - 0), (unsigned)(((3 * w + 1) * 64 + l) / 24 * 12288 + ((((3 * w + 1) * 64 + l) % 24) ^ ((((3 * w + 1) * 64 + l) / 48) & 6)) * 16 - 1024), (unsigned)(((3 * w + 2) * 64 + l) / 24 * 12288 + ((((3 * w + 2) * 64 + l) % 24) ^ ((((3 * w + 2) * 64 + l) / 48) & 6)) * 16 - 2048), (unsigned)((8 * lq + ((l >> 2) & 1) + ((l >> 1) & 4)) * 384 + (w ^ (((8 * lq + ((l >> 2) & 1) + ((l >> 1) & 4)) >> 2) & 3)) * 32 + (l & 3) * 8 + ((w + 0) & 3) * 12288)};
    const u32x4 v4_ = {(unsigned)((8 * lq + ((l >> 2) & 1) + ((l >> 1) & 4)) * 384 + (w ^ (((8 * lq + ((l >> 2) & 1) + ((l >> 1) & 4)) >> 2) & 3)) * 32 + (l & 3) * 8 + ((w + 1) & 3) * 12288), (unsigned)((8 * lq + ((l >> 2) & 1) + ((l >> 1) & 4)) * 384 + (w ^ (((8 * lq + ((l >> 2) & 1) + ((l >> 1) & 4)) >> 2) & 3)) * 32 + (l & 3) * 8 + ((w + 2) & 3) * 12288), (unsigned)((8 * lq + ((l >> 2) & 1) + ((l >> 1) & 4)) * 384 + (w ^ (((8 * lq + ((l >> 2) & 1) + ((l >> 1) & 4)) >> 2) & 3)) * 32 + (l & 3) * 8 + ((w + 3) & 3) * 12288), (unsigned)((8 * lq + ((l >> 2) & 1) + ((l >> 1) & 4)) * 384 + (w ^ (((8 * lq + ((l >> 2) & 1) + ((l >> 1) & 4)) >> 2) & 3)) * 32 + (l & 3) * 8)};
    const u32x4 v92_ = {(unsigned)(l * 16 + ((w + 0) & 3) * 11264), (unsigned)(l * 16 + ((w + 1) & 3) * 11264), (unsigned)(l * 16 + ((w + 2) & 3) * 11264), (unsigned)(l * 16 + ((w + 3) & 3) * 11264)};
    const u32x4 v96_ = {(unsigned)(l * 16), (unsigned)(l * 16 + 53248 + w * 20480), (unsigned)((l & 2) ? 0xeeeeu : 0x4444u), (unsigned)(0xff800000u)};
    const u32x4 v100_ = {(unsigned)(l16 == 0 + 4 * lq ? 0x3f80u : (l16 == 0 + 4 * lq + 1 ? 0x3f800000u : 0u)), (unsigned)(l16 == 2 + 4 * lq ? 0x3f80u : (l16 == 2 + 4 * lq + 1 ? 0x3f800000u : 0u)), (unsigned)(135168 + w * 1936 + (2 * (l16 >> 2) + (l & 1)) * 240 + (l & 2) * 32 + lq * 16), (unsigned)(135168 + (l16 < 10 ? (4 * (w & 1) + lq) * 240 + l16 * 16 : 1920))};
    const u32x4 v104_ = {(unsigned)(142912 + w * 4352 + l16 * 272 + lq * 16), (unsigned)(142912 + (4 * w + lq) * 272 + l16 * 16), (unsigned)(pm), (unsigned)(pt)};
    asm volatile(
    "s_mov_b32 s40, 0\n"
    "s_mov_b64 s[42:43], 0\n"
    "s_mov_b32 s45, 0\n"
    "s_cmp_eq_u32 s23, 0\n"
    "s_cselect_b32 s44, 0, 28\n"
    "s_sub_u32 s44, s44, 0\n"
    ".Lqk_ho1_%=:\n"
    "s_sub_u32 s44, s44, 1\n"
    "s_cbranch_scc1 .Lqk_hd2_%=\n"
    "s_sleep 17\n"
    "s_branch .Lqk_ho1_%=\n"
    ".Lqk_hd2_%=:\n"
    "s_mul_i32 s51, s22, 3072\n"
    "s_mov_b32 s60, 0\n"
    "s_add_u32 m0, s51, 0\n"
    "s_nop 0\n"
    "buffer_load_dwordx4 v0, s[8:11], s60 offen offset:0 nt lds\n"
    "buffer_load_dwordx4 v1, s[8:11], s60 offen offset:1024 nt lds\n"
    "buffer_load_dwordx4 v2, s[8:11], s60 offen offset:2048 nt lds\n"
    "s_mov_b32 s60, 1572864\n"
    "s_add_u32 m0, s51, 12288\n"
    "s_nop 0\n"
    "buffer_load_dwordx4 v0, s[8:11], s60 offen offset:0 nt lds\n"
    "buffer_load_dwordx4 v1, s[8:11], s60 offen offset:1024 nt lds\n"
    "buffer_load_dwordx4 v2, s[8:11], s60 offen offset:2048 nt lds\n"
    "s_mov_b32 s60, 3145728\n"
    "s_add_u32 m0, s51, 24576\n"
    "s_nop 0\n"
    "buffer_load_dwordx4 v0, s[8:11], s60 offen offset:0 nt lds\n"
    "buffer_load_dwordx4 v1, s[8:11], s60 offen offset:1024 nt lds\n"
    "buffer_load_dwordx4 v2, s[8:11], s60 offen offset:2048 nt lds\n"
    "s_mov_b32 s60, 4718592\n"
    "s_add_u32 m0, s51, 36864\n"
    "s_nop 0\n"
    "buffer_load_dwordx4 v0, s[8:11], s60 offen offset:0 nt lds\n"
    "buffer_load_dwordx4 v1, s[8:11], s60 offen offset:1024 nt lds\n"
    "buffer_load_dwordx4 v2, s[8:11], s60 offen offset:2048 nt lds\n"
    "s_mov_b32 s60, 393216\n"
    "s_add_u32 m0, s51, 49152\n"
    "s_nop 0\n"
    "buffer_load_dwordx4 v0, s[8:11], s60 offen offset:0 nt lds\n"
    "buffer_load_dwordx4 v1, s[8:11], s60 offen offset:1024 nt lds\n"
    "buffer_load_dwordx4 v2, s[8:11], s60 offen offset:2048 nt lds\n"
    "s_mov_b32 s60, 1966080\n"
    "s_add_u32 m0, s51, 61440\n"
    "s_nop 0\n"
    "buffer_load_dwordx4 v0, s[8:11], s60 offen offset:0 nt lds\n"
    "buffer_load_dwordx4 v1, s[8:11], s60 offen offset:1024 nt lds\n"
    "buffer_load_dwordx4 v2, s[8:11], s60 offen offset:2048 nt lds\n"
    "s_mov_b32 s60, 3538944\n"
    "s_add_u32 m0, s51, 73728\n"
    "s_nop 0\n"
    "buffer_load_dwordx4 v0, s[8:11], s60 offen offset:0 nt lds\n"
    "buffer_load_dwordx4 v1, s[8:11], s60 offen offset:1024 nt lds\n"
    "buffer_load_dwordx4 v2, s[8:11], s60 offen offset:2048 nt lds\n"
    "s_mov_b32 s60, 5111808\n"
    "s_add_u32 m0, s51, 86016\n"
    "s_nop 0\n"
    "buffer_load_dwordx4 v0, s[8:11], s60 offen offset:0 nt lds\n"
    "buffer_load_dwordx4 v1, s[8:11], s60 offen offset:1024 nt lds\n"
    "buffer_load_dwordx4 v2, s[8:11], s60 offen offset:2048 nt lds\n"
    "s_mov_b32 s60, 786432\n"
    "s_add_u32 m0, s51, 98304\n"
    "s_nop 0\n"
    "buffer_load_dwordx4 v0, s[8:11], s60 offen offset:0 nt lds\n"
    "buffer_load_dwordx4 v1, s[8:11], s60 offen offset:1024 nt lds\n"
    "buffer_load_dwordx4 v2, s[8:11], s60 offen offset:2048 nt lds\n"
    "s_mov_b32 s60, 2359296\n"
    "s_add_u32 m0, s51, 110592\n"
    "s_nop 0\n"
    "buffer_load_dwordx4 v0, s[8:11], s60 offen offset:0 nt lds\n"
    "buffer_load_dwordx4 v1, s[8:11], s60 offen offset:1024 nt lds\n"
    "buffer_load_dwordx4 v2, s[8:11], s60 offen offset:2048 nt lds\n"
    "s_mov_b32 s60, 3932160\n"
    "s_add_u32 m0, s51, 122880\n"
    "s_nop 0\n"
    "buffer_load_dwordx4 v0, s[8:11], s60 offen offset:0 nt lds\n"
    "buffer_load_dwordx4 v1, s[8:11], s60 offen offset:1024 nt lds\n"
    "buffer_load_dwordx4 v2, s[8:11], s60 offen offset:2048 nt lds\n"
    "s_mov_b32 s60, 5505024\n"
    "s_add_u32 m0, s51, 135168\n"
    "s_nop 0\n"
    "buffer_load_dwordx4 v0, s[8:11], s60 offen offset:0 nt lds\n"
    "buffer_load_dwordx4 v1, s[8:11], s60 offen offset:1024 nt lds\n"
    "buffer_load_dwordx4 v2, s[8:11], s60 offen offset:2048 nt lds\n"
    "v_mov_b64 v[108:109], -1\n"
    "buffer_store_dwordx2 v[108:109], v106, s[0:3], 0 offen sc1\n"
    "buffer_store_dwordx2 v[108:109], v107, s[0:3], 0 offen sc1\n"
    "s_waitcnt vmcnt(26)\n"
    "s_barrier\n"
    "v_add_u32 v8, 0, v3\n"
    "ds_read_b64_tr_b16 a[240:241], v8 offset:0\n"
    "ds_read_b64_tr_b16 a[242:243], v8 offset:768\n"
    "ds_read_b64_tr_b16 v[176:177], v8 offset:128\n"
    "ds_read_b64_tr_b16 v[178:179], v8 offset:896\n"
    "ds_read_b64_tr_b16 v[12:13], v8 offset:256\n"
    "ds_read_b64_tr_b16 v[14:15], v8 offset:1024\n"
    "v_add_u32 v9, 0, v4\n"
    "ds_read_b64_tr_b16 v[112:113], v9 offset:0\n"
    "ds_read_b64_tr_b16 v[114:115], v9 offset:768\n"
    "ds_read_b64_tr_b16 v[192:193], v9 offset:128\n"
    "ds_read_b64_tr_b16 v[194:195], v9 offset:896\n"
    "ds_read_b64_tr_b16 v[28:29], v9 offset:256\n"
    "ds_read_b64_tr_b16 v[30:31], v9 offset:1024\n"
    "v_add_u32 v10, 0, v5\n"
    "ds_read_b64_tr_b16 v[128:129], v10 offset:0\n"
    "ds_read_b64_tr_b16 v[130:131], v10 offset:768\n"
    "ds_read_b64_tr_b16 v[208:209], v10 offset:128\n"
    "ds_read_b64_tr_b16 v[210:211], v10 offset:896\n"
    "ds_read_b64_tr_b16 v[44:45], v10 offset:256\n"
    "ds_read_b64_tr_b16 v[46:47], v10 offset:1024\n"
    "v_add_u32 v11, 0, v6\n"
    "ds_read_b64_tr_b16 v[144:145], v11 offset:0\n"
    "ds_read_b64_tr_b16 v[146:147], v11 offset:768\n"
    "ds_read_b64_tr_b16 v[224:225], v11 offset:128\n"
    "ds_read_b64_tr_b16 v[226:227], v11 offset:896\n"
    "ds_read_b64_tr_b16 v[60:61], v11 offset:256\n"
    "ds_read_b64_tr_b16 v[62:63], v11 offset:1024\n"
    "s_waitcnt lgkmcnt(0)\n"
    "s_barrier\n"
    "s_mov_b32 s60, 1179648\n"
    "s_add_u32 m0, s51, 0\n"
    "s_nop 0\n"
    "buffer_load_dwordx4 v0, s[8:11], s60 offen offset:0 nt lds\n"
    "buffer_load_dwordx4 v1, s[8:11], s60 offen offset:1024 nt lds\n"
    "buffer_load_dwordx4 v2, s[8:11], s60 offen offset:2048 nt lds\n"
    "s_mov_b32 s60, 2752512\n"
    "s_add_u32 m0, s51, 12288\n"
    "s_nop 0\n"
    "buffer_load_dwordx4 v0, s[8:11], s60 offen offset:0 nt lds\n"
    "buffer_load_dwordx4 v1, s[8:11], s60 offen offset:1024 nt lds\n"
    "buffer_load_dwordx4 v2, s[8:11], s60 offen offset:2048 nt lds\n"
    "s_mov_b32 s60, 4325376\n"
    "s_add_u32 m0, s51, 24576\n"
    "s_nop 0\n"
    "buffer_load_dwordx4 v0, s[8:11], s60 offen offset:0 nt lds\n"
    "buffer_load_dwordx4 v1, s[8:11], s60 offen offset:1024 nt lds\n"
    "buffer_load_dwordx4 v2, s[8:11], s60 offen offset:2048 nt lds\n"
    "s_mov_b32 s60, 5898240\n"
    "s_add_u32 m0, s51, 36864\n"
    "s_nop 0\n"
    "buffer_load_dwordx4 v0, s[8:11], s60 offen offset:0 nt lds\n"
    "buffer_load_dwordx4 v1, s[8:11], s60 offen offset:1024 nt lds\n"
    "buffer_load_dwordx4 v2, s[8:11], s60 offen offset:2048 nt lds\n"
    "v_and_b32 v108, 0x70, v96\n"
    "v_lshlrev_b32 v108, 2, v108\n"
    "v_or_b32 v108, 0x8000, v108\n"
    "buffer_load_dword v109, v108, s[0:3], 0 offen sc1\n"
    "s_waitcnt vmcnt(27)\n"
    "s_barrier\n"
    "v_add_u32 v8, 49152, v3\n"
    "ds_read_b64_tr_b16 a[244:245], v8 offset:0\n"
    "ds_read_b64_tr_b16 a[246:247], v8 offset:768\n"
    "ds_read_b64_tr_b16 v[180:181], v8 offset:128\n"
    "ds_read_b64_tr_b16 v[182:183], v8 offset:896\n"
    "ds_read_b64_tr_b16 v[16:17], v8 offset:256\n"
    "ds_read_b64_tr_b16 v[18:19], v8 offset:1024\n"
    "v_add_u32 v9, 49152, v4\n"
    "ds_read_b64_tr_b16 v[116:117], v9 offset:0\n"
    "ds_read_b64_tr_b16 v[118:119], v9 offset:768\n"
    "ds_read_b64_tr_b16 v[196:197], v9 offset:128\n"
    "ds_read_b64_tr_b16 v[198:199], v9 offset:896\n"
    "ds_read_b64_tr_b16 v[32:33], v9 offset:256\n"
    "ds_read_b64_tr_b16 v[34:35], v9 offset:1024\n"
    "v_add_u32 v10, 49152, v5\n"
    "ds_read_b64_tr_b16 v[132:133], v10 offset:0\n"
    "ds_read_b64_tr_b16 v[134:135], v10 offset:768\n"
    "ds_read_b64_tr_b16 v[212:213], v10 offset:128\n"
    "ds_read_b64_tr_b16 v[214:215], v10 offset:896\n"
    "ds_read_b64_tr_b16 v[48:49], v10 offset:256\n"
    "ds_read_b64_tr_b16 v[50:51], v10 offset:1024\n"
    "v_add_u32 v11, 49152, v6\n"
    "ds_read_b64_tr_b16 v[148:149], v11 offset:0\n"
    "ds_read_b64_tr_b16 v[150:151], v11 offset:768\n"
    "ds_read_b64_tr_b16 v[228:229], v11 offset:128\n"
    "ds_read_b64_tr_b16 v[230:231], v11 offset:896\n"
    "ds_read_b64_tr_b16 v[64:65], v11 offset:256\n"
    "ds_read_b64_tr_b16 v[66:67], v11 offset:1024\n"
    "s_waitcnt lgkmcnt(0)\n"
    "s_barrier\n"
    "s_mov_b32 s60, 6291456\n"
    "s_add_u32 m0, s51, 49152\n"
    "s_nop 0\n"
    "buffer_load_dwordx4 v0, s[8:11], s60 offen offset:0 nt lds\n"
    "buffer_load_dwordx4 v1, s[8:11], s60 offen offset:1024 nt lds\n"
    "buffer_load_dwordx4 v2, s[8:11], s60 offen offset:2048 nt lds\n"
    "s_mov_b32 s60, 6684672\n"
    "s_add_u32 m0, s51, 61440\n"
    "s_nop 0\n"
    "buffer_load_dwordx4 v0, s[8:11], s60 offen offset:0 nt lds\n"
    "buffer_load_dwordx4 v1, s[8:11], s60 offen offset:1024 nt lds\n"
    "buffer_load_dwordx4 v2, s[8:11], s60 offen offset:2048 nt lds\n"
    "s_mov_b32 s60, 7077888\n"
    "s_add_u32 m0, s51, 73728\n"
    "s_nop 0\n"
    "buffer_load_dwordx4 v0, s[8:11], s60 offen offset:0 nt lds\n"
    "buffer_load_dwordx4 v1, s[8:11], s60 offen offset:1024 nt lds\n"
    "buffer_load_dwordx4 v2, s[8:11], s60 offen offset:2048 nt lds\n"
    "s_mov_b32 s60, 7471104\n"
    "s_add_u32 m0, s51, 86016\n"
    "s_nop 0\n"
    "buffer_load_dwordx4 v0, s[8:11], s60 offen offset:0 nt lds\n"
    "buffer_load_dwordx4 v1, s[8:11], s60 offen offset:1024 nt lds\n"
    "buffer_load_dwordx4 v2, s[8:11], s60 offen offset:2048 nt lds\n"
    "s_waitcnt vmcnt(27)\n"
    "s_barrier\n"
    "v_add_u32 v8, 98304, v3\n"
    "ds_read_b64_tr_b16 a[248:249], v8 offset:0\n"
    "ds_read_b64_tr_b16 a[250:251], v8 offset:768\n"
    "ds_read_b64_tr_b16 v[184:185], v8 offset:128\n"
    "ds_read_b64_tr_b16 v[186:187], v8 offset:896\n"
    "ds_read_b64_tr_b16 v[20:21], v8 offset:256\n"
    "ds_read_b64_tr_b16 v[22:23], v8 offset:1024\n"
    "v_add_u32 v9, 98304, v4\n"
    "ds_read_b64_tr_b16 v[120:121], v9 offset:0\n"
    "ds_read_b64_tr_b16 v[122:123], v9 offset:768\n"
    "ds_read_b64_tr_b16 v[200:201], v9 offset:128\n"
    "ds_read_b64_tr_b16 v[202:203], v9 offset:896\n"
    "ds_read_b64_tr_b16 v[36:37], v9 offset:256\n"
    "ds_read_b64_tr_b16 v[38:39], v9 offset:1024\n"
    "v_add_u32 v10, 98304, v5\n"
    "ds_read_b64_tr_b16 v[136:137], v10 offset:0\n"
    "ds_read_b64_tr_b16 v[138:139], v10 offset:768\n"
    "ds_read_b64_tr_b16 v[216:217], v10 offset:128\n"
    "ds_read_b64_tr_b16 v[218:219], v10 offset:896\n"
    "ds_read_b64_tr_b16 v[52:53], v10 offset:256\n"
    "ds_read_b64_tr_b16 v[54:55], v10 offset:1024\n"
    "v_add_u32 v11, 98304, v6\n"
    "ds_read_b64_tr_b16 v[152:153], v11 offset:0\n"
    "ds_read_b64_tr_b16 v[154:155], v11 offset:768\n"
    "ds_read_b64_tr_b16 v[232:233], v11 offset:128\n"
    "ds_read_b64_tr_b16 v[234:235], v11 offset:896\n"
    "ds_read_b64_tr_b16 v[68:69], v11 offset:256\n"
    "ds_read_b64_tr_b16 v[70:71], v11 offset:1024\n"
    "s_waitcnt lgkmcnt(0)\n"
    "s_barrier\n"
    "s_mov_b32 s60, 0\n"
    "s_add_u32 m0, s51, 98304\n"
    "s_nop 0\n"
    "buffer_load_dwordx4 v0, s[4:7], s60 offen offset:0 nt lds\n"
    "buffer_load_dwordx4 v1, s[4:7], s60 offen offset:1024 nt lds\n"
    "buffer_load_dwordx4 v2, s[4:7], s60 offen offset:2048 nt lds\n"
    "s_mov_b32 s60, 1572864\n"
    "s_add_u32 m0, s51, 110592\n"
    "s_nop 0\n"
    "buffer_load_dwordx4 v0, s[4:7], s60 offen offset:0 nt lds\n"
    "buffer_load_dwordx4 v1, s[4:7], s60 offen offset:1024 nt lds\n"
    "buffer_load_dwordx4 v2, s[4:7], s60 offen offset:2048 nt lds\n"
    "s_mov_b32 s60, 3145728\n"
    "s_add_u32 m0, s51, 122880\n"
    "s_nop 0\n"
    "buffer_load_dwordx4 v0, s[4:7], s60 offen offset:0 nt lds\n"
    "buffer_load_dwordx4 v1, s[4:7], s60 offen offset:1024 nt lds\n"
    "buffer_load_dwordx4 v2, s[4:7], s60 offen offset:2048 nt lds\n"
    "s_mov_b32 s60, 4718592\n"
    "s_add_u32 m0, s51, 135168\n"
    "s_nop 0\n"
    "buffer_load_dwordx4 v0, s[4:7], s60 offen offset:0 nt lds\n"
    "buffer_load_dwordx4 v1, s[4:7], s60 offen offset:1024 nt lds\n"
    "buffer_load_dwordx4 v2, s[4:7], s60 offen offset:2048 nt lds\n"
    "s_waitcnt vmcnt(25)\n"
    "s_barrier\n"
    "v_add_u32 v8, 0, v3\n"
    "ds_read_b64_tr_b16 a[252:253], v8 offset:0\n"
    "ds_read_b64_tr_b16 a[254:255], v8 offset:768\n"
    "ds_read_b64_tr_b16 v[188:189], v8 offset:128\n"
    "ds_read_b64_tr_b16 v[190:191], v8 offset:896\n"
    "ds_read_b64_tr_b16 v[24:25], v8 offset:256\n"
    "ds_read_b64_tr_b16 v[26:27], v8 offset:1024\n"
    "v_add_u32 v9, 0, v4\n"
    "ds_read_b64_tr_b16 v[124:125], v9 offset:0\n"
    "ds_read_b64_tr_b16 v[126:127], v9 offset:768\n"
    "ds_read_b64_tr_b16 v[204:205], v9 offset:128\n"
    "ds_read_b64_tr_b16 v[206:207], v9 offset:896\n"
    "ds_read_b64_tr_b16 v[40:41], v9 offset:256\n"
    "ds_read_b64_tr_b16 v[42:43], v9 offset:1024\n"
    "v_add_u32 v10, 0, v5\n"
    "ds_read_b64_tr_b16 v[140:141], v10 offset:0\n"
    "ds_read_b64_tr_b16 v[142:143], v10 offset:768\n"
    "ds_read_b64_tr_b16 v[220:221], v10 offset:128\n"
    "ds_read_b64_tr_b16 v[222:223], v10 offset:896\n"
    "ds_read_b64_tr_b16 v[56:57], v10 offset:256\n"
    "ds_read_b64_tr_b16 v[58:59], v10 offset:1024\n"
    "v_add_u32 v11, 0, v6\n"
    "ds_read_b64_tr_b16 v[156:157], v11 offset:0\n"
    "ds_read_b64_tr_b16 v[158:159], v11 offset:768\n"
    "ds_read_b64_tr_b16 v[236:237], v11 offset:128\n"
    "ds_read_b64_tr_b16 v[238:239], v11 offset:896\n"
    "ds_read_b64_tr_b16 v[72:73], v11 offset:256\n"
    "ds_read_b64_tr_b16 v[74:75], v11 offset:1024\n"
    "s_waitcnt lgkmcnt(0)\n"
    "s_barrier\n"
    "s_mov_b32 s60, 393216\n"
    "s_add_u32 m0, s51, 0\n"
    "s_nop 0\n"
    "buffer_load_dwordx4 v0, s[4:7], s60 offen offset:0 nt lds\n"
    "buffer_load_dwordx4 v1, s[4:7], s60 offen offset:1024 nt lds\n"
    "buffer_load_dwordx4 v2, s[4:7], s60 offen offset:2048 nt lds\n"
    "s_mov_b32 s60, 1966080\n"
    "s_add_u32 m0, s51, 12288\n"
    "s_nop 0\n"
    "buffer_load_dwordx4 v0, s[4:7], s60 offen offset:0 nt lds\n"
    "buffer_load_dwordx4 v1, s[4:7], s60 offen offset:1024 nt lds\n"
    "buffer_load_dwordx4 v2, s[4:7], s60 offen offset:2048 nt lds\n"
    "s_mov_b32 s60, 3538944\n"
    "s_add_u32 m0, s51, 24576\n"
    "s_nop 0\n"
    "buffer_load_dwordx4 v0, s[4:7], s60 offen offset:0 nt lds\n"
    "buffer_load_dwordx4 v1, s[4:7], s60 offen offset:1024 nt lds\n"
    "buffer_load_dwordx4 v2, s[4:7], s60 offen offset:2048 nt lds\n"
    "s_mov_b32 s60, 5111808\n"
    "s_add_u32 m0, s51, 36864\n"
    "s_nop 0\n"
    "buffer_load_dwordx4 v0, s[4:7], s60 offen offset:0 nt lds\n"
    "buffer_load_dwordx4 v1, s[4:7], s60 offen offset:1024 nt lds\n"
    "buffer_load_dwordx4 v2, s[4:7], s60 offen offset:2048 nt lds\n"
    "s_waitcnt vmcnt(24)\n"
    "s_barrier\n"
    "v_add_u32 v8, 49152, v7\n"
    "ds_read_b64_tr_b16 v[160:161], v8 offset:0\n"
    "ds_read_b64_tr_b16 v[162:163], v8 offset:768\n"
    "ds_read_b64_tr_b16 v[240:241], v8 offset:128\n"
    "ds_read_b64_tr_b16 v[242:243], v8 offset:896\n"
    "ds_read_b64_tr_b16 v[76:77], v8 offset:256\n"
    "ds_read_b64_tr_b16 v[78:79], v8 offset:1024\n"
    "v_add_u32 v9, 61440, v7\n"
    "ds_read_b64_tr_b16 v[164:165], v9 offset:0\n"
    "ds_read_b64_tr_b16 v[166:167], v9 offset:768\n"
    "ds_read_b64_tr_b16 v[244:245], v9 offset:128\n"
    "ds_read_b64_tr_b16 v[246:247], v9 offset:896\n"
    "ds_read_b64_tr_b16 v[80:81], v9 offset:256\n"
    "ds_read_b64_tr_b16 v[82:83], v9 offset:1024\n"
    "v_add_u32 v10, 73728, v7\n"
    "ds_read_b64_tr_b16 v[168:169], v10 offset:0\n"
    "ds_read_b64_tr_b16 v[170:171], v10 offset:768\n"
    "ds_read_b64_tr_b16 v[248:249], v10 offset:128\n"
    "ds_read_b64_tr_b16 v[250:251], v10 offset:896\n"
    "ds_read_b64_tr_b16 v[84:85], v10 offset:256\n"
    "ds_read_b64_tr_b16 v[86:87], v10 offset:1024\n"
    "v_add_u32 v11, 86016, v7\n"
    "ds_read_b64_tr_b16 v[172:173], v11 offset:0\n"
    "ds_read_b64_tr_b16 v[174:175], v11 offset:768\n"
    "ds_read_b64_tr_b16 v[252:253], v11 offset:128\n"
    "ds_read_b64_tr_b16 v[254:255], v11 offset:896\n"
    "ds_read_b64_tr_b16 v[88:89], v11 offset:256\n"
    "ds_read_b64_tr_b16 v[90:91], v11 offset:1024\n"
    "s_waitcnt lgkmcnt(0)\n"
    "s_barrier\n"
    "s_waitcnt vmcnt(36)\n"
    "v_cmp_gt_i32 vcc, 32, v109\n"
    "s_nop 0\n"
    "s_cbranch_vccnz .Lqk_tk0_%=\n"
    ".Lqk_tk1_%=:\n"
    "s_mov_b32 s60, 786432\n"
    "s_add_u32 m0, s51, 49152\n"
    "s_nop 0\n"
    "buffer_load_dwordx4 v0, s[4:7], s60 offen offset:0 nt lds\n"
    "buffer_load_dwordx4 v1, s[4:7], s60 offen offset:1024 nt lds\n"
    "buffer_load_dwordx4 v2, s[4:7], s60 offen offset:2048 nt lds\n"
    "s_mov_b32 s60, 2359296\n"
    "s_add_u32 m0, s51, 61440\n"
    "s_nop 0\n"
    "buffer_load_dwordx4 v0, s[4:7], s60 offen offset:0 nt lds\n"
    "buffer_load_dwordx4 v1, s[4:7], s60 offen offset:1024 nt lds\n"
    "buffer_load_dwordx4 v2, s[4:7], s60 offen offset:2048 nt lds\n"
    "s_mov_b32 s60, 3932160\n"
    "s_add_u32 m0, s51, 73728\n"
    "s_nop 0\n"
    "buffer_load_dwordx4 v0, s[4:7], s60 offen offset:0 nt lds\n"
    "buffer_load_dwordx4 v1, s[4:7], s60 offen offset:1024 nt lds\n"
    "buffer_load_dwordx4 v2, s[4:7], s60 offen offset:2048 nt lds\n"
    "s_mov_b32 s60, 5505024\n"
    "s_add_u32 m0, s51, 86016\n"
    "s_nop 0\n"
    "buffer_load_dwordx4 v0, s[4:7], s60 offen offset:0 nt lds\n"
    "buffer_load_dwordx4 v1, s[4:7], s60 offen offset:1024 nt lds\n"
    "buffer_load_dwordx4 v2, s[4:7], s60 offen offset:2048 nt lds\n"
    "v_lshlrev_b32 v108, 4, v96\n"
    "v_mov_b32 v109, 1\n"
    "s_mov_b64 exec, 15\n"
    "buffer_store_dword v109, v108, s[0:3], s35 offen sc1\n"
    "s_mov_b64 exec, -1\n"
    "s_waitcnt vmcnt(25)\n"
    "s_barrier\n"
    "v_add_u32 v8, 98304, v3\n"
    "ds_read_b64_tr_b16 a[0:1], v8 offset:0\n"
    "ds_read_b64_tr_b16 a[2:3], v8 offset:768\n"
    "ds_read_b64_tr_b16 a[80:81], v8 offset:128\n"
    "ds_read_b64_tr_b16 a[82:83], v8 offset:896\n"
    "ds_read_b64_tr_b16 a[160:161], v8 offset:256\n"
    "ds_read_b64_tr_b16 a[162:163], v8 offset:1024\n"
    "v_add_u32 v9, 98304, v4\n"
    "ds_read_b64_tr_b16 a[16:17], v9 offset:0\n"
    "ds_read_b64_tr_b16 a[18:19], v9 offset:768\n"
    "ds_read_b64_tr_b16 a[96:97], v9 offset:128\n"
    "ds_read_b64_tr_b16 a[98:99], v9 offset:896\n"
    "ds_read_b64_tr_b16 a[176:177], v9 offset:256\n"
    "ds_read_b64_tr_b16 a[178:179], v9 offset:1024\n"
    "v_add_u32 v10, 98304, v5\n"
    "ds_read_b64_tr_b16 a[32:33], v10 offset:0\n"
    "ds_read_b64_tr_b16 a[34:35], v10 offset:768\n"
    "ds_read_b64_tr_b16 a[112:113], v10 offset:128\n"
    "ds_read_b64_tr_b16 a[114:115], v10 offset:896\n"
    "ds_read_b64_tr_b16 a[192:193], v10 offset:256\n"
    "ds_read_b64_tr_b16 a[194:195], v10 offset:1024\n"
    "v_add_u32 v11, 98304, v6\n"
    "ds_read_b64_tr_b16 a[48:49], v11 offset:0\n"
    "ds_read_b64_tr_b16 a[50:51], v11 offset:768\n"
    "ds_read_b64_tr_b16 a[128:129], v11 offset:128\n"
    "ds_read_b64_tr_b16 a[130:131], v11 offset:896\n"
    "ds_read_b64_tr_b16 a[208:209], v11 offset:256\n"
    "ds_read_b64_tr_b16 a[210:211], v11 offset:1024\n"
    "s_waitcnt lgkmcnt(0)\n"
    "s_barrier\n"
    "s_mov_b32 s60, 1179648\n"
    "s_add_u32 m0, s51, 98304\n"
    "s_nop 0\n"
    "buffer_load_dwordx4 v0, s[4:7], s60 offen offset:0 nt lds\n"
    "buffer_load_dwordx4 v1, s[4:7], s60 offen offset:1024 nt lds\n"
    "buffer_load_dwordx4 v2, s[4:7], s60 offen offset:2048 nt lds\n"
    "s_mov_b32 s60, 2752512\n"
    "s_add_u32 m0, s51, 110592\n"
    "s_nop 0\n"
    "buffer_load_dwordx4 v0, s[4:7], s60 offen offset:0 nt lds\n"
    "buffer_load_dwordx4 v1, s[4:7], s60 offen offset:1024 nt lds\n"
    "buffer_load_dwordx4 v2, s[4:7], s60 offen offset:2048 nt lds\n"
    "s_mov_b32 s60, 4325376\n"
    "s_add_u32 m0, s51, 122880\n"
    "s_nop 0\n"
    "buffer_load_dwordx4 v0, s[4:7], s60 offen offset:0 nt lds\n"
    "buffer_load_dwordx4 v1, s[4:7], s60 offen offset:1024 nt lds\n"
    "buffer_load_dwordx4 v2, s[4:7], s60 offen offset:2048 nt lds\n"
    "s_mov_b32 s60, 5898240\n"
    "s_add_u32 m0, s51, 135168\n"
    "s_nop 0\n"
    "buffer_load_dwordx4 v0, s[4:7], s60 offen offset:0 nt lds\n"
    "buffer_load_dwordx4 v1, s[4:7], s60 offen offset:1024 nt lds\n"
    "buffer_load_dwordx4 v2, s[4:7], s60 offen offset:2048 nt lds\n"
    "s_waitcnt vmcnt(25)\n"
    "s_barrier\n"
    "v_add_u32 v8, 0, v3\n"
    "ds_read_b64_tr_b16 a[4:5], v8 offset:0\n"
    "ds_read_b64_tr_b16 a[6:7], v8 offset:768\n"
    "ds_read_b64_tr_b16 a[84:85], v8 offset:128\n"
    "ds_read_b64_tr_b16 a[86:87], v8 offset:896\n"
    "ds_read_b64_tr_b16 a[164:165], v8 offset:256\n"
    "ds_read_b64_tr_b16 a[166:167], v8 offset:1024\n"
    "v_add_u32 v9, 0, v4\n"
    "ds_read_b64_tr_b16 a[20:21], v9 offset:0\n"
    "ds_read_b64_tr_b16 a[22:23], v9 offset:768\n"
    "ds_read_b64_tr_b16 a[100:101], v9 offset:128\n"
    "ds_read_b64_tr_b16 a[102:103], v9 offset:896\n"
    "ds_read_b64_tr_b16 a[180:181], v9 offset:256\n"
    "ds_read_b64_tr_b16 a[182:183], v9 offset:1024\n"
    "v_add_u32 v10, 0, v5\n"
    "ds_read_b64_tr_b16 a[36:37], v10 offset:0\n"
    "ds_read_b64_tr_b16 a[38:39], v10 offset:768\n"
    "ds_read_b64_tr_b16 a[116:117], v10 offset:128\n"
    "ds_read_b64_tr_b16 a[118:119], v10 offset:896\n"
    "ds_read_b64_tr_b16 a[196:197], v10 offset:256\n"
    "ds_read_b64_tr_b16 a[198:199], v10 offset:1024\n"
    "v_add_u32 v11, 0, v6\n"
    "ds_read_b64_tr_b16 a[52:53], v11 offset:0\n"
    "ds_read_b64_tr_b16 a[54:55], v11 offset:768\n"
    "ds_read_b64_tr_b16 a[132:133], v11 offset:128\n"
    "ds_read_b64_tr_b16 a[134:135], v11 offset:896\n"
    "ds_read_b64_tr_b16 a[212:213], v11 offset:256\n"
    "ds_read_b64_tr_b16 a[214:215], v11 offset:1024\n"
    "s_waitcnt lgkmcnt(0)\n"
    "s_barrier\n"
    "s_mov_b32 s60, 6291456\n"
    "s_add_u32 m0, s51, 0\n"
    "s_nop 0\n"
    "buffer_load_dwordx4 v0, s[4:7], s60 offen offset:0 nt lds\n"
    "buffer_load_dwordx4 v1, s[4:7], s60 offen offset:1024 nt lds\n"
    "buffer_load_dwordx4 v2, s[4:7], s60 offen offset:2048 nt lds\n"
    "s_mov_b32 s60, 6684672\n"
    "s_add_u32 m0, s51, 12288\n"
    "s_nop 0\n"
    "buffer_load_dwordx4 v0, s[4:7], s60 offen offset:0 nt lds\n"
    "buffer_load_dwordx4 v1, s[4:7], s60 offen offset:1024 nt lds\n"
    "buffer_load_dwordx4 v2, s[4:7], s60 offen offset:2048 nt lds\n"
    "s_mov_b32 s60, 7077888\n"
    "s_add_u32 m0, s51, 24576\n"
    "s_nop 0\n"
    "buffer_load_dwordx4 v0, s[4:7], s60 offen offset:0 nt lds\n"
    "buffer_load_dwordx4 v1, s[4:7], s60 offen offset:1024 nt lds\n"
    "buffer_load_dwordx4 v2, s[4:7], s60 offen offset:2048 nt lds\n"
    "s_mov_b32 s60, 7471104\n"
    "s_add_u32 m0, s51, 36864\n"
    "s_nop 0\n"
    "buffer_load_dwordx4 v0, s[4:7], s60 offen offset:0 nt lds\n"
    "buffer_load_dwordx4 v1, s[4:7], s60 offen offset:1024 nt lds\n"
    "buffer_load_dwordx4 v2, s[4:7], s60 offen offset:2048 nt lds\n"
    "s_waitcnt vmcnt(25)\n"
    "s_barrier\n"
    "v_add_u32 v8, 49152, v3\n"
    "ds_read_b64_tr_b16 a[8:9], v8 offset:0\n"
    "ds_read_b64_tr_b16 a[10:11], v8 offset:768\n"
    "ds_read_b64_tr_b16 a[88:89], v8 offset:128\n"
    "ds_read_b64_tr_b16 a[90:91], v8 offset:896\n"
    "ds_read_b64_tr_b16 a[168:169], v8 offset:256\n"
    "ds_read_b64_tr_b16 a[170:171], v8 offset:1024\n"
    "v_add_u32 v9, 49152, v4\n"
    "ds_read_b64_tr_b16 a[24:25], v9 offset:0\n"
    "ds_read_b64_tr_b16 a[26:27], v9 offset:768\n"
    "ds_read_b64_tr_b16 a[104:105], v9 offset:128\n"
    "ds_read_b64_tr_b16 a[106:107], v9 offset:896\n"
    "ds_read_b64_tr_b16 a[184:185], v9 offset:256\n"
    "ds_read_b64_tr_b16 a[186:187], v9 offset:1024\n"
    "v_add_u32 v10, 49152, v5\n"
    "ds_read_b64_tr_b16 a[40:41], v10 offset:0\n"
    "ds_read_b64_tr_b16 a[42:43], v10 offset:768\n"
    "ds_read_b64_tr_b16 a[120:121], v10 offset:128\n"
    "ds_read_b64_tr_b16 a[122:123], v10 offset:896\n"
    "ds_read_b64_tr_b16 a[200:201], v10 offset:256\n"
    "ds_read_b64_tr_b16 a[202:203], v10 offset:1024\n"
    "v_add_u32 v11, 49152, v6\n"
    "ds_read_b64_tr_b16 a[56:57], v11 offset:0\n"
    "ds_read_b64_tr_b16 a[58:59], v11 offset:768\n"
    "ds_read_b64_tr_b16 a[136:137], v11 offset:128\n"
    "ds_read_b64_tr_b16 a[138:139], v11 offset:896\n"
    "ds_read_b64_tr_b16 a[216:217], v11 offset:256\n"
    "ds_read_b64_tr_b16 a[218:219], v11 offset:1024\n"
    "s_waitcnt lgkmcnt(0)\n"
    "s_barrier\n"
    "s_waitcnt vmcnt(12)\n"
    "s_barrier\n"
    "v_add_u32 v8, 98304, v3\n"
    "ds_read_b64_tr_b16 a[12:13], v8 offset:0\n"
    "ds_read_b64_tr_b16 a[14:15], v8 offset:768\n"
    "ds_read_b64_tr_b16 a[92:93], v8 offset:128\n"
    "ds_read_b64_tr_b16 a[94:95], v8 offset:896\n"
    "ds_read_b64_tr_b16 a[172:173], v8 offset:256\n"
    "ds_read_b64_tr_b16 a[174:175], v8 offset:1024\n"
    "v_add_u32 v9, 98304, v4\n"
    "ds_read_b64_tr_b16 a[28:29], v9 offset:0\n"
    "ds_read_b64_tr_b16 a[30:31], v9 offset:768\n"
    "ds_read_b64_tr_b16 a[108:109], v9 offset:128\n"
    "ds_read_b64_tr_b16 a[110:111], v9 offset:896\n"
    "ds_read_b64_tr_b16 a[188:189], v9 offset:256\n"
    "ds_read_b64_tr_b16 a[190:191], v9 offset:1024\n"
    "v_add_u32 v10, 98304, v5\n"
    "ds_read_b64_tr_b16 a[44:45], v10 offset:0\n"
    "ds_read_b64_tr_b16 a[46:47], v10 offset:768\n"
    "ds_read_b64_tr_b16 a[124:125], v10 offset:128\n"
    "ds_read_b64_tr_b16 a[126:127], v10 offset:896\n"
    "ds_read_b64_tr_b16 a[204:205], v10 offset:256\n"
    "ds_read_b64_tr_b16 a[206:207], v10 offset:1024\n"
    "v_add_u32 v11, 98304, v6\n"
    "ds_read_b64_tr_b16 a[60:61], v11 offset:0\n"
    "ds_read_b64_tr_b16 a[62:63], v11 offset:768\n"
    "ds_read_b64_tr_b16 a[140:141], v11 offset:128\n"
    "ds_read_b64_tr_b16 a[142:143], v11 offset:896\n"
    "ds_read_b64_tr_b16 a[220:221], v11 offset:256\n"
    "ds_read_b64_tr_b16 a[222:223], v11 offset:1024\n"
    "s_waitcnt lgkmcnt(0)\n"
    "s_barrier\n"
    "s_waitcnt vmcnt(0)\n"
    "s_barrier\n"
    "v_add_u32 v8, 0, v7\n"
    "ds_read_b64_tr_b16 a[64:65], v8 offset:0\n"
    "ds_read_b64_tr_b16 a[66:67], v8 offset:768\n"
    "ds_read_b64_tr_b16 a[144:145], v8 offset:128\n"
    "ds_read_b64_tr_b16 a[146:147], v8 offset:896\n"
    "ds_read_b64_tr_b16 a[224:225], v8 offset:256\n"
    "ds_read_b64_tr_b16 a[226:227], v8 offset:1024\n"
    "v_add_u32 v9, 12288, v7\n"
    "ds_read_b64_tr_b16 a[68:69], v9 offset:0\n"
    "ds_read_b64_tr_b16 a[70:71], v9 offset:768\n"
    "ds_read_b64_tr_b16 a[148:149], v9 offset:128\n"
    "ds_read_b64_tr_b16 a[150:151], v9 offset:896\n"
    "ds_read_b64_tr_b16 a[228:229], v9 offset:256\n"
    "ds_read_b64_tr_b16 a[230:231], v9 offset:1024\n"
    "v_add_u32 v10, 24576, v7\n"
    "ds_read_b64_tr_b16 a[72:73], v10 offset:0\n"
    "ds_read_b64_tr_b16 a[74:75], v10 offset:768\n"
    "ds_read_b64_tr_b16 a[152:153], v10 offset:128\n"
    "ds_read_b64_tr_b16 a[154:155], v10 offset:896\n"
    "ds_read_b64_tr_b16 a[232:233], v10 offset:256\n"
    "ds_read_b64_tr_b16 a[234:235], v10 offset:1024\n"
    "v_add_u32 v11, 36864, v7\n"
    "ds_read_b64_tr_b16 a[76:77], v11 offset:0\n"
    "ds_read_b64_tr_b16 a[78:79], v11 offset:768\n"
    "ds_read_b64_tr_b16 a[156:157], v11 offset:128\n"
    "ds_read_b64_tr_b16 a[158:159], v11 offset:896\n"
    "ds_read_b64_tr_b16 a[236:237], v11 offset:256\n"
    "ds_read_b64_tr_b16 a[238:239], v11 offset:1024\n"
    "s_waitcnt lgkmcnt(0)\n"
    "s_barrier\n"
    "ds_write_b128 v97, v[12:15] offset:0\n"
    "ds_write_b128 v97, v[16:19] offset:1024\n"
    "ds_write_b128 v97, v[20:23] offset:2048\n"
    "ds_write_b128 v97, v[24:27] offset:3072\n"
    "ds_write_b128 v97, v[28:31] offset:4096\n"
    "ds_write_b128 v97, v[32:35] offset:5120\n"
    "ds_write_b128 v97, v[36:39] offset:6144\n"
    "ds_write_b128 v97, v[40:43] offset:7168\n"
    "ds_write_b128 v97, v[44:47] offset:8192\n"
    "ds_write_b128 v97, v[48:51] offset:9216\n"
    "ds_write_b128 v97, v[52:55] offset:10240\n"
    "ds_write_b128 v97, v[56:59] offset:11264\n"
    "ds_write_b128 v97, v[60:63] offset:12288\n"
    "ds_write_b128 v97, v[64:67] offset:13312\n"
    "ds_write_b128 v97, v[68:71] offset:14336\n"
    "ds_write_b128 v97, v[72:75] offset:15360\n"
    "ds_write_b128 v97, v[76:79] offset:16384\n"
    "ds_write_b128 v97, v[80:83] offset:17408\n"
    "ds_write_b128 v97, v[84:87] offset:18432\n"
    "ds_write_b128 v97, v[88:91] offset:19456\n"
    "s_mov_b32 s46, -2\n"
    "s_mov_b32 s62, 1037384597\n"
    "s_mov_b32 s63, 1037384597\n"
    "s_mov_b32 s64, 858993459\n"
    "s_mov_b32 s65, 858993459\n"
    "s_mov_b32 s66, 67044351\n"
    "s_mov_b32 s67, 67044351\n"
    "v_mov_b32 v8, v99\n"
    "v_mov_b32 v9, v99\n"
    "v_mov_b32 v10, 137088\n"
    "ds_write_b64 v10, v[8:9] offset:0\n"
    "ds_write_b64 v10, v[8:9] offset:8\n"
    "ds_write_b64 v10, v[8:9] offset:1936\n"
    "ds_write_b64 v10, v[8:9] offset:1944\n"
    "ds_write_b64 v10, v[8:9] offset:3872\n"
    "ds_write_b64 v10, v[8:9] offset:3880\n"
    "ds_write_b64 v10, v[8:9] offset:5808\n"
    "ds_write_b64 v10, v[8:9] offset:5816\n"
    "s_lshl_b32 s59, s22, 11\n"
    "s_add_u32 s59, s59, 45056\n"
    "v_lshrrev_b32 v4, 2, v96\n"
    "v_and_b32 v6, 12, v4\n"
    "v_lshlrev_b32 v5, 1, v96\n"
    "v_and_b32 v5, 0x380, v5\n"
    "v_min_u32 v5, 0x200, v5\n"
    "v_add_u32 v5, v5, v6\n"
    "s_lshr_b32 s60, s23, 13\n"
    "s_mul_i32 s60, s60, 3\n"
    ".Lqk_po6_%=:\n"
    "buffer_load_dword v8, v4, s[0:3], s32 offen sc1\n"
    "s_waitcnt vmcnt(0)\n"
    "v_cmp_gt_i32 vcc, 1, v8\n"
    "s_nop 0\n"
    "s_cbranch_vccz .Lqk_pd7_%=\n"
    "s_add_u32 s40, s40, 1\n"
    "s_cmp_lt_u32 s40, 0x40000\n"
    "s_cbranch_scc1 .Lqk_sp8_%=\n"
    "v_mov_b32 v8, 20\n"
    "s_mov_b32 s44, 33728\n"
    "buffer_store_dword v8, off, s[0:3], s44 sc1\n"
    "s_waitcnt vmcnt(0)\n"
    "s_branch .Lqk_pd7_%=\n"
    ".Lqk_sp8_%=:\n"
    "s_sleep 4\n"
    "s_branch .Lqk_po6_%=\n"
    ".Lqk_pd7_%=:\n"
    ".Lqk_po9_%=:\n"
    "buffer_load_dword v8, v5, s[0:3], s33 offen sc1\n"
    "s_waitcnt vmcnt(0)\n"
    "v_cmp_gt_i32 vcc, s60, v8\n"
    "s_nop 0\n"
    "s_cbranch_vccz .Lqk_pd10_%=\n"
    "s_add_u32 s40, s40, 1\n"
    "s_cmp_lt_u32 s40, 0x40000\n"
    "s_cbranch_scc1 .Lqk_sp11_%=\n"
    "v_mov_b32 v8, 21\n"
    "s_mov_b32 s44, 33728\n"
    "buffer_store_dword v8, off, s[0:3], s44 sc1\n"
    "s_waitcnt vmcnt(0)\n"
    "s_branch .Lqk_pd10_%=\n"
    ".Lqk_sp11_%=:\n"
    "s_sleep 4\n"
    "s_branch .Lqk_po9_%=\n"
    ".Lqk_pd10_%=:\n"
    "s_add_u32 s53, s27, s23\n"
    "s_sub_u32 m0, s26, 8192\n"
    "s_nop 0\n"
    "buffer_load_dwordx4 v96, s[0:3], s53 offen offset:0 sc1 lds\n"
    "buffer_load_dwordx4 v96, s[0:3], s53 offen offset:1024 sc1 lds\n"
    "buffer_load_dwordx4 v96, s[0:3], s53 offen offset:2048 sc1 lds\n"
    "buffer_load_dwordx4 v96, s[0:3], s53 offen offset:3072 sc1 lds\n"
    "s_sub_u32 m0, s26, 4096\n"
    "s_add_u32 s53, s53, 4096\n"
    "buffer_load_dwordx4 v96, s[0:3], s53 offen offset:0 sc1 lds\n"
    "buffer_load_dwordx4 v96, s[0:3], s53 offen offset:1024 sc1 lds\n"
    ".Lqk_po12_%=:\n"
    "buffer_load_dword v8, v6, s[0:3], s34 offen sc1\n"
    "s_waitcnt vmcnt(0)\n"
    "v_cmp_gt_i32 vcc, 2, v8\n"
    "s_nop 0\n"
    "s_cbranch_vccz .Lqk_pd13_%=\n"
    "s_add_u32 s40, s40, 1\n"
    "s_cmp_lt_u32 s40, 0x40000\n"
    "s_cbranch_scc1 .Lqk_sp14_%=\n"
    "v_mov_b32 v8, 22\n"
    "s_mov_b32 s44, 33728\n"
    "buffer_store_dword v8, off, s[0:3], s44 sc1\n"
    "s_waitcnt vmcnt(0)\n"
    "s_branch .Lqk_pd13_%=\n"
    ".Lqk_sp14_%=:\n"
    "s_sleep 4\n"
    "s_branch .Lqk_po12_%=\n"
    ".Lqk_pd13_%=:\n"
    "s_memrealtime s[42:43]\n"
    "s_waitcnt lgkmcnt(0)\n"
    "s_sub_u32 s57, s42, 792\n"
    "s_mov_b32 s46, -1\n"
    "s_mov_b32 s47, -5\n"
    "s_mov_b32 s50, 2130706432\n"
    "s_mov_b32 s48, 2130706432\n"
    "s_mov_b32 s49, 4194304\n"
    "s_mov_b32 s55, s31\n"
    "s_add_u32 s54, s29, s23\n"
    "s_barrier\n"
    "v_mov_b32 v110, v104\n"
    "ds_read_b128 v[32:35], v92 offset:0\n"
    "ds_read_b128 v[36:39], v92 offset:1024\n"
    "ds_read_b128 v[40:43], v93 offset:0\n"
    "ds_read_b128 v[44:47], v93 offset:1024\n"
    "ds_read_b128 v[48:51], v94 offset:0\n"
    "ds_read_b128 v[52:55], v94 offset:1024\n"
    "s_waitcnt lgkmcnt(0)\n"
    "s_branch .Lqk_bulk_%=\n"
    ".Lqk_take_%=:\n"
    "s_waitcnt vmcnt(2)\n"
    ".Lqk_mk0x14_%=:\n"
    "ds_read_b128 v[56:59], v92 offset:8192\n"
    "s_waitcnt vmcnt(1)\n"
    "ds_read_b128 v[60:63], v92 offset:9216\n"
    "s_waitcnt lgkmcnt(1)\n"
    "v_smfmac_f32_16x16x64_bf16 v[8:11], v[56:59], v[32:39], v98\n"
    "v_max_u32 v108, v56, v58\n"
    "v_smfmac_f32_16x16x64_bf16 v[12:15], v[56:59], a[160:167], v98\n"
    "s_waitcnt lgkmcnt(0)\n"
    "ds_read_b128 v[32:35], v97 offset:6144\n"
    "v_max3_u32 v108, v108, v60, v62\n"
    "v_smfmac_f32_16x16x64_bf16 v[0:3], v[56:59], v[176:183], v98\n"
    "ds_read_b128 v[36:39], v97 offset:7168\n"
    "v_cmp_eq_u32 vcc, -1, v108\n"
    "v_smfmac_f32_16x16x64_bf16 v[4:7], v[56:59], a[80:87], v98\n"
    "s_cbranch_vccnz .Lqk_retry_%=\n"
    ".Lqk_go_%=:\n"
    "s_barrier\n"
    ".Lqk_mk1x15_%=:\n"
    "v_smfmac_f32_16x16x64_bf16 v[16:19], v[56:59], a[240:247], v98\n"
    "ds_read_b128 v[24:27], v93 offset:8192\n"
    "ds_read_b128 v[28:31], v93 offset:9216\n"
    "v_smfmac_f32_16x16x64_bf16 v[20:23], v[56:59], a[0:7], v98\n"
    "s_waitcnt vmcnt(0)\n"
    "ds_read_b128 v[64:67], v92 offset:10240\n"
    "v_smfmac_f32_16x16x64_bf16 v[8:11], v[60:63], v[40:47], v98\n"
    "ds_read_b128 v[56:59], v94 offset:9216\n"
    "v_smfmac_f32_16x16x64_bf16 v[12:15], v[60:63], a[168:175], v98\n"
    "ds_read_b128 v[40:43], v97 offset:8192\n"
    "ds_read_b128 v[44:47], v97 offset:9216\n"
    "v_smfmac_f32_16x16x64_bf16 v[0:3], v[60:63], v[184:191], v98\n"
    "v_smfmac_f32_16x16x64_bf16 v[4:7], v[60:63], a[88:95], v98\n"
    "v_smfmac_f32_16x16x64_bf16 v[16:19], v[60:63], a[248:255], v98\n"
    "s_waitcnt lgkmcnt(3)\n"
    "v_max_u32 v108, v64, v66\n"
    "v_smfmac_f32_16x16x64_bf16 v[20:23], v[60:63], a[8:15], v98\n"
    "v_cmp_eq_u32 vcc, -1, v108\n"
    "v_smfmac_f32_16x16x64_bf16 v[8:11], v[24:27], v[48:55], v98\n"
    "s_cbranch_vccnz .Lqk_r2_%=\n"
    ".Lqk_go2_%=:\n"
    "s_barrier\n"
    "v_smfmac_f32_16x16x64_bf16 v[12:15], v[24:27], a[176:183], v98\n"
    "s_memrealtime s[68:69]\n"
    "ds_read_b128 v[64:67], v94 offset:8192\n"
    "ds_read_b128 v[60:63], v95 offset:8192\n"
    "v_smfmac_f32_16x16x64_bf16 v[0:3], v[24:27], v[192:199], v98\n"
    "ds_read_b128 v[48:51], v97 offset:10240\n"
    "ds_read_b128 v[52:55], v97 offset:11264\n"
    "v_smfmac_f32_16x16x64_bf16 v[4:7], v[24:27], a[96:103], v98\n"
    "v_mov_b32 v109, s47\n"
    "v_smfmac_f32_16x16x64_bf16 v[16:19], v[24:27], v[112:119], v98\n"
    "v_lshlrev_b32 v110, 10, v96\n"
    "v_smfmac_f32_16x16x64_bf16 v[20:23], v[24:27], a[16:23], v98\n"
    "ds_read_b128 v[24:27], v95 offset:9216\n"
    "s_mov_b64 vcc, s[64:65]\n"
    "v_smfmac_f32_16x16x64_bf16 v[8:11], v[28:31], v[32:39], v98\n"
    "ds_read_b128 v[32:35], v97 offset:12288\n"
    "ds_read_b128 v[36:39], v97 offset:13312\n"
    "v_smfmac_f32_16x16x64_bf16 v[12:15], v[28:31], a[184:191], v98\n"
    "v_smfmac_f32_16x16x64_bf16 v[0:3], v[28:31], v[200:207], v98\n"
    "v_smfmac_f32_16x16x64_bf16 v[4:7], v[28:31], a[104:111], v98\n"
    "v_smfmac_f32_16x16x64_bf16 v[16:19], v[28:31], v[120:127], v98\n"
    "v_smfmac_f32_16x16x64_bf16 v[20:23], v[28:31], a[24:31], v98\n"
    "s_waitcnt lgkmcnt(0)\n"
    "v_smfmac_f32_16x16x64_bf16 v[8:11], v[64:67], v[40:47], v98\n"
    "s_sub_u32 s44, s68, s57\n"
    "ds_read_b128 v[40:43], v97 offset:14336\n"
    "ds_read_b128 v[44:47], v97 offset:15360\n"
    "v_smfmac_f32_16x16x64_bf16 v[12:15], v[64:67], a[192:199], v98\n"
    "ds_read_b128 v[28:31], v96 offset:10240\n"
    "s_add_u32 s45, s68, 10\n"
    "v_smfmac_f32_16x16x64_bf16 v[0:3], v[64:67], v[208:215], v98\n"
    "s_cmp_gt_i32 s44, 6\n"
    "s_cselect_b32 s57, s45, s57\n"
    "v_mov_b32 v108, s57\n"
    "v_smfmac_f32_16x16x64_bf16 v[4:7], v[64:67], a[112:119], v98\n"
    "s_mov_b64 exec, 15\n"
    "buffer_store_dwordx2 v[108:109], v110, s[0:3], s56 offen sc1\n"
    "s_mov_b64 exec, -1\n"
    "v_smfmac_f32_16x16x64_bf16 v[16:19], v[64:67], v[128:135], v98\n"
    "v_smfmac_f32_16x16x64_bf16 v[20:23], v[64:67], a[32:39], v98\n"
    "v_bfe_u32 v111, v96, 8, 2\n"
    "v_smfmac_f32_16x16x64_bf16 v[8:11], v[56:59], v[48:55], v98\n"
    "ds_read_b128 v[48:51], v97 offset:16384\n"
    "ds_read_b128 v[52:55], v97 offset:17408\n"
    "v_smfmac_f32_16x16x64_bf16 v[12:15], v[56:59], a[200:207], v98\n"
    "ds_read_b128 v[64:67], v96 offset:21504\n"
    "v_mad_i32_i24 v110, v111, -8, v104\n"
    "v_smfmac_f32_16x16x64_bf16 v[8:11], v[60:63], v[32:39], v98\n"
    "ds_read_b128 v[32:35], v97 offset:18432\n"
    "ds_read_b128 v[36:39], v97 offset:19456\n"
    "v_smfmac_f32_16x16x64_bf16 v[12:15], v[60:63], a[208:215], v98\n"
    "s_waitcnt lgkmcnt(5)\n"
    "v_mad_i32_i24 v111, v111, -8, v102\n"
    "v_smfmac_f32_16x16x64_bf16 v[8:11], v[24:27], v[40:47], v98\n"
    "v_smfmac_f32_16x16x64_bf16 v[12:15], v[24:27], a[216:223], v98\n"
    "v_smfmac_f32_16x16x64_bf16 v[0:3], v[56:59], v[216:223], v98\n"
    "v_smfmac_f32_16x16x64_bf16 v[4:7], v[56:59], a[120:127], v98\n"
    "s_waitcnt lgkmcnt(3)\n"
    "v_smfmac_f32_16x16x64_bf16 v[8:11], v[28:31], v[48:55], v98\n"
    "v_smfmac_f32_16x16x64_bf16 v[12:15], v[28:31], a[224:231], v98\n"
    "s_waitcnt lgkmcnt(0)\n"
    "v_smfmac_f32_16x16x64_bf16 v[8:11], v[64:67], v[32:39], v98\n"
    "v_smfmac_f32_16x16x64_bf16 v[12:15], v[64:67], a[232:239], v98\n"
    "ds_read_b128 v[32:35], v92 offset:0\n"
    "ds_read_b128 v[36:39], v92 offset:1024\n"
    "v_smfmac_f32_16x16x64_bf16 v[0:3], v[60:63], v[224:231], v98\n"
    "s_nop 1\n"
    "v_add_f32 v8, v8, v10\n"
    "v_smfmac_f32_16x16x64_bf16 v[4:7], v[60:63], a[128:135], v98\n"
    "ds_read_b128 v[40:43], v93 offset:0\n"
    "v_add_f32 v9, v9, v11\n"
    "v_smfmac_f32_16x16x64_bf16 v[0:3], v[24:27], v[232:239], v98\n"
    "ds_read_b128 v[44:47], v93 offset:1024\n"
    "v_cvt_pk_bf16_f32 v8, v8, v9\n"
    "v_smfmac_f32_16x16x64_bf16 v[0:3], v[28:31], v[240:247], v98\n"
    "ds_read_b128 v[48:51], v94 offset:0\n"
    "v_add_f32 v12, v12, v14\n"
    "v_smfmac_f32_16x16x64_bf16 v[0:3], v[64:67], v[248:255], v98\n"
    "ds_read_b128 v[52:55], v94 offset:1024\n"
    "v_add_f32 v13, v13, v15\n"
    "v_smfmac_f32_16x16x64_bf16 v[4:7], v[24:27], a[136:143], v98\n"
    "v_cvt_pk_bf16_f32 v9, v12, v13\n"
    "v_smfmac_f32_16x16x64_bf16 v[4:7], v[28:31], a[144:151], v98\n"
    "s_nop 0\n"
    "v_mfma_f32_16x16x16_bf16 v[12:15], v[8:9], v[100:101], 0\n"
    "v_add_f32 v0, v0, v2\n"
    "v_add_f32 v1, v1, v3\n"
    "v_smfmac_f32_16x16x64_bf16 v[4:7], v[64:67], a[152:159], v98\n"
    "v_cvt_pk_bf16_f32 v0, v0, v1\n"
    "v_smfmac_f32_16x16x64_bf16 v[16:19], v[56:59], v[136:143], v98\n"
    "s_nop 1\n"
    "v_cvt_pk_bf16_f32 v15, v14, v15\n"
    "v_smfmac_f32_16x16x64_bf16 v[20:23], v[56:59], a[40:47], v98\n"
    "s_nop 0\n"
    "v_add_f32 v4, v4, v6\n"
    "v_smfmac_f32_16x16x64_bf16 v[16:19], v[60:63], v[144:151], v98\n"
    "v_add_f32 v5, v5, v7\n"
    "v_smfmac_f32_16x16x64_bf16 v[20:23], v[60:63], a[48:55], v98\n"
    "v_cvt_pk_bf16_f32 v1, v4, v5\n"
    "v_smfmac_f32_16x16x64_bf16 v[16:19], v[24:27], v[152:159], v98\n"
    "s_nop 0\n"
    "v_mfma_f32_16x16x16_bf16 v[4:7], v[0:1], v[100:101], 0\n"
    "v_cvt_pk_bf16_f32 v14, v12, v13\n"
    "v_smfmac_f32_16x16x64_bf16 v[20:23], v[24:27], a[56:63], v98\n"
    "v_smfmac_f32_16x16x64_bf16 v[16:19], v[28:31], v[160:167], v98\n"
    "s_nop 1\n"
    "v_cvt_pk_bf16_f32 v13, v6, v7\n"
    "v_smfmac_f32_16x16x64_bf16 v[20:23], v[28:31], a[64:71], v98\n"
    "v_cvt_pk_bf16_f32 v12, v4, v5\n"
    "v_smfmac_f32_16x16x64_bf16 v[16:19], v[64:67], v[168:175], v98\n"
    "v_smfmac_f32_16x16x64_bf16 v[20:23], v[64:67], a[72:79], v98\n"
    ".Lqk_mk2x16_%=:\n"
    "v_mfma_f32_16x16x32_bf16 v[24:27], v[12:15], v[12:15], 0\n"
    "s_nop 1\n"
    "v_add_f32 v16, v16, v18\n"
    "v_add_f32 v17, v17, v19\n"
    "v_cvt_pk_bf16_f32 v16, v16, v17\n"
    "v_add_f32 v20, v20, v22\n"
    "v_add_f32 v21, v21, v23\n"
    "v_cvt_pk_bf16_f32 v17, v20, v21\n"
    "s_nop 1\n"
    "v_mfma_f32_16x16x16_bf16 v[20:23], v[16:17], v[100:101], 0\n"
    "v_mfma_f32_16x16x32_bf16 v[0:3], v[72:75], v[12:15], 0\n"
    "v_mfma_f32_16x16x32_bf16 v[8:11], v[68:71], v[12:15], 0\n"
    "s_nop 1\n"
    "v_cvt_pk_bf16_f32 v17, v22, v23\n"
    "v_cvt_pk_bf16_f32 v16, v20, v21\n"
    "s_nop 1\n"
    "v_mfma_f32_16x16x16_bf16 v[24:27], v[16:17], v[16:17], v[24:27]\n"
    "v_mfma_f32_16x16x16_bf16 v[0:3], v[78:79], v[16:17], v[0:3]\n"
    "v_mfma_f32_16x16x16_bf16 v[8:11], v[76:77], v[16:17], v[8:11]\n"
    "s_nop 1\n"
    "ds_write_b64 v111, v[26:27] offset:128\n"
    "v_mfma_f32_16x16x32_bf16 v[56:59], v[12:15], v[80:83], 0\n"
    "s_nop 3\n"
    "v_cndmask_b32_dpp v8, v0, v8, vcc quad_perm:[0,1,0,1] row_mask:0xf bank_mask:0xf\n"
    "v_cndmask_b32_dpp v9, v1, v9, vcc quad_perm:[0,1,0,1] row_mask:0xf bank_mask:0xf\n"
    "v_cndmask_b32_dpp v10, v2, v10, vcc quad_perm:[0,1,0,1] row_mask:0xf bank_mask:0xf\n"
    "v_cndmask_b32_dpp v11, v3, v11, vcc quad_perm:[0,1,0,1] row_mask:0xf bank_mask:0xf\n"
    "ds_write_b128 v102, v[8:11]\n"
    "v_mfma_f32_16x16x32_bf16 v[60:63], v[12:15], v[84:87], 0\n"
    "s_mov_b64 exec, s[20:21]\n"
    "s_waitcnt lgkmcnt(0)\n"
    "s_barrier\n"
    ".Lqk_mk3x17_%=:\n"
    "ds_read_b128 v[72:75], v103 offset:0\n"
    "ds_read_b128 v[76:79], v103 offset:1936\n"
    "ds_read_b128 v[80:83], v103 offset:3872\n"
    "ds_read_b128 v[84:87], v103 offset:5808\n"
    "s_mov_b64 exec, -1\n"
    "v_mfma_f32_16x16x16_bf16 v[56:59], v[16:17], v[88:89], v[56:59]\n"
    "v_mfma_f32_16x16x16_bf16 v[60:63], v[16:17], v[90:91], v[60:63]\n"
    "s_waitcnt lgkmcnt(2)\n"
    "v_pk_add_f32 v[72:73], v[72:73], v[76:77]\n"
    "v_pk_add_f32 v[74:75], v[74:75], v[78:79]\n"
    "s_waitcnt lgkmcnt(1)\n"
    "v_pk_add_f32 v[72:73], v[72:73], v[80:81]\n"
    "v_pk_add_f32 v[74:75], v[74:75], v[82:83]\n"
    "s_waitcnt lgkmcnt(0)\n"
    "v_pk_add_f32 v[72:73], v[72:73], v[84:85]\n"
    "v_pk_add_f32 v[74:75], v[74:75], v[86:87]\n"
    "v_max3_f32 v76, v72, v73, v74\n"
    "v_max_f32 v76, v76, v75\n"
    "v_pk_mul_f32 v[72:73], v[72:73], s[62:63]\n"
    "v_pk_mul_f32 v[74:75], v[74:75], s[62:63]\n"
    "v_max_f32_dpp v76, v76, v76 row_ror:8 row_mask:0xf bank_mask:0xf\n"
    "s_nop 1\n"
    "v_max_f32_dpp v76, v76, v76 row_ror:4 row_mask:0xf bank_mask:0xf\n"
    "s_nop 1\n"
    "v_max_f32_dpp v76, v76, v76 row_ror:2 row_mask:0xf bank_mask:0xf\n"
    "s_nop 1\n"
    "v_max_f32_dpp v76, v76, v76 row_ror:1 row_mask:0xf bank_mask:0xf\n"
    "v_pk_fma_f32 v[72:73], v[76:77], s[62:63], v[72:73] op_sel_hi:[0,1,1] neg_lo:[1,0,0] neg_hi:[1,0,0]\n"
    "v_pk_fma_f32 v[74:75], v[76:77], s[62:63], v[74:75] op_sel_hi:[0,1,1] neg_lo:[1,0,0] neg_hi:[1,0,0]\n"
    "v_exp_f32 v72, v72\n"
    "v_exp_f32 v74, v74\n"
    "v_exp_f32 v73, v73\n"
    "v_exp_f32 v75, v75\n"
    "v_add_f32 v76, v72, v74\n"
    "v_add_f32 v77, v73, v75\n"
    "v_add_f32 v76, v76, v77\n"
    "s_nop 1\n"
    "v_add_f32_dpp v76, v76, v76 row_ror:8 row_mask:0xf bank_mask:0xf\n"
    "s_nop 1\n"
    "v_add_f32_dpp v76, v76, v76 row_ror:4 row_mask:0xf bank_mask:0xf\n"
    "s_nop 1\n"
    "v_add_f32_dpp v76, v76, v76 row_ror:2 row_mask:0xf bank_mask:0xf\n"
    "s_nop 1\n"
    "v_add_f32_dpp v76, v76, v76 row_ror:1 row_mask:0xf bank_mask:0xf\n"
    "v_rcp_f32 v76, v76\n"
    "s_nop 0\n"
    "v_pk_mul_f32 v[72:73], v[72:73], v[76:77] op_sel_hi:[1,0] clamp\n"
    "v_pk_mul_f32 v[74:75], v[74:75], v[76:77] op_sel_hi:[1,0] clamp\n"
    "v_cvt_pk_bf16_f32 v72, v72, v73\n"
    "v_cvt_pk_bf16_f32 v73, v74, v75\n"
    "buffer_store_dwordx2 v[72:73], v106, s[0:3], s48 offen sc1\n"
    ".Lqk_mk4x18_%=:\n"
    ".Lqk_bulk_%=:\n"
    "v_mfma_f32_16x16x32_bf16 v[0:3], a[80:83], v[32:35], 0\n"
    "ds_write_b64 v110, v[58:59] offset:224\n"
    "v_mfma_f32_16x16x32_bf16 v[4:7], a[80:83], v[36:39], 0\n"
    "ds_write_b64 v110, v[62:63] offset:128\n"
    ".Lqk_mkL0_%=:\n"
    "v_mfma_f32_16x16x32_bf16 v[8:11], a[160:163], v[32:35], 0\n"
    "v_mfma_f32_16x16x32_bf16 v[12:15], a[160:163], v[36:39], 0\n"
    "v_mfma_f32_16x16x32_bf16 v[16:19], a[0:3], v[32:35], 0\n"
    "v_mfma_f32_16x16x32_bf16 v[20:23], a[0:3], v[36:39], 0\n"
    "s_waitcnt lgkmcnt(0)\n"
    "s_barrier\n"
    "ds_read_b128 v[56:59], v105 offset:0\n"
    "v_mfma_f32_16x16x32_bf16 v[24:27], a[240:243], v[32:35], 0\n"
    "ds_read_b128 v[60:63], v105 offset:4352\n"
    "v_mfma_f32_16x16x32_bf16 v[28:31], a[240:243], v[36:39], 0\n"
    "v_mfma_f32_16x16x32_bf16 v[0:3], a[96:99], v[40:43], v[0:3]\n"
    "s_sub_u32 m0, s26, 4096\n"
    "ds_read_b128 v[32:35], v95 offset:0\n"
    "ds_read_b128 v[36:39], v95 offset:1024\n"
    "v_mfma_f32_16x16x32_bf16 v[4:7], a[96:99], v[44:47], v[4:7]\n"
    "ds_read_b128 v[64:67], v105 offset:8704\n"
    "v_mfma_f32_16x16x32_bf16 v[8:11], a[176:179], v[40:43], v[8:11]\n"
    "buffer_load_dwordx4 v96, s[0:3], s53 offen offset:2048 sc1 lds\n"
    "v_mfma_f32_16x16x32_bf16 v[12:15], a[176:179], v[44:47], v[12:15]\n"
    "ds_read_b128 v[68:71], v105 offset:13056\n"
    "v_mfma_f32_16x16x32_bf16 v[16:19], a[16:19], v[40:43], v[16:19]\n"
    "v_mfma_f32_16x16x32_bf16 v[20:23], a[16:19], v[44:47], v[20:23]\n"
    "v_mfma_f32_16x16x32_bf16 v[24:27], v[112:115], v[40:43], v[24:27]\n"
    "buffer_load_dwordx4 v96, s[0:3], s53 offen offset:3072 sc1 lds\n"
    "v_mfma_f32_16x16x32_bf16 v[28:31], v[112:115], v[44:47], v[28:31]\n"
    "s_mov_b32 m0, s59\n"
    "s_add_u32 s53, s53, s28\n"
    "s_waitcnt lgkmcnt(4)\n"
    "v_mfma_f32_16x16x32_bf16 v[0:3], a[112:115], v[48:51], v[0:3]\n"
    "ds_read_b128 v[40:43], v92 offset:2048\n"
    "ds_read_b128 v[44:47], v92 offset:3072\n"
    "v_mfma_f32_16x16x32_bf16 v[4:7], a[112:115], v[52:55], v[4:7]\n"
    "v_add_f32 v56, v56, v60\n"
    "v_mfma_f32_16x16x32_bf16 v[8:11], a[192:195], v[48:51], v[8:11]\n"
    "buffer_load_dwordx4 v96, s[0:3], s53 offen offset:0 sc1 lds\n"
    "v_mfma_f32_16x16x32_bf16 v[12:15], a[192:195], v[52:55], v[12:15]\n"
    "v_add_f32 v57, v57, v61\n"
    "v_add_f32 v58, v58, v62\n"
    "v_mfma_f32_16x16x32_bf16 v[16:19], a[32:35], v[48:51], v[16:19]\n"
    "v_add_f32 v59, v59, v63\n"
    "s_waitcnt lgkmcnt(3)\n"
    "v_mfma_f32_16x16x32_bf16 v[20:23], a[32:35], v[52:55], v[20:23]\n"
    "v_add_f32 v56, v56, v64\n"
    ".Lqk_mkL1_%=:\n"
    "v_mfma_f32_16x16x32_bf16 v[24:27], v[128:131], v[48:51], v[24:27]\n"
    "buffer_load_dwordx4 v96, s[0:3], s53 offen offset:1024 sc1 lds\n"
    "v_mfma_f32_16x16x32_bf16 v[28:31], v[128:131], v[52:55], v[28:31]\n"
    "s_waitcnt lgkmcnt(4)\n"
    "v_mov_b64 v[110:111], -1\n"
    "v_add_f32 v57, v57, v65\n"
    "v_mfma_f32_16x16x32_bf16 v[0:3], a[128:131], v[32:35], v[0:3]\n"
    "ds_read_b128 v[48:51], v93 offset:2048\n"
    "ds_read_b128 v[52:55], v93 offset:3072\n"
    "v_mfma_f32_16x16x32_bf16 v[4:7], a[128:131], v[36:39], v[4:7]\n"
    "v_add_f32 v58, v58, v66\n"
    "v_mfma_f32_16x16x32_bf16 v[8:11], a[208:211], v[32:35], v[8:11]\n"
    "buffer_load_dwordx4 v[80:83], v96, s[0:3], s54 offen sc1\n"
    "v_mfma_f32_16x16x32_bf16 v[12:15], a[208:211], v[36:39], v[12:15]\n"
    "v_add_f32 v59, v59, v67\n"
    "s_waitcnt lgkmcnt(4)\n"
    "v_mfma_f32_16x16x32_bf16 v[16:19], a[48:51], v[32:35], v[16:19]\n"
    "v_add_f32 v56, v56, v68\n"
    "v_add_f32 v57, v57, v69\n"
    "v_mfma_f32_16x16x32_bf16 v[20:23], a[48:51], v[36:39], v[20:23]\n"
    "v_add_f32 v58, v58, v70\n"
    "v_mfma_f32_16x16x32_bf16 v[24:27], v[144:147], v[32:35], v[24:27]\n"
    "buffer_load_dwordx4 v[84:87], v96, s[0:3], s54 offen offset:1024 sc1\n"
    "v_mfma_f32_16x16x32_bf16 v[28:31], v[144:147], v[36:39], v[28:31]\n"
    "s_waitcnt lgkmcnt(2)\n"
    "v_add_f32 v59, v59, v71\n"
    "s_barrier\n"
    "v_mfma_f32_16x16x32_bf16 v[0:3], a[84:87], v[40:43], v[0:3]\n"
    "ds_read_b128 v[32:35], v94 offset:2048\n"
    "ds_read_b128 v[36:39], v94 offset:3072\n"
    "v_mfma_f32_16x16x32_bf16 v[4:7], a[84:87], v[44:47], v[4:7]\n"
    "v_cndmask_b32 v56, v99, v56, s[12:13]\n"
    "v_cndmask_b32 v57, v99, v57, s[14:15]\n"
    "v_mfma_f32_16x16x32_bf16 v[8:11], a[164:167], v[40:43], v[8:11]\n"
    "buffer_store_dwordx2 v[110:111], v106, s[0:3], s49 offen sc1\n"
    "v_cndmask_b32 v58, v99, v58, s[16:17]\n"
    "v_mfma_f32_16x16x32_bf16 v[12:15], a[164:167], v[44:47], v[12:15]\n"
    "v_cndmask_b32 v59, v99, v59, s[18:19]\n"
    "v_max3_f32 v72, v56, v57, v58\n"
    "v_mfma_f32_16x16x32_bf16 v[16:19], a[4:7], v[40:43], v[16:19]\n"
    "v_max_f32 v72, v72, v59\n"
    "v_cndmask_b32 v74, v99, v72, s[66:67]\n"
    "v_mfma_f32_16x16x32_bf16 v[20:23], a[4:7], v[44:47], v[20:23]\n"
    "v_cndmask_b32 v75, v72, v99, s[66:67]\n"
    "v_max_f32_dpp v74, v74, v74 row_ror:8 row_mask:0xf bank_mask:0xf\n"
    "v_mfma_f32_16x16x32_bf16 v[24:27], a[244:247], v[40:43], v[24:27]\n"
    "v_max_f32_dpp v75, v75, v75 row_ror:8 row_mask:0xf bank_mask:0xf\n"
    "v_max_f32_dpp v74, v74, v74 row_ror:4 row_mask:0xf bank_mask:0xf\n"
    "v_mfma_f32_16x16x32_bf16 v[28:31], a[244:247], v[44:47], v[28:31]\n"
    "s_waitcnt lgkmcnt(2)\n"
    "v_max_f32_dpp v75, v75, v75 row_ror:4 row_mask:0xf bank_mask:0xf\n"
    "v_max_f32_dpp v74, v74, v74 row_ror:2 row_mask:0xf bank_mask:0xf\n"
    "v_mfma_f32_16x16x32_bf16 v[0:3], a[100:103], v[48:51], v[0:3]\n"
    "ds_read_b128 v[40:43], v95 offset:2048\n"
    "ds_read_b128 v[44:47], v95 offset:3072\n"
    "v_mfma_f32_16x16x32_bf16 v[4:7], a[100:103], v[52:55], v[4:7]\n"
    "v_max_f32_dpp v75, v75, v75 row_ror:2 row_mask:0xf bank_mask:0xf\n"
    "v_max_f32_dpp v74, v74, v74 row_ror:1 row_mask:0xf bank_mask:0xf\n"
    "v_mfma_f32_16x16x32_bf16 v[8:11], a[180:183], v[48:51], v[8:11]\n"
    "buffer_store_dwordx2 v[110:111], v107, s[0:3], s49 offen sc1\n"
    "v_max_f32_dpp v75, v75, v75 row_ror:1 row_mask:0xf bank_mask:0xf\n"
    ".Lqk_mkL2_%=:\n"
    "v_mfma_f32_16x16x32_bf16 v[12:15], a[180:183], v[52:55], v[12:15]\n"
    "v_cndmask_b32 v72, v75, v74, s[66:67]\n"
    "v_mul_f32 v72, s62, v72\n"
    "v_mfma_f32_16x16x32_bf16 v[16:19], a[20:23], v[48:51], v[16:19]\n"
    "v_fma_f32 v56, v56, s62, -v72\n"
    "v_fma_f32 v57, v57, s62, -v72\n"
    "v_mfma_f32_16x16x32_bf16 v[20:23], a[20:23], v[52:55], v[20:23]\n"
    "v_fma_f32 v58, v58, s62, -v72\n"
    "v_fma_f32 v59, v59, s62, -v72\n"
    "v_mfma_f32_16x16x32_bf16 v[24:27], v[116:119], v[48:51], v[24:27]\n"
    "v_exp_f32 v56, v56\n"
    "v_mfma_f32_16x16x32_bf16 v[28:31], v[116:119], v[52:55], v[28:31]\n"
    "s_waitcnt lgkmcnt(2)\n"
    "v_exp_f32 v58, v58\n"
    "v_mfma_f32_16x16x32_bf16 v[0:3], a[116:119], v[32:35], v[0:3]\n"
    "ds_read_b128 v[48:51], v92 offset:4096\n"
    "ds_read_b128 v[52:55], v92 offset:5120\n"
    "v_mfma_f32_16x16x32_bf16 v[4:7], a[116:119], v[36:39], v[4:7]\n"
    "v_mfma_f32_16x16x32_bf16 v[8:11], a[196:199], v[32:35], v[8:11]\n"
    "buffer_load_dwordx2 v[108:109], off, s[0:3], s55 sc1\n"
    "v_mfma_f32_16x16x32_bf16 v[12:15], a[196:199], v[36:39], v[12:15]\n"
    "v_exp_f32 v57, v57\n"
    "v_mfma_f32_16x16x32_bf16 v[16:19], a[36:39], v[32:35], v[16:19]\n"
    "v_exp_f32 v59, v59\n"
    "v_mfma_f32_16x16x32_bf16 v[20:23], a[36:39], v[36:39], v[20:23]\n"
    "v_add_f32 v72, v56, v58\n"
    "v_add_f32 v73, v57, v59\n"
    "v_mfma_f32_16x16x32_bf16 v[24:27], v[132:135], v[32:35], v[24:27]\n"
    "v_add_f32 v72, v72, v73\n"
    "v_cndmask_b32 v74, 0, v72, s[66:67]\n"
    "v_mfma_f32_16x16x32_bf16 v[28:31], v[132:135], v[36:39], v[28:31]\n"
    "s_waitcnt lgkmcnt(2)\n"
    "v_cndmask_b32 v75, v72, 0, s[66:67]\n"
    "v_add_f32_dpp v74, v74, v74 row_ror:8 row_mask:0xf bank_mask:0xf\n"
    "v_mfma_f32_16x16x32_bf16 v[0:3], a[132:135], v[40:43], v[0:3]\n"
    "ds_read_b128 v[32:35], v93 offset:4096\n"
    "ds_read_b128 v[36:39], v93 offset:5120\n"
    "v_mfma_f32_16x16x32_bf16 v[4:7], a[132:135], v[44:47], v[4:7]\n"
    "v_add_f32_dpp v75, v75, v75 row_ror:8 row_mask:0xf bank_mask:0xf\n"
    "v_add_f32_dpp v74, v74, v74 row_ror:4 row_mask:0xf bank_mask:0xf\n"
    "v_mfma_f32_16x16x32_bf16 v[8:11], a[212:215], v[40:43], v[8:11]\n"
    "v_add_f32_dpp v75, v75, v75 row_ror:4 row_mask:0xf bank_mask:0xf\n"
    "v_add_f32_dpp v74, v74, v74 row_ror:2 row_mask:0xf bank_mask:0xf\n"
    "v_mfma_f32_16x16x32_bf16 v[12:15], a[212:215], v[44:47], v[12:15]\n"
    "v_add_f32_dpp v75, v75, v75 row_ror:2 row_mask:0xf bank_mask:0xf\n"
    "v_add_f32_dpp v74, v74, v74 row_ror:1 row_mask:0xf bank_mask:0xf\n"
    "v_mfma_f32_16x16x32_bf16 v[16:19], a[52:55], v[40:43], v[16:19]\n"
    "v_add_f32_dpp v75, v75, v75 row_ror:1 row_mask:0xf bank_mask:0xf\n"
    "v_cndmask_b32 v72, v75, v74, s[66:67]\n"
    "v_mfma_f32_16x16x32_bf16 v[20:23], a[52:55], v[44:47], v[20:23]\n"
    "v_rcp_f32 v72, v72\n"
    "v_mfma_f32_16x16x32_bf16 v[24:27], v[148:151], v[40:43], v[24:27]\n"
    "v_mul_f32 v56, v56, v72 clamp\n"
    "v_mul_f32 v57, v57, v72 clamp\n"
    "v_mfma_f32_16x16x32_bf16 v[28:31], v[148:151], v[44:47], v[28:31]\n"
    "s_waitcnt lgkmcnt(2)\n"
    "v_mul_f32 v58, v58, v72 clamp\n"
    "v_mul_f32 v59, v59, v72 clamp\n"
    "v_mfma_f32_16x16x32_bf16 v[0:3], a[88:91], v[48:51], v[0:3]\n"
    "ds_read_b128 v[40:43], v94 offset:4096\n"
    "ds_read_b128 v[44:47], v94 offset:5120\n"
    "v_mfma_f32_16x16x32_bf16 v[4:7], a[88:91], v[52:55], v[4:7]\n"
    "v_cvt_pk_bf16_f32 v56, v56, v57\n"
    "v_cvt_pk_bf16_f32 v57, v58, v59\n"
    "v_mfma_f32_16x16x32_bf16 v[8:11], a[168:171], v[48:51], v[8:11]\n"
    "buffer_store_dwordx2 v[56:57], v107, s[0:3], s50 offen sc1\n"
    ".Lqk_mkL3_%=:\n"
    "v_mfma_f32_16x16x32_bf16 v[12:15], a[168:171], v[52:55], v[12:15]\n"
    "s_cmp_eq_u32 s46, 63\n"
    "s_cbranch_scc1 .Lqk_end_%=\n"
    "v_mfma_f32_16x16x32_bf16 v[16:19], a[8:11], v[48:51], v[16:19]\n"
    "s_add_u32 s46, s46, 1\n"
    "s_add_u32 s60, s46, 1\n"
    "s_lshl_b32 s53, s60, 19\n"
    "v_mfma_f32_16x16x32_bf16 v[20:23], a[8:11], v[52:55], v[20:23]\n"
    "s_lshl_b32 s45, s60, 11\n"
    "s_xor_b32 s45, s45, s23\n"
    "s_and_b32 s45, s45, 0x2000\n"
    "v_mfma_f32_16x16x32_bf16 v[24:27], a[248:251], v[48:51], v[24:27]\n"
    "s_add_u32 s53, s53, s45\n"
    "s_add_u32 s53, s53, s27\n"
    "s_add_u32 s48, s46, 0\n"
    "v_mfma_f32_16x16x32_bf16 v[28:31], a[248:251], v[52:55], v[28:31]\n"
    "s_waitcnt lgkmcnt(2)\n"
    "s_and_b32 s48, s48, 3\n"
    "s_lshl_b32 s48, s48, 22\n"
    "v_mfma_f32_16x16x32_bf16 v[0:3], a[104:107], v[32:35], v[0:3]\n"
    "s_add_u32 s49, s46, 2\n"
    "ds_read_b128 v[48:51], v95 offset:4096\n"
    "ds_read_b128 v[52:55], v95 offset:5120\n"
    "v_mfma_f32_16x16x32_bf16 v[4:7], a[104:107], v[36:39], v[4:7]\n"
    "s_and_b32 s49, s49, 3\n"
    "s_lshl_b32 s49, s49, 22\n"
    "s_and_b32 s55, s46, 63\n"
    "v_mfma_f32_16x16x32_bf16 v[8:11], a[184:187], v[32:35], v[8:11]\n"
    "s_lshl_b32 s55, s55, 7\n"
    "s_mov_b32 s50, s48\n"
    "s_add_u32 s51, s24, s48\n"
    "v_mfma_f32_16x16x32_bf16 v[12:15], a[184:187], v[36:39], v[12:15]\n"
    "s_add_u32 s52, s25, s48\n"
    "s_add_u32 s56, s30, s55\n"
    "s_add_u32 s55, s31, s55\n"
    "v_mfma_f32_16x16x32_bf16 v[16:19], a[24:27], v[32:35], v[16:19]\n"
    "s_lshl_b32 s54, s60, 20\n"
    "s_lshl_b32 s45, s60, 12\n"
    "s_xor_b32 s45, s45, s23\n"
    "v_mfma_f32_16x16x32_bf16 v[20:23], a[24:27], v[36:39], v[20:23]\n"
    "s_and_b32 s45, s45, 0x2000\n"
    "s_add_u32 s54, s54, s45\n"
    "s_add_u32 s54, s54, s29\n"
    "v_mfma_f32_16x16x32_bf16 v[24:27], v[120:123], v[32:35], v[24:27]\n"
    "s_cmp_gt_i32 s60, 63\n"
    "s_cselect_b32 s53, 2130706432, s53\n"
    "s_cselect_b32 s54, 2130706432, s54\n"
    "v_mfma_f32_16x16x32_bf16 v[28:31], v[120:123], v[36:39], v[28:31]\n"
    "s_waitcnt lgkmcnt(2)\n"
    "s_waitcnt vmcnt(6) lgkmcnt(0)\n"
    "s_barrier\n"
    "v_mfma_f32_16x16x32_bf16 v[0:3], a[120:123], v[40:43], v[0:3]\n"
    "s_sub_u32 m0, s26, 8192\n"
    "ds_read_b128 v[32:35], v92 offset:6144\n"
    "ds_read_b128 v[36:39], v92 offset:7168\n"
    "v_mfma_f32_16x16x32_bf16 v[4:7], a[120:123], v[44:47], v[4:7]\n"
    "buffer_load_dwordx4 v96, s[0:3], s53 offen offset:0 sc1 lds\n"
    "v_mfma_f32_16x16x32_bf16 v[8:11], a[200:203], v[40:43], v[8:11]\n"
    ".Lqk_mkL4_%=:\n"
    "v_mfma_f32_16x16x32_bf16 v[12:15], a[200:203], v[44:47], v[12:15]\n"
    "v_mfma_f32_16x16x32_bf16 v[16:19], a[40:43], v[40:43], v[16:19]\n"
    "v_mfma_f32_16x16x32_bf16 v[20:23], a[40:43], v[44:47], v[20:23]\n"
    "buffer_load_dwordx4 v96, s[0:3], s53 offen offset:1024 sc1 lds\n"
    "v_mfma_f32_16x16x32_bf16 v[24:27], v[136:139], v[40:43], v[24:27]\n"
    "v_mfma_f32_16x16x32_bf16 v[28:31], v[136:139], v[44:47], v[28:31]\n"
    "s_waitcnt lgkmcnt(2)\n"
    "v_mfma_f32_16x16x32_bf16 v[0:3], a[136:139], v[48:51], v[0:3]\n"
    "ds_read_b128 v[40:43], v93 offset:6144\n"
    "ds_read_b128 v[44:47], v93 offset:7168\n"
    "v_mfma_f32_16x16x32_bf16 v[4:7], a[136:139], v[52:55], v[4:7]\n"
    "buffer_load_dwordx4 v96, s[0:3], s53 offen offset:2048 sc1 lds\n"
    "v_mfma_f32_16x16x32_bf16 v[8:11], a[216:219], v[48:51], v[8:11]\n"
    "v_mfma_f32_16x16x32_bf16 v[12:15], a[216:219], v[52:55], v[12:15]\n"
    "v_mfma_f32_16x16x32_bf16 v[16:19], a[56:59], v[48:51], v[16:19]\n"
    "v_mfma_f32_16x16x32_bf16 v[20:23], a[56:59], v[52:55], v[20:23]\n"
    "buffer_load_dwordx4 v96, s[0:3], s53 offen offset:3072 sc1 lds\n"
    "v_mfma_f32_16x16x32_bf16 v[24:27], v[152:155], v[48:51], v[24:27]\n"
    "s_sub_u32 m0, s26, 4096\n"
    "s_add_u32 s53, s53, 4096\n"
    "v_mfma_f32_16x16x32_bf16 v[28:31], v[152:155], v[52:55], v[28:31]\n"
    "s_waitcnt lgkmcnt(2)\n"
    "v_mfma_f32_16x16x32_bf16 v[0:3], a[92:95], v[32:35], v[0:3]\n"
    "ds_read_b128 v[48:51], v94 offset:6144\n"
    "ds_read_b128 v[52:55], v94 offset:7168\n"
    "v_mfma_f32_16x16x32_bf16 v[4:7], a[92:95], v[36:39], v[4:7]\n"
    "buffer_load_dwordx4 v96, s[0:3], s53 offen offset:0 sc1 lds\n"
    "v_mfma_f32_16x16x32_bf16 v[8:11], a[172:175], v[32:35], v[8:11]\n"
    "v_mfma_f32_16x16x32_bf16 v[12:15], a[172:175], v[36:39], v[12:15]\n"
    "v_mfma_f32_16x16x32_bf16 v[16:19], a[12:15], v[32:35], v[16:19]\n"
    "v_mfma_f32_16x16x32_bf16 v[20:23], a[12:15], v[36:39], v[20:23]\n"
    "buffer_load_dwordx4 v96, s[0:3], s53 offen offset:1024 sc1 lds\n"
    "v_mfma_f32_16x16x32_bf16 v[24:27], a[252:255], v[32:35], v[24:27]\n"
    "v_mfma_f32_16x16x32_bf16 v[28:31], a[252:255], v[36:39], v[28:31]\n"
    "s_waitcnt lgkmcnt(2)\n"
    "v_mfma_f32_16x16x32_bf16 v[0:3], a[108:111], v[40:43], v[0:3]\n"
    "ds_read_b128 v[32:35], v95 offset:6144\n"
    "ds_read_b128 v[36:39], v95 offset:7168\n"
    "v_mfma_f32_16x16x32_bf16 v[4:7], a[108:111], v[44:47], v[4:7]\n"
    "v_mfma_f32_16x16x32_bf16 v[8:11], a[188:191], v[40:43], v[8:11]\n"
    ".Lqk_mkL5_%=:\n"
    "v_mfma_f32_16x16x32_bf16 v[12:15], a[188:191], v[44:47], v[12:15]\n"
    "v_mfma_f32_16x16x32_bf16 v[16:19], a[28:31], v[40:43], v[16:19]\n"
    "v_mfma_f32_16x16x32_bf16 v[20:23], a[28:31], v[44:47], v[20:23]\n"
    "v_mfma_f32_16x16x32_bf16 v[24:27], v[124:127], v[40:43], v[24:27]\n"
    "v_mfma_f32_16x16x32_bf16 v[28:31], v[124:127], v[44:47], v[28:31]\n"
    "s_waitcnt lgkmcnt(2)\n"
    "v_mfma_f32_16x16x32_bf16 v[0:3], a[124:127], v[48:51], v[0:3]\n"
    "ds_read_b128 v[40:43], v96 offset:45056\n"
    "ds_read_b128 v[44:47], v96 offset:46080\n"
    "v_mfma_f32_16x16x32_bf16 v[4:7], a[124:127], v[52:55], v[4:7]\n"
    "v_mfma_f32_16x16x32_bf16 v[8:11], a[204:207], v[48:51], v[8:11]\n"
    "v_mfma_f32_16x16x32_bf16 v[12:15], a[204:207], v[52:55], v[12:15]\n"
    "v_mfma_f32_16x16x32_bf16 v[16:19], a[44:47], v[48:51], v[16:19]\n"
    "v_mfma_f32_16x16x32_bf16 v[20:23], a[44:47], v[52:55], v[20:23]\n"
    "v_mfma_f32_16x16x32_bf16 v[24:27], v[140:143], v[48:51], v[24:27]\n"
    "v_mfma_f32_16x16x32_bf16 v[28:31], v[140:143], v[52:55], v[28:31]\n"
    "s_waitcnt lgkmcnt(2)\n"
    "v_mfma_f32_16x16x32_bf16 v[0:3], a[140:143], v[32:35], v[0:3]\n"
    "ds_read_b128 v[48:51], v96 offset:47104\n"
    "ds_read_b128 v[52:55], v96 offset:48128\n"
    "v_mfma_f32_16x16x32_bf16 v[4:7], a[140:143], v[36:39], v[4:7]\n"
    "v_mfma_f32_16x16x32_bf16 v[8:11], a[220:223], v[32:35], v[8:11]\n"
    "v_mfma_f32_16x16x32_bf16 v[12:15], a[220:223], v[36:39], v[12:15]\n"
    "v_mfma_f32_16x16x32_bf16 v[16:19], a[60:63], v[32:35], v[16:19]\n"
    "v_mfma_f32_16x16x32_bf16 v[20:23], a[60:63], v[36:39], v[20:23]\n"
    "v_mfma_f32_16x16x32_bf16 v[24:27], v[156:159], v[32:35], v[24:27]\n"
    "v_mfma_f32_16x16x32_bf16 v[28:31], v[156:159], v[36:39], v[28:31]\n"
    "s_waitcnt lgkmcnt(2)\n"
    "v_mfma_f32_16x16x32_bf16 v[0:3], a[144:147], v[40:43], v[0:3]\n"
    "s_waitcnt vmcnt(7)\n"
    "ds_read_b128 v[32:35], v96 offset:49152\n"
    "ds_read_b128 v[36:39], v96 offset:50176\n"
    "s_memrealtime s[70:71]\n"
    "v_mfma_f32_16x16x32_bf16 v[4:7], a[144:147], v[44:47], v[4:7]\n"
    "v_readfirstlane_b32 s44, v108\n"
    "v_readfirstlane_b32 s45, v109\n"
    "v_mfma_f32_16x16x32_bf16 v[8:11], a[224:227], v[40:43], v[8:11]\n"
    "v_mfma_f32_16x16x32_bf16 v[12:15], a[224:227], v[44:47], v[12:15]\n"
    "s_cmp_eq_u32 s45, s47\n"
    "s_cselect_b32 s57, s44, s57\n"
    "s_add_u32 s57, s57, 264\n"
    "v_mfma_f32_16x16x32_bf16 v[16:19], a[64:67], v[40:43], v[16:19]\n"
    "s_sub_u32 s58, s57, 59\n"
    "s_mov_b32 s47, s60\n"
    "s_mov_b32 m0, s26\n"
    "v_mfma_f32_16x16x32_bf16 v[20:23], a[64:67], v[44:47], v[20:23]\n"
    ".Lqk_mkL6_%=:\n"
    "v_mfma_f32_16x16x32_bf16 v[24:27], v[160:163], v[40:43], v[24:27]\n"
    "s_sub_u32 s72, s58, s70\n"
    "v_mfma_f32_16x16x32_bf16 v[28:31], v[160:163], v[44:47], v[28:31]\n"
    "s_waitcnt lgkmcnt(2)\n"
    ".Lqk_mk6x19_%=:\n"
    "s_cmp_gt_i32 s72, 4\n"
    "s_cbranch_scc1 .Lqk_early_%=\n"
    "buffer_load_dwordx4 v96, s[0:3], s51 offen sc1 lds\n"
    "buffer_load_dwordx4 v96, s[0:3], s51 offen offset:1024 sc1 lds\n"
    ".Lqk_asked_%=:\n"
    ".Lqk_mk7x21_%=:\n"
    "v_mfma_f32_16x16x32_bf16 v[0:3], a[148:151], v[48:51], v[0:3]\n"
    "ds_read_b128 v[40:43], v96 offset:51200\n"
    "ds_read_b128 v[44:47], v96 offset:52224\n"
    "v_mfma_f32_16x16x32_bf16 v[4:7], a[148:151], v[52:55], v[4:7]\n"
    ".Lqk_mkM0_%=:\n"
    "v_mfma_f32_16x16x32_bf16 v[8:11], a[228:231], v[48:51], v[8:11]\n"
    "v_mfma_f32_16x16x32_bf16 v[12:15], a[228:231], v[52:55], v[12:15]\n"
    "v_mfma_f32_16x16x32_bf16 v[16:19], a[68:71], v[48:51], v[16:19]\n"
    "v_mfma_f32_16x16x32_bf16 v[20:23], a[68:71], v[52:55], v[20:23]\n"
    ".Lqk_mkM1_%=:\n"
    "v_mfma_f32_16x16x32_bf16 v[24:27], v[164:167], v[48:51], v[24:27]\n"
    "v_mfma_f32_16x16x32_bf16 v[28:31], v[164:167], v[52:55], v[28:31]\n"
    "s_waitcnt lgkmcnt(2)\n"
    "v_mfma_f32_16x16x32_bf16 v[0:3], a[152:155], v[32:35], v[0:3]\n"
    "buffer_load_dwordx4 v96, s[0:3], s52 offen offset:2048 sc1 lds\n"
    "v_mfma_f32_16x16x32_bf16 v[4:7], a[152:155], v[36:39], v[4:7]\n"
    "v_mfma_f32_16x16x32_bf16 v[8:11], a[232:235], v[32:35], v[8:11]\n"
    ".Lqk_mkM2_%=:\n"
    "v_mfma_f32_16x16x32_bf16 v[12:15], a[232:235], v[36:39], v[12:15]\n"
    "v_mfma_f32_16x16x32_bf16 v[16:19], a[72:75], v[32:35], v[16:19]\n"
    "v_mfma_f32_16x16x32_bf16 v[20:23], a[72:75], v[36:39], v[20:23]\n"
    "v_mfma_f32_16x16x32_bf16 v[24:27], v[168:171], v[32:35], v[24:27]\n"
    ".Lqk_mkM3_%=:\n"
    "v_mfma_f32_16x16x32_bf16 v[28:31], v[168:171], v[36:39], v[28:31]\n"
    "s_waitcnt lgkmcnt(0)\n"
    "v_mfma_f32_16x16x32_bf16 v[0:3], a[156:159], v[40:43], v[0:3]\n"
    "v_mfma_f32_16x16x32_bf16 v[4:7], a[156:159], v[44:47], v[4:7]\n"
    "v_mfma_f32_16x16x32_bf16 v[8:11], a[236:239], v[40:43], v[8:11]\n"
    "v_mfma_f32_16x16x32_bf16 v[12:15], a[236:239], v[44:47], v[12:15]\n"
    "v_mfma_f32_16x16x32_bf16 v[16:19], a[76:79], v[40:43], v[16:19]\n"
    "v_mfma_f32_16x16x32_bf16 v[20:23], a[76:79], v[44:47], v[20:23]\n"
    "v_mfma_f32_16x16x32_bf16 v[24:27], v[172:175], v[40:43], v[24:27]\n"
    ".Lqk_mkM4_%=:\n"
    "v_mfma_f32_16x16x32_bf16 v[28:31], v[172:175], v[44:47], v[28:31]\n"
    "ds_read_b128 v[32:35], v97 offset:0\n"
    "ds_read_b128 v[36:39], v97 offset:1024\n"
    "ds_read_b128 v[40:43], v97 offset:2048\n"
    "ds_read_b128 v[44:47], v97 offset:3072\n"
    "ds_read_b128 v[48:51], v97 offset:4096\n"
    "ds_read_b128 v[52:55], v97 offset:5120\n"
    "v_cvt_pk_bf16_f32 v68, v0, v1\n"
    "v_cvt_pk_bf16_f32 v69, v2, v3\n"
    "v_cvt_pk_bf16_f32 v70, v8, v9\n"
    "v_cvt_pk_bf16_f32 v71, v10, v11\n"
    "v_cvt_pk_bf16_f32 v72, v4, v5\n"
    "v_cvt_pk_bf16_f32 v73, v6, v7\n"
    "v_cvt_pk_bf16_f32 v74, v12, v13\n"
    "v_cvt_pk_bf16_f32 v75, v14, v15\n"
    "v_mfma_f32_16x16x32_bf16 v[108:111], v[68:71], v[84:87], 0\n"
    "v_cvt_pk_bf16_f32 v88, v24, v25\n"
    "v_cvt_pk_bf16_f32 v89, v26, v27\n"
    "v_cvt_pk_bf16_f32 v90, v28, v29\n"
    "v_cvt_pk_bf16_f32 v91, v30, v31\n"
    "v_mfma_f32_16x16x32_bf16 v[24:27], v[72:75], v[84:87], 0\n"
    "v_cvt_pk_bf16_f32 v76, v16, v17\n"
    "v_cvt_pk_bf16_f32 v77, v18, v19\n"
    "v_cvt_pk_bf16_f32 v78, v20, v21\n"
    "v_cvt_pk_bf16_f32 v79, v22, v23\n"
    "v_mfma_f32_16x16x32_bf16 v[28:31], v[68:71], v[80:83], 0\n"
    "v_mov_b64 v[0:1], 0\n"
    "v_mov_b64 v[2:3], 0\n"
    "v_mfma_f32_16x16x16_bf16 v[108:111], v[76:77], v[90:91], v[108:111]\n"
    "v_mov_b64 v[4:5], 0\n"
    "v_mov_b64 v[6:7], 0\n"
    "v_mfma_f32_16x16x16_bf16 v[24:27], v[78:79], v[90:91], v[24:27]\n"
    "v_mov_b64 v[8:9], 0\n"
    "v_mov_b64 v[10:11], 0\n"
    "v_mfma_f32_16x16x16_bf16 v[28:31], v[76:77], v[88:89], v[28:31]\n"
    "v_mov_b64 v[12:13], 0\n"
    "v_mov_b64 v[14:15], 0\n"
    "v_mov_b64 v[16:17], 0\n"
    "v_mov_b64 v[18:19], 0\n"
    "v_mov_b64 v[20:21], 0\n"
    "v_mov_b64 v[22:23], 0\n"
    "ds_write_b128 v104, v[108:111]\n"
    "ds_write_b128 v104, v[24:27] offset:64\n"
    "ds_write_b128 v104, v[28:31] offset:160\n"
    ".Lqk_mk8x22_%=:\n"
    "s_branch .Lqk_take_%=\n"
    ".Lqk_tk0_%=:\n"
    ".Lqk_po3_%=:\n"
    "buffer_load_dword v109, v108, s[0:3], 0 offen sc1\n"
    "s_waitcnt vmcnt(0)\n"
    "v_cmp_gt_i32 vcc, 32, v109\n"
    "s_nop 0\n"
    "s_cbranch_vccz .Lqk_pd4_%=\n"
    "s_add_u32 s40, s40, 1\n"
    "s_cmp_lt_u32 s40, 0x40000\n"
    "s_cbranch_scc1 .Lqk_sp5_%=\n"
    "v_mov_b32 v109, 90\n"
    "s_mov_b32 s44, 33728\n"
    "buffer_store_dword v109, off, s[0:3], s44 sc1\n"
    "s_waitcnt vmcnt(0)\n"
    "s_branch .Lqk_pd4_%=\n"
    ".Lqk_sp5_%=:\n"
    "s_sleep 8\n"
    "s_branch .Lqk_po3_%=\n"
    ".Lqk_pd4_%=:\n"
    "s_branch .Lqk_tk1_%=\n"
    ".Lqk_early_%=:\n"
    ".Lqk_rt21_%=:\n"
    "s_memrealtime s[42:43]\n"
    "s_waitcnt lgkmcnt(0)\n"
    "s_sub_u32 s44, s58, s42\n"
    "s_cmp_lt_u32 s44, 600\n"
    "s_cbranch_scc1 .Lqk_rt21_%=\n"
    "buffer_load_dwordx4 v96, s[0:3], s51 offen sc1 lds\n"
    "buffer_load_dwordx4 v96, s[0:3], s51 offen offset:1024 sc1 lds\n"
    "s_branch .Lqk_asked_%=\n"
    ".Lqk_retry_%=:\n"
    "s_add_u32 s40, s40, 1\n"
    "s_cmp_lt_u32 s40, 0x40000\n"
    "s_cbranch_scc1 .Lqk_sp24_%=\n"
    "v_mov_b32 v108, 1\n"
    "s_mov_b32 s44, 33728\n"
    "buffer_store_dword v108, off, s[0:3], s44 sc1\n"
    "s_waitcnt vmcnt(0)\n"
    "s_branch .Lqk_go_%=\n"
    ".Lqk_sp24_%=:\n"
    "s_waitcnt lgkmcnt(0)\n"
    "s_nop 7\n"
    "ds_read_b128 v[32:35], v97 offset:0\n"
    "ds_read_b128 v[36:39], v97 offset:1024\n"
    "v_mov_b64 v[0:1], 0\n"
    "v_mov_b64 v[2:3], 0\n"
    "v_mov_b64 v[4:5], 0\n"
    "v_mov_b64 v[6:7], 0\n"
    "v_mov_b64 v[8:9], 0\n"
    "v_mov_b64 v[10:11], 0\n"
    "v_mov_b64 v[12:13], 0\n"
    "v_mov_b64 v[14:15], 0\n"
    "v_mov_b64 v[16:17], 0\n"
    "v_mov_b64 v[18:19], 0\n"
    "v_mov_b64 v[20:21], 0\n"
    "v_mov_b64 v[22:23], 0\n"
    "buffer_load_dwordx4 v96, s[0:3], s51 offen sc1 lds\n"
    "buffer_load_dwordx4 v96, s[0:3], s51 offen offset:1024 sc1 lds\n"
    "buffer_load_dwordx4 v96, s[0:3], s52 offen offset:2048 sc1 lds\n"
    ".Lqk_r1_%=:\n"
    "s_waitcnt vmcnt(0)\n"
    "s_branch .Lqk_take_%=\n"
    ".Lqk_r2_%=:\n"
    "s_add_u32 s40, s40, 1\n"
    "s_cmp_lt_u32 s40, 0x40000\n"
    "s_cbranch_scc1 .Lqk_sp25_%=\n"
    "v_mov_b32 v108, 5\n"
    "s_mov_b32 s44, 33728\n"
    "buffer_store_dword v108, off, s[0:3], s44 sc1\n"
    "s_waitcnt vmcnt(0)\n"
    "s_branch .Lqk_go2_%=\n"
    ".Lqk_sp25_%=:\n"
    "buffer_load_dwordx4 v96, s[0:3], s52 offen offset:2048 sc1 lds\n"
    ".Lqk_r3_%=:\n"
    "s_waitcnt vmcnt(0)\n"
    "ds_read_b128 v[64:67], v92 offset:10240\n"
    "s_waitcnt lgkmcnt(0)\n"
    "v_max_u32 v108, v64, v66\n"
    "v_cmp_eq_u32 vcc, -1, v108\n"
    "s_nop 0\n"
    "s_cbranch_vccnz .Lqk_r2_%=\n"
    "s_branch .Lqk_go2_%=\n"
    ".Lqk_end_%=:\n"
    "s_waitcnt vmcnt(0) lgkmcnt(0)\n"
    "s_endpgm\n"
    :: "{s[0:3]}"(s0_), "{s[4:7]}"(s4_), "{s[8:11]}"(s8_), "{s[12:15]}"(s12_), "{s[16:19]}"(s16_), "{s[20:23]}"(s20_), "{s[24:27]}"(s24_), "{s[28:31]}"(s28_), "{s[32:35]}"(s32_), "{s[36:39]}"(s36_), "{v[0:3]}"(v0_), "{v[4:7]}"(v4_), "{v[92:95]}"(v92_), "{v[96:99]}"(v96_), "{v[100:103]}"(v100_), "{v[104:107]}"(v104_) : "memory", "v255", "a255", "s95");
    __builtin_unreachable();
}
DEV void vo_role(const MegaArgs& A, const int L, const int h, const int rep) {
    ROLE_HEAD;
    const unsigned um = xl(RG_UM + ((8 * w + lq) * 4 + (h >> 3)) * 2048 + (h & 7) * 256 + l16 * 16, L);
#define XU 0
    const unsigned umx = XU ? OOB : xl(RG_UMX + ((4 * (2 * w + (lq & 1)) + (l16 >> 2)) * 4 + (h >> 3)) * 512 + (h & 7) * 64 + (l & 3) * 16 + (lq >> 1) * 8, L);
    const u32x4 s0_ = {RFL(rx[0]), RFL(rx[1]), RFL(rx[2]), RFL(rx[3])};
    const u32x4 s4_ = {RFL(lo(A.Wo + h * 192 * 640)), RFL(hi(A.Wo + h * 192 * 640)), RFL(192u * 1280u), RFL(0x20000u)};
    const u32x4 s8_ = {RFL(lo(A.Wv + h * 192)), RFL(hi(A.Wv + h * 192)), RFL(WBYTES - h * 384), RFL(0x20000u)};
    const u32x4 s12_ = {RFL(w), RFL(L << 13), RFL(L), RFL(xl(RG_MEM + rep * 16384, L) + w * 2048)};
    const u32x4 s16_ = {RFL(w < 2 ? xl(RG_MEM + rep * 16384 + 8192, L) + w * 1024 - 2048 : OOB), RFL(w * 13312 + 8192), RFL(REC0 + w * 16384), RFL(65536 - 4096 - w * 14336)};
    const u32x4 s20_ = {RFL(QT0 + h * 32768 + 16384 + w * 1024), RFL(146432 + w * 1024), RFL(xl(RG_UT + (32 * w + (h >> 3)) * 8192 + (h & 7) * 1024, L)), RFL(xl(Z_X + 8192, L))};
    const u32x4 s24_ = {RFL(xl(Z_CT + (h & 7) * 256, L)), RFL(w == 0 ? xl(Z_QV + (32 + h) * 4, L) : OOB), RFL(DBG0 + ((L * 128 + 32 + h) * 4 + w) * 2048), RFL(0u)};
    const u32x4 v0_ = {(unsigned)(((3 * w + 0) * 64 + l) / 24 * 12288 + ((((3 * w + 0) * 64 + l) % 24) ^ ((((3 * w + 0) * 64 + l) / 48) & 6)) * 16 - 0), (unsigned)(((3 * w + 1) * 64 + l) / 24 * 12288 + ((((3 * w + 1) * 64 + l) % 24) ^ ((((3 * w + 1) * 64 + l) / 48) & 6)) * 16 - 1024), (unsigned)(((3 * w + 2) * 64 + l) / 24 * 12288 + ((((3 * w + 2) * 64 + l) % 24) ^ ((((3 * w + 2) * 64 + l) / 48) & 6)) * 16 - 2048), (unsigned)((8 * lq + ((l >> 2) & 1) + ((l >> 1) & 4)) * 384 + (w ^ (((8 * lq + ((l >> 2) & 1) + ((l >> 1) & 4)) >> 2) & 3)) * 32 + (l & 3) * 8 + ((w + 0) & 3) * 12288)};
    const u32x4 v4_ = {(unsigned)((8 * lq + ((l >> 2) & 1) + ((l >> 1) & 4)) * 384 + (w ^ (((8 * lq + ((l >> 2) & 1) + ((l >> 1) & 4)) >> 2) & 3)) * 32 + (l & 3) * 8 + ((w + 1) & 3) * 12288), (unsigned)((8 * lq + ((l >> 2) & 1) + ((l >> 1) & 4)) * 384 + (w ^ (((8 * lq + ((l >> 2) & 1) + ((l >> 1) & 4)) >> 2) & 3)) * 32 + (l & 3) * 8 + ((w + 2) & 3) * 12288), (unsigned)((8 * lq + ((l >> 2) & 1) + ((l >> 1) & 4)) * 384 + (w ^ (((8 * lq + ((l >> 2) & 1) + ((l >> 1) & 4)) >> 2) & 3)) * 32 + (l & 3) * 8 + ((w + 3) & 3) * 12288), (unsigned)((8 * lq + ((l >> 2) & 1) + ((l >> 1) & 4)) * 384 + (w ^ (((8 * lq + ((l >> 2) & 1) + ((l >> 1) & 4)) >> 2) & 3)) * 32 + (l & 3) * 8)};
    const u32x4 v8_ = {(unsigned)(0), (unsigned)(0), (unsigned)(0), (unsigned)(0)};
    const u32x4 v12_ = {(unsigned)((4 * 0 + lq) * 1280 + 256 * w + ((l16 ^ (2 * ((4 * 0 + lq) & 7))) * 16) - 0), (unsigned)((4 * 1 + lq) * 1280 + 256 * w + ((l16 ^ (2 * ((4 * 1 + lq) & 7))) * 16) - 1024), (unsigned)((4 * 2 + lq) * 1280 + 256 * w + ((l16 ^ (2 * ((4 * 2 + lq) & 7))) * 16) - 2048), (unsigned)((4 * 3 + lq) * 1280 + 256 * w + ((l16 ^ (2 * ((4 * 3 + lq) & 7))) * 16) - 3072)};
    const u32x4 v16_ = {(unsigned)((4 * w + lq) * 1280 + 1024 + ((l16 ^ (2 * ((4 * w + lq) & 7))) * 16)), (unsigned)(w * 8192 + (l >> 2) * 256 + ((l >> 2) & 7) * 32 + (l & 3) * 8), (unsigned)(32768 + (l >> 2) * 256 + (((l >> 2) & 7) ^ (2 * w)) * 32 + (l & 3) * 8), (unsigned)(0u)};
    const u32x4 v92_ = {(unsigned)(l * 16 + ((w + 0) & 3) * 13312), (unsigned)(l * 16 + ((w + 1) & 3) * 13312), (unsigned)(l * 16 + ((w + 2) & 3) * 13312), (unsigned)(l * 16 + ((w + 3) & 3) * 13312)};
    const u32x4 v96_ = {(unsigned)(l * 16), (unsigned)(l * 16 + 64512 + w * 20480), (unsigned)((l & 2) ? 0xeeeeu : 0x4444u), (unsigned)(61440 + 512 * w + (l16 >> 2) * 64 + (l & 1) * 32 + sw2(lq) * 8 + (l & 2) * 2)};
    const u32x4 v100_ = {(unsigned)(61440 + 2048 + 256 * w + (l16 >> 2) * 64 + (l & 1) * 32 + sw2(lq) * 8 + (l & 2) * 2), (unsigned)(61440 + 512 * (lq >> 1) + 128 * (lq & 1) + (l & 4) * 8 + (l & 8) * 32 + (l & 3) * 8), (unsigned)(um), (unsigned)(umx)};
    const u32x4 v104_ = {(unsigned)(xl(RG_UTX + ((8 * w + (l16 >> 2)) * 4 + (h >> 3)) * 2048 + (h & 1) * 1024 + ((h & 7) >> 1) * 256 + lq * 64 + (l & 3) * 16, L)), (unsigned)(l < 4 || l > 8 ? xl(Z_QW + h * 128 + (l & 3) * 4, L) : (L ? xl(Z_XN + rep * 2048 + (l - 4) * 128, 0) : OOB)), (unsigned)(146432 + l * 16), (unsigned)(148480 + l * 16)};
    asm volatile(
    "s_mov_b32 s28, 0\n"
    "s_mov_b64 s[30:31], 0\n"
    "s_mov_b32 s33, 0\n"
    "s_cmp_eq_u32 s13, 0\n"
    "s_cselect_b32 s32, 0, 28\n"
    "s_sub_u32 s32, s32, 0\n"
    ".Lvo_ho1_%=:\n"
    "s_sub_u32 s32, s32, 1\n"
    "s_cbranch_scc1 .Lvo_hd2_%=\n"
    "s_sleep 17\n"
    "s_branch .Lvo_ho1_%=\n"
    ".Lvo_hd2_%=:\n"
    "s_mul_i32 s41, s12, 3072\n"
    "s_lshl_b32 s42, s12, 13\n"
    "s_lshl_b32 s43, s12, 10\n"
    "s_mov_b32 s49, 0\n"
    "s_add_u32 m0, s42, 0\n"
    "s_nop 0\n"
    "buffer_load_dwordx4 v12, s[4:7], s49 offen offset:0 nt lds\n"
    "buffer_load_dwordx4 v13, s[4:7], s49 offen offset:1024 nt lds\n"
    "buffer_load_dwordx4 v14, s[4:7], s49 offen offset:2048 nt lds\n"
    "buffer_load_dwordx4 v15, s[4:7], s49 offen offset:3072 nt lds\n"
    "s_add_u32 m0, s43, 32768\n"
    "s_nop 0\n"
    "buffer_load_dwordx4 v16, s[4:7], s49 offen nt lds\n"
    "s_mov_b32 s49, 81920\n"
    "s_add_u32 m0, s42, 4096\n"
    "s_nop 0\n"
    "buffer_load_dwordx4 v12, s[4:7], s49 offen offset:0 nt lds\n"
    "buffer_load_dwordx4 v13, s[4:7], s49 offen offset:1024 nt lds\n"
    "buffer_load_dwordx4 v14, s[4:7], s49 offen offset:2048 nt lds\n"
    "buffer_load_dwordx4 v15, s[4:7], s49 offen offset:3072 nt lds\n"
    "s_add_u32 m0, s43, 36864\n"
    "s_nop 0\n"
    "buffer_load_dwordx4 v16, s[4:7], s49 offen nt lds\n"
    "s_mov_b32 s49, 20480\n"
    "s_add_u32 m0, s42, 49152\n"
    "s_nop 0\n"
    "buffer_load_dwordx4 v12, s[4:7], s49 offen offset:0 nt lds\n"
    "buffer_load_dwordx4 v13, s[4:7], s49 offen offset:1024 nt lds\n"
    "buffer_load_dwordx4 v14, s[4:7], s49 offen offset:2048 nt lds\n"
    "buffer_load_dwordx4 v15, s[4:7], s49 offen offset:3072 nt lds\n"
    "s_add_u32 m0, s43, 81920\n"
    "s_nop 0\n"
    "buffer_load_dwordx4 v16, s[4:7], s49 offen nt lds\n"
    "s_mov_b32 s49, 102400\n"
    "s_add_u32 m0, s42, 53248\n"
    "s_nop 0\n"
    "buffer_load_dwordx4 v12, s[4:7], s49 offen offset:0 nt lds\n"
    "buffer_load_dwordx4 v13, s[4:7], s49 offen offset:1024 nt lds\n"
    "buffer_load_dwordx4 v14, s[4:7], s49 offen offset:2048 nt lds\n"
    "buffer_load_dwordx4 v15, s[4:7], s49 offen offset:3072 nt lds\n"
    "s_add_u32 m0, s43, 86016\n"
    "s_nop 0\n"
    "buffer_load_dwordx4 v16, s[4:7], s49 offen nt lds\n"
    "s_mov_b32 s49, 40960\n"
    "s_add_u32 m0, s42, 98304\n"
    "s_nop 0\n"
    "buffer_load_dwordx4 v12, s[4:7], s49 offen offset:0 nt lds\n"
    "buffer_load_dwordx4 v13, s[4:7], s49 offen offset:1024 nt lds\n"
    "buffer_load_dwordx4 v14, s[4:7], s49 offen offset:2048 nt lds\n"
    "buffer_load_dwordx4 v15, s[4:7], s49 offen offset:3072 nt lds\n"
    "s_add_u32 m0, s43, 131072\n"
    "s_nop 0\n"
    "buffer_load_dwordx4 v16, s[4:7], s49 offen nt lds\n"
    "s_mov_b32 s49, 122880\n"
    "s_add_u32 m0, s42, 102400\n"
    "s_nop 0\n"
    "buffer_load_dwordx4 v12, s[4:7], s49 offen offset:0 nt lds\n"
    "buffer_load_dwordx4 v13, s[4:7], s49 offen offset:1024 nt lds\n"
    "buffer_load_dwordx4 v14, s[4:7], s49 offen offset:2048 nt lds\n"
    "buffer_load_dwordx4 v15, s[4:7], s49 offen offset:3072 nt lds\n"
    "s_add_u32 m0, s43, 135168\n"
    "s_nop 0\n"
    "buffer_load_dwordx4 v16, s[4:7], s49 offen nt lds\n"
    "v_mov_b64 v[108:109], -1\n"
    "v_mov_b64 v[110:111], -1\n"
    "s_mov_b32 s49, 0x10000\n"
    "buffer_store_dwordx4 v[108:111], v102, s[0:3], 0 offen sc1\n"
    "buffer_store_dwordx4 v[108:111], v102, s[0:3], s49 offen sc1\n"
    "buffer_store_dwordx2 v[108:109], v103, s[0:3], 0 offen sc1\n"
    "s_waitcnt vmcnt(23)\n"
    "s_barrier\n"
    "v_add_u32 v24, 0, v17\n"
    "v_add_u32 v32, 0, v18\n"
    "v_xor_b32 v33, 32, v32\n"
    "v_xor_b32 v25, 32, v24\n"
    "v_xor_b32 v26, 64, v24\n"
    "v_xor_b32 v27, 96, v24\n"
    "v_xor_b32 v28, 128, v24\n"
    "v_xor_b32 v29, 160, v24\n"
    "v_xor_b32 v30, 192, v24\n"
    "v_xor_b32 v31, 224, v24\n"
    "ds_read_b64_tr_b16 a[0:1], v24 offset:0\n"
    "ds_read_b64_tr_b16 a[2:3], v24 offset:4096\n"
    "ds_read_b64_tr_b16 a[24:25], v25 offset:0\n"
    "ds_read_b64_tr_b16 a[26:27], v25 offset:4096\n"
    "ds_read_b64_tr_b16 a[48:49], v26 offset:0\n"
    "ds_read_b64_tr_b16 a[50:51], v26 offset:4096\n"
    "ds_read_b64_tr_b16 a[72:73], v27 offset:0\n"
    "ds_read_b64_tr_b16 a[74:75], v27 offset:4096\n"
    "ds_read_b64_tr_b16 a[96:97], v28 offset:0\n"
    "ds_read_b64_tr_b16 a[98:99], v28 offset:4096\n"
    "ds_read_b64_tr_b16 a[120:121], v29 offset:0\n"
    "ds_read_b64_tr_b16 a[122:123], v29 offset:4096\n"
    "ds_read_b64_tr_b16 a[144:145], v30 offset:0\n"
    "ds_read_b64_tr_b16 a[146:147], v30 offset:4096\n"
    "ds_read_b64_tr_b16 a[168:169], v31 offset:0\n"
    "ds_read_b64_tr_b16 a[170:171], v31 offset:4096\n"
    "ds_read_b64_tr_b16 a[192:193], v32 offset:0\n"
    "ds_read_b64_tr_b16 a[194:195], v32 offset:4096\n"
    "ds_read_b64_tr_b16 a[216:217], v33 offset:0\n"
    "ds_read_b64_tr_b16 a[218:219], v33 offset:4096\n"
    "s_waitcnt lgkmcnt(0)\n"
    "s_barrier\n"
    "s_mov_b32 s49, 61440\n"
    "s_add_u32 m0, s42, 0\n"
    "s_nop 0\n"
    "buffer_load_dwordx4 v12, s[4:7], s49 offen offset:0 nt lds\n"
    "buffer_load_dwordx4 v13, s[4:7], s49 offen offset:1024 nt lds\n"
    "buffer_load_dwordx4 v14, s[4:7], s49 offen offset:2048 nt lds\n"
    "buffer_load_dwordx4 v15, s[4:7], s49 offen offset:3072 nt lds\n"
    "s_add_u32 m0, s43, 32768\n"
    "s_nop 0\n"
    "buffer_load_dwordx4 v16, s[4:7], s49 offen nt lds\n"
    "s_mov_b32 s49, 143360\n"
    "s_add_u32 m0, s42, 4096\n"
    "s_nop 0\n"
    "buffer_load_dwordx4 v12, s[4:7], s49 offen offset:0 nt lds\n"
    "buffer_load_dwordx4 v13, s[4:7], s49 offen offset:1024 nt lds\n"
    "buffer_load_dwordx4 v14, s[4:7], s49 offen offset:2048 nt lds\n"
    "buffer_load_dwordx4 v15, s[4:7], s49 offen offset:3072 nt lds\n"
    "s_add_u32 m0, s43, 36864\n"
    "s_nop 0\n"
    "buffer_load_dwordx4 v16, s[4:7], s49 offen nt lds\n"
    "s_waitcnt vmcnt(23)\n"
    "s_barrier\n"
    "v_add_u32 v24, 49152, v17\n"
    "v_add_u32 v32, 49152, v18\n"
    "v_xor_b32 v33, 32, v32\n"
    "v_xor_b32 v25, 32, v24\n"
    "v_xor_b32 v26, 64, v24\n"
    "v_xor_b32 v27, 96, v24\n"
    "v_xor_b32 v28, 128, v24\n"
    "v_xor_b32 v29, 160, v24\n"
    "v_xor_b32 v30, 192, v24\n"
    "v_xor_b32 v31, 224, v24\n"
    "ds_read_b64_tr_b16 a[4:5], v24 offset:0\n"
    "ds_read_b64_tr_b16 a[6:7], v24 offset:4096\n"
    "ds_read_b64_tr_b16 a[28:29], v25 offset:0\n"
    "ds_read_b64_tr_b16 a[30:31], v25 offset:4096\n"
    "ds_read_b64_tr_b16 a[52:53], v26 offset:0\n"
    "ds_read_b64_tr_b16 a[54:55], v26 offset:4096\n"
    "ds_read_b64_tr_b16 a[76:77], v27 offset:0\n"
    "ds_read_b64_tr_b16 a[78:79], v27 offset:4096\n"
    "ds_read_b64_tr_b16 a[100:101], v28 offset:0\n"
    "ds_read_b64_tr_b16 a[102:103], v28 offset:4096\n"
    "ds_read_b64_tr_b16 a[124:125], v29 offset:0\n"
    "ds_read_b64_tr_b16 a[126:127], v29 offset:4096\n"
    "ds_read_b64_tr_b16 a[148:149], v30 offset:0\n"
    "ds_read_b64_tr_b16 a[150:151], v30 offset:4096\n"
    "ds_read_b64_tr_b16 a[172:173], v31 offset:0\n"
    "ds_read_b64_tr_b16 a[174:175], v31 offset:4096\n"
    "ds_read_b64_tr_b16 a[196:197], v32 offset:0\n"
    "ds_read_b64_tr_b16 a[198:199], v32 offset:4096\n"
    "ds_read_b64_tr_b16 a[220:221], v33 offset:0\n"
    "ds_read_b64_tr_b16 a[222:223], v33 offset:4096\n"
    "s_waitcnt lgkmcnt(0)\n"
    "s_barrier\n"
    "s_mov_b32 s49, 163840\n"
    "s_add_u32 m0, s42, 49152\n"
    "s_nop 0\n"
    "buffer_load_dwordx4 v12, s[4:7], s49 offen offset:0 nt lds\n"
    "buffer_load_dwordx4 v13, s[4:7], s49 offen offset:1024 nt lds\n"
    "buffer_load_dwordx4 v14, s[4:7], s49 offen offset:2048 nt lds\n"
    "buffer_load_dwordx4 v15, s[4:7], s49 offen offset:3072 nt lds\n"
    "s_add_u32 m0, s43, 81920\n"
    "s_nop 0\n"
    "buffer_load_dwordx4 v16, s[4:7], s49 offen nt lds\n"
    "s_mov_b32 s49, 184320\n"
    "s_add_u32 m0, s42, 53248\n"
    "s_nop 0\n"
    "buffer_load_dwordx4 v12, s[4:7], s49 offen offset:0 nt lds\n"
    "buffer_load_dwordx4 v13, s[4:7], s49 offen offset:1024 nt lds\n"
    "buffer_load_dwordx4 v14, s[4:7], s49 offen offset:2048 nt lds\n"
    "buffer_load_dwordx4 v15, s[4:7], s49 offen offset:3072 nt lds\n"
    "s_add_u32 m0, s43, 86016\n"
    "s_nop 0\n"
    "buffer_load_dwordx4 v16, s[4:7], s49 offen nt lds\n"
    "v_and_b32 v108, 0x70, v96\n"
    "v_lshlrev_b32 v108, 2, v108\n"
    "v_or_b32 v108, 0x8000, v108\n"
    "buffer_load_dword v109, v108, s[0:3], 0 offen sc1\n"
    "s_waitcnt vmcnt(24)\n"
    "s_barrier\n"
    "v_add_u32 v24, 98304, v17\n"
    "v_add_u32 v32, 98304, v18\n"
    "v_xor_b32 v33, 32, v32\n"
    "v_xor_b32 v25, 32, v24\n"
    "v_xor_b32 v26, 64, v24\n"
    "v_xor_b32 v27, 96, v24\n"
    "v_xor_b32 v28, 128, v24\n"
    "v_xor_b32 v29, 160, v24\n"
    "v_xor_b32 v30, 192, v24\n"
    "v_xor_b32 v31, 224, v24\n"
    "ds_read_b64_tr_b16 a[8:9], v24 offset:0\n"
    "ds_read_b64_tr_b16 a[10:11], v24 offset:4096\n"
    "ds_read_b64_tr_b16 a[32:33], v25 offset:0\n"
    "ds_read_b64_tr_b16 a[34:35], v25 offset:4096\n"
    "ds_read_b64_tr_b16 a[56:57], v26 offset:0\n"
    "ds_read_b64_tr_b16 a[58:59], v26 offset:4096\n"
    "ds_read_b64_tr_b16 a[80:81], v27 offset:0\n"
    "ds_read_b64_tr_b16 a[82:83], v27 offset:4096\n"
    "ds_read_b64_tr_b16 a[104:105], v28 offset:0\n"
    "ds_read_b64_tr_b16 a[106:107], v28 offset:4096\n"
    "ds_read_b64_tr_b16 a[128:129], v29 offset:0\n"
    "ds_read_b64_tr_b16 a[130:131], v29 offset:4096\n"
    "ds_read_b64_tr_b16 a[152:153], v30 offset:0\n"
    "ds_read_b64_tr_b16 a[154:155], v30 offset:4096\n"
    "ds_read_b64_tr_b16 a[176:177], v31 offset:0\n"
    "ds_read_b64_tr_b16 a[178:179], v31 offset:4096\n"
    "ds_read_b64_tr_b16 a[200:201], v32 offset:0\n"
    "ds_read_b64_tr_b16 a[202:203], v32 offset:4096\n"
    "ds_read_b64_tr_b16 a[224:225], v33 offset:0\n"
    "ds_read_b64_tr_b16 a[226:227], v33 offset:4096\n"
    "s_waitcnt lgkmcnt(0)\n"
    "s_barrier\n"
    "s_mov_b32 s49, 204800\n"
    "s_add_u32 m0, s42, 98304\n"
    "s_nop 0\n"
    "buffer_load_dwordx4 v12, s[4:7], s49 offen offset:0 nt lds\n"
    "buffer_load_dwordx4 v13, s[4:7], s49 offen offset:1024 nt lds\n"
    "buffer_load_dwordx4 v14, s[4:7], s49 offen offset:2048 nt lds\n"
    "buffer_load_dwordx4 v15, s[4:7], s49 offen offset:3072 nt lds\n"
    "s_add_u32 m0, s43, 131072\n"
    "s_nop 0\n"
    "buffer_load_dwordx4 v16, s[4:7], s49 offen nt lds\n"
    "s_mov_b32 s49, 225280\n"
    "s_add_u32 m0, s42, 102400\n"
    "s_nop 0\n"
    "buffer_load_dwordx4 v12, s[4:7], s49 offen offset:0 nt lds\n"
    "buffer_load_dwordx4 v13, s[4:7], s49 offen offset:1024 nt lds\n"
    "buffer_load_dwordx4 v14, s[4:7], s49 offen offset:2048 nt lds\n"
    "buffer_load_dwordx4 v15, s[4:7], s49 offen offset:3072 nt lds\n"
    "s_add_u32 m0, s43, 135168\n"
    "s_nop 0\n"
    "buffer_load_dwordx4 v16, s[4:7], s49 offen nt lds\n"
    "s_waitcnt vmcnt(21)\n"
    "s_barrier\n"
    "v_add_u32 v24, 0, v17\n"
    "v_add_u32 v32, 0, v18\n"
    "v_xor_b32 v33, 32, v32\n"
    "v_xor_b32 v25, 32, v24\n"
    "v_xor_b32 v26, 64, v24\n"
    "v_xor_b32 v27, 96, v24\n"
    "v_xor_b32 v28, 128, v24\n"
    "v_xor_b32 v29, 160, v24\n"
    "v_xor_b32 v30, 192, v24\n"
    "v_xor_b32 v31, 224, v24\n"
    "ds_read_b64_tr_b16 a[12:13], v24 offset:0\n"
    "ds_read_b64_tr_b16 a[14:15], v24 offset:4096\n"
    "ds_read_b64_tr_b16 a[36:37], v25 offset:0\n"
    "ds_read_b64_tr_b16 a[38:39], v25 offset:4096\n"
    "ds_read_b64_tr_b16 a[60:61], v26 offset:0\n"
    "ds_read_b64_tr_b16 a[62:63], v26 offset:4096\n"
    "ds_read_b64_tr_b16 a[84:85], v27 offset:0\n"
    "ds_read_b64_tr_b16 a[86:87], v27 offset:4096\n"
    "ds_read_b64_tr_b16 a[108:109], v28 offset:0\n"
    "ds_read_b64_tr_b16 a[110:111], v28 offset:4096\n"
    "ds_read_b64_tr_b16 a[132:133], v29 offset:0\n"
    "ds_read_b64_tr_b16 a[134:135], v29 offset:4096\n"
    "ds_read_b64_tr_b16 a[156:157], v30 offset:0\n"
    "ds_read_b64_tr_b16 a[158:159], v30 offset:4096\n"
    "ds_read_b64_tr_b16 a[180:181], v31 offset:0\n"
    "ds_read_b64_tr_b16 a[182:183], v31 offset:4096\n"
    "ds_read_b64_tr_b16 a[204:205], v32 offset:0\n"
    "ds_read_b64_tr_b16 a[206:207], v32 offset:4096\n"
    "ds_read_b64_tr_b16 a[228:229], v33 offset:0\n"
    "ds_read_b64_tr_b16 a[230:231], v33 offset:4096\n"
    "s_waitcnt lgkmcnt(0)\n"
    "s_barrier\n"
    "s_mov_b32 s49, 0\n"
    "s_add_u32 m0, s41, 0\n"
    "s_nop 0\n"
    "buffer_load_dwordx4 v0, s[8:11], s49 offen offset:0 nt lds\n"
    "buffer_load_dwordx4 v1, s[8:11], s49 offen offset:1024 nt lds\n"
    "buffer_load_dwordx4 v2, s[8:11], s49 offen offset:2048 nt lds\n"
    "s_mov_b32 s49, 1572864\n"
    "s_add_u32 m0, s41, 12288\n"
    "s_nop 0\n"
    "buffer_load_dwordx4 v0, s[8:11], s49 offen offset:0 nt lds\n"
    "buffer_load_dwordx4 v1, s[8:11], s49 offen offset:1024 nt lds\n"
    "buffer_load_dwordx4 v2, s[8:11], s49 offen offset:2048 nt lds\n"
    "s_mov_b32 s49, 3145728\n"
    "s_add_u32 m0, s41, 24576\n"
    "s_nop 0\n"
    "buffer_load_dwordx4 v0, s[8:11], s49 offen offset:0 nt lds\n"
    "buffer_load_dwordx4 v1, s[8:11], s49 offen offset:1024 nt lds\n"
    "buffer_load_dwordx4 v2, s[8:11], s49 offen offset:2048 nt lds\n"
    "s_mov_b32 s49, 4718592\n"
    "s_add_u32 m0, s41, 36864\n"
    "s_nop 0\n"
    "buffer_load_dwordx4 v0, s[8:11], s49 offen offset:0 nt lds\n"
    "buffer_load_dwordx4 v1, s[8:11], s49 offen offset:1024 nt lds\n"
    "buffer_load_dwordx4 v2, s[8:11], s49 offen offset:2048 nt lds\n"
    "s_waitcnt vmcnt(23)\n"
    "s_barrier\n"
    "v_add_u32 v24, 49152, v17\n"
    "v_add_u32 v32, 49152, v18\n"
    "v_xor_b32 v33, 32, v32\n"
    "v_xor_b32 v25, 32, v24\n"
    "v_xor_b32 v26, 64, v24\n"
    "v_xor_b32 v27, 96, v24\n"
    "v_xor_b32 v28, 128, v24\n"
    "v_xor_b32 v29, 160, v24\n"
    "v_xor_b32 v30, 192, v24\n"
    "v_xor_b32 v31, 224, v24\n"
    "ds_read_b64_tr_b16 a[16:17], v24 offset:0\n"
    "ds_read_b64_tr_b16 a[18:19], v24 offset:4096\n"
    "ds_read_b64_tr_b16 a[40:41], v25 offset:0\n"
    "ds_read_b64_tr_b16 a[42:43], v25 offset:4096\n"
    "ds_read_b64_tr_b16 a[64:65], v26 offset:0\n"
    "ds_read_b64_tr_b16 a[66:67], v26 offset:4096\n"
    "ds_read_b64_tr_b16 a[88:89], v27 offset:0\n"
    "ds_read_b64_tr_b16 a[90:91], v27 offset:4096\n"
    "ds_read_b64_tr_b16 a[112:113], v28 offset:0\n"
    "ds_read_b64_tr_b16 a[114:115], v28 offset:4096\n"
    "ds_read_b64_tr_b16 a[136:137], v29 offset:0\n"
    "ds_read_b64_tr_b16 a[138:139], v29 offset:4096\n"
    "ds_read_b64_tr_b16 a[160:161], v30 offset:0\n"
    "ds_read_b64_tr_b16 a[162:163], v30 offset:4096\n"
    "ds_read_b64_tr_b16 a[184:185], v31 offset:0\n"
    "ds_read_b64_tr_b16 a[186:187], v31 offset:4096\n"
    "ds_read_b64_tr_b16 a[208:209], v32 offset:0\n"
    "ds_read_b64_tr_b16 a[210:211], v32 offset:4096\n"
    "ds_read_b64_tr_b16 a[232:233], v33 offset:0\n"
    "ds_read_b64_tr_b16 a[234:235], v33 offset:4096\n"
    "s_waitcnt lgkmcnt(0)\n"
    "s_barrier\n"
    "s_mov_b32 s49, 393216\n"
    "s_add_u32 m0, s41, 49152\n"
    "s_nop 0\n"
    "buffer_load_dwordx4 v0, s[8:11], s49 offen offset:0 nt lds\n"
    "buffer_load_dwordx4 v1, s[8:11], s49 offen offset:1024 nt lds\n"
    "buffer_load_dwordx4 v2, s[8:11], s49 offen offset:2048 nt lds\n"
    "s_mov_b32 s49, 1966080\n"
    "s_add_u32 m0, s41, 61440\n"
    "s_nop 0\n"
    "buffer_load_dwordx4 v0, s[8:11], s49 offen offset:0 nt lds\n"
    "buffer_load_dwordx4 v1, s[8:11], s49 offen offset:1024 nt lds\n"
    "buffer_load_dwordx4 v2, s[8:11], s49 offen offset:2048 nt lds\n"
    "s_mov_b32 s49, 3538944\n"
    "s_add_u32 m0, s41, 73728\n"
    "s_nop 0\n"
    "buffer_load_dwordx4 v0, s[8:11], s49 offen offset:0 nt lds\n"
    "buffer_load_dwordx4 v1, s[8:11], s49 offen offset:1024 nt lds\n"
    "buffer_load_dwordx4 v2, s[8:11], s49 offen offset:2048 nt lds\n"
    "s_mov_b32 s49, 5111808\n"
    "s_add_u32 m0, s41, 86016\n"
    "s_nop 0\n"
    "buffer_load_dwordx4 v0, s[8:11], s49 offen offset:0 nt lds\n"
    "buffer_load_dwordx4 v1, s[8:11], s49 offen offset:1024 nt lds\n"
    "buffer_load_dwordx4 v2, s[8:11], s49 offen offset:2048 nt lds\n"
    "s_waitcnt vmcnt(24)\n"
    "s_barrier\n"
    "v_add_u32 v24, 98304, v17\n"
    "v_add_u32 v32, 98304, v18\n"
    "v_xor_b32 v33, 32, v32\n"
    "v_xor_b32 v25, 32, v24\n"
    "v_xor_b32 v26, 64, v24\n"
    "v_xor_b32 v27, 96, v24\n"
    "v_xor_b32 v28, 128, v24\n"
    "v_xor_b32 v29, 160, v24\n"
    "v_xor_b32 v30, 192, v24\n"
    "v_xor_b32 v31, 224, v24\n"
    "ds_read_b64_tr_b16 a[20:21], v24 offset:0\n"
    "ds_read_b64_tr_b16 a[22:23], v24 offset:4096\n"
    "ds_read_b64_tr_b16 a[44:45], v25 offset:0\n"
    "ds_read_b64_tr_b16 a[46:47], v25 offset:4096\n"
    "ds_read_b64_tr_b16 a[68:69], v26 offset:0\n"
    "ds_read_b64_tr_b16 a[70:71], v26 offset:4096\n"
    "ds_read_b64_tr_b16 a[92:93], v27 offset:0\n"
    "ds_read_b64_tr_b16 a[94:95], v27 offset:4096\n"
    "ds_read_b64_tr_b16 a[116:117], v28 offset:0\n"
    "ds_read_b64_tr_b16 a[118:119], v28 offset:4096\n"
    "ds_read_b64_tr_b16 a[140:141], v29 offset:0\n"
    "ds_read_b64_tr_b16 a[142:143], v29 offset:4096\n"
    "ds_read_b64_tr_b16 a[164:165], v30 offset:0\n"
    "ds_read_b64_tr_b16 a[166:167], v30 offset:4096\n"
    "ds_read_b64_tr_b16 a[188:189], v31 offset:0\n"
    "ds_read_b64_tr_b16 a[190:191], v31 offset:4096\n"
    "ds_read_b64_tr_b16 a[212:213], v32 offset:0\n"
    "ds_read_b64_tr_b16 a[214:215], v32 offset:4096\n"
    "ds_read_b64_tr_b16 a[236:237], v33 offset:0\n"
    "ds_read_b64_tr_b16 a[238:239], v33 offset:4096\n"
    "s_waitcnt lgkmcnt(0)\n"
    "s_barrier\n"
    "s_mov_b32 s49, 786432\n"
    "s_add_u32 m0, s41, 98304\n"
    "s_nop 0\n"
    "buffer_load_dwordx4 v0, s[8:11], s49 offen offset:0 nt lds\n"
    "buffer_load_dwordx4 v1, s[8:11], s49 offen offset:1024 nt lds\n"
    "buffer_load_dwordx4 v2, s[8:11], s49 offen offset:2048 nt lds\n"
    "s_mov_b32 s49, 2359296\n"
    "s_add_u32 m0, s41, 110592\n"
    "s_nop 0\n"
    "buffer_load_dwordx4 v0, s[8:11], s49 offen offset:0 nt lds\n"
    "buffer_load_dwordx4 v1, s[8:11], s49 offen offset:1024 nt lds\n"
    "buffer_load_dwordx4 v2, s[8:11], s49 offen offset:2048 nt lds\n"
    "s_mov_b32 s49, 3932160\n"
    "s_add_u32 m0, s41, 122880\n"
    "s_nop 0\n"
    "buffer_load_dwordx4 v0, s[8:11], s49 offen offset:0 nt lds\n"
    "buffer_load_dwordx4 v1, s[8:11], s49 offen offset:1024 nt lds\n"
    "buffer_load_dwordx4 v2, s[8:11], s49 offen offset:2048 nt lds\n"
    "s_mov_b32 s49, 5505024\n"
    "s_add_u32 m0, s41, 135168\n"
    "s_nop 0\n"
    "buffer_load_dwordx4 v0, s[8:11], s49 offen offset:0 nt lds\n"
    "buffer_load_dwordx4 v1, s[8:11], s49 offen offset:1024 nt lds\n"
    "buffer_load_dwordx4 v2, s[8:11], s49 offen offset:2048 nt lds\n"
    "s_waitcnt vmcnt(24)\n"
    "s_barrier\n"
    "v_add_u32 v8, 0, v3\n"
    "ds_read_b64_tr_b16 a[240:241], v8 offset:0\n"
    "ds_read_b64_tr_b16 a[242:243], v8 offset:768\n"
    "ds_read_b64_tr_b16 v[176:177], v8 offset:128\n"
    "ds_read_b64_tr_b16 v[178:179], v8 offset:896\n"
    "ds_read_b64_tr_b16 v[12:13], v8 offset:256\n"
    "ds_read_b64_tr_b16 v[14:15], v8 offset:1024\n"
    "v_add_u32 v9, 0, v4\n"
    "ds_read_b64_tr_b16 v[112:113], v9 offset:0\n"
    "ds_read_b64_tr_b16 v[114:115], v9 offset:768\n"
    "ds_read_b64_tr_b16 v[192:193], v9 offset:128\n"
    "ds_read_b64_tr_b16 v[194:195], v9 offset:896\n"
    "ds_read_b64_tr_b16 v[28:29], v9 offset:256\n"
    "ds_read_b64_tr_b16 v[30:31], v9 offset:1024\n"
    "v_add_u32 v10, 0, v5\n"
    "ds_read_b64_tr_b16 v[128:129], v10 offset:0\n"
    "ds_read_b64_tr_b16 v[130:131], v10 offset:768\n"
    "ds_read_b64_tr_b16 v[208:209], v10 offset:128\n"
    "ds_read_b64_tr_b16 v[210:211], v10 offset:896\n"
    "ds_read_b64_tr_b16 v[44:45], v10 offset:256\n"
    "ds_read_b64_tr_b16 v[46:47], v10 offset:1024\n"
    "v_add_u32 v11, 0, v6\n"
    "ds_read_b64_tr_b16 v[144:145], v11 offset:0\n"
    "ds_read_b64_tr_b16 v[146:147], v11 offset:768\n"
    "ds_read_b64_tr_b16 v[224:225], v11 offset:128\n"
    "ds_read_b64_tr_b16 v[226:227], v11 offset:896\n"
    "ds_read_b64_tr_b16 v[60:61], v11 offset:256\n"
    "ds_read_b64_tr_b16 v[62:63], v11 offset:1024\n"
    "s_waitcnt lgkmcnt(0)\n"
    "s_barrier\n"
    "s_mov_b32 s49, 1179648\n"
    "s_add_u32 m0, s41, 0\n"
    "s_nop 0\n"
    "buffer_load_dwordx4 v0, s[8:11], s49 offen offset:0 nt lds\n"
    "buffer_load_dwordx4 v1, s[8:11], s49 offen offset:1024 nt lds\n"
    "buffer_load_dwordx4 v2, s[8:11], s49 offen offset:2048 nt lds\n"
    "s_mov_b32 s49, 2752512\n"
    "s_add_u32 m0, s41, 12288\n"
    "s_nop 0\n"
    "buffer_load_dwordx4 v0, s[8:11], s49 offen offset:0 nt lds\n"
    "buffer_load_dwordx4 v1, s[8:11], s49 offen offset:1024 nt lds\n"
    "buffer_load_dwordx4 v2, s[8:11], s49 offen offset:2048 nt lds\n"
    "s_mov_b32 s49, 4325376\n"
    "s_add_u32 m0, s41, 24576\n"
    "s_nop 0\n"
    "buffer_load_dwordx4 v0, s[8:11], s49 offen offset:0 nt lds\n"
    "buffer_load_dwordx4 v1, s[8:11], s49 offen offset:1024 nt lds\n"
    "buffer_load_dwordx4 v2, s[8:11], s49 offen offset:2048 nt lds\n"
    "s_mov_b32 s49, 5898240\n"
    "s_add_u32 m0, s41, 36864\n"
    "s_nop 0\n"
    "buffer_load_dwordx4 v0, s[8:11], s49 offen offset:0 nt lds\n"
    "buffer_load_dwordx4 v1, s[8:11], s49 offen offset:1024 nt lds\n"
    "buffer_load_dwordx4 v2, s[8:11], s49 offen offset:2048 nt lds\n"
    "s_waitcnt vmcnt(24)\n"
    "s_barrier\n"
    "v_add_u32 v8, 49152, v3\n"
    "ds_read_b64_tr_b16 a[244:245], v8 offset:0\n"
    "ds_read_b64_tr_b16 a[246:247], v8 offset:768\n"
    "ds_read_b64_tr_b16 v[180:181], v8 offset:128\n"
    "ds_read_b64_tr_b16 v[182:183], v8 offset:896\n"
    "ds_read_b64_tr_b16 v[16:17], v8 offset:256\n"
    "ds_read_b64_tr_b16 v[18:19], v8 offset:1024\n"
    "v_add_u32 v9, 49152, v4\n"
    "ds_read_b64_tr_b16 v[116:117], v9 offset:0\n"
    "ds_read_b64_tr_b16 v[118:119], v9 offset:768\n"
    "ds_read_b64_tr_b16 v[196:197], v9 offset:128\n"
    "ds_read_b64_tr_b16 v[198:199], v9 offset:896\n"
    "ds_read_b64_tr_b16 v[32:33], v9 offset:256\n"
    "ds_read_b64_tr_b16 v[34:35], v9 offset:1024\n"
    "v_add_u32 v10, 49152, v5\n"
    "ds_read_b64_tr_b16 v[132:133], v10 offset:0\n"
    "ds_read_b64_tr_b16 v[134:135], v10 offset:768\n"
    "ds_read_b64_tr_b16 v[212:213], v10 offset:128\n"
    "ds_read_b64_tr_b16 v[214:215], v10 offset:896\n"
    "ds_read_b64_tr_b16 v[48:49], v10 offset:256\n"
    "ds_read_b64_tr_b16 v[50:51], v10 offset:1024\n"
    "v_add_u32 v11, 49152, v6\n"
    "ds_read_b64_tr_b16 v[148:149], v11 offset:0\n"
    "ds_read_b64_tr_b16 v[150:151], v11 offset:768\n"
    "ds_read_b64_tr_b16 v[228:229], v11 offset:128\n"
    "ds_read_b64_tr_b16 v[230:231], v11 offset:896\n"
    "ds_read_b64_tr_b16 v[64:65], v11 offset:256\n"
    "ds_read_b64_tr_b16 v[66:67], v11 offset:1024\n"
    "s_waitcnt lgkmcnt(0)\n"
    "s_barrier\n"
    "s_waitcnt vmcnt(58)\n"
    "v_cmp_gt_i32 vcc, 32, v109\n"
    "s_nop 0\n"
    "s_cbranch_vccnz .Lvo_tk0_%=\n"
    ".Lvo_tk1_%=:\n"
    "s_lshl_b32 s48, s12, 11\n"
    "s_add_u32 s48, s48, 53248\n"
    "v_lshrrev_b32 v108, 2, v96\n"
    "s_mov_b32 s50, 0\n"
    "s_mov_b32 s51, s14\n"
    "s_add_u32 s43, s18, s13\n"
    "v_mov_b32 v111, s50\n"
    "s_mov_b64 exec, 0x1f0\n"
    "v_mov_b32 v111, s51\n"
    "s_mov_b64 exec, -1\n"
    "buffer_load_dword v109, v108, s[0:3], s24 offen sc1\n"
    "buffer_load_dword v110, v105, s[0:3], 0 offen sc1\n"
    "s_mov_b32 s49, 6291456\n"
    "s_add_u32 m0, s41, 49152\n"
    "s_nop 0\n"
    "buffer_load_dwordx4 v0, s[8:11], s49 offen offset:0 nt lds\n"
    "buffer_load_dwordx4 v1, s[8:11], s49 offen offset:1024 nt lds\n"
    "buffer_load_dwordx4 v2, s[8:11], s49 offen offset:2048 nt lds\n"
    "s_mov_b32 s49, 6684672\n"
    "s_add_u32 m0, s41, 61440\n"
    "s_nop 0\n"
    "buffer_load_dwordx4 v0, s[8:11], s49 offen offset:0 nt lds\n"
    "buffer_load_dwordx4 v1, s[8:11], s49 offen offset:1024 nt lds\n"
    "buffer_load_dwordx4 v2, s[8:11], s49 offen offset:2048 nt lds\n"
    "s_mov_b32 s49, 7077888\n"
    "s_add_u32 m0, s41, 73728\n"
    "s_nop 0\n"
    "buffer_load_dwordx4 v0, s[8:11], s49 offen offset:0 nt lds\n"
    "buffer_load_dwordx4 v1, s[8:11], s49 offen offset:1024 nt lds\n"
    "buffer_load_dwordx4 v2, s[8:11], s49 offen offset:2048 nt lds\n"
    "s_mov_b32 s49, 7471104\n"
    "s_add_u32 m0, s41, 86016\n"
    "s_nop 0\n"
    "buffer_load_dwordx4 v0, s[8:11], s49 offen offset:0 nt lds\n"
    "buffer_load_dwordx4 v1, s[8:11], s49 offen offset:1024 nt lds\n"
    "buffer_load_dwordx4 v2, s[8:11], s49 offen offset:2048 nt lds\n"
    "s_waitcnt vmcnt(26)\n"
    "s_barrier\n"
    "v_add_u32 v8, 98304, v3\n"
    "ds_read_b64_tr_b16 a[248:249], v8 offset:0\n"
    "ds_read_b64_tr_b16 a[250:251], v8 offset:768\n"
    "ds_read_b64_tr_b16 v[184:185], v8 offset:128\n"
    "ds_read_b64_tr_b16 v[186:187], v8 offset:896\n"
    "ds_read_b64_tr_b16 v[20:21], v8 offset:256\n"
    "ds_read_b64_tr_b16 v[22:23], v8 offset:1024\n"
    "v_add_u32 v9, 98304, v4\n"
    "ds_read_b64_tr_b16 v[120:121], v9 offset:0\n"
    "ds_read_b64_tr_b16 v[122:123], v9 offset:768\n"
    "ds_read_b64_tr_b16 v[200:201], v9 offset:128\n"
    "ds_read_b64_tr_b16 v[202:203], v9 offset:896\n"
    "ds_read_b64_tr_b16 v[36:37], v9 offset:256\n"
    "ds_read_b64_tr_b16 v[38:39], v9 offset:1024\n"
    "v_add_u32 v10, 98304, v5\n"
    "ds_read_b64_tr_b16 v[136:137], v10 offset:0\n"
    "ds_read_b64_tr_b16 v[138:139], v10 offset:768\n"
    "ds_read_b64_tr_b16 v[216:217], v10 offset:128\n"
    "ds_read_b64_tr_b16 v[218:219], v10 offset:896\n"
    "ds_read_b64_tr_b16 v[52:53], v10 offset:256\n"
    "ds_read_b64_tr_b16 v[54:55], v10 offset:1024\n"
    "v_add_u32 v11, 98304, v6\n"
    "ds_read_b64_tr_b16 v[152:153], v11 offset:0\n"
    "ds_read_b64_tr_b16 v[154:155], v11 offset:768\n"
    "ds_read_b64_tr_b16 v[232:233], v11 offset:128\n"
    "ds_read_b64_tr_b16 v[234:235], v11 offset:896\n"
    "ds_read_b64_tr_b16 v[68:69], v11 offset:256\n"
    "ds_read_b64_tr_b16 v[70:71], v11 offset:1024\n"
    "s_waitcnt lgkmcnt(0)\n"
    "s_barrier\n"
    "s_waitcnt vmcnt(14)\n"
    "s_barrier\n"
    "v_add_u32 v8, 0, v3\n"
    "ds_read_b64_tr_b16 a[252:253], v8 offset:0\n"
    "ds_read_b64_tr_b16 a[254:255], v8 offset:768\n"
    "ds_read_b64_tr_b16 v[188:189], v8 offset:128\n"
    "ds_read_b64_tr_b16 v[190:191], v8 offset:896\n"
    "ds_read_b64_tr_b16 v[24:25], v8 offset:256\n"
    "ds_read_b64_tr_b16 v[26:27], v8 offset:1024\n"
    "v_add_u32 v9, 0, v4\n"
    "ds_read_b64_tr_b16 v[124:125], v9 offset:0\n"
    "ds_read_b64_tr_b16 v[126:127], v9 offset:768\n"
    "ds_read_b64_tr_b16 v[204:205], v9 offset:128\n"
    "ds_read_b64_tr_b16 v[206:207], v9 offset:896\n"
    "ds_read_b64_tr_b16 v[40:41], v9 offset:256\n"
    "ds_read_b64_tr_b16 v[42:43], v9 offset:1024\n"
    "v_add_u32 v10, 0, v5\n"
    "ds_read_b64_tr_b16 v[140:141], v10 offset:0\n"
    "ds_read_b64_tr_b16 v[142:143], v10 offset:768\n"
    "ds_read_b64_tr_b16 v[220:221], v10 offset:128\n"
    "ds_read_b64_tr_b16 v[222:223], v10 offset:896\n"
    "ds_read_b64_tr_b16 v[56:57], v10 offset:256\n"
    "ds_read_b64_tr_b16 v[58:59], v10 offset:1024\n"
    "v_add_u32 v11, 0, v6\n"
    "ds_read_b64_tr_b16 v[156:157], v11 offset:0\n"
    "ds_read_b64_tr_b16 v[158:159], v11 offset:768\n"
    "ds_read_b64_tr_b16 v[236:237], v11 offset:128\n"
    "ds_read_b64_tr_b16 v[238:239], v11 offset:896\n"
    "ds_read_b64_tr_b16 v[72:73], v11 offset:256\n"
    "ds_read_b64_tr_b16 v[74:75], v11 offset:1024\n"
    "s_waitcnt lgkmcnt(0)\n"
    "s_barrier\n"
    "s_waitcnt vmcnt(12)\n"
    "v_cmp_gt_i32 vcc, 1, v109\n"
    "s_nop 0\n"
    "s_cbranch_vccnz .Lvo_e0_%=\n"
    "v_cmp_gt_i32 vcc, v111, v110\n"
    "s_nop 0\n"
    "s_cbranch_vccnz .Lvo_e0_%=\n"
    ".Lvo_e1_%=:\n"
    "s_sub_u32 m0, s17, 8192\n"
    "s_nop 0\n"
    "buffer_load_dwordx4 v96, s[0:3], s43 offen offset:0 sc1 lds\n"
    "buffer_load_dwordx4 v96, s[0:3], s43 offen offset:1024 sc1 lds\n"
    "buffer_load_dwordx4 v96, s[0:3], s43 offen offset:2048 sc1 lds\n"
    "buffer_load_dwordx4 v96, s[0:3], s43 offen offset:3072 sc1 lds\n"
    "s_sub_u32 m0, s17, 4096\n"
    "s_add_u32 s43, s43, 4096\n"
    "buffer_load_dwordx4 v96, s[0:3], s43 offen offset:0 sc1 lds\n"
    "buffer_load_dwordx4 v96, s[0:3], s43 offen offset:1024 sc1 lds\n"
    "buffer_load_dwordx4 v96, s[0:3], s43 offen offset:2048 sc1 lds\n"
    "buffer_load_dwordx4 v96, s[0:3], s43 offen offset:3072 sc1 lds\n"
    "s_waitcnt vmcnt(8)\n"
    "s_barrier\n"
    "v_add_u32 v8, 49152, v7\n"
    "ds_read_b64_tr_b16 v[160:161], v8 offset:0\n"
    "ds_read_b64_tr_b16 v[162:163], v8 offset:768\n"
    "ds_read_b64_tr_b16 v[240:241], v8 offset:128\n"
    "ds_read_b64_tr_b16 v[242:243], v8 offset:896\n"
    "ds_read_b64_tr_b16 v[76:77], v8 offset:256\n"
    "ds_read_b64_tr_b16 v[78:79], v8 offset:1024\n"
    "v_add_u32 v9, 61440, v7\n"
    "ds_read_b64_tr_b16 v[164:165], v9 offset:0\n"
    "ds_read_b64_tr_b16 v[166:167], v9 offset:768\n"
    "ds_read_b64_tr_b16 v[244:245], v9 offset:128\n"
    "ds_read_b64_tr_b16 v[246:247], v9 offset:896\n"
    "ds_read_b64_tr_b16 v[80:81], v9 offset:256\n"
    "ds_read_b64_tr_b16 v[82:83], v9 offset:1024\n"
    "v_add_u32 v10, 73728, v7\n"
    "ds_read_b64_tr_b16 v[168:169], v10 offset:0\n"
    "ds_read_b64_tr_b16 v[170:171], v10 offset:768\n"
    "ds_read_b64_tr_b16 v[248:249], v10 offset:128\n"
    "ds_read_b64_tr_b16 v[250:251], v10 offset:896\n"
    "ds_read_b64_tr_b16 v[84:85], v10 offset:256\n"
    "ds_read_b64_tr_b16 v[86:87], v10 offset:1024\n"
    "v_add_u32 v11, 86016, v7\n"
    "ds_read_b64_tr_b16 v[172:173], v11 offset:0\n"
    "ds_read_b64_tr_b16 v[174:175], v11 offset:768\n"
    "ds_read_b64_tr_b16 v[252:253], v11 offset:128\n"
    "ds_read_b64_tr_b16 v[254:255], v11 offset:896\n"
    "ds_read_b64_tr_b16 v[88:89], v11 offset:256\n"
    "ds_read_b64_tr_b16 v[90:91], v11 offset:1024\n"
    "s_waitcnt lgkmcnt(0)\n"
    "s_barrier\n"
    "s_mov_b32 m0, s48\n"
    "s_add_u32 s43, s43, s19\n"
    "buffer_load_dwordx4 v96, s[0:3], s43 offen offset:0 sc1 lds\n"
    "buffer_load_dwordx4 v96, s[0:3], s43 offen offset:1024 sc1 lds\n"
    "ds_write_b128 v97, v[12:15] offset:0\n"
    "ds_write_b128 v97, v[16:19] offset:1024\n"
    "ds_write_b128 v97, v[20:23] offset:2048\n"
    "ds_write_b128 v97, v[24:27] offset:3072\n"
    "ds_write_b128 v97, v[28:31] offset:4096\n"
    "ds_write_b128 v97, v[32:35] offset:5120\n"
    "ds_write_b128 v97, v[36:39] offset:6144\n"
    "ds_write_b128 v97, v[40:43] offset:7168\n"
    "ds_write_b128 v97, v[44:47] offset:8192\n"
    "ds_write_b128 v97, v[48:51] offset:9216\n"
    "ds_write_b128 v97, v[52:55] offset:10240\n"
    "ds_write_b128 v97, v[56:59] offset:11264\n"
    "ds_write_b128 v97, v[60:63] offset:12288\n"
    "ds_write_b128 v97, v[64:67] offset:13312\n"
    "ds_write_b128 v97, v[68:71] offset:14336\n"
    "ds_write_b128 v97, v[72:75] offset:15360\n"
    "ds_write_b128 v97, v[76:79] offset:16384\n"
    "ds_write_b128 v97, v[80:83] offset:17408\n"
    "ds_write_b128 v97, v[84:87] offset:18432\n"
    "ds_write_b128 v97, v[88:91] offset:19456\n"
    "s_mov_b32 s34, -4\n"
    "s_memrealtime s[30:31]\n"
    "s_waitcnt vmcnt(0) lgkmcnt(0)\n"
    "s_barrier\n"
    "ds_read_b128 v[24:27], v92 offset:0\n"
    "ds_read_b128 v[28:31], v92 offset:1024\n"
    "ds_read_b128 v[32:35], v93 offset:0\n"
    "ds_read_b128 v[36:39], v93 offset:1024\n"
    "ds_read_b128 v[40:43], v94 offset:0\n"
    "ds_read_b128 v[44:47], v94 offset:1024\n"
    "s_sub_u32 s46, s30, 1320\n"
    "s_mov_b32 s34, -3\n"
    "s_mov_b32 s35, -5\n"
    "s_mov_b32 s45, s23\n"
    "s_mov_b32 s38, 2130706432\n"
    "s_mov_b32 s39, 2130706432\n"
    "s_mov_b32 s40, 2130706432\n"
    "s_waitcnt lgkmcnt(0)\n"
    "s_branch .Lvo_vph_%=\n"
    ".Lvo_take_%=:\n"
    "v_mfma_f32_16x16x32_bf16 v[84:87], v[248:251], v[24:27], v[84:87]\n"
    ".Lvo_mk8x5_%=:\n"
    "s_waitcnt vmcnt(3)\n"
    ".Lvo_mk0x6_%=:\n"
    "ds_read_b128 v[72:75], v92 offset:8192\n"
    "v_cvt_pk_bf16_f32 v40, v64, v65\n"
    "v_mfma_f32_16x16x32_bf16 v[84:87], v[252:255], v[32:35], v[84:87]\n"
    "s_waitcnt vmcnt(2)\n"
    "ds_read_b128 v[76:79], v92 offset:9216\n"
    "v_cvt_pk_bf16_f32 v41, v66, v67\n"
    "v_mfma_f32_16x16x32_bf16 v[68:71], v[164:167], v[44:47], v[68:71]\n"
    "v_mov_b64 v[64:65], 0\n"
    "v_mov_b64 v[66:67], 0\n"
    "v_mfma_f32_16x16x32_bf16 v[68:71], v[168:171], v[28:31], v[68:71]\n"
    "s_waitcnt lgkmcnt(2)\n"
    "ds_read_b128 v[24:27], v97 offset:6144\n"
    "v_cvt_pk_bf16_f32 v42, v84, v85\n"
    "v_mfma_f32_16x16x32_bf16 v[68:71], v[172:175], v[36:39], v[68:71]\n"
    "v_cvt_pk_bf16_f32 v43, v86, v87\n"
    "v_mov_b64 v[84:85], 0\n"
    "v_mfma_f32_16x16x32_bf16 v[88:91], v[244:247], v[44:47], v[88:91]\n"
    "ds_write_b128 v92, v[40:43] offset:11264\n"
    "v_mfma_f32_16x16x32_bf16 v[88:91], v[248:251], v[28:31], v[88:91]\n"
    "s_waitcnt lgkmcnt(2)\n"
    "v_max_u32 v108, v72, v74\n"
    "v_max3_u32 v108, v108, v76, v78\n"
    "v_mfma_f32_16x16x32_bf16 v[88:91], v[252:255], v[36:39], v[88:91]\n"
    "v_cmp_le_u32 vcc, s52, v108\n"
    "v_mov_b64 v[86:87], 0\n"
    "v_smfmac_f32_16x16x64_bf16 v[64:67], v[72:75], a[240:247], v98\n"
    "s_cbranch_vccnz .Lvo_retry_%=\n"
    ".Lvo_go_%=:\n"
    "s_barrier\n"
    ".Lvo_mk1x7_%=:\n"
    "ds_read_b128 v[32:35], v93 offset:8192\n"
    "v_cvt_pk_bf16_f32 v44, v68, v69\n"
    "v_smfmac_f32_16x16x64_bf16 v[64:67], v[76:79], a[248:255], v98\n"
    "ds_read_b128 v[36:39], v93 offset:9216\n"
    "v_cvt_pk_bf16_f32 v45, v70, v71\n"
    "v_smfmac_f32_16x16x64_bf16 v[84:87], v[72:75], v[0:7], v98\n"
    "s_waitcnt vmcnt(1)\n"
    "ds_read_b128 v[80:83], v92 offset:10240\n"
    "v_mov_b64 v[68:69], 0\n"
    "v_mov_b64 v[70:71], 0\n"
    "v_smfmac_f32_16x16x64_bf16 v[84:87], v[76:79], v[8:15], v98\n"
    "ds_read_b128 v[28:31], v97 offset:7168\n"
    "v_cvt_pk_bf16_f32 v46, v88, v89\n"
    "v_smfmac_f32_16x16x64_bf16 v[68:71], v[72:75], v[176:183], v98\n"
    "ds_read_b128 v[0:3], v97 offset:12288\n"
    "v_cvt_pk_bf16_f32 v47, v90, v91\n"
    "v_smfmac_f32_16x16x64_bf16 v[68:71], v[76:79], v[184:191], v98\n"
    "s_waitcnt lgkmcnt(4)\n"
    "ds_read_b128 v[88:91], v94 offset:8192\n"
    "ds_write_b128 v92, v[44:47] offset:12288\n"
    "v_smfmac_f32_16x16x64_bf16 v[84:87], v[32:35], v[16:23], v98\n"
    "ds_read_b128 v[40:43], v94 offset:9216\n"
    "v_smfmac_f32_16x16x64_bf16 v[64:67], v[32:35], v[112:119], v98\n"
    "s_waitcnt lgkmcnt(5)\n"
    "ds_read_b128 v[4:7], v97 offset:13312\n"
    "v_max_u32 v108, v80, v82\n"
    "v_smfmac_f32_16x16x64_bf16 v[68:71], v[32:35], v[192:199], v98\n"
    "s_waitcnt lgkmcnt(5)\n"
    "ds_read_b128 v[44:47], v95 offset:8192\n"
    "v_cmp_eq_u32 vcc, -1, v108\n"
    "v_smfmac_f32_16x16x64_bf16 v[84:87], v[36:39], v[24:31], v98\n"
    "ds_read_b128 v[8:11], v97 offset:14336\n"
    "ds_read_b128 v[12:15], v97 offset:15360\n"
    "v_smfmac_f32_16x16x64_bf16 v[64:67], v[36:39], v[120:127], v98\n"
    "ds_read_b128 v[16:19], v97 offset:16384\n"
    "v_smfmac_f32_16x16x64_bf16 v[68:71], v[36:39], v[200:207], v98\n"
    "s_waitcnt lgkmcnt(6)\n"
    "s_cbranch_vccnz .Lvo_r2_%=\n"
    ".Lvo_go2_%=:\n"
    "s_barrier\n"
    "ds_read_b128 v[72:75], v96 offset:10240\n"
    "v_smfmac_f32_16x16x64_bf16 v[84:87], v[88:91], v[48:55], v98\n"
    "s_mov_b32 m0, s21\n"
    "ds_read_b128 v[80:83], v95 offset:9216\n"
    "ds_read_b128 v[76:79], v96 offset:23552\n"
    "v_smfmac_f32_16x16x64_bf16 v[64:67], v[88:91], v[128:135], v98\n"
    "s_waitcnt vmcnt(0)\n"
    "ds_read_b128 v[20:23], v97 offset:17408\n"
    "v_cmp_gt_i32 vcc, v111, v110\n"
    "v_smfmac_f32_16x16x64_bf16 v[68:71], v[88:91], v[208:215], v98\n"
    "s_waitcnt lgkmcnt(7)\n"
    "s_cbranch_vccnz .Lvo_wf_%=\n"
    ".Lvo_wfb_%=:\n"
    "v_smfmac_f32_16x16x64_bf16 v[64:67], v[40:43], v[136:143], v98\n"
    "buffer_load_dwordx4 v96, s[0:3], s44 offen sc1 lds\n"
    "v_smfmac_f32_16x16x64_bf16 v[64:67], v[44:47], v[144:151], v98\n"
    "s_waitcnt lgkmcnt(3)\n"
    "ds_read_b128 v[24:27], v97 offset:18432\n"
    "v_mov_b64 v[32:33], 0\n"
    "v_smfmac_f32_16x16x64_bf16 v[64:67], v[72:75], v[160:167], v98\n"
    "s_waitcnt lgkmcnt(3)\n"
    "ds_read_b128 v[28:31], v97 offset:19456\n"
    "v_mov_b64 v[34:35], 0\n"
    "v_smfmac_f32_16x16x64_bf16 v[64:67], v[80:83], v[152:159], v98\n"
    "s_waitcnt lgkmcnt(3)\n"
    "v_mov_b64 v[36:37], 0\n"
    "v_smfmac_f32_16x16x64_bf16 v[64:67], v[76:79], v[168:175], v98\n"
    "s_sub_u32 m0, s17, 8192\n"
    "v_mov_b64 v[38:39], 0\n"
    "v_smfmac_f32_16x16x64_bf16 v[84:87], v[40:43], v[56:63], v98\n"
    "v_mov_b64 v[48:49], 0\n"
    "v_smfmac_f32_16x16x64_bf16 v[84:87], v[44:47], v[0:7], v98\n"
    "ds_read_b128 v[0:3], v96 offset:11264\n"
    "s_nop 0\n"
    "v_add_f32 v64, v64, v66\n"
    "v_smfmac_f32_16x16x64_bf16 v[84:87], v[80:83], v[8:15], v98\n"
    "s_waitcnt lgkmcnt(3)\n"
    "ds_read_b128 v[4:7], v96 offset:12288\n"
    "v_add_f32 v65, v65, v67\n"
    "v_smfmac_f32_16x16x64_bf16 v[84:87], v[72:75], v[16:23], v98\n"
    "s_waitcnt lgkmcnt(2)\n"
    "ds_read_b128 v[8:11], v96 offset:24576\n"
    "v_cvt_pk_bf16_f32 v64, v64, v65\n"
    "v_smfmac_f32_16x16x64_bf16 v[84:87], v[76:79], v[24:31], v98\n"
    "ds_write_b32 v99, v64 offset:0\n"
    "v_smfmac_f32_16x16x64_bf16 v[68:71], v[40:43], v[216:223], v98\n"
    "ds_read_b128 v[12:15], v96 offset:25600\n"
    "v_mov_b64 v[50:51], 0\n"
    "v_smfmac_f32_16x16x64_bf16 v[68:71], v[44:47], v[224:231], v98\n"
    "ds_read_b128 v[16:19], v96 offset:37888\n"
    "s_nop 0\n"
    "v_add_f32 v84, v84, v86\n"
    "v_smfmac_f32_16x16x64_bf16 v[68:71], v[80:83], v[232:239], v98\n"
    "ds_read_b128 v[20:23], v96 offset:38912\n"
    "v_add_f32 v85, v85, v87\n"
    "v_smfmac_f32_16x16x64_bf16 v[68:71], v[72:75], v[240:247], v98\n"
    "ds_read_b128 v[24:27], v96 offset:51200\n"
    "v_cvt_pk_bf16_f32 v84, v84, v85\n"
    "v_smfmac_f32_16x16x64_bf16 v[68:71], v[76:79], v[248:255], v98\n"
    "ds_write_b32 v100, v84 offset:0\n"
    "s_waitcnt lgkmcnt(7)\n"
    "v_mfma_f32_16x16x32_bf16 v[40:43], v[0:3], a[0:3], 0\n"
    "ds_read_b128 v[28:31], v96 offset:52224\n"
    "v_mov_b64 v[52:53], 0\n"
    "v_mfma_f32_16x16x32_bf16 v[44:47], v[4:7], a[0:3], 0\n"
    "s_nop 1\n"
    "v_add_f32 v68, v68, v70\n"
    "v_add_f32 v69, v69, v71\n"
    "v_cvt_pk_bf16_f32 v68, v68, v69\n"
    "ds_write_b32 v99, v68 offset:256\n"
    "s_waitcnt lgkmcnt(4)\n"
    "v_mfma_f32_16x16x32_bf16 v[40:43], v[8:11], a[4:7], v[40:43]\n"
    "v_mov_b64 v[54:55], 0\n"
    "v_mov_b64 v[56:57], 0\n"
    "v_mfma_f32_16x16x32_bf16 v[44:47], v[12:15], a[4:7], v[44:47]\n"
    "v_mov_b64 v[58:59], 0\n"
    "v_mov_b64 v[60:61], 0\n"
    "v_mfma_f32_16x16x32_bf16 v[40:43], v[16:19], a[8:11], v[40:43]\n"
    "v_mov_b64 v[62:63], 0\n"
    "v_mov_b64 v[64:65], 0\n"
    "v_mfma_f32_16x16x32_bf16 v[44:47], v[20:23], a[8:11], v[44:47]\n"
    "v_mov_b64 v[66:67], 0\n"
    "v_mov_b64 v[68:69], 0\n"
    "s_waitcnt lgkmcnt(0)\n"
    "s_barrier\n"
    ".Lvo_mk2x8_%=:\n"
    "ds_read_b64_tr_b16 v[72:73], v101 offset:0\n"
    "ds_read_b64_tr_b16 v[74:75], v101 offset:64\n"
    "v_mfma_f32_16x16x32_bf16 v[40:43], v[24:27], a[12:15], v[40:43]\n"
    "ds_read_b64_tr_b16 v[76:77], v101 offset:1024\n"
    "ds_read_b64_tr_b16 v[78:79], v101 offset:1088\n"
    "v_mfma_f32_16x16x32_bf16 v[44:47], v[28:31], a[12:15], v[44:47]\n"
    "ds_read_b64_tr_b16 v[80:81], v101 offset:2048\n"
    "ds_read_b64_tr_b16 v[82:83], v101 offset:2112\n"
    "v_mov_b64 v[70:71], 0\n"
    "v_mov_b64 v[84:85], 0\n"
    "v_mov_b64 v[86:87], 0\n"
    "v_mov_b64 v[88:89], 0\n"
    "v_mov_b64 v[90:91], 0\n"
    "s_waitcnt lgkmcnt(4)\n"
    "v_smfmac_f32_16x16x64_bf16 v[32:35], v[72:75], a[192:199], v98\n"
    "buffer_load_dwordx4 v96, s[0:3], s43 offen offset:0 sc1 lds\n"
    "s_waitcnt lgkmcnt(2)\n"
    "v_smfmac_f32_16x16x64_bf16 v[32:35], v[76:79], a[200:207], v98\n"
    "s_waitcnt lgkmcnt(0)\n"
    "v_smfmac_f32_16x16x64_bf16 v[32:35], v[80:83], a[208:215], v98\n"
    "v_smfmac_f32_16x16x64_bf16 v[36:39], v[72:75], a[216:223], v98\n"
    "buffer_load_dwordx4 v96, s[0:3], s43 offen offset:1024 sc1 lds\n"
    "v_smfmac_f32_16x16x64_bf16 v[36:39], v[76:79], a[224:231], v98\n"
    "s_nop 1\n"
    "v_add_f32 v32, v32, v34\n"
    "v_smfmac_f32_16x16x64_bf16 v[36:39], v[80:83], a[232:239], v98\n"
    "v_add_f32 v33, v33, v35\n"
    "v_smfmac_f32_16x16x64_bf16 v[48:51], v[72:75], a[0:7], v98\n"
    "v_cvt_pk_bf16_f32 v108, v32, v33\n"
    "v_smfmac_f32_16x16x64_bf16 v[48:51], v[76:79], a[8:15], v98\n"
    "s_nop 1\n"
    "v_add_f32 v36, v36, v38\n"
    "v_smfmac_f32_16x16x64_bf16 v[48:51], v[80:83], a[16:23], v98\n"
    "v_add_f32 v37, v37, v39\n"
    "v_smfmac_f32_16x16x64_bf16 v[52:55], v[72:75], a[24:31], v98\n"
    "v_cvt_pk_bf16_f32 v109, v36, v37\n"
    "v_smfmac_f32_16x16x64_bf16 v[52:55], v[76:79], a[32:39], v98\n"
    "s_nop 1\n"
    "v_add_f32 v48, v48, v50\n"
    "v_permlane16_swap_b32 v108, v109\n"
    "v_min_u32 v108, -2, v108\n"
    "v_smfmac_f32_16x16x64_bf16 v[52:55], v[80:83], a[40:47], v98\n"
    "buffer_store_dwordx2 v[108:109], v103, s[0:3], s36 offen sc1\n"
    "v_add_f32 v49, v49, v51\n"
    "v_smfmac_f32_16x16x64_bf16 v[56:59], v[72:75], a[48:55], v98\n"
    "v_cvt_pk_bf16_f32 v48, v48, v49\n"
    "v_smfmac_f32_16x16x64_bf16 v[56:59], v[76:79], a[56:63], v98\n"
    "s_nop 1\n"
    "v_add_f32 v52, v52, v54\n"
    "v_smfmac_f32_16x16x64_bf16 v[56:59], v[80:83], a[64:71], v98\n"
    "v_add_f32 v53, v53, v55\n"
    "v_smfmac_f32_16x16x64_bf16 v[60:63], v[72:75], a[72:79], v98\n"
    "v_cvt_pk_bf16_f32 v49, v52, v53\n"
    "v_smfmac_f32_16x16x64_bf16 v[60:63], v[76:79], a[80:87], v98\n"
    "s_waitcnt vmcnt(3)\n"
    "s_barrier\n"
    "v_add_f32 v56, v56, v58\n"
    "v_smfmac_f32_16x16x64_bf16 v[60:63], v[80:83], a[88:95], v98\n"
    "ds_read2_b64 v[32:35], v106 offset0:0 offset1:128\n"
    "v_add_f32 v57, v57, v59\n"
    "v_smfmac_f32_16x16x64_bf16 v[64:67], v[72:75], a[96:103], v98\n"
    "ds_read2_b64 v[36:39], v106 offset0:1 offset1:129\n"
    "v_cvt_pk_bf16_f32 v50, v56, v57\n"
    "v_smfmac_f32_16x16x64_bf16 v[64:67], v[76:79], a[104:111], v98\n"
    "s_nop 1\n"
    "v_add_f32 v60, v60, v62\n"
    "v_permlane32_swap_b32 v48, v50\n"
    "v_smfmac_f32_16x16x64_bf16 v[64:67], v[80:83], a[112:119], v98\n"
    "v_add_f32 v61, v61, v63\n"
    "v_smfmac_f32_16x16x64_bf16 v[68:71], v[72:75], a[120:127], v98\n"
    "v_cvt_pk_bf16_f32 v51, v60, v61\n"
    "v_smfmac_f32_16x16x64_bf16 v[68:71], v[76:79], a[128:135], v98\n"
    "s_nop 0\n"
    "v_permlane32_swap_b32 v49, v51\n"
    "v_smfmac_f32_16x16x64_bf16 v[68:71], v[80:83], a[136:143], v98\n"
    "s_nop 0\n"
    "v_permlane16_swap_b32 v50, v51\n"
    "v_permlane16_swap_b32 v48, v49\n"
    "v_min_u32 v48, -2, v48\n"
    "v_smfmac_f32_16x16x64_bf16 v[84:87], v[72:75], a[144:151], v98\n"
    "buffer_store_dwordx4 v[48:51], v102, s[0:3], s36 offen sc1\n"
    "v_add_f32 v64, v64, v66\n"
    "v_add_f32 v65, v65, v67\n"
    "v_cvt_pk_bf16_f32 v64, v64, v65\n"
    "v_smfmac_f32_16x16x64_bf16 v[84:87], v[76:79], a[152:159], v98\n"
    "v_add_f32 v68, v68, v70\n"
    "v_smfmac_f32_16x16x64_bf16 v[84:87], v[80:83], a[160:167], v98\n"
    "v_add_f32 v69, v69, v71\n"
    "v_smfmac_f32_16x16x64_bf16 v[88:91], v[72:75], a[168:175], v98\n"
    "v_cvt_pk_bf16_f32 v65, v68, v69\n"
    "v_smfmac_f32_16x16x64_bf16 v[88:91], v[76:79], a[176:183], v98\n"
    "s_nop 1\n"
    "v_add_f32 v84, v84, v86\n"
    "v_smfmac_f32_16x16x64_bf16 v[88:91], v[80:83], a[184:191], v98\n"
    ".Lvo_mk3x9_%=:\n"
    ".Lvo_uph_%=:\n"
    "s_waitcnt lgkmcnt(0)\n"
    "v_add_f32 v85, v85, v87\n"
    "v_mfma_f32_16x16x32_bf16 v[56:59], v[32:35], a[16:19], v[40:43]\n"
    "v_cvt_pk_bf16_f32 v66, v84, v85\n"
    "v_mfma_f32_16x16x32_bf16 v[60:63], v[36:39], a[16:19], v[44:47]\n"
    "s_nop 0\n"
    "v_permlane32_swap_b32 v64, v66\n"
    "v_add_f32 v88, v88, v90\n"
    "v_add_f32 v89, v89, v91\n"
    "v_cvt_pk_bf16_f32 v67, v88, v89\n"
    "s_nop 1\n"
    "v_permlane32_swap_b32 v65, v67\n"
    "s_nop 1\n"
    "v_permlane16_swap_b32 v66, v67\n"
    "v_permlane16_swap_b32 v64, v65\n"
    "v_min_u32 v64, -2, v64\n"
    "buffer_store_dwordx4 v[64:67], v102, s[0:3], s37 offen sc1\n"
    ".Lvo_mkum_%=:\n"
    "s_cmp_eq_u32 s34, 63\n"
    "s_cbranch_scc1 .Lvo_end_%=\n"
    "ds_read2_b64 v[40:43], v107 offset0:0 offset1:128\n"
    "ds_read2_b64 v[44:47], v107 offset0:1 offset1:129\n"
    "v_mfma_f32_16x16x32_bf16 v[48:51], v[0:3], a[24:27], 0\n"
    "v_mfma_f32_16x16x32_bf16 v[52:55], v[4:7], a[24:27], 0\n"
    ".Lvo_mkL0_%=:\n"
    "v_mfma_f32_16x16x32_bf16 v[48:51], v[8:11], a[28:31], v[48:51]\n"
    "v_mfma_f32_16x16x32_bf16 v[52:55], v[12:15], a[28:31], v[52:55]\n"
    "v_mfma_f32_16x16x32_bf16 v[48:51], v[16:19], a[32:35], v[48:51]\n"
    "v_mov_b64 v[88:89], -1\n"
    "v_mov_b64 v[90:91], -1\n"
    "v_mfma_f32_16x16x32_bf16 v[52:55], v[20:23], a[32:35], v[52:55]\n"
    "s_waitcnt lgkmcnt(0)\n"
    "v_mfma_f32_16x16x32_bf16 v[56:59], v[40:43], a[20:23], v[56:59]\n"
    "buffer_load_dwordx4 v96, s[0:3], s43 offen offset:2048 sc1 lds\n"
    "v_mfma_f32_16x16x32_bf16 v[60:63], v[44:47], a[20:23], v[60:63]\n"
    "v_mfma_f32_16x16x32_bf16 v[48:51], v[24:27], a[36:39], v[48:51]\n"
    "s_nop 1\n"
    "v_cvt_pk_bf16_f32 v72, v56, v57\n"
    "v_mfma_f32_16x16x32_bf16 v[52:55], v[28:31], a[36:39], v[52:55]\n"
    "v_cvt_pk_bf16_f32 v74, v58, v59\n"
    "v_cvt_pk_bf16_f32 v73, v60, v61\n"
    "v_mfma_f32_16x16x32_bf16 v[48:51], v[32:35], a[40:43], v[48:51]\n"
    "v_cvt_pk_bf16_f32 v75, v62, v63\n"
    "v_mfma_f32_16x16x32_bf16 v[52:55], v[36:39], a[40:43], v[52:55]\n"
    "buffer_load_dwordx4 v96, s[0:3], s43 offen offset:3072 sc1 lds\n"
    "v_mfma_f32_16x16x32_bf16 v[48:51], v[40:43], a[44:47], v[48:51]\n"
    "s_sub_u32 m0, s17, 4096\n"
    "s_add_u32 s43, s43, 4096\n"
    "v_mfma_f32_16x16x32_bf16 v[52:55], v[44:47], a[44:47], v[52:55]\n"
    "v_mfma_f32_16x16x32_bf16 v[56:59], v[0:3], a[48:51], 0\n"
    "s_nop 1\n"
    "v_cvt_pk_bf16_f32 v76, v48, v49\n"
    "v_mfma_f32_16x16x32_bf16 v[60:63], v[4:7], a[48:51], 0\n"
    "v_cvt_pk_bf16_f32 v78, v50, v51\n"
    "v_cvt_pk_bf16_f32 v77, v52, v53\n"
    "v_mfma_f32_16x16x32_bf16 v[56:59], v[8:11], a[52:55], v[56:59]\n"
    "v_cvt_pk_bf16_f32 v79, v54, v55\n"
    "v_mfma_f32_16x16x32_bf16 v[60:63], v[12:15], a[52:55], v[60:63]\n"
    "buffer_load_dwordx4 v96, s[0:3], s43 offen offset:0 sc1 lds\n"
    "v_mfma_f32_16x16x32_bf16 v[56:59], v[16:19], a[56:59], v[56:59]\n"
    "v_mfma_f32_16x16x32_bf16 v[60:63], v[20:23], a[56:59], v[60:63]\n"
    "v_mfma_f32_16x16x32_bf16 v[56:59], v[24:27], a[60:63], v[56:59]\n"
    "v_mfma_f32_16x16x32_bf16 v[60:63], v[28:31], a[60:63], v[60:63]\n"
    "buffer_load_dwordx4 v96, s[0:3], s43 offen offset:1024 sc1 lds\n"
    "v_mfma_f32_16x16x32_bf16 v[56:59], v[32:35], a[64:67], v[56:59]\n"
    "v_mfma_f32_16x16x32_bf16 v[60:63], v[36:39], a[64:67], v[60:63]\n"
    "v_mfma_f32_16x16x32_bf16 v[56:59], v[40:43], a[68:71], v[56:59]\n"
    "v_mfma_f32_16x16x32_bf16 v[60:63], v[44:47], a[68:71], v[60:63]\n"
    "buffer_load_dwordx4 v96, s[0:3], s43 offen offset:2048 sc1 lds\n"
    "v_mfma_f32_16x16x32_bf16 v[48:51], v[0:3], a[72:75], 0\n"
    "s_nop 1\n"
    "v_cvt_pk_bf16_f32 v80, v56, v57\n"
    "v_mfma_f32_16x16x32_bf16 v[52:55], v[4:7], a[72:75], 0\n"
    "v_cvt_pk_bf16_f32 v82, v58, v59\n"
    "v_cvt_pk_bf16_f32 v81, v60, v61\n"
    "v_mfma_f32_16x16x32_bf16 v[48:51], v[8:11], a[76:79], v[48:51]\n"
    "v_cvt_pk_bf16_f32 v83, v62, v63\n"
    "v_mfma_f32_16x16x32_bf16 v[52:55], v[12:15], a[76:79], v[52:55]\n"
    "buffer_store_dwordx4 v[72:75], v96, s[0:3], s40 offen sc1\n"
    "s_add_u32 s40, s40, 0x10000\n"
    "v_mfma_f32_16x16x32_bf16 v[48:51], v[16:19], a[80:83], v[48:51]\n"
    ".Lvo_mkL1_%=:\n"
    "v_mfma_f32_16x16x32_bf16 v[52:55], v[20:23], a[80:83], v[52:55]\n"
    "v_mfma_f32_16x16x32_bf16 v[48:51], v[24:27], a[84:87], v[48:51]\n"
    "v_mfma_f32_16x16x32_bf16 v[52:55], v[28:31], a[84:87], v[52:55]\n"
    "v_mfma_f32_16x16x32_bf16 v[48:51], v[32:35], a[88:91], v[48:51]\n"
    "v_mfma_f32_16x16x32_bf16 v[52:55], v[36:39], a[88:91], v[52:55]\n"
    "v_mfma_f32_16x16x32_bf16 v[48:51], v[40:43], a[92:95], v[48:51]\n"
    "buffer_load_dwordx4 v96, s[0:3], s43 offen offset:3072 sc1 lds\n"
    "v_mfma_f32_16x16x32_bf16 v[52:55], v[44:47], a[92:95], v[52:55]\n"
    "v_mfma_f32_16x16x32_bf16 v[56:59], v[0:3], a[96:99], 0\n"
    "s_nop 1\n"
    "v_cvt_pk_bf16_f32 v72, v48, v49\n"
    "v_mfma_f32_16x16x32_bf16 v[60:63], v[4:7], a[96:99], 0\n"
    "v_cvt_pk_bf16_f32 v74, v50, v51\n"
    "v_cvt_pk_bf16_f32 v73, v52, v53\n"
    "v_mfma_f32_16x16x32_bf16 v[56:59], v[8:11], a[100:103], v[56:59]\n"
    "buffer_store_dwordx4 v[76:79], v96, s[0:3], s40 offen sc1\n"
    "v_cvt_pk_bf16_f32 v75, v54, v55\n"
    "s_add_u32 s40, s40, 0x10000\n"
    "s_mov_b32 m0, s48\n"
    "v_mfma_f32_16x16x32_bf16 v[60:63], v[12:15], a[100:103], v[60:63]\n"
    "s_add_u32 s43, s43, s19\n"
    "v_mfma_f32_16x16x32_bf16 v[56:59], v[16:19], a[104:107], v[56:59]\n"
    "v_mfma_f32_16x16x32_bf16 v[60:63], v[20:23], a[104:107], v[60:63]\n"
    "v_mfma_f32_16x16x32_bf16 v[56:59], v[24:27], a[108:111], v[56:59]\n"
    "v_mfma_f32_16x16x32_bf16 v[60:63], v[28:31], a[108:111], v[60:63]\n"
    "v_mfma_f32_16x16x32_bf16 v[56:59], v[32:35], a[112:115], v[56:59]\n"
    "v_mfma_f32_16x16x32_bf16 v[60:63], v[36:39], a[112:115], v[60:63]\n"
    "buffer_load_dwordx4 v96, s[0:3], s43 offen offset:0 sc1 lds\n"
    "v_mfma_f32_16x16x32_bf16 v[56:59], v[40:43], a[116:119], v[56:59]\n"
    "v_mfma_f32_16x16x32_bf16 v[60:63], v[44:47], a[116:119], v[60:63]\n"
    "v_mfma_f32_16x16x32_bf16 v[48:51], v[0:3], a[120:123], 0\n"
    "s_nop 1\n"
    "v_cvt_pk_bf16_f32 v76, v56, v57\n"
    "v_mfma_f32_16x16x32_bf16 v[52:55], v[4:7], a[120:123], 0\n"
    "buffer_store_dwordx4 v[80:83], v96, s[0:3], s40 offen sc1\n"
    "v_cvt_pk_bf16_f32 v78, v58, v59\n"
    "v_cvt_pk_bf16_f32 v77, v60, v61\n"
    "s_add_u32 s40, s40, 0x10000\n"
    "v_mfma_f32_16x16x32_bf16 v[48:51], v[8:11], a[124:127], v[48:51]\n"
    "v_cvt_pk_bf16_f32 v79, v62, v63\n"
    "v_mfma_f32_16x16x32_bf16 v[52:55], v[12:15], a[124:127], v[52:55]\n"
    "v_mfma_f32_16x16x32_bf16 v[48:51], v[16:19], a[128:131], v[48:51]\n"
    "v_mfma_f32_16x16x32_bf16 v[52:55], v[20:23], a[128:131], v[52:55]\n"
    "v_mfma_f32_16x16x32_bf16 v[48:51], v[24:27], a[132:135], v[48:51]\n"
    "v_mfma_f32_16x16x32_bf16 v[52:55], v[28:31], a[132:135], v[52:55]\n"
    "v_mfma_f32_16x16x32_bf16 v[48:51], v[32:35], a[136:139], v[48:51]\n"
    "buffer_load_dwordx4 v96, s[0:3], s43 offen offset:1024 sc1 lds\n"
    "v_mfma_f32_16x16x32_bf16 v[52:55], v[36:39], a[136:139], v[52:55]\n"
    "v_mfma_f32_16x16x32_bf16 v[48:51], v[40:43], a[140:143], v[48:51]\n"
    ".Lvo_mkL2_%=:\n"
    "v_mfma_f32_16x16x32_bf16 v[52:55], v[44:47], a[140:143], v[52:55]\n"
    "v_mfma_f32_16x16x32_bf16 v[56:59], v[0:3], a[144:147], 0\n"
    "buffer_store_dwordx4 v[72:75], v96, s[0:3], s40 offen sc1\n"
    "s_nop 0\n"
    "v_cvt_pk_bf16_f32 v80, v48, v49\n"
    "s_add_u32 s40, s40, 0x10000\n"
    "v_mfma_f32_16x16x32_bf16 v[60:63], v[4:7], a[144:147], 0\n"
    "v_cvt_pk_bf16_f32 v82, v50, v51\n"
    "v_cvt_pk_bf16_f32 v81, v52, v53\n"
    "v_mfma_f32_16x16x32_bf16 v[56:59], v[8:11], a[148:151], v[56:59]\n"
    "v_cvt_pk_bf16_f32 v83, v54, v55\n"
    "v_mfma_f32_16x16x32_bf16 v[60:63], v[12:15], a[148:151], v[60:63]\n"
    "v_mfma_f32_16x16x32_bf16 v[56:59], v[16:19], a[152:155], v[56:59]\n"
    "v_mfma_f32_16x16x32_bf16 v[60:63], v[20:23], a[152:155], v[60:63]\n"
    "v_mfma_f32_16x16x32_bf16 v[56:59], v[24:27], a[156:159], v[56:59]\n"
    "v_mfma_f32_16x16x32_bf16 v[60:63], v[28:31], a[156:159], v[60:63]\n"
    "buffer_load_dwordx2 v[108:109], off, s[0:3], s45 sc1\n"
    "v_mfma_f32_16x16x32_bf16 v[56:59], v[32:35], a[160:163], v[56:59]\n"
    "v_mfma_f32_16x16x32_bf16 v[60:63], v[36:39], a[160:163], v[60:63]\n"
    "v_mfma_f32_16x16x32_bf16 v[56:59], v[40:43], a[164:167], v[56:59]\n"
    "v_mfma_f32_16x16x32_bf16 v[60:63], v[44:47], a[164:167], v[60:63]\n"
    "buffer_store_dwordx4 v[76:79], v96, s[0:3], s40 offen sc1\n"
    "s_add_u32 s40, s40, 0x10000\n"
    "v_mfma_f32_16x16x32_bf16 v[48:51], v[0:3], a[168:171], 0\n"
    "s_nop 1\n"
    "v_cvt_pk_bf16_f32 v72, v56, v57\n"
    "v_mfma_f32_16x16x32_bf16 v[52:55], v[4:7], a[168:171], 0\n"
    "v_cvt_pk_bf16_f32 v74, v58, v59\n"
    "v_cvt_pk_bf16_f32 v73, v60, v61\n"
    "v_mfma_f32_16x16x32_bf16 v[48:51], v[8:11], a[172:175], v[48:51]\n"
    "v_cvt_pk_bf16_f32 v75, v62, v63\n"
    "v_mfma_f32_16x16x32_bf16 v[52:55], v[12:15], a[172:175], v[52:55]\n"
    "v_mfma_f32_16x16x32_bf16 v[48:51], v[16:19], a[176:179], v[48:51]\n"
    "v_mfma_f32_16x16x32_bf16 v[52:55], v[20:23], a[176:179], v[52:55]\n"
    "v_mfma_f32_16x16x32_bf16 v[48:51], v[24:27], a[180:183], v[48:51]\n"
    "buffer_store_dwordx4 v[88:91], v102, s[0:3], s38 offen sc1\n"
    "v_mfma_f32_16x16x32_bf16 v[52:55], v[28:31], a[180:183], v[52:55]\n"
    "v_mfma_f32_16x16x32_bf16 v[48:51], v[32:35], a[184:187], v[48:51]\n"
    "v_mfma_f32_16x16x32_bf16 v[52:55], v[36:39], a[184:187], v[52:55]\n"
    "v_mfma_f32_16x16x32_bf16 v[48:51], v[40:43], a[188:191], v[48:51]\n"
    "v_mfma_f32_16x16x32_bf16 v[52:55], v[44:47], a[188:191], v[52:55]\n"
    "v_mfma_f32_16x16x32_bf16 v[56:59], v[0:3], a[192:195], 0\n"
    "s_nop 1\n"
    "v_cvt_pk_bf16_f32 v76, v48, v49\n"
    "v_mfma_f32_16x16x32_bf16 v[60:63], v[4:7], a[192:195], 0\n"
    "buffer_store_dwordx4 v[80:83], v96, s[0:3], s40 offen sc1\n"
    "v_cvt_pk_bf16_f32 v78, v50, v51\n"
    "v_cvt_pk_bf16_f32 v77, v52, v53\n"
    "s_add_u32 s40, s40, 0x10000\n"
    "v_mfma_f32_16x16x32_bf16 v[56:59], v[8:11], a[196:199], v[56:59]\n"
    "v_cvt_pk_bf16_f32 v79, v54, v55\n"
    "v_mfma_f32_16x16x32_bf16 v[60:63], v[12:15], a[196:199], v[60:63]\n"
    "v_mfma_f32_16x16x32_bf16 v[56:59], v[16:19], a[200:203], v[56:59]\n"
    ".Lvo_mkL3_%=:\n"
    "v_mfma_f32_16x16x32_bf16 v[60:63], v[20:23], a[200:203], v[60:63]\n"
    "v_mfma_f32_16x16x32_bf16 v[56:59], v[24:27], a[204:207], v[56:59]\n"
    "v_mfma_f32_16x16x32_bf16 v[60:63], v[28:31], a[204:207], v[60:63]\n"
    "v_mfma_f32_16x16x32_bf16 v[56:59], v[32:35], a[208:211], v[56:59]\n"
    "buffer_store_dwordx4 v[88:91], v102, s[0:3], s39 offen sc1\n"
    "v_mfma_f32_16x16x32_bf16 v[60:63], v[36:39], a[208:211], v[60:63]\n"
    "v_mfma_f32_16x16x32_bf16 v[56:59], v[40:43], a[212:215], v[56:59]\n"
    "v_mfma_f32_16x16x32_bf16 v[60:63], v[44:47], a[212:215], v[60:63]\n"
    "s_waitcnt vmcnt(15)\n"
    "s_barrier\n"
    "v_mfma_f32_16x16x32_bf16 v[48:51], v[24:27], a[228:231], 0\n"
    "s_nop 1\n"
    "v_cvt_pk_bf16_f32 v80, v56, v57\n"
    "v_mfma_f32_16x16x32_bf16 v[52:55], v[28:31], a[228:231], 0\n"
    "v_cvt_pk_bf16_f32 v82, v58, v59\n"
    "v_cvt_pk_bf16_f32 v81, v60, v61\n"
    "v_mfma_f32_16x16x32_bf16 v[48:51], v[32:35], a[232:235], v[48:51]\n"
    "ds_read_b128 v[24:27], v92 offset:0\n"
    "v_cvt_pk_bf16_f32 v83, v62, v63\n"
    "v_mfma_f32_16x16x32_bf16 v[52:55], v[36:39], a[232:235], v[52:55]\n"
    "buffer_store_dwordx4 v[72:75], v96, s[0:3], s40 offen sc1\n"
    "ds_read_b128 v[28:31], v92 offset:1024\n"
    "s_add_u32 s40, s40, 0x10000\n"
    "v_mfma_f32_16x16x32_bf16 v[48:51], v[40:43], a[236:239], v[48:51]\n"
    "ds_read_b128 v[32:35], v93 offset:0\n"
    "v_mfma_f32_16x16x32_bf16 v[52:55], v[44:47], a[236:239], v[52:55]\n"
    "ds_read_b128 v[36:39], v93 offset:1024\n"
    "v_mfma_f32_16x16x32_bf16 v[48:51], v[0:3], a[216:219], v[48:51]\n"
    "ds_read_b128 v[40:43], v94 offset:0\n"
    "v_mfma_f32_16x16x32_bf16 v[52:55], v[4:7], a[216:219], v[52:55]\n"
    "ds_read_b128 v[44:47], v94 offset:1024\n"
    "v_mfma_f32_16x16x32_bf16 v[48:51], v[8:11], a[220:223], v[48:51]\n"
    ".Lvo_mkL4_%=:\n"
    "v_mfma_f32_16x16x32_bf16 v[52:55], v[12:15], a[220:223], v[52:55]\n"
    "v_mfma_f32_16x16x32_bf16 v[48:51], v[16:19], a[224:227], v[48:51]\n"
    "buffer_store_dwordx2 v[88:89], v103, s[0:3], s38 offen sc1\n"
    "v_mfma_f32_16x16x32_bf16 v[52:55], v[20:23], a[224:227], v[52:55]\n"
    ".Lvo_mk4x10_%=:\n"
    ".Lvo_vph_%=:\n"
    "s_waitcnt lgkmcnt(4)\n"
    "v_mfma_f32_16x16x32_bf16 v[64:67], a[240:243], v[24:27], 0\n"
    "s_nop 1\n"
    "v_cvt_pk_bf16_f32 v72, v48, v49\n"
    "v_mfma_f32_16x16x32_bf16 v[84:87], v[176:179], v[24:27], 0\n"
    "s_add_u32 s34, s34, 1\n"
    "v_cvt_pk_bf16_f32 v74, v50, v51\n"
    "v_cvt_pk_bf16_f32 v73, v52, v53\n"
    "v_mfma_f32_16x16x32_bf16 v[68:71], a[240:243], v[28:31], 0\n"
    "s_add_u32 s36, s34, 0\n"
    "ds_read_b128 v[24:27], v95 offset:0\n"
    "v_cvt_pk_bf16_f32 v75, v54, v55\n"
    "v_mfma_f32_16x16x32_bf16 v[88:91], v[176:179], v[28:31], 0\n"
    "s_waitcnt lgkmcnt(3)\n"
    "ds_read_b128 v[28:31], v95 offset:1024\n"
    "s_and_b32 s36, s36, 3\n"
    "v_mfma_f32_16x16x32_bf16 v[64:67], v[112:115], v[32:35], v[64:67]\n"
    "s_lshl_b32 s36, s36, 22\n"
    "s_add_u32 s41, s15, s36\n"
    "s_add_u32 s42, s16, s36\n"
    "v_mfma_f32_16x16x32_bf16 v[84:87], v[192:195], v[32:35], v[84:87]\n"
    "s_add_u32 s37, s36, 0x10000\n"
    "s_add_u32 s50, s34, 3\n"
    "ds_read_b128 v[0:3], v97 offset:0\n"
    "v_mfma_f32_16x16x32_bf16 v[68:71], v[112:115], v[36:39], v[68:71]\n"
    "buffer_store_dwordx4 v[76:79], v96, s[0:3], s40 offen sc1\n"
    "ds_read_b128 v[32:35], v92 offset:2048\n"
    "s_add_u32 s40, s40, 0x10000\n"
    "s_cmp_gt_i32 s50, 64\n"
    "s_cselect_b32 s50, 0, s50\n"
    "v_mfma_f32_16x16x32_bf16 v[88:91], v[192:195], v[36:39], v[88:91]\n"
    "s_waitcnt lgkmcnt(4)\n"
    "ds_read_b128 v[36:39], v92 offset:3072\n"
    "s_add_u32 s51, s34, 4\n"
    "v_mfma_f32_16x16x32_bf16 v[64:67], v[128:131], v[40:43], v[64:67]\n"
    "s_cmp_gt_i32 s51, 64\n"
    "s_cselect_b32 s51, 0, s51\n"
    "s_mul_i32 s51, s51, s14\n"
    "v_mfma_f32_16x16x32_bf16 v[84:87], v[208:211], v[40:43], v[84:87]\n"
    "ds_read_b128 v[4:7], v97 offset:1024\n"
    "v_mov_b32 v111, s50\n"
    "v_mfma_f32_16x16x32_bf16 v[68:71], v[128:131], v[44:47], v[68:71]\n"
    "ds_read_b128 v[40:43], v93 offset:2048\n"
    "v_mfma_f32_16x16x32_bf16 v[88:91], v[208:211], v[44:47], v[88:91]\n"
    "s_waitcnt lgkmcnt(5)\n"
    "ds_read_b128 v[44:47], v93 offset:3072\n"
    "v_mfma_f32_16x16x32_bf16 v[64:67], v[144:147], v[24:27], v[64:67]\n"
    "s_mov_b64 exec, 0x1f0\n"
    "v_mov_b32 v111, s51\n"
    "s_mov_b64 exec, -1\n"
    "v_mfma_f32_16x16x32_bf16 v[84:87], v[224:227], v[24:27], v[84:87]\n"
    "buffer_store_dwordx4 v[80:83], v104, s[0:3], s38 offen sc1\n"
    "ds_read_b128 v[8:11], v97 offset:2048\n"
    "v_mfma_f32_16x16x32_bf16 v[68:71], v[144:147], v[28:31], v[68:71]\n"
    "ds_read_b128 v[24:27], v94 offset:2048\n"
    "v_mfma_f32_16x16x32_bf16 v[88:91], v[224:227], v[28:31], v[88:91]\n"
    "s_waitcnt lgkmcnt(5)\n"
    "ds_read_b128 v[28:31], v94 offset:3072\n"
    "v_mfma_f32_16x16x32_bf16 v[64:67], a[244:247], v[32:35], v[64:67]\n"
    "ds_read_b128 v[12:15], v97 offset:3072\n"
    "v_mfma_f32_16x16x32_bf16 v[84:87], v[180:183], v[32:35], v[84:87]\n"
    "ds_read_b128 v[16:19], v97 offset:4096\n"
    "v_mfma_f32_16x16x32_bf16 v[68:71], a[244:247], v[36:39], v[68:71]\n"
    "ds_read_b128 v[32:35], v95 offset:2048\n"
    "v_mfma_f32_16x16x32_bf16 v[88:91], v[180:183], v[36:39], v[88:91]\n"
    "s_waitcnt lgkmcnt(6)\n"
    "ds_read_b128 v[36:39], v95 offset:3072\n"
    "v_mfma_f32_16x16x32_bf16 v[64:67], v[116:119], v[40:43], v[64:67]\n"
    "buffer_store_dwordx4 v[72:75], v104, s[0:3], s39 offen sc1\n"
    "s_waitcnt vmcnt(11)\n"
    "ds_read_b128 v[20:23], v97 offset:5120\n"
    "v_mfma_f32_16x16x32_bf16 v[84:87], v[196:199], v[40:43], v[84:87]\n"
    "s_barrier\n"
    "ds_read_b128 v[48:51], v97 offset:8192\n"
    "v_mfma_f32_16x16x32_bf16 v[68:71], v[116:119], v[44:47], v[68:71]\n"
    "ds_read_b128 v[40:43], v92 offset:4096\n"
    "v_mfma_f32_16x16x32_bf16 v[88:91], v[196:199], v[44:47], v[88:91]\n"
    "s_waitcnt lgkmcnt(7)\n"
    "ds_read_b128 v[44:47], v92 offset:5120\n"
    "s_waitcnt vmcnt(9)\n"
    "v_mfma_f32_16x16x32_bf16 v[64:67], v[132:135], v[24:27], v[64:67]\n"
    "s_memrealtime s[54:55]\n"
    "v_readfirstlane_b32 s32, v108\n"
    "v_readfirstlane_b32 s33, v109\n"
    ".Lvo_mkL5_%=:\n"
    "v_mfma_f32_16x16x32_bf16 v[84:87], v[212:215], v[24:27], v[84:87]\n"
    "ds_read_b128 v[52:55], v97 offset:9216\n"
    "v_mfma_f32_16x16x32_bf16 v[68:71], v[132:135], v[28:31], v[68:71]\n"
    "s_cmp_eq_u32 s33, s35\n"
    "s_cselect_b32 s46, s32, s46\n"
    "ds_read_b128 v[24:27], v93 offset:4096\n"
    "v_mfma_f32_16x16x32_bf16 v[88:91], v[212:215], v[28:31], v[88:91]\n"
    "s_waitcnt lgkmcnt(6)\n"
    "ds_read_b128 v[28:31], v93 offset:5120\n"
    "s_add_u32 s46, s46, 264\n"
    "v_mfma_f32_16x16x32_bf16 v[64:67], v[148:151], v[32:35], v[64:67]\n"
    "s_sub_u32 s47, s46, 59\n"
    "s_mov_b32 m0, s17\n"
    "ds_read_b128 v[56:59], v97 offset:10240\n"
    "v_mfma_f32_16x16x32_bf16 v[84:87], v[228:231], v[32:35], v[84:87]\n"
    "ds_read_b128 v[60:63], v97 offset:11264\n"
    ".Lvo_mkL6_%=:\n"
    "v_mfma_f32_16x16x32_bf16 v[68:71], v[148:151], v[36:39], v[68:71]\n"
    "ds_read_b128 v[32:35], v94 offset:4096\n"
    "s_sub_u32 s56, s47, s54\n"
    "v_mfma_f32_16x16x32_bf16 v[88:91], v[228:231], v[36:39], v[88:91]\n"
    "s_waitcnt lgkmcnt(6)\n"
    "ds_read_b128 v[36:39], v94 offset:5120\n"
    ".Lvo_mk6x11_%=:\n"
    "s_cmp_gt_i32 s56, 4\n"
    "s_cbranch_scc1 .Lvo_early_%=\n"
    "buffer_load_dwordx4 v96, s[0:3], s41 offen sc1 lds\n"
    "buffer_load_dwordx4 v96, s[0:3], s41 offen offset:1024 sc1 lds\n"
    ".Lvo_asked_%=:\n"
    ".Lvo_mk7x13_%=:\n"
    "v_mfma_f32_16x16x32_bf16 v[64:67], a[248:251], v[40:43], v[64:67]\n"
    "s_add_u32 s49, s34, 3\n"
    "s_lshl_b32 s43, s49, 19\n"
    "s_lshl_b32 s33, s49, 11\n"
    "v_mfma_f32_16x16x32_bf16 v[84:87], v[184:187], v[40:43], v[84:87]\n"
    "s_xor_b32 s33, s33, s13\n"
    "s_and_b32 s33, s33, 0x2000\n"
    "s_add_u32 s43, s43, s33\n"
    ".Lvo_mkM0_%=:\n"
    "v_mfma_f32_16x16x32_bf16 v[68:71], a[248:251], v[44:47], v[68:71]\n"
    "ds_read_b128 v[40:43], v95 offset:4096\n"
    "s_add_u32 s43, s43, s18\n"
    "v_mfma_f32_16x16x32_bf16 v[88:91], v[184:187], v[44:47], v[88:91]\n"
    "s_waitcnt lgkmcnt(5)\n"
    "ds_read_b128 v[44:47], v95 offset:5120\n"
    "v_mfma_f32_16x16x32_bf16 v[64:67], v[120:123], v[24:27], v[64:67]\n"
    "s_cmp_gt_i32 s49, 63\n"
    "s_cselect_b32 s43, 2130706432, s43\n"
    "s_add_u32 s49, s34, 2\n"
    "v_mfma_f32_16x16x32_bf16 v[84:87], v[200:203], v[24:27], v[84:87]\n"
    "s_lshl_b32 s44, s49, 20\n"
    "s_lshl_b32 s33, s49, 12\n"
    "s_xor_b32 s33, s33, s13\n"
    "v_mfma_f32_16x16x32_bf16 v[68:71], v[120:123], v[28:31], v[68:71]\n"
    "ds_read_b128 v[24:27], v92 offset:6144\n"
    "s_and_b32 s33, s33, 0x2000\n"
    "v_mfma_f32_16x16x32_bf16 v[88:91], v[200:203], v[28:31], v[88:91]\n"
    "s_waitcnt lgkmcnt(3)\n"
    "ds_read_b128 v[28:31], v92 offset:7168\n"
    "s_add_u32 s44, s44, s33\n"
    "v_mfma_f32_16x16x32_bf16 v[64:67], v[136:139], v[32:35], v[64:67]\n"
    "buffer_load_dwordx4 v96, s[0:3], s42 offen offset:2048 sc1 lds\n"
    "v_mfma_f32_16x16x32_bf16 v[84:87], v[216:219], v[32:35], v[84:87]\n"
    "s_add_u32 s44, s44, s20\n"
    "s_cmp_gt_i32 s49, 63\n"
    "s_cselect_b32 s44, 2130706432, s44\n"
    "v_mfma_f32_16x16x32_bf16 v[68:71], v[136:139], v[36:39], v[68:71]\n"
    "ds_read_b128 v[32:35], v93 offset:6144\n"
    "s_add_u32 s38, s34, 2\n"
    ".Lvo_mkM1_%=:\n"
    "v_mfma_f32_16x16x32_bf16 v[88:91], v[216:219], v[36:39], v[88:91]\n"
    "s_waitcnt lgkmcnt(3)\n"
    "ds_read_b128 v[36:39], v93 offset:7168\n"
    "s_and_b32 s38, s38, 3\n"
    "v_mfma_f32_16x16x32_bf16 v[64:67], v[152:155], v[40:43], v[64:67]\n"
    "s_lshl_b32 s38, s38, 22\n"
    "s_add_u32 s39, s38, 0x10000\n"
    "s_add_u32 s40, s22, s38\n"
    "v_mfma_f32_16x16x32_bf16 v[84:87], v[232:235], v[40:43], v[84:87]\n"
    "s_and_b32 s45, s34, 63\n"
    "s_lshl_b32 s45, s45, 7\n"
    "s_add_u32 s45, s23, s45\n"
    "v_mfma_f32_16x16x32_bf16 v[68:71], v[152:155], v[44:47], v[68:71]\n"
    "ds_read_b128 v[40:43], v94 offset:6144\n"
    "s_add_u32 s35, s34, 1\n"
    "v_mfma_f32_16x16x32_bf16 v[88:91], v[232:235], v[44:47], v[88:91]\n"
    "s_waitcnt lgkmcnt(3)\n"
    "ds_read_b128 v[44:47], v94 offset:7168\n"
    "v_mfma_f32_16x16x32_bf16 v[64:67], a[252:255], v[24:27], v[64:67]\n"
    "s_cmp_lt_i32 s34, 0\n"
    "s_cselect_b32 s35, -5, s35\n"
    "s_cselect_b32 s52, 0, -1\n"
    "v_mfma_f32_16x16x32_bf16 v[84:87], v[188:191], v[24:27], v[84:87]\n"
    "buffer_load_dword v110, v105, s[0:3], 0 offen sc1\n"
    "v_mfma_f32_16x16x32_bf16 v[68:71], a[252:255], v[28:31], v[68:71]\n"
    "ds_read_b128 v[24:27], v95 offset:6144\n"
    "v_mfma_f32_16x16x32_bf16 v[88:91], v[188:191], v[28:31], v[88:91]\n"
    "s_waitcnt lgkmcnt(3)\n"
    "ds_read_b128 v[28:31], v95 offset:7168\n"
    "v_mfma_f32_16x16x32_bf16 v[64:67], v[124:127], v[32:35], v[64:67]\n"
    ".Lvo_mkM2_%=:\n"
    "v_mfma_f32_16x16x32_bf16 v[84:87], v[204:207], v[32:35], v[84:87]\n"
    "v_mfma_f32_16x16x32_bf16 v[68:71], v[124:127], v[36:39], v[68:71]\n"
    "ds_read_b128 v[32:35], v96 offset:53248\n"
    "v_mfma_f32_16x16x32_bf16 v[88:91], v[204:207], v[36:39], v[88:91]\n"
    "s_waitcnt lgkmcnt(3)\n"
    "ds_read_b128 v[36:39], v96 offset:54272\n"
    "v_mfma_f32_16x16x32_bf16 v[64:67], v[140:143], v[40:43], v[64:67]\n"
    "v_mfma_f32_16x16x32_bf16 v[84:87], v[220:223], v[40:43], v[84:87]\n"
    "v_mfma_f32_16x16x32_bf16 v[68:71], v[140:143], v[44:47], v[68:71]\n"
    "ds_read_b128 v[40:43], v96 offset:55296\n"
    ".Lvo_mkM3_%=:\n"
    "v_mfma_f32_16x16x32_bf16 v[88:91], v[220:223], v[44:47], v[88:91]\n"
    "s_waitcnt lgkmcnt(3)\n"
    "ds_read_b128 v[44:47], v96 offset:56320\n"
    "v_mfma_f32_16x16x32_bf16 v[64:67], v[156:159], v[24:27], v[64:67]\n"
    "v_mfma_f32_16x16x32_bf16 v[84:87], v[236:239], v[24:27], v[84:87]\n"
    "v_mfma_f32_16x16x32_bf16 v[68:71], v[156:159], v[28:31], v[68:71]\n"
    "ds_read_b128 v[24:27], v96 offset:57344\n"
    "v_mfma_f32_16x16x32_bf16 v[88:91], v[236:239], v[28:31], v[88:91]\n"
    "s_waitcnt lgkmcnt(3)\n"
    "ds_read_b128 v[28:31], v96 offset:58368\n"
    "v_mfma_f32_16x16x32_bf16 v[64:67], v[160:163], v[32:35], v[64:67]\n"
    "v_mfma_f32_16x16x32_bf16 v[84:87], v[240:243], v[32:35], v[84:87]\n"
    "ds_read_b128 v[32:35], v96 offset:59392\n"
    "v_mfma_f32_16x16x32_bf16 v[68:71], v[160:163], v[36:39], v[68:71]\n"
    "v_mfma_f32_16x16x32_bf16 v[88:91], v[240:243], v[36:39], v[88:91]\n"
    "s_waitcnt lgkmcnt(3)\n"
    "ds_read_b128 v[36:39], v96 offset:60416\n"
    "v_mfma_f32_16x16x32_bf16 v[64:67], v[164:167], v[40:43], v[64:67]\n"
    "s_waitcnt lgkmcnt(2)\n"
    "v_mfma_f32_16x16x32_bf16 v[64:67], v[168:171], v[24:27], v[64:67]\n"
    "s_waitcnt lgkmcnt(1)\n"
    "v_mfma_f32_16x16x32_bf16 v[64:67], v[172:175], v[32:35], v[64:67]\n"
    ".Lvo_mkM4_%=:\n"
    "v_mfma_f32_16x16x32_bf16 v[84:87], v[244:247], v[40:43], v[84:87]\n"
    "s_branch .Lvo_take_%=\n"
    ".Lvo_tk0_%=:\n"
    ".Lvo_po3_%=:\n"
    "buffer_load_dword v109, v108, s[0:3], 0 offen sc1\n"
    "s_waitcnt vmcnt(0)\n"
    "v_cmp_gt_i32 vcc, 32, v109\n"
    "s_nop 0\n"
    "s_cbranch_vccz .Lvo_pd4_%=\n"
    "s_add_u32 s28, s28, 1\n"
    "s_cmp_lt_u32 s28, 0x40000\n"
    "s_cbranch_scc1 .Lvo_sp5_%=\n"
    "v_mov_b32 v109, 90\n"
    "s_mov_b32 s32, 33728\n"
    "buffer_store_dword v109, off, s[0:3], s32 sc1\n"
    "s_waitcnt vmcnt(0)\n"
    "s_branch .Lvo_pd4_%=\n"
    ".Lvo_sp5_%=:\n"
    "s_sleep 8\n"
    "s_branch .Lvo_po3_%=\n"
    ".Lvo_pd4_%=:\n"
    "s_branch .Lvo_tk1_%=\n"
    ".Lvo_early_%=:\n"
    ".Lvo_rt13_%=:\n"
    "s_memrealtime s[30:31]\n"
    "s_waitcnt lgkmcnt(0)\n"
    "s_sub_u32 s32, s47, s30\n"
    "s_cmp_lt_u32 s32, 600\n"
    "s_cbranch_scc1 .Lvo_rt13_%=\n"
    "buffer_load_dwordx4 v96, s[0:3], s41 offen sc1 lds\n"
    "buffer_load_dwordx4 v96, s[0:3], s41 offen offset:1024 sc1 lds\n"
    "s_branch .Lvo_asked_%=\n"
    ".Lvo_retry_%=:\n"
    "s_nop 7\n"
    "s_nop 7\n"
    "s_cmp_lt_i32 s34, 0\n"
    "s_cbranch_scc0 .Lvo_again_%=\n"
    "v_cvt_pk_bf16_f32 v44, v68, v69\n"
    "v_cvt_pk_bf16_f32 v45, v70, v71\n"
    "v_cvt_pk_bf16_f32 v46, v88, v89\n"
    "v_cvt_pk_bf16_f32 v47, v90, v91\n"
    "ds_write_b128 v92, v[44:47] offset:12288\n"
    "s_waitcnt vmcnt(0) lgkmcnt(0)\n"
    "s_barrier\n"
    "v_cmp_gt_i32 vcc, v111, v110\n"
    "s_nop 0\n"
    "s_cbranch_vccz .Lvo_e2_%=\n"
    ".Lvo_po15_%=:\n"
    "buffer_load_dword v110, v105, s[0:3], 0 offen sc1\n"
    "s_waitcnt vmcnt(0)\n"
    "v_cmp_gt_i32 vcc, v111, v110\n"
    "s_nop 0\n"
    "s_cbranch_vccz .Lvo_pd16_%=\n"
    "s_add_u32 s28, s28, 1\n"
    "s_cmp_lt_u32 s28, 0x40000\n"
    "s_cbranch_scc1 .Lvo_sp17_%=\n"
    "v_mov_b32 v110, 32\n"
    "s_mov_b32 s32, 33728\n"
    "buffer_store_dword v110, off, s[0:3], s32 sc1\n"
    "s_waitcnt vmcnt(0)\n"
    "s_branch .Lvo_pd16_%=\n"
    ".Lvo_sp17_%=:\n"
    "s_sleep 4\n"
    "s_branch .Lvo_po15_%=\n"
    ".Lvo_pd16_%=:\n"
    ".Lvo_e2_%=:\n"
    "s_mov_b32 m0, s21\n"
    "s_nop 0\n"
    "buffer_load_dwordx4 v96, s[0:3], s44 offen sc1 lds\n"
    "s_sub_u32 m0, s17, 8192\n"
    "s_nop 0\n"
    "buffer_load_dwordx4 v96, s[0:3], s43 offen offset:0 sc1 lds\n"
    "buffer_load_dwordx4 v96, s[0:3], s43 offen offset:1024 sc1 lds\n"
    "s_add_u32 s49, s34, 3\n"
    "v_lshlrev_b32 v108, 4, v96\n"
    "v_mov_b32 v109, s49\n"
    "s_mov_b64 exec, 15\n"
    "buffer_store_dword v109, v108, s[0:3], s25 offen sc1\n"
    "s_mov_b64 exec, -1\n"
    "ds_read_b128 v[0:3], v96 offset:11264\n"
    "ds_read_b128 v[4:7], v96 offset:12288\n"
    "ds_read_b128 v[8:11], v96 offset:24576\n"
    "ds_read_b128 v[12:15], v96 offset:25600\n"
    "ds_read_b128 v[16:19], v96 offset:37888\n"
    "ds_read_b128 v[20:23], v96 offset:38912\n"
    "ds_read_b128 v[24:27], v96 offset:51200\n"
    "ds_read_b128 v[28:31], v96 offset:52224\n"
    "s_waitcnt vmcnt(3) lgkmcnt(0)\n"
    "s_barrier\n"
    "ds_read2_b64 v[32:35], v106 offset0:0 offset1:128\n"
    "ds_read2_b64 v[36:39], v106 offset0:1 offset1:129\n"
    "v_mfma_f32_16x16x32_bf16 v[40:43], v[0:3], a[0:3], 0\n"
    "v_mfma_f32_16x16x32_bf16 v[44:47], v[4:7], a[0:3], 0\n"
    "v_mfma_f32_16x16x32_bf16 v[40:43], v[8:11], a[4:7], v[40:43]\n"
    "v_mfma_f32_16x16x32_bf16 v[44:47], v[12:15], a[4:7], v[44:47]\n"
    "v_mfma_f32_16x16x32_bf16 v[40:43], v[16:19], a[8:11], v[40:43]\n"
    "v_mfma_f32_16x16x32_bf16 v[44:47], v[20:23], a[8:11], v[44:47]\n"
    "v_mfma_f32_16x16x32_bf16 v[40:43], v[24:27], a[12:15], v[40:43]\n"
    "v_mfma_f32_16x16x32_bf16 v[44:47], v[28:31], a[12:15], v[44:47]\n"
    "s_waitcnt lgkmcnt(0)\n"
    "s_mov_b32 s37, 2130706432\n"
    "s_mov_b32 s36, 2130706432\n"
    "s_nop 7\n"
    "s_branch .Lvo_uph_%=\n"
    ".Lvo_e0_%=:\n"
    ".Lvo_po18_%=:\n"
    "buffer_load_dword v109, v108, s[0:3], s24 offen sc1\n"
    "s_waitcnt vmcnt(0)\n"
    "v_cmp_gt_i32 vcc, 1, v109\n"
    "s_nop 0\n"
    "s_cbranch_vccz .Lvo_pd19_%=\n"
    "s_add_u32 s28, s28, 1\n"
    "s_cmp_lt_u32 s28, 0x40000\n"
    "s_cbranch_scc1 .Lvo_sp20_%=\n"
    "v_mov_b32 v109, 30\n"
    "s_mov_b32 s32, 33728\n"
    "buffer_store_dword v109, off, s[0:3], s32 sc1\n"
    "s_waitcnt vmcnt(0)\n"
    "s_branch .Lvo_pd19_%=\n"
    ".Lvo_sp20_%=:\n"
    "s_sleep 4\n"
    "s_branch .Lvo_po18_%=\n"
    ".Lvo_pd19_%=:\n"
    ".Lvo_po21_%=:\n"
    "buffer_load_dword v110, v105, s[0:3], 0 offen sc1\n"
    "s_waitcnt vmcnt(0)\n"
    "v_cmp_gt_i32 vcc, v111, v110\n"
    "s_nop 0\n"
    "s_cbranch_vccz .Lvo_pd22_%=\n"
    "s_add_u32 s28, s28, 1\n"
    "s_cmp_lt_u32 s28, 0x40000\n"
    "s_cbranch_scc1 .Lvo_sp23_%=\n"
    "v_mov_b32 v110, 31\n"
    "s_mov_b32 s32, 33728\n"
    "buffer_store_dword v110, off, s[0:3], s32 sc1\n"
    "s_waitcnt vmcnt(0)\n"
    "s_branch .Lvo_pd22_%=\n"
    ".Lvo_sp23_%=:\n"
    "s_sleep 4\n"
    "s_branch .Lvo_po21_%=\n"
    ".Lvo_pd22_%=:\n"
    "s_branch .Lvo_e1_%=\n"
    ".Lvo_again_%=:\n"
    "s_add_u32 s28, s28, 1\n"
    "s_cmp_lt_u32 s28, 0x40000\n"
    "s_cbranch_scc1 .Lvo_sp24_%=\n"
    "v_mov_b32 v108, 2\n"
    "s_mov_b32 s32, 33728\n"
    "buffer_store_dword v108, off, s[0:3], s32 sc1\n"
    "s_waitcnt vmcnt(0)\n"
    "s_branch .Lvo_go_%=\n"
    ".Lvo_sp24_%=:\n"
    "buffer_load_dwordx4 v96, s[0:3], s41 offen sc1 lds\n"
    "buffer_load_dwordx4 v96, s[0:3], s41 offen offset:1024 sc1 lds\n"
    "buffer_load_dwordx4 v96, s[0:3], s42 offen offset:2048 sc1 lds\n"
    ".Lvo_ag1_%=:\n"
    "s_waitcnt vmcnt(0)\n"
    "ds_read_b128 v[72:75], v92 offset:8192\n"
    "ds_read_b128 v[76:79], v92 offset:9216\n"
    "s_waitcnt lgkmcnt(0)\n"
    "v_mov_b64 v[64:65], 0\n"
    "v_mov_b64 v[66:67], 0\n"
    "v_max_u32 v108, v72, v74\n"
    "v_max3_u32 v108, v108, v76, v78\n"
    "v_cmp_le_u32 vcc, s52, v108\n"
    "s_nop 0\n"
    "s_cbranch_vccnz .Lvo_again_%=\n"
    "v_smfmac_f32_16x16x64_bf16 v[64:67], v[72:75], a[240:247], v98\n"
    "s_branch .Lvo_go_%=\n"
    ".Lvo_r2_%=:\n"
    "s_add_u32 s28, s28, 1\n"
    "s_cmp_lt_u32 s28, 0x40000\n"
    "s_cbranch_scc1 .Lvo_sp25_%=\n"
    "v_mov_b32 v108, 6\n"
    "s_mov_b32 s32, 33728\n"
    "buffer_store_dword v108, off, s[0:3], s32 sc1\n"
    "s_waitcnt vmcnt(0)\n"
    "s_branch .Lvo_go2_%=\n"
    ".Lvo_sp25_%=:\n"
    "buffer_load_dwordx4 v96, s[0:3], s42 offen offset:2048 sc1 lds\n"
    ".Lvo_r3_%=:\n"
    "s_waitcnt vmcnt(0)\n"
    "ds_read_b128 v[80:83], v92 offset:10240\n"
    "s_waitcnt lgkmcnt(0)\n"
    "v_max_u32 v108, v80, v82\n"
    "v_cmp_eq_u32 vcc, -1, v108\n"
    "s_nop 0\n"
    "s_cbranch_vccnz .Lvo_r2_%=\n"
    "s_branch .Lvo_go2_%=\n"
    ".Lvo_wf_%=:\n"
    ".Lvo_po26_%=:\n"
    "buffer_load_dword v110, v105, s[0:3], 0 offen sc1\n"
    "s_waitcnt vmcnt(0)\n"
    "v_cmp_gt_i32 vcc, v111, v110\n"
    "s_nop 0\n"
    "s_cbranch_vccz .Lvo_pd27_%=\n"
    "s_add_u32 s28, s28, 1\n"
    "s_cmp_lt_u32 s28, 0x40000\n"
    "s_cbranch_scc1 .Lvo_sp28_%=\n"
    "v_mov_b32 v110, 33\n"
    "s_mov_b32 s32, 33728\n"
    "buffer_store_dword v110, off, s[0:3], s32 sc1\n"
    "s_waitcnt vmcnt(0)\n"
    "s_branch .Lvo_pd27_%=\n"
    ".Lvo_sp28_%=:\n"
    "s_sleep 4\n"
    "s_branch .Lvo_po26_%=\n"
    ".Lvo_pd27_%=:\n"
    "s_branch .Lvo_wfb_%=\n"
    ".Lvo_end_%=:\n"
    "s_waitcnt vmcnt(0) lgkmcnt(0)\n"
    "s_endpgm\n"
    :: "{s[0:3]}"(s0_), "{s[4:7]}"(s4_), "{s[8:11]}"(s8_), "{s[12:15]}"(s12_), "{s[16:19]}"(s16_), "{s[20:23]}"(s20_), "{s[24:27]}"(s24_), "{v[0:3]}"(v0_), "{v[4:7]}"(v4_), "{v[8:11]}"(v8_), "{v[12:15]}"(v12_), "{v[16:19]}"(v16_), "{v[92:95]}"(v92_), "{v[96:99]}"(v96_), "{v[100:103]}"(v100_), "{v[104:107]}"(v104_) : "memory", "v255", "a255", "s95");
    __builtin_unreachable();
}
DEV void ct_role(const MegaArgs& A, const int L, const int u, char* smem) {
    ROLE_HEAD;
    const int r5 = l / 5, q5 = l - 5 * r5, c0 = q5 < 4 ? 16 * u + 4 * q5 : 512 + 4 * u, tk = 8 * w + r5;
    u32x2 m0;
    {
        const int sg = 2 * u + L, rt = w & 1, j0 = 10 * (w >> 1);
        float* ex = (float*)(smem + 1024);
        u32x4 xa[10];
        const unsigned xo = ((32 * sg + 16 * rt + l16) * D_ + 32 * j0 + 8 * lq) * 2;
#pragma unroll
        for (int j = 0; j < 10; ++j) asm volatile("global_load_dwordx4 %0, %1, %2 offset:%3" : "=&v"(xa[j]) : "v"(xo), "s"(A.x0), "n"(64 * j) : "memory");
        asm volatile("global_load_dwordx2 %0, %1, %2" : "=&v"(m0) : "v"(((r5 & 7) * D_ + c0) * 2), "s"(A.mem0) : "memory");
        asm volatile("s_waitcnt vmcnt(0)" ::: "memory");
#pragma unroll
        for (int j = 0; j < 10; ++j) asm volatile("" : "+v"(xa[j]));
        asm volatile("" : "+v"(m0));
        float s = 0.f, q = 0.f;
#pragma unroll
        for (int j = 0; j < 10; ++j)
#pragma unroll
            for (int e = 0; e < 4; ++e) s += lo16(xa[j][e]) + hi16(xa[j][e]);
        s += __shfl_xor(s, 16); s += __shfl_xor(s, 32);
        ex[threadIdx.x] = s;
        asm volatile("s_waitcnt lgkmcnt(0)\n\ts_barrier" ::: "memory");
        const float mean = (ex[64 * rt + l16] + ex[128 + 64 * rt + l16]) * (1.0f / D_);
#pragma unroll
        for (int j = 0; j < 10; ++j)
#pragma unroll
            for (int e = 0; e < 4; ++e) { const float d = lo16(xa[j][e]) - mean, d2 = hi16(xa[j][e]) - mean; q += d * d; q += d2 * d2; }
        q += __shfl_xor(q, 16); q += __shfl_xor(q, 32);
        ex[256 + threadIdx.x] = q;
        asm volatile("s_waitcnt lgkmcnt(0)\n\ts_barrier" ::: "memory");
        q = ex[256 + 64 * rt + l16] + ex[384 + 64 * rt + l16];
        const float mb = rb(mean), rd = __builtin_amdgcn_rcpf(rb(sqrtf(rb(rb(q * (1.0f / D_)) + 1e-5f))));
#pragma unroll
        for (int j = 0; j < 10; ++j) {
            u32x4 o;
#pragma unroll
            for (int e = 0; e < 4; ++e) { const unsigned d = pk2(lo16(xa[j][e]) - mb, hi16(xa[j][e]) - mb); o[sw2(e)] = pk2(lo16(d) * rd, hi16(d) * rd); }
            bst16(rr, rec(sg, 0) + ((j0 + j) >> 2) * 16384 + ((j0 + j) & 3) * 2048 + rt * 1024 + l * 16, o);
        }
    }
#define XM 5
    const unsigned mpos = l < 40 && q5 < XM ? xl(RG_MEM + w * 16384 + (c0 >> 6) * 1024 + (((c0 >> 4) & 3) * 16 + 4 * (r5 >> 1) + ((c0 >> 1) & 2) + (r5 & 1)) * 16 + (c0 & 8), L) : OOB;
    const unsigned xpos = l < 40 ? (c0 >> 7) * 16384 + ((c0 >> 5) & 3) * 2048 + (tk >> 4) * 1024 + (((c0 >> 3) & 3) * 16 + (tk & 15)) * 16 + (c0 & 4) * 2 : OOB;
    const unsigned rpos = (L || l >= 40) ? xpos + RC_X2 : tk * 1280 + c0 * 2;
    const u32x4 rs = L ? rx : u32x4{lo(A.x0), hi(A.x0), 2048u * 1280u, 0x20000u};
#pragma unroll
    for (int k = 0; k < 4; ++k) bst16(rr, rec(2 * u + (k >> 1), L) + RC_X1 + 65536 + w * 2048 + (k & 1) * 1024 + l * 16, ones);
    bst8(rr, mpos, u32x2{nosent(m0[0]), m0[1]}); bst8(rr, mpos + SLOT, ones2); bst8(rr, mpos + 2 * SLOT, ones2);
    wait_drawn(A.ar);
    DRAIN_BARRIER();
    bst4(rr, threadIdx.x < 16 ? xl(Z_CT + (l & 7) * 256 + (32 * L + u) * 4, l >> 3) : OOB, 1u);
    if (!((A.layers >> L) & 1)) return;
    const u32x4 s0_ = {RFL(rx[0]), RFL(rx[1]), RFL(rx[2]), RFL(rx[3])};
    const u32x4 s4_ = {RFL(rs[0]), RFL(rs[1]), RFL(rs[2]), RFL(rs[3])};
    const u32x4 s8_ = {RFL(L << 13), RFL(REC0), RFL(xl(RG_UT + (u * 4 + w) * 8192, L)), RFL(xl(RG_UTX + (u * 4 + w) * 2048, L))};
    const u32x4 s12_ = {RFL(xl(RG_UM + (u * 4 + w) * 2048, L)), RFL(xl(RG_UMX + (u * 4 + w) * 512, L)), RFL(xl(RG_PT + w * 16384, L)), RFL(xl(RG_PM + w * 8192, L))};
    const u32x4 s16_ = {RFL(xl(Z_X + 16384 + (u & 1) * 8192, L)), RFL(xl(Z_QV + (u & 3) * 256, L)), RFL(DBG0 + ((L * 128 + 64 + u) * 4 + w) * 2048), RFL(DBG0 + (2 << 20) + ((L * 32 + u) * 4 + w) * 8192)};
    const u32x4 v192_ = {(unsigned)(l * 16), (unsigned)(l * 16 + 4096), (unsigned)((l & 2) ? 0xeeeeu : 0x4444u), (unsigned)(0x4444u)};
    const u32x4 v196_ = {(unsigned)((l16 >> 2) * 256 + lq * 64 + (l & 3) * 16), (unsigned)(lq * 64 + (l & 3) * 16), (unsigned)((lq >> 1) * 2048 + 1024 + (lq & 1) * 256 + l16 * 16), (unsigned)(lq * 2048 + 1536 + l16 * 16)};
    const u32x4 v200_ = {(unsigned)(0 + w * 640 + lq * 160 + l16 * 4), (unsigned)(l16 < 4 ? 0 + w * 640 + lq * 160 + 64 + l16 * 4 : 12800), (unsigned)(0 + (l < 40 ? l : 0) * 16), (unsigned)(mpos)};
    const u32x4 v204_ = {(unsigned)(2560 + w * 2560 + lq * 320 + l16 * 4), (unsigned)(l16 < 4 ? 2560 + w * 2560 + lq * 320 + 64 + l16 * 4 : 12800), (unsigned)(3840 + w * 2560 + lq * 320 + l16 * 4), (unsigned)(l16 < 4 ? 3840 + w * 2560 + lq * 320 + 64 + l16 * 4 : 12800)};
    const u32x4 v208_ = {(unsigned)(2560 + w * 640 + (l < 40 ? l : 0) * 16), (unsigned)(xpos + RC_X1), (unsigned)(rpos), (unsigned)(l < 4 && w == 1 ? xl(Z_X1 + l * 4096 + u * 128, L) : OOB)};
    const u32x4 v212_ = {(unsigned)(l < 16 ? ((u >> 2) & 1) * 16384 + (u & 3) * 2048 + l * 128 : OOB), (unsigned)(0u), (unsigned)(0u), (unsigned)(0u)};
    asm volatile(
    "s_mov_b32 s20, 0\n"
    "s_mov_b64 s[22:23], 0\n"
    "s_mov_b32 s25, 0\n"
    "s_mov_b32 s28, -2\n"
    "v_lshrrev_b32 v4, 2, v192\n"
    "v_lshrrev_b32 v5, 9, v192\n"
    "v_add_u32 v5, 1, v5\n"
    ".Lct_po1_%=:\n"
    "buffer_load_dword v8, v4, s[0:3], s17 offen sc1\n"
    "s_waitcnt vmcnt(0)\n"
    "v_cmp_gt_i32 vcc, v5, v8\n"
    "s_nop 0\n"
    "s_cbranch_vccz .Lct_pd2_%=\n"
    "s_add_u32 s20, s20, 1\n"
    "s_cmp_lt_u32 s20, 0x40000\n"
    "s_cbranch_scc1 .Lct_sp3_%=\n"
    "v_mov_b32 v8, 40\n"
    "s_mov_b32 s24, 33728\n"
    "buffer_store_dword v8, off, s[0:3], s24 sc1\n"
    "s_waitcnt vmcnt(0)\n"
    "s_branch .Lct_pd2_%=\n"
    ".Lct_sp3_%=:\n"
    "s_sleep 16\n"
    "s_branch .Lct_po1_%=\n"
    ".Lct_pd2_%=:\n"
    "s_memrealtime s[22:23]\n"
    "s_waitcnt lgkmcnt(0)\n"
    "s_sub_u32 s26, s22, 792\n"
    "s_mov_b32 s28, -1\n"
    "s_add_u32 s28, s28, 1\n"
    "s_add_u32 s27, s28, 1\n"
    "s_add_u32 s29, s28, 0\n"
    "s_and_b32 s29, s29, 3\n"
    "s_lshl_b32 s29, s29, 22\n"
    "s_add_u32 s30, s28, 1\n"
    "s_and_b32 s30, s30, 3\n"
    "s_lshl_b32 s30, s30, 22\n"
    "s_add_u32 s31, s28, 3\n"
    "s_and_b32 s31, s31, 3\n"
    "s_lshl_b32 s31, s31, 22\n"
    "s_and_b32 s41, s28, 63\n"
    "s_lshl_b32 s41, s41, 7\n"
    "s_add_u32 s32, s12, s29\n"
    "s_add_u32 s33, s13, s29\n"
    "s_add_u32 s34, s15, s29\n"
    "s_add_u32 s35, s14, s29\n"
    "s_add_u32 s39, s10, s30\n"
    "s_add_u32 s40, s11, s30\n"
    "s_add_u32 s41, s16, s41\n"
    "s_add_u32 s36, s35, 4096\n"
    "s_add_u32 s37, s35, 16384\n"
    "s_add_u32 s38, s35, 20480\n"
    "s_add_u32 s43, s26, 304\n"
    "s_lshl_b32 s45, s28, 19\n"
    "s_lshl_b32 s25, s28, 11\n"
    "s_xor_b32 s25, s25, s8\n"
    "s_and_b32 s25, s25, 0x2000\n"
    "s_add_u32 s45, s45, s25\n"
    "s_add_u32 s45, s45, s9\n"
    "s_mul_i32 s46, s28, 40960\n"
    "s_cmp_eq_u32 s8, 0\n"
    "s_cselect_b32 s46, s46, s45\n"
    "s_cmp_eq_u32 s28, 63\n"
    "s_cselect_b32 s39, 2130706432, s39\n"
    "s_cselect_b32 s40, 2130706432, s40\n"
    "v_mov_b64 v[76:77], 0\n"
    "v_mov_b64 v[78:79], 0\n"
    "v_mov_b64 v[80:81], 0\n"
    "v_mov_b64 v[82:83], 0\n"
    "s_mov_b32 s39, s10\n"
    "s_mov_b32 s40, s11\n"
    "buffer_load_dwordx4 a[0:3], v192, s[0:3], s39 offen offset:0 sc1\n"
    "buffer_load_dwordx4 a[4:7], v192, s[0:3], s39 offen offset:1024 sc1\n"
    "buffer_load_dwordx4 a[8:11], v192, s[0:3], s39 offen offset:2048 sc1\n"
    "buffer_load_dwordx4 a[12:15], v192, s[0:3], s39 offen offset:3072 sc1\n"
    "buffer_load_dwordx4 a[16:19], v193, s[0:3], s39 offen offset:0 sc1\n"
    "buffer_load_dwordx4 a[20:23], v193, s[0:3], s39 offen offset:1024 sc1\n"
    "buffer_load_dwordx4 a[24:27], v193, s[0:3], s39 offen offset:2048 sc1\n"
    "buffer_load_dwordx4 a[28:31], v193, s[0:3], s39 offen offset:3072 sc1\n"
    "buffer_load_dwordx4 v[0:3], v196, s[0:3], s40 offen offset:0 sc1\n"
    "buffer_load_dwordx4 v[4:7], v196, s[0:3], s40 offen offset:1024 sc1\n"
    "s_add_u32 s39, s10, s30\n"
    "s_add_u32 s40, s11, s30\n"
    "buffer_load_dwordx2 v[190:191], off, s[0:3], s41 sc1\n"
    "s_waitcnt vmcnt(0)\n"
    ".Lct_top_%=:\n"
    "s_waitcnt vmcnt(2)\n"
    "v_mov_b64 v[8:9], v[0:1]\n"
    "v_mov_b64 v[10:11], v[2:3]\n"
    "v_mov_b64 v[12:13], v[4:5]\n"
    "v_mov_b64 v[14:15], v[6:7]\n"
    "v_mov_b32_dpp v16, v0 row_shl:4 row_mask:0xf bank_mask:0xf bound_ctrl:1\n"
    "v_mov_b32_dpp v17, v1 row_shl:4 row_mask:0xf bank_mask:0xf bound_ctrl:1\n"
    "v_mov_b32_dpp v18, v2 row_shl:4 row_mask:0xf bank_mask:0xf bound_ctrl:1\n"
    "v_mov_b32_dpp v19, v3 row_shl:4 row_mask:0xf bank_mask:0xf bound_ctrl:1\n"
    "v_mov_b32_dpp v20, v4 row_shl:4 row_mask:0xf bank_mask:0xf bound_ctrl:1\n"
    "v_mov_b32_dpp v21, v5 row_shl:4 row_mask:0xf bank_mask:0xf bound_ctrl:1\n"
    "v_mov_b32_dpp v22, v6 row_shl:4 row_mask:0xf bank_mask:0xf bound_ctrl:1\n"
    "v_mov_b32_dpp v23, v7 row_shl:4 row_mask:0xf bank_mask:0xf bound_ctrl:1\n"
    "v_mov_b32_dpp v24, v0 row_shl:8 row_mask:0xf bank_mask:0xf bound_ctrl:1\n"
    "v_mov_b32_dpp v25, v1 row_shl:8 row_mask:0xf bank_mask:0xf bound_ctrl:1\n"
    "v_mov_b32_dpp v26, v2 row_shl:8 row_mask:0xf bank_mask:0xf bound_ctrl:1\n"
    "v_mov_b32_dpp v27, v3 row_shl:8 row_mask:0xf bank_mask:0xf bound_ctrl:1\n"
    "v_mov_b32_dpp v28, v4 row_shl:8 row_mask:0xf bank_mask:0xf bound_ctrl:1\n"
    "v_mov_b32_dpp v29, v5 row_shl:8 row_mask:0xf bank_mask:0xf bound_ctrl:1\n"
    "v_mov_b32_dpp v30, v6 row_shl:8 row_mask:0xf bank_mask:0xf bound_ctrl:1\n"
    "v_mov_b32_dpp v31, v7 row_shl:8 row_mask:0xf bank_mask:0xf bound_ctrl:1\n"
    "v_mov_b32_dpp v32, v0 row_shl:12 row_mask:0xf bank_mask:0xf bound_ctrl:1\n"
    "v_mov_b32_dpp v33, v1 row_shl:12 row_mask:0xf bank_mask:0xf bound_ctrl:1\n"
    "v_mov_b32_dpp v34, v2 row_shl:12 row_mask:0xf bank_mask:0xf bound_ctrl:1\n"
    "v_mov_b32_dpp v35, v3 row_shl:12 row_mask:0xf bank_mask:0xf bound_ctrl:1\n"
    "v_mov_b32_dpp v36, v4 row_shl:12 row_mask:0xf bank_mask:0xf bound_ctrl:1\n"
    "v_mov_b32_dpp v37, v5 row_shl:12 row_mask:0xf bank_mask:0xf bound_ctrl:1\n"
    "v_mov_b32_dpp v38, v6 row_shl:12 row_mask:0xf bank_mask:0xf bound_ctrl:1\n"
    "v_mov_b32_dpp v39, v7 row_shl:12 row_mask:0xf bank_mask:0xf bound_ctrl:1\n"
    "s_waitcnt vmcnt(1)\n"
    ".Lct_x0_%=:\n"
    "v_readfirstlane_b32 s25, v191\n"
    "v_readfirstlane_b32 s24, v190\n"
    "s_cmp_eq_u32 s25, s27\n"
    "s_cselect_b32 s26, s24, s26\n"
    "s_cbranch_scc0 .Lct_xr0_%=\n"
    ".Lct_xg0_%=:\n"
    "s_add_u32 s43, s26, 97\n"
    "s_add_u32 s44, s26, 105\n"
    ".Lct_mk0x3_%=:\n"
    ".Lct_rt5_%=:\n"
    "s_memrealtime s[22:23]\n"
    "s_waitcnt lgkmcnt(0)\n"
    "s_sub_u32 s24, s43, s22\n"
    "s_cmp_lt_u32 s24, 600\n"
    "s_cbranch_scc1 .Lct_rt5_%=\n"
    "buffer_load_dwordx4 v[40:43], v192, s[0:3], s32 offen offset:0 sc1\n"
    "buffer_load_dwordx4 v[44:47], v192, s[0:3], s32 offen offset:1024 sc1\n"
    "buffer_load_dwordx4 v[48:51], v197, s[0:3], s33 offen offset:0 sc1\n"
    "buffer_load_dwordx4 v[52:55], v197, s[0:3], s33 offen offset:256 sc1\n"
    ".Lct_rt6_%=:\n"
    "s_memrealtime s[22:23]\n"
    "s_waitcnt lgkmcnt(0)\n"
    "s_sub_u32 s24, s44, s22\n"
    "s_cmp_lt_u32 s24, 600\n"
    "s_cbranch_scc1 .Lct_rt6_%=\n"
    "buffer_load_dwordx4 v[72:75], v193, s[0:3], s34 offen sc1\n"
    "buffer_load_dwordx4 v[56:59], v192, s[0:3], s34 offen offset:0 sc1\n"
    "buffer_load_dwordx4 v[60:63], v192, s[0:3], s34 offen offset:1024 sc1\n"
    "buffer_load_dwordx4 v[64:67], v192, s[0:3], s34 offen offset:2048 sc1\n"
    "buffer_load_dwordx4 v[68:71], v192, s[0:3], s34 offen offset:3072 sc1\n"
    "v_mov_b32 v148, s28\n"
    "v_mov_b32 v149, s28\n"
    "v_mov_b32 v150, s28\n"
    "v_mov_b32 v151, s28\n"
    "s_waitcnt vmcnt(9)\n"
    "s_barrier\n"
    "buffer_store_dwordx4 v[148:151], v211, s[0:3], 0 offen sc1\n"
    ".Lct_mk1x6_%=:\n"
    ".Lct_take0_%=:\n"
    "s_waitcnt vmcnt(6)\n"
    "v_max_u32 v184, v40, v44\n"
    "v_max3_u32 v184, v48, v50, v184\n"
    "v_max3_u32 v184, v52, v54, v184\n"
    "s_waitcnt vmcnt(5)\n"
    "v_smfmac_f32_16x16x64_bf16 v[76:79], v[72:75], v[40:47], v194\n"
    "v_max3_u32 v184, v72, v74, v184\n"
    "v_smfmac_f32_16x16x64_bf16 v[80:83], v[72:75], v[48:55], v194\n"
    "s_waitcnt vmcnt(4)\n"
    "v_smfmac_f32_16x16x64_bf16 v[80:83], v[56:59], v[8:15], v194\n"
    "v_max3_u32 v184, v56, v58, v184\n"
    "v_smfmac_f32_16x16x64_bf16 v[76:79], v[56:59], a[0:7], v194\n"
    "s_waitcnt vmcnt(3)\n"
    "v_smfmac_f32_16x16x64_bf16 v[80:83], v[60:63], v[16:23], v194\n"
    "v_max3_u32 v184, v60, v62, v184\n"
    "v_smfmac_f32_16x16x64_bf16 v[76:79], v[60:63], a[8:15], v194\n"
    "s_waitcnt vmcnt(2)\n"
    "v_smfmac_f32_16x16x64_bf16 v[80:83], v[64:67], v[24:31], v194\n"
    "v_max3_u32 v184, v64, v66, v184\n"
    "v_smfmac_f32_16x16x64_bf16 v[76:79], v[64:67], a[16:23], v194\n"
    "s_waitcnt vmcnt(1)\n"
    "v_smfmac_f32_16x16x64_bf16 v[80:83], v[68:71], v[32:39], v194\n"
    "v_max3_u32 v184, v68, v70, v184\n"
    "v_smfmac_f32_16x16x64_bf16 v[76:79], v[68:71], a[24:31], v194\n"
    ".Lct_mk2x7_%=:\n"
    "s_nop 1\n"
    "v_cmp_eq_u32 vcc, -1, v184\n"
    "s_nop 1\n"
    "v_add_f32 v80, v80, v82\n"
    "v_add_f32 v81, v81, v83\n"
    "s_nop 0\n"
    "v_pk_add_f32 v[76:77], v[76:77], v[78:79]\n"
    "ds_write2_b32 v201, v80, v81 offset1:20\n"
    "ds_write2_b32 v200, v76, v77 offset1:20\n"
    "s_cbranch_vccnz .Lct_retry0_%=\n"
    ".Lct_go0_%=:\n"
    "s_waitcnt lgkmcnt(0)\n"
    "s_barrier\n"
    "ds_read_b128 v[164:167], v202 offset:0\n"
    "ds_read_b128 v[168:171], v202 offset:640\n"
    "ds_read_b128 v[172:175], v202 offset:1280\n"
    "ds_read_b128 v[176:179], v202 offset:1920\n"
    "s_waitcnt lgkmcnt(2)\n"
    "v_pk_add_f32 v[164:165], v[164:165], v[168:169]\n"
    "v_pk_add_f32 v[166:167], v[166:167], v[170:171]\n"
    "s_waitcnt lgkmcnt(1)\n"
    "v_pk_add_f32 v[164:165], v[164:165], v[172:173]\n"
    "v_pk_add_f32 v[166:167], v[166:167], v[174:175]\n"
    "s_waitcnt lgkmcnt(0)\n"
    "v_pk_add_f32 v[164:165], v[164:165], v[176:177]\n"
    "v_pk_add_f32 v[166:167], v[166:167], v[178:179]\n"
    "v_cvt_pk_bf16_f32 v164, v164, v165\n"
    "v_cvt_pk_bf16_f32 v165, v166, v167\n"
    "v_min_u32 v164, -2, v164\n"
    "buffer_store_dwordx2 v[164:165], v203, s[0:3], s30 offen sc1\n"
    ".Lct_mk3x8_%=:\n"
    "s_sleep 2\n"
    "v_mov_b64 v[166:167], -1\n"
    "buffer_load_dwordx4 v[132:135], v199, s[0:3], s35 offen offset:0 sc1\n"
    "buffer_load_dwordx4 v[136:139], v199, s[0:3], s37 offen offset:0 sc1\n"
    "buffer_load_dwordx4 v[140:143], v199, s[0:3], s35 offen offset:256 sc1\n"
    "buffer_load_dwordx4 v[144:147], v199, s[0:3], s37 offen offset:256 sc1\n"
    "buffer_load_dwordx4 v[116:119], v198, s[0:3], s35 offen sc1\n"
    "buffer_load_dwordx4 v[84:87], v192, s[0:3], s35 offen offset:0 sc1\n"
    "buffer_load_dwordx4 v[88:91], v192, s[0:3], s35 offen offset:2048 sc1\n"
    "buffer_load_dwordx4 v[120:123], v198, s[0:3], s36 offen sc1\n"
    "buffer_load_dwordx4 v[92:95], v192, s[0:3], s36 offen offset:0 sc1\n"
    "buffer_load_dwordx4 v[96:99], v192, s[0:3], s36 offen offset:2048 sc1\n"
    ".Lct_mk7x9_%=:\n"
    "buffer_load_dwordx4 v[124:127], v198, s[0:3], s37 offen sc1\n"
    "s_waitcnt vmcnt(10)\n"
    "v_max3_u32 v184, v132, v134, v132\n"
    "v_swap_b32 v133, v134\n"
    "s_nop 1\n"
    "v_mfma_f32_16x16x32_bf16 v[148:151], v[132:135], v[40:43], 0\n"
    "v_mfma_f32_16x16x32_bf16 v[152:155], v[132:135], v[48:51], 0\n"
    "buffer_load_dwordx4 v[100:103], v192, s[0:3], s37 offen offset:0 sc1\n"
    "s_waitcnt vmcnt(10)\n"
    "v_max3_u32 v184, v136, v138, v184\n"
    "v_swap_b32 v137, v138\n"
    "s_nop 1\n"
    "v_mfma_f32_16x16x32_bf16 v[148:151], v[136:139], v[44:47], v[148:151]\n"
    "v_mfma_f32_16x16x32_bf16 v[152:155], v[136:139], v[52:55], v[152:155]\n"
    "buffer_load_dwordx4 v[104:107], v192, s[0:3], s37 offen offset:2048 sc1\n"
    "s_waitcnt vmcnt(10)\n"
    "v_max3_u32 v184, v140, v142, v184\n"
    "v_swap_b32 v141, v142\n"
    "s_nop 1\n"
    "v_mfma_f32_16x16x32_bf16 v[156:159], v[140:143], v[40:43], 0\n"
    "v_mfma_f32_16x16x32_bf16 v[160:163], v[140:143], v[48:51], 0\n"
    "buffer_load_dwordx4 v[128:131], v198, s[0:3], s38 offen sc1\n"
    "s_waitcnt vmcnt(10)\n"
    "v_max3_u32 v184, v144, v146, v184\n"
    "v_swap_b32 v145, v146\n"
    "s_nop 1\n"
    "v_mfma_f32_16x16x32_bf16 v[156:159], v[144:147], v[44:47], v[156:159]\n"
    "v_mfma_f32_16x16x32_bf16 v[160:163], v[144:147], v[52:55], v[160:163]\n"
    "buffer_load_dwordx4 v[108:111], v192, s[0:3], s38 offen offset:0 sc1\n"
    "s_waitcnt vmcnt(10)\n"
    "v_max3_u32 v184, v116, v118, v184\n"
    "v_smfmac_f32_16x16x64_bf16 v[148:151], v[116:119], a[0:7], v195\n"
    "v_smfmac_f32_16x16x64_bf16 v[152:155], v[116:119], v[8:15], v195\n"
    "buffer_load_dwordx4 v[112:115], v192, s[0:3], s38 offen offset:2048 sc1\n"
    "s_waitcnt vmcnt(10)\n"
    "v_max3_u32 v184, v84, v86, v184\n"
    "v_swap_b32 v85, v86\n"
    "s_nop 1\n"
    "v_mfma_f32_16x16x32_bf16 v[156:159], v[84:87], a[0:3], v[156:159]\n"
    "v_mfma_f32_16x16x32_bf16 v[160:163], v[84:87], v[8:11], v[160:163]\n"
    "buffer_load_dwordx2 v[186:187], v210, s[4:7], s46 offen sc1\n"
    "s_waitcnt vmcnt(10)\n"
    "v_max3_u32 v184, v88, v90, v184\n"
    "v_swap_b32 v89, v90\n"
    "s_nop 1\n"
    "v_mfma_f32_16x16x32_bf16 v[156:159], v[88:91], a[4:7], v[156:159]\n"
    "v_mfma_f32_16x16x32_bf16 v[160:163], v[88:91], v[12:15], v[160:163]\n"
    "buffer_store_dwordx2 v[166:167], v203, s[0:3], s31 offen sc1\n"
    "s_waitcnt vmcnt(10)\n"
    "v_max3_u32 v184, v120, v122, v184\n"
    "v_smfmac_f32_16x16x64_bf16 v[148:151], v[120:123], a[8:15], v195\n"
    "v_smfmac_f32_16x16x64_bf16 v[152:155], v[120:123], v[16:23], v195\n"
    "buffer_load_dwordx4 a[32:35], v192, s[0:3], s39 offen offset:0 sc1\n"
    "s_waitcnt vmcnt(10)\n"
    "v_max3_u32 v184, v92, v94, v184\n"
    "v_swap_b32 v93, v94\n"
    "s_nop 1\n"
    "v_mfma_f32_16x16x32_bf16 v[156:159], v[92:95], a[8:11], v[156:159]\n"
    "v_mfma_f32_16x16x32_bf16 v[160:163], v[92:95], v[16:19], v[160:163]\n"
    "buffer_load_dwordx4 a[36:39], v192, s[0:3], s39 offen offset:1024 sc1\n"
    "s_waitcnt vmcnt(10)\n"
    "v_max3_u32 v184, v96, v98, v184\n"
    "v_swap_b32 v97, v98\n"
    "s_nop 1\n"
    "v_mfma_f32_16x16x32_bf16 v[156:159], v[96:99], a[12:15], v[156:159]\n"
    "v_mfma_f32_16x16x32_bf16 v[160:163], v[96:99], v[20:23], v[160:163]\n"
    "buffer_load_dwordx4 a[40:43], v192, s[0:3], s39 offen offset:2048 sc1\n"
    "s_waitcnt vmcnt(10)\n"
    "v_max3_u32 v184, v124, v126, v184\n"
    "v_smfmac_f32_16x16x64_bf16 v[148:151], v[124:127], a[16:23], v195\n"
    "v_smfmac_f32_16x16x64_bf16 v[152:155], v[124:127], v[24:31], v195\n"
    "buffer_load_dwordx4 a[44:47], v192, s[0:3], s39 offen offset:3072 sc1\n"
    "s_waitcnt vmcnt(10)\n"
    "v_max3_u32 v184, v100, v102, v184\n"
    "v_swap_b32 v101, v102\n"
    "s_nop 1\n"
    "v_mfma_f32_16x16x32_bf16 v[156:159], v[100:103], a[16:19], v[156:159]\n"
    "v_mfma_f32_16x16x32_bf16 v[160:163], v[100:103], v[24:27], v[160:163]\n"
    "buffer_load_dwordx4 a[48:51], v193, s[0:3], s39 offen offset:0 sc1\n"
    "s_waitcnt vmcnt(10)\n"
    "v_max3_u32 v184, v104, v106, v184\n"
    "v_swap_b32 v105, v106\n"
    "s_nop 1\n"
    "v_mfma_f32_16x16x32_bf16 v[156:159], v[104:107], a[20:23], v[156:159]\n"
    "v_mfma_f32_16x16x32_bf16 v[160:163], v[104:107], v[28:31], v[160:163]\n"
    "buffer_load_dwordx4 a[52:55], v193, s[0:3], s39 offen offset:1024 sc1\n"
    "s_waitcnt vmcnt(10)\n"
    "v_max3_u32 v184, v128, v130, v184\n"
    "v_smfmac_f32_16x16x64_bf16 v[148:151], v[128:131], a[24:31], v195\n"
    "v_smfmac_f32_16x16x64_bf16 v[152:155], v[128:131], v[32:39], v195\n"
    "buffer_load_dwordx4 a[56:59], v193, s[0:3], s39 offen offset:2048 sc1\n"
    "s_waitcnt vmcnt(10)\n"
    "v_max3_u32 v184, v108, v110, v184\n"
    "v_swap_b32 v109, v110\n"
    "s_nop 1\n"
    "v_mfma_f32_16x16x32_bf16 v[156:159], v[108:111], a[24:27], v[156:159]\n"
    "v_mfma_f32_16x16x32_bf16 v[160:163], v[108:111], v[32:35], v[160:163]\n"
    "buffer_load_dwordx4 a[60:63], v193, s[0:3], s39 offen offset:3072 sc1\n"
    "s_waitcnt vmcnt(10)\n"
    "v_max3_u32 v184, v112, v114, v184\n"
    "v_swap_b32 v113, v114\n"
    "s_nop 1\n"
    "v_mfma_f32_16x16x32_bf16 v[156:159], v[112:115], a[28:31], v[156:159]\n"
    "v_mfma_f32_16x16x32_bf16 v[160:163], v[112:115], v[36:39], v[160:163]\n"
    "v_cmp_eq_u32 vcc, -1, v184\n"
    "s_nop 0\n"
    "s_cbranch_vccnz .Lct_tr0_%=\n"
    ".Lct_tg0_%=:\n"
    ".Lct_mk4x10_%=:\n"
    "ds_write2_b32 v204, v148, v149 offset0:0 offset1:20\n"
    "ds_write2_b32 v204, v150, v151 offset0:40 offset1:60\n"
    "ds_write2_b32 v205, v152, v153 offset0:0 offset1:20\n"
    "ds_write2_b32 v205, v154, v155 offset0:40 offset1:60\n"
    "ds_write2_b32 v206, v156, v157 offset0:0 offset1:20\n"
    "ds_write2_b32 v206, v158, v159 offset0:40 offset1:60\n"
    "ds_write2_b32 v207, v160, v161 offset0:0 offset1:20\n"
    "ds_write2_b32 v207, v162, v163 offset0:40 offset1:60\n"
    "s_mov_b32 s42, s45\n"
    "buffer_load_dwordx4 v[0:3], v196, s[0:3], s40 offen offset:0 sc1\n"
    "buffer_load_dwordx4 v[4:7], v196, s[0:3], s40 offen offset:1024 sc1\n"
    "s_add_u32 s28, s28, 1\n"
    "s_add_u32 s27, s28, 1\n"
    "s_add_u32 s29, s28, 0\n"
    "s_and_b32 s29, s29, 3\n"
    "s_lshl_b32 s29, s29, 22\n"
    "s_add_u32 s30, s28, 1\n"
    "s_and_b32 s30, s30, 3\n"
    "s_lshl_b32 s30, s30, 22\n"
    "s_add_u32 s31, s28, 3\n"
    "s_and_b32 s31, s31, 3\n"
    "s_lshl_b32 s31, s31, 22\n"
    "s_and_b32 s41, s28, 63\n"
    "s_lshl_b32 s41, s41, 7\n"
    "s_add_u32 s32, s12, s29\n"
    "s_add_u32 s33, s13, s29\n"
    "s_add_u32 s34, s15, s29\n"
    "s_add_u32 s35, s14, s29\n"
    "s_add_u32 s39, s10, s30\n"
    "s_add_u32 s40, s11, s30\n"
    "s_add_u32 s41, s16, s41\n"
    "s_add_u32 s36, s35, 4096\n"
    "s_add_u32 s37, s35, 16384\n"
    "s_add_u32 s38, s35, 20480\n"
    "s_add_u32 s43, s26, 304\n"
    "s_lshl_b32 s45, s28, 19\n"
    "s_lshl_b32 s25, s28, 11\n"
    "s_xor_b32 s25, s25, s8\n"
    "s_and_b32 s25, s25, 0x2000\n"
    "s_add_u32 s45, s45, s25\n"
    "s_add_u32 s45, s45, s9\n"
    "s_mul_i32 s46, s28, 40960\n"
    "s_cmp_eq_u32 s8, 0\n"
    "s_cselect_b32 s46, s46, s45\n"
    "s_cmp_eq_u32 s28, 63\n"
    "s_cselect_b32 s39, 2130706432, s39\n"
    "s_cselect_b32 s40, 2130706432, s40\n"
    "v_mov_b64 v[76:77], 0\n"
    "v_mov_b64 v[78:79], 0\n"
    "v_mov_b64 v[80:81], 0\n"
    "v_mov_b64 v[82:83], 0\n"
    "s_waitcnt vmcnt(11)\n"
    "v_lshlrev_b32 v148, 16, v186\n"
    "v_and_b32 v149, 0xffff0000, v186\n"
    "v_lshlrev_b32 v150, 16, v187\n"
    "v_and_b32 v151, 0xffff0000, v187\n"
    "s_memrealtime s[22:23]\n"
    "s_waitcnt lgkmcnt(0)\n"
    "s_barrier\n"
    "s_sub_u32 s24, s43, s22\n"
    "s_cmp_gt_i32 s24, 10\n"
    "s_cselect_b32 s24, 2130706432, s41\n"
    "buffer_load_dwordx2 v[190:191], off, s[0:3], s24 sc1\n"
    ".Lct_mk6x11_%=:\n"
    "ds_read_b128 v[164:167], v208 offset:0\n"
    "ds_read_b128 v[168:171], v208 offset:2560\n"
    "ds_read_b128 v[172:175], v208 offset:5120\n"
    "ds_read_b128 v[176:179], v208 offset:7680\n"
    "s_waitcnt lgkmcnt(2)\n"
    "v_pk_add_f32 v[164:165], v[164:165], v[168:169]\n"
    "v_pk_add_f32 v[166:167], v[166:167], v[170:171]\n"
    "s_waitcnt lgkmcnt(1)\n"
    "v_pk_add_f32 v[164:165], v[164:165], v[172:173]\n"
    "v_pk_add_f32 v[166:167], v[166:167], v[174:175]\n"
    "s_waitcnt lgkmcnt(0)\n"
    "v_pk_add_f32 v[164:165], v[164:165], v[176:177]\n"
    "v_pk_add_f32 v[166:167], v[166:167], v[178:179]\n"
    "v_cvt_pk_bf16_f32 v164, v164, v165\n"
    "v_cvt_pk_bf16_f32 v165, v166, v167\n"
    "v_lshlrev_b32 v168, 16, v164\n"
    "v_and_b32 v169, 0xffff0000, v164\n"
    "v_lshlrev_b32 v170, 16, v165\n"
    "v_and_b32 v171, 0xffff0000, v165\n"
    "v_pk_add_f32 v[168:169], v[168:169], v[148:149]\n"
    "v_pk_add_f32 v[170:171], v[170:171], v[150:151]\n"
    "v_cvt_pk_bf16_f32 v164, v168, v169\n"
    "v_cvt_pk_bf16_f32 v165, v170, v171\n"
    "buffer_store_dwordx2 v[164:165], v209, s[0:3], s42 offen sc1\n"
    ".Lct_mk5x12_%=:\n"
    "s_waitcnt vmcnt(2)\n"
    "v_mov_b64 v[8:9], v[0:1]\n"
    "v_mov_b64 v[10:11], v[2:3]\n"
    "v_mov_b64 v[12:13], v[4:5]\n"
    "v_mov_b64 v[14:15], v[6:7]\n"
    "v_mov_b32_dpp v16, v0 row_shl:4 row_mask:0xf bank_mask:0xf bound_ctrl:1\n"
    "v_mov_b32_dpp v17, v1 row_shl:4 row_mask:0xf bank_mask:0xf bound_ctrl:1\n"
    "v_mov_b32_dpp v18, v2 row_shl:4 row_mask:0xf bank_mask:0xf bound_ctrl:1\n"
    "v_mov_b32_dpp v19, v3 row_shl:4 row_mask:0xf bank_mask:0xf bound_ctrl:1\n"
    "v_mov_b32_dpp v20, v4 row_shl:4 row_mask:0xf bank_mask:0xf bound_ctrl:1\n"
    "v_mov_b32_dpp v21, v5 row_shl:4 row_mask:0xf bank_mask:0xf bound_ctrl:1\n"
    "v_mov_b32_dpp v22, v6 row_shl:4 row_mask:0xf bank_mask:0xf bound_ctrl:1\n"
    "v_mov_b32_dpp v23, v7 row_shl:4 row_mask:0xf bank_mask:0xf bound_ctrl:1\n"
    "v_mov_b32_dpp v24, v0 row_shl:8 row_mask:0xf bank_mask:0xf bound_ctrl:1\n"
    "v_mov_b32_dpp v25, v1 row_shl:8 row_mask:0xf bank_mask:0xf bound_ctrl:1\n"
    "v_mov_b32_dpp v26, v2 row_shl:8 row_mask:0xf bank_mask:0xf bound_ctrl:1\n"
    "v_mov_b32_dpp v27, v3 row_shl:8 row_mask:0xf bank_mask:0xf bound_ctrl:1\n"
    "v_mov_b32_dpp v28, v4 row_shl:8 row_mask:0xf bank_mask:0xf bound_ctrl:1\n"
    "v_mov_b32_dpp v29, v5 row_shl:8 row_mask:0xf bank_mask:0xf bound_ctrl:1\n"
    "v_mov_b32_dpp v30, v6 row_shl:8 row_mask:0xf bank_mask:0xf bound_ctrl:1\n"
    "v_mov_b32_dpp v31, v7 row_shl:8 row_mask:0xf bank_mask:0xf bound_ctrl:1\n"
    "v_mov_b32_dpp v32, v0 row_shl:12 row_mask:0xf bank_mask:0xf bound_ctrl:1\n"
    "v_mov_b32_dpp v33, v1 row_shl:12 row_mask:0xf bank_mask:0xf bound_ctrl:1\n"
    "v_mov_b32_dpp v34, v2 row_shl:12 row_mask:0xf bank_mask:0xf bound_ctrl:1\n"
    "v_mov_b32_dpp v35, v3 row_shl:12 row_mask:0xf bank_mask:0xf bound_ctrl:1\n"
    "v_mov_b32_dpp v36, v4 row_shl:12 row_mask:0xf bank_mask:0xf bound_ctrl:1\n"
    "v_mov_b32_dpp v37, v5 row_shl:12 row_mask:0xf bank_mask:0xf bound_ctrl:1\n"
    "v_mov_b32_dpp v38, v6 row_shl:12 row_mask:0xf bank_mask:0xf bound_ctrl:1\n"
    "v_mov_b32_dpp v39, v7 row_shl:12 row_mask:0xf bank_mask:0xf bound_ctrl:1\n"
    "s_waitcnt vmcnt(1)\n"
    ".Lct_x1_%=:\n"
    "v_readfirstlane_b32 s25, v191\n"
    "v_readfirstlane_b32 s24, v190\n"
    "s_cmp_eq_u32 s25, s27\n"
    "s_cselect_b32 s26, s24, s26\n"
    "s_cbranch_scc0 .Lct_xr1_%=\n"
    ".Lct_xg1_%=:\n"
    "s_add_u32 s43, s26, 97\n"
    "s_add_u32 s44, s26, 105\n"
    ".Lct_mk0x17_%=:\n"
    ".Lct_rt19_%=:\n"
    "s_memrealtime s[22:23]\n"
    "s_waitcnt lgkmcnt(0)\n"
    "s_sub_u32 s24, s43, s22\n"
    "s_cmp_lt_u32 s24, 600\n"
    "s_cbranch_scc1 .Lct_rt19_%=\n"
    "buffer_load_dwordx4 v[40:43], v192, s[0:3], s32 offen offset:0 sc1\n"
    "buffer_load_dwordx4 v[44:47], v192, s[0:3], s32 offen offset:1024 sc1\n"
    "buffer_load_dwordx4 v[48:51], v197, s[0:3], s33 offen offset:0 sc1\n"
    "buffer_load_dwordx4 v[52:55], v197, s[0:3], s33 offen offset:256 sc1\n"
    ".Lct_rt20_%=:\n"
    "s_memrealtime s[22:23]\n"
    "s_waitcnt lgkmcnt(0)\n"
    "s_sub_u32 s24, s44, s22\n"
    "s_cmp_lt_u32 s24, 600\n"
    "s_cbranch_scc1 .Lct_rt20_%=\n"
    "buffer_load_dwordx4 v[72:75], v193, s[0:3], s34 offen sc1\n"
    "buffer_load_dwordx4 v[56:59], v192, s[0:3], s34 offen offset:0 sc1\n"
    "buffer_load_dwordx4 v[60:63], v192, s[0:3], s34 offen offset:1024 sc1\n"
    "buffer_load_dwordx4 v[64:67], v192, s[0:3], s34 offen offset:2048 sc1\n"
    "buffer_load_dwordx4 v[68:71], v192, s[0:3], s34 offen offset:3072 sc1\n"
    "v_mov_b32 v148, s28\n"
    "v_mov_b32 v149, s28\n"
    "v_mov_b32 v150, s28\n"
    "v_mov_b32 v151, s28\n"
    "s_waitcnt vmcnt(9)\n"
    "s_barrier\n"
    "buffer_store_dwordx4 v[148:151], v211, s[0:3], 0 offen sc1\n"
    ".Lct_mk1x20_%=:\n"
    ".Lct_take1_%=:\n"
    "s_waitcnt vmcnt(6)\n"
    "v_max_u32 v184, v40, v44\n"
    "v_max3_u32 v184, v48, v50, v184\n"
    "v_max3_u32 v184, v52, v54, v184\n"
    "s_waitcnt vmcnt(5)\n"
    "v_smfmac_f32_16x16x64_bf16 v[76:79], v[72:75], v[40:47], v194\n"
    "v_max3_u32 v184, v72, v74, v184\n"
    "v_smfmac_f32_16x16x64_bf16 v[80:83], v[72:75], v[48:55], v194\n"
    "s_waitcnt vmcnt(4)\n"
    "v_smfmac_f32_16x16x64_bf16 v[80:83], v[56:59], v[8:15], v194\n"
    "v_max3_u32 v184, v56, v58, v184\n"
    "v_smfmac_f32_16x16x64_bf16 v[76:79], v[56:59], a[32:39], v194\n"
    "s_waitcnt vmcnt(3)\n"
    "v_smfmac_f32_16x16x64_bf16 v[80:83], v[60:63], v[16:23], v194\n"
    "v_max3_u32 v184, v60, v62, v184\n"
    "v_smfmac_f32_16x16x64_bf16 v[76:79], v[60:63], a[40:47], v194\n"
    "s_waitcnt vmcnt(2)\n"
    "v_smfmac_f32_16x16x64_bf16 v[80:83], v[64:67], v[24:31], v194\n"
    "v_max3_u32 v184, v64, v66, v184\n"
    "v_smfmac_f32_16x16x64_bf16 v[76:79], v[64:67], a[48:55], v194\n"
    "s_waitcnt vmcnt(1)\n"
    "v_smfmac_f32_16x16x64_bf16 v[80:83], v[68:71], v[32:39], v194\n"
    "v_max3_u32 v184, v68, v70, v184\n"
    "v_smfmac_f32_16x16x64_bf16 v[76:79], v[68:71], a[56:63], v194\n"
    ".Lct_mk2x21_%=:\n"
    "s_nop 1\n"
    "v_cmp_eq_u32 vcc, -1, v184\n"
    "s_nop 1\n"
    "v_add_f32 v80, v80, v82\n"
    "v_add_f32 v81, v81, v83\n"
    "s_nop 0\n"
    "v_pk_add_f32 v[76:77], v[76:77], v[78:79]\n"
    "ds_write2_b32 v201, v80, v81 offset1:20\n"
    "ds_write2_b32 v200, v76, v77 offset1:20\n"
    "s_cbranch_vccnz .Lct_retry1_%=\n"
    ".Lct_go1_%=:\n"
    "s_waitcnt lgkmcnt(0)\n"
    "s_barrier\n"
    "ds_read_b128 v[164:167], v202 offset:0\n"
    "ds_read_b128 v[168:171], v202 offset:640\n"
    "ds_read_b128 v[172:175], v202 offset:1280\n"
    "ds_read_b128 v[176:179], v202 offset:1920\n"
    "s_waitcnt lgkmcnt(2)\n"
    "v_pk_add_f32 v[164:165], v[164:165], v[168:169]\n"
    "v_pk_add_f32 v[166:167], v[166:167], v[170:171]\n"
    "s_waitcnt lgkmcnt(1)\n"
    "v_pk_add_f32 v[164:165], v[164:165], v[172:173]\n"
    "v_pk_add_f32 v[166:167], v[166:167], v[174:175]\n"
    "s_waitcnt lgkmcnt(0)\n"
    "v_pk_add_f32 v[164:165], v[164:165], v[176:177]\n"
    "v_pk_add_f32 v[166:167], v[166:167], v[178:179]\n"
    "v_cvt_pk_bf16_f32 v164, v164, v165\n"
    "v_cvt_pk_bf16_f32 v165, v166, v167\n"
    "v_min_u32 v164, -2, v164\n"
    "buffer_store_dwordx2 v[164:165], v203, s[0:3], s30 offen sc1\n"
    ".Lct_mk3x22_%=:\n"
    "s_sleep 2\n"
    "v_mov_b64 v[166:167], -1\n"
    "buffer_load_dwordx4 v[132:135], v199, s[0:3], s35 offen offset:0 sc1\n"
    "buffer_load_dwordx4 v[136:139], v199, s[0:3], s37 offen offset:0 sc1\n"
    "buffer_load_dwordx4 v[140:143], v199, s[0:3], s35 offen offset:256 sc1\n"
    "buffer_load_dwordx4 v[144:147], v199, s[0:3], s37 offen offset:256 sc1\n"
    "buffer_load_dwordx4 v[116:119], v198, s[0:3], s35 offen sc1\n"
    "buffer_load_dwordx4 v[84:87], v192, s[0:3], s35 offen offset:0 sc1\n"
    "buffer_load_dwordx4 v[88:91], v192, s[0:3], s35 offen offset:2048 sc1\n"
    "buffer_load_dwordx4 v[120:123], v198, s[0:3], s36 offen sc1\n"
    "buffer_load_dwordx4 v[92:95], v192, s[0:3], s36 offen offset:0 sc1\n"
    "buffer_load_dwordx4 v[96:99], v192, s[0:3], s36 offen offset:2048 sc1\n"
    ".Lct_mk7x23_%=:\n"
    "buffer_load_dwordx4 v[124:127], v198, s[0:3], s37 offen sc1\n"
    "s_waitcnt vmcnt(10)\n"
    "v_max3_u32 v184, v132, v134, v132\n"
    "v_swap_b32 v133, v134\n"
    "s_nop 1\n"
    "v_mfma_f32_16x16x32_bf16 v[148:151], v[132:135], v[40:43], 0\n"
    "v_mfma_f32_16x16x32_bf16 v[152:155], v[132:135], v[48:51], 0\n"
    "buffer_load_dwordx4 v[100:103], v192, s[0:3], s37 offen offset:0 sc1\n"
    "s_waitcnt vmcnt(10)\n"
    "v_max3_u32 v184, v136, v138, v184\n"
    "v_swap_b32 v137, v138\n"
    "s_nop 1\n"
    "v_mfma_f32_16x16x32_bf16 v[148:151], v[136:139], v[44:47], v[148:151]\n"
    "v_mfma_f32_16x16x32_bf16 v[152:155], v[136:139], v[52:55], v[152:155]\n"
    "buffer_load_dwordx4 v[104:107], v192, s[0:3], s37 offen offset:2048 sc1\n"
    "s_waitcnt vmcnt(10)\n"
    "v_max3_u32 v184, v140, v142, v184\n"
    "v_swap_b32 v141, v142\n"
    "s_nop 1\n"
    "v_mfma_f32_16x16x32_bf16 v[156:159], v[140:143], v[40:43], 0\n"
    "v_mfma_f32_16x16x32_bf16 v[160:163], v[140:143], v[48:51], 0\n"
    "buffer_load_dwordx4 v[128:131], v198, s[0:3], s38 offen sc1\n"
    "s_waitcnt vmcnt(10)\n"
    "v_max3_u32 v184, v144, v146, v184\n"
    "v_swap_b32 v145, v146\n"
    "s_nop 1\n"
    "v_mfma_f32_16x16x32_bf16 v[156:159], v[144:147], v[44:47], v[156:159]\n"
    "v_mfma_f32_16x16x32_bf16 v[160:163], v[144:147], v[52:55], v[160:163]\n"
    "buffer_load_dwordx4 v[108:111], v192, s[0:3], s38 offen offset:0 sc1\n"
    "s_waitcnt vmcnt(10)\n"
    "v_max3_u32 v184, v116, v118, v184\n"
    "v_smfmac_f32_16x16x64_bf16 v[148:151], v[116:119], a[32:39], v195\n"
    "v_smfmac_f32_16x16x64_bf16 v[152:155], v[116:119], v[8:15], v195\n"
    "buffer_load_dwordx4 v[112:115], v192, s[0:3], s38 offen offset:2048 sc1\n"
    "s_waitcnt vmcnt(10)\n"
    "v_max3_u32 v184, v84, v86, v184\n"
    "v_swap_b32 v85, v86\n"
    "s_nop 1\n"
    "v_mfma_f32_16x16x32_bf16 v[156:159], v[84:87], a[32:35], v[156:159]\n"
    "v_mfma_f32_16x16x32_bf16 v[160:163], v[84:87], v[8:11], v[160:163]\n"
    "buffer_load_dwordx2 v[186:187], v210, s[4:7], s46 offen sc1\n"
    "s_waitcnt vmcnt(10)\n"
    "v_max3_u32 v184, v88, v90, v184\n"
    "v_swap_b32 v89, v90\n"
    "s_nop 1\n"
    "v_mfma_f32_16x16x32_bf16 v[156:159], v[88:91], a[36:39], v[156:159]\n"
    "v_mfma_f32_16x16x32_bf16 v[160:163], v[88:91], v[12:15], v[160:163]\n"
    "buffer_store_dwordx2 v[166:167], v203, s[0:3], s31 offen sc1\n"
    "s_waitcnt vmcnt(10)\n"
    "v_max3_u32 v184, v120, v122, v184\n"
    "v_smfmac_f32_16x16x64_bf16 v[148:151], v[120:123], a[40:47], v195\n"
    "v_smfmac_f32_16x16x64_bf16 v[152:155], v[120:123], v[16:23], v195\n"
    "buffer_load_dwordx4 a[0:3], v192, s[0:3], s39 offen offset:0 sc1\n"
    "s_waitcnt vmcnt(10)\n"
    "v_max3_u32 v184, v92, v94, v184\n"
    "v_swap_b32 v93, v94\n"
    "s_nop 1\n"
    "v_mfma_f32_16x16x32_bf16 v[156:159], v[92:95], a[40:43], v[156:159]\n"
    "v_mfma_f32_16x16x32_bf16 v[160:163], v[92:95], v[16:19], v[160:163]\n"
    "buffer_load_dwordx4 a[4:7], v192, s[0:3], s39 offen offset:1024 sc1\n"
    "s_waitcnt vmcnt(10)\n"
    "v_max3_u32 v184, v96, v98, v184\n"
    "v_swap_b32 v97, v98\n"
    "s_nop 1\n"
    "v_mfma_f32_16x16x32_bf16 v[156:159], v[96:99], a[44:47], v[156:159]\n"
    "v_mfma_f32_16x16x32_bf16 v[160:163], v[96:99], v[20:23], v[160:163]\n"
    "buffer_load_dwordx4 a[8:11], v192, s[0:3], s39 offen offset:2048 sc1\n"
    "s_waitcnt vmcnt(10)\n"
    "v_max3_u32 v184, v124, v126, v184\n"
    "v_smfmac_f32_16x16x64_bf16 v[148:151], v[124:127], a[48:55], v195\n"
    "v_smfmac_f32_16x16x64_bf16 v[152:155], v[124:127], v[24:31], v195\n"
    "buffer_load_dwordx4 a[12:15], v192, s[0:3], s39 offen offset:3072 sc1\n"
    "s_waitcnt vmcnt(10)\n"
    "v_max3_u32 v184, v100, v102, v184\n"
    "v_swap_b32 v101, v102\n"
    "s_nop 1\n"
    "v_mfma_f32_16x16x32_bf16 v[156:159], v[100:103], a[48:51], v[156:159]\n"
    "v_mfma_f32_16x16x32_bf16 v[160:163], v[100:103], v[24:27], v[160:163]\n"
    "buffer_load_dwordx4 a[16:19], v193, s[0:3], s39 offen offset:0 sc1\n"
    "s_waitcnt vmcnt(10)\n"
    "v_max3_u32 v184, v104, v106, v184\n"
    "v_swap_b32 v105, v106\n"
    "s_nop 1\n"
    "v_mfma_f32_16x16x32_bf16 v[156:159], v[104:107], a[52:55], v[156:159]\n"
    "v_mfma_f32_16x16x32_bf16 v[160:163], v[104:107], v[28:31], v[160:163]\n"
    "buffer_load_dwordx4 a[20:23], v193, s[0:3], s39 offen offset:1024 sc1\n"
    "s_waitcnt vmcnt(10)\n"
    "v_max3_u32 v184, v128, v130, v184\n"
    "v_smfmac_f32_16x16x64_bf16 v[148:151], v[128:131], a[56:63], v195\n"
    "v_smfmac_f32_16x16x64_bf16 v[152:155], v[128:131], v[32:39], v195\n"
    "buffer_load_dwordx4 a[24:27], v193, s[0:3], s39 offen offset:2048 sc1\n"
    "s_waitcnt vmcnt(10)\n"
    "v_max3_u32 v184, v108, v110, v184\n"
    "v_swap_b32 v109, v110\n"
    "s_nop 1\n"
    "v_mfma_f32_16x16x32_bf16 v[156:159], v[108:111], a[56:59], v[156:159]\n"
    "v_mfma_f32_16x16x32_bf16 v[160:163], v[108:111], v[32:35], v[160:163]\n"
    "buffer_load_dwordx4 a[28:31], v193, s[0:3], s39 offen offset:3072 sc1\n"
    "s_waitcnt vmcnt(10)\n"
    "v_max3_u32 v184, v112, v114, v184\n"
    "v_swap_b32 v113, v114\n"
    "s_nop 1\n"
    "v_mfma_f32_16x16x32_bf16 v[156:159], v[112:115], a[60:63], v[156:159]\n"
    "v_mfma_f32_16x16x32_bf16 v[160:163], v[112:115], v[36:39], v[160:163]\n"
    "v_cmp_eq_u32 vcc, -1, v184\n"
    "s_nop 0\n"
    "s_cbranch_vccnz .Lct_tr1_%=\n"
    ".Lct_tg1_%=:\n"
    ".Lct_mk4x24_%=:\n"
    "ds_write2_b32 v204, v148, v149 offset0:0 offset1:20\n"
    "ds_write2_b32 v204, v150, v151 offset0:40 offset1:60\n"
    "ds_write2_b32 v205, v152, v153 offset0:0 offset1:20\n"
    "ds_write2_b32 v205, v154, v155 offset0:40 offset1:60\n"
    "ds_write2_b32 v206, v156, v157 offset0:0 offset1:20\n"
    "ds_write2_b32 v206, v158, v159 offset0:40 offset1:60\n"
    "ds_write2_b32 v207, v160, v161 offset0:0 offset1:20\n"
    "ds_write2_b32 v207, v162, v163 offset0:40 offset1:60\n"
    "s_mov_b32 s42, s45\n"
    "buffer_load_dwordx4 v[0:3], v196, s[0:3], s40 offen offset:0 sc1\n"
    "buffer_load_dwordx4 v[4:7], v196, s[0:3], s40 offen offset:1024 sc1\n"
    "s_add_u32 s28, s28, 1\n"
    "s_add_u32 s27, s28, 1\n"
    "s_add_u32 s29, s28, 0\n"
    "s_and_b32 s29, s29, 3\n"
    "s_lshl_b32 s29, s29, 22\n"
    "s_add_u32 s30, s28, 1\n"
    "s_and_b32 s30, s30, 3\n"
    "s_lshl_b32 s30, s30, 22\n"
    "s_add_u32 s31, s28, 3\n"
    "s_and_b32 s31, s31, 3\n"
    "s_lshl_b32 s31, s31, 22\n"
    "s_and_b32 s41, s28, 63\n"
    "s_lshl_b32 s41, s41, 7\n"
    "s_add_u32 s32, s12, s29\n"
    "s_add_u32 s33, s13, s29\n"
    "s_add_u32 s34, s15, s29\n"
    "s_add_u32 s35, s14, s29\n"
    "s_add_u32 s39, s10, s30\n"
    "s_add_u32 s40, s11, s30\n"
    "s_add_u32 s41, s16, s41\n"
    "s_add_u32 s36, s35, 4096\n"
    "s_add_u32 s37, s35, 16384\n"
    "s_add_u32 s38, s35, 20480\n"
    "s_add_u32 s43, s26, 304\n"
    "s_lshl_b32 s45, s28, 19\n"
    "s_lshl_b32 s25, s28, 11\n"
    "s_xor_b32 s25, s25, s8\n"
    "s_and_b32 s25, s25, 0x2000\n"
    "s_add_u32 s45, s45, s25\n"
    "s_add_u32 s45, s45, s9\n"
    "s_mul_i32 s46, s28, 40960\n"
    "s_cmp_eq_u32 s8, 0\n"
    "s_cselect_b32 s46, s46, s45\n"
    "s_cmp_eq_u32 s28, 63\n"
    "s_cselect_b32 s39, 2130706432, s39\n"
    "s_cselect_b32 s40, 2130706432, s40\n"
    "v_mov_b64 v[76:77], 0\n"
    "v_mov_b64 v[78:79], 0\n"
    "v_mov_b64 v[80:81], 0\n"
    "v_mov_b64 v[82:83], 0\n"
    "s_waitcnt vmcnt(11)\n"
    "v_lshlrev_b32 v148, 16, v186\n"
    "v_and_b32 v149, 0xffff0000, v186\n"
    "v_lshlrev_b32 v150, 16, v187\n"
    "v_and_b32 v151, 0xffff0000, v187\n"
    "s_memrealtime s[22:23]\n"
    "s_waitcnt lgkmcnt(0)\n"
    "s_barrier\n"
    "s_sub_u32 s24, s43, s22\n"
    "s_cmp_gt_i32 s24, 10\n"
    "s_cselect_b32 s24, 2130706432, s41\n"
    "buffer_load_dwordx2 v[190:191], off, s[0:3], s24 sc1\n"
    ".Lct_mk6x25_%=:\n"
    "ds_read_b128 v[164:167], v208 offset:0\n"
    "ds_read_b128 v[168:171], v208 offset:2560\n"
    "ds_read_b128 v[172:175], v208 offset:5120\n"
    "ds_read_b128 v[176:179], v208 offset:7680\n"
    "s_waitcnt lgkmcnt(2)\n"
    "v_pk_add_f32 v[164:165], v[164:165], v[168:169]\n"
    "v_pk_add_f32 v[166:167], v[166:167], v[170:171]\n"
    "s_waitcnt lgkmcnt(1)\n"
    "v_pk_add_f32 v[164:165], v[164:165], v[172:173]\n"
    "v_pk_add_f32 v[166:167], v[166:167], v[174:175]\n"
    "s_waitcnt lgkmcnt(0)\n"
    "v_pk_add_f32 v[164:165], v[164:165], v[176:177]\n"
    "v_pk_add_f32 v[166:167], v[166:167], v[178:179]\n"
    "v_cvt_pk_bf16_f32 v164, v164, v165\n"
    "v_cvt_pk_bf16_f32 v165, v166, v167\n"
    "v_lshlrev_b32 v168, 16, v164\n"
    "v_and_b32 v169, 0xffff0000, v164\n"
    "v_lshlrev_b32 v170, 16, v165\n"
    "v_and_b32 v171, 0xffff0000, v165\n"
    "v_pk_add_f32 v[168:169], v[168:169], v[148:149]\n"
    "v_pk_add_f32 v[170:171], v[170:171], v[150:151]\n"
    "v_cvt_pk_bf16_f32 v164, v168, v169\n"
    "v_cvt_pk_bf16_f32 v165, v170, v171\n"
    "buffer_store_dwordx2 v[164:165], v209, s[0:3], s42 offen sc1\n"
    ".Lct_mk5x26_%=:\n"
    "s_cmp_lt_u32 s28, 64\n"
    "s_cbranch_scc1 .Lct_top_%=\n"
    "v_mov_b32 v148, s28\n"
    "v_mov_b32 v149, s28\n"
    "v_mov_b32 v150, s28\n"
    "v_mov_b32 v151, s28\n"
    "s_waitcnt vmcnt(0)\n"
    "s_barrier\n"
    "buffer_store_dwordx4 v[148:151], v211, s[0:3], 0 offen sc1\n"
    "s_waitcnt vmcnt(0) lgkmcnt(0)\n"
    "s_endpgm\n"
    ".Lct_xr0_%=:\n"
    "s_add_u32 s20, s20, 1\n"
    "s_cmp_lt_u32 s20, 0x40000\n"
    "s_cbranch_scc1 .Lct_sp14_%=\n"
    "v_mov_b32 v184, 42\n"
    "s_mov_b32 s24, 33728\n"
    "buffer_store_dword v184, off, s[0:3], s24 sc1\n"
    "s_waitcnt vmcnt(0)\n"
    "s_branch .Lct_xg0_%=\n"
    ".Lct_sp14_%=:\n"
    ".Lct_rt15_%=:\n"
    "s_memrealtime s[22:23]\n"
    "s_waitcnt lgkmcnt(0)\n"
    "s_sub_u32 s24, s43, s22\n"
    "s_cmp_lt_u32 s24, 600\n"
    "s_cbranch_scc1 .Lct_rt15_%=\n"
    "s_add_u32 s43, s22, 44\n"
    "buffer_load_dwordx2 v[190:191], off, s[0:3], s41 sc1\n"
    "s_waitcnt vmcnt(0)\n"
    "s_branch .Lct_x0_%=\n"
    ".Lct_retry0_%=:\n"
    "s_add_u32 s20, s20, 1\n"
    "s_cmp_lt_u32 s20, 0x40000\n"
    "s_cbranch_scc1 .Lct_sp16_%=\n"
    "v_mov_b32 v184, 3\n"
    "s_mov_b32 s24, 33728\n"
    "buffer_store_dword v184, off, s[0:3], s24 sc1\n"
    "s_waitcnt vmcnt(0)\n"
    "s_branch .Lct_go0_%=\n"
    ".Lct_sp16_%=:\n"
    "s_waitcnt vmcnt(0) lgkmcnt(0)\n"
    "v_mov_b64 v[76:77], 0\n"
    "v_mov_b64 v[78:79], 0\n"
    "v_mov_b64 v[80:81], 0\n"
    "v_mov_b64 v[82:83], 0\n"
    "buffer_load_dwordx4 v[40:43], v192, s[0:3], s32 offen offset:0 sc1\n"
    "buffer_load_dwordx4 v[44:47], v192, s[0:3], s32 offen offset:1024 sc1\n"
    "buffer_load_dwordx4 v[48:51], v197, s[0:3], s33 offen offset:0 sc1\n"
    "buffer_load_dwordx4 v[52:55], v197, s[0:3], s33 offen offset:256 sc1\n"
    "buffer_load_dwordx4 v[72:75], v193, s[0:3], s34 offen sc1\n"
    "buffer_load_dwordx4 v[56:59], v192, s[0:3], s34 offen offset:0 sc1\n"
    "buffer_load_dwordx4 v[60:63], v192, s[0:3], s34 offen offset:1024 sc1\n"
    "buffer_load_dwordx4 v[64:67], v192, s[0:3], s34 offen offset:2048 sc1\n"
    "buffer_load_dwordx4 v[68:71], v192, s[0:3], s34 offen offset:3072 sc1\n"
    "buffer_store_dwordx4 v[148:151], v211, s[0:3], 0 offen sc1\n"
    "s_waitcnt vmcnt(0)\n"
    "s_branch .Lct_take0_%=\n"
    ".Lct_tr0_%=:\n"
    "s_add_u32 s20, s20, 1\n"
    "s_cmp_lt_u32 s20, 0x40000\n"
    "s_cbranch_scc1 .Lct_sp17_%=\n"
    "v_mov_b32 v184, 4\n"
    "s_mov_b32 s24, 33728\n"
    "buffer_store_dword v184, off, s[0:3], s24 sc1\n"
    "s_waitcnt vmcnt(0)\n"
    "s_branch .Lct_tg0_%=\n"
    ".Lct_sp17_%=:\n"
    "buffer_load_dwordx4 v[132:135], v199, s[0:3], s35 offen offset:0 sc1\n"
    "buffer_load_dwordx4 v[136:139], v199, s[0:3], s37 offen offset:0 sc1\n"
    "buffer_load_dwordx4 v[140:143], v199, s[0:3], s35 offen offset:256 sc1\n"
    "buffer_load_dwordx4 v[144:147], v199, s[0:3], s37 offen offset:256 sc1\n"
    "buffer_load_dwordx4 v[116:119], v198, s[0:3], s35 offen sc1\n"
    "buffer_load_dwordx4 v[84:87], v192, s[0:3], s35 offen offset:0 sc1\n"
    "buffer_load_dwordx4 v[88:91], v192, s[0:3], s35 offen offset:2048 sc1\n"
    "buffer_load_dwordx4 v[120:123], v198, s[0:3], s36 offen sc1\n"
    "buffer_load_dwordx4 v[92:95], v192, s[0:3], s36 offen offset:0 sc1\n"
    "buffer_load_dwordx4 v[96:99], v192, s[0:3], s36 offen offset:2048 sc1\n"
    "buffer_load_dwordx4 v[124:127], v198, s[0:3], s37 offen sc1\n"
    "buffer_load_dwordx4 v[100:103], v192, s[0:3], s37 offen offset:0 sc1\n"
    "buffer_load_dwordx4 v[104:107], v192, s[0:3], s37 offen offset:2048 sc1\n"
    "buffer_load_dwordx4 v[128:131], v198, s[0:3], s38 offen sc1\n"
    "buffer_load_dwordx4 v[108:111], v192, s[0:3], s38 offen offset:0 sc1\n"
    "buffer_load_dwordx4 v[112:115], v192, s[0:3], s38 offen offset:2048 sc1\n"
    "s_waitcnt vmcnt(0)\n"
    "v_max3_u32 v184, v132, v134, v132\n"
    "v_swap_b32 v133, v134\n"
    "s_nop 1\n"
    "v_mfma_f32_16x16x32_bf16 v[148:151], v[132:135], v[40:43], 0\n"
    "v_mfma_f32_16x16x32_bf16 v[152:155], v[132:135], v[48:51], 0\n"
    "v_max3_u32 v184, v136, v138, v184\n"
    "v_swap_b32 v137, v138\n"
    "s_nop 1\n"
    "v_mfma_f32_16x16x32_bf16 v[148:151], v[136:139], v[44:47], v[148:151]\n"
    "v_mfma_f32_16x16x32_bf16 v[152:155], v[136:139], v[52:55], v[152:155]\n"
    "v_max3_u32 v184, v140, v142, v184\n"
    "v_swap_b32 v141, v142\n"
    "s_nop 1\n"
    "v_mfma_f32_16x16x32_bf16 v[156:159], v[140:143], v[40:43], 0\n"
    "v_mfma_f32_16x16x32_bf16 v[160:163], v[140:143], v[48:51], 0\n"
    "v_max3_u32 v184, v144, v146, v184\n"
    "v_swap_b32 v145, v146\n"
    "s_nop 1\n"
    "v_mfma_f32_16x16x32_bf16 v[156:159], v[144:147], v[44:47], v[156:159]\n"
    "v_mfma_f32_16x16x32_bf16 v[160:163], v[144:147], v[52:55], v[160:163]\n"
    "v_max3_u32 v184, v116, v118, v184\n"
    "v_smfmac_f32_16x16x64_bf16 v[148:151], v[116:119], a[0:7], v195\n"
    "v_smfmac_f32_16x16x64_bf16 v[152:155], v[116:119], v[8:15], v195\n"
    "v_max3_u32 v184, v84, v86, v184\n"
    "v_swap_b32 v85, v86\n"
    "s_nop 1\n"
    "v_mfma_f32_16x16x32_bf16 v[156:159], v[84:87], a[0:3], v[156:159]\n"
    "v_mfma_f32_16x16x32_bf16 v[160:163], v[84:87], v[8:11], v[160:163]\n"
    "v_max3_u32 v184, v88, v90, v184\n"
    "v_swap_b32 v89, v90\n"
    "s_nop 1\n"
    "v_mfma_f32_16x16x32_bf16 v[156:159], v[88:91], a[4:7], v[156:159]\n"
    "v_mfma_f32_16x16x32_bf16 v[160:163], v[88:91], v[12:15], v[160:163]\n"
    "v_max3_u32 v184, v120, v122, v184\n"
    "v_smfmac_f32_16x16x64_bf16 v[148:151], v[120:123], a[8:15], v195\n"
    "v_smfmac_f32_16x16x64_bf16 v[152:155], v[120:123], v[16:23], v195\n"
    "v_max3_u32 v184, v92, v94, v184\n"
    "v_swap_b32 v93, v94\n"
    "s_nop 1\n"
    "v_mfma_f32_16x16x32_bf16 v[156:159], v[92:95], a[8:11], v[156:159]\n"
    "v_mfma_f32_16x16x32_bf16 v[160:163], v[92:95], v[16:19], v[160:163]\n"
    "v_max3_u32 v184, v96, v98, v184\n"
    "v_swap_b32 v97, v98\n"
    "s_nop 1\n"
    "v_mfma_f32_16x16x32_bf16 v[156:159], v[96:99], a[12:15], v[156:159]\n"
    "v_mfma_f32_16x16x32_bf16 v[160:163], v[96:99], v[20:23], v[160:163]\n"
    "v_max3_u32 v184, v124, v126, v184\n"
    "v_smfmac_f32_16x16x64_bf16 v[148:151], v[124:127], a[16:23], v195\n"
    "v_smfmac_f32_16x16x64_bf16 v[152:155], v[124:127], v[24:31], v195\n"
    "v_max3_u32 v184, v100, v102, v184\n"
    "v_swap_b32 v101, v102\n"
    "s_nop 1\n"
    "v_mfma_f32_16x16x32_bf16 v[156:159], v[100:103], a[16:19], v[156:159]\n"
    "v_mfma_f32_16x16x32_bf16 v[160:163], v[100:103], v[24:27], v[160:163]\n"
    "v_max3_u32 v184, v104, v106, v184\n"
    "v_swap_b32 v105, v106\n"
    "s_nop 1\n"
    "v_mfma_f32_16x16x32_bf16 v[156:159], v[104:107], a[20:23], v[156:159]\n"
    "v_mfma_f32_16x16x32_bf16 v[160:163], v[104:107], v[28:31], v[160:163]\n"
    "v_max3_u32 v184, v128, v130, v184\n"
    "v_smfmac_f32_16x16x64_bf16 v[148:151], v[128:131], a[24:31], v195\n"
    "v_smfmac_f32_16x16x64_bf16 v[152:155], v[128:131], v[32:39], v195\n"
    "v_max3_u32 v184, v108, v110, v184\n"
    "v_swap_b32 v109, v110\n"
    "s_nop 1\n"
    "v_mfma_f32_16x16x32_bf16 v[156:159], v[108:111], a[24:27], v[156:159]\n"
    "v_mfma_f32_16x16x32_bf16 v[160:163], v[108:111], v[32:35], v[160:163]\n"
    "v_max3_u32 v184, v112, v114, v184\n"
    "v_swap_b32 v113, v114\n"
    "s_nop 1\n"
    "v_mfma_f32_16x16x32_bf16 v[156:159], v[112:115], a[28:31], v[156:159]\n"
    "v_mfma_f32_16x16x32_bf16 v[160:163], v[112:115], v[36:39], v[160:163]\n"
    "v_cmp_eq_u32 vcc, -1, v184\n"
    "s_nop 0\n"
    "s_cbranch_vccnz .Lct_tr0_%=\n"
    "s_nop 7\n"
    "s_branch .Lct_tg0_%=\n"
    ".Lct_xr1_%=:\n"
    "s_add_u32 s20, s20, 1\n"
    "s_cmp_lt_u32 s20, 0x40000\n"
    "s_cbranch_scc1 .Lct_sp28_%=\n"
    "v_mov_b32 v184, 42\n"
    "s_mov_b32 s24, 33728\n"
    "buffer_store_dword v184, off, s[0:3], s24 sc1\n"
    "s_waitcnt vmcnt(0)\n"
    "s_branch .Lct_xg1_%=\n"
    ".Lct_sp28_%=:\n"
    ".Lct_rt29_%=:\n"
    "s_memrealtime s[22:23]\n"
    "s_waitcnt lgkmcnt(0)\n"
    "s_sub_u32 s24, s43, s22\n"
    "s_cmp_lt_u32 s24, 600\n"
    "s_cbranch_scc1 .Lct_rt29_%=\n"
    "s_add_u32 s43, s22, 44\n"
    "buffer_load_dwordx2 v[190:191], off, s[0:3], s41 sc1\n"
    "s_waitcnt vmcnt(0)\n"
    "s_branch .Lct_x1_%=\n"
    ".Lct_retry1_%=:\n"
    "s_add_u32 s20, s20, 1\n"
    "s_cmp_lt_u32 s20, 0x40000\n"
    "s_cbranch_scc1 .Lct_sp30_%=\n"
    "v_mov_b32 v184, 3\n"
    "s_mov_b32 s24, 33728\n"
    "buffer_store_dword v184, off, s[0:3], s24 sc1\n"
    "s_waitcnt vmcnt(0)\n"
    "s_branch .Lct_go1_%=\n"
    ".Lct_sp30_%=:\n"
    "s_waitcnt vmcnt(0) lgkmcnt(0)\n"
    "v_mov_b64 v[76:77], 0\n"
    "v_mov_b64 v[78:79], 0\n"
    "v_mov_b64 v[80:81], 0\n"
    "v_mov_b64 v[82:83], 0\n"
    "buffer_load_dwordx4 v[40:43], v192, s[0:3], s32 offen offset:0 sc1\n"
    "buffer_load_dwordx4 v[44:47], v192, s[0:3], s32 offen offset:1024 sc1\n"
    "buffer_load_dwordx4 v[48:51], v197, s[0:3], s33 offen offset:0 sc1\n"
    "buffer_load_dwordx4 v[52:55], v197, s[0:3], s33 offen offset:256 sc1\n"
    "buffer_load_dwordx4 v[72:75], v193, s[0:3], s34 offen sc1\n"
    "buffer_load_dwordx4 v[56:59], v192, s[0:3], s34 offen offset:0 sc1\n"
    "buffer_load_dwordx4 v[60:63], v192, s[0:3], s34 offen offset:1024 sc1\n"
    "buffer_load_dwordx4 v[64:67], v192, s[0:3], s34 offen offset:2048 sc1\n"
    "buffer_load_dwordx4 v[68:71], v192, s[0:3], s34 offen offset:3072 sc1\n"
    "buffer_store_dwordx4 v[148:151], v211, s[0:3], 0 offen sc1\n"
    "s_waitcnt vmcnt(0)\n"
    "s_branch .Lct_take1_%=\n"
    ".Lct_tr1_%=:\n"
    "s_add_u32 s20, s20, 1\n"
    "s_cmp_lt_u32 s20, 0x40000\n"
    "s_cbranch_scc1 .Lct_sp31_%=\n"
    "v_mov_b32 v184, 4\n"
    "s_mov_b32 s24, 33728\n"
    "buffer_store_dword v184, off, s[0:3], s24 sc1\n"
    "s_waitcnt vmcnt(0)\n"
    "s_branch .Lct_tg1_%=\n"
    ".Lct_sp31_%=:\n"
    "buffer_load_dwordx4 v[132:135], v199, s[0:3], s35 offen offset:0 sc1\n"
    "buffer_load_dwordx4 v[136:139], v199, s[0:3], s37 offen offset:0 sc1\n"
    "buffer_load_dwordx4 v[140:143], v199, s[0:3], s35 offen offset:256 sc1\n"
    "buffer_load_dwordx4 v[144:147], v199, s[0:3], s37 offen offset:256 sc1\n"
    "buffer_load_dwordx4 v[116:119], v198, s[0:3], s35 offen sc1\n"
    "buffer_load_dwordx4 v[84:87], v192, s[0:3], s35 offen offset:0 sc1\n"
    "buffer_load_dwordx4 v[88:91], v192, s[0:3], s35 offen offset:2048 sc1\n"
    "buffer_load_dwordx4 v[120:123], v198, s[0:3], s36 offen sc1\n"
    "buffer_load_dwordx4 v[92:95], v192, s[0:3], s36 offen offset:0 sc1\n"
    "buffer_load_dwordx4 v[96:99], v192, s[0:3], s36 offen offset:2048 sc1\n"
    "buffer_load_dwordx4 v[124:127], v198, s[0:3], s37 offen sc1\n"
    "buffer_load_dwordx4 v[100:103], v192, s[0:3], s37 offen offset:0 sc1\n"
    "buffer_load_dwordx4 v[104:107], v192, s[0:3], s37 offen offset:2048 sc1\n"
    "buffer_load_dwordx4 v[128:131], v198, s[0:3], s38 offen sc1\n"
    "buffer_load_dwordx4 v[108:111], v192, s[0:3], s38 offen offset:0 sc1\n"
    "buffer_load_dwordx4 v[112:115], v192, s[0:3], s38 offen offset:2048 sc1\n"
    "s_waitcnt vmcnt(0)\n"
    "v_max3_u32 v184, v132, v134, v132\n"
    "v_swap_b32 v133, v134\n"
    "s_nop 1\n"
    "v_mfma_f32_16x16x32_bf16 v[148:151], v[132:135], v[40:43], 0\n"
    "v_mfma_f32_16x16x32_bf16 v[152:155], v[132:135], v[48:51], 0\n"
    "v_max3_u32 v184, v136, v138, v184\n"
    "v_swap_b32 v137, v138\n"
    "s_nop 1\n"
    "v_mfma_f32_16x16x32_bf16 v[148:151], v[136:139], v[44:47], v[148:151]\n"
    "v_mfma_f32_16x16x32_bf16 v[152:155], v[136:139], v[52:55], v[152:155]\n"
    "v_max3_u32 v184, v140, v142, v184\n"
    "v_swap_b32 v141, v142\n"
    "s_nop 1\n"
    "v_mfma_f32_16x16x32_bf16 v[156:159], v[140:143], v[40:43], 0\n"
    "v_mfma_f32_16x16x32_bf16 v[160:163], v[140:143], v[48:51], 0\n"
    "v_max3_u32 v184, v144, v146, v184\n"
    "v_swap_b32 v145, v146\n"
    "s_nop 1\n"
    "v_mfma_f32_16x16x32_bf16 v[156:159], v[144:147], v[44:47], v[156:159]\n"
    "v_mfma_f32_16x16x32_bf16 v[160:163], v[144:147], v[52:55], v[160:163]\n"
    "v_max3_u32 v184, v116, v118, v184\n"
    "v_smfmac_f32_16x16x64_bf16 v[148:151], v[116:119], a[32:39], v195\n"
    "v_smfmac_f32_16x16x64_bf16 v[152:155], v[116:119], v[8:15], v195\n"
    "v_max3_u32 v184, v84, v86, v184\n"
    "v_swap_b32 v85, v86\n"
    "s_nop 1\n"
    "v_mfma_f32_16x16x32_bf16 v[156:159], v[84:87], a[32:35], v[156:159]\n"
    "v_mfma_f32_16x16x32_bf16 v[160:163], v[84:87], v[8:11], v[160:163]\n"
    "v_max3_u32 v184, v88, v90, v184\n"
    "v_swap_b32 v89, v90\n"
    "s_nop 1\n"
    "v_mfma_f32_16x16x32_bf16 v[156:159], v[88:91], a[36:39], v[156:159]\n"
    "v_mfma_f32_16x16x32_bf16 v[160:163], v[88:91], v[12:15], v[160:163]\n"
    "v_max3_u32 v184, v120, v122, v184\n"
    "v_smfmac_f32_16x16x64_bf16 v[148:151], v[120:123], a[40:47], v195\n"
    "v_smfmac_f32_16x16x64_bf16 v[152:155], v[120:123], v[16:23], v195\n"
    "v_max3_u32 v184, v92, v94, v184\n"
    "v_swap_b32 v93, v94\n"
    "s_nop 1\n"
    "v_mfma_f32_16x16x32_bf16 v[156:159], v[92:95], a[40:43], v[156:159]\n"
    "v_mfma_f32_16x16x32_bf16 v[160:163], v[92:95], v[16:19], v[160:163]\n"
    "v_max3_u32 v184, v96, v98, v184\n"
    "v_swap_b32 v97, v98\n"
    "s_nop 1\n"
    "v_mfma_f32_16x16x32_bf16 v[156:159], v[96:99], a[44:47], v[156:159]\n"
    "v_mfma_f32_16x16x32_bf16 v[160:163], v[96:99], v[20:23], v[160:163]\n"
    "v_max3_u32 v184, v124, v126, v184\n"
    "v_smfmac_f32_16x16x64_bf16 v[148:151], v[124:127], a[48:55], v195\n"
    "v_smfmac_f32_16x16x64_bf16 v[152:155], v[124:127], v[24:31], v195\n"
    "v_max3_u32 v184, v100, v102, v184\n"
    "v_swap_b32 v101, v102\n"
    "s_nop 1\n"
    "v_mfma_f32_16x16x32_bf16 v[156:159], v[100:103], a[48:51], v[156:159]\n"
    "v_mfma_f32_16x16x32_bf16 v[160:163], v[100:103], v[24:27], v[160:163]\n"
    "v_max3_u32 v184, v104, v106, v184\n"
    "v_swap_b32 v105, v106\n"
    "s_nop 1\n"
    "v_mfma_f32_16x16x32_bf16 v[156:159], v[104:107], a[52:55], v[156:159]\n"
    "v_mfma_f32_16x16x32_bf16 v[160:163], v[104:107], v[28:31], v[160:163]\n"
    "v_max3_u32 v184, v128, v130, v184\n"
    "v_smfmac_f32_16x16x64_bf16 v[148:151], v[128:131], a[56:63], v195\n"
    "v_smfmac_f32_16x16x64_bf16 v[152:155], v[128:131], v[32:39], v195\n"
    "v_max3_u32 v184, v108, v110, v184\n"
    "v_swap_b32 v109, v110\n"
    "s_nop 1\n"
    "v_mfma_f32_16x16x32_bf16 v[156:159], v[108:111], a[56:59], v[156:159]\n"
    "v_mfma_f32_16x16x32_bf16 v[160:163], v[108:111], v[32:35], v[160:163]\n"
    "v_max3_u32 v184, v112, v114, v184\n"
    "v_swap_b32 v113, v114\n"
    "s_nop 1\n"
    "v_mfma_f32_16x16x32_bf16 v[156:159], v[112:115], a[60:63], v[156:159]\n"
    "v_mfma_f32_16x16x32_bf16 v[160:163], v[112:115], v[36:39], v[160:163]\n"
    "v_cmp_eq_u32 vcc, -1, v184\n"
    "s_nop 0\n"
    "s_cbranch_vccnz .Lct_tr1_%=\n"
    "s_nop 7\n"
    "s_branch .Lct_tg1_%=\n"
    :: "{s[0:3]}"(s0_), "{s[4:7]}"(s4_), "{s[8:11]}"(s8_), "{s[12:15]}"(s12_), "{s[16:19]}"(s16_), "{v[192:195]}"(v192_), "{v[196:199]}"(v196_), "{v[200:203]}"(v200_), "{v[204:207]}"(v204_), "{v[208:211]}"(v208_), "{v[212:215]}"(v212_) : "memory", "v255", "a255", "s95");
    __builtin_unreachable();
}
DEV void qw_role(const MegaArgs& A, const int L, const int h, const int rep) {
    ROLE_HEAD;
    const u32x4 s0_ = {RFL(rx[0]), RFL(rx[1]), RFL(rx[2]), RFL(rx[3])};
    const u32x4 s4_ = {RFL(lo(A.Wq + h * 192 + 64)), RFL(hi(A.Wq + h * 192 + 64)), RFL(WBYTES - h * 384 - 128), RFL(0x20000u)};
    const u32x4 s8_ = {RFL(lo(A.Wv + h * 192 + 128)), RFL(hi(A.Wv + h * 192 + 128)), RFL(WBYTES - h * 384 - 256), RFL(0x20000u)};
    const u32x4 s12_ = {RFL(w), RFL(REC0 + w * 2048), RFL(QT0 + h * 32768), RFL((A.layers & 2) ? 64 : 0)};
    const u32x4 s16_ = {RFL(xl(Z_CT + (h & 7) * 256, L)), RFL(DBG0 + ((L * 128 + 112 + (h >> 3) * 4 + (h & 3)) * 4 + w) * 2048), RFL(0u), RFL(0u)};
    const u32x4 v0_ = {(unsigned)((8 * w + (l >> 3)) * 12288 + ((l & 7) ^ (((8 * w + (l >> 3)) >> 1) & 6)) * 16), (unsigned)((8 * w + (l >> 3)) * 12288 + ((l & 7) ^ (((8 * w + (l >> 3)) >> 1) & 6)) * 16 + 128), (unsigned)((8 * lq + ((l >> 2) & 1) + ((l >> 1) & 4)) * 128 + (w ^ (((8 * lq + ((l >> 2) & 1) + ((l >> 1) & 4)) >> 2) & 3)) * 32 + (l & 3) * 8), (unsigned)(0u)};
    const u32x4 v64_ = {(unsigned)(l * 16), (unsigned)(l * 16 + 40960), (unsigned)(l * 16 + w * 2048), (unsigned)(l * 16 + w * 1024 + 16384)};
    const u32x4 v68_ = {(unsigned)(l == 99 ? xl(Z_X1 + rep * 4096 + h * 128, 1) : xl(Z_XN + rep * 2048, 0) + (l < 20 ? (l >> 2) * 128 + (l & 3) * 4 : 0)), (unsigned)(l == 0 ? xl(Z_QW + h * 128 + w * 4, 0) : OOB), (unsigned)(l == 20 ? 64 : 0), (unsigned)(0u)};
    asm volatile(
    "s_mov_b32 s20, 0\n"
    "s_mov_b64 s[22:23], 0\n"
    "s_mov_b32 s25, 0\n"
    "s_lshl_b32 s32, s12, 10\n"
    "s_mov_b32 s38, 0\n"
    "s_add_u32 m0, s32, 0\n"
    "s_nop 0\n"
    "buffer_load_dwordx4 v0, s[4:7], s38 offen nt lds\n"
    "s_add_u32 m0, s32, 4096\n"
    "s_nop 0\n"
    "buffer_load_dwordx4 v1, s[4:7], s38 offen nt lds\n"
    "s_add_u32 m0, s32, 8192\n"
    "s_nop 0\n"
    "buffer_load_dwordx4 v0, s[8:11], s38 offen nt lds\n"
    "s_mov_b32 s38, 1572864\n"
    "s_add_u32 m0, s32, 12288\n"
    "s_nop 0\n"
    "buffer_load_dwordx4 v0, s[4:7], s38 offen nt lds\n"
    "s_add_u32 m0, s32, 16384\n"
    "s_nop 0\n"
    "buffer_load_dwordx4 v1, s[4:7], s38 offen nt lds\n"
    "s_add_u32 m0, s32, 20480\n"
    "s_nop 0\n"
    "buffer_load_dwordx4 v0, s[8:11], s38 offen nt lds\n"
    "s_mov_b32 s38, 3145728\n"
    "s_add_u32 m0, s32, 24576\n"
    "s_nop 0\n"
    "buffer_load_dwordx4 v0, s[4:7], s38 offen nt lds\n"
    "s_add_u32 m0, s32, 28672\n"
    "s_nop 0\n"
    "buffer_load_dwordx4 v1, s[4:7], s38 offen nt lds\n"
    "s_add_u32 m0, s32, 32768\n"
    "s_nop 0\n"
    "buffer_load_dwordx4 v0, s[8:11], s38 offen nt lds\n"
    "s_mov_b32 s38, 4718592\n"
    "s_add_u32 m0, s32, 36864\n"
    "s_nop 0\n"
    "buffer_load_dwordx4 v0, s[4:7], s38 offen nt lds\n"
    "s_add_u32 m0, s32, 40960\n"
    "s_nop 0\n"
    "buffer_load_dwordx4 v1, s[4:7], s38 offen nt lds\n"
    "s_add_u32 m0, s32, 45056\n"
    "s_nop 0\n"
    "buffer_load_dwordx4 v0, s[8:11], s38 offen nt lds\n"
    "s_mov_b32 s38, 393216\n"
    "s_add_u32 m0, s32, 49152\n"
    "s_nop 0\n"
    "buffer_load_dwordx4 v0, s[4:7], s38 offen nt lds\n"
    "s_add_u32 m0, s32, 53248\n"
    "s_nop 0\n"
    "buffer_load_dwordx4 v1, s[4:7], s38 offen nt lds\n"
    "s_add_u32 m0, s32, 57344\n"
    "s_nop 0\n"
    "buffer_load_dwordx4 v0, s[8:11], s38 offen nt lds\n"
    "s_mov_b32 s38, 1966080\n"
    "s_add_u32 m0, s32, 61440\n"
    "s_nop 0\n"
    "buffer_load_dwordx4 v0, s[4:7], s38 offen nt lds\n"
    "s_add_u32 m0, s32, 65536\n"
    "s_nop 0\n"
    "buffer_load_dwordx4 v1, s[4:7], s38 offen nt lds\n"
    "s_add_u32 m0, s32, 69632\n"
    "s_nop 0\n"
    "buffer_load_dwordx4 v0, s[8:11], s38 offen nt lds\n"
    "s_mov_b32 s38, 3538944\n"
    "s_add_u32 m0, s32, 73728\n"
    "s_nop 0\n"
    "buffer_load_dwordx4 v0, s[4:7], s38 offen nt lds\n"
    "s_add_u32 m0, s32, 77824\n"
    "s_nop 0\n"
    "buffer_load_dwordx4 v1, s[4:7], s38 offen nt lds\n"
    "s_add_u32 m0, s32, 81920\n"
    "s_nop 0\n"
    "buffer_load_dwordx4 v0, s[8:11], s38 offen nt lds\n"
    "s_mov_b32 s38, 5111808\n"
    "s_add_u32 m0, s32, 86016\n"
    "s_nop 0\n"
    "buffer_load_dwordx4 v0, s[4:7], s38 offen nt lds\n"
    "s_add_u32 m0, s32, 90112\n"
    "s_nop 0\n"
    "buffer_load_dwordx4 v1, s[4:7], s38 offen nt lds\n"
    "s_add_u32 m0, s32, 94208\n"
    "s_nop 0\n"
    "buffer_load_dwordx4 v0, s[8:11], s38 offen nt lds\n"
    "s_mov_b32 s38, 786432\n"
    "s_add_u32 m0, s32, 98304\n"
    "s_nop 0\n"
    "buffer_load_dwordx4 v0, s[4:7], s38 offen nt lds\n"
    "s_add_u32 m0, s32, 102400\n"
    "s_nop 0\n"
    "buffer_load_dwordx4 v1, s[4:7], s38 offen nt lds\n"
    "s_add_u32 m0, s32, 106496\n"
    "s_nop 0\n"
    "buffer_load_dwordx4 v0, s[8:11], s38 offen nt lds\n"
    "s_mov_b32 s38, 2359296\n"
    "s_add_u32 m0, s32, 110592\n"
    "s_nop 0\n"
    "buffer_load_dwordx4 v0, s[4:7], s38 offen nt lds\n"
    "s_add_u32 m0, s32, 114688\n"
    "s_nop 0\n"
    "buffer_load_dwordx4 v1, s[4:7], s38 offen nt lds\n"
    "s_add_u32 m0, s32, 118784\n"
    "s_nop 0\n"
    "buffer_load_dwordx4 v0, s[8:11], s38 offen nt lds\n"
    "s_mov_b32 s38, 3932160\n"
    "s_add_u32 m0, s32, 122880\n"
    "s_nop 0\n"
    "buffer_load_dwordx4 v0, s[4:7], s38 offen nt lds\n"
    "s_add_u32 m0, s32, 126976\n"
    "s_nop 0\n"
    "buffer_load_dwordx4 v1, s[4:7], s38 offen nt lds\n"
    "s_add_u32 m0, s32, 131072\n"
    "s_nop 0\n"
    "buffer_load_dwordx4 v0, s[8:11], s38 offen nt lds\n"
    "s_mov_b32 s38, 5505024\n"
    "s_add_u32 m0, s32, 135168\n"
    "s_nop 0\n"
    "buffer_load_dwordx4 v0, s[4:7], s38 offen nt lds\n"
    "s_add_u32 m0, s32, 139264\n"
    "s_nop 0\n"
    "buffer_load_dwordx4 v1, s[4:7], s38 offen nt lds\n"
    "s_add_u32 m0, s32, 143360\n"
    "s_nop 0\n"
    "buffer_load_dwordx4 v0, s[8:11], s38 offen nt lds\n"
    "s_waitcnt vmcnt(24)\n"
    "s_barrier\n"
    "v_add_u32 v8, 0, v2\n"
    "ds_read_b64_tr_b16 a[0:1], v8 offset:0\n"
    "ds_read_b64_tr_b16 a[2:3], v8 offset:256\n"
    "ds_read_b64_tr_b16 a[80:81], v8 offset:4096\n"
    "ds_read_b64_tr_b16 a[82:83], v8 offset:4352\n"
    "ds_read_b64_tr_b16 a[160:161], v8 offset:8192\n"
    "ds_read_b64_tr_b16 a[162:163], v8 offset:8448\n"
    "ds_read_b64_tr_b16 a[16:17], v8 offset:12288\n"
    "ds_read_b64_tr_b16 a[18:19], v8 offset:12544\n"
    "ds_read_b64_tr_b16 a[96:97], v8 offset:16384\n"
    "ds_read_b64_tr_b16 a[98:99], v8 offset:16640\n"
    "ds_read_b64_tr_b16 a[176:177], v8 offset:20480\n"
    "ds_read_b64_tr_b16 a[178:179], v8 offset:20736\n"
    "ds_read_b64_tr_b16 a[32:33], v8 offset:24576\n"
    "ds_read_b64_tr_b16 a[34:35], v8 offset:24832\n"
    "ds_read_b64_tr_b16 a[112:113], v8 offset:28672\n"
    "ds_read_b64_tr_b16 a[114:115], v8 offset:28928\n"
    "ds_read_b64_tr_b16 a[192:193], v8 offset:32768\n"
    "ds_read_b64_tr_b16 a[194:195], v8 offset:33024\n"
    "ds_read_b64_tr_b16 a[48:49], v8 offset:36864\n"
    "ds_read_b64_tr_b16 a[50:51], v8 offset:37120\n"
    "ds_read_b64_tr_b16 a[128:129], v8 offset:40960\n"
    "ds_read_b64_tr_b16 a[130:131], v8 offset:41216\n"
    "ds_read_b64_tr_b16 a[208:209], v8 offset:45056\n"
    "ds_read_b64_tr_b16 a[210:211], v8 offset:45312\n"
    "s_waitcnt lgkmcnt(0)\n"
    "s_barrier\n"
    "s_mov_b32 s38, 1179648\n"
    "s_add_u32 m0, s32, 0\n"
    "s_nop 0\n"
    "buffer_load_dwordx4 v0, s[4:7], s38 offen nt lds\n"
    "s_add_u32 m0, s32, 4096\n"
    "s_nop 0\n"
    "buffer_load_dwordx4 v1, s[4:7], s38 offen nt lds\n"
    "s_add_u32 m0, s32, 8192\n"
    "s_nop 0\n"
    "buffer_load_dwordx4 v0, s[8:11], s38 offen nt lds\n"
    "s_mov_b32 s38, 2752512\n"
    "s_add_u32 m0, s32, 12288\n"
    "s_nop 0\n"
    "buffer_load_dwordx4 v0, s[4:7], s38 offen nt lds\n"
    "s_add_u32 m0, s32, 16384\n"
    "s_nop 0\n"
    "buffer_load_dwordx4 v1, s[4:7], s38 offen nt lds\n"
    "s_add_u32 m0, s32, 20480\n"
    "s_nop 0\n"
    "buffer_load_dwordx4 v0, s[8:11], s38 offen nt lds\n"
    "s_mov_b32 s38, 4325376\n"
    "s_add_u32 m0, s32, 24576\n"
    "s_nop 0\n"
    "buffer_load_dwordx4 v0, s[4:7], s38 offen nt lds\n"
    "s_add_u32 m0, s32, 28672\n"
    "s_nop 0\n"
    "buffer_load_dwordx4 v1, s[4:7], s38 offen nt lds\n"
    "s_add_u32 m0, s32, 32768\n"
    "s_nop 0\n"
    "buffer_load_dwordx4 v0, s[8:11], s38 offen nt lds\n"
    "s_mov_b32 s38, 5898240\n"
    "s_add_u32 m0, s32, 36864\n"
    "s_nop 0\n"
    "buffer_load_dwordx4 v0, s[4:7], s38 offen nt lds\n"
    "s_add_u32 m0, s32, 40960\n"
    "s_nop 0\n"
    "buffer_load_dwordx4 v1, s[4:7], s38 offen nt lds\n"
    "s_add_u32 m0, s32, 45056\n"
    "s_nop 0\n"
    "buffer_load_dwordx4 v0, s[8:11], s38 offen nt lds\n"
    "v_and_b32 v4, 0x70, v64\n"
    "v_lshlrev_b32 v4, 2, v4\n"
    "v_or_b32 v4, 0x8000, v4\n"
    "buffer_load_dword v5, v4, s[0:3], 0 offen sc1\n"
    "s_waitcnt vmcnt(25)\n"
    "s_barrier\n"
    "v_add_u32 v8, 49152, v2\n"
    "ds_read_b64_tr_b16 a[4:5], v8 offset:0\n"
    "ds_read_b64_tr_b16 a[6:7], v8 offset:256\n"
    "ds_read_b64_tr_b16 a[84:85], v8 offset:4096\n"
    "ds_read_b64_tr_b16 a[86:87], v8 offset:4352\n"
    "ds_read_b64_tr_b16 a[164:165], v8 offset:8192\n"
    "ds_read_b64_tr_b16 a[166:167], v8 offset:8448\n"
    "ds_read_b64_tr_b16 a[20:21], v8 offset:12288\n"
    "ds_read_b64_tr_b16 a[22:23], v8 offset:12544\n"
    "ds_read_b64_tr_b16 a[100:101], v8 offset:16384\n"
    "ds_read_b64_tr_b16 a[102:103], v8 offset:16640\n"
    "ds_read_b64_tr_b16 a[180:181], v8 offset:20480\n"
    "ds_read_b64_tr_b16 a[182:183], v8 offset:20736\n"
    "ds_read_b64_tr_b16 a[36:37], v8 offset:24576\n"
    "ds_read_b64_tr_b16 a[38:39], v8 offset:24832\n"
    "ds_read_b64_tr_b16 a[116:117], v8 offset:28672\n"
    "ds_read_b64_tr_b16 a[118:119], v8 offset:28928\n"
    "ds_read_b64_tr_b16 a[196:197], v8 offset:32768\n"
    "ds_read_b64_tr_b16 a[198:199], v8 offset:33024\n"
    "ds_read_b64_tr_b16 a[52:53], v8 offset:36864\n"
    "ds_read_b64_tr_b16 a[54:55], v8 offset:37120\n"
    "ds_read_b64_tr_b16 a[132:133], v8 offset:40960\n"
    "ds_read_b64_tr_b16 a[134:135], v8 offset:41216\n"
    "ds_read_b64_tr_b16 a[212:213], v8 offset:45056\n"
    "ds_read_b64_tr_b16 a[214:215], v8 offset:45312\n"
    "s_waitcnt lgkmcnt(0)\n"
    "s_barrier\n"
    "s_mov_b32 s38, 6291456\n"
    "s_add_u32 m0, s32, 49152\n"
    "s_nop 0\n"
    "buffer_load_dwordx4 v0, s[4:7], s38 offen nt lds\n"
    "s_add_u32 m0, s32, 53248\n"
    "s_nop 0\n"
    "buffer_load_dwordx4 v1, s[4:7], s38 offen nt lds\n"
    "s_add_u32 m0, s32, 57344\n"
    "s_nop 0\n"
    "buffer_load_dwordx4 v0, s[8:11], s38 offen nt lds\n"
    "s_mov_b32 s38, 6684672\n"
    "s_add_u32 m0, s32, 61440\n"
    "s_nop 0\n"
    "buffer_load_dwordx4 v0, s[4:7], s38 offen nt lds\n"
    "s_add_u32 m0, s32, 65536\n"
    "s_nop 0\n"
    "buffer_load_dwordx4 v1, s[4:7], s38 offen nt lds\n"
    "s_add_u32 m0, s32, 69632\n"
    "s_nop 0\n"
    "buffer_load_dwordx4 v0, s[8:11], s38 offen nt lds\n"
    "s_mov_b32 s38, 7077888\n"
    "s_add_u32 m0, s32, 73728\n"
    "s_nop 0\n"
    "buffer_load_dwordx4 v0, s[4:7], s38 offen nt lds\n"
    "s_add_u32 m0, s32, 77824\n"
    "s_nop 0\n"
    "buffer_load_dwordx4 v1, s[4:7], s38 offen nt lds\n"
    "s_add_u32 m0, s32, 81920\n"
    "s_nop 0\n"
    "buffer_load_dwordx4 v0, s[8:11], s38 offen nt lds\n"
    "s_mov_b32 s38, 7471104\n"
    "s_add_u32 m0, s32, 86016\n"
    "s_nop 0\n"
    "buffer_load_dwordx4 v0, s[4:7], s38 offen nt lds\n"
    "s_add_u32 m0, s32, 90112\n"
    "s_nop 0\n"
    "buffer_load_dwordx4 v1, s[4:7], s38 offen nt lds\n"
    "s_add_u32 m0, s32, 94208\n"
    "s_nop 0\n"
    "buffer_load_dwordx4 v0, s[8:11], s38 offen nt lds\n"
    "s_waitcnt vmcnt(25)\n"
    "s_barrier\n"
    "v_add_u32 v8, 98304, v2\n"
    "ds_read_b64_tr_b16 a[8:9], v8 offset:0\n"
    "ds_read_b64_tr_b16 a[10:11], v8 offset:256\n"
    "ds_read_b64_tr_b16 a[88:89], v8 offset:4096\n"
    "ds_read_b64_tr_b16 a[90:91], v8 offset:4352\n"
    "ds_read_b64_tr_b16 a[168:169], v8 offset:8192\n"
    "ds_read_b64_tr_b16 a[170:171], v8 offset:8448\n"
    "ds_read_b64_tr_b16 a[24:25], v8 offset:12288\n"
    "ds_read_b64_tr_b16 a[26:27], v8 offset:12544\n"
    "ds_read_b64_tr_b16 a[104:105], v8 offset:16384\n"
    "ds_read_b64_tr_b16 a[106:107], v8 offset:16640\n"
    "ds_read_b64_tr_b16 a[184:185], v8 offset:20480\n"
    "ds_read_b64_tr_b16 a[186:187], v8 offset:20736\n"
    "ds_read_b64_tr_b16 a[40:41], v8 offset:24576\n"
    "ds_read_b64_tr_b16 a[42:43], v8 offset:24832\n"
    "ds_read_b64_tr_b16 a[120:121], v8 offset:28672\n"
    "ds_read_b64_tr_b16 a[122:123], v8 offset:28928\n"
    "ds_read_b64_tr_b16 a[200:201], v8 offset:32768\n"
    "ds_read_b64_tr_b16 a[202:203], v8 offset:33024\n"
    "ds_read_b64_tr_b16 a[56:57], v8 offset:36864\n"
    "ds_read_b64_tr_b16 a[58:59], v8 offset:37120\n"
    "ds_read_b64_tr_b16 a[136:137], v8 offset:40960\n"
    "ds_read_b64_tr_b16 a[138:139], v8 offset:41216\n"
    "ds_read_b64_tr_b16 a[216:217], v8 offset:45056\n"
    "ds_read_b64_tr_b16 a[218:219], v8 offset:45312\n"
    "s_waitcnt lgkmcnt(0)\n"
    "s_barrier\n"
    "s_waitcnt vmcnt(13)\n"
    "s_barrier\n"
    "v_add_u32 v8, 0, v2\n"
    "ds_read_b64_tr_b16 a[12:13], v8 offset:0\n"
    "ds_read_b64_tr_b16 a[14:15], v8 offset:256\n"
    "ds_read_b64_tr_b16 a[92:93], v8 offset:4096\n"
    "ds_read_b64_tr_b16 a[94:95], v8 offset:4352\n"
    "ds_read_b64_tr_b16 a[172:173], v8 offset:8192\n"
    "ds_read_b64_tr_b16 a[174:175], v8 offset:8448\n"
    "ds_read_b64_tr_b16 a[28:29], v8 offset:12288\n"
    "ds_read_b64_tr_b16 a[30:31], v8 offset:12544\n"
    "ds_read_b64_tr_b16 a[108:109], v8 offset:16384\n"
    "ds_read_b64_tr_b16 a[110:111], v8 offset:16640\n"
    "ds_read_b64_tr_b16 a[188:189], v8 offset:20480\n"
    "ds_read_b64_tr_b16 a[190:191], v8 offset:20736\n"
    "ds_read_b64_tr_b16 a[44:45], v8 offset:24576\n"
    "ds_read_b64_tr_b16 a[46:47], v8 offset:24832\n"
    "ds_read_b64_tr_b16 a[124:125], v8 offset:28672\n"
    "ds_read_b64_tr_b16 a[126:127], v8 offset:28928\n"
    "ds_read_b64_tr_b16 a[204:205], v8 offset:32768\n"
    "ds_read_b64_tr_b16 a[206:207], v8 offset:33024\n"
    "ds_read_b64_tr_b16 a[60:61], v8 offset:36864\n"
    "ds_read_b64_tr_b16 a[62:63], v8 offset:37120\n"
    "ds_read_b64_tr_b16 a[140:141], v8 offset:40960\n"
    "ds_read_b64_tr_b16 a[142:143], v8 offset:41216\n"
    "ds_read_b64_tr_b16 a[220:221], v8 offset:45056\n"
    "ds_read_b64_tr_b16 a[222:223], v8 offset:45312\n"
    "s_waitcnt lgkmcnt(0)\n"
    "s_barrier\n"
    "s_waitcnt vmcnt(0)\n"
    "s_barrier\n"
    "v_add_u32 v8, 49152, v2\n"
    "ds_read_b64_tr_b16 a[64:65], v8 offset:0\n"
    "ds_read_b64_tr_b16 a[66:67], v8 offset:256\n"
    "ds_read_b64_tr_b16 a[144:145], v8 offset:4096\n"
    "ds_read_b64_tr_b16 a[146:147], v8 offset:4352\n"
    "ds_read_b64_tr_b16 a[224:225], v8 offset:8192\n"
    "ds_read_b64_tr_b16 a[226:227], v8 offset:8448\n"
    "ds_read_b64_tr_b16 a[68:69], v8 offset:12288\n"
    "ds_read_b64_tr_b16 a[70:71], v8 offset:12544\n"
    "ds_read_b64_tr_b16 a[148:149], v8 offset:16384\n"
    "ds_read_b64_tr_b16 a[150:151], v8 offset:16640\n"
    "ds_read_b64_tr_b16 a[228:229], v8 offset:20480\n"
    "ds_read_b64_tr_b16 a[230:231], v8 offset:20736\n"
    "ds_read_b64_tr_b16 a[72:73], v8 offset:24576\n"
    "ds_read_b64_tr_b16 a[74:75], v8 offset:24832\n"
    "ds_read_b64_tr_b16 a[152:153], v8 offset:28672\n"
    "ds_read_b64_tr_b16 a[154:155], v8 offset:28928\n"
    "ds_read_b64_tr_b16 a[232:233], v8 offset:32768\n"
    "ds_read_b64_tr_b16 a[234:235], v8 offset:33024\n"
    "ds_read_b64_tr_b16 a[76:77], v8 offset:36864\n"
    "ds_read_b64_tr_b16 a[78:79], v8 offset:37120\n"
    "ds_read_b64_tr_b16 a[156:157], v8 offset:40960\n"
    "ds_read_b64_tr_b16 a[158:159], v8 offset:41216\n"
    "ds_read_b64_tr_b16 a[236:237], v8 offset:45056\n"
    "ds_read_b64_tr_b16 a[238:239], v8 offset:45312\n"
    "s_waitcnt lgkmcnt(0)\n"
    "s_barrier\n"
    "s_waitcnt vmcnt(12)\n"
    "v_cmp_gt_i32 vcc, 32, v5\n"
    "s_nop 0\n"
    "s_cbranch_vccnz .Lqw_tk0_%=\n"
    ".Lqw_tk1_%=:\n"
    "s_mov_b32 s26, -2\n"
    "v_lshrrev_b32 v4, 2, v64\n"
    ".Lqw_po4_%=:\n"
    "buffer_load_dword v8, v4, s[0:3], s16 offen sc1\n"
    "s_waitcnt vmcnt(0)\n"
    "v_cmp_gt_i32 vcc, 1, v8\n"
    "s_nop 0\n"
    "s_cbranch_vccz .Lqw_pd5_%=\n"
    "s_add_u32 s20, s20, 1\n"
    "s_cmp_lt_u32 s20, 0x40000\n"
    "s_cbranch_scc1 .Lqw_sp6_%=\n"
    "v_mov_b32 v8, 50\n"
    "s_mov_b32 s24, 33728\n"
    "buffer_store_dword v8, off, s[0:3], s24 sc1\n"
    "s_waitcnt vmcnt(0)\n"
    "s_branch .Lqw_pd5_%=\n"
    ".Lqw_sp6_%=:\n"
    "s_sleep 4\n"
    "s_branch .Lqw_po4_%=\n"
    ".Lqw_pd5_%=:\n"
    "s_mov_b32 s26, -1\n"
    "s_mov_b32 s35, 0\n"
    "s_lshl_b32 s37, s12, 11\n"
    "s_mov_b32 s45, 0\n"
    "v_mov_b32 v60, 0\n"
    "s_mov_b32 s43, 2130706432\n"
    "s_mov_b32 s41, 2130706432\n"
    "s_mov_b32 s46, 2130706432\n"
    "s_mov_b32 s34, 2130706432\n"
    "s_add_u32 s26, s26, 1\n"
    "s_sub_u32 s27, s26, 14\n"
    "s_ashr_i32 s28, s27, 1\n"
    "s_and_b32 s29, s27, 1\n"
    "s_add_u32 s30, s28, 14\n"
    "s_cmp_lt_i32 s27, 0\n"
    "s_cselect_b32 s29, 0, s29\n"
    "s_cselect_b32 s30, s26, s30\n"
    "s_cmp_eq_u32 s29, 1\n"
    "s_cselect_b32 s30, s28, s30\n"
    "s_cselect_b32 s27, s15, 64\n"
    "s_lshl_b32 s31, s29, 13\n"
    "s_add_u32 s40, s30, 1\n"
    "s_lshl_b32 s32, s30, 19\n"
    "s_lshl_b32 s25, s30, 11\n"
    "s_xor_b32 s25, s25, s31\n"
    "s_and_b32 s25, s25, 0x2000\n"
    "s_add_u32 s32, s32, s25\n"
    "s_add_u32 s32, s32, s13\n"
    "s_lshl_b32 s33, s30, 20\n"
    "s_lshl_b32 s25, s30, 12\n"
    "s_xor_b32 s25, s25, s31\n"
    "s_and_b32 s25, s25, 0x2000\n"
    "s_add_u32 s33, s33, s25\n"
    "s_add_u32 s33, s33, s14\n"
    "s_mul_i32 s47, s40, s29\n"
    "s_cmp_lt_u32 s30, s27\n"
    "s_cselect_b32 s36, 1, 0\n"
    "s_cselect_b32 s32, s32, 2130706432\n"
    "s_cmp_lt_u32 s30, s27\n"
    "s_cselect_b32 s39, s31, 2130706432\n"
    "s_cselect_b32 s47, s47, 0\n"
    "s_cmp_gt_u32 s47, 0\n"
    "s_cselect_b32 s38, 0, 2130706432\n"
    ".Lqw_top_%=:\n"
    "ds_read_b128 v[24:27], v65 offset:0\n"
    "ds_read_b128 v[28:31], v65 offset:1024\n"
    "ds_read_b128 v[32:35], v65 offset:2048\n"
    "ds_read_b128 v[36:39], v65 offset:3072\n"
    "ds_read_b128 v[40:43], v65 offset:4096\n"
    "ds_read_b128 v[44:47], v65 offset:5120\n"
    "v_add_u32 v60, v60, v70\n"
    "v_cmp_gt_i32 vcc, s47, v60\n"
    "s_nop 0\n"
    "s_cbranch_vccnz .Lqw_wx_%=\n"
    ".Lqw_wxb_%=:\n"
    "s_cmp_eq_u32 s35, 0\n"
    "s_cbranch_scc1 .Lqw_skip_%=\n"
    ".Lqw_mk0x6_%=:\n"
    "s_waitcnt lgkmcnt(4)\n"
    "v_mfma_f32_16x16x32_bf16 v[0:3], a[0:3], v[24:27], 0\n"
    "buffer_store_dwordx4 v[48:51], v66, s[0:3], s46 offen offset:0 sc1\n"
    "v_mfma_f32_16x16x32_bf16 v[4:7], a[0:3], v[28:31], 0\n"
    "v_mfma_f32_16x16x32_bf16 v[8:11], a[80:83], v[24:27], 0\n"
    "v_mfma_f32_16x16x32_bf16 v[12:15], a[80:83], v[28:31], 0\n"
    "v_mfma_f32_16x16x32_bf16 v[16:19], a[160:163], v[24:27], 0\n"
    "ds_read_b128 v[24:27], v65 offset:6144\n"
    "v_mfma_f32_16x16x32_bf16 v[20:23], a[160:163], v[28:31], 0\n"
    "ds_read_b128 v[28:31], v65 offset:7168\n"
    "s_waitcnt lgkmcnt(4)\n"
    "v_mfma_f32_16x16x32_bf16 v[0:3], a[4:7], v[32:35], v[0:3]\n"
    "v_mfma_f32_16x16x32_bf16 v[4:7], a[4:7], v[36:39], v[4:7]\n"
    "buffer_store_dwordx4 v[52:55], v66, s[0:3], s46 offen offset:1024 sc1\n"
    "v_mfma_f32_16x16x32_bf16 v[8:11], a[84:87], v[32:35], v[8:11]\n"
    "v_mfma_f32_16x16x32_bf16 v[12:15], a[84:87], v[36:39], v[12:15]\n"
    "v_mfma_f32_16x16x32_bf16 v[16:19], a[164:167], v[32:35], v[16:19]\n"
    "ds_read_b128 v[32:35], v65 offset:8192\n"
    "v_mfma_f32_16x16x32_bf16 v[20:23], a[164:167], v[36:39], v[20:23]\n"
    "ds_read_b128 v[36:39], v65 offset:9216\n"
    "s_waitcnt lgkmcnt(4)\n"
    "v_mfma_f32_16x16x32_bf16 v[0:3], a[8:11], v[40:43], v[0:3]\n"
    "v_mfma_f32_16x16x32_bf16 v[4:7], a[8:11], v[44:47], v[4:7]\n"
    "v_mfma_f32_16x16x32_bf16 v[8:11], a[88:91], v[40:43], v[8:11]\n"
    "buffer_store_dwordx4 v[56:59], v67, s[0:3], s46 offen sc1\n"
    "s_add_u32 m0, s37, 0\n"
    "v_mfma_f32_16x16x32_bf16 v[12:15], a[88:91], v[44:47], v[12:15]\n"
    "v_mfma_f32_16x16x32_bf16 v[16:19], a[168:171], v[40:43], v[16:19]\n"
    "ds_read_b128 v[40:43], v65 offset:10240\n"
    "v_mfma_f32_16x16x32_bf16 v[20:23], a[168:171], v[44:47], v[20:23]\n"
    "ds_read_b128 v[44:47], v65 offset:11264\n"
    "s_waitcnt lgkmcnt(4)\n"
    "v_mfma_f32_16x16x32_bf16 v[0:3], a[12:15], v[24:27], v[0:3]\n"
    "v_mfma_f32_16x16x32_bf16 v[4:7], a[12:15], v[28:31], v[4:7]\n"
    "v_mfma_f32_16x16x32_bf16 v[8:11], a[92:95], v[24:27], v[8:11]\n"
    "v_mfma_f32_16x16x32_bf16 v[12:15], a[92:95], v[28:31], v[12:15]\n"
    "buffer_load_dwordx4 v64, s[0:3], s32 offen sc1 lds\n"
    "v_mfma_f32_16x16x32_bf16 v[16:19], a[172:175], v[24:27], v[16:19]\n"
    "ds_read_b128 v[24:27], v65 offset:12288\n"
    "v_mfma_f32_16x16x32_bf16 v[20:23], a[172:175], v[28:31], v[20:23]\n"
    "ds_read_b128 v[28:31], v65 offset:13312\n"
    "s_waitcnt lgkmcnt(4)\n"
    "v_mfma_f32_16x16x32_bf16 v[0:3], a[16:19], v[32:35], v[0:3]\n"
    "v_mfma_f32_16x16x32_bf16 v[4:7], a[16:19], v[36:39], v[4:7]\n"
    "buffer_load_dwordx4 v64, s[0:3], s32 offen offset:1024 sc1 lds\n"
    "v_mfma_f32_16x16x32_bf16 v[8:11], a[96:99], v[32:35], v[8:11]\n"
    "s_add_u32 m0, s37, 8192\n"
    "s_add_u32 s32, s32, 0x4000\n"
    "v_mfma_f32_16x16x32_bf16 v[12:15], a[96:99], v[36:39], v[12:15]\n"
    "v_mfma_f32_16x16x32_bf16 v[16:19], a[176:179], v[32:35], v[16:19]\n"
    "ds_read_b128 v[32:35], v65 offset:14336\n"
    "v_mfma_f32_16x16x32_bf16 v[20:23], a[176:179], v[36:39], v[20:23]\n"
    "ds_read_b128 v[36:39], v65 offset:15360\n"
    "s_waitcnt lgkmcnt(4)\n"
    "v_mfma_f32_16x16x32_bf16 v[0:3], a[20:23], v[40:43], v[0:3]\n"
    "buffer_load_dwordx4 v64, s[0:3], s32 offen sc1 lds\n"
    "v_mfma_f32_16x16x32_bf16 v[4:7], a[20:23], v[44:47], v[4:7]\n"
    "v_mfma_f32_16x16x32_bf16 v[8:11], a[100:103], v[40:43], v[8:11]\n"
    "v_mfma_f32_16x16x32_bf16 v[12:15], a[100:103], v[44:47], v[12:15]\n"
    "v_mfma_f32_16x16x32_bf16 v[16:19], a[180:183], v[40:43], v[16:19]\n"
    "ds_read_b128 v[40:43], v65 offset:16384\n"
    "v_mfma_f32_16x16x32_bf16 v[20:23], a[180:183], v[44:47], v[20:23]\n"
    "ds_read_b128 v[44:47], v65 offset:17408\n"
    "s_waitcnt lgkmcnt(4)\n"
    "v_mfma_f32_16x16x32_bf16 v[0:3], a[24:27], v[24:27], v[0:3]\n"
    "buffer_load_dwordx4 v64, s[0:3], s32 offen offset:1024 sc1 lds\n"
    "v_mfma_f32_16x16x32_bf16 v[4:7], a[24:27], v[28:31], v[4:7]\n"
    "s_add_u32 m0, s37, 16384\n"
    "s_add_u32 s32, s32, 0x4000\n"
    "v_mfma_f32_16x16x32_bf16 v[8:11], a[104:107], v[24:27], v[8:11]\n"
    "v_mfma_f32_16x16x32_bf16 v[12:15], a[104:107], v[28:31], v[12:15]\n"
    "v_mfma_f32_16x16x32_bf16 v[16:19], a[184:187], v[24:27], v[16:19]\n"
    "ds_read_b128 v[24:27], v65 offset:18432\n"
    "v_mfma_f32_16x16x32_bf16 v[20:23], a[184:187], v[28:31], v[20:23]\n"
    "ds_read_b128 v[28:31], v65 offset:19456\n"
    "s_waitcnt lgkmcnt(4)\n"
    "v_mfma_f32_16x16x32_bf16 v[0:3], a[28:31], v[32:35], v[0:3]\n"
    "buffer_load_dwordx4 v64, s[0:3], s32 offen sc1 lds\n"
    "v_mfma_f32_16x16x32_bf16 v[4:7], a[28:31], v[36:39], v[4:7]\n"
    "v_mfma_f32_16x16x32_bf16 v[8:11], a[108:111], v[32:35], v[8:11]\n"
    "v_mfma_f32_16x16x32_bf16 v[12:15], a[108:111], v[36:39], v[12:15]\n"
    "v_mfma_f32_16x16x32_bf16 v[16:19], a[188:191], v[32:35], v[16:19]\n"
    "ds_read_b128 v[32:35], v65 offset:20480\n"
    "v_mfma_f32_16x16x32_bf16 v[20:23], a[188:191], v[36:39], v[20:23]\n"
    "ds_read_b128 v[36:39], v65 offset:21504\n"
    "s_waitcnt lgkmcnt(4)\n"
    "v_mfma_f32_16x16x32_bf16 v[0:3], a[32:35], v[40:43], v[0:3]\n"
    "buffer_load_dwordx4 v64, s[0:3], s32 offen offset:1024 sc1 lds\n"
    "v_mfma_f32_16x16x32_bf16 v[4:7], a[32:35], v[44:47], v[4:7]\n"
    "s_add_u32 m0, s37, 24576\n"
    "s_add_u32 s32, s32, 0x4000\n"
    "v_mfma_f32_16x16x32_bf16 v[8:11], a[112:115], v[40:43], v[8:11]\n"
    "v_mfma_f32_16x16x32_bf16 v[12:15], a[112:115], v[44:47], v[12:15]\n"
    "v_mfma_f32_16x16x32_bf16 v[16:19], a[192:195], v[40:43], v[16:19]\n"
    "ds_read_b128 v[40:43], v65 offset:22528\n"
    "v_mfma_f32_16x16x32_bf16 v[20:23], a[192:195], v[44:47], v[20:23]\n"
    "ds_read_b128 v[44:47], v65 offset:23552\n"
    "s_waitcnt lgkmcnt(4)\n"
    "v_mfma_f32_16x16x32_bf16 v[0:3], a[36:39], v[24:27], v[0:3]\n"
    "buffer_load_dwordx4 v64, s[0:3], s32 offen sc1 lds\n"
    "v_mfma_f32_16x16x32_bf16 v[4:7], a[36:39], v[28:31], v[4:7]\n"
    "v_mfma_f32_16x16x32_bf16 v[8:11], a[116:119], v[24:27], v[8:11]\n"
    "v_mfma_f32_16x16x32_bf16 v[12:15], a[116:119], v[28:31], v[12:15]\n"
    "v_mfma_f32_16x16x32_bf16 v[16:19], a[196:199], v[24:27], v[16:19]\n"
    "ds_read_b128 v[24:27], v65 offset:24576\n"
    "v_mfma_f32_16x16x32_bf16 v[20:23], a[196:199], v[28:31], v[20:23]\n"
    "ds_read_b128 v[28:31], v65 offset:25600\n"
    "s_waitcnt lgkmcnt(4)\n"
    "v_mfma_f32_16x16x32_bf16 v[0:3], a[40:43], v[32:35], v[0:3]\n"
    "buffer_load_dwordx4 v64, s[0:3], s32 offen offset:1024 sc1 lds\n"
    "v_mfma_f32_16x16x32_bf16 v[4:7], a[40:43], v[36:39], v[4:7]\n"
    "s_add_u32 m0, s37, 32768\n"
    "s_add_u32 s32, s32, 0x4000\n"
    "v_mfma_f32_16x16x32_bf16 v[8:11], a[120:123], v[32:35], v[8:11]\n"
    "v_mfma_f32_16x16x32_bf16 v[12:15], a[120:123], v[36:39], v[12:15]\n"
    "v_mfma_f32_16x16x32_bf16 v[16:19], a[200:203], v[32:35], v[16:19]\n"
    "ds_read_b128 v[32:35], v65 offset:26624\n"
    "v_mfma_f32_16x16x32_bf16 v[20:23], a[200:203], v[36:39], v[20:23]\n"
    "ds_read_b128 v[36:39], v65 offset:27648\n"
    "s_waitcnt lgkmcnt(4)\n"
    "v_mfma_f32_16x16x32_bf16 v[0:3], a[44:47], v[40:43], v[0:3]\n"
    "buffer_load_dwordx4 v64, s[0:3], s32 offen sc1 lds\n"
    "v_mfma_f32_16x16x32_bf16 v[4:7], a[44:47], v[44:47], v[4:7]\n"
    "v_mfma_f32_16x16x32_bf16 v[8:11], a[124:127], v[40:43], v[8:11]\n"
    "v_mfma_f32_16x16x32_bf16 v[12:15], a[124:127], v[44:47], v[12:15]\n"
    "v_mfma_f32_16x16x32_bf16 v[16:19], a[204:207], v[40:43], v[16:19]\n"
    "ds_read_b128 v[40:43], v65 offset:28672\n"
    "v_mfma_f32_16x16x32_bf16 v[20:23], a[204:207], v[44:47], v[20:23]\n"
    "ds_read_b128 v[44:47], v65 offset:29696\n"
    "s_waitcnt lgkmcnt(4)\n"
    "v_mfma_f32_16x16x32_bf16 v[0:3], a[48:51], v[24:27], v[0:3]\n"
    "buffer_load_dwordx4 v64, s[0:3], s32 offen offset:1024 sc1 lds\n"
    "v_mfma_f32_16x16x32_bf16 v[4:7], a[48:51], v[28:31], v[4:7]\n"
    "s_waitcnt vmcnt(10)\n"
    "v_mov_b32 v61, s44\n"
    "v_mfma_f32_16x16x32_bf16 v[8:11], a[128:131], v[24:27], v[8:11]\n"
    "v_mfma_f32_16x16x32_bf16 v[12:15], a[128:131], v[28:31], v[12:15]\n"
    "v_mfma_f32_16x16x32_bf16 v[16:19], a[208:211], v[24:27], v[16:19]\n"
    "buffer_store_dword v61, v69, s[0:3], s43 offen sc1\n"
    "ds_read_b128 v[24:27], v65 offset:30720\n"
    "v_mfma_f32_16x16x32_bf16 v[20:23], a[208:211], v[28:31], v[20:23]\n"
    "s_mov_b32 s46, s34\n"
    "ds_read_b128 v[28:31], v65 offset:31744\n"
    "s_waitcnt lgkmcnt(4)\n"
    "v_mfma_f32_16x16x32_bf16 v[0:3], a[52:55], v[32:35], v[0:3]\n"
    "s_mov_b32 s43, s41\n"
    "s_mov_b32 s44, s42\n"
    "s_mov_b32 s41, s39\n"
    "v_mfma_f32_16x16x32_bf16 v[4:7], a[52:55], v[36:39], v[4:7]\n"
    "s_mov_b32 s42, s40\n"
    "s_mov_b32 s34, s33\n"
    "s_mov_b32 s35, s36\n"
    "v_mfma_f32_16x16x32_bf16 v[8:11], a[132:135], v[32:35], v[8:11]\n"
    "s_xor_b32 s37, s37, 40960\n"
    "s_add_u32 s26, s26, 1\n"
    "s_sub_u32 s27, s26, 14\n"
    "v_mfma_f32_16x16x32_bf16 v[12:15], a[132:135], v[36:39], v[12:15]\n"
    "s_ashr_i32 s28, s27, 1\n"
    "s_and_b32 s29, s27, 1\n"
    "s_add_u32 s30, s28, 14\n"
    "v_mfma_f32_16x16x32_bf16 v[16:19], a[212:215], v[32:35], v[16:19]\n"
    "ds_read_b128 v[32:35], v65 offset:32768\n"
    "v_mfma_f32_16x16x32_bf16 v[20:23], a[212:215], v[36:39], v[20:23]\n"
    "ds_read_b128 v[36:39], v65 offset:33792\n"
    "s_waitcnt lgkmcnt(4)\n"
    "v_mfma_f32_16x16x32_bf16 v[0:3], a[56:59], v[40:43], v[0:3]\n"
    "s_cmp_lt_i32 s27, 0\n"
    "s_cselect_b32 s29, 0, s29\n"
    "s_cselect_b32 s30, s26, s30\n"
    "v_mfma_f32_16x16x32_bf16 v[4:7], a[56:59], v[44:47], v[4:7]\n"
    "s_cmp_eq_u32 s29, 1\n"
    "s_cselect_b32 s30, s28, s30\n"
    "s_cselect_b32 s27, s15, 64\n"
    "v_mfma_f32_16x16x32_bf16 v[8:11], a[136:139], v[40:43], v[8:11]\n"
    "s_lshl_b32 s31, s29, 13\n"
    "s_add_u32 s40, s30, 1\n"
    "s_lshl_b32 s32, s30, 19\n"
    "v_mfma_f32_16x16x32_bf16 v[12:15], a[136:139], v[44:47], v[12:15]\n"
    "s_lshl_b32 s25, s30, 11\n"
    "s_xor_b32 s25, s25, s31\n"
    "s_and_b32 s25, s25, 0x2000\n"
    "v_mfma_f32_16x16x32_bf16 v[16:19], a[216:219], v[40:43], v[16:19]\n"
    "ds_read_b128 v[40:43], v65 offset:34816\n"
    "s_add_u32 s32, s32, s25\n"
    "v_mfma_f32_16x16x32_bf16 v[20:23], a[216:219], v[44:47], v[20:23]\n"
    "s_add_u32 s32, s32, s13\n"
    "ds_read_b128 v[44:47], v65 offset:35840\n"
    "s_waitcnt lgkmcnt(4)\n"
    "v_mfma_f32_16x16x32_bf16 v[0:3], a[60:63], v[24:27], v[0:3]\n"
    "s_lshl_b32 s33, s30, 20\n"
    "s_lshl_b32 s25, s30, 12\n"
    "s_xor_b32 s25, s25, s31\n"
    "v_mfma_f32_16x16x32_bf16 v[4:7], a[60:63], v[28:31], v[4:7]\n"
    "s_and_b32 s25, s25, 0x2000\n"
    "s_add_u32 s33, s33, s25\n"
    "s_add_u32 s33, s33, s14\n"
    "v_mfma_f32_16x16x32_bf16 v[8:11], a[140:143], v[24:27], v[8:11]\n"
    "s_mul_i32 s47, s40, s29\n"
    "v_mfma_f32_16x16x32_bf16 v[12:15], a[140:143], v[28:31], v[12:15]\n"
    "s_cmp_lt_u32 s30, s27\n"
    "s_cselect_b32 s36, 1, 0\n"
    "s_cselect_b32 s32, s32, 2130706432\n"
    "v_mfma_f32_16x16x32_bf16 v[16:19], a[220:223], v[24:27], v[16:19]\n"
    "ds_read_b128 v[24:27], v65 offset:36864\n"
    "v_mfma_f32_16x16x32_bf16 v[20:23], a[220:223], v[28:31], v[20:23]\n"
    "ds_read_b128 v[28:31], v65 offset:37888\n"
    "s_waitcnt lgkmcnt(4)\n"
    "v_mfma_f32_16x16x32_bf16 v[0:3], a[64:67], v[32:35], v[0:3]\n"
    "s_cmp_lt_u32 s30, s27\n"
    "s_cselect_b32 s39, s31, 2130706432\n"
    "s_cselect_b32 s47, s47, 0\n"
    "v_mfma_f32_16x16x32_bf16 v[4:7], a[64:67], v[36:39], v[4:7]\n"
    "s_cmp_gt_u32 s47, 0\n"
    "s_cselect_b32 s38, 0, 2130706432\n"
    "v_mfma_f32_16x16x32_bf16 v[8:11], a[144:147], v[32:35], v[8:11]\n"
    "buffer_load_dword v60, v68, s[0:3], s38 offen sc1\n"
    "v_mfma_f32_16x16x32_bf16 v[12:15], a[144:147], v[36:39], v[12:15]\n"
    "v_mfma_f32_16x16x32_bf16 v[16:19], a[224:227], v[32:35], v[16:19]\n"
    "ds_read_b128 v[32:35], v65 offset:38912\n"
    "v_mfma_f32_16x16x32_bf16 v[20:23], a[224:227], v[36:39], v[20:23]\n"
    "ds_read_b128 v[36:39], v65 offset:39936\n"
    "s_waitcnt lgkmcnt(4)\n"
    "v_mfma_f32_16x16x32_bf16 v[0:3], a[68:71], v[40:43], v[0:3]\n"
    "v_mfma_f32_16x16x32_bf16 v[4:7], a[68:71], v[44:47], v[4:7]\n"
    "v_mfma_f32_16x16x32_bf16 v[8:11], a[148:151], v[40:43], v[8:11]\n"
    "v_mfma_f32_16x16x32_bf16 v[12:15], a[148:151], v[44:47], v[12:15]\n"
    "v_mfma_f32_16x16x32_bf16 v[16:19], a[228:231], v[40:43], v[16:19]\n"
    "v_mfma_f32_16x16x32_bf16 v[20:23], a[228:231], v[44:47], v[20:23]\n"
    "s_waitcnt lgkmcnt(2)\n"
    "v_mfma_f32_16x16x32_bf16 v[0:3], a[72:75], v[24:27], v[0:3]\n"
    "v_mfma_f32_16x16x32_bf16 v[4:7], a[72:75], v[28:31], v[4:7]\n"
    "v_mfma_f32_16x16x32_bf16 v[8:11], a[152:155], v[24:27], v[8:11]\n"
    "v_mfma_f32_16x16x32_bf16 v[12:15], a[152:155], v[28:31], v[12:15]\n"
    "v_mfma_f32_16x16x32_bf16 v[16:19], a[232:235], v[24:27], v[16:19]\n"
    "v_mfma_f32_16x16x32_bf16 v[20:23], a[232:235], v[28:31], v[20:23]\n"
    "s_waitcnt lgkmcnt(0)\n"
    "v_mfma_f32_16x16x32_bf16 v[0:3], a[76:79], v[32:35], v[0:3]\n"
    "v_mfma_f32_16x16x32_bf16 v[4:7], a[76:79], v[36:39], v[4:7]\n"
    "v_mfma_f32_16x16x32_bf16 v[8:11], a[156:159], v[32:35], v[8:11]\n"
    "v_mfma_f32_16x16x32_bf16 v[12:15], a[156:159], v[36:39], v[12:15]\n"
    "v_mfma_f32_16x16x32_bf16 v[16:19], a[236:239], v[32:35], v[16:19]\n"
    "v_mfma_f32_16x16x32_bf16 v[20:23], a[236:239], v[36:39], v[20:23]\n"
    "v_cvt_pk_bf16_f32 v48, v0, v1\n"
    "v_cvt_pk_bf16_f32 v49, v2, v3\n"
    "v_cvt_pk_bf16_f32 v50, v8, v9\n"
    "v_cvt_pk_bf16_f32 v51, v10, v11\n"
    "v_cvt_pk_bf16_f32 v52, v4, v5\n"
    "v_cvt_pk_bf16_f32 v53, v6, v7\n"
    "v_cvt_pk_bf16_f32 v54, v12, v13\n"
    "v_cvt_pk_bf16_f32 v55, v14, v15\n"
    "v_cvt_pk_bf16_f32 v56, v16, v17\n"
    "v_cvt_pk_bf16_f32 v57, v18, v19\n"
    "v_cvt_pk_bf16_f32 v58, v20, v21\n"
    "v_cvt_pk_bf16_f32 v59, v22, v23\n"
    ".Lqw_mk1x7_%=:\n"
    ".Lqw_join_%=:\n"
    "v_xor_b32 v65, 40960, v65\n"
    "s_waitcnt vmcnt(0) lgkmcnt(0)\n"
    "s_barrier\n"
    ".Lqw_mk2x8_%=:\n"
    "s_cmp_le_u32 s26, 142\n"
    "s_cbranch_scc1 .Lqw_top_%=\n"
    "buffer_store_dwordx4 v[48:51], v66, s[0:3], s46 offen offset:0 sc1\n"
    "buffer_store_dwordx4 v[52:55], v66, s[0:3], s46 offen offset:1024 sc1\n"
    "buffer_store_dwordx4 v[56:59], v67, s[0:3], s46 offen sc1\n"
    "s_waitcnt vmcnt(0)\n"
    "v_mov_b32 v61, s44\n"
    "buffer_store_dword v61, v69, s[0:3], s43 offen sc1\n"
    "s_waitcnt vmcnt(0) lgkmcnt(0)\n"
    "s_endpgm\n"
    ".Lqw_tk0_%=:\n"
    ".Lqw_po1_%=:\n"
    "buffer_load_dword v5, v4, s[0:3], 0 offen sc1\n"
    "s_waitcnt vmcnt(0)\n"
    "v_cmp_gt_i32 vcc, 32, v5\n"
    "s_nop 0\n"
    "s_cbranch_vccz .Lqw_pd2_%=\n"
    "s_add_u32 s20, s20, 1\n"
    "s_cmp_lt_u32 s20, 0x40000\n"
    "s_cbranch_scc1 .Lqw_sp3_%=\n"
    "v_mov_b32 v5, 90\n"
    "s_mov_b32 s24, 33728\n"
    "buffer_store_dword v5, off, s[0:3], s24 sc1\n"
    "s_waitcnt vmcnt(0)\n"
    "s_branch .Lqw_pd2_%=\n"
    ".Lqw_sp3_%=:\n"
    "s_sleep 8\n"
    "s_branch .Lqw_po1_%=\n"
    ".Lqw_pd2_%=:\n"
    "s_branch .Lqw_tk1_%=\n"
    ".Lqw_wx_%=:\n"
    "buffer_store_dwordx4 v[48:51], v66, s[0:3], s46 offen offset:0 sc1\n"
    "buffer_store_dwordx4 v[52:55], v66, s[0:3], s46 offen offset:1024 sc1\n"
    "buffer_store_dwordx4 v[56:59], v67, s[0:3], s46 offen sc1\n"
    "s_waitcnt vmcnt(0)\n"
    "v_mov_b32 v61, s44\n"
    "buffer_store_dword v61, v69, s[0:3], s43 offen sc1\n"
    "s_mov_b32 s43, 2130706432\n"
    "s_mov_b32 s46, 2130706432\n"
    ".Lqw_rt10_%=:\n"
    "s_memrealtime s[22:23]\n"
    "s_waitcnt lgkmcnt(0)\n"
    "s_sub_u32 s24, s45, s22\n"
    "s_cmp_lt_u32 s24, 600\n"
    "s_cbranch_scc1 .Lqw_rt10_%=\n"
    ".Lqw_po11_%=:\n"
    "buffer_load_dword v60, v68, s[0:3], 0 offen sc1\n"
    "s_waitcnt vmcnt(0)\n"
    "v_add_u32 v60, v60, v70\n"
    "s_memrealtime s[22:23]\n"
    "s_waitcnt lgkmcnt(0)\n"
    "s_add_u32 s45, s22, 232\n"
    "v_cmp_gt_i32 vcc, s47, v60\n"
    "s_nop 0\n"
    "s_cbranch_vccz .Lqw_pd12_%=\n"
    "s_add_u32 s20, s20, 1\n"
    "s_cmp_lt_u32 s20, 0x40000\n"
    "s_cbranch_scc1 .Lqw_sp13_%=\n"
    "v_mov_b32 v60, 51\n"
    "s_mov_b32 s24, 33728\n"
    "buffer_store_dword v60, off, s[0:3], s24 sc1\n"
    "s_waitcnt vmcnt(0)\n"
    "s_branch .Lqw_pd12_%=\n"
    ".Lqw_sp13_%=:\n"
    "s_sleep 10\n"
    "s_branch .Lqw_po11_%=\n"
    ".Lqw_pd12_%=:\n"
    "s_branch .Lqw_wxb_%=\n"
    ".Lqw_skip_%=:\n"
    "buffer_store_dwordx4 v[48:51], v66, s[0:3], s46 offen offset:0 sc1\n"
    "buffer_store_dwordx4 v[52:55], v66, s[0:3], s46 offen offset:1024 sc1\n"
    "buffer_store_dwordx4 v[56:59], v67, s[0:3], s46 offen sc1\n"
    "s_add_u32 m0, s37, 0\n"
    "s_nop 0\n"
    "buffer_load_dwordx4 v64, s[0:3], s32 offen sc1 lds\n"
    "buffer_load_dwordx4 v64, s[0:3], s32 offen offset:1024 sc1 lds\n"
    "s_add_u32 m0, s37, 8192\n"
    "s_add_u32 s32, s32, 0x4000\n"
    "buffer_load_dwordx4 v64, s[0:3], s32 offen sc1 lds\n"
    "buffer_load_dwordx4 v64, s[0:3], s32 offen offset:1024 sc1 lds\n"
    "s_add_u32 m0, s37, 16384\n"
    "s_add_u32 s32, s32, 0x4000\n"
    "buffer_load_dwordx4 v64, s[0:3], s32 offen sc1 lds\n"
    "buffer_load_dwordx4 v64, s[0:3], s32 offen offset:1024 sc1 lds\n"
    "s_add_u32 m0, s37, 24576\n"
    "s_add_u32 s32, s32, 0x4000\n"
    "buffer_load_dwordx4 v64, s[0:3], s32 offen sc1 lds\n"
    "buffer_load_dwordx4 v64, s[0:3], s32 offen offset:1024 sc1 lds\n"
    "s_add_u32 m0, s37, 32768\n"
    "s_add_u32 s32, s32, 0x4000\n"
    "buffer_load_dwordx4 v64, s[0:3], s32 offen sc1 lds\n"
    "buffer_load_dwordx4 v64, s[0:3], s32 offen offset:1024 sc1 lds\n"
    "s_waitcnt vmcnt(0)\n"
    "v_mov_b32 v61, s44\n"
    "buffer_store_dword v61, v69, s[0:3], s43 offen sc1\n"
    "s_mov_b32 s34, 2130706432\n"
    "s_mov_b32 s46, s34\n"
    "s_mov_b32 s43, s41\n"
    "s_mov_b32 s44, s42\n"
    "s_mov_b32 s41, s39\n"
    "s_mov_b32 s42, s40\n"
    "s_mov_b32 s34, s33\n"
    "s_mov_b32 s35, s36\n"
    "s_xor_b32 s37, s37, 40960\n"
    "s_add_u32 s26, s26, 1\n"
    "s_sub_u32 s27, s26, 14\n"
    "s_ashr_i32 s28, s27, 1\n"
    "s_and_b32 s29, s27, 1\n"
    "s_add_u32 s30, s28, 14\n"
    "s_cmp_lt_i32 s27, 0\n"
    "s_cselect_b32 s29, 0, s29\n"
    "s_cselect_b32 s30, s26, s30\n"
    "s_cmp_eq_u32 s29, 1\n"
    "s_cselect_b32 s30, s28, s30\n"
    "s_cselect_b32 s27, s15, 64\n"
    "s_lshl_b32 s31, s29, 13\n"
    "s_add_u32 s40, s30, 1\n"
    "s_lshl_b32 s32, s30, 19\n"
    "s_lshl_b32 s25, s30, 11\n"
    "s_xor_b32 s25, s25, s31\n"
    "s_and_b32 s25, s25, 0x2000\n"
    "s_add_u32 s32, s32, s25\n"
    "s_add_u32 s32, s32, s13\n"
    "s_lshl_b32 s33, s30, 20\n"
    "s_lshl_b32 s25, s30, 12\n"
    "s_xor_b32 s25, s25, s31\n"
    "s_and_b32 s25, s25, 0x2000\n"
    "s_add_u32 s33, s33, s25\n"
    "s_add_u32 s33, s33, s14\n"
    "s_mul_i32 s47, s40, s29\n"
    "s_cmp_lt_u32 s30, s27\n"
    "s_cselect_b32 s36, 1, 0\n"
    "s_cselect_b32 s32, s32, 2130706432\n"
    "s_cmp_lt_u32 s30, s27\n"
    "s_cselect_b32 s39, s31, 2130706432\n"
    "s_cselect_b32 s47, s47, 0\n"
    "s_cmp_gt_u32 s47, 0\n"
    "s_cselect_b32 s38, 0, 2130706432\n"
    "buffer_load_dword v60, v68, s[0:3], s38 offen sc1\n"
    "s_branch .Lqw_join_%=\n"
    :: "{s[0:3]}"(s0_), "{s[4:7]}"(s4_), "{s[8:11]}"(s8_), "{s[12:15]}"(s12_), "{s[16:19]}"(s16_), "{v[0:3]}"(v0_), "{v[64:67]}"(v64_), "{v[68:71]}"(v68_) : "memory", "v255", "a255", "s95");
    __builtin_unreachable();
}
DEV void b1_role(const MegaArgs& A, const int L, const int i, const int rep) {
    ROLE_HEAD;
    const int qd = 4 * w + lq, sw = (l16 & 6) << 3, hw = w < 3 ? w : 2, hd = 3 * i + w;
    const bool live = w < 3 && hd < 32;
    const unsigned om = live ? RC_O - RC_X1 - w * 2048 + (hd >> 3) * 16384 + ((hd >> 1) & 3) * 2048 + (lq & 1) * 1024 + (2 * (hd & 1) + (lq >> 1)) * 256 + l16 * 16 : OOB;
    const unsigned ox = live && l < 16 ? RC_O - RC_X1 - w * 2048 + 65536 + hd * 256 + l16 * 16 : OOB;
    u64 wm[4];
#pragma unroll
    for (int e = 0; e < 4; ++e) { const int d = l16 + 16 - 16 * (e >> 1) - 4 * lq - 2 * (e & 1) - (l16 & 1); wm[e] = __ballot(d >= 0 && d <= 14); }
    const u32x4 s0_ = {RFL(rx[0]), RFL(rx[1]), RFL(rx[2]), RFL(rx[3])};
    const u32x4 s4_ = {RFL(lo(A.wqkv + 60 * i + 16 * w)), RFL(hi(A.wqkv + 60 * i + 16 * w)), RFL((640 * 1920 - 60 * i - 16 * w) * 2), RFL(0x20000u)};
    const u32x4 s8_ = {RFL((unsigned)wm[0]), RFL((unsigned)(wm[0] >> 32)), RFL((unsigned)wm[1]), RFL((unsigned)(wm[1] >> 32))};
    const u32x4 s12_ = {RFL((unsigned)wm[2]), RFL((unsigned)(wm[2] >> 32)), RFL((unsigned)wm[3]), RFL((unsigned)(wm[3] >> 32))};
    const u32x4 s16_ = {RFL(w == 3 ? 0xf000f000u : 0u), RFL(w == 3 ? 0xf000f000u : 0u), RFL(w), RFL(L << 13)};
    const u32x4 s20_ = {RFL(REC0 + RC_X1 + w * 2048), RFL(xl(Z_X1 + rep * 4096, L)), RFL(DBG0 + ((L * 128 + 96 + i) * 4 + w) * 2048), RFL(blockIdx.x == 0 ? 6 : 0)};
    const u32x4 v0_ = {(unsigned)((l >> 1) * 3840 + (l & 1) * 16 + 0), (unsigned)((l >> 1) * 3840 + (l & 1) * 16 + 256), (unsigned)((l >> 1) * 3840 + (l & 1) * 16 + 512), (unsigned)(w * 40960 + (8 * lq + (l16 >> 2)) * 32 + (l & 3) * 8)};
    const u32x4 v224_ = {(unsigned)(l * 16 + ((w + 0) & 3) * 2048), (unsigned)(l * 16 + ((w + 1) & 3) * 2048), (unsigned)(l * 16 + ((w + 2) & 3) * 2048), (unsigned)(l * 16 + ((w + 3) & 3) * 2048)};
    const u32x4 v228_ = {(unsigned)(l * 16), (unsigned)((l & 31) * 128), (unsigned)(l < 4 && w == 3 ? xl(Z_O + l * 2048 + i * 128, L) : OOB), (unsigned)(qd == 15 ? 86016 : 40960 + (qd / 5) * 2048 + l16 * 64 + (((qd % 5) * 8) ^ sw))};
    const u32x4 v232_ = {(unsigned)(qd == 15 ? 86016 : 49152 + (qd / 5) * 4096 + l16 * 64 + (((qd % 5) * 8) ^ sw)), (unsigned)(qd == 15 ? 86016 : 61440 + (qd / 5) * 4096 + l16 * 64 + (((qd % 5) * 8) ^ sw)), (unsigned)(40960 + hw * 2048 + l16 * 64 + ((lq * 16) ^ sw)), (unsigned)(49152 + hw * 4096 + l16 * 64 + ((lq * 16) ^ sw))};
    const u32x4 v236_ = {(unsigned)(51200 + hw * 4096 + l16 * 64 + ((lq * 16) ^ sw)), (unsigned)(61440 + hw * 4096 + (l >> 2) * 64 + ((0 + (l & 3) * 8) ^ (((l >> 2) & 6) << 3))), (unsigned)(63488 + hw * 4096 + (l >> 2) * 64 + ((0 + (l & 3) * 8) ^ (((l >> 2) & 6) << 3))), (unsigned)(61440 + hw * 4096 + (l >> 2) * 64 + ((32 + (l & 3) * 8) ^ (((l >> 2) & 6) << 3)))};
    const u32x4 v240_ = {(unsigned)(63488 + hw * 4096 + (l >> 2) * 64 + ((32 + (l & 3) * 8) ^ (((l >> 2) & 6) << 3))), (unsigned)(73728 + w * 2048 + l * 16), (unsigned)(73728 + (l >> 5) * 1024 + ((l16 >> 2) * 16 + l16) * 16 + (l & 3) * 4), (unsigned)(81920 + w * 512 + l * 4)};
    const u32x4 v244_ = {(unsigned)(81920 + 3 * 512 + (l >> 5) * 256 + (48 + l16) * 4), (unsigned)(om), (unsigned)(ox), (unsigned)((l & 1) ? 0x01000c0cu : 0x0c0c0100u)};
    const u32x4 v248_ = {(unsigned)((l & 1) ? 0x03020c0cu : 0x0c0c0302u), (unsigned)(0xff800000u), (unsigned)(0u), (unsigned)(0u)};
    asm volatile(
    "s_mov_b32 s24, 0\n"
    "s_mov_b64 s[26:27], 0\n"
    "s_mov_b32 s29, 0\n"
    "s_cmp_eq_u32 s19, 0\n"
    "s_cselect_b32 s28, 12, 50\n"
    "s_sub_u32 s28, s28, s23\n"
    ".Lb1_ho1_%=:\n"
    "s_sub_u32 s28, s28, 1\n"
    "s_cbranch_scc1 .Lb1_hd2_%=\n"
    "s_sleep 17\n"
    "s_branch .Lb1_ho1_%=\n"
    ".Lb1_hd2_%=:\n"
    "v_and_b32 v30, 0x70, v228\n"
    "v_lshlrev_b32 v30, 2, v30\n"
    "v_or_b32 v30, 0x8000, v30\n"
    "buffer_load_dword v31, v30, s[0:3], 0 offen sc1\n"
    "s_waitcnt vmcnt(0)\n"
    "v_cmp_gt_i32 vcc, 32, v31\n"
    "s_nop 0\n"
    "s_cbranch_vccnz .Lb1_tk0_%=\n"
    ".Lb1_tk1_%=:\n"
    "s_mul_i32 s32, s18, 40960\n"
    "s_add_u32 s35, s18, 0\n"
    "s_and_b32 s35, s35, 3\n"
    "s_add_u32 s35, s35, 0\n"
    "s_mul_i32 s35, s35, 122880\n"
    "s_add_u32 m0, s32, 0\n"
    "s_nop 0\n"
    "buffer_load_dwordx4 v0, s[4:7], s35 offen offset:0 lds\n"
    "buffer_load_dwordx4 v1, s[4:7], s35 offen offset:1024 lds\n"
    "buffer_load_dwordx4 v2, s[4:7], s35 offen offset:2048 lds\n"
    "s_add_u32 s35, s18, 1\n"
    "s_and_b32 s35, s35, 3\n"
    "s_add_u32 s35, s35, 0\n"
    "s_mul_i32 s35, s35, 122880\n"
    "s_add_u32 m0, s32, 3072\n"
    "s_nop 0\n"
    "buffer_load_dwordx4 v0, s[4:7], s35 offen offset:0 lds\n"
    "buffer_load_dwordx4 v1, s[4:7], s35 offen offset:1024 lds\n"
    "buffer_load_dwordx4 v2, s[4:7], s35 offen offset:2048 lds\n"
    "s_add_u32 s35, s18, 2\n"
    "s_and_b32 s35, s35, 3\n"
    "s_add_u32 s35, s35, 0\n"
    "s_mul_i32 s35, s35, 122880\n"
    "s_add_u32 m0, s32, 6144\n"
    "s_nop 0\n"
    "buffer_load_dwordx4 v0, s[4:7], s35 offen offset:0 lds\n"
    "buffer_load_dwordx4 v1, s[4:7], s35 offen offset:1024 lds\n"
    "buffer_load_dwordx4 v2, s[4:7], s35 offen offset:2048 lds\n"
    "s_add_u32 s35, s18, 3\n"
    "s_and_b32 s35, s35, 3\n"
    "s_add_u32 s35, s35, 0\n"
    "s_mul_i32 s35, s35, 122880\n"
    "s_add_u32 m0, s32, 9216\n"
    "s_nop 0\n"
    "buffer_load_dwordx4 v0, s[4:7], s35 offen offset:0 lds\n"
    "buffer_load_dwordx4 v1, s[4:7], s35 offen offset:1024 lds\n"
    "buffer_load_dwordx4 v2, s[4:7], s35 offen offset:2048 lds\n"
    "s_add_u32 s35, s18, 0\n"
    "s_and_b32 s35, s35, 3\n"
    "s_add_u32 s35, s35, 4\n"
    "s_mul_i32 s35, s35, 122880\n"
    "s_add_u32 m0, s32, 12288\n"
    "s_nop 0\n"
    "buffer_load_dwordx4 v0, s[4:7], s35 offen offset:0 lds\n"
    "buffer_load_dwordx4 v1, s[4:7], s35 offen offset:1024 lds\n"
    "buffer_load_dwordx4 v2, s[4:7], s35 offen offset:2048 lds\n"
    "s_add_u32 s35, s18, 1\n"
    "s_and_b32 s35, s35, 3\n"
    "s_add_u32 s35, s35, 4\n"
    "s_mul_i32 s35, s35, 122880\n"
    "s_add_u32 m0, s32, 15360\n"
    "s_nop 0\n"
    "buffer_load_dwordx4 v0, s[4:7], s35 offen offset:0 lds\n"
    "buffer_load_dwordx4 v1, s[4:7], s35 offen offset:1024 lds\n"
    "buffer_load_dwordx4 v2, s[4:7], s35 offen offset:2048 lds\n"
    "s_add_u32 s35, s18, 2\n"
    "s_and_b32 s35, s35, 3\n"
    "s_add_u32 s35, s35, 4\n"
    "s_mul_i32 s35, s35, 122880\n"
    "s_add_u32 m0, s32, 18432\n"
    "s_nop 0\n"
    "buffer_load_dwordx4 v0, s[4:7], s35 offen offset:0 lds\n"
    "buffer_load_dwordx4 v1, s[4:7], s35 offen offset:1024 lds\n"
    "buffer_load_dwordx4 v2, s[4:7], s35 offen offset:2048 lds\n"
    "s_add_u32 s35, s18, 3\n"
    "s_and_b32 s35, s35, 3\n"
    "s_add_u32 s35, s35, 4\n"
    "s_mul_i32 s35, s35, 122880\n"
    "s_add_u32 m0, s32, 21504\n"
    "s_nop 0\n"
    "buffer_load_dwordx4 v0, s[4:7], s35 offen offset:0 lds\n"
    "buffer_load_dwordx4 v1, s[4:7], s35 offen offset:1024 lds\n"
    "buffer_load_dwordx4 v2, s[4:7], s35 offen offset:2048 lds\n"
    "s_add_u32 s35, s18, 0\n"
    "s_and_b32 s35, s35, 3\n"
    "s_add_u32 s35, s35, 8\n"
    "s_mul_i32 s35, s35, 122880\n"
    "s_add_u32 m0, s32, 24576\n"
    "s_nop 0\n"
    "buffer_load_dwordx4 v0, s[4:7], s35 offen offset:0 lds\n"
    "buffer_load_dwordx4 v1, s[4:7], s35 offen offset:1024 lds\n"
    "buffer_load_dwordx4 v2, s[4:7], s35 offen offset:2048 lds\n"
    "s_add_u32 s35, s18, 1\n"
    "s_and_b32 s35, s35, 3\n"
    "s_add_u32 s35, s35, 8\n"
    "s_mul_i32 s35, s35, 122880\n"
    "s_add_u32 m0, s32, 27648\n"
    "s_nop 0\n"
    "buffer_load_dwordx4 v0, s[4:7], s35 offen offset:0 lds\n"
    "buffer_load_dwordx4 v1, s[4:7], s35 offen offset:1024 lds\n"
    "buffer_load_dwordx4 v2, s[4:7], s35 offen offset:2048 lds\n"
    "s_add_u32 s35, s18, 2\n"
    "s_and_b32 s35, s35, 3\n"
    "s_add_u32 s35, s35, 8\n"
    "s_mul_i32 s35, s35, 122880\n"
    "s_add_u32 m0, s32, 30720\n"
    "s_nop 0\n"
    "buffer_load_dwordx4 v0, s[4:7], s35 offen offset:0 lds\n"
    "buffer_load_dwordx4 v1, s[4:7], s35 offen offset:1024 lds\n"
    "buffer_load_dwordx4 v2, s[4:7], s35 offen offset:2048 lds\n"
    "s_add_u32 s35, s18, 3\n"
    "s_and_b32 s35, s35, 3\n"
    "s_add_u32 s35, s35, 8\n"
    "s_mul_i32 s35, s35, 122880\n"
    "s_add_u32 m0, s32, 33792\n"
    "s_nop 0\n"
    "buffer_load_dwordx4 v0, s[4:7], s35 offen offset:0 lds\n"
    "buffer_load_dwordx4 v1, s[4:7], s35 offen offset:1024 lds\n"
    "buffer_load_dwordx4 v2, s[4:7], s35 offen offset:2048 lds\n"
    "s_add_u32 s35, s18, 0\n"
    "s_and_b32 s35, s35, 3\n"
    "s_add_u32 s35, s35, 12\n"
    "s_mul_i32 s35, s35, 122880\n"
    "s_add_u32 m0, s32, 36864\n"
    "s_nop 0\n"
    "buffer_load_dwordx4 v0, s[4:7], s35 offen offset:0 lds\n"
    "buffer_load_dwordx4 v1, s[4:7], s35 offen offset:1024 lds\n"
    "buffer_load_dwordx4 v2, s[4:7], s35 offen offset:2048 lds\n"
    "s_waitcnt vmcnt(36)\n"
    "ds_read_b64_tr_b16 a[0:1], v3 offset:0\n"
    "ds_read_b64_tr_b16 a[2:3], v3 offset:128\n"
    "ds_read_b64_tr_b16 a[80:81], v3 offset:1024\n"
    "ds_read_b64_tr_b16 a[82:83], v3 offset:1152\n"
    "ds_read_b64_tr_b16 a[160:161], v3 offset:2048\n"
    "ds_read_b64_tr_b16 a[162:163], v3 offset:2176\n"
    "s_waitcnt lgkmcnt(0)\n"
    "s_add_u32 s35, s18, 1\n"
    "s_and_b32 s35, s35, 3\n"
    "s_add_u32 s35, s35, 12\n"
    "s_mul_i32 s35, s35, 122880\n"
    "s_add_u32 m0, s32, 0\n"
    "s_nop 0\n"
    "buffer_load_dwordx4 v0, s[4:7], s35 offen offset:0 lds\n"
    "buffer_load_dwordx4 v1, s[4:7], s35 offen offset:1024 lds\n"
    "buffer_load_dwordx4 v2, s[4:7], s35 offen offset:2048 lds\n"
    "s_waitcnt vmcnt(36)\n"
    "ds_read_b64_tr_b16 a[4:5], v3 offset:3072\n"
    "ds_read_b64_tr_b16 a[6:7], v3 offset:3200\n"
    "ds_read_b64_tr_b16 a[84:85], v3 offset:4096\n"
    "ds_read_b64_tr_b16 a[86:87], v3 offset:4224\n"
    "ds_read_b64_tr_b16 a[164:165], v3 offset:5120\n"
    "ds_read_b64_tr_b16 a[166:167], v3 offset:5248\n"
    "s_waitcnt lgkmcnt(0)\n"
    "s_add_u32 s35, s18, 2\n"
    "s_and_b32 s35, s35, 3\n"
    "s_add_u32 s35, s35, 12\n"
    "s_mul_i32 s35, s35, 122880\n"
    "s_add_u32 m0, s32, 3072\n"
    "s_nop 0\n"
    "buffer_load_dwordx4 v0, s[4:7], s35 offen offset:0 lds\n"
    "buffer_load_dwordx4 v1, s[4:7], s35 offen offset:1024 lds\n"
    "buffer_load_dwordx4 v2, s[4:7], s35 offen offset:2048 lds\n"
    "s_waitcnt vmcnt(36)\n"
    "ds_read_b64_tr_b16 a[8:9], v3 offset:6144\n"
    "ds_read_b64_tr_b16 a[10:11], v3 offset:6272\n"
    "ds_read_b64_tr_b16 a[88:89], v3 offset:7168\n"
    "ds_read_b64_tr_b16 a[90:91], v3 offset:7296\n"
    "ds_read_b64_tr_b16 a[168:169], v3 offset:8192\n"
    "ds_read_b64_tr_b16 a[170:171], v3 offset:8320\n"
    "s_waitcnt lgkmcnt(0)\n"
    "s_add_u32 s35, s18, 3\n"
    "s_and_b32 s35, s35, 3\n"
    "s_add_u32 s35, s35, 12\n"
    "s_mul_i32 s35, s35, 122880\n"
    "s_add_u32 m0, s32, 6144\n"
    "s_nop 0\n"
    "buffer_load_dwordx4 v0, s[4:7], s35 offen offset:0 lds\n"
    "buffer_load_dwordx4 v1, s[4:7], s35 offen offset:1024 lds\n"
    "buffer_load_dwordx4 v2, s[4:7], s35 offen offset:2048 lds\n"
    "s_waitcnt vmcnt(36)\n"
    "ds_read_b64_tr_b16 a[12:13], v3 offset:9216\n"
    "ds_read_b64_tr_b16 a[14:15], v3 offset:9344\n"
    "ds_read_b64_tr_b16 a[92:93], v3 offset:10240\n"
    "ds_read_b64_tr_b16 a[94:95], v3 offset:10368\n"
    "ds_read_b64_tr_b16 a[172:173], v3 offset:11264\n"
    "ds_read_b64_tr_b16 a[174:175], v3 offset:11392\n"
    "s_waitcnt lgkmcnt(0)\n"
    "s_add_u32 s35, s18, 0\n"
    "s_and_b32 s35, s35, 3\n"
    "s_add_u32 s35, s35, 16\n"
    "s_mul_i32 s35, s35, 122880\n"
    "s_add_u32 m0, s32, 9216\n"
    "s_nop 0\n"
    "buffer_load_dwordx4 v0, s[4:7], s35 offen offset:0 lds\n"
    "buffer_load_dwordx4 v1, s[4:7], s35 offen offset:1024 lds\n"
    "buffer_load_dwordx4 v2, s[4:7], s35 offen offset:2048 lds\n"
    "s_waitcnt vmcnt(36)\n"
    "ds_read_b64_tr_b16 a[16:17], v3 offset:12288\n"
    "ds_read_b64_tr_b16 a[18:19], v3 offset:12416\n"
    "ds_read_b64_tr_b16 a[96:97], v3 offset:13312\n"
    "ds_read_b64_tr_b16 a[98:99], v3 offset:13440\n"
    "ds_read_b64_tr_b16 a[176:177], v3 offset:14336\n"
    "ds_read_b64_tr_b16 a[178:179], v3 offset:14464\n"
    "s_waitcnt lgkmcnt(0)\n"
    "s_add_u32 s35, s18, 1\n"
    "s_and_b32 s35, s35, 3\n"
    "s_add_u32 s35, s35, 16\n"
    "s_mul_i32 s35, s35, 122880\n"
    "s_add_u32 m0, s32, 12288\n"
    "s_nop 0\n"
    "buffer_load_dwordx4 v0, s[4:7], s35 offen offset:0 lds\n"
    "buffer_load_dwordx4 v1, s[4:7], s35 offen offset:1024 lds\n"
    "buffer_load_dwordx4 v2, s[4:7], s35 offen offset:2048 lds\n"
    "s_waitcnt vmcnt(36)\n"
    "ds_read_b64_tr_b16 a[20:21], v3 offset:15360\n"
    "ds_read_b64_tr_b16 a[22:23], v3 offset:15488\n"
    "ds_read_b64_tr_b16 a[100:101], v3 offset:16384\n"
    "ds_read_b64_tr_b16 a[102:103], v3 offset:16512\n"
    "ds_read_b64_tr_b16 a[180:181], v3 offset:17408\n"
    "ds_read_b64_tr_b16 a[182:183], v3 offset:17536\n"
    "s_waitcnt lgkmcnt(0)\n"
    "s_add_u32 s35, s18, 2\n"
    "s_and_b32 s35, s35, 3\n"
    "s_add_u32 s35, s35, 16\n"
    "s_mul_i32 s35, s35, 122880\n"
    "s_add_u32 m0, s32, 15360\n"
    "s_nop 0\n"
    "buffer_load_dwordx4 v0, s[4:7], s35 offen offset:0 lds\n"
    "buffer_load_dwordx4 v1, s[4:7], s35 offen offset:1024 lds\n"
    "buffer_load_dwordx4 v2, s[4:7], s35 offen offset:2048 lds\n"
    "s_waitcnt vmcnt(36)\n"
    "ds_read_b64_tr_b16 a[24:25], v3 offset:18432\n"
    "ds_read_b64_tr_b16 a[26:27], v3 offset:18560\n"
    "ds_read_b64_tr_b16 a[104:105], v3 offset:19456\n"
    "ds_read_b64_tr_b16 a[106:107], v3 offset:19584\n"
    "ds_read_b64_tr_b16 a[184:185], v3 offset:20480\n"
    "ds_read_b64_tr_b16 a[186:187], v3 offset:20608\n"
    "s_waitcnt lgkmcnt(0)\n"
    "s_add_u32 s35, s18, 3\n"
    "s_and_b32 s35, s35, 3\n"
    "s_add_u32 s35, s35, 16\n"
    "s_mul_i32 s35, s35, 122880\n"
    "s_add_u32 m0, s32, 18432\n"
    "s_nop 0\n"
    "buffer_load_dwordx4 v0, s[4:7], s35 offen offset:0 lds\n"
    "buffer_load_dwordx4 v1, s[4:7], s35 offen offset:1024 lds\n"
    "buffer_load_dwordx4 v2, s[4:7], s35 offen offset:2048 lds\n"
    "s_waitcnt vmcnt(36)\n"
    "ds_read_b64_tr_b16 a[28:29], v3 offset:21504\n"
    "ds_read_b64_tr_b16 a[30:31], v3 offset:21632\n"
    "ds_read_b64_tr_b16 a[108:109], v3 offset:22528\n"
    "ds_read_b64_tr_b16 a[110:111], v3 offset:22656\n"
    "ds_read_b64_tr_b16 a[188:189], v3 offset:23552\n"
    "ds_read_b64_tr_b16 a[190:191], v3 offset:23680\n"
    "s_waitcnt lgkmcnt(0)\n"
    "s_waitcnt vmcnt(33)\n"
    "ds_read_b64_tr_b16 a[32:33], v3 offset:24576\n"
    "ds_read_b64_tr_b16 a[34:35], v3 offset:24704\n"
    "ds_read_b64_tr_b16 a[112:113], v3 offset:25600\n"
    "ds_read_b64_tr_b16 a[114:115], v3 offset:25728\n"
    "ds_read_b64_tr_b16 a[192:193], v3 offset:26624\n"
    "ds_read_b64_tr_b16 a[194:195], v3 offset:26752\n"
    "s_waitcnt lgkmcnt(0)\n"
    "s_waitcnt vmcnt(30)\n"
    "ds_read_b64_tr_b16 a[36:37], v3 offset:27648\n"
    "ds_read_b64_tr_b16 a[38:39], v3 offset:27776\n"
    "ds_read_b64_tr_b16 a[116:117], v3 offset:28672\n"
    "ds_read_b64_tr_b16 a[118:119], v3 offset:28800\n"
    "ds_read_b64_tr_b16 a[196:197], v3 offset:29696\n"
    "ds_read_b64_tr_b16 a[198:199], v3 offset:29824\n"
    "s_waitcnt lgkmcnt(0)\n"
    "s_waitcnt vmcnt(27)\n"
    "ds_read_b64_tr_b16 a[40:41], v3 offset:30720\n"
    "ds_read_b64_tr_b16 a[42:43], v3 offset:30848\n"
    "ds_read_b64_tr_b16 a[120:121], v3 offset:31744\n"
    "ds_read_b64_tr_b16 a[122:123], v3 offset:31872\n"
    "ds_read_b64_tr_b16 a[200:201], v3 offset:32768\n"
    "ds_read_b64_tr_b16 a[202:203], v3 offset:32896\n"
    "s_waitcnt lgkmcnt(0)\n"
    "s_waitcnt vmcnt(24)\n"
    "ds_read_b64_tr_b16 a[44:45], v3 offset:33792\n"
    "ds_read_b64_tr_b16 a[46:47], v3 offset:33920\n"
    "ds_read_b64_tr_b16 a[124:125], v3 offset:34816\n"
    "ds_read_b64_tr_b16 a[126:127], v3 offset:34944\n"
    "ds_read_b64_tr_b16 a[204:205], v3 offset:35840\n"
    "ds_read_b64_tr_b16 a[206:207], v3 offset:35968\n"
    "s_waitcnt lgkmcnt(0)\n"
    "s_waitcnt vmcnt(21)\n"
    "ds_read_b64_tr_b16 a[48:49], v3 offset:36864\n"
    "ds_read_b64_tr_b16 a[50:51], v3 offset:36992\n"
    "ds_read_b64_tr_b16 a[128:129], v3 offset:37888\n"
    "ds_read_b64_tr_b16 a[130:131], v3 offset:38016\n"
    "ds_read_b64_tr_b16 a[208:209], v3 offset:38912\n"
    "ds_read_b64_tr_b16 a[210:211], v3 offset:39040\n"
    "s_waitcnt lgkmcnt(0)\n"
    "s_waitcnt vmcnt(18)\n"
    "ds_read_b64_tr_b16 a[52:53], v3 offset:0\n"
    "ds_read_b64_tr_b16 a[54:55], v3 offset:128\n"
    "ds_read_b64_tr_b16 a[132:133], v3 offset:1024\n"
    "ds_read_b64_tr_b16 a[134:135], v3 offset:1152\n"
    "ds_read_b64_tr_b16 a[212:213], v3 offset:2048\n"
    "ds_read_b64_tr_b16 a[214:215], v3 offset:2176\n"
    "s_waitcnt lgkmcnt(0)\n"
    "s_waitcnt vmcnt(15)\n"
    "ds_read_b64_tr_b16 a[56:57], v3 offset:3072\n"
    "ds_read_b64_tr_b16 a[58:59], v3 offset:3200\n"
    "ds_read_b64_tr_b16 a[136:137], v3 offset:4096\n"
    "ds_read_b64_tr_b16 a[138:139], v3 offset:4224\n"
    "ds_read_b64_tr_b16 a[216:217], v3 offset:5120\n"
    "ds_read_b64_tr_b16 a[218:219], v3 offset:5248\n"
    "s_waitcnt lgkmcnt(0)\n"
    "s_waitcnt vmcnt(12)\n"
    "ds_read_b64_tr_b16 a[60:61], v3 offset:6144\n"
    "ds_read_b64_tr_b16 a[62:63], v3 offset:6272\n"
    "ds_read_b64_tr_b16 a[140:141], v3 offset:7168\n"
    "ds_read_b64_tr_b16 a[142:143], v3 offset:7296\n"
    "ds_read_b64_tr_b16 a[220:221], v3 offset:8192\n"
    "ds_read_b64_tr_b16 a[222:223], v3 offset:8320\n"
    "s_waitcnt lgkmcnt(0)\n"
    "s_waitcnt vmcnt(9)\n"
    "ds_read_b64_tr_b16 a[64:65], v3 offset:9216\n"
    "ds_read_b64_tr_b16 a[66:67], v3 offset:9344\n"
    "ds_read_b64_tr_b16 a[144:145], v3 offset:10240\n"
    "ds_read_b64_tr_b16 a[146:147], v3 offset:10368\n"
    "ds_read_b64_tr_b16 a[224:225], v3 offset:11264\n"
    "ds_read_b64_tr_b16 a[226:227], v3 offset:11392\n"
    "s_waitcnt lgkmcnt(0)\n"
    "s_waitcnt vmcnt(6)\n"
    "ds_read_b64_tr_b16 a[68:69], v3 offset:12288\n"
    "ds_read_b64_tr_b16 a[70:71], v3 offset:12416\n"
    "ds_read_b64_tr_b16 a[148:149], v3 offset:13312\n"
    "ds_read_b64_tr_b16 a[150:151], v3 offset:13440\n"
    "ds_read_b64_tr_b16 a[228:229], v3 offset:14336\n"
    "ds_read_b64_tr_b16 a[230:231], v3 offset:14464\n"
    "s_waitcnt lgkmcnt(0)\n"
    "s_waitcnt vmcnt(3)\n"
    "ds_read_b64_tr_b16 a[72:73], v3 offset:15360\n"
    "ds_read_b64_tr_b16 a[74:75], v3 offset:15488\n"
    "ds_read_b64_tr_b16 a[152:153], v3 offset:16384\n"
    "ds_read_b64_tr_b16 a[154:155], v3 offset:16512\n"
    "ds_read_b64_tr_b16 a[232:233], v3 offset:17408\n"
    "ds_read_b64_tr_b16 a[234:235], v3 offset:17536\n"
    "s_waitcnt lgkmcnt(0)\n"
    "s_waitcnt vmcnt(0)\n"
    "ds_read_b64_tr_b16 a[76:77], v3 offset:18432\n"
    "ds_read_b64_tr_b16 a[78:79], v3 offset:18560\n"
    "ds_read_b64_tr_b16 a[156:157], v3 offset:19456\n"
    "ds_read_b64_tr_b16 a[158:159], v3 offset:19584\n"
    "ds_read_b64_tr_b16 a[236:237], v3 offset:20480\n"
    "ds_read_b64_tr_b16 a[238:239], v3 offset:20608\n"
    "s_waitcnt lgkmcnt(0)\n"
    "s_waitcnt lgkmcnt(0)\n"
    "s_barrier\n"
    "v_mov_b32 v32, 0x3f803f80\n"
    "v_mov_b32 v36, 0\n"
    "v_mov_b32 v33, 0x3f803f80\n"
    "v_mov_b32 v37, 0\n"
    "v_mov_b32 v34, 0x3f803f80\n"
    "v_mov_b32 v38, 0\n"
    "v_mov_b32 v35, 0x3f803f80\n"
    "v_mov_b32 v39, 0\n"
    "s_nop 0\n"
    "v_mfma_f32_16x16x32_bf16 v[96:99], a[0:3], v[32:35], 0\n"
    "v_mfma_f32_16x16x32_bf16 v[100:103], a[80:83], v[32:35], 0\n"
    "v_mfma_f32_16x16x32_bf16 v[104:107], a[160:163], v[32:35], 0\n"
    "v_mfma_f32_16x16x32_bf16 v[96:99], a[4:7], v[32:35], v[96:99]\n"
    "v_mfma_f32_16x16x32_bf16 v[100:103], a[84:87], v[32:35], v[100:103]\n"
    "v_mfma_f32_16x16x32_bf16 v[104:107], a[164:167], v[32:35], v[104:107]\n"
    "v_mfma_f32_16x16x32_bf16 v[96:99], a[8:11], v[32:35], v[96:99]\n"
    "v_mfma_f32_16x16x32_bf16 v[100:103], a[88:91], v[32:35], v[100:103]\n"
    "v_mfma_f32_16x16x32_bf16 v[104:107], a[168:171], v[32:35], v[104:107]\n"
    "v_mfma_f32_16x16x32_bf16 v[96:99], a[12:15], v[32:35], v[96:99]\n"
    "v_mfma_f32_16x16x32_bf16 v[100:103], a[92:95], v[32:35], v[100:103]\n"
    "v_mfma_f32_16x16x32_bf16 v[104:107], a[172:175], v[32:35], v[104:107]\n"
    "v_mfma_f32_16x16x32_bf16 v[96:99], a[16:19], v[32:35], v[96:99]\n"
    "v_mfma_f32_16x16x32_bf16 v[100:103], a[96:99], v[32:35], v[100:103]\n"
    "v_mfma_f32_16x16x32_bf16 v[104:107], a[176:179], v[32:35], v[104:107]\n"
    "v_mfma_f32_16x16x32_bf16 v[96:99], a[20:23], v[32:35], v[96:99]\n"
    "v_mfma_f32_16x16x32_bf16 v[100:103], a[100:103], v[32:35], v[100:103]\n"
    "v_mfma_f32_16x16x32_bf16 v[104:107], a[180:183], v[32:35], v[104:107]\n"
    "v_mfma_f32_16x16x32_bf16 v[96:99], a[24:27], v[32:35], v[96:99]\n"
    "v_mfma_f32_16x16x32_bf16 v[100:103], a[104:107], v[32:35], v[100:103]\n"
    "v_mfma_f32_16x16x32_bf16 v[104:107], a[184:187], v[32:35], v[104:107]\n"
    "v_mfma_f32_16x16x32_bf16 v[96:99], a[28:31], v[32:35], v[96:99]\n"
    "v_mfma_f32_16x16x32_bf16 v[100:103], a[108:111], v[32:35], v[100:103]\n"
    "v_mfma_f32_16x16x32_bf16 v[104:107], a[188:191], v[32:35], v[104:107]\n"
    "v_mfma_f32_16x16x32_bf16 v[96:99], a[32:35], v[32:35], v[96:99]\n"
    "v_mfma_f32_16x16x32_bf16 v[100:103], a[112:115], v[32:35], v[100:103]\n"
    "v_mfma_f32_16x16x32_bf16 v[104:107], a[192:195], v[32:35], v[104:107]\n"
    "v_mfma_f32_16x16x32_bf16 v[96:99], a[36:39], v[32:35], v[96:99]\n"
    "v_mfma_f32_16x16x32_bf16 v[100:103], a[116:119], v[32:35], v[100:103]\n"
    "v_mfma_f32_16x16x32_bf16 v[104:107], a[196:199], v[32:35], v[104:107]\n"
    "v_mfma_f32_16x16x32_bf16 v[96:99], a[40:43], v[32:35], v[96:99]\n"
    "v_mfma_f32_16x16x32_bf16 v[100:103], a[120:123], v[32:35], v[100:103]\n"
    "v_mfma_f32_16x16x32_bf16 v[104:107], a[200:203], v[32:35], v[104:107]\n"
    "v_mfma_f32_16x16x32_bf16 v[96:99], a[44:47], v[32:35], v[96:99]\n"
    "v_mfma_f32_16x16x32_bf16 v[100:103], a[124:127], v[32:35], v[100:103]\n"
    "v_mfma_f32_16x16x32_bf16 v[104:107], a[204:207], v[32:35], v[104:107]\n"
    "v_mfma_f32_16x16x32_bf16 v[96:99], a[48:51], v[32:35], v[96:99]\n"
    "v_mfma_f32_16x16x32_bf16 v[100:103], a[128:131], v[32:35], v[100:103]\n"
    "v_mfma_f32_16x16x32_bf16 v[104:107], a[208:211], v[32:35], v[104:107]\n"
    "v_mfma_f32_16x16x32_bf16 v[96:99], a[52:55], v[32:35], v[96:99]\n"
    "v_mfma_f32_16x16x32_bf16 v[100:103], a[132:135], v[32:35], v[100:103]\n"
    "v_mfma_f32_16x16x32_bf16 v[104:107], a[212:215], v[32:35], v[104:107]\n"
    "v_mfma_f32_16x16x32_bf16 v[96:99], a[56:59], v[32:35], v[96:99]\n"
    "v_mfma_f32_16x16x32_bf16 v[100:103], a[136:139], v[32:35], v[100:103]\n"
    "v_mfma_f32_16x16x32_bf16 v[104:107], a[216:219], v[32:35], v[104:107]\n"
    "v_mfma_f32_16x16x32_bf16 v[96:99], a[60:63], v[32:35], v[96:99]\n"
    "v_mfma_f32_16x16x32_bf16 v[100:103], a[140:143], v[32:35], v[100:103]\n"
    "v_mfma_f32_16x16x32_bf16 v[104:107], a[220:223], v[32:35], v[104:107]\n"
    "v_mfma_f32_16x16x32_bf16 v[96:99], a[64:67], v[32:35], v[96:99]\n"
    "v_mfma_f32_16x16x32_bf16 v[100:103], a[144:147], v[32:35], v[100:103]\n"
    "v_mfma_f32_16x16x32_bf16 v[104:107], a[224:227], v[32:35], v[104:107]\n"
    "v_mfma_f32_16x16x32_bf16 v[96:99], a[68:71], v[32:35], v[96:99]\n"
    "v_mfma_f32_16x16x32_bf16 v[100:103], a[148:151], v[32:35], v[100:103]\n"
    "v_mfma_f32_16x16x32_bf16 v[104:107], a[228:231], v[32:35], v[104:107]\n"
    "v_mfma_f32_16x16x32_bf16 v[96:99], a[72:75], v[32:35], v[96:99]\n"
    "v_mfma_f32_16x16x32_bf16 v[100:103], a[152:155], v[32:35], v[100:103]\n"
    "v_mfma_f32_16x16x32_bf16 v[104:107], a[232:235], v[32:35], v[104:107]\n"
    "v_mfma_f32_16x16x32_bf16 v[96:99], a[76:79], v[32:35], v[96:99]\n"
    "v_mfma_f32_16x16x32_bf16 v[100:103], a[156:159], v[32:35], v[100:103]\n"
    "v_mfma_f32_16x16x32_bf16 v[104:107], a[236:239], v[32:35], v[104:107]\n"
    "s_lshl_b32 s35, s18, 13\n"
    "v_add_u32 v8, s35, v228\n"
    "ds_write_b128 v8, v[36:39] offset:40960\n"
    "ds_write_b128 v8, v[36:39] offset:41984\n"
    "ds_write_b128 v8, v[36:39] offset:43008\n"
    "ds_write_b128 v8, v[36:39] offset:44032\n"
    "ds_write_b128 v8, v[36:39] offset:45056\n"
    "ds_write_b128 v8, v[36:39] offset:46080\n"
    "ds_write_b128 v8, v[36:39] offset:47104\n"
    "ds_write_b128 v8, v[36:39] offset:48128\n"
    "s_nop 7\n"
    "s_nop 7\n"
    "s_mov_b64 exec, s[16:17]\n"
    "v_accvgpr_write_b32 a0, v32\n"
    "v_accvgpr_write_b32 a1, v32\n"
    "v_accvgpr_write_b32 a2, v32\n"
    "v_accvgpr_write_b32 a3, v32\n"
    "v_accvgpr_write_b32 a4, v32\n"
    "v_accvgpr_write_b32 a5, v32\n"
    "v_accvgpr_write_b32 a6, v32\n"
    "v_accvgpr_write_b32 a7, v32\n"
    "v_accvgpr_write_b32 a8, v32\n"
    "v_accvgpr_write_b32 a9, v32\n"
    "v_accvgpr_write_b32 a10, v32\n"
    "v_accvgpr_write_b32 a11, v32\n"
    "v_accvgpr_write_b32 a12, v32\n"
    "v_accvgpr_write_b32 a13, v32\n"
    "v_accvgpr_write_b32 a14, v32\n"
    "v_accvgpr_write_b32 a15, v32\n"
    "v_accvgpr_write_b32 a16, v32\n"
    "v_accvgpr_write_b32 a17, v32\n"
    "v_accvgpr_write_b32 a18, v32\n"
    "v_accvgpr_write_b32 a19, v32\n"
    "v_accvgpr_write_b32 a20, v32\n"
    "v_accvgpr_write_b32 a21, v32\n"
    "v_accvgpr_write_b32 a22, v32\n"
    "v_accvgpr_write_b32 a23, v32\n"
    "v_accvgpr_write_b32 a24, v32\n"
    "v_accvgpr_write_b32 a25, v32\n"
    "v_accvgpr_write_b32 a26, v32\n"
    "v_accvgpr_write_b32 a27, v32\n"
    "v_accvgpr_write_b32 a28, v32\n"
    "v_accvgpr_write_b32 a29, v32\n"
    "v_accvgpr_write_b32 a30, v32\n"
    "v_accvgpr_write_b32 a31, v32\n"
    "v_accvgpr_write_b32 a32, v32\n"
    "v_accvgpr_write_b32 a33, v32\n"
    "v_accvgpr_write_b32 a34, v32\n"
    "v_accvgpr_write_b32 a35, v32\n"
    "v_accvgpr_write_b32 a36, v32\n"
    "v_accvgpr_write_b32 a37, v32\n"
    "v_accvgpr_write_b32 a38, v32\n"
    "v_accvgpr_write_b32 a39, v32\n"
    "v_accvgpr_write_b32 a40, v32\n"
    "v_accvgpr_write_b32 a41, v32\n"
    "v_accvgpr_write_b32 a42, v32\n"
    "v_accvgpr_write_b32 a43, v32\n"
    "v_accvgpr_write_b32 a44, v32\n"
    "v_accvgpr_write_b32 a45, v32\n"
    "v_accvgpr_write_b32 a46, v32\n"
    "v_accvgpr_write_b32 a47, v32\n"
    "v_accvgpr_write_b32 a48, v32\n"
    "v_accvgpr_write_b32 a49, v32\n"
    "v_accvgpr_write_b32 a50, v32\n"
    "v_accvgpr_write_b32 a51, v32\n"
    "v_accvgpr_write_b32 a52, v32\n"
    "v_accvgpr_write_b32 a53, v32\n"
    "v_accvgpr_write_b32 a54, v32\n"
    "v_accvgpr_write_b32 a55, v32\n"
    "v_accvgpr_write_b32 a56, v32\n"
    "v_accvgpr_write_b32 a57, v32\n"
    "v_accvgpr_write_b32 a58, v32\n"
    "v_accvgpr_write_b32 a59, v32\n"
    "v_accvgpr_write_b32 a60, v32\n"
    "v_accvgpr_write_b32 a61, v32\n"
    "v_accvgpr_write_b32 a62, v32\n"
    "v_accvgpr_write_b32 a63, v32\n"
    "v_accvgpr_write_b32 a64, v32\n"
    "v_accvgpr_write_b32 a65, v32\n"
    "v_accvgpr_write_b32 a66, v32\n"
    "v_accvgpr_write_b32 a67, v32\n"
    "v_accvgpr_write_b32 a68, v32\n"
    "v_accvgpr_write_b32 a69, v32\n"
    "v_accvgpr_write_b32 a70, v32\n"
    "v_accvgpr_write_b32 a71, v32\n"
    "v_accvgpr_write_b32 a72, v32\n"
    "v_accvgpr_write_b32 a73, v32\n"
    "v_accvgpr_write_b32 a74, v32\n"
    "v_accvgpr_write_b32 a75, v32\n"
    "v_accvgpr_write_b32 a76, v32\n"
    "v_accvgpr_write_b32 a77, v32\n"
    "v_accvgpr_write_b32 a78, v32\n"
    "v_accvgpr_write_b32 a79, v32\n"
    "s_mov_b64 exec, -1\n"
    "s_mov_b32 s30, -2\n"
    "s_mov_b32 s30, 0\n"
    "s_mov_b32 s31, 1\n"
    "s_lshl_b32 s34, s18, 11\n"
    "s_add_u32 s32, s20, s19\n"
    "s_mov_b32 s37, 0x3ea52b5c\n"
    "s_mov_b64 s[38:39], 0\n"
    "s_mov_b64 s[40:41], 0\n"
    "s_mov_b32 s36, 0\n"
    "v_mov_b32 v108, 0\n"
    "s_mov_b32 s42, 0xaaaaaaaa\n"
    "s_mov_b32 s43, 0xaaaaaaaa\n"
    "s_waitcnt lgkmcnt(0)\n"
    ".Lb1_top_%=:\n"
    "s_cmp_lg_u32 s18, 0\n"
    "s_cbranch_scc1 .Lb1_pd7_%=\n"
    "s_add_u32 s29, s26, 248\n"
    "v_min3_i32 v108, v108, v109, v110\n"
    "v_min_i32 v108, v108, v111\n"
    "v_cmp_gt_i32 vcc, s31, v108\n"
    "s_nop 0\n"
    "s_cbranch_vccz .Lb1_pd7_%=\n"
    ".Lb1_rt8_%=:\n"
    "s_memrealtime s[26:27]\n"
    "s_waitcnt lgkmcnt(0)\n"
    "s_sub_u32 s28, s36, s26\n"
    "s_cmp_lt_u32 s28, 600\n"
    "s_cbranch_scc1 .Lb1_rt8_%=\n"
    ".Lb1_po6_%=:\n"
    "buffer_load_dwordx4 v[108:111], v229, s[0:3], s21 offen sc1\n"
    "s_memrealtime s[26:27]\n"
    "s_waitcnt vmcnt(0) lgkmcnt(0)\n"
    "s_add_u32 s29, s26, 248\n"
    "v_min3_i32 v108, v108, v109, v110\n"
    "v_min_i32 v108, v108, v111\n"
    "v_cmp_gt_i32 vcc, s31, v108\n"
    "s_nop 0\n"
    "s_cbranch_vccz .Lb1_pd7_%=\n"
    "s_add_u32 s24, s24, 1\n"
    "s_cmp_lt_u32 s24, 0x40000\n"
    "s_cbranch_scc1 .Lb1_sp9_%=\n"
    "v_mov_b32 v108, 60\n"
    "s_mov_b32 s28, 33728\n"
    "buffer_store_dword v108, off, s[0:3], s28 sc1\n"
    "s_waitcnt vmcnt(0)\n"
    "s_branch .Lb1_pd7_%=\n"
    ".Lb1_sp9_%=:\n"
    "s_sleep 6\n"
    "s_branch .Lb1_po6_%=\n"
    ".Lb1_pd7_%=:\n"
    "s_barrier\n"
    "s_cmp_eq_u32 s31, 63\n"
    "s_cselect_b32 s36, s26, s29\n"
    ".Lb1_mk0x9_%=:\n"
    "s_mov_b32 s33, s32\n"
    "s_add_u32 m0, s34, 0\n"
    "s_nop 0\n"
    "buffer_load_dwordx4 v228, s[0:3], s32 offen sc1 lds\n"
    "buffer_load_dwordx4 v228, s[0:3], s32 offen offset:1024 sc1 lds\n"
    "s_add_u32 m0, s34, 8192\n"
    "s_add_u32 s32, s32, 0x4000\n"
    "buffer_load_dwordx4 v228, s[0:3], s32 offen sc1 lds\n"
    "buffer_load_dwordx4 v228, s[0:3], s32 offen offset:1024 sc1 lds\n"
    "s_add_u32 m0, s34, 16384\n"
    "s_add_u32 s32, s32, 0x4000\n"
    "buffer_load_dwordx4 v228, s[0:3], s32 offen sc1 lds\n"
    "buffer_load_dwordx4 v228, s[0:3], s32 offen offset:1024 sc1 lds\n"
    "s_add_u32 m0, s34, 24576\n"
    "s_add_u32 s32, s32, 0x4000\n"
    "buffer_load_dwordx4 v228, s[0:3], s32 offen sc1 lds\n"
    "buffer_load_dwordx4 v228, s[0:3], s32 offen offset:1024 sc1 lds\n"
    "s_add_u32 m0, s34, 32768\n"
    "s_add_u32 s32, s32, 0x4000\n"
    "buffer_load_dwordx4 v228, s[0:3], s32 offen sc1 lds\n"
    "buffer_load_dwordx4 v228, s[0:3], s32 offen offset:1024 sc1 lds\n"
    "s_waitcnt vmcnt(8)\n"
    "s_barrier\n"
    ".Lb1_mk1x10_%=:\n"
    "ds_read_b128 v[32:35], v224 offset:0\n"
    "ds_read_b128 v[36:39], v224 offset:1024\n"
    "ds_read_b128 v[40:43], v225 offset:0\n"
    "ds_read_b128 v[44:47], v225 offset:1024\n"
    "s_waitcnt lgkmcnt(2)\n"
    "v_mfma_f32_16x16x32_bf16 v[24:27], v[32:35], v[32:35], 0\n"
    "ds_read_b128 v[48:51], v226 offset:0\n"
    "ds_read_b128 v[52:55], v226 offset:1024\n"
    "v_mfma_f32_16x16x32_bf16 v[28:31], v[36:39], v[36:39], 0\n"
    "v_mfma_f32_16x16x32_bf16 v[0:3], a[0:3], v[32:35], 0\n"
    "v_mfma_f32_16x16x32_bf16 v[4:7], a[0:3], v[36:39], 0\n"
    "v_mfma_f32_16x16x32_bf16 v[8:11], a[80:83], v[32:35], 0\n"
    "s_add_u32 s35, s30, 1\n"
    "s_lshl_b32 s32, s35, 19\n"
    "s_lshl_b32 s29, s35, 11\n"
    "v_mfma_f32_16x16x32_bf16 v[12:15], a[80:83], v[36:39], 0\n"
    "s_xor_b32 s29, s29, s19\n"
    "s_and_b32 s29, s29, 0x2000\n"
    "s_add_u32 s32, s32, s29\n"
    "v_mfma_f32_16x16x32_bf16 v[16:19], a[160:163], v[32:35], 0\n"
    "s_add_u32 s32, s32, s20\n"
    "v_xor_b32 v232, 0x800, v232\n"
    "v_xor_b32 v233, 0x800, v233\n"
    "v_mfma_f32_16x16x32_bf16 v[20:23], a[160:163], v[36:39], 0\n"
    "s_waitcnt lgkmcnt(2)\n"
    "v_xor_b32 v235, 0x800, v235\n"
    "v_xor_b32 v236, 0x800, v236\n"
    "v_mfma_f32_16x16x32_bf16 v[0:3], a[4:7], v[40:43], v[0:3]\n"
    "ds_read_b128 v[56:59], v227 offset:0\n"
    "ds_read_b128 v[60:63], v227 offset:1024\n"
    "v_mfma_f32_16x16x32_bf16 v[4:7], a[4:7], v[44:47], v[4:7]\n"
    "v_xor_b32 v237, 0x800, v237\n"
    "v_xor_b32 v238, 0x800, v238\n"
    "v_mfma_f32_16x16x32_bf16 v[8:11], a[84:87], v[40:43], v[8:11]\n"
    "v_xor_b32 v239, 0x800, v239\n"
    "v_xor_b32 v240, 0x800, v240\n"
    "v_mfma_f32_16x16x32_bf16 v[12:15], a[84:87], v[44:47], v[12:15]\n"
    "v_mov_b32 v116, s31\n"
    "v_mov_b32 v117, s31\n"
    "v_mfma_f32_16x16x32_bf16 v[16:19], a[164:167], v[40:43], v[16:19]\n"
    "v_mov_b32 v118, s31\n"
    "v_mov_b32 v119, s31\n"
    "v_mfma_f32_16x16x32_bf16 v[20:23], a[164:167], v[44:47], v[20:23]\n"
    "s_waitcnt lgkmcnt(2)\n"
    "v_mfma_f32_16x16x32_bf16 v[0:3], a[8:11], v[48:51], v[0:3]\n"
    "s_waitcnt vmcnt(6)\n"
    "s_barrier\n"
    "ds_read_b128 v[64:67], v224 offset:8192\n"
    "v_mfma_f32_16x16x32_bf16 v[4:7], a[8:11], v[52:55], v[4:7]\n"
    "ds_read_b128 v[68:71], v224 offset:9216\n"
    "v_mfma_f32_16x16x32_bf16 v[8:11], a[88:91], v[48:51], v[8:11]\n"
    "v_mfma_f32_16x16x32_bf16 v[12:15], a[88:91], v[52:55], v[12:15]\n"
    "v_mfma_f32_16x16x32_bf16 v[16:19], a[168:171], v[48:51], v[16:19]\n"
    "v_mfma_f32_16x16x32_bf16 v[20:23], a[168:171], v[52:55], v[20:23]\n"
    "s_waitcnt lgkmcnt(2)\n"
    "v_mfma_f32_16x16x32_bf16 v[0:3], a[12:15], v[56:59], v[0:3]\n"
    "ds_read_b128 v[72:75], v225 offset:8192\n"
    "ds_read_b128 v[76:79], v225 offset:9216\n"
    "v_mfma_f32_16x16x32_bf16 v[4:7], a[12:15], v[60:63], v[4:7]\n"
    "v_mfma_f32_16x16x32_bf16 v[8:11], a[92:95], v[56:59], v[8:11]\n"
    "v_mfma_f32_16x16x32_bf16 v[12:15], a[92:95], v[60:63], v[12:15]\n"
    "v_mfma_f32_16x16x32_bf16 v[16:19], a[172:175], v[56:59], v[16:19]\n"
    "v_mfma_f32_16x16x32_bf16 v[20:23], a[172:175], v[60:63], v[20:23]\n"
    "s_waitcnt lgkmcnt(2)\n"
    "v_mfma_f32_16x16x32_bf16 v[24:27], v[64:67], v[64:67], v[24:27]\n"
    "ds_read_b128 v[80:83], v226 offset:8192\n"
    "ds_read_b128 v[84:87], v226 offset:9216\n"
    "v_mfma_f32_16x16x32_bf16 v[28:31], v[68:71], v[68:71], v[28:31]\n"
    "v_mfma_f32_16x16x32_bf16 v[0:3], a[16:19], v[64:67], v[0:3]\n"
    "v_mfma_f32_16x16x32_bf16 v[4:7], a[16:19], v[68:71], v[4:7]\n"
    "v_mfma_f32_16x16x32_bf16 v[8:11], a[96:99], v[64:67], v[8:11]\n"
    "v_mfma_f32_16x16x32_bf16 v[12:15], a[96:99], v[68:71], v[12:15]\n"
    "v_mfma_f32_16x16x32_bf16 v[16:19], a[176:179], v[64:67], v[16:19]\n"
    "v_mfma_f32_16x16x32_bf16 v[20:23], a[176:179], v[68:71], v[20:23]\n"
    "s_waitcnt lgkmcnt(2)\n"
    "v_mfma_f32_16x16x32_bf16 v[0:3], a[20:23], v[72:75], v[0:3]\n"
    "ds_read_b128 v[88:91], v227 offset:8192\n"
    "ds_read_b128 v[92:95], v227 offset:9216\n"
    "v_mfma_f32_16x16x32_bf16 v[4:7], a[20:23], v[76:79], v[4:7]\n"
    "v_mfma_f32_16x16x32_bf16 v[8:11], a[100:103], v[72:75], v[8:11]\n"
    "v_mfma_f32_16x16x32_bf16 v[12:15], a[100:103], v[76:79], v[12:15]\n"
    "v_mfma_f32_16x16x32_bf16 v[16:19], a[180:183], v[72:75], v[16:19]\n"
    "v_mfma_f32_16x16x32_bf16 v[20:23], a[180:183], v[76:79], v[20:23]\n"
    "s_waitcnt lgkmcnt(2)\n"
    "v_mfma_f32_16x16x32_bf16 v[0:3], a[24:27], v[80:83], v[0:3]\n"
    "s_waitcnt vmcnt(4)\n"
    "s_barrier\n"
    "ds_read_b128 v[32:35], v224 offset:16384\n"
    "v_mfma_f32_16x16x32_bf16 v[4:7], a[24:27], v[84:87], v[4:7]\n"
    "ds_read_b128 v[36:39], v224 offset:17408\n"
    "v_mfma_f32_16x16x32_bf16 v[8:11], a[104:107], v[80:83], v[8:11]\n"
    "v_mfma_f32_16x16x32_bf16 v[12:15], a[104:107], v[84:87], v[12:15]\n"
    "v_mfma_f32_16x16x32_bf16 v[16:19], a[184:187], v[80:83], v[16:19]\n"
    "v_mfma_f32_16x16x32_bf16 v[20:23], a[184:187], v[84:87], v[20:23]\n"
    "s_waitcnt lgkmcnt(2)\n"
    "v_mfma_f32_16x16x32_bf16 v[0:3], a[28:31], v[88:91], v[0:3]\n"
    "ds_read_b128 v[40:43], v225 offset:16384\n"
    "ds_read_b128 v[44:47], v225 offset:17408\n"
    "v_mfma_f32_16x16x32_bf16 v[4:7], a[28:31], v[92:95], v[4:7]\n"
    "v_mfma_f32_16x16x32_bf16 v[8:11], a[108:111], v[88:91], v[8:11]\n"
    "v_mfma_f32_16x16x32_bf16 v[12:15], a[108:111], v[92:95], v[12:15]\n"
    "v_mfma_f32_16x16x32_bf16 v[16:19], a[188:191], v[88:91], v[16:19]\n"
    "v_mfma_f32_16x16x32_bf16 v[20:23], a[188:191], v[92:95], v[20:23]\n"
    "s_waitcnt lgkmcnt(2)\n"
    "v_mfma_f32_16x16x32_bf16 v[24:27], v[32:35], v[32:35], v[24:27]\n"
    "ds_read_b128 v[48:51], v226 offset:16384\n"
    "ds_read_b128 v[52:55], v226 offset:17408\n"
    "v_mfma_f32_16x16x32_bf16 v[28:31], v[36:39], v[36:39], v[28:31]\n"
    "v_mfma_f32_16x16x32_bf16 v[0:3], a[32:35], v[32:35], v[0:3]\n"
    "v_mfma_f32_16x16x32_bf16 v[4:7], a[32:35], v[36:39], v[4:7]\n"
    "v_mfma_f32_16x16x32_bf16 v[8:11], a[112:115], v[32:35], v[8:11]\n"
    "v_mfma_f32_16x16x32_bf16 v[12:15], a[112:115], v[36:39], v[12:15]\n"
    "v_mfma_f32_16x16x32_bf16 v[16:19], a[192:195], v[32:35], v[16:19]\n"
    "v_mfma_f32_16x16x32_bf16 v[20:23], a[192:195], v[36:39], v[20:23]\n"
    "s_waitcnt lgkmcnt(2)\n"
    "v_mfma_f32_16x16x32_bf16 v[0:3], a[36:39], v[40:43], v[0:3]\n"
    "ds_read_b128 v[56:59], v227 offset:16384\n"
    "ds_read_b128 v[60:63], v227 offset:17408\n"
    "v_mfma_f32_16x16x32_bf16 v[4:7], a[36:39], v[44:47], v[4:7]\n"
    "v_mfma_f32_16x16x32_bf16 v[8:11], a[116:119], v[40:43], v[8:11]\n"
    "v_mfma_f32_16x16x32_bf16 v[12:15], a[116:119], v[44:47], v[12:15]\n"
    "v_mfma_f32_16x16x32_bf16 v[16:19], a[196:199], v[40:43], v[16:19]\n"
    "v_mfma_f32_16x16x32_bf16 v[20:23], a[196:199], v[44:47], v[20:23]\n"
    "s_waitcnt lgkmcnt(2)\n"
    "v_mfma_f32_16x16x32_bf16 v[0:3], a[40:43], v[48:51], v[0:3]\n"
    "s_waitcnt vmcnt(2)\n"
    "s_barrier\n"
    "ds_read_b128 v[64:67], v224 offset:24576\n"
    "v_mfma_f32_16x16x32_bf16 v[4:7], a[40:43], v[52:55], v[4:7]\n"
    "ds_read_b128 v[68:71], v224 offset:25600\n"
    "ds_read_b128 v[72:75], v225 offset:24576\n"
    "v_mfma_f32_16x16x32_bf16 v[8:11], a[120:123], v[48:51], v[8:11]\n"
    "v_mfma_f32_16x16x32_bf16 v[12:15], a[120:123], v[52:55], v[12:15]\n"
    "ds_read_b128 v[76:79], v225 offset:25600\n"
    "v_mfma_f32_16x16x32_bf16 v[16:19], a[200:203], v[48:51], v[16:19]\n"
    "ds_read_b128 v[80:83], v226 offset:24576\n"
    "ds_read_b128 v[84:87], v226 offset:25600\n"
    "v_mfma_f32_16x16x32_bf16 v[20:23], a[200:203], v[52:55], v[20:23]\n"
    "s_waitcnt lgkmcnt(6)\n"
    "v_mfma_f32_16x16x32_bf16 v[0:3], a[44:47], v[56:59], v[0:3]\n"
    "ds_read_b128 v[88:91], v227 offset:24576\n"
    "v_mfma_f32_16x16x32_bf16 v[4:7], a[44:47], v[60:63], v[4:7]\n"
    "ds_read_b128 v[92:95], v227 offset:25600\n"
    "s_waitcnt vmcnt(0)\n"
    "v_mfma_f32_16x16x32_bf16 v[8:11], a[124:127], v[56:59], v[8:11]\n"
    "s_barrier\n"
    "ds_read_b128 v[32:35], v224 offset:32768\n"
    "v_mfma_f32_16x16x32_bf16 v[12:15], a[124:127], v[60:63], v[12:15]\n"
    "ds_read_b128 v[36:39], v224 offset:33792\n"
    "v_mfma_f32_16x16x32_bf16 v[16:19], a[204:207], v[56:59], v[16:19]\n"
    "ds_read_b128 v[40:43], v225 offset:32768\n"
    "v_mfma_f32_16x16x32_bf16 v[20:23], a[204:207], v[60:63], v[20:23]\n"
    "s_waitcnt lgkmcnt(9)\n"
    "ds_read_b128 v[44:47], v225 offset:33792\n"
    "v_mfma_f32_16x16x32_bf16 v[24:27], v[64:67], v[64:67], v[24:27]\n"
    "ds_read_b128 v[48:51], v226 offset:32768\n"
    "v_mfma_f32_16x16x32_bf16 v[28:31], v[68:71], v[68:71], v[28:31]\n"
    "ds_read_b128 v[52:55], v226 offset:33792\n"
    "v_mfma_f32_16x16x32_bf16 v[0:3], a[48:51], v[64:67], v[0:3]\n"
    "ds_read_b128 v[56:59], v227 offset:32768\n"
    "v_mfma_f32_16x16x32_bf16 v[4:7], a[48:51], v[68:71], v[4:7]\n"
    "s_waitcnt lgkmcnt(11)\n"
    "ds_read_b128 v[60:63], v227 offset:33792\n"
    "v_mfma_f32_16x16x32_bf16 v[0:3], a[52:55], v[72:75], v[0:3]\n"
    "v_mfma_f32_16x16x32_bf16 v[4:7], a[52:55], v[76:79], v[4:7]\n"
    "s_waitcnt lgkmcnt(10)\n"
    "v_mfma_f32_16x16x32_bf16 v[0:3], a[56:59], v[80:83], v[0:3]\n"
    "v_mfma_f32_16x16x32_bf16 v[4:7], a[56:59], v[84:87], v[4:7]\n"
    "s_waitcnt lgkmcnt(8)\n"
    "v_mfma_f32_16x16x32_bf16 v[0:3], a[60:63], v[88:91], v[0:3]\n"
    "v_mfma_f32_16x16x32_bf16 v[4:7], a[60:63], v[92:95], v[4:7]\n"
    "s_waitcnt lgkmcnt(6)\n"
    "v_mfma_f32_16x16x32_bf16 v[24:27], v[32:35], v[32:35], v[24:27]\n"
    "v_mfma_f32_16x16x32_bf16 v[28:31], v[36:39], v[36:39], v[28:31]\n"
    "v_mfma_f32_16x16x32_bf16 v[0:3], a[64:67], v[32:35], v[0:3]\n"
    "v_mfma_f32_16x16x32_bf16 v[4:7], a[64:67], v[36:39], v[4:7]\n"
    "s_waitcnt lgkmcnt(4)\n"
    "v_mfma_f32_16x16x32_bf16 v[0:3], a[68:71], v[40:43], v[0:3]\n"
    "v_mfma_f32_16x16x32_bf16 v[4:7], a[68:71], v[44:47], v[4:7]\n"
    "s_waitcnt lgkmcnt(2)\n"
    "v_mfma_f32_16x16x32_bf16 v[0:3], a[72:75], v[48:51], v[0:3]\n"
    "v_mfma_f32_16x16x32_bf16 v[4:7], a[72:75], v[52:55], v[4:7]\n"
    "s_waitcnt lgkmcnt(0)\n"
    "v_mfma_f32_16x16x32_bf16 v[0:3], a[76:79], v[56:59], v[0:3]\n"
    "v_mfma_f32_16x16x32_bf16 v[4:7], a[76:79], v[60:63], v[4:7]\n"
    "ds_write_b128 v241, v[24:27]\n"
    "v_mfma_f32_16x16x32_bf16 v[8:11], a[128:131], v[64:67], v[8:11]\n"
    "ds_write_b128 v241, v[28:31] offset:1024\n"
    "v_mfma_f32_16x16x32_bf16 v[12:15], a[128:131], v[68:71], v[12:15]\n"
    "ds_write2st64_b32 v243, v0, v4 offset1:1\n"
    "v_mfma_f32_16x16x32_bf16 v[8:11], a[132:135], v[72:75], v[8:11]\n"
    "s_waitcnt lgkmcnt(0)\n"
    "s_barrier\n"
    "ds_read2st64_b32 v[24:25], v242 offset1:8\n"
    "v_mfma_f32_16x16x32_bf16 v[12:15], a[132:135], v[76:79], v[12:15]\n"
    "ds_read2st64_b32 v[26:27], v242 offset0:16 offset1:24\n"
    "ds_read_b32 v108, v244\n"
    "v_mfma_f32_16x16x32_bf16 v[8:11], a[136:139], v[80:83], v[8:11]\n"
    "v_mfma_f32_16x16x32_bf16 v[12:15], a[136:139], v[84:87], v[12:15]\n"
    "v_mfma_f32_16x16x32_bf16 v[8:11], a[140:143], v[88:91], v[8:11]\n"
    "v_mfma_f32_16x16x32_bf16 v[12:15], a[140:143], v[92:95], v[12:15]\n"
    "v_mfma_f32_16x16x32_bf16 v[8:11], a[144:147], v[32:35], v[8:11]\n"
    "s_waitcnt lgkmcnt(0)\n"
    "v_add_f32 v24, v24, v26\n"
    "v_add_f32 v25, v25, v27\n"
    "v_mfma_f32_16x16x32_bf16 v[12:15], a[144:147], v[36:39], v[12:15]\n"
    "v_add_f32 v112, v24, v25\n"
    "v_mul_f32 v108, 0x3acccccd, v108\n"
    "v_mfma_f32_16x16x32_bf16 v[8:11], a[148:151], v[40:43], v[8:11]\n"
    "v_mul_f32 v112, 0x3acccccd, v112\n"
    "v_fma_f32 v112, -v108, v108, v112\n"
    "v_mfma_f32_16x16x32_bf16 v[12:15], a[148:151], v[44:47], v[12:15]\n"
    "v_max_f32 v112, 0, v112\n"
    "v_cvt_pk_bf16_f32 v108, v108, v112\n"
    "v_mfma_f32_16x16x32_bf16 v[8:11], a[152:155], v[48:51], v[8:11]\n"
    "v_and_b32 v112, 0xffff0000, v108\n"
    "v_lshlrev_b32 v108, 16, v108\n"
    "v_mfma_f32_16x16x32_bf16 v[12:15], a[152:155], v[52:55], v[12:15]\n"
    "v_add_f32 v112, 0x3727c5ac, v112\n"
    "v_mov_b32 v110, v108\n"
    "v_mfma_f32_16x16x32_bf16 v[8:11], a[156:159], v[56:59], v[8:11]\n"
    "v_cvt_pk_bf16_f32 v112, v112, v112\n"
    "v_lshlrev_b32 v112, 16, v112\n"
    "v_mfma_f32_16x16x32_bf16 v[12:15], a[156:159], v[60:63], v[12:15]\n"
    "v_sqrt_f32 v112, v112\n"
    "v_mfma_f32_16x16x32_bf16 v[16:19], a[208:211], v[64:67], v[16:19]\n"
    "v_permlane32_swap_b32 v108, v110\n"
    "v_mfma_f32_16x16x32_bf16 v[20:23], a[208:211], v[68:71], v[20:23]\n"
    "v_cvt_pk_bf16_f32 v112, v112, v112\n"
    "v_lshlrev_b32 v112, 16, v112\n"
    "v_mfma_f32_16x16x32_bf16 v[16:19], a[212:215], v[72:75], v[16:19]\n"
    "v_rcp_f32 v112, v112\n"
    "v_mfma_f32_16x16x32_bf16 v[20:23], a[212:215], v[76:79], v[20:23]\n"
    "v_mov_b32 v114, v112\n"
    "v_mfma_f32_16x16x32_bf16 v[16:19], a[216:219], v[80:83], v[16:19]\n"
    "s_nop 0\n"
    "v_permlane32_swap_b32 v112, v114\n"
    "v_mfma_f32_16x16x32_bf16 v[20:23], a[216:219], v[84:87], v[20:23]\n"
    "v_fma_f32 v0, v96, -v108, v0\n"
    "v_fma_f32 v1, v97, -v108, v1\n"
    "v_mfma_f32_16x16x32_bf16 v[16:19], a[220:223], v[88:91], v[16:19]\n"
    "v_fma_f32 v2, v98, -v108, v2\n"
    "v_fma_f32 v3, v99, -v108, v3\n"
    "v_mfma_f32_16x16x32_bf16 v[20:23], a[220:223], v[92:95], v[20:23]\n"
    "v_fma_f32 v4, v96, -v110, v4\n"
    "v_fma_f32 v5, v97, -v110, v5\n"
    "v_mfma_f32_16x16x32_bf16 v[16:19], a[224:227], v[32:35], v[16:19]\n"
    "v_fma_f32 v6, v98, -v110, v6\n"
    "v_fma_f32 v7, v99, -v110, v7\n"
    "v_mfma_f32_16x16x32_bf16 v[20:23], a[224:227], v[36:39], v[20:23]\n"
    "v_mul_f32 v0, v0, v112\n"
    "v_mul_f32 v1, v1, v112\n"
    "v_mfma_f32_16x16x32_bf16 v[16:19], a[228:231], v[40:43], v[16:19]\n"
    "v_mul_f32 v2, v2, v112\n"
    "v_mul_f32 v3, v3, v112\n"
    "v_mfma_f32_16x16x32_bf16 v[20:23], a[228:231], v[44:47], v[20:23]\n"
    "v_mul_f32 v4, v4, v114\n"
    "v_mul_f32 v5, v5, v114\n"
    "v_mfma_f32_16x16x32_bf16 v[16:19], a[232:235], v[48:51], v[16:19]\n"
    "v_mul_f32 v6, v6, v114\n"
    "v_mul_f32 v7, v7, v114\n"
    "v_mfma_f32_16x16x32_bf16 v[20:23], a[232:235], v[52:55], v[20:23]\n"
    "v_cvt_pk_bf16_f32 v0, v0, v1\n"
    "v_cvt_pk_bf16_f32 v1, v2, v3\n"
    "v_mfma_f32_16x16x32_bf16 v[16:19], a[236:239], v[56:59], v[16:19]\n"
    "ds_write_b64 v231, v[0:1] offset:0\n"
    "v_mfma_f32_16x16x32_bf16 v[20:23], a[236:239], v[60:63], v[20:23]\n"
    "v_cvt_pk_bf16_f32 v4, v4, v5\n"
    "v_cvt_pk_bf16_f32 v5, v6, v7\n"
    ".Lb1_mk2x11_%=:\n"
    "ds_write_b64 v231, v[4:5] offset:1024\n"
    "v_pk_fma_f32 v[8:9], v[100:101], v[108:109], v[8:9] op_sel_hi:[1,0,1] neg_lo:[0,1,0] neg_hi:[0,1,0]\n"
    "v_pk_fma_f32 v[10:11], v[102:103], v[108:109], v[10:11] op_sel_hi:[1,0,1] neg_lo:[0,1,0] neg_hi:[0,1,0]\n"
    "v_pk_fma_f32 v[12:13], v[100:101], v[110:111], v[12:13] op_sel_hi:[1,0,1] neg_lo:[0,1,0] neg_hi:[0,1,0]\n"
    "v_pk_fma_f32 v[14:15], v[102:103], v[110:111], v[14:15] op_sel_hi:[1,0,1] neg_lo:[0,1,0] neg_hi:[0,1,0]\n"
    "v_pk_mul_f32 v[8:9], v[8:9], v[112:113] op_sel_hi:[1,0]\n"
    "v_pk_mul_f32 v[10:11], v[10:11], v[112:113] op_sel_hi:[1,0]\n"
    "v_pk_mul_f32 v[12:13], v[12:13], v[114:115] op_sel_hi:[1,0]\n"
    "v_pk_mul_f32 v[14:15], v[14:15], v[114:115] op_sel_hi:[1,0]\n"
    "v_cvt_pk_bf16_f32 v8, v8, v9\n"
    "v_cvt_pk_bf16_f32 v9, v10, v11\n"
    "ds_write_b64 v232, v[8:9] offset:0\n"
    "v_cvt_pk_bf16_f32 v12, v12, v13\n"
    "v_cvt_pk_bf16_f32 v13, v14, v15\n"
    "ds_write_b64 v232, v[12:13] offset:1024\n"
    "v_pk_fma_f32 v[16:17], v[104:105], v[108:109], v[16:17] op_sel_hi:[1,0,1] neg_lo:[0,1,0] neg_hi:[0,1,0]\n"
    "v_pk_fma_f32 v[18:19], v[106:107], v[108:109], v[18:19] op_sel_hi:[1,0,1] neg_lo:[0,1,0] neg_hi:[0,1,0]\n"
    "v_pk_fma_f32 v[20:21], v[104:105], v[110:111], v[20:21] op_sel_hi:[1,0,1] neg_lo:[0,1,0] neg_hi:[0,1,0]\n"
    "v_pk_fma_f32 v[22:23], v[106:107], v[110:111], v[22:23] op_sel_hi:[1,0,1] neg_lo:[0,1,0] neg_hi:[0,1,0]\n"
    "v_pk_mul_f32 v[16:17], v[16:17], v[112:113] op_sel_hi:[1,0]\n"
    "v_pk_mul_f32 v[18:19], v[18:19], v[112:113] op_sel_hi:[1,0]\n"
    "v_pk_mul_f32 v[20:21], v[20:21], v[114:115] op_sel_hi:[1,0]\n"
    "v_pk_mul_f32 v[22:23], v[22:23], v[114:115] op_sel_hi:[1,0]\n"
    "v_cvt_pk_bf16_f32 v16, v16, v17\n"
    "v_cvt_pk_bf16_f32 v17, v18, v19\n"
    "ds_write_b64 v233, v[16:17] offset:0\n"
    "v_cvt_pk_bf16_f32 v20, v20, v21\n"
    "v_cvt_pk_bf16_f32 v21, v22, v23\n"
    "ds_write_b64 v233, v[20:21] offset:1024\n"
    "s_waitcnt lgkmcnt(0)\n"
    "s_barrier\n"
    ".Lb1_mk3x12_%=:\n"
    "ds_read_b128 v[32:35], v236 offset:1024\n"
    "ds_read_b128 v[44:47], v234 offset:0\n"
    "ds_read_b128 v[36:39], v235 offset:0\n"
    "ds_read_b128 v[48:51], v234 offset:1024\n"
    "ds_read_b128 v[40:43], v235 offset:1024\n"
    "ds_read_b64_tr_b16 v[52:53], v238 offset:1024\n"
    "ds_read_b64_tr_b16 v[54:55], v237 offset:0\n"
    "ds_read_b64_tr_b16 v[56:57], v237 offset:1024\n"
    "ds_read_b64_tr_b16 v[58:59], v240 offset:1024\n"
    "ds_read_b64_tr_b16 v[60:61], v239 offset:0\n"
    "ds_read_b64_tr_b16 v[62:63], v239 offset:1024\n"
    "s_waitcnt lgkmcnt(9)\n"
    "v_mfma_f32_16x16x32_bf16 v[64:67], v[32:35], v[44:47], 0\n"
    "s_waitcnt lgkmcnt(8)\n"
    "v_mfma_f32_16x16x32_bf16 v[68:71], v[36:39], v[44:47], 0\n"
    "s_waitcnt lgkmcnt(7)\n"
    "v_mfma_f32_16x16x32_bf16 v[72:75], v[36:39], v[48:51], 0\n"
    "s_waitcnt lgkmcnt(6)\n"
    "v_mfma_f32_16x16x32_bf16 v[76:79], v[40:43], v[48:51], 0\n"
    "v_cndmask_b32 v64, v64, v65, s[42:43]\n"
    "v_cndmask_b32 v65, v66, v67, s[42:43]\n"
    "v_cndmask_b32 v66, v68, v69, s[42:43]\n"
    "v_cndmask_b32 v67, v70, v71, s[42:43]\n"
    "s_nop 0\n"
    "v_cndmask_b32 v68, v72, v73, s[42:43]\n"
    "v_cndmask_b32 v69, v74, v75, s[42:43]\n"
    "s_nop 0\n"
    "v_cndmask_b32 v70, v76, v77, s[42:43]\n"
    "v_cndmask_b32 v71, v78, v79, s[42:43]\n"
    "v_cndmask_b32 v64, v249, v64, s[38:39]\n"
    "v_cndmask_b32 v65, v249, v65, s[40:41]\n"
    "v_cndmask_b32 v66, v249, v66, s[12:13]\n"
    "v_cndmask_b32 v67, v249, v67, s[14:15]\n"
    "v_cndmask_b32 v68, v249, v68, s[8:9]\n"
    "v_cndmask_b32 v69, v249, v69, s[10:11]\n"
    "v_cndmask_b32 v70, v249, v70, s[12:13]\n"
    "v_cndmask_b32 v71, v249, v71, s[14:15]\n"
    "v_max3_f32 v72, v64, v65, v66\n"
    "v_max3_f32 v74, v68, v69, v70\n"
    "v_max_f32 v72, v72, v67\n"
    "v_max_f32 v74, v74, v71\n"
    "s_nop 1\n"
    "v_permlane16_swap_b32 v72, v74\n"
    "v_max_f32 v72, v72, v74\n"
    "v_mov_b32 v74, v72\n"
    "s_nop 1\n"
    "v_permlane32_swap_b32 v72, v74\n"
    "v_max_f32 v72, v72, v74\n"
    "v_mul_f32 v72, s37, v72\n"
    "v_mov_b32 v74, v72\n"
    "s_nop 1\n"
    "v_permlane16_swap_b32 v72, v74\n"
    "v_fma_f32 v64, v64, s37, -v72\n"
    "v_fma_f32 v65, v65, s37, -v72\n"
    "v_fma_f32 v66, v66, s37, -v72\n"
    "v_fma_f32 v67, v67, s37, -v72\n"
    "v_fma_f32 v68, v68, s37, -v74\n"
    "v_fma_f32 v69, v69, s37, -v74\n"
    "v_fma_f32 v70, v70, s37, -v74\n"
    "v_fma_f32 v71, v71, s37, -v74\n"
    "v_exp_f32 v64, v64\n"
    "v_exp_f32 v65, v65\n"
    "v_exp_f32 v66, v66\n"
    "v_exp_f32 v67, v67\n"
    "v_exp_f32 v68, v68\n"
    "v_exp_f32 v69, v69\n"
    "v_exp_f32 v70, v70\n"
    "v_exp_f32 v71, v71\n"
    "v_pk_add_f32 v[72:73], v[64:65], v[66:67]\n"
    "v_pk_add_f32 v[74:75], v[68:69], v[70:71]\n"
    "v_add_f32 v72, v72, v73\n"
    "v_add_f32 v74, v74, v75\n"
    "s_nop 1\n"
    "v_permlane16_swap_b32 v72, v74\n"
    "v_add_f32 v72, v72, v74\n"
    "v_mov_b32 v74, v72\n"
    "s_nop 1\n"
    "v_permlane32_swap_b32 v72, v74\n"
    "v_add_f32 v72, v72, v74\n"
    "v_rcp_f32 v72, v72\n"
    "s_nop 0\n"
    "v_mov_b32 v74, v72\n"
    "s_nop 1\n"
    "v_permlane16_swap_b32 v72, v74\n"
    "v_pk_mul_f32 v[64:65], v[64:65], v[72:73] op_sel_hi:[1,0]\n"
    "v_pk_mul_f32 v[66:67], v[66:67], v[72:73] op_sel_hi:[1,0]\n"
    "v_pk_mul_f32 v[68:69], v[68:69], v[74:75] op_sel_hi:[1,0]\n"
    "v_pk_mul_f32 v[70:71], v[70:71], v[74:75] op_sel_hi:[1,0]\n"
    "v_cvt_pk_bf16_f32 v64, v64, v65\n"
    "v_cvt_pk_bf16_f32 v66, v66, v67\n"
    "v_cvt_pk_bf16_f32 v68, v68, v69\n"
    "v_cvt_pk_bf16_f32 v70, v70, v71\n"
    "v_perm_b32 v80, v64, v64, v247\n"
    "v_perm_b32 v81, v64, v64, v248\n"
    "v_perm_b32 v82, v66, v66, v247\n"
    "v_perm_b32 v83, v66, v66, v248\n"
    "v_perm_b32 v84, v68, v68, v247\n"
    "v_perm_b32 v85, v68, v68, v248\n"
    "v_perm_b32 v86, v70, v70, v247\n"
    "v_perm_b32 v87, v70, v70, v248\n"
    "s_waitcnt lgkmcnt(0)\n"
    "v_mfma_f32_16x16x32_bf16 v[0:3], v[52:55], v[80:83], 0\n"
    "v_mfma_f32_16x16x32_bf16 v[4:7], v[54:57], v[84:87], 0\n"
    "v_mfma_f32_16x16x32_bf16 v[8:11], v[58:61], v[80:83], 0\n"
    "v_mfma_f32_16x16x32_bf16 v[12:15], v[60:63], v[84:87], 0\n"
    "s_mov_b64 s[38:39], s[8:9]\n"
    "s_mov_b64 s[40:41], s[10:11]\n"
    "v_cvt_pk_bf16_f32 v16, v0, v1\n"
    "v_cvt_pk_bf16_f32 v17, v2, v3\n"
    "v_cvt_pk_bf16_f32 v18, v4, v5\n"
    "v_cvt_pk_bf16_f32 v19, v6, v7\n"
    "s_nop 0\n"
    "v_permlane16_swap_b32 v16, v18\n"
    "v_permlane16_swap_b32 v17, v19\n"
    "buffer_store_dwordx4 v[16:19], v245, s[0:3], s33 offen sc1\n"
    "v_cvt_pk_bf16_f32 v20, v8, v9\n"
    "v_cvt_pk_bf16_f32 v21, v10, v11\n"
    "v_cvt_pk_bf16_f32 v22, v12, v13\n"
    "v_cvt_pk_bf16_f32 v23, v14, v15\n"
    "buffer_store_dwordx4 v[20:23], v246, s[0:3], s33 offen sc1\n"
    ".Lb1_mk4x13_%=:\n"
    "v_mov_b32 v108, 0\n"
    "s_memrealtime s[26:27]\n"
    "s_cmp_lg_u32 s18, 0\n"
    "s_waitcnt lgkmcnt(0)\n"
    "s_cbranch_scc1 .Lb1_pl15_%=\n"
    "s_sub_u32 s28, s36, s26\n"
    "s_cmp_gt_i32 s28, 40\n"
    "s_cbranch_scc1 .Lb1_pl15_%=\n"
    "buffer_load_dwordx4 v[108:111], v229, s[0:3], s21 offen sc1\n"
    ".Lb1_pl15_%=:\n"
    "s_waitcnt vmcnt(0)\n"
    "s_barrier\n"
    "buffer_store_dwordx4 v[116:119], v230, s[0:3], 0 offen sc1\n"
    ".Lb1_mk5x15_%=:\n"
    "s_mov_b32 s30, s31\n"
    "s_add_u32 s31, s31, 1\n"
    "s_cmp_lt_u32 s30, 64\n"
    "s_cbranch_scc1 .Lb1_top_%=\n"
    "s_waitcnt vmcnt(0) lgkmcnt(0)\n"
    "s_endpgm\n"
    ".Lb1_tk0_%=:\n"
    ".Lb1_po3_%=:\n"
    "buffer_load_dword v31, v30, s[0:3], 0 offen sc1\n"
    "s_waitcnt vmcnt(0)\n"
    "v_cmp_gt_i32 vcc, 32, v31\n"
    "s_nop 0\n"
    "s_cbranch_vccz .Lb1_pd4_%=\n"
    "s_add_u32 s24, s24, 1\n"
    "s_cmp_lt_u32 s24, 0x40000\n"
    "s_cbranch_scc1 .Lb1_sp5_%=\n"
    "v_mov_b32 v31, 90\n"
    "s_mov_b32 s28, 33728\n"
    "buffer_store_dword v31, off, s[0:3], s28 sc1\n"
    "s_waitcnt vmcnt(0)\n"
    "s_branch .Lb1_pd4_%=\n"
    ".Lb1_sp5_%=:\n"
    "s_sleep 8\n"
    "s_branch .Lb1_po3_%=\n"
    ".Lb1_pd4_%=:\n"
    "s_branch .Lb1_tk1_%=\n"
    :: "{s[0:3]}"(s0_), "{s[4:7]}"(s4_), "{s[8:11]}"(s8_), "{s[12:15]}"(s12_), "{s[16:19]}"(s16_), "{s[20:23]}"(s20_), "{v[0:3]}"(v0_), "{v[224:227]}"(v224_), "{v[228:231]}"(v228_), "{v[232:235]}"(v232_), "{v[236:239]}"(v236_), "{v[240:243]}"(v240_), "{v[244:247]}"(v244_), "{v[248:251]}"(v248_) : "memory", "v255", "a255", "s95");
    __builtin_unreachable();
}
DEV void b2_role(const MegaArgs& A, const int L, const int c, const int rep) {
    ROLE_HEAD;
    const int b = 4 * c + w;
    const u32x4 ro = L ? u32x4{lo(A.out), hi(A.out), 2048u * 1280u, 0x20000u} : rx;
    if (L == 0) {
#pragma unroll
        for (int k = 0; k < 16; ++k) bst16(rr, ST0 + b * 32768 + (k >> 3) * 16384 + (k & 7) * 1024 + l * 16, ones);
        DRAIN();
        wait_drawn(A.ar);
        DRAIN_BARRIER();
        bst4(rr, threadIdx.x == 0 ? xl(Z_ST + c * 128, 0) : OOB, 1u);
    }
    const u32x4 s0_ = {RFL(rx[0]), RFL(rx[1]), RFL(rx[2]), RFL(rx[3])};
    const u32x4 s4_ = {RFL(lo(A.wo + 32 * b)), RFL(hi(A.wo + 32 * b)), RFL((640 * 640 - 32 * b) * 2), RFL(0x20000u)};
    const u32x4 s8_ = {RFL(ro[0]), RFL(ro[1]), RFL(ro[2]), RFL(ro[3])};
    const u32x4 s12_ = {RFL(w), RFL(L << 13), RFL(REC0 + RC_O + w * 2048), RFL(xl(Z_O + rep * 2048, L))};
    const u32x4 s16_ = {RFL(xl(Z_ST, 0)), RFL(ST0), RFL(DBG0 + ((L * 128 + 107 + c) * 4 + w) * 2048), RFL(blockIdx.x == 0 ? 6 : 0)};
    const u32x4 v0_ = {(unsigned)((0 + (l >> 2)) * 1280 + (l & 3) * 16 - 0), (unsigned)((16 + (l >> 2)) * 1280 + (l & 3) * 16 - 1024), (unsigned)((32 + (l >> 2)) * 1280 + (l & 3) * 16 - 2048), (unsigned)((48 + (l >> 2)) * 1280 + (l & 3) * 16 - 3072)};
    const u32x4 v4_ = {(unsigned)(w * 40960 + (8 * (lq & 1) + 20 * (lq >> 1) + (l16 >> 2)) * 64 + (l & 3) * 16), (unsigned)(w * 40960 + (20 * lq + 16 + (l16 >> 2)) * 64 + (l & 3) * 16), (unsigned)((l < 5 ? l : 0) * 128), (unsigned)(0u)};
    const u32x4 v224_ = {(unsigned)(l * 16), (unsigned)((l < 11 ? l : 0) * 128), (unsigned)(l < 4 && w == 1 ? xl(Z_XN + l * 2048 + c * 128, 0) : OOB), (unsigned)(c * 16384 + l * 16)};
    const u32x4 v228_ = {(unsigned)(L ? (l16 + 0) * 1280 + b * 64 + lq * 16 : RC_X2 + c * 16384 + 0 + l * 16), (unsigned)(L ? (l16 + 16) * 1280 + b * 64 + lq * 16 : RC_X2 + c * 16384 + 1024 + l * 16), (unsigned)(l < 16 ? b * 32768 + l * 16 : OOB), (unsigned)((0 + lq) * 32768 + l16 * 16)};
    const u32x4 v232_ = {(unsigned)((4 + lq) * 32768 + l16 * 16), (unsigned)((8 + lq) * 32768 + l16 * 16), (unsigned)((12 + lq) * 32768 + l16 * 16), (unsigned)((16 + lq) * 32768 + l16 * 16)};
    asm volatile(
    "s_mov_b32 s20, 0\n"
    "s_mov_b64 s[22:23], 0\n"
    "s_mov_b32 s25, 0\n"
    "s_cmp_eq_u32 s13, 0\n"
    "s_cselect_b32 s24, 24, 60\n"
    "s_sub_u32 s24, s24, s19\n"
    ".Lb2_ho1_%=:\n"
    "s_sub_u32 s24, s24, 1\n"
    "s_cbranch_scc1 .Lb2_hd2_%=\n"
    "s_sleep 17\n"
    "s_branch .Lb2_ho1_%=\n"
    ".Lb2_hd2_%=:\n"
    "v_and_b32 v30, 0x70, v224\n"
    "v_lshlrev_b32 v30, 2, v30\n"
    "v_or_b32 v30, 0x8000, v30\n"
    "buffer_load_dword v31, v30, s[0:3], 0 offen sc1\n"
    "s_waitcnt vmcnt(0)\n"
    "v_cmp_gt_i32 vcc, 32, v31\n"
    "s_nop 0\n"
    "s_cbranch_vccnz .Lb2_tk0_%=\n"
    ".Lb2_tk1_%=:\n"
    "s_mul_i32 s28, s12, 40960\n"
    "s_mov_b32 s34, 0\n"
    "s_add_u32 m0, s28, 0\n"
    "s_nop 0\n"
    "buffer_load_dwordx4 v0, s[4:7], s34 offen offset:0 lds\n"
    "buffer_load_dwordx4 v1, s[4:7], s34 offen offset:1024 lds\n"
    "buffer_load_dwordx4 v2, s[4:7], s34 offen offset:2048 lds\n"
    "buffer_load_dwordx4 v3, s[4:7], s34 offen offset:3072 lds\n"
    "s_mov_b32 s34, 81920\n"
    "s_add_u32 m0, s28, 4096\n"
    "s_nop 0\n"
    "buffer_load_dwordx4 v0, s[4:7], s34 offen offset:0 lds\n"
    "buffer_load_dwordx4 v1, s[4:7], s34 offen offset:1024 lds\n"
    "buffer_load_dwordx4 v2, s[4:7], s34 offen offset:2048 lds\n"
    "buffer_load_dwordx4 v3, s[4:7], s34 offen offset:3072 lds\n"
    "s_mov_b32 s34, 163840\n"
    "s_add_u32 m0, s28, 8192\n"
    "s_nop 0\n"
    "buffer_load_dwordx4 v0, s[4:7], s34 offen offset:0 lds\n"
    "buffer_load_dwordx4 v1, s[4:7], s34 offen offset:1024 lds\n"
    "buffer_load_dwordx4 v2, s[4:7], s34 offen offset:2048 lds\n"
    "buffer_load_dwordx4 v3, s[4:7], s34 offen offset:3072 lds\n"
    "s_mov_b32 s34, 245760\n"
    "s_add_u32 m0, s28, 12288\n"
    "s_nop 0\n"
    "buffer_load_dwordx4 v0, s[4:7], s34 offen offset:0 lds\n"
    "buffer_load_dwordx4 v1, s[4:7], s34 offen offset:1024 lds\n"
    "buffer_load_dwordx4 v2, s[4:7], s34 offen offset:2048 lds\n"
    "buffer_load_dwordx4 v3, s[4:7], s34 offen offset:3072 lds\n"
    "s_mov_b32 s34, 327680\n"
    "s_add_u32 m0, s28, 16384\n"
    "s_nop 0\n"
    "buffer_load_dwordx4 v0, s[4:7], s34 offen offset:0 lds\n"
    "buffer_load_dwordx4 v1, s[4:7], s34 offen offset:1024 lds\n"
    "buffer_load_dwordx4 v2, s[4:7], s34 offen offset:2048 lds\n"
    "buffer_load_dwordx4 v3, s[4:7], s34 offen offset:3072 lds\n"
    "s_mov_b32 s34, 409600\n"
    "s_add_u32 m0, s28, 20480\n"
    "s_nop 0\n"
    "buffer_load_dwordx4 v0, s[4:7], s34 offen offset:0 lds\n"
    "buffer_load_dwordx4 v1, s[4:7], s34 offen offset:1024 lds\n"
    "buffer_load_dwordx4 v2, s[4:7], s34 offen offset:2048 lds\n"
    "buffer_load_dwordx4 v3, s[4:7], s34 offen offset:3072 lds\n"
    "s_mov_b32 s34, 491520\n"
    "s_add_u32 m0, s28, 24576\n"
    "s_nop 0\n"
    "buffer_load_dwordx4 v0, s[4:7], s34 offen offset:0 lds\n"
    "buffer_load_dwordx4 v1, s[4:7], s34 offen offset:1024 lds\n"
    "buffer_load_dwordx4 v2, s[4:7], s34 offen offset:2048 lds\n"
    "buffer_load_dwordx4 v3, s[4:7], s34 offen offset:3072 lds\n"
    "s_mov_b32 s34, 573440\n"
    "s_add_u32 m0, s28, 28672\n"
    "s_nop 0\n"
    "buffer_load_dwordx4 v0, s[4:7], s34 offen offset:0 lds\n"
    "buffer_load_dwordx4 v1, s[4:7], s34 offen offset:1024 lds\n"
    "buffer_load_dwordx4 v2, s[4:7], s34 offen offset:2048 lds\n"
    "buffer_load_dwordx4 v3, s[4:7], s34 offen offset:3072 lds\n"
    "s_mov_b32 s34, 655360\n"
    "s_add_u32 m0, s28, 32768\n"
    "s_nop 0\n"
    "buffer_load_dwordx4 v0, s[4:7], s34 offen offset:0 lds\n"
    "buffer_load_dwordx4 v1, s[4:7], s34 offen offset:1024 lds\n"
    "buffer_load_dwordx4 v2, s[4:7], s34 offen offset:2048 lds\n"
    "buffer_load_dwordx4 v3, s[4:7], s34 offen offset:3072 lds\n"
    "s_mov_b32 s34, 737280\n"
    "s_add_u32 m0, s28, 36864\n"
    "s_nop 0\n"
    "buffer_load_dwordx4 v0, s[4:7], s34 offen offset:0 lds\n"
    "buffer_load_dwordx4 v1, s[4:7], s34 offen offset:1024 lds\n"
    "buffer_load_dwordx4 v2, s[4:7], s34 offen offset:2048 lds\n"
    "buffer_load_dwordx4 v3, s[4:7], s34 offen offset:3072 lds\n"
    "s_waitcnt vmcnt(0)\n"
    "ds_read_b64_tr_b16 a[0:1], v4 offset:0\n"
    "ds_read_b64_tr_b16 a[2:3], v4 offset:256\n"
    "ds_read_b64_tr_b16 a[80:81], v4 offset:8\n"
    "ds_read_b64_tr_b16 a[82:83], v4 offset:264\n"
    "s_waitcnt lgkmcnt(0)\n"
    "ds_read_b64_tr_b16 a[4:5], v4 offset:2560\n"
    "ds_read_b64_tr_b16 a[6:7], v4 offset:2816\n"
    "ds_read_b64_tr_b16 a[84:85], v4 offset:2568\n"
    "ds_read_b64_tr_b16 a[86:87], v4 offset:2824\n"
    "s_waitcnt lgkmcnt(0)\n"
    "ds_read_b64_tr_b16 a[8:9], v4 offset:5120\n"
    "ds_read_b64_tr_b16 a[10:11], v4 offset:5376\n"
    "ds_read_b64_tr_b16 a[88:89], v4 offset:5128\n"
    "ds_read_b64_tr_b16 a[90:91], v4 offset:5384\n"
    "s_waitcnt lgkmcnt(0)\n"
    "ds_read_b64_tr_b16 a[12:13], v4 offset:7680\n"
    "ds_read_b64_tr_b16 a[14:15], v4 offset:7936\n"
    "ds_read_b64_tr_b16 a[92:93], v4 offset:7688\n"
    "ds_read_b64_tr_b16 a[94:95], v4 offset:7944\n"
    "s_waitcnt lgkmcnt(0)\n"
    "ds_read_b64_tr_b16 a[16:17], v4 offset:10240\n"
    "ds_read_b64_tr_b16 a[18:19], v4 offset:10496\n"
    "ds_read_b64_tr_b16 a[96:97], v4 offset:10248\n"
    "ds_read_b64_tr_b16 a[98:99], v4 offset:10504\n"
    "s_waitcnt lgkmcnt(0)\n"
    "ds_read_b64_tr_b16 a[20:21], v4 offset:12800\n"
    "ds_read_b64_tr_b16 a[22:23], v4 offset:13056\n"
    "ds_read_b64_tr_b16 a[100:101], v4 offset:12808\n"
    "ds_read_b64_tr_b16 a[102:103], v4 offset:13064\n"
    "s_waitcnt lgkmcnt(0)\n"
    "ds_read_b64_tr_b16 a[24:25], v4 offset:15360\n"
    "ds_read_b64_tr_b16 a[26:27], v4 offset:15616\n"
    "ds_read_b64_tr_b16 a[104:105], v4 offset:15368\n"
    "ds_read_b64_tr_b16 a[106:107], v4 offset:15624\n"
    "s_waitcnt lgkmcnt(0)\n"
    "ds_read_b64_tr_b16 a[28:29], v4 offset:17920\n"
    "ds_read_b64_tr_b16 a[30:31], v4 offset:18176\n"
    "ds_read_b64_tr_b16 a[108:109], v4 offset:17928\n"
    "ds_read_b64_tr_b16 a[110:111], v4 offset:18184\n"
    "s_waitcnt lgkmcnt(0)\n"
    "ds_read_b64_tr_b16 a[32:33], v4 offset:20480\n"
    "ds_read_b64_tr_b16 a[34:35], v4 offset:20736\n"
    "ds_read_b64_tr_b16 a[112:113], v4 offset:20488\n"
    "ds_read_b64_tr_b16 a[114:115], v4 offset:20744\n"
    "s_waitcnt lgkmcnt(0)\n"
    "ds_read_b64_tr_b16 a[36:37], v4 offset:23040\n"
    "ds_read_b64_tr_b16 a[38:39], v4 offset:23296\n"
    "ds_read_b64_tr_b16 a[116:117], v4 offset:23048\n"
    "ds_read_b64_tr_b16 a[118:119], v4 offset:23304\n"
    "s_waitcnt lgkmcnt(0)\n"
    "ds_read_b64_tr_b16 a[40:41], v4 offset:25600\n"
    "ds_read_b64_tr_b16 a[42:43], v4 offset:25856\n"
    "ds_read_b64_tr_b16 a[120:121], v4 offset:25608\n"
    "ds_read_b64_tr_b16 a[122:123], v4 offset:25864\n"
    "s_waitcnt lgkmcnt(0)\n"
    "ds_read_b64_tr_b16 a[44:45], v4 offset:28160\n"
    "ds_read_b64_tr_b16 a[46:47], v4 offset:28416\n"
    "ds_read_b64_tr_b16 a[124:125], v4 offset:28168\n"
    "ds_read_b64_tr_b16 a[126:127], v4 offset:28424\n"
    "s_waitcnt lgkmcnt(0)\n"
    "ds_read_b64_tr_b16 a[48:49], v4 offset:30720\n"
    "ds_read_b64_tr_b16 a[50:51], v4 offset:30976\n"
    "ds_read_b64_tr_b16 a[128:129], v4 offset:30728\n"
    "ds_read_b64_tr_b16 a[130:131], v4 offset:30984\n"
    "s_waitcnt lgkmcnt(0)\n"
    "ds_read_b64_tr_b16 a[52:53], v4 offset:33280\n"
    "ds_read_b64_tr_b16 a[54:55], v4 offset:33536\n"
    "ds_read_b64_tr_b16 a[132:133], v4 offset:33288\n"
    "ds_read_b64_tr_b16 a[134:135], v4 offset:33544\n"
    "s_waitcnt lgkmcnt(0)\n"
    "ds_read_b64_tr_b16 a[56:57], v4 offset:35840\n"
    "ds_read_b64_tr_b16 a[58:59], v4 offset:36096\n"
    "ds_read_b64_tr_b16 a[136:137], v4 offset:35848\n"
    "ds_read_b64_tr_b16 a[138:139], v4 offset:36104\n"
    "s_waitcnt lgkmcnt(0)\n"
    "ds_read_b64_tr_b16 a[60:61], v4 offset:38400\n"
    "ds_read_b64_tr_b16 a[62:63], v4 offset:38656\n"
    "ds_read_b64_tr_b16 a[140:141], v4 offset:38408\n"
    "ds_read_b64_tr_b16 a[142:143], v4 offset:38664\n"
    "s_waitcnt lgkmcnt(0)\n"
    "ds_read_b64_tr_b16 a[64:65], v5 offset:0\n"
    "ds_read_b64_tr_b16 a[66:67], v5 offset:5120\n"
    "ds_read_b64_tr_b16 a[144:145], v5 offset:8\n"
    "ds_read_b64_tr_b16 a[146:147], v5 offset:5128\n"
    "s_waitcnt lgkmcnt(0)\n"
    "ds_read_b64_tr_b16 a[68:69], v5 offset:10240\n"
    "ds_read_b64_tr_b16 a[70:71], v5 offset:15360\n"
    "ds_read_b64_tr_b16 a[148:149], v5 offset:10248\n"
    "ds_read_b64_tr_b16 a[150:151], v5 offset:15368\n"
    "s_waitcnt lgkmcnt(0)\n"
    "ds_read_b64_tr_b16 a[72:73], v5 offset:20480\n"
    "ds_read_b64_tr_b16 a[74:75], v5 offset:25600\n"
    "ds_read_b64_tr_b16 a[152:153], v5 offset:20488\n"
    "ds_read_b64_tr_b16 a[154:155], v5 offset:25608\n"
    "s_waitcnt lgkmcnt(0)\n"
    "ds_read_b64_tr_b16 a[76:77], v5 offset:30720\n"
    "ds_read_b64_tr_b16 a[78:79], v5 offset:35840\n"
    "ds_read_b64_tr_b16 a[156:157], v5 offset:30728\n"
    "ds_read_b64_tr_b16 a[158:159], v5 offset:35848\n"
    "s_waitcnt lgkmcnt(0)\n"
    "s_mov_b32 s26, -2\n"
    ".Lb2_po6_%=:\n"
    "buffer_load_dword v8, v6, s[0:3], s16 offen sc1\n"
    "s_waitcnt vmcnt(0)\n"
    "v_cmp_gt_i32 vcc, 1, v8\n"
    "s_nop 0\n"
    "s_cbranch_vccz .Lb2_pd7_%=\n"
    "s_add_u32 s20, s20, 1\n"
    "s_cmp_lt_u32 s20, 0x40000\n"
    "s_cbranch_scc1 .Lb2_sp8_%=\n"
    "v_mov_b32 v8, 70\n"
    "s_mov_b32 s24, 33728\n"
    "buffer_store_dword v8, off, s[0:3], s24 sc1\n"
    "s_waitcnt vmcnt(0)\n"
    "s_branch .Lb2_pd7_%=\n"
    ".Lb2_sp8_%=:\n"
    "s_sleep 4\n"
    "s_branch .Lb2_po6_%=\n"
    ".Lb2_pd7_%=:\n"
    "s_mov_b32 s26, 0\n"
    "s_lshl_b32 s33, s12, 11\n"
    "s_mov_b32 s35, 0\n"
    "v_mov_b32 v84, 0\n"
    ".Lb2_top_%=:\n"
    "s_lshl_b32 s28, s26, 19\n"
    "s_lshl_b32 s25, s26, 11\n"
    "s_xor_b32 s25, s25, s13\n"
    "s_and_b32 s25, s25, 0x2000\n"
    "s_add_u32 s28, s28, s25\n"
    "s_add_u32 s28, s28, s14\n"
    "s_add_u32 s27, s26, 1\n"
    "s_sub_u32 s29, s28, 81920\n"
    "s_sub_u32 s31, s28, 163840\n"
    "s_xor_b32 s31, s31, 0x2000\n"
    "s_mul_i32 s30, s26, 40960\n"
    "s_cmp_eq_u32 s13, 0\n"
    "s_cselect_b32 s30, s31, s30\n"
    "s_and_b32 s32, s26, 32\n"
    "s_add_u32 s32, s32, s26\n"
    "s_lshl_b32 s32, s32, 8\n"
    "s_add_u32 s32, s32, s17\n"
    "s_cmp_lg_u32 s12, 0\n"
    "s_cbranch_scc1 .Lb2_pd10_%=\n"
    "s_add_u32 s25, s22, 248\n"
    "v_min3_i32 v84, v84, v85, v86\n"
    "v_min_i32 v84, v84, v87\n"
    "v_cmp_gt_i32 vcc, s27, v84\n"
    "s_nop 0\n"
    "s_cbranch_vccz .Lb2_pd10_%=\n"
    ".Lb2_rt11_%=:\n"
    "s_memrealtime s[22:23]\n"
    "s_waitcnt lgkmcnt(0)\n"
    "s_sub_u32 s24, s35, s22\n"
    "s_cmp_lt_u32 s24, 600\n"
    "s_cbranch_scc1 .Lb2_rt11_%=\n"
    ".Lb2_po9_%=:\n"
    "buffer_load_dwordx4 v[84:87], v225, s[0:3], s15 offen sc1\n"
    "s_memrealtime s[22:23]\n"
    "s_waitcnt vmcnt(0) lgkmcnt(0)\n"
    "s_add_u32 s25, s22, 248\n"
    "v_min3_i32 v84, v84, v85, v86\n"
    "v_min_i32 v84, v84, v87\n"
    "v_cmp_gt_i32 vcc, s27, v84\n"
    "s_nop 0\n"
    "s_cbranch_vccz .Lb2_pd10_%=\n"
    "s_add_u32 s20, s20, 1\n"
    "s_cmp_lt_u32 s20, 0x40000\n"
    "s_cbranch_scc1 .Lb2_sp12_%=\n"
    "v_mov_b32 v84, 71\n"
    "s_mov_b32 s24, 33728\n"
    "buffer_store_dword v84, off, s[0:3], s24 sc1\n"
    "s_waitcnt vmcnt(0)\n"
    "s_branch .Lb2_pd10_%=\n"
    ".Lb2_sp12_%=:\n"
    "s_sleep 6\n"
    "s_branch .Lb2_po9_%=\n"
    ".Lb2_pd10_%=:\n"
    "s_barrier\n"
    "s_cmp_eq_u32 s27, 63\n"
    "s_cselect_b32 s35, s22, s25\n"
    ".Lb2_mk0x12_%=:\n"
    "s_add_u32 m0, s33, 0\n"
    "s_nop 0\n"
    "buffer_load_dwordx4 v224, s[0:3], s28 offen sc1 lds\n"
    "buffer_load_dwordx4 v224, s[0:3], s28 offen offset:1024 sc1 lds\n"
    "s_add_u32 m0, s33, 8192\n"
    "s_add_u32 s28, s28, 0x4000\n"
    "buffer_load_dwordx4 v224, s[0:3], s28 offen sc1 lds\n"
    "buffer_load_dwordx4 v224, s[0:3], s28 offen offset:1024 sc1 lds\n"
    "s_add_u32 m0, s33, 16384\n"
    "s_add_u32 s28, s28, 0x4000\n"
    "buffer_load_dwordx4 v224, s[0:3], s28 offen sc1 lds\n"
    "buffer_load_dwordx4 v224, s[0:3], s28 offen offset:1024 sc1 lds\n"
    "s_add_u32 m0, s33, 24576\n"
    "s_add_u32 s28, s28, 0x4000\n"
    "buffer_load_dwordx4 v224, s[0:3], s28 offen sc1 lds\n"
    "buffer_load_dwordx4 v224, s[0:3], s28 offen offset:1024 sc1 lds\n"
    "s_add_u32 m0, s33, 32768\n"
    "s_add_u32 s28, s28, 0x4000\n"
    "buffer_load_dwordx4 v224, s[0:3], s28 offen sc1 lds\n"
    "buffer_load_dwordx4 v224, s[0:3], s28 offen offset:1024 sc1 lds\n"
    "buffer_load_dwordx4 v[48:51], v227, s[0:3], s29 offen offset:0 sc1\n"
    "buffer_load_dwordx4 v[52:55], v227, s[0:3], s29 offen offset:1024 sc1\n"
    "s_waitcnt vmcnt(10)\n"
    "s_barrier\n"
    ".Lb2_mk1x13_%=:\n"
    "ds_read_b128 v[16:19], v224 offset:0\n"
    "ds_read_b128 v[20:23], v224 offset:1024\n"
    "ds_read_b128 v[24:27], v224 offset:2048\n"
    "ds_read_b128 v[28:31], v224 offset:3072\n"
    "ds_read_b128 v[32:35], v224 offset:4096\n"
    "ds_read_b128 v[36:39], v224 offset:5120\n"
    "s_waitcnt lgkmcnt(4)\n"
    "v_mfma_f32_16x16x32_bf16 v[0:3], a[0:3], v[16:19], 0\n"
    "ds_read_b128 v[40:43], v224 offset:6144\n"
    "ds_read_b128 v[44:47], v224 offset:7168\n"
    "v_mfma_f32_16x16x32_bf16 v[8:11], a[0:3], v[20:23], 0\n"
    "v_mfma_f32_16x16x32_bf16 v[4:7], a[80:83], v[16:19], 0\n"
    "v_mfma_f32_16x16x32_bf16 v[12:15], a[80:83], v[20:23], 0\n"
    "s_waitcnt lgkmcnt(4)\n"
    "v_mfma_f32_16x16x32_bf16 v[0:3], a[4:7], v[24:27], v[0:3]\n"
    "s_waitcnt vmcnt(8)\n"
    "s_barrier\n"
    "ds_read_b128 v[16:19], v224 offset:8192\n"
    "v_mfma_f32_16x16x32_bf16 v[8:11], a[4:7], v[28:31], v[8:11]\n"
    "ds_read_b128 v[20:23], v224 offset:9216\n"
    "v_mfma_f32_16x16x32_bf16 v[4:7], a[84:87], v[24:27], v[4:7]\n"
    "v_mfma_f32_16x16x32_bf16 v[12:15], a[84:87], v[28:31], v[12:15]\n"
    "s_waitcnt lgkmcnt(4)\n"
    "v_mfma_f32_16x16x32_bf16 v[0:3], a[8:11], v[32:35], v[0:3]\n"
    "ds_read_b128 v[24:27], v224 offset:10240\n"
    "ds_read_b128 v[28:31], v224 offset:11264\n"
    "v_mfma_f32_16x16x32_bf16 v[8:11], a[8:11], v[36:39], v[8:11]\n"
    "v_mfma_f32_16x16x32_bf16 v[4:7], a[88:91], v[32:35], v[4:7]\n"
    "v_mfma_f32_16x16x32_bf16 v[12:15], a[88:91], v[36:39], v[12:15]\n"
    "s_waitcnt lgkmcnt(4)\n"
    "v_mfma_f32_16x16x32_bf16 v[0:3], a[12:15], v[40:43], v[0:3]\n"
    "ds_read_b128 v[32:35], v224 offset:12288\n"
    "ds_read_b128 v[36:39], v224 offset:13312\n"
    "v_mfma_f32_16x16x32_bf16 v[8:11], a[12:15], v[44:47], v[8:11]\n"
    "v_mfma_f32_16x16x32_bf16 v[4:7], a[92:95], v[40:43], v[4:7]\n"
    "v_mfma_f32_16x16x32_bf16 v[12:15], a[92:95], v[44:47], v[12:15]\n"
    "s_waitcnt lgkmcnt(4)\n"
    "v_mfma_f32_16x16x32_bf16 v[0:3], a[16:19], v[16:19], v[0:3]\n"
    "ds_read_b128 v[40:43], v224 offset:14336\n"
    "ds_read_b128 v[44:47], v224 offset:15360\n"
    "v_mfma_f32_16x16x32_bf16 v[8:11], a[16:19], v[20:23], v[8:11]\n"
    "v_mfma_f32_16x16x32_bf16 v[4:7], a[96:99], v[16:19], v[4:7]\n"
    "v_mfma_f32_16x16x32_bf16 v[12:15], a[96:99], v[20:23], v[12:15]\n"
    "s_waitcnt lgkmcnt(4)\n"
    "v_mfma_f32_16x16x32_bf16 v[0:3], a[20:23], v[24:27], v[0:3]\n"
    "s_waitcnt vmcnt(6)\n"
    "s_barrier\n"
    "ds_read_b128 v[16:19], v224 offset:16384\n"
    "v_mfma_f32_16x16x32_bf16 v[8:11], a[20:23], v[28:31], v[8:11]\n"
    "ds_read_b128 v[20:23], v224 offset:17408\n"
    "v_mfma_f32_16x16x32_bf16 v[4:7], a[100:103], v[24:27], v[4:7]\n"
    "v_mfma_f32_16x16x32_bf16 v[12:15], a[100:103], v[28:31], v[12:15]\n"
    "s_waitcnt lgkmcnt(4)\n"
    "v_mfma_f32_16x16x32_bf16 v[0:3], a[24:27], v[32:35], v[0:3]\n"
    "ds_read_b128 v[24:27], v224 offset:18432\n"
    "ds_read_b128 v[28:31], v224 offset:19456\n"
    "v_mfma_f32_16x16x32_bf16 v[8:11], a[24:27], v[36:39], v[8:11]\n"
    "v_mfma_f32_16x16x32_bf16 v[4:7], a[104:107], v[32:35], v[4:7]\n"
    "v_mfma_f32_16x16x32_bf16 v[12:15], a[104:107], v[36:39], v[12:15]\n"
    "s_waitcnt lgkmcnt(4)\n"
    "v_mfma_f32_16x16x32_bf16 v[0:3], a[28:31], v[40:43], v[0:3]\n"
    "ds_read_b128 v[32:35], v224 offset:20480\n"
    "ds_read_b128 v[36:39], v224 offset:21504\n"
    "v_mfma_f32_16x16x32_bf16 v[8:11], a[28:31], v[44:47], v[8:11]\n"
    "v_mfma_f32_16x16x32_bf16 v[4:7], a[108:111], v[40:43], v[4:7]\n"
    "v_mfma_f32_16x16x32_bf16 v[12:15], a[108:111], v[44:47], v[12:15]\n"
    "s_waitcnt lgkmcnt(4)\n"
    "v_mfma_f32_16x16x32_bf16 v[0:3], a[32:35], v[16:19], v[0:3]\n"
    "ds_read_b128 v[40:43], v224 offset:22528\n"
    "ds_read_b128 v[44:47], v224 offset:23552\n"
    "v_mfma_f32_16x16x32_bf16 v[8:11], a[32:35], v[20:23], v[8:11]\n"
    "v_mfma_f32_16x16x32_bf16 v[4:7], a[112:115], v[16:19], v[4:7]\n"
    "v_mfma_f32_16x16x32_bf16 v[12:15], a[112:115], v[20:23], v[12:15]\n"
    "s_waitcnt lgkmcnt(4)\n"
    "v_mfma_f32_16x16x32_bf16 v[0:3], a[36:39], v[24:27], v[0:3]\n"
    "s_waitcnt vmcnt(4)\n"
    "s_barrier\n"
    "ds_read_b128 v[16:19], v224 offset:24576\n"
    "v_mfma_f32_16x16x32_bf16 v[8:11], a[36:39], v[28:31], v[8:11]\n"
    "ds_read_b128 v[20:23], v224 offset:25600\n"
    "v_mfma_f32_16x16x32_bf16 v[4:7], a[116:119], v[24:27], v[4:7]\n"
    "v_mfma_f32_16x16x32_bf16 v[12:15], a[116:119], v[28:31], v[12:15]\n"
    "s_waitcnt lgkmcnt(4)\n"
    "v_mfma_f32_16x16x32_bf16 v[0:3], a[40:43], v[32:35], v[0:3]\n"
    "ds_read_b128 v[24:27], v224 offset:26624\n"
    "ds_read_b128 v[28:31], v224 offset:27648\n"
    "v_mfma_f32_16x16x32_bf16 v[8:11], a[40:43], v[36:39], v[8:11]\n"
    "v_mfma_f32_16x16x32_bf16 v[4:7], a[120:123], v[32:35], v[4:7]\n"
    "v_mfma_f32_16x16x32_bf16 v[12:15], a[120:123], v[36:39], v[12:15]\n"
    "s_waitcnt lgkmcnt(4)\n"
    "v_mfma_f32_16x16x32_bf16 v[0:3], a[44:47], v[40:43], v[0:3]\n"
    "ds_read_b128 v[32:35], v224 offset:28672\n"
    "ds_read_b128 v[36:39], v224 offset:29696\n"
    "v_mfma_f32_16x16x32_bf16 v[8:11], a[44:47], v[44:47], v[8:11]\n"
    "v_mfma_f32_16x16x32_bf16 v[4:7], a[124:127], v[40:43], v[4:7]\n"
    "v_mfma_f32_16x16x32_bf16 v[12:15], a[124:127], v[44:47], v[12:15]\n"
    "s_waitcnt lgkmcnt(4)\n"
    "v_mfma_f32_16x16x32_bf16 v[0:3], a[48:51], v[16:19], v[0:3]\n"
    "ds_read_b128 v[40:43], v224 offset:30720\n"
    "ds_read_b128 v[44:47], v224 offset:31744\n"
    "v_mfma_f32_16x16x32_bf16 v[8:11], a[48:51], v[20:23], v[8:11]\n"
    "v_mfma_f32_16x16x32_bf16 v[4:7], a[128:131], v[16:19], v[4:7]\n"
    "v_mfma_f32_16x16x32_bf16 v[12:15], a[128:131], v[20:23], v[12:15]\n"
    "s_waitcnt lgkmcnt(4)\n"
    "v_mfma_f32_16x16x32_bf16 v[0:3], a[52:55], v[24:27], v[0:3]\n"
    "s_waitcnt vmcnt(2)\n"
    "s_barrier\n"
    "ds_read_b128 v[16:19], v224 offset:32768\n"
    "v_mfma_f32_16x16x32_bf16 v[8:11], a[52:55], v[28:31], v[8:11]\n"
    "ds_read_b128 v[20:23], v224 offset:33792\n"
    "v_mfma_f32_16x16x32_bf16 v[4:7], a[132:135], v[24:27], v[4:7]\n"
    "v_mfma_f32_16x16x32_bf16 v[12:15], a[132:135], v[28:31], v[12:15]\n"
    "s_waitcnt lgkmcnt(4)\n"
    "v_mfma_f32_16x16x32_bf16 v[0:3], a[56:59], v[32:35], v[0:3]\n"
    "ds_read_b128 v[24:27], v224 offset:34816\n"
    "ds_read_b128 v[28:31], v224 offset:35840\n"
    "v_mfma_f32_16x16x32_bf16 v[8:11], a[56:59], v[36:39], v[8:11]\n"
    "v_mfma_f32_16x16x32_bf16 v[4:7], a[136:139], v[32:35], v[4:7]\n"
    "s_waitcnt vmcnt(0)\n"
    "v_lshlrev_b32 v96, 16, v48\n"
    "v_and_b32 v97, 0xffff0000, v48\n"
    "v_mfma_f32_16x16x32_bf16 v[12:15], a[136:139], v[36:39], v[12:15]\n"
    "s_waitcnt lgkmcnt(4)\n"
    "v_lshlrev_b32 v98, 16, v49\n"
    "v_and_b32 v99, 0xffff0000, v49\n"
    "v_mfma_f32_16x16x32_bf16 v[0:3], a[60:63], v[40:43], v[0:3]\n"
    "s_waitcnt lgkmcnt(2)\n"
    "ds_read_b128 v[32:35], v224 offset:36864\n"
    "ds_read_b128 v[36:39], v224 offset:37888\n"
    "v_mfma_f32_16x16x32_bf16 v[8:11], a[60:63], v[44:47], v[8:11]\n"
    "v_swap_b32 v18, v20\n"
    "v_mfma_f32_16x16x32_bf16 v[4:7], a[140:143], v[40:43], v[4:7]\n"
    "v_swap_b32 v19, v21\n"
    "v_mfma_f32_16x16x32_bf16 v[12:15], a[140:143], v[44:47], v[12:15]\n"
    "v_lshlrev_b32 v100, 16, v50\n"
    "v_and_b32 v101, 0xffff0000, v50\n"
    "v_mfma_f32_16x16x32_bf16 v[0:3], a[64:67], v[16:19], v[0:3]\n"
    "s_waitcnt lgkmcnt(2)\n"
    "ds_read_b128 v[40:43], v224 offset:38912\n"
    "ds_read_b128 v[44:47], v224 offset:39936\n"
    "v_mfma_f32_16x16x32_bf16 v[8:11], a[64:67], v[20:23], v[8:11]\n"
    "v_swap_b32 v26, v28\n"
    "v_mfma_f32_16x16x32_bf16 v[4:7], a[144:147], v[16:19], v[4:7]\n"
    "v_swap_b32 v27, v29\n"
    "v_mfma_f32_16x16x32_bf16 v[12:15], a[144:147], v[20:23], v[12:15]\n"
    "v_lshlrev_b32 v102, 16, v51\n"
    "v_and_b32 v103, 0xffff0000, v51\n"
    "v_mfma_f32_16x16x32_bf16 v[0:3], a[68:71], v[24:27], v[0:3]\n"
    "s_waitcnt lgkmcnt(2)\n"
    "v_lshlrev_b32 v104, 16, v52\n"
    "v_and_b32 v105, 0xffff0000, v52\n"
    "v_mfma_f32_16x16x32_bf16 v[8:11], a[68:71], v[28:31], v[8:11]\n"
    "v_swap_b32 v34, v36\n"
    "v_mfma_f32_16x16x32_bf16 v[4:7], a[148:151], v[24:27], v[4:7]\n"
    "v_swap_b32 v35, v37\n"
    "v_mfma_f32_16x16x32_bf16 v[12:15], a[148:151], v[28:31], v[12:15]\n"
    "v_lshlrev_b32 v106, 16, v53\n"
    "v_and_b32 v107, 0xffff0000, v53\n"
    "v_mfma_f32_16x16x32_bf16 v[0:3], a[72:75], v[32:35], v[0:3]\n"
    "s_waitcnt lgkmcnt(0)\n"
    "v_lshlrev_b32 v108, 16, v54\n"
    "v_and_b32 v109, 0xffff0000, v54\n"
    "v_mfma_f32_16x16x32_bf16 v[8:11], a[72:75], v[36:39], v[8:11]\n"
    "v_swap_b32 v42, v44\n"
    "v_mfma_f32_16x16x32_bf16 v[4:7], a[152:155], v[32:35], v[4:7]\n"
    "v_swap_b32 v43, v45\n"
    "v_mfma_f32_16x16x32_bf16 v[12:15], a[152:155], v[36:39], v[12:15]\n"
    "v_lshlrev_b32 v110, 16, v55\n"
    "v_and_b32 v111, 0xffff0000, v55\n"
    "v_mfma_f32_16x16x32_bf16 v[0:3], a[76:79], v[40:43], v[0:3]\n"
    "v_mfma_f32_16x16x32_bf16 v[8:11], a[76:79], v[44:47], v[8:11]\n"
    "v_mfma_f32_16x16x32_bf16 v[4:7], a[156:159], v[40:43], v[4:7]\n"
    "v_mfma_f32_16x16x32_bf16 v[12:15], a[156:159], v[44:47], v[12:15]\n"
    ".Lb2_mk2x14_%=:\n"
    "v_pk_add_f32 v[0:1], v[0:1], v[96:97]\n"
    "v_pk_add_f32 v[2:3], v[2:3], v[98:99]\n"
    "v_pk_add_f32 v[8:9], v[8:9], v[104:105]\n"
    "v_pk_add_f32 v[10:11], v[10:11], v[106:107]\n"
    "s_nop 0\n"
    "v_pk_add_f32 v[4:5], v[4:5], v[100:101]\n"
    "v_pk_add_f32 v[6:7], v[6:7], v[102:103]\n"
    "s_nop 0\n"
    "v_pk_add_f32 v[12:13], v[12:13], v[108:109]\n"
    "v_pk_add_f32 v[14:15], v[14:15], v[110:111]\n"
    "s_cmp_lg_u32 s13, 0\n"
    "s_cbranch_scc1 .Lb2_out_%=\n"
    "v_pk_add_f32 v[64:65], v[0:1], v[2:3]\n"
    "v_pk_add_f32 v[68:69], v[8:9], v[10:11]\n"
    "v_pk_mul_f32 v[66:67], v[0:1], v[0:1]\n"
    "v_pk_mul_f32 v[70:71], v[8:9], v[8:9]\n"
    "v_pk_add_f32 v[96:97], v[4:5], v[6:7]\n"
    "v_pk_add_f32 v[98:99], v[12:13], v[14:15]\n"
    "v_pk_fma_f32 v[66:67], v[2:3], v[2:3], v[66:67]\n"
    "v_pk_fma_f32 v[70:71], v[10:11], v[10:11], v[70:71]\n"
    "v_pk_add_f32 v[64:65], v[64:65], v[96:97]\n"
    "v_pk_add_f32 v[68:69], v[68:69], v[98:99]\n"
    "v_pk_fma_f32 v[66:67], v[4:5], v[4:5], v[66:67]\n"
    "v_pk_fma_f32 v[70:71], v[12:13], v[12:13], v[70:71]\n"
    "v_add_f32 v64, v64, v65\n"
    "v_add_f32 v68, v68, v69\n"
    "v_pk_fma_f32 v[66:67], v[6:7], v[6:7], v[66:67]\n"
    "v_pk_fma_f32 v[70:71], v[14:15], v[14:15], v[70:71]\n"
    "v_add_f32 v66, v66, v67\n"
    "v_add_f32 v70, v70, v71\n"
    "s_nop 0\n"
    "v_permlane16_swap_b32 v64, v66\n"
    "v_permlane16_swap_b32 v68, v70\n"
    "v_add_f32 v64, v64, v66\n"
    "v_add_f32 v68, v68, v70\n"
    "s_nop 1\n"
    "v_permlane32_swap_b32 v64, v68\n"
    "v_add_f32 v64, v64, v68\n"
    "v_min_u32 v64, -2, v64\n"
    "v_mov_b32 v65, v64\n"
    "v_cvt_pk_bf16_f32 v56, v0, v1\n"
    "v_cvt_pk_bf16_f32 v57, v2, v3\n"
    "v_permlane16_swap_b32 v64, v65\n"
    "v_mov_b32 v66, v64\n"
    "v_mov_b32 v67, v65\n"
    "v_cvt_pk_bf16_f32 v58, v4, v5\n"
    "v_cvt_pk_bf16_f32 v59, v6, v7\n"
    "v_permlane32_swap_b32 v64, v66\n"
    "v_permlane32_swap_b32 v65, v67\n"
    "buffer_store_dwordx4 v[64:67], v230, s[0:3], s32 offen sc1\n"
    "v_cvt_pk_bf16_f32 v60, v8, v9\n"
    "v_cvt_pk_bf16_f32 v61, v10, v11\n"
    "v_cvt_pk_bf16_f32 v62, v12, v13\n"
    "v_cvt_pk_bf16_f32 v63, v14, v15\n"
    ".Lb2_mk3x15_%=:\n"
    "v_lshlrev_b32 v0, 16, v56\n"
    "v_and_b32 v1, 0xffff0000, v56\n"
    "v_lshlrev_b32 v2, 16, v57\n"
    "v_and_b32 v3, 0xffff0000, v57\n"
    "v_lshlrev_b32 v4, 16, v58\n"
    "v_and_b32 v5, 0xffff0000, v58\n"
    "v_lshlrev_b32 v6, 16, v59\n"
    "v_and_b32 v7, 0xffff0000, v59\n"
    "v_lshlrev_b32 v8, 16, v60\n"
    "v_and_b32 v9, 0xffff0000, v60\n"
    "v_lshlrev_b32 v10, 16, v61\n"
    "v_and_b32 v11, 0xffff0000, v61\n"
    "v_lshlrev_b32 v12, 16, v62\n"
    "v_and_b32 v13, 0xffff0000, v62\n"
    "v_lshlrev_b32 v14, 16, v63\n"
    "v_and_b32 v15, 0xffff0000, v63\n"
    "s_sleep 6\n"
    "buffer_load_dwordx4 v[64:67], v231, s[0:3], s32 offen sc1\n"
    "buffer_load_dwordx4 v[68:71], v232, s[0:3], s32 offen sc1\n"
    "buffer_load_dwordx4 v[72:75], v233, s[0:3], s32 offen sc1\n"
    "buffer_load_dwordx4 v[76:79], v234, s[0:3], s32 offen sc1\n"
    "buffer_load_dwordx4 v[80:83], v235, s[0:3], s32 offen sc1\n"
    "buffer_store_dwordx4 v[56:59], v228, s[8:11], s30 offen sc1\n"
    "buffer_store_dwordx4 v[60:63], v229, s[8:11], s30 offen sc1\n"
    ".Lb2_sl_%=:\n"
    "s_waitcnt vmcnt(6)\n"
    "v_max_u32 v84, v64, v64\n"
    "s_waitcnt vmcnt(5)\n"
    "v_max_u32 v84, v68, v84\n"
    "v_pk_add_f32 v[64:65], v[64:65], v[68:69]\n"
    "v_pk_add_f32 v[66:67], v[66:67], v[70:71]\n"
    "s_waitcnt vmcnt(4)\n"
    "v_max_u32 v84, v72, v84\n"
    "v_pk_add_f32 v[64:65], v[64:65], v[72:73]\n"
    "v_pk_add_f32 v[66:67], v[66:67], v[74:75]\n"
    "s_waitcnt vmcnt(3)\n"
    "v_max_u32 v84, v76, v84\n"
    "v_pk_add_f32 v[64:65], v[64:65], v[76:77]\n"
    "v_pk_add_f32 v[66:67], v[66:67], v[78:79]\n"
    "s_waitcnt vmcnt(2)\n"
    "v_max_u32 v84, v80, v84\n"
    "v_pk_add_f32 v[64:65], v[64:65], v[80:81]\n"
    "v_pk_add_f32 v[66:67], v[66:67], v[82:83]\n"
    "v_cmp_eq_u32 vcc, -1, v84\n"
    "s_nop 0\n"
    "s_cbranch_vccnz .Lb2_sr_%=\n"
    ".Lb2_sg_%=:\n"
    ".Lb2_mk4x16_%=:\n"
    "v_permlane16_swap_b32 v64, v65\n"
    "v_permlane16_swap_b32 v66, v67\n"
    "v_add_f32 v64, v64, v65\n"
    "v_add_f32 v66, v66, v67\n"
    "s_nop 1\n"
    "v_permlane32_swap_b32 v64, v66\n"
    "v_add_f32 v64, v64, v66\n"
    "v_mov_b32 v66, v64\n"
    "s_nop 1\n"
    "v_permlane16_swap_b32 v64, v66\n"
    "v_mul_f32 v64, 0x3acccccd, v64\n"
    "v_mul_f32 v66, 0x3acccccd, v66\n"
    "v_fma_f32 v66, -v64, v64, v66\n"
    "v_max_f32 v66, 0, v66\n"
    "v_cvt_pk_bf16_f32 v64, v64, v66\n"
    "v_and_b32 v66, 0xffff0000, v64\n"
    "v_lshlrev_b32 v64, 16, v64\n"
    "v_add_f32 v66, 0x3727c5ac, v66\n"
    "v_mov_b32 v68, v64\n"
    "v_cvt_pk_bf16_f32 v66, v66, v66\n"
    "v_lshlrev_b32 v66, 16, v66\n"
    "v_sqrt_f32 v66, v66\n"
    "v_permlane32_swap_b32 v64, v68\n"
    "v_cvt_pk_bf16_f32 v66, v66, v66\n"
    "v_lshlrev_b32 v66, 16, v66\n"
    "v_rcp_f32 v66, v66\n"
    "s_nop 0\n"
    "v_mov_b32 v70, v66\n"
    "s_nop 1\n"
    "v_permlane32_swap_b32 v66, v70\n"
    "v_mul_f32 v64, v64, v66\n"
    "v_mul_f32 v68, v68, v70\n"
    "v_pk_fma_f32 v[0:1], v[0:1], v[66:67], v[64:65] op_sel_hi:[1,0,0] neg_lo:[0,0,1] neg_hi:[0,0,1]\n"
    "v_pk_fma_f32 v[2:3], v[2:3], v[66:67], v[64:65] op_sel_hi:[1,0,0] neg_lo:[0,0,1] neg_hi:[0,0,1]\n"
    "v_pk_fma_f32 v[4:5], v[4:5], v[66:67], v[64:65] op_sel_hi:[1,0,0] neg_lo:[0,0,1] neg_hi:[0,0,1]\n"
    "v_pk_fma_f32 v[6:7], v[6:7], v[66:67], v[64:65] op_sel_hi:[1,0,0] neg_lo:[0,0,1] neg_hi:[0,0,1]\n"
    "v_cvt_pk_bf16_f32 v96, v0, v1\n"
    "v_cvt_pk_bf16_f32 v98, v2, v3\n"
    "v_cvt_pk_bf16_f32 v97, v4, v5\n"
    "v_cvt_pk_bf16_f32 v99, v6, v7\n"
    "buffer_store_dwordx4 v[96:99], v227, s[0:3], s31 offen offset:0 sc1\n"
    "v_pk_fma_f32 v[8:9], v[8:9], v[70:71], v[68:69] op_sel_hi:[1,0,0] neg_lo:[0,0,1] neg_hi:[0,0,1]\n"
    "v_pk_fma_f32 v[10:11], v[10:11], v[70:71], v[68:69] op_sel_hi:[1,0,0] neg_lo:[0,0,1] neg_hi:[0,0,1]\n"
    "v_pk_fma_f32 v[12:13], v[12:13], v[70:71], v[68:69] op_sel_hi:[1,0,0] neg_lo:[0,0,1] neg_hi:[0,0,1]\n"
    "v_pk_fma_f32 v[14:15], v[14:15], v[70:71], v[68:69] op_sel_hi:[1,0,0] neg_lo:[0,0,1] neg_hi:[0,0,1]\n"
    "v_cvt_pk_bf16_f32 v100, v8, v9\n"
    "v_cvt_pk_bf16_f32 v102, v10, v11\n"
    "v_cvt_pk_bf16_f32 v101, v12, v13\n"
    "v_cvt_pk_bf16_f32 v103, v14, v15\n"
    "buffer_store_dwordx4 v[100:103], v227, s[0:3], s31 offen offset:1024 sc1\n"
    "v_mov_b32 v88, s27\n"
    "v_mov_b32 v89, s27\n"
    "v_mov_b32 v90, s27\n"
    "v_mov_b32 v91, s27\n"
    ".Lb2_mk5x17_%=:\n"
    "v_mov_b32 v84, 0\n"
    "s_memrealtime s[22:23]\n"
    "s_cmp_lg_u32 s12, 0\n"
    "s_waitcnt lgkmcnt(0)\n"
    "s_cbranch_scc1 .Lb2_pl19_%=\n"
    "s_sub_u32 s24, s35, s22\n"
    "s_cmp_gt_i32 s24, 40\n"
    "s_cbranch_scc1 .Lb2_pl19_%=\n"
    "buffer_load_dwordx4 v[84:87], v225, s[0:3], s15 offen sc1\n"
    ".Lb2_pl19_%=:\n"
    "s_waitcnt vmcnt(0)\n"
    "s_barrier\n"
    "buffer_store_dwordx4 v[88:91], v226, s[0:3], 0 offen sc1\n"
    ".Lb2_mk6x19_%=:\n"
    ".Lb2_nx_%=:\n"
    "s_mov_b32 s26, s27\n"
    "s_cmp_lt_u32 s26, 64\n"
    "s_cbranch_scc1 .Lb2_top_%=\n"
    "s_waitcnt vmcnt(0) lgkmcnt(0)\n"
    "s_endpgm\n"
    ".Lb2_tk0_%=:\n"
    ".Lb2_po3_%=:\n"
    "buffer_load_dword v31, v30, s[0:3], 0 offen sc1\n"
    "s_waitcnt vmcnt(0)\n"
    "v_cmp_gt_i32 vcc, 32, v31\n"
    "s_nop 0\n"
    "s_cbranch_vccz .Lb2_pd4_%=\n"
    "s_add_u32 s20, s20, 1\n"
    "s_cmp_lt_u32 s20, 0x40000\n"
    "s_cbranch_scc1 .Lb2_sp5_%=\n"
    "v_mov_b32 v31, 90\n"
    "s_mov_b32 s24, 33728\n"
    "buffer_store_dword v31, off, s[0:3], s24 sc1\n"
    "s_waitcnt vmcnt(0)\n"
    "s_branch .Lb2_pd4_%=\n"
    ".Lb2_sp5_%=:\n"
    "s_sleep 8\n"
    "s_branch .Lb2_po3_%=\n"
    ".Lb2_pd4_%=:\n"
    "s_branch .Lb2_tk1_%=\n"
    ".Lb2_out_%=:\n"
    "v_cvt_pk_bf16_f32 v56, v0, v1\n"
    "v_cvt_pk_bf16_f32 v57, v2, v3\n"
    "v_cvt_pk_bf16_f32 v58, v4, v5\n"
    "v_cvt_pk_bf16_f32 v59, v6, v7\n"
    "v_cvt_pk_bf16_f32 v60, v8, v9\n"
    "v_cvt_pk_bf16_f32 v61, v10, v11\n"
    "v_cvt_pk_bf16_f32 v62, v12, v13\n"
    "v_cvt_pk_bf16_f32 v63, v14, v15\n"
    "buffer_store_dwordx4 v[56:59], v228, s[8:11], s30 offen\n"
    "buffer_store_dwordx4 v[60:63], v229, s[8:11], s30 offen\n"
    "v_mov_b32 v84, 0\n"
    "s_branch .Lb2_nx_%=\n"
    ".Lb2_sr_%=:\n"
    "s_add_u32 s20, s20, 1\n"
    "s_cmp_lt_u32 s20, 0x40000\n"
    "s_cbranch_scc1 .Lb2_sp21_%=\n"
    "v_mov_b32 v84, 72\n"
    "s_mov_b32 s24, 33728\n"
    "buffer_store_dword v84, off, s[0:3], s24 sc1\n"
    "s_waitcnt vmcnt(0)\n"
    "s_branch .Lb2_sg_%=\n"
    ".Lb2_sp21_%=:\n"
    "s_sleep 2\n"
    "buffer_load_dwordx4 v[64:67], v231, s[0:3], s32 offen sc1\n"
    "buffer_load_dwordx4 v[68:71], v232, s[0:3], s32 offen sc1\n"
    "buffer_load_dwordx4 v[72:75], v233, s[0:3], s32 offen sc1\n"
    "buffer_load_dwordx4 v[76:79], v234, s[0:3], s32 offen sc1\n"
    "buffer_load_dwordx4 v[80:83], v235, s[0:3], s32 offen sc1\n"
    "s_waitcnt vmcnt(0)\n"
    "s_branch .Lb2_sl_%=\n"
    :: "{s[0:3]}"(s0_), "{s[4:7]}"(s4_), "{s[8:11]}"(s8_), "{s[12:15]}"(s12_), "{s[16:19]}"(s16_), "{v[0:3]}"(v0_), "{v[4:7]}"(v4_), "{v[224:227]}"(v224_), "{v[228:231]}"(v228_), "{v[232:235]}"(v232_) : "memory", "v255", "a255", "s95");
    __builtin_unreachable();
}
#define SWP 112 - ((k - 24) >> 2) * 16
#define BOX A.ar - 262144
#define NP 4
constexpr int MEGA_LDS = 163840;
__global__ void __launch_bounds__(256, 1) mega_kernel(MegaArgs A) {
    extern __shared__ __attribute__((aligned(16))) char smem[];
    int* sRole = (int*)smem;
    asm volatile("" :: "s"(A.x0), "s"(A.mem0), "s"(A.Wq), "s"(A.Wk), "s"(A.Wv), "s"(A.Wo), "s"(A.wqkv), "s"(A.wo));
    const int x4 = (int)(__builtin_amdgcn_s_getreg((3 << 11) | 20) & 3u);
    const rsrc_t rf = mk_rsrc(arena_base(A.ar), ARENA);
    unsigned* const cnt = (unsigned*)(arena_base(A.ar) + xl(Z_TK, 0));
    u64* const box = (u64*)(BOX);
    const int ln = threadIdx.x & 63;
    static_assert(2 * (Z_END - Z_TK) == 256 * 640, "");
    if (threadIdx.x < 64) {
        bst16(rf, ln < 40 ? 2 * Z_TK + blockIdx.x * 640 + ln * 16 : OOB, u32x4{0u, 0u, 0u, 0u});
        int want;
        if (blockIdx.x == 0) {
            DRAIN();
            u64 sent[4];
            for (int n = 0;; ++n) {
                u64 v[4];
#pragma unroll
                for (int k = 0; k < 4; ++k) v[k] = __hip_atomic_load(box + 8 * (((64 * k + ln) & 31) * 8 + ((64 * k + ln) >> 5)), RLX_AGENT);
                const unsigned c = n < 2 ? 0u : __hip_atomic_load(cnt + 16 * (ln & 7), RLX_AGENT);
                const unsigned open = (unsigned)__ballot(c < 32u) & 255u;
                want = __ffs(open) - 1;
                if ((open & (open - 1)) == 0 && __all(c >= 31u)) break;
#pragma unroll
                for (int k = 0; k < 4; ++k) { if (n == 0 || v[k] != sent[k]) __hip_atomic_store(box + 2048 + 64 * k + ln, v[k], RLX_AGENT); sent[k] = v[k]; }
                if (n > 0) __builtin_amdgcn_s_sleep(16);
            }
        } else {
            const u64 mine = __builtin_amdgcn_s_memrealtime();
            if (threadIdx.x == 0) __hip_atomic_store(box + 8 * blockIdx.x, mine, RLX_AGENT);
            unsigned tm[2] = {~0u, ~0u};
            u64* const in = box + 2048 + (blockIdx.x & 7) * 32 + (blockIdx.x >> 3);
            u64 back;
            const unsigned pr = (blockIdx.x >> 7) * 16384 + (blockIdx.x & 127) * 64;
            if (threadIdx.x < 2) bst4(rf, pr + threadIdx.x * 8192, 0u);
            DRAIN();
#pragma unroll
            for (int k = 0; k < NP; ++k) {
                const unsigned long long t0 = __builtin_amdgcn_s_memtime();
                if (threadIdx.x == 0) bst4(rf, pr + (k & 1) * 8192, 0u);
                if (k < NP - 1) DRAIN();
                else asm volatile("global_load_dwordx2 %0, %1, %2 sc1\n\ts_waitcnt vmcnt(1)" : "=&v"(back) : "v"(0u), "s"(in) : "memory");
                tm[k & 1] = min(tm[k & 1], (unsigned)(__builtin_amdgcn_s_memtime() - t0));
            }
            asm volatile("s_waitcnt vmcnt(0)" : "+v"(back) :: "memory");
            while (back != mine) { __builtin_amdgcn_s_sleep(2); back = __hip_atomic_load(in, RLX_AGENT); }
            want = (tm[1] < tm[0] ? 4 : 0) + x4;
        }
        asm volatile("" ::: "memory");
        if (threadIdx.x == 0) {
            int role = -1;
            for (int p = -1; role < 0; ++p) {
                const int pool = p < 0 ? want : p, x = pool & 3;
                const int k = (int)__hip_atomic_fetch_add(cnt + 16 * pool, 1u, RLX_AGENT);
                if (k < 32) role = (pool >> 2) * 128 + (k < 24 ? (k >> 3) * 32 + 8 * x + (k & 7) : SWP + 4 * x + (k & 3));
            }
            *sRole = role;
        }
    }
    __syncthreads();
    const int role = RFL(*sRole), L = role >> 7, r = role & 127;
    __syncthreads();
    const bool on = (A.layers >> L) & 1;
    if (r < 32) { if (on) qk_role(A, L, r, x4); }
    else if (r < 64) { if (on) vo_role(A, L, r - 32, x4); }
    else if (r < 96) ct_role(A, L, r - 64, smem);
    else if (r < 112) { if (on) { if (r < 107) b1_role(A, L, r - 96, x4); else b2_role(A, L, r - 107, x4); } }
    else qw_role(A, L, 8 * ((r - 112) >> 2) + 4 * L + (r & 3), x4);
}

#define LAYERS 3
extern "C" void kernel_launch(void* const* d_in, const int* in_sizes, int n_in, void* d_out, int out_size, void* d_ws, size_t ws_size, hipStream_t stream) {
    MegaArgs A;
    A.x0 = (const bf16_t*)d_in[0]; A.mem0 = (const bf16_t*)d_in[1]; A.Wq = (const bf16_t*)d_in[2]; A.Wk = (const bf16_t*)d_in[3]; A.Wv = (const bf16_t*)d_in[4];
    A.Wo = (const bf16_t*)d_in[5]; A.wqkv = (const bf16_t*)d_in[8]; A.wo = (const bf16_t*)d_in[9];
    A.ar = (char*)d_ws + 266240;
    A.out = (bf16_t*)d_out; A.layers = LAYERS; A.pad = 0;
    (void)hipFuncSetAttribute((const void*)mega_kernel, hipFuncAttributeMaxDynamicSharedMemorySize, MEGA_LDS);
    mega_kernel<<<256, 256, MEGA_LDS, stream>>>(A);
}
```

```cpp
#include <hip/hip_runtime.h>
#include <hip/hip_bf16.h>
#include <stdint.h>

typedef unsigned short bf16_t;
typedef short bf16x8 __attribute__((ext_vector_type(8)));
typedef short bf16x4 __attribute__((ext_vector_type(4)));
typedef float f32x4 __attribute__((ext_vector_type(4)));
typedef unsigned u32x4 __attribute__((ext_vector_type(4)));
typedef unsigned u32x2 __attribute__((ext_vector_type(2)));
typedef unsigned long long u64;
typedef __bf16 bf16v2 __attribute__((ext_vector_type(2)));
typedef float f32v2 __attribute__((ext_vector_type(2)));
#define DEV __device__ __forceinline__
DEV unsigned pk2(float a, float b) { f32v2 f = {a, b}; bf16v2 r = __builtin_convertvector(f, bf16v2); return __builtin_bit_cast(unsigned, r); }
DEV float rb(float a) { return __uint_as_float((pk2(a, 0.f) & 0xffffu) << 16); }
DEV float lo16(unsigned u) { return __uint_as_float(u << 16); }
DEV float hi16(unsigned u) { return __uint_as_float(u & 0xffff0000u); }
constexpr int D_ = 640;
#define RLX_AGENT __ATOMIC_RELAXED, __HIP_MEMORY_SCOPE_AGENT
#define AUX_SC1 16
typedef __amdgpu_buffer_rsrc_t rsrc_t;
DEV rsrc_t mk_rsrc(const void* p, unsigned bytes) { return __builtin_amdgcn_make_buffer_rsrc((void*)p, 0, (int)bytes, 0x00020000); }
DEV void bst16(rsrc_t r, unsigned off, u32x4 v) { __builtin_amdgcn_raw_buffer_store_b128(v, r, off, 0, AUX_SC1); }
DEV void bst8(rsrc_t r, unsigned off, u32x2 v) { __builtin_amdgcn_raw_buffer_store_b64(v, r, off, 0, AUX_SC1); }
DEV void bst4(rsrc_t r, unsigned off, unsigned v) { __builtin_amdgcn_raw_buffer_store_b32(v, r, off, 0, AUX_SC1); }
#define DRAIN() asm volatile("s_waitcnt vmcnt(0)" ::: "memory")
#define DRAIN_BARRIER() asm volatile("s_waitcnt vmcnt(0) lgkmcnt(0)\n\ts_barrier" ::: "memory")
#define RFL(x) ((unsigned)__builtin_amdgcn_readfirstlane((int)(x)))
DEV unsigned lo(const void* p) { return (unsigned)(size_t)p; }
DEV unsigned hi(const void* p) { return (unsigned)((size_t)p >> 32) & 0xffffu; }
DEV unsigned nosent(unsigned x) { return min(x, 0xfffffffeu); }
DEV int sw2(int x) { return ((x & 1) << 1) | (x >> 1); }

DEV unsigned xl(unsigned o, int side) { const unsigned ph = ((o >> 13) << 14) | (o & 8191u); return ph | ((((ph >> 21) ^ side) & 1u) << 13); }
constexpr unsigned Z_TK = 16384, Z_QV = Z_TK + 1024, Z_CT = Z_QV + 1024, Z_QW = Z_CT + 2048, Z_X = 3 * 8192, Z_X1 = 7 * 8192, Z_XN = 9 * 8192, Z_O = 10 * 8192, Z_ST = 11 * 8192, Z_END = 12 * 8192;
constexpr unsigned RG_UT = 1u << 21, RG_UTX = RG_UT + (1u << 20), RG_UM = RG_UTX + 262144, RG_UMX = RG_UM + 262144, RG_PT = RG_UMX + 65536, RG_PM = RG_PT + 65536, RG_MEM = RG_PM + 32768;
constexpr unsigned SLOT = 1u << 22;
constexpr unsigned REC0 = 20u << 20, RC_X1 = 5 * 16384, RC_O = 10 * 16384, RC_X2 = 15 * 16384, ST0 = 1u << 20, QT0 = 52u << 20, DBG0 = 116u << 20, ARENA = 120u << 20;
DEV unsigned rec(int s, int L) { return REC0 + (s << 19) + ((((s >> 2) ^ L) & 1) << 13); }
constexpr unsigned OOB = 0x7f000000u, WBYTES = 640u * 6144u * 2u;
DEV char* arena_base(char* p) { return p + ((0 - (size_t)p) & 0x3fffff); }

struct MegaArgs {
    const bf16_t* x0; const bf16_t* mem0; const bf16_t* Wq; const bf16_t* Wk; const bf16_t* Wv; const bf16_t* Wo; const bf16_t* wqkv; const bf16_t* wo;
    char* ar; bf16_t* out; int layers, pad;
};
DEV bool all_drawn(char* ar) { return __all(__hip_atomic_load((unsigned*)(arena_base(ar) + xl(Z_TK, 0)) + 16 * (threadIdx.x & 7), RLX_AGENT) >= 32u); }
DEV void wait_drawn(char* ar) { while (!all_drawn(ar)) __builtin_amdgcn_s_sleep(8); asm volatile("" ::: "memory"); }
#define ROLE_HEAD const int l = threadIdx.x & 63, l16 = l & 15, lq = l >> 4, w = RFL(threadIdx.x >> 6); char* const ab = arena_base(A.ar); \
    const rsrc_t rr = mk_rsrc(ab, ARENA); const u32x4 rx = {lo(ab), hi(ab), ARENA, 0x20000u}; const u32x4 ones = {~0u, ~0u, ~0u, ~0u}; const u32x2 ones2 = {~0u, ~0u}
DEV void qk_role(const MegaArgs& A, const int L, const int h, const int rep) {
    ROLE_HEAD;
#define XP 10
    const int i = 4 * (w & 1) + lq, p = l16 < 8 ? l16 >> 2 : l16 & 1, rho = 4 * (i >> 1) + 2 * p + (i & 1), tp = 4 * w + lq;
    const unsigned pm = (w >= 2 || l16 >= XP) ? OOB : xl(RG_PM + (h >> 3) * 8192 +
        (l16 < 8 ? ((h & 7) >> 1) * 1024 + ((2 * (h & 1) + ((l16 >> 1) & 1)) * 16 + rho) * 16 + (l16 & 1) * 8 : 4096 + (((h & 7) >> 1) * 16 + rho) * 16 + (h & 1) * 8), L);
    const unsigned pt = xl(RG_PT + h * 2048 + tp * 16 + (l16 < 8 ? (l16 & 3) * 256 + (l16 >> 2) * 8 : (l16 & 1) * 8 + (l16 < 10 ? 1792 : (l16 < 14 ? 1024 + ((l16 - 10) >> 1) * 256 : 1536))), L);
    u64 cm[4];
    const int k0 = (l16 >= 4 && l16 < 8) ? 4 * (l16 - 4) : ((l16 >= 10 && l16 < 14) ? 4 * (l16 - 10) : -4);
#pragma unroll
    for (int r = 0; r < 4; ++r) cm[r] = __ballot(k0 + r <= tp);
    const u32x4 s0_ = {RFL(rx[0]), RFL(rx[1]), RFL(rx[2]), RFL(rx[3])};
    const u32x4 s4_ = {RFL(lo(A.Wk + h * 192)), RFL(hi(A.Wk + h * 192)), RFL(WBYTES - h * 384), RFL(0x20000u)};
    const u32x4 s8_ = {RFL(lo(A.Wq + h * 192)), RFL(hi(A.Wq + h * 192)), RFL(WBYTES - h * 384), RFL(0x20000u)};
    const u32x4 s12_ = {RFL((unsigned)cm[0]), RFL((unsigned)(cm[0] >> 32)), RFL((unsigned)cm[1]), RFL((unsigned)(cm[1] >> 32))};
    const u32x4 s16_ = {RFL((unsigned)cm[2]), RFL((unsigned)(cm[2] >> 32)), RFL((unsigned)cm[3]), RFL((unsigned)(cm[3] >> 32))};
    const u32x4 s20_ = {RFL(w < 2 ? ~0u : 1u), RFL(w < 2 ? ~0u : 0u), RFL(w), RFL(L << 13)};
    const u32x4 s24_ = {RFL(xl(RG_MEM + rep * 16384, L) + w * 2048), RFL(w < 2 ? xl(RG_MEM + rep * 16384 + 8192, L) + w * 1024 - 2048 : OOB), RFL(w * 11264 + 8192), RFL(REC0 + w * 16384)};
    const u32x4 s28_ = {RFL(65536 - 4096 - w * 14336), RFL(QT0 + h * 32768 + w * 2048), RFL(h == 0 && w == 0 ? xl(Z_X, L) : OOB), RFL(xl(Z_X, L))};
    const u32x4 s32_ = {RFL(xl(Z_CT + (h & 7) * 256, L)), RFL(xl(Z_XN + rep * 2048, 0)), RFL(xl(Z_QW + h * 128, L)), RFL(w == 0 ? xl(Z_QV + h * 4, L) : OOB)};
    const u32x4 s36_ = {RFL(DBG0 + ((L * 128 + h) * 4 + w) * 2048), RFL(0u), RFL(0u), RFL(0u)};
    const u32x4 v0_ = {(unsigned)(((3 * w + 0) * 64 + l) / 24 * 12288 + ((((3 * w + 0) * 64 + l) % 24) ^ ((((3 * w + 0) * 64 + l) / 48) & 6)) * 16 - 0), (unsigned)(((3 * w + 1) * 64 + l) / 24 * 12288 + ((((3 * w + 1) * 64 + l) % 24) ^ ((((3 * w + 1) * 64 + l) / 48) & 6)) * 16 - 1024), (unsigned)(((3 * w + 2) * 64 + l) / 24 * 12288 + ((((3 * w + 2) * 64 + l) % 24) ^ ((((3 * w + 2) * 64 + l) / 48) & 6)) * 16 - 2048), (unsigned)((8 * lq + ((l >> 2) & 1) + ((l >> 1) & 4)) * 384 + (w ^ (((8 * lq + ((l >> 2) & 1) + ((l >> 1) & 4)) >> 2) & 3)) * 32 + (l & 3) * 8 + ((w + 0) & 3) * 12288)};
    const u32x4 v4_ = {(unsigned)((8 * lq + ((l >> 2) & 1) + ((l >> 1) & 4)) * 384 + (w ^ (((8 * lq + ((l >> 2) & 1) + ((l >> 1) & 4)) >> 2) & 3)) * 32 + (l & 3) * 8 + ((w + 1) & 3) * 12288), (unsigned)((8 * lq + ((l >> 2) & 1) + ((l >> 1) & 4)) * 384 + (w ^ (((8 * lq + ((l >> 2) & 1) + ((l >> 1) & 4)) >> 2) & 3)) * 32 + (l & 3) * 8 + ((w + 2) & 3) * 12288), (unsigned)((8 * lq + ((l >> 2) & 1) + ((l >> 1) & 4)) * 384 + (w ^ (((8 * lq + ((l >> 2) & 1) + ((l >> 1) & 4)) >> 2) & 3)) * 32 + (l & 3) * 8 + ((w + 3) & 3) * 12288), (unsigned)((8 * lq + ((l >> 2) & 1) + ((l >> 1) & 4)) * 384 + (w ^ (((8 * lq + ((l >> 2) & 1) + ((l >> 1) & 4)) >> 2) & 3)) * 32 + (l & 3) * 8)};
    const u32x4 v92_ = {(unsigned)(l * 16 + ((w + 0) & 3) * 11264), (unsigned)(l * 16 + ((w + 1) & 3) * 11264), (unsigned)(l * 16 + ((w + 2) & 3) * 11264), (unsigned)(l * 16 + ((w + 3) & 3) * 11264)};
    const u32x4 v96_ = {(unsigned)(l * 16), (unsigned)(l * 16 + 53248 + w * 20480), (unsigned)((l & 2) ? 0xeeeeu : 0x4444u), (unsigned)(0xff800000u)};
    const u32x4 v100_ = {(unsigned)(l16 == 0 + 4 * lq ? 0x3f80u : (l16 == 0 + 4 * lq + 1 ? 0x3f800000u : 0u)), (unsigned)(l16 == 2 + 4 * lq ? 0x3f80u : (l16 == 2 + 4 * lq + 1 ? 0x3f800000u : 0u)), (unsigned)(135168 + w * 1936 + (2 * (l16 >> 2) + (l & 1)) * 240 + (l & 2) * 32 + lq * 16), (unsigned)(135168 + (l16 < 10 ? (4 * (w & 1) + lq) * 240 + l16 * 16 : 1920))};
    const u32x4 v104_ = {(unsigned)(142912 + w * 4352 + l16 * 272 + lq * 16), (unsigned)(142912 + (4 * w + lq) * 272 + l16 * 16), (unsigned)(pm), (unsigned)(pt)};
    asm volatile(
    "s_mov_b32 s40, 0\n"
    "s_mov_b64 s[42:43], 0\n"
    "s_mov_b32 s45, 0\n"
    "s_cmp_eq_u32 s23, 0\n"
    "s_cselect_b32 s44, 0, 28\n"
    "s_sub_u32 s44, s44, 0\n"
    ".Lqk_ho1_%=:\n"
    "s_sub_u32 s44, s44, 1\n"
    "s_cbranch_scc1 .Lqk_hd2_%=\n"
    "s_sleep 17\n"
    "s_branch .Lqk_ho1_%=\n"
    ".Lqk_hd2_%=:\n"
    "s_mul_i32 s51, s22, 3072\n"
    "s_mov_b32 s60, 0\n"
    "s_add_u32 m0, s51, 0\n"
    "s_nop 0\n"
    "buffer_load_dwordx4 v0, s[8:11], s60 offen offset:0 nt lds\n"
    "buffer_load_dwordx4 v1, s[8:11], s60 offen offset:1024 nt lds\n"
    "buffer_load_dwordx4 v2, s[8:11], s60 offen offset:2048 nt lds\n"
    "s_mov_b32 s60, 1572864\n"
    "s_add_u32 m0, s51, 12288\n"
    "s_nop 0\n"
    "buffer_load_dwordx4 v0, s[8:11], s60 offen offset:0 nt lds\n"
    "buffer_load_dwordx4 v1, s[8:11], s60 offen offset:1024 nt lds\n"
    "buffer_load_dwordx4 v2, s[8:11], s60 offen offset:2048 nt lds\n"
    "s_mov_b32 s60, 3145728\n"
    "s_add_u32 m0, s51, 24576\n"
    "s_nop 0\n"
    "buffer_load_dwordx4 v0, s[8:11], s60 offen offset:0 nt lds\n"
    "buffer_load_dwordx4 v1, s[8:11], s60 offen offset:1024 nt lds\n"
    "buffer_load_dwordx4 v2, s[8:11], s60 offen offset:2048 nt lds\n"
    "s_mov_b32 s60, 4718592\n"
    "s_add_u32 m0, s51, 36864\n"
    "s_nop 0\n"
    "buffer_load_dwordx4 v0, s[8:11], s60 offen offset:0 nt lds\n"
    "buffer_load_dwordx4 v1, s[8:11], s60 offen offset:1024 nt lds\n"
    "buffer_load_dwordx4 v2, s[8:11], s60 offen offset:2048 nt lds\n"
    "s_mov_b32 s60, 393216\n"
    "s_add_u32 m0, s51, 49152\n"
    "s_nop 0\n"
    "buffer_load_dwordx4 v0, s[8:11], s60 offen offset:0 nt lds\n"
    "buffer_load_dwordx4 v1, s[8:11], s60 offen offset:1024 nt lds\n"
    "buffer_load_dwordx4 v2, s[8:11], s60 offen offset:2048 nt lds\n"
    "s_mov_b32 s60, 1966080\n"
    "s_add_u32 m0, s51, 61440\n"
    "s_nop 0\n"
    "buffer_load_dwordx4 v0, s[8:11], s60 offen offset:0 nt lds\n"
    "buffer_load_dwordx4 v1, s[8:11], s60 offen offset:1024 nt lds\n"
    "buffer_load_dwordx4 v2, s[8:11], s60 offen offset:2048 nt lds\n"
    "s_mov_b32 s60, 3538944\n"
    "s_add_u32 m0, s51, 73728\n"
    "s_nop 0\n"
    "buffer_load_dwordx4 v0, s[8:11], s60 offen offset:0 nt lds\n"
    "buffer_load_dwordx4 v1, s[8:11], s60 offen offset:1024 nt lds\n"
    "buffer_load_dwordx4 v2, s[8:11], s60 offen offset:2048 nt lds\n"
    "s_mov_b32 s60, 5111808\n"
    "s_add_u32 m0, s51, 86016\n"
    "s_nop 0\n"
    "buffer_load_dwordx4 v0, s[8:11], s60 offen offset:0 nt lds\n"
    "buffer_load_dwordx4 v1, s[8:11], s60 offen offset:1024 nt lds\n"
    "buffer_load_dwordx4 v2, s[8:11], s60 offen offset:2048 nt lds\n"
    "s_mov_b32 s60, 786432\n"
    "s_add_u32 m0, s51, 98304\n"
    "s_nop 0\n"
    "buffer_load_dwordx4 v0, s[8:11], s60 offen offset:0 nt lds\n"
    "buffer_load_dwordx4 v1, s[8:11], s60 offen offset:1024 nt lds\n"
    "buffer_load_dwordx4 v2, s[8:11], s60 offen offset:2048 nt lds\n"
    "s_mov_b32 s60, 2359296\n"
    "s_add_u32 m0, s51, 110592\n"
    "s_nop 0\n"
    "buffer_load_dwordx4 v0, s[8:11], s60 offen offset:0 nt lds\n"
    "buffer_load_dwordx4 v1, s[8:11], s60 offen offset:1024 nt lds\n"
    "buffer_load_dwordx4 v2, s[8:11], s60 offen offset:2048 nt lds\n"
    "s_mov_b32 s60, 3932160\n"
    "s_add_u32 m0, s51, 122880\n"
    "s_nop 0\n"
    "buffer_load_dwordx4 v0, s[8:11], s60 offen offset:0 nt lds\n"
    "buffer_load_dwordx4 v1, s[8:11], s60 offen offset:1024 nt lds\n"
    "buffer_load_dwordx4 v2, s[8:11], s60 offen offset:2048 nt lds\n"
    "s_mov_b32 s60, 5505024\n"
    "s_add_u32 m0, s51, 135168\n"
    "s_nop 0\n"
    "buffer_load_dwordx4 v0, s[8:11], s60 offen offset:0 nt lds\n"
    "buffer_load_dwordx4 v1, s[8:11], s60 offen offset:1024 nt lds\n"
    "buffer_load_dwordx4 v2, s[8:11], s60 offen offset:2048 nt lds\n"
    "v_mov_b64 v[108:109], -1\n"
    "buffer_store_dwordx2 v[108:109], v106, s[0:3], 0 offen sc1\n"
    "buffer_store_dwordx2 v[108:109], v107, s[0:3], 0 offen sc1\n"
    "s_waitcnt vmcnt(26)\n"
    "s_barrier\n"
    "v_add_u32 v8, 0, v3\n"
    "ds_read_b64_tr_b16 a[240:241], v8 offset:0\n"
    "ds_read_b64_tr_b16 a[242:243], v8 offset:768\n"
    "ds_read_b64_tr_b16 v[176:177], v8 offset:128\n"
    "ds_read_b64_tr_b16 v[178:179], v8 offset:896\n"
    "ds_read_b64_tr_b16 v[12:13], v8 offset:256\n"
    "ds_read_b64_tr_b16 v[14:15], v8 offset:1024\n"
    "v_add_u32 v9, 0, v4\n"
    "ds_read_b64_tr_b16 v[112:113], v9 offset:0\n"
    "ds_read_b64_tr_b16 v[114:115], v9 offset:768\n"
    "ds_read_b64_tr_b16 v[192:193], v9 offset:128\n"
    "ds_read_b64_tr_b16 v[194:195], v9 offset:896\n"
    "ds_read_b64_tr_b16 v[28:29], v9 offset:256\n"
    "ds_read_b64_tr_b16 v[30:31], v9 offset:1024\n"
    "v_add_u32 v10, 0, v5\n"
    "ds_read_b64_tr_b16 v[128:129], v10 offset:0\n"
    "ds_read_b64_tr_b16 v[130:131], v10 offset:768\n"
    "ds_read_b64_tr_b16 v[208:209], v10 offset:128\n"
    "ds_read_b64_tr_b16 v[210:211], v10 offset:896\n"
    "ds_read_b64_tr_b16 v[44:45], v10 offset:256\n"
    "ds_read_b64_tr_b16 v[46:47], v10 offset:1024\n"
    "v_add_u32 v11, 0, v6\n"
    "ds_read_b64_tr_b16 v[144:145], v11 offset:0\n"
    "ds_read_b64_tr_b16 v[146:147], v11 offset:768\n"
    "ds_read_b64_tr_b16 v[224:225], v11 offset:128\n"
    "ds_read_b64_tr_b16 v[226:227], v11 offset:896\n"
    "ds_read_b64_tr_b16 v[60:61], v11 offset:256\n"
    "ds_read_b64_tr_b16 v[62:63], v11 offset:1024\n"
    "s_waitcnt lgkmcnt(0)\n"
    "s_barrier\n"
    "s_mov_b32 s60, 1179648\n"
    "s_add_u32 m0, s51, 0\n"
    "s_nop 0\n"
    "buffer_load_dwordx4 v0, s[8:11], s60 offen offset:0 nt lds\n"
    "buffer_load_dwordx4 v1, s[8:11], s60 offen offset:1024 nt lds\n"
    "buffer_load_dwordx4 v2, s[8:11], s60 offen offset:2048 nt lds\n"
    "s_mov_b32 s60, 2752512\n"
    "s_add_u32 m0, s51, 12288\n"
    "s_nop 0\n"
    "buffer_load_dwordx4 v0, s[8:11], s60 offen offset:0 nt lds\n"
    "buffer_load_dwordx4 v1, s[8:11], s60 offen offset:1024 nt lds\n"
    "buffer_load_dwordx4 v2, s[8:11], s60 offen offset:2048 nt lds\n"
    "s_mov_b32 s60, 4325376\n"
    "s_add_u32 m0, s51, 24576\n"
    "s_nop 0\n"
    "buffer_load_dwordx4 v0, s[8:11], s60 offen offset:0 nt lds\n"
    "buffer_load_dwordx4 v1, s[8:11], s60 offen offset:1024 nt lds\n"
    "buffer_load_dwordx4 v2, s[8:11], s60 offen offset:2048 nt lds\n"
    "s_mov_b32 s60, 5898240\n"
    "s_add_u32 m0, s51, 36864\n"
    "s_nop 0\n"
    "buffer_load_dwordx4 v0, s[8:11], s60 offen offset:0 nt lds\n"
    "buffer_load_dwordx4 v1, s[8:11], s60 offen offset:1024 nt lds\n"
    "buffer_load_dwordx4 v2, s[8:11], s60 offen offset:2048 nt lds\n"
    "v_and_b32 v108, 0x70, v96\n"
    "v_lshlrev_b32 v108, 2, v108\n"
    "v_or_b32 v108, 0x8000, v108\n"
    "buffer_load_dword v109, v108, s[0:3], 0 offen sc1\n"
    "s_waitcnt vmcnt(27)\n"
    "s_barrier\n"
    "v_add_u32 v8, 49152, v3\n"
    "ds_read_b64_tr_b16 a[244:245], v8 offset:0\n"
    "ds_read_b64_tr_b16 a[246:247], v8 offset:768\n"
    "ds_read_b64_tr_b16 v[180:181], v8 offset:128\n"
    "ds_read_b64_tr_b16 v[182:183], v8 offset:896\n"
    "ds_read_b64_tr_b16 v[16:17], v8 offset:256\n"
    "ds_read_b64_tr_b16 v[18:19], v8 offset:1024\n"
    "v_add_u32 v9, 49152, v4\n"
    "ds_read_b64_tr_b16 v[116:117], v9 offset:0\n"
    "ds_read_b64_tr_b16 v[118:119], v9 offset:768\n"
    "ds_read_b64_tr_b16 v[196:197], v9 offset:128\n"
    "ds_read_b64_tr_b16 v[198:199], v9 offset:896\n"
    "ds_read_b64_tr_b16 v[32:33], v9 offset:256\n"
    "ds_read_b64_tr_b16 v[34:35], v9 offset:1024\n"
    "v_add_u32 v10, 49152, v5\n"
    "ds_read_b64_tr_b16 v[132:133], v10 offset:0\n"
    "ds_read_b64_tr_b16 v[134:135], v10 offset:768\n"
    "ds_read_b64_tr_b16 v[212:213], v10 offset:128\n"
    "ds_read_b64_tr_b16 v[214:215], v10 offset:896\n"
    "ds_read_b64_tr_b16 v[48:49], v10 offset:256\n"
    "ds_read_b64_tr_b16 v[50:51], v10 offset:1024\n"
    "v_add_u32 v11, 49152, v6\n"
    "ds_read_b64_tr_b16 v[148:149], v11 offset:0\n"
    "ds_read_b64_tr_b16 v[150:151], v11 offset:768\n"
    "ds_read_b64_tr_b16 v[228:229], v11 offset:128\n"
    "ds_read_b64_tr_b16 v[230:231], v11 offset:896\n"
    "ds_read_b64_tr_b16 v[64:65], v11 offset:256\n"
    "ds_read_b64_tr_b16 v[66:67], v11 offset:1024\n"
    "s_waitcnt lgkmcnt(0)\n"
    "s_barrier\n"
    "s_mov_b32 s60, 6291456\n"
    "s_add_u32 m0, s51, 49152\n"
    "s_nop 0\n"
    "buffer_load_dwordx4 v0, s[8:11], s60 offen offset:0 nt lds\n"
    "buffer_load_dwordx4 v1, s[8:11], s60 offen offset:1024 nt lds\n"
    "buffer_load_dwordx4 v2, s[8:11], s60 offen offset:2048 nt lds\n"
    "s_mov_b32 s60, 6684672\n"
    "s_add_u32 m0, s51, 61440\n"
    "s_nop 0\n"
    "buffer_load_dwordx4 v0, s[8:11], s60 offen offset:0 nt lds\n"
    "buffer_load_dwordx4 v1, s[8:11], s60 offen offset:1024 nt lds\n"
    "buffer_load_dwordx4 v2, s[8:11], s60 offen offset:2048 nt lds\n"
    "s_mov_b32 s60, 7077888\n"
    "s_add_u32 m0, s51, 73728\n"
    "s_nop 0\n"
    "buffer_load_dwordx4 v0, s[8:11], s60 offen offset:0 nt lds\n"
    "buffer_load_dwordx4 v1, s[8:11], s60 offen offset:1024 nt lds\n"
    "buffer_load_dwordx4 v2, s[8:11], s60 offen offset:2048 nt lds\n"
    "s_mov_b32 s60, 7471104\n"
    "s_add_u32 m0, s51, 86016\n"
    "s_nop 0\n"
    "buffer_load_dwordx4 v0, s[8:11], s60 offen offset:0 nt lds\n"
    "buffer_load_dwordx4 v1, s[8:11], s60 offen offset:1024 nt lds\n"
    "buffer_load_dwordx4 v2, s[8:11], s60 offen offset:2048 nt lds\n"
    "s_waitcnt vmcnt(27)\n"
    "s_barrier\n"
    "v_add_u32 v8, 98304, v3\n"
    "ds_read_b64_tr_b16 a[248:249], v8 offset:0\n"
    "ds_read_b64_tr_b16 a[250:251], v8 offset:768\n"
    "ds_read_b64_tr_b16 v[184:185], v8 offset:128\n"
    "ds_read_b64_tr_b16 v[186:187], v8 offset:896\n"
    "ds_read_b64_tr_b16 v[20:21], v8 offset:256\n"
    "ds_read_b64_tr_b16 v[22:23], v8 offset:1024\n"
    "v_add_u32 v9, 98304, v4\n"
    "ds_read_b64_tr_b16 v[120:121], v9 offset:0\n"
    "ds_read_b64_tr_b16 v[122:123], v9 offset:768\n"
    "ds_read_b64_tr_b16 v[200:201], v9 offset:128\n"
    "ds_read_b64_tr_b16 v[202:203], v9 offset:896\n"
    "ds_read_b64_tr_b16 v[36:37], v9 offset:256\n"
    "ds_read_b64_tr_b16 v[38:39], v9 offset:1024\n"
    "v_add_u32 v10, 98304, v5\n"
    "ds_read_b64_tr_b16 v[136:137], v10 offset:0\n"
    "ds_read_b64_tr_b16 v[138:139], v10 offset:768\n"
    "ds_read_b64_tr_b16 v[216:217], v10 offset:128\n"
    "ds_read_b64_tr_b16 v[218:219], v10 offset:896\n"
    "ds_read_b64_tr_b16 v[52:53], v10 offset:256\n"
    "ds_read_b64_tr_b16 v[54:55], v10 offset:1024\n"
    "v_add_u32 v11, 98304, v6\n"
    "ds_read_b64_tr_b16 v[152:153], v11 offset:0\n"
    "ds_read_b64_tr_b16 v[154:155], v11 offset:768\n"
    "ds_read_b64_tr_b16 v[232:233], v11 offset:128\n"
    "ds_read_b64_tr_b16 v[234:235], v11 offset:896\n"
    "ds_read_b64_tr_b16 v[68:69], v11 offset:256\n"
    "ds_read_b64_tr_b16 v[70:71], v11 offset:1024\n"
    "s_waitcnt lgkmcnt(0)\n"
    "s_barrier\n"
    "s_mov_b32 s60, 0\n"
    "s_add_u32 m0, s51, 98304\n"
    "s_nop 0\n"
    "buffer_load_dwordx4 v0, s[4:7], s60 offen offset:0 nt lds\n"
    "buffer_load_dwordx4 v1, s[4:7], s60 offen offset:1024 nt lds\n"
    "buffer_load_dwordx4 v2, s[4:7], s60 offen offset:2048 nt lds\n"
    "s_mov_b32 s60, 1572864\n"
    "s_add_u32 m0, s51, 110592\n"
    "s_nop 0\n"
    "buffer_load_dwordx4 v0, s[4:7], s60 offen offset:0 nt lds\n"
    "buffer_load_dwordx4 v1, s[4:7], s60 offen offset:1024 nt lds\n"
    "buffer_load_dwordx4 v2, s[4:7], s60 offen offset:2048 nt lds\n"
    "s_mov_b32 s60, 3145728\n"
    "s_add_u32 m0, s51, 122880\n"
    "s_nop 0\n"
    "buffer_load_dwordx4 v0, s[4:7], s60 offen offset:0 nt lds\n"
    "buffer_load_dwordx4 v1, s[4:7], s60 offen offset:1024 nt lds\n"
    "buffer_load_dwordx4 v2, s[4:7], s60 offen offset:2048 nt lds\n"
    "s_mov_b32 s60, 4718592\n"
    "s_add_u32 m0, s51, 135168\n"
    "s_nop 0\n"
    "buffer_load_dwordx4 v0, s[4:7], s60 offen offset:0 nt lds\n"
    "buffer_load_dwordx4 v1, s[4:7], s60 offen offset:1024 nt lds\n"
    "buffer_load_dwordx4 v2, s[4:7], s60 offen offset:2048 nt lds\n"
    "s_waitcnt vmcnt(25)\n"
    "s_barrier\n"
    "v_add_u32 v8, 0, v3\n"
    "ds_read_b64_tr_b16 a[252:253], v8 offset:0\n"
    "ds_read_b64_tr_b16 a[254:255], v8 offset:768\n"
    "ds_read_b64_tr_b16 v[188:189], v8 offset:128\n"
    "ds_read_b64_tr_b16 v[190:191], v8 offset:896\n"
    "ds_read_b64_tr_b16 v[24:25], v8 offset:256\n"
    "ds_read_b64_tr_b16 v[26:27], v8 offset:1024\n"
    "v_add_u32 v9, 0, v4\n"
    "ds_read_b64_tr_b16 v[124:125], v9 offset:0\n"
    "ds_read_b64_tr_b16 v[126:127], v9 offset:768\n"
    "ds_read_b64_tr_b16 v[204:205], v9 offset:128\n"
    "ds_read_b64_tr_b16 v[206:207], v9 offset:896\n"
    "ds_read_b64_tr_b16 v[40:41], v9 offset:256\n"
    "ds_read_b64_tr_b16 v[42:43], v9 offset:1024\n"
    "v_add_u32 v10, 0, v5\n"
    "ds_read_b64_tr_b16 v[140:141], v10 offset:0\n"
    "ds_read_b64_tr_b16 v[142:143], v10 offset:768\n"
    "ds_read_b64_tr_b16 v[220:221], v10 offset:128\n"
    "ds_read_b64_tr_b16 v[222:223], v10 offset:896\n"
    "ds_read_b64_tr_b16 v[56:57], v10 offset:256\n"
    "ds_read_b64_tr_b16 v[58:59], v10 offset:1024\n"
    "v_add_u32 v11, 0, v6\n"
    "ds_read_b64_tr_b16 v[156:157], v11 offset:0\n"
    "ds_read_b64_tr_b16 v[158:159], v11 offset:768\n"
    "ds_read_b64_tr_b16 v[236:237], v11 offset:128\n"
    "ds_read_b64_tr_b16 v[238:239], v11 offset:896\n"
    "ds_read_b64_tr_b16 v[72:73], v11 offset:256\n"
    "ds_read_b64_tr_b16 v[74:75], v11 offset:1024\n"
    "s_waitcnt lgkmcnt(0)\n"
    "s_barrier\n"
    "s_mov_b32 s60, 393216\n"
    "s_add_u32 m0, s51, 0\n"
    "s_nop 0\n"
    "buffer_load_dwordx4 v0, s[4:7], s60 offen offset:0 nt lds\n"
    "buffer_load_dwordx4 v1, s[4:7], s60 offen offset:1024 nt lds\n"
    "buffer_load_dwordx4 v2, s[4:7], s60 offen offset:2048 nt lds\n"
    "s_mov_b32 s60, 1966080\n"
    "s_add_u32 m0, s51, 12288\n"
    "s_nop 0\n"
    "buffer_load_dwordx4 v0, s[4:7], s60 offen offset:0 nt lds\n"
    "buffer_load_dwordx4 v1, s[4:7], s60 offen offset:1024 nt lds\n"
    "buffer_load_dwordx4 v2, s[4:7], s60 offen offset:2048 nt lds\n"
    "s_mov_b32 s60, 3538944\n"
    "s_add_u32 m0, s51, 24576\n"
    "s_nop 0\n"
    "buffer_load_dwordx4 v0, s[4:7], s60 offen offset:0 nt lds\n"
    "buffer_load_dwordx4 v1, s[4:7], s60 offen offset:1024 nt lds\n"
    "buffer_load_dwordx4 v2, s[4:7], s60 offen offset:2048 nt lds\n"
    "s_mov_b32 s60, 5111808\n"
    "s_add_u32 m0, s51, 36864\n"
    "s_nop 0\n"
    "buffer_load_dwordx4 v0, s[4:7], s60 offen offset:0 nt lds\n"
    "buffer_load_dwordx4 v1, s[4:7], s60 offen offset:1024 nt lds\n"
    "buffer_load_dwordx4 v2, s[4:7], s60 offen offset:2048 nt lds\n"
    "s_waitcnt vmcnt(24)\n"
    "s_barrier\n"
    "v_add_u32 v8, 49152, v7\n"
    "ds_read_b64_tr_b16 v[160:161], v8 offset:0\n"
    "ds_read_b64_tr_b16 v[162:163], v8 offset:768\n"
    "ds_read_b64_tr_b16 v[240:241], v8 offset:128\n"
    "ds_read_b64_tr_b16 v[242:243], v8 offset:896\n"
    "ds_read_b64_tr_b16 v[76:77], v8 offset:256\n"
    "ds_read_b64_tr_b16 v[78:79], v8 offset:1024\n"
    "v_add_u32 v9, 61440, v7\n"
    "ds_read_b64_tr_b16 v[164:165], v9 offset:0\n"
    "ds_read_b64_tr_b16 v[166:167], v9 offset:768\n"
    "ds_read_b64_tr_b16 v[244:245], v9 offset:128\n"
    "ds_read_b64_tr_b16 v[246:247], v9 offset:896\n"
    "ds_read_b64_tr_b16 v[80:81], v9 offset:256\n"
    "ds_read_b64_tr_b16 v[82:83], v9 offset:1024\n"
    "v_add_u32 v10, 73728, v7\n"
    "ds_read_b64_tr_b16 v[168:169], v10 offset:0\n"
    "ds_read_b64_tr_b16 v[170:171], v10 offset:768\n"
    "ds_read_b64_tr_b16 v[248:249], v10 offset:128\n"
    "ds_read_b64_tr_b16 v[250:251], v10 offset:896\n"
    "ds_read_b64_tr_b16 v[84:85], v10 offset:256\n"
    "ds_read_b64_tr_b16 v[86:87], v10 offset:1024\n"
    "v_add_u32 v11, 86016, v7\n"
    "ds_read_b64_tr_b16 v[172:173], v11 offset:0\n"
    "ds_read_b64_tr_b16 v[174:175], v11 offset:768\n"
    "ds_read_b64_tr_b16 v[252:253], v11 offset:128\n"
    "ds_read_b64_tr_b16 v[254:255], v11 offset:896\n"
    "ds_read_b64_tr_b16 v[88:89], v11 offset:256\n"
    "ds_read_b64_tr_b16 v[90:91], v11 offset:1024\n"
    "s_waitcnt lgkmcnt(0)\n"
    "s_barrier\n"
    "s_waitcnt vmcnt(36)\n"
    "v_cmp_gt_i32 vcc, 32, v109\n"
    "s_nop 0\n"
    "s_cbranch_vccnz .Lqk_tk0_%=\n"
    ".Lqk_tk1_%=:\n"
    "s_mov_b32 s60, 786432\n"
    "s_add_u32 m0, s51, 49152\n"
    "s_nop 0\n"
    "buffer_load_dwordx4 v0, s[4:7], s60 offen offset:0 nt lds\n"
    "buffer_load_dwordx4 v1, s[4:7], s60 offen offset:1024 nt lds\n"
    "buffer_load_dwordx4 v2, s[4:7], s60 offen offset:2048 nt lds\n"
    "s_mov_b32 s60, 2359296\n"
    "s_add_u32 m0, s51, 61440\n"
    "s_nop 0\n"
    "buffer_load_dwordx4 v0, s[4:7], s60 offen offset:0 nt lds\n"
    "buffer_load_dwordx4 v1, s[4:7], s60 offen offset:1024 nt lds\n"
    "buffer_load_dwordx4 v2, s[4:7], s60 offen offset:2048 nt lds\n"
    "s_mov_b32 s60, 3932160\n"
    "s_add_u32 m0, s51, 73728\n"
    "s_nop 0\n"
    "buffer_load_dwordx4 v0, s[4:7], s60 offen offset:0 nt lds\n"
    "buffer_load_dwordx4 v1, s[4:7], s60 offen offset:1024 nt lds\n"
    "buffer_load_dwordx4 v2, s[4:7], s60 offen offset:2048 nt lds\n"
    "s_mov_b32 s60, 5505024\n"
    "s_add_u32 m0, s51, 86016\n"
    "s_nop 0\n"
    "buffer_load_dwordx4 v0, s[4:7], s60 offen offset:0 nt lds\n"
    "buffer_load_dwordx4 v1, s[4:7], s60 offen offset:1024 nt lds\n"
    "buffer_load_dwordx4 v2, s[4:7], s60 offen offset:2048 nt lds\n"
    "v_lshlrev_b32 v108, 4, v96\n"
    "v_mov_b32 v109, 1\n"
    "s_mov_b64 exec, 15\n"
    "buffer_store_dword v109, v108, s[0:3], s35 offen sc1\n"
    "s_mov_b64 exec, -1\n"
    "s_waitcnt vmcnt(25)\n"
    "s_barrier\n"
    "v_add_u32 v8, 98304, v3\n"
    "ds_read_b64_tr_b16 a[0:1], v8 offset:0\n"
    "ds_read_b64_tr_b16 a[2:3], v8 offset:768\n"
    "ds_read_b64_tr_b16 a[80:81], v8 offset:128\n"
    "ds_read_b64_tr_b16 a[82:83], v8 offset:896\n"
    "ds_read_b64_tr_b16 a[160:161], v8 offset:256\n"
    "ds_read_b64_tr_b16 a[162:163], v8 offset:1024\n"
    "v_add_u32 v9, 98304, v4\n"
    "ds_read_b64_tr_b16 a[16:17], v9 offset:0\n"
    "ds_read_b64_tr_b16 a[18:19], v9 offset:768\n"
    "ds_read_b64_tr_b16 a[96:97], v9 offset:128\n"
    "ds_read_b64_tr_b16 a[98:99], v9 offset:896\n"
    "ds_read_b64_tr_b16 a[176:177], v9 offset:256\n"
    "ds_read_b64_tr_b16 a[178:179], v9 offset:1024\n"
    "v_add_u32 v10, 98304, v5\n"
    "ds_read_b64_tr_b16 a[32:33], v10 offset:0\n"
    "ds_read_b64_tr_b16 a[34:35], v10 offset:768\n"
    "ds_read_b64_tr_b16 a[112:113], v10 offset:128\n"
    "ds_read_b64_tr_b16 a[114:115], v10 offset:896\n"
    "ds_read_b64_tr_b16 a[192:193], v10 offset:256\n"
    "ds_read_b64_tr_b16 a[194:195], v10 offset:1024\n"
    "v_add_u32 v11, 98304, v6\n"
    "ds_read_b64_tr_b16 a[48:49], v11 offset:0\n"
    "ds_read_b64_tr_b16 a[50:51], v11 offset:768\n"
    "ds_read_b64_tr_b16 a[128:129], v11 offset:128\n"
    "ds_read_b64_tr_b16 a[130:131], v11 offset:896\n"
    "ds_read_b64_tr_b16 a[208:209], v11 offset:256\n"
    "ds_read_b64_tr_b16 a[210:211], v11 offset:1024\n"
    "s_waitcnt lgkmcnt(0)\n"
    "s_barrier\n"
    "s_mov_b32 s60, 1179648\n"
    "s_add_u32 m0, s51, 98304\n"
    "s_nop 0\n"
    "buffer_load_dwordx4 v0, s[4:7], s60 offen offset:0 nt lds\n"
    "buffer_load_dwordx4 v1, s[4:7], s60 offen offset:1024 nt lds\n"
    "buffer_load_dwordx4 v2, s[4:7], s60 offen offset:2048 nt lds\n"
    "s_mov_b32 s60, 2752512\n"
    "s_add_u32 m0, s51, 110592\n"
    "s_nop 0\n"
    "buffer_load_dwordx4 v0, s[4:7], s60 offen offset:0 nt lds\n"
    "buffer_load_dwordx4 v1, s[4:7], s60 offen offset:1024 nt lds\n"
    "buffer_load_dwordx4 v2, s[4:7], s60 offen offset:2048 nt lds\n"
    "s_mov_b32 s60, 4325376\n"
    "s_add_u32 m0, s51, 122880\n"
    "s_nop 0\n"
    "buffer_load_dwordx4 v0, s[4:7], s60 offen offset:0 nt lds\n"
    "buffer_load_dwordx4 v1, s[4:7], s60 offen offset:1024 nt lds\n"
    "buffer_load_dwordx4 v2, s[4:7], s60 offen offset:2048 nt lds\n"
    "s_mov_b32 s60, 5898240\n"
    "s_add_u32 m0, s51, 135168\n"
    "s_nop 0\n"
    "buffer_load_dwordx4 v0, s[4:7], s60 offen offset:0 nt lds\n"
    "buffer_load_dwordx4 v1, s[4:7], s60 offen offset:1024 nt lds\n"
    "buffer_load_dwordx4 v2, s[4:7], s60 offen offset:2048 nt lds\n"
    "s_waitcnt vmcnt(25)\n"
    "s_barrier\n"
    "v_add_u32 v8, 0, v3\n"
    "ds_read_b64_tr_b16 a[4:5], v8 offset:0\n"
    "ds_read_b64_tr_b16 a[6:7], v8 offset:768\n"
    "ds_read_b64_tr_b16 a[84:85], v8 offset:128\n"
    "ds_read_b64_tr_b16 a[86:87], v8 offset:896\n"
    "ds_read_b64_tr_b16 a[164:165], v8 offset:256\n"
    "ds_read_b64_tr_b16 a[166:167], v8 offset:1024\n"
    "v_add_u32 v9, 0, v4\n"
    "ds_read_b64_tr_b16 a[20:21], v9 offset:0\n"
    "ds_read_b64_tr_b16 a[22:23], v9 offset:768\n"
    "ds_read_b64_tr_b16 a[100:101], v9 offset:128\n"
    "ds_read_b64_tr_b16 a[102:103], v9 offset:896\n"
    "ds_read_b64_tr_b16 a[180:181], v9 offset:256\n"
    "ds_read_b64_tr_b16 a[182:183], v9 offset:1024\n"
    "v_add_u32 v10, 0, v5\n"
    "ds_read_b64_tr_b16 a[36:37], v10 offset:0\n"
    "ds_read_b64_tr_b16 a[38:39], v10 offset:768\n"
    "ds_read_b64_tr_b16 a[116:117], v10 offset:128\n"
    "ds_read_b64_tr_b16 a[118:119], v10 offset:896\n"
    "ds_read_b64_tr_b16 a[196:197], v10 offset:256\n"
    "ds_read_b64_tr_b16 a[198:199], v10 offset:1024\n"
    "v_add_u32 v11, 0, v6\n"
    "ds_read_b64_tr_b16 a[52:53], v11 offset:0\n"
    "ds_read_b64_tr_b16 a[54:55], v11 offset:768\n"
    "ds_read_b64_tr_b16 a[132:133], v11 offset:128\n"
    "ds_read_b64_tr_b16 a[134:135], v11 offset:896\n"
    "ds_read_b64_tr_b16 a[212:213], v11 offset:256\n"
    "ds_read_b64_tr_b16 a[214:215], v11 offset:1024\n"
    "s_waitcnt lgkmcnt(0)\n"
    "s_barrier\n"
    "s_mov_b32 s60, 6291456\n"
    "s_add_u32 m0, s51, 0\n"
    "s_nop 0\n"
    "buffer_load_dwordx4 v0, s[4:7], s60 offen offset:0 nt lds\n"
    "buffer_load_dwordx4 v1, s[4:7], s60 offen offset:1024 nt lds\n"
    "buffer_load_dwordx4 v2, s[4:7], s60 offen offset:2048 nt lds\n"
    "s_mov_b32 s60, 6684672\n"
    "s_add_u32 m0, s51, 12288\n"
    "s_nop 0\n"
    "buffer_load_dwordx4 v0, s[4:7], s60 offen offset:0 nt lds\n"
    "buffer_load_dwordx4 v1, s[4:7], s60 offen offset:1024 nt lds\n"
    "buffer_load_dwordx4 v2, s[4:7], s60 offen offset:2048 nt lds\n"
    "s_mov_b32 s60, 7077888\n"
    "s_add_u32 m0, s51, 24576\n"
    "s_nop 0\n"
    "buffer_load_dwordx4 v0, s[4:7], s60 offen offset:0 nt lds\n"
    "buffer_load_dwordx4 v1, s[4:7], s60 offen offset:1024 nt lds\n"
    "buffer_load_dwordx4 v2, s[4:7], s60 offen offset:2048 nt lds\n"
    "s_mov_b32 s60, 7471104\n"
    "s_add_u32 m0, s51, 36864\n"
    "s_nop 0\n"
    "buffer_load_dwordx4 v0, s[4:7], s60 offen offset:0 nt lds\n"
    "buffer_load_dwordx4 v1, s[4:7], s60 offen offset:1024 nt lds\n"
    "buffer_load_dwordx4 v2, s[4:7], s60 offen offset:2048 nt lds\n"
    "s_waitcnt vmcnt(25)\n"
    "s_barrier\n"
    "v_add_u32 v8, 49152, v3\n"
    "ds_read_b64_tr_b16 a[8:9], v8 offset:0\n"
    "ds_read_b64_tr_b16 a[10:11], v8 offset:768\n"
    "ds_read_b64_tr_b16 a[88:89], v8 offset:128\n"
    "ds_read_b64_tr_b16 a[90:91], v8 offset:896\n"
    "ds_read_b64_tr_b16 a[168:169], v8 offset:256\n"
    "ds_read_b64_tr_b16 a[170:171], v8 offset:1024\n"
    "v_add_u32 v9, 49152, v4\n"
    "ds_read_b64_tr_b16 a[24:25], v9 offset:0\n"
    "ds_read_b64_tr_b16 a[26:27], v9 offset:768\n"
    "ds_read_b64_tr_b16 a[104:105], v9 offset:128\n"
    "ds_read_b64_tr_b16 a[106:107], v9 offset:896\n"
    "ds_read_b64_tr_b16 a[184:185], v9 offset:256\n"
    "ds_read_b64_tr_b16 a[186:187], v9 offset:1024\n"
    "v_add_u32 v10, 49152, v5\n"
    "ds_read_b64_tr_b16 a[40:41], v10 offset:0\n"
    "ds_read_b64_tr_b16 a[42:43], v10 offset:768\n"
    "ds_read_b64_tr_b16 a[120:121], v10 offset:128\n"
    "ds_read_b64_tr_b16 a[122:123], v10 offset:896\n"
    "ds_read_b64_tr_b16 a[200:201], v10 offset:256\n"
    "ds_read_b64_tr_b16 a[202:203], v10 offset:1024\n"
    "v_add_u32 v11, 49152, v6\n"
    "ds_read_b64_tr_b16 a[56:57], v11 offset:0\n"
    "ds_read_b64_tr_b16 a[58:59], v11 offset:768\n"
    "ds_read_b64_tr_b16 a[136:137], v11 offset:128\n"
    "ds_read_b64_tr_b16 a[138:139], v11 offset:896\n"
    "ds_read_b64_tr_b16 a[216:217], v11 offset:256\n"
    "ds_read_b64_tr_b16 a[218:219], v11 offset:1024\n"
    "s_waitcnt lgkmcnt(0)\n"
    "s_barrier\n"
    "s_waitcnt vmcnt(12)\n"
    "s_barrier\n"
    "v_add_u32 v8, 98304, v3\n"
    "ds_read_b64_tr_b16 a[12:13], v8 offset:0\n"
    "ds_read_b64_tr_b16 a[14:15], v8 offset:768\n"
    "ds_read_b64_tr_b16 a[92:93], v8 offset:128\n"
    "ds_read_b64_tr_b16 a[94:95], v8 offset:896\n"
    "ds_read_b64_tr_b16 a[172:173], v8 offset:256\n"
    "ds_read_b64_tr_b16 a[174:175], v8 offset:1024\n"
    "v_add_u32 v9, 98304, v4\n"
    "ds_read_b64_tr_b16 a[28:29], v9 offset:0\n"
    "ds_read_b64_tr_b16 a[30:31], v9 offset:768\n"
    "ds_read_b64_tr_b16 a[108:109], v9 offset:128\n"
    "ds_read_b64_tr_b16 a[110:111], v9 offset:896\n"
    "ds_read_b64_tr_b16 a[188:189], v9 offset:256\n"
    "ds_read_b64_tr_b16 a[190:191], v9 offset:1024\n"
    "v_add_u32 v10, 98304, v5\n"
    "ds_read_b64_tr_b16 a[44:45], v10 offset:0\n"
    "ds_read_b64_tr_b16 a[46:47], v10 offset:768\n"
    "ds_read_b64_tr_b16 a[124:125], v10 offset:128\n"
    "ds_read_b64_tr_b16 a[126:127], v10 offset:896\n"
    "ds_read_b64_tr_b16 a[204:205], v10 offset:256\n"
    "ds_read_b64_tr_b16 a[206:207], v10 offset:1024\n"
    "v_add_u32 v11, 98304, v6\n"
    "ds_read_b64_tr_b16 a[60:61], v11 offset:0\n"
    "ds_read_b64_tr_b16 a[62:63], v11 offset:768\n"
    "ds_read_b64_tr_b16 a[140:141], v11 offset:128\n"
    "ds_read_b64_tr_b16 a[142:143], v11 offset:896\n"
    "ds_read_b64_tr_b16 a[220:221], v11 offset:256\n"
    "ds_read_b64_tr_b16 a[222:223], v11 offset:1024\n"
    "s_waitcnt lgkmcnt(0)\n"
    "s_barrier\n"
    "s_waitcnt vmcnt(0)\n"
    "s_barrier\n"
    "v_add_u32 v8, 0, v7\n"
    "ds_read_b64_tr_b16 a[64:65], v8 offset:0\n"
    "ds_read_b64_tr_b16 a[66:67], v8 offset:768\n"
    "ds_read_b64_tr_b16 a[144:145], v8 offset:128\n"
    "ds_read_b64_tr_b16 a[146:147], v8 offset:896\n"
    "ds_read_b64_tr_b16 a[224:225], v8 offset:256\n"
    "ds_read_b64_tr_b16 a[226:227], v8 offset:1024\n"
    "v_add_u32 v9, 12288, v7\n"
    "ds_read_b64_tr_b16 a[68:69], v9 offset:0\n"
    "ds_read_b64_tr_b16 a[70:71], v9 offset:768\n"
    "ds_read_b64_tr_b16 a[148:149], v9 offset:128\n"
    "ds_read_b64_tr_b16 a[150:151], v9 offset:896\n"
    "ds_read_b64_tr_b16 a[228:229], v9 offset:256\n"
    "ds_read_b64_tr_b16 a[230:231], v9 offset:1024\n"
    "v_add_u32 v10, 24576, v7\n"
    "ds_read_b64_tr_b16 a[72:73], v10 offset:0\n"
    "ds_read_b64_tr_b16 a[74:75], v10 offset:768\n"
    "ds_read_b64_tr_b16 a[152:153], v10 offset:128\n"
    "ds_read_b64_tr_b16 a[154:155], v10 offset:896\n"
    "ds_read_b64_tr_b16 a[232:233], v10 offset:256\n"
    "ds_read_b64_tr_b16 a[234:235], v10 offset:1024\n"
    "v_add_u32 v11, 36864, v7\n"
    "ds_read_b64_tr_b16 a[76:77], v11 offset:0\n"
    "ds_read_b64_tr_b16 a[78:79], v11 offset:768\n"
    "ds_read_b64_tr_b16 a[156:157], v11 offset:128\n"
    "ds_read_b64_tr_b16 a[158:159], v11 offset:896\n"
    "ds_read_b64_tr_b16 a[236:237], v11 offset:256\n"
    "ds_read_b64_tr_b16 a[238:239], v11 offset:1024\n"
    "s_waitcnt lgkmcnt(0)\n"
    "s_barrier\n"
    "ds_write_b128 v97, v[12:15] offset:0\n"
    "ds_write_b128 v97, v[16:19] offset:1024\n"
    "ds_write_b128 v97, v[20:23] offset:2048\n"
    "ds_write_b128 v97, v[24:27] offset:3072\n"
    "ds_write_b128 v97, v[28:31] offset:4096\n"
    "ds_write_b128 v97, v[32:35] offset:5120\n"
    "ds_write_b128 v97, v[36:39] offset:6144\n"
    "ds_write_b128 v97, v[40:43] offset:7168\n"
    "ds_write_b128 v97, v[44:47] offset:8192\n"
    "ds_write_b128 v97, v[48:51] offset:9216\n"
    "ds_write_b128 v97, v[52:55] offset:10240\n"
    "ds_write_b128 v97, v[56:59] offset:11264\n"
    "ds_write_b128 v97, v[60:63] offset:12288\n"
    "ds_write_b128 v97, v[64:67] offset:13312\n"
    "ds_write_b128 v97, v[68:71] offset:14336\n"
    "ds_write_b128 v97, v[72:75] offset:15360\n"
    "ds_write_b128 v97, v[76:79] offset:16384\n"
    "ds_write_b128 v97, v[80:83] offset:17408\n"
    "ds_write_b128 v97, v[84:87] offset:18432\n"
    "ds_write_b128 v97, v[88:91] offset:19456\n"
    "s_mov_b32 s46, -2\n"
    "s_mov_b32 s66, 1037384597\n"
    "s_mov_b32 s67, 1037384597\n"
    "s_mov_b32 s68, 858993459\n"
    "s_mov_b32 s69, 858993459\n"
    "s_mov_b32 s70, 67044351\n"
    "s_mov_b32 s71, 67044351\n"
    "v_mov_b32 v8, v99\n"
    "v_mov_b32 v9, v99\n"
    "v_mov_b32 v10, 137088\n"
    "ds_write_b64 v10, v[8:9] offset:0\n"
    "ds_write_b64 v10, v[8:9] offset:8\n"
    "ds_write_b64 v10, v[8:9] offset:1936\n"
    "ds_write_b64 v10, v[8:9] offset:1944\n"
    "ds_write_b64 v10, v[8:9] offset:3872\n"
    "ds_write_b64 v10, v[8:9] offset:3880\n"
    "ds_write_b64 v10, v[8:9] offset:5808\n"
    "ds_write_b64 v10, v[8:9] offset:5816\n"
    "s_lshl_b32 s59, s22, 11\n"
    "s_add_u32 s59, s59, 45056\n"
    "v_lshrrev_b32 v4, 2, v96\n"
    "v_and_b32 v6, 12, v4\n"
    "v_lshlrev_b32 v5, 1, v96\n"
    "v_and_b32 v5, 0x380, v5\n"
    "v_min_u32 v5, 0x200, v5\n"
    "v_add_u32 v5, v5, v6\n"
    "s_lshr_b32 s60, s23, 13\n"
    "s_mul_i32 s60, s60, 3\n"
    ".Lqk_po6_%=:\n"
    "buffer_load_dword v8, v4, s[0:3], s32 offen sc1\n"
    "s_waitcnt vmcnt(0)\n"
    "v_cmp_gt_i32 vcc, 1, v8\n"
    "s_nop 0\n"
    "s_cbranch_vccz .Lqk_pd7_%=\n"
    "s_add_u32 s40, s40, 1\n"
    "s_cmp_lt_u32 s40, 0x40000\n"
    "s_cbranch_scc1 .Lqk_sp8_%=\n"
    "v_mov_b32 v8, 20\n"
    "s_mov_b32 s44, 33728\n"
    "buffer_store_dword v8, off, s[0:3], s44 sc1\n"
    "s_waitcnt vmcnt(0)\n"
    "s_branch .Lqk_pd7_%=\n"
    ".Lqk_sp8_%=:\n"
    "s_sleep 4\n"
    "s_branch .Lqk_po6_%=\n"
    ".Lqk_pd7_%=:\n"
    ".Lqk_po9_%=:\n"
    "buffer_load_dword v8, v5, s[0:3], s33 offen sc1\n"
    "s_waitcnt vmcnt(0)\n"
    "v_cmp_gt_i32 vcc, s60, v8\n"
    "s_nop 0\n"
    "s_cbranch_vccz .Lqk_pd10_%=\n"
    "s_add_u32 s40, s40, 1\n"
    "s_cmp_lt_u32 s40, 0x40000\n"
    "s_cbranch_scc1 .Lqk_sp11_%=\n"
    "v_mov_b32 v8, 21\n"
    "s_mov_b32 s44, 33728\n"
    "buffer_store_dword v8, off, s[0:3], s44 sc1\n"
    "s_waitcnt vmcnt(0)\n"
    "s_branch .Lqk_pd10_%=\n"
    ".Lqk_sp11_%=:\n"
    "s_sleep 4\n"
    "s_branch .Lqk_po9_%=\n"
    ".Lqk_pd10_%=:\n"
    "s_add_u32 s53, s27, s23\n"
    "s_sub_u32 m0, s26, 8192\n"
    "s_nop 0\n"
    "buffer_load_dwordx4 v96, s[0:3], s53 offen offset:0 sc1 lds\n"
    "buffer_load_dwordx4 v96, s[0:3], s53 offen offset:1024 sc1 lds\n"
    "buffer_load_dwordx4 v96, s[0:3], s53 offen offset:2048 sc1 lds\n"
    "buffer_load_dwordx4 v96, s[0:3], s53 offen offset:3072 sc1 lds\n"
    "s_sub_u32 m0, s26, 4096\n"
    "s_add_u32 s53, s53, 4096\n"
    "buffer_load_dwordx4 v96, s[0:3], s53 offen offset:0 sc1 lds\n"
    "buffer_load_dwordx4 v96, s[0:3], s53 offen offset:1024 sc1 lds\n"
    ".Lqk_po12_%=:\n"
    "buffer_load_dword v8, v6, s[0:3], s34 offen sc1\n"
    "s_waitcnt vmcnt(0)\n"
    "v_cmp_gt_i32 vcc, 2, v8\n"
    "s_nop 0\n"
    "s_cbranch_vccz .Lqk_pd13_%=\n"
    "s_add_u32 s40, s40, 1\n"
    "s_cmp_lt_u32 s40, 0x40000\n"
    "s_cbranch_scc1 .Lqk_sp14_%=\n"
    "v_mov_b32 v8, 22\n"
    "s_mov_b32 s44, 33728\n"
    "buffer_store_dword v8, off, s[0:3], s44 sc1\n"
    "s_waitcnt vmcnt(0)\n"
    "s_branch .Lqk_pd13_%=\n"
    ".Lqk_sp14_%=:\n"
    "s_sleep 4\n"
    "s_branch .Lqk_po12_%=\n"
    ".Lqk_pd13_%=:\n"
    "s_memrealtime s[42:43]\n"
    "s_waitcnt lgkmcnt(0)\n"
    "s_sub_u32 s57, s42, 780\n"
    "s_mov_b32 s46, -1\n"
    "s_mov_b32 s47, -5\n"
    "s_mov_b32 s50, 2130706432\n"
    "s_mov_b32 s48, 2130706432\n"
    "s_mov_b32 s49, 4194304\n"
    "s_mov_b32 s55, s31\n"
    "s_add_u32 s54, s29, s23\n"
    "s_mov_b32 s62, -12\n"
    "s_mov_b32 s63, 0\n"
    "s_barrier\n"
    "v_mov_b32 v110, v104\n"
    "ds_read_b128 v[32:35], v92 offset:0\n"
    "ds_read_b128 v[36:39], v92 offset:1024\n"
    "ds_read_b128 v[40:43], v93 offset:0\n"
    "ds_read_b128 v[44:47], v93 offset:1024\n"
    "ds_read_b128 v[48:51], v94 offset:0\n"
    "ds_read_b128 v[52:55], v94 offset:1024\n"
    "s_waitcnt lgkmcnt(0)\n"
    "s_branch .Lqk_bulk_%=\n"
    ".Lqk_take_%=:\n"
    "s_waitcnt vmcnt(2)\n"
    ".Lqk_mk0x14_%=:\n"
    "ds_read_b128 v[56:59], v92 offset:8192\n"
    "s_waitcnt vmcnt(1)\n"
    "ds_read_b128 v[60:63], v92 offset:9216\n"
    "s_waitcnt lgkmcnt(1)\n"
    "v_smfmac_f32_16x16x64_bf16 v[8:11], v[56:59], v[32:39], v98\n"
    "v_max_u32 v108, v56, v58\n"
    "v_smfmac_f32_16x16x64_bf16 v[12:15], v[56:59], a[160:167], v98\n"
    "s_waitcnt lgkmcnt(0)\n"
    "ds_read_b128 v[32:35], v97 offset:6144\n"
    "v_max3_u32 v108, v108, v60, v62\n"
    "v_smfmac_f32_16x16x64_bf16 v[0:3], v[56:59], v[176:183], v98\n"
    "ds_read_b128 v[36:39], v97 offset:7168\n"
    "v_cmp_eq_u32 vcc, -1, v108\n"
    "v_smfmac_f32_16x16x64_bf16 v[4:7], v[56:59], a[80:87], v98\n"
    "s_cbranch_vccnz .Lqk_retry_%=\n"
    ".Lqk_go_%=:\n"
    "s_barrier\n"
    ".Lqk_mk1x15_%=:\n"
    "v_smfmac_f32_16x16x64_bf16 v[16:19], v[56:59], a[240:247], v98\n"
    "ds_read_b128 v[24:27], v93 offset:8192\n"
    "ds_read_b128 v[28:31], v93 offset:9216\n"
    "v_smfmac_f32_16x16x64_bf16 v[20:23], v[56:59], a[0:7], v98\n"
    "s_waitcnt vmcnt(0)\n"
    "ds_read_b128 v[64:67], v92 offset:10240\n"
    "v_smfmac_f32_16x16x64_bf16 v[8:11], v[60:63], v[40:47], v98\n"
    "ds_read_b128 v[56:59], v94 offset:9216\n"
    "v_smfmac_f32_16x16x64_bf16 v[12:15], v[60:63], a[168:175], v98\n"
    "ds_read_b128 v[40:43], v97 offset:8192\n"
    "ds_read_b128 v[44:47], v97 offset:9216\n"
    "v_smfmac_f32_16x16x64_bf16 v[0:3], v[60:63], v[184:191], v98\n"
    "v_smfmac_f32_16x16x64_bf16 v[4:7], v[60:63], a[88:95], v98\n"
    "v_smfmac_f32_16x16x64_bf16 v[16:19], v[60:63], a[248:255], v98\n"
    "s_waitcnt lgkmcnt(3)\n"
    "v_max_u32 v108, v64, v66\n"
    "v_smfmac_f32_16x16x64_bf16 v[20:23], v[60:63], a[8:15], v98\n"
    "v_cmp_eq_u32 vcc, -1, v108\n"
    "v_smfmac_f32_16x16x64_bf16 v[8:11], v[24:27], v[48:55], v98\n"
    "s_cbranch_vccnz .Lqk_r2_%=\n"
    ".Lqk_go2_%=:\n"
    "s_barrier\n"
    "v_smfmac_f32_16x16x64_bf16 v[12:15], v[24:27], a[176:183], v98\n"
    "s_memrealtime s[72:73]\n"
    "ds_read_b128 v[64:67], v94 offset:8192\n"
    "ds_read_b128 v[60:63], v95 offset:8192\n"
    "v_smfmac_f32_16x16x64_bf16 v[0:3], v[24:27], v[192:199], v98\n"
    "ds_read_b128 v[48:51], v97 offset:10240\n"
    "ds_read_b128 v[52:55], v97 offset:11264\n"
    "v_smfmac_f32_16x16x64_bf16 v[4:7], v[24:27], a[96:103], v98\n"
    "v_mov_b32 v109, s47\n"
    "v_smfmac_f32_16x16x64_bf16 v[16:19], v[24:27], v[112:119], v98\n"
    "v_lshlrev_b32 v110, 10, v96\n"
    "v_smfmac_f32_16x16x64_bf16 v[20:23], v[24:27], a[16:23], v98\n"
    "ds_read_b128 v[24:27], v95 offset:9216\n"
    "s_mov_b64 vcc, s[68:69]\n"
    "v_smfmac_f32_16x16x64_bf16 v[8:11], v[28:31], v[32:39], v98\n"
    "s_add_u32 s64, s64, s57\n"
    "ds_read_b128 v[32:35], v97 offset:12288\n"
    "ds_read_b128 v[36:39], v97 offset:13312\n"
    "v_smfmac_f32_16x16x64_bf16 v[12:15], v[28:31], a[184:191], v98\n"
    "v_smfmac_f32_16x16x64_bf16 v[0:3], v[28:31], v[200:207], v98\n"
    "v_smfmac_f32_16x16x64_bf16 v[4:7], v[28:31], a[104:111], v98\n"
    "v_smfmac_f32_16x16x64_bf16 v[16:19], v[28:31], v[120:127], v98\n"
    "v_smfmac_f32_16x16x64_bf16 v[20:23], v[28:31], a[24:31], v98\n"
    "s_waitcnt lgkmcnt(0)\n"
    "v_smfmac_f32_16x16x64_bf16 v[8:11], v[64:67], v[40:47], v98\n"
    "s_sub_u32 s44, s72, s57\n"
    "ds_read_b128 v[40:43], v97 offset:14336\n"
    "ds_read_b128 v[44:47], v97 offset:15360\n"
    "v_smfmac_f32_16x16x64_bf16 v[12:15], v[64:67], a[192:199], v98\n"
    "ds_read_b128 v[28:31], v96 offset:10240\n"
    "s_add_u32 s45, s72, 14\n"
    "v_smfmac_f32_16x16x64_bf16 v[0:3], v[64:67], v[208:215], v98\n"
    "s_cmp_gt_i32 s44, 6\n"
    "s_cselect_b32 s57, s45, s64\n"
    "s_cselect_b32 s63, 12, -1\n"
    "v_smfmac_f32_16x16x64_bf16 v[4:7], v[64:67], a[112:119], v98\n"
    "v_mov_b32 v108, s57\n"
    "v_smfmac_f32_16x16x64_bf16 v[16:19], v[64:67], v[128:135], v98\n"
    "s_mov_b64 exec, 15\n"
    "buffer_store_dwordx2 v[108:109], v110, s[0:3], s56 offen sc1\n"
    "s_mov_b64 exec, -1\n"
    "v_smfmac_f32_16x16x64_bf16 v[20:23], v[64:67], a[32:39], v98\n"
    "v_smfmac_f32_16x16x64_bf16 v[8:11], v[56:59], v[48:55], v98\n"
    "ds_read_b128 v[48:51], v97 offset:16384\n"
    "ds_read_b128 v[52:55], v97 offset:17408\n"
    "v_smfmac_f32_16x16x64_bf16 v[12:15], v[56:59], a[200:207], v98\n"
    "ds_read_b128 v[64:67], v96 offset:21504\n"
    "v_bfe_u32 v111, v96, 8, 2\n"
    "v_smfmac_f32_16x16x64_bf16 v[8:11], v[60:63], v[32:39], v98\n"
    "ds_read_b128 v[32:35], v97 offset:18432\n"
    "ds_read_b128 v[36:39], v97 offset:19456\n"
    "v_smfmac_f32_16x16x64_bf16 v[12:15], v[60:63], a[208:215], v98\n"
    "s_waitcnt lgkmcnt(5)\n"
    "v_mad_i32_i24 v110, v111, -8, v104\n"
    "v_smfmac_f32_16x16x64_bf16 v[8:11], v[24:27], v[40:47], v98\n"
    "v_mad_i32_i24 v111, v111, -8, v102\n"
    "v_smfmac_f32_16x16x64_bf16 v[12:15], v[24:27], a[216:223], v98\n"
    "v_smfmac_f32_16x16x64_bf16 v[0:3], v[56:59], v[216:223], v98\n"
    "v_smfmac_f32_16x16x64_bf16 v[4:7], v[56:59], a[120:127], v98\n"
    "s_waitcnt lgkmcnt(3)\n"
    "v_smfmac_f32_16x16x64_bf16 v[8:11], v[28:31], v[48:55], v98\n"
    "v_smfmac_f32_16x16x64_bf16 v[12:15], v[28:31], a[224:231], v98\n"
    "s_waitcnt lgkmcnt(0)\n"
    "v_smfmac_f32_16x16x64_bf16 v[8:11], v[64:67], v[32:39], v98\n"
    "v_smfmac_f32_16x16x64_bf16 v[12:15], v[64:67], a[232:239], v98\n"
    "ds_read_b128 v[32:35], v92 offset:0\n"
    "ds_read_b128 v[36:39], v92 offset:1024\n"
    "v_smfmac_f32_16x16x64_bf16 v[0:3], v[60:63], v[224:231], v98\n"
    "s_nop 1\n"
    "v_add_f32 v8, v8, v10\n"
    "v_smfmac_f32_16x16x64_bf16 v[4:7], v[60:63], a[128:135], v98\n"
    "ds_read_b128 v[40:43], v93 offset:0\n"
    "v_add_f32 v9, v9, v11\n"
    "v_smfmac_f32_16x16x64_bf16 v[0:3], v[24:27], v[232:239], v98\n"
    "ds_read_b128 v[44:47], v93 offset:1024\n"
    "v_cvt_pk_bf16_f32 v8, v8, v9\n"
    "v_smfmac_f32_16x16x64_bf16 v[0:3], v[28:31], v[240:247], v98\n"
    "ds_read_b128 v[48:51], v94 offset:0\n"
    "v_add_f32 v12, v12, v14\n"
    "v_smfmac_f32_16x16x64_bf16 v[0:3], v[64:67], v[248:255], v98\n"
    "ds_read_b128 v[52:55], v94 offset:1024\n"
    "v_add_f32 v13, v13, v15\n"
    "v_smfmac_f32_16x16x64_bf16 v[4:7], v[24:27], a[136:143], v98\n"
    "v_cvt_pk_bf16_f32 v9, v12, v13\n"
    "v_smfmac_f32_16x16x64_bf16 v[4:7], v[28:31], a[144:151], v98\n"
    "s_nop 0\n"
    "v_mfma_f32_16x16x16_bf16 v[12:15], v[8:9], v[100:101], 0\n"
    "v_add_f32 v0, v0, v2\n"
    "v_add_f32 v1, v1, v3\n"
    "v_smfmac_f32_16x16x64_bf16 v[4:7], v[64:67], a[152:159], v98\n"
    "v_cvt_pk_bf16_f32 v0, v0, v1\n"
    "v_smfmac_f32_16x16x64_bf16 v[16:19], v[56:59], v[136:143], v98\n"
    "s_nop 1\n"
    "v_cvt_pk_bf16_f32 v15, v14, v15\n"
    "v_smfmac_f32_16x16x64_bf16 v[20:23], v[56:59], a[40:47], v98\n"
    "s_nop 0\n"
    "v_add_f32 v4, v4, v6\n"
    "v_smfmac_f32_16x16x64_bf16 v[16:19], v[60:63], v[144:151], v98\n"
    "v_add_f32 v5, v5, v7\n"
    "v_smfmac_f32_16x16x64_bf16 v[20:23], v[60:63], a[48:55], v98\n"
    "v_cvt_pk_bf16_f32 v1, v4, v5\n"
    "v_smfmac_f32_16x16x64_bf16 v[16:19], v[24:27], v[152:159], v98\n"
    "s_nop 0\n"
    "v_mfma_f32_16x16x16_bf16 v[4:7], v[0:1], v[100:101], 0\n"
    "v_cvt_pk_bf16_f32 v14, v12, v13\n"
    "v_smfmac_f32_16x16x64_bf16 v[20:23], v[24:27], a[56:63], v98\n"
    "v_smfmac_f32_16x16x64_bf16 v[16:19], v[28:31], v[160:167], v98\n"
    "s_nop 1\n"
    "v_cvt_pk_bf16_f32 v13, v6, v7\n"
    "v_smfmac_f32_16x16x64_bf16 v[20:23], v[28:31], a[64:71], v98\n"
    "v_cvt_pk_bf16_f32 v12, v4, v5\n"
    "v_smfmac_f32_16x16x64_bf16 v[16:19], v[64:67], v[168:175], v98\n"
    "v_smfmac_f32_16x16x64_bf16 v[20:23], v[64:67], a[72:79], v98\n"
    ".Lqk_mk2x16_%=:\n"
    "v_mfma_f32_16x16x32_bf16 v[24:27], v[12:15], v[12:15], 0\n"
    "s_nop 1\n"
    "v_add_f32 v16, v16, v18\n"
    "v_add_f32 v17, v17, v19\n"
    "v_cvt_pk_bf16_f32 v16, v16, v17\n"
    "v_add_f32 v20, v20, v22\n"
    "v_add_f32 v21, v21, v23\n"
    "v_cvt_pk_bf16_f32 v17, v20, v21\n"
    "s_nop 1\n"
    "v_mfma_f32_16x16x16_bf16 v[20:23], v[16:17], v[100:101], 0\n"
    "v_mfma_f32_16x16x32_bf16 v[0:3], v[72:75], v[12:15], 0\n"
    "v_mfma_f32_16x16x32_bf16 v[8:11], v[68:71], v[12:15], 0\n"
    "s_nop 1\n"
    "v_cvt_pk_bf16_f32 v17, v22, v23\n"
    "v_cvt_pk_bf16_f32 v16, v20, v21\n"
    "s_nop 1\n"
    "v_mfma_f32_16x16x16_bf16 v[24:27], v[16:17], v[16:17], v[24:27]\n"
    "v_mfma_f32_16x16x16_bf16 v[0:3], v[78:79], v[16:17], v[0:3]\n"
    "v_mfma_f32_16x16x16_bf16 v[8:11], v[76:77], v[16:17], v[8:11]\n"
    "s_nop 1\n"
    "ds_write_b64 v111, v[26:27] offset:128\n"
    "v_mfma_f32_16x16x32_bf16 v[56:59], v[12:15], v[80:83], 0\n"
    "s_nop 3\n"
    "v_cndmask_b32_dpp v8, v0, v8, vcc quad_perm:[0,1,0,1] row_mask:0xf bank_mask:0xf\n"
    "v_cndmask_b32_dpp v9, v1, v9, vcc quad_perm:[0,1,0,1] row_mask:0xf bank_mask:0xf\n"
    "v_cndmask_b32_dpp v10, v2, v10, vcc quad_perm:[0,1,0,1] row_mask:0xf bank_mask:0xf\n"
    "v_cndmask_b32_dpp v11, v3, v11, vcc quad_perm:[0,1,0,1] row_mask:0xf bank_mask:0xf\n"
    "ds_write_b128 v102, v[8:11]\n"
    "v_mfma_f32_16x16x32_bf16 v[60:63], v[12:15], v[84:87], 0\n"
    "s_mov_b64 exec, s[20:21]\n"
    "s_waitcnt lgkmcnt(0)\n"
    "s_barrier\n"
    ".Lqk_mk3x17_%=:\n"
    "ds_read_b128 v[72:75], v103 offset:0\n"
    "ds_read_b128 v[76:79], v103 offset:1936\n"
    "ds_read_b128 v[80:83], v103 offset:3872\n"
    "ds_read_b128 v[84:87], v103 offset:5808\n"
    "s_mov_b64 exec, -1\n"
    "v_mfma_f32_16x16x16_bf16 v[56:59], v[16:17], v[88:89], v[56:59]\n"
    "v_mfma_f32_16x16x16_bf16 v[60:63], v[16:17], v[90:91], v[60:63]\n"
    "s_waitcnt lgkmcnt(2)\n"
    "v_pk_add_f32 v[72:73], v[72:73], v[76:77]\n"
    "v_pk_add_f32 v[74:75], v[74:75], v[78:79]\n"
    "s_waitcnt lgkmcnt(1)\n"
    "v_pk_add_f32 v[72:73], v[72:73], v[80:81]\n"
    "v_pk_add_f32 v[74:75], v[74:75], v[82:83]\n"
    "s_waitcnt lgkmcnt(0)\n"
    "v_pk_add_f32 v[72:73], v[72:73], v[84:85]\n"
    "v_pk_add_f32 v[74:75], v[74:75], v[86:87]\n"
    "v_max3_f32 v76, v72, v73, v74\n"
    "v_max_f32 v76, v76, v75\n"
    "v_pk_mul_f32 v[72:73], v[72:73], s[66:67]\n"
    "v_pk_mul_f32 v[74:75], v[74:75], s[66:67]\n"
    "v_max_f32_dpp v76, v76, v76 row_ror:8 row_mask:0xf bank_mask:0xf\n"
    "s_nop 1\n"
    "v_max_f32_dpp v76, v76, v76 row_ror:4 row_mask:0xf bank_mask:0xf\n"
    "s_nop 1\n"
    "v_max_f32_dpp v76, v76, v76 row_ror:2 row_mask:0xf bank_mask:0xf\n"
    "s_nop 1\n"
    "v_max_f32_dpp v76, v76, v76 row_ror:1 row_mask:0xf bank_mask:0xf\n"
    "v_pk_fma_f32 v[72:73], v[76:77], s[66:67], v[72:73] op_sel_hi:[0,1,1] neg_lo:[1,0,0] neg_hi:[1,0,0]\n"
    "v_pk_fma_f32 v[74:75], v[76:77], s[66:67], v[74:75] op_sel_hi:[0,1,1] neg_lo:[1,0,0] neg_hi:[1,0,0]\n"
    "v_exp_f32 v72, v72\n"
    "v_exp_f32 v74, v74\n"
    "v_exp_f32 v73, v73\n"
    "v_exp_f32 v75, v75\n"
    "v_add_f32 v76, v72, v74\n"
    "v_add_f32 v77, v73, v75\n"
    "v_add_f32 v76, v76, v77\n"
    "s_nop 1\n"
    "v_add_f32_dpp v76, v76, v76 row_ror:8 row_mask:0xf bank_mask:0xf\n"
    "s_nop 1\n"
    "v_add_f32_dpp v76, v76, v76 row_ror:4 row_mask:0xf bank_mask:0xf\n"
    "s_nop 1\n"
    "v_add_f32_dpp v76, v76, v76 row_ror:2 row_mask:0xf bank_mask:0xf\n"
    "s_nop 1\n"
    "v_add_f32_dpp v76, v76, v76 row_ror:1 row_mask:0xf bank_mask:0xf\n"
    "v_rcp_f32 v76, v76\n"
    "s_nop 0\n"
    "v_pk_mul_f32 v[72:73], v[72:73], v[76:77] op_sel_hi:[1,0] clamp\n"
    "v_pk_mul_f32 v[74:75], v[74:75], v[76:77] op_sel_hi:[1,0] clamp\n"
    "v_cvt_pk_bf16_f32 v72, v72, v73\n"
    "v_cvt_pk_bf16_f32 v73, v74, v75\n"
    "buffer_store_dwordx2 v[72:73], v106, s[0:3], s48 offen sc1\n"
    ".Lqk_mk4x18_%=:\n"
    ".Lqk_bulk_%=:\n"
    "v_mfma_f32_16x16x32_bf16 v[0:3], a[80:83], v[32:35], 0\n"
    "ds_write_b64 v110, v[58:59] offset:224\n"
    "v_mfma_f32_16x16x32_bf16 v[4:7], a[80:83], v[36:39], 0\n"
    "ds_write_b64 v110, v[62:63] offset:128\n"
    ".Lqk_mkL0_%=:\n"
    "v_mfma_f32_16x16x32_bf16 v[8:11], a[160:163], v[32:35], 0\n"
    "v_mfma_f32_16x16x32_bf16 v[12:15], a[160:163], v[36:39], 0\n"
    "v_mfma_f32_16x16x32_bf16 v[16:19], a[0:3], v[32:35], 0\n"
    "v_mfma_f32_16x16x32_bf16 v[20:23], a[0:3], v[36:39], 0\n"
    "s_waitcnt lgkmcnt(0)\n"
    "s_barrier\n"
    "ds_read_b128 v[56:59], v105 offset:0\n"
    "v_mfma_f32_16x16x32_bf16 v[24:27], a[240:243], v[32:35], 0\n"
    "ds_read_b128 v[60:63], v105 offset:4352\n"
    "v_mfma_f32_16x16x32_bf16 v[28:31], a[240:243], v[36:39], 0\n"
    "v_mfma_f32_16x16x32_bf16 v[0:3], a[96:99], v[40:43], v[0:3]\n"
    "s_sub_u32 m0, s26, 4096\n"
    "ds_read_b128 v[32:35], v95 offset:0\n"
    "ds_read_b128 v[36:39], v95 offset:1024\n"
    "v_mfma_f32_16x16x32_bf16 v[4:7], a[96:99], v[44:47], v[4:7]\n"
    "ds_read_b128 v[64:67], v105 offset:8704\n"
    "v_mfma_f32_16x16x32_bf16 v[8:11], a[176:179], v[40:43], v[8:11]\n"
    "buffer_load_dwordx4 v96, s[0:3], s53 offen offset:2048 sc1 lds\n"
    "v_mfma_f32_16x16x32_bf16 v[12:15], a[176:179], v[44:47], v[12:15]\n"
    "ds_read_b128 v[68:71], v105 offset:13056\n"
    "v_mfma_f32_16x16x32_bf16 v[16:19], a[16:19], v[40:43], v[16:19]\n"
    "v_mfma_f32_16x16x32_bf16 v[20:23], a[16:19], v[44:47], v[20:23]\n"
    "v_mfma_f32_16x16x32_bf16 v[24:27], v[112:115], v[40:43], v[24:27]\n"
    "buffer_load_dwordx4 v96, s[0:3], s53 offen offset:3072 sc1 lds\n"
    "v_mfma_f32_16x16x32_bf16 v[28:31], v[112:115], v[44:47], v[28:31]\n"
    "s_mov_b32 m0, s59\n"
    "s_add_u32 s53, s53, s28\n"
    "s_waitcnt lgkmcnt(4)\n"
    "v_mfma_f32_16x16x32_bf16 v[0:3], a[112:115], v[48:51], v[0:3]\n"
    "ds_read_b128 v[40:43], v92 offset:2048\n"
    "ds_read_b128 v[44:47], v92 offset:3072\n"
    "v_mfma_f32_16x16x32_bf16 v[4:7], a[112:115], v[52:55], v[4:7]\n"
    "v_add_f32 v56, v56, v60\n"
    "v_mfma_f32_16x16x32_bf16 v[8:11], a[192:195], v[48:51], v[8:11]\n"
    "buffer_load_dwordx4 v96, s[0:3], s53 offen offset:0 sc1 lds\n"
    "v_mfma_f32_16x16x32_bf16 v[12:15], a[192:195], v[52:55], v[12:15]\n"
    "v_add_f32 v57, v57, v61\n"
    "v_add_f32 v58, v58, v62\n"
    "v_mfma_f32_16x16x32_bf16 v[16:19], a[32:35], v[48:51], v[16:19]\n"
    "v_add_f32 v59, v59, v63\n"
    "s_waitcnt lgkmcnt(3)\n"
    "v_mfma_f32_16x16x32_bf16 v[20:23], a[32:35], v[52:55], v[20:23]\n"
    "v_add_f32 v56, v56, v64\n"
    ".Lqk_mkL1_%=:\n"
    "v_mfma_f32_16x16x32_bf16 v[24:27], v[128:131], v[48:51], v[24:27]\n"
    "buffer_load_dwordx4 v96, s[0:3], s53 offen offset:1024 sc1 lds\n"
    "v_mfma_f32_16x16x32_bf16 v[28:31], v[128:131], v[52:55], v[28:31]\n"
    "s_waitcnt lgkmcnt(4)\n"
    "v_mov_b64 v[110:111], -1\n"
    "v_add_f32 v57, v57, v65\n"
    "v_mfma_f32_16x16x32_bf16 v[0:3], a[128:131], v[32:35], v[0:3]\n"
    "ds_read_b128 v[48:51], v93 offset:2048\n"
    "ds_read_b128 v[52:55], v93 offset:3072\n"
    "v_mfma_f32_16x16x32_bf16 v[4:7], a[128:131], v[36:39], v[4:7]\n"
    "v_add_f32 v58, v58, v66\n"
    "v_mfma_f32_16x16x32_bf16 v[8:11], a[208:211], v[32:35], v[8:11]\n"
    "buffer_load_dwordx4 v[80:83], v96, s[0:3], s54 offen sc1\n"
    "v_mfma_f32_16x16x32_bf16 v[12:15], a[208:211], v[36:39], v[12:15]\n"
    "v_add_f32 v59, v59, v67\n"
    "s_waitcnt lgkmcnt(4)\n"
    "v_mfma_f32_16x16x32_bf16 v[16:19], a[48:51], v[32:35], v[16:19]\n"
    "v_add_f32 v56, v56, v68\n"
    "v_add_f32 v57, v57, v69\n"
    "v_mfma_f32_16x16x32_bf16 v[20:23], a[48:51], v[36:39], v[20:23]\n"
    "v_add_f32 v58, v58, v70\n"
    "v_mfma_f32_16x16x32_bf16 v[24:27], v[144:147], v[32:35], v[24:27]\n"
    "buffer_load_dwordx4 v[84:87], v96, s[0:3], s54 offen offset:1024 sc1\n"
    "v_mfma_f32_16x16x32_bf16 v[28:31], v[144:147], v[36:39], v[28:31]\n"
    "s_waitcnt lgkmcnt(2)\n"
    "v_add_f32 v59, v59, v71\n"
    "s_barrier\n"
    "v_mfma_f32_16x16x32_bf16 v[0:3], a[84:87], v[40:43], v[0:3]\n"
    "ds_read_b128 v[32:35], v94 offset:2048\n"
    "ds_read_b128 v[36:39], v94 offset:3072\n"
    "v_mfma_f32_16x16x32_bf16 v[4:7], a[84:87], v[44:47], v[4:7]\n"
    "v_cndmask_b32 v56, v99, v56, s[12:13]\n"
    "v_cndmask_b32 v57, v99, v57, s[14:15]\n"
    "v_mfma_f32_16x16x32_bf16 v[8:11], a[164:167], v[40:43], v[8:11]\n"
    "buffer_store_dwordx2 v[110:111], v106, s[0:3], s49 offen sc1\n"
    "v_cndmask_b32 v58, v99, v58, s[16:17]\n"
    "v_mfma_f32_16x16x32_bf16 v[12:15], a[164:167], v[44:47], v[12:15]\n"
    "v_cndmask_b32 v59, v99, v59, s[18:19]\n"
    "v_max3_f32 v72, v56, v57, v58\n"
    "v_mfma_f32_16x16x32_bf16 v[16:19], a[4:7], v[40:43], v[16:19]\n"
    "v_max_f32 v72, v72, v59\n"
    "v_cndmask_b32 v74, v99, v72, s[70:71]\n"
    "v_mfma_f32_16x16x32_bf16 v[20:23], a[4:7], v[44:47], v[20:23]\n"
    "v_cndmask_b32 v75, v72, v99, s[70:71]\n"
    "v_max_f32_dpp v74, v74, v74 row_ror:8 row_mask:0xf bank_mask:0xf\n"
    "v_mfma_f32_16x16x32_bf16 v[24:27], a[244:247], v[40:43], v[24:27]\n"
    "v_max_f32_dpp v75, v75, v75 row_ror:8 row_mask:0xf bank_mask:0xf\n"
    "v_max_f32_dpp v74, v74, v74 row_ror:4 row_mask:0xf bank_mask:0xf\n"
    "v_mfma_f32_16x16x32_bf16 v[28:31], a[244:247], v[44:47], v[28:31]\n"
    "s_waitcnt lgkmcnt(2)\n"
    "v_max_f32_dpp v75, v75, v75 row_ror:4 row_mask:0xf bank_mask:0xf\n"
    "v_max_f32_dpp v74, v74, v74 row_ror:2 row_mask:0xf bank_mask:0xf\n"
    "v_mfma_f32_16x16x32_bf16 v[0:3], a[100:103], v[48:51], v[0:3]\n"
    "ds_read_b128 v[40:43], v95 offset:2048\n"
    "ds_read_b128 v[44:47], v95 offset:3072\n"
    "v_mfma_f32_16x16x32_bf16 v[4:7], a[100:103], v[52:55], v[4:7]\n"
    "v_max_f32_dpp v75, v75, v75 row_ror:2 row_mask:0xf bank_mask:0xf\n"
    "v_max_f32_dpp v74, v74, v74 row_ror:1 row_mask:0xf bank_mask:0xf\n"
    "v_mfma_f32_16x16x32_bf16 v[8:11], a[180:183], v[48:51], v[8:11]\n"
    "buffer_store_dwordx2 v[110:111], v107, s[0:3], s49 offen sc1\n"
    "v_max_f32_dpp v75, v75, v75 row_ror:1 row_mask:0xf bank_mask:0xf\n"
    ".Lqk_mkL2_%=:\n"
    "v_mfma_f32_16x16x32_bf16 v[12:15], a[180:183], v[52:55], v[12:15]\n"
    "v_cndmask_b32 v72, v75, v74, s[70:71]\n"
    "v_mul_f32 v72, s66, v72\n"
    "v_mfma_f32_16x16x32_bf16 v[16:19], a[20:23], v[48:51], v[16:19]\n"
    "v_fma_f32 v56, v56, s66, -v72\n"
    "v_fma_f32 v57, v57, s66, -v72\n"
    "v_mfma_f32_16x16x32_bf16 v[20:23], a[20:23], v[52:55], v[20:23]\n"
    "v_fma_f32 v58, v58, s66, -v72\n"
    "v_fma_f32 v59, v59, s66, -v72\n"
    "v_mfma_f32_16x16x32_bf16 v[24:27], v[116:119], v[48:51], v[24:27]\n"
    "v_exp_f32 v56, v56\n"
    "v_mfma_f32_16x16x32_bf16 v[28:31], v[116:119], v[52:55], v[28:31]\n"
    "s_waitcnt lgkmcnt(2)\n"
    "v_exp_f32 v58, v58\n"
    "v_mfma_f32_16x16x32_bf16 v[0:3], a[116:119], v[32:35], v[0:3]\n"
    "ds_read_b128 v[48:51], v92 offset:4096\n"
    "ds_read_b128 v[52:55], v92 offset:5120\n"
    "v_mfma_f32_16x16x32_bf16 v[4:7], a[116:119], v[36:39], v[4:7]\n"
    "v_mfma_f32_16x16x32_bf16 v[8:11], a[196:199], v[32:35], v[8:11]\n"
    "buffer_load_dwordx2 v[108:109], off, s[0:3], s55 sc1\n"
    "v_mfma_f32_16x16x32_bf16 v[12:15], a[196:199], v[36:39], v[12:15]\n"
    "v_exp_f32 v57, v57\n"
    "v_mfma_f32_16x16x32_bf16 v[16:19], a[36:39], v[32:35], v[16:19]\n"
    "v_exp_f32 v59, v59\n"
    "v_mfma_f32_16x16x32_bf16 v[20:23], a[36:39], v[36:39], v[20:23]\n"
    "v_add_f32 v72, v56, v58\n"
    "v_add_f32 v73, v57, v59\n"
    "v_mfma_f32_16x16x32_bf16 v[24:27], v[132:135], v[32:35], v[24:27]\n"
    "v_add_f32 v72, v72, v73\n"
    "v_cndmask_b32 v74, 0, v72, s[70:71]\n"
    "v_mfma_f32_16x16x32_bf16 v[28:31], v[132:135], v[36:39], v[28:31]\n"
    "s_waitcnt lgkmcnt(2)\n"
    "v_cndmask_b32 v75, v72, 0, s[70:71]\n"
    "v_add_f32_dpp v74, v74, v74 row_ror:8 row_mask:0xf bank_mask:0xf\n"
    "v_mfma_f32_16x16x32_bf16 v[0:3], a[132:135], v[40:43], v[0:3]\n"
    "ds_read_b128 v[32:35], v93 offset:4096\n"
    "ds_read_b128 v[36:39], v93 offset:5120\n"
    "v_mfma_f32_16x16x32_bf16 v[4:7], a[132:135], v[44:47], v[4:7]\n"
    "v_add_f32_dpp v75, v75, v75 row_ror:8 row_mask:0xf bank_mask:0xf\n"
    "v_add_f32_dpp v74, v74, v74 row_ror:4 row_mask:0xf bank_mask:0xf\n"
    "v_mfma_f32_16x16x32_bf16 v[8:11], a[212:215], v[40:43], v[8:11]\n"
    "v_add_f32_dpp v75, v75, v75 row_ror:4 row_mask:0xf bank_mask:0xf\n"
    "v_add_f32_dpp v74, v74, v74 row_ror:2 row_mask:0xf bank_mask:0xf\n"
    "v_mfma_f32_16x16x32_bf16 v[12:15], a[212:215], v[44:47], v[12:15]\n"
    "v_add_f32_dpp v75, v75, v75 row_ror:2 row_mask:0xf bank_mask:0xf\n"
    "v_add_f32_dpp v74, v74, v74 row_ror:1 row_mask:0xf bank_mask:0xf\n"
    "v_mfma_f32_16x16x32_bf16 v[16:19], a[52:55], v[40:43], v[16:19]\n"
    "v_add_f32_dpp v75, v75, v75 row_ror:1 row_mask:0xf bank_mask:0xf\n"
    "v_cndmask_b32 v72, v75, v74, s[70:71]\n"
    "v_mfma_f32_16x16x32_bf16 v[20:23], a[52:55], v[44:47], v[20:23]\n"
    "v_rcp_f32 v72, v72\n"
    "v_mfma_f32_16x16x32_bf16 v[24:27], v[148:151], v[40:43], v[24:27]\n"
    "v_mul_f32 v56, v56, v72 clamp\n"
    "v_mul_f32 v57, v57, v72 clamp\n"
    "v_mfma_f32_16x16x32_bf16 v[28:31], v[148:151], v[44:47], v[28:31]\n"
    "s_waitcnt lgkmcnt(2)\n"
    "v_mul_f32 v58, v58, v72 clamp\n"
    "v_mul_f32 v59, v59, v72 clamp\n"
    "v_mfma_f32_16x16x32_bf16 v[0:3], a[88:91], v[48:51], v[0:3]\n"
    "ds_read_b128 v[40:43], v94 offset:4096\n"
    "ds_read_b128 v[44:47], v94 offset:5120\n"
    "v_mfma_f32_16x16x32_bf16 v[4:7], a[88:91], v[52:55], v[4:7]\n"
    "v_cvt_pk_bf16_f32 v56, v56, v57\n"
    "v_cvt_pk_bf16_f32 v57, v58, v59\n"
    "v_mfma_f32_16x16x32_bf16 v[8:11], a[168:171], v[48:51], v[8:11]\n"
    "buffer_store_dwordx2 v[56:57], v107, s[0:3], s50 offen sc1\n"
    ".Lqk_mkL3_%=:\n"
    "v_mfma_f32_16x16x32_bf16 v[12:15], a[168:171], v[52:55], v[12:15]\n"
    "s_cmp_eq_u32 s46, 63\n"
    "s_cbranch_scc1 .Lqk_end_%=\n"
    "v_mfma_f32_16x16x32_bf16 v[16:19], a[8:11], v[48:51], v[16:19]\n"
    "s_add_u32 s46, s46, 1\n"
    "s_add_u32 s60, s46, 1\n"
    "s_lshl_b32 s53, s60, 19\n"
    "v_mfma_f32_16x16x32_bf16 v[20:23], a[8:11], v[52:55], v[20:23]\n"
    "s_lshl_b32 s45, s60, 11\n"
    "s_xor_b32 s45, s45, s23\n"
    "s_and_b32 s45, s45, 0x2000\n"
    "v_mfma_f32_16x16x32_bf16 v[24:27], a[248:251], v[48:51], v[24:27]\n"
    "s_add_u32 s53, s53, s45\n"
    "s_add_u32 s53, s53, s27\n"
    "s_add_u32 s48, s46, 0\n"
    "v_mfma_f32_16x16x32_bf16 v[28:31], a[248:251], v[52:55], v[28:31]\n"
    "s_waitcnt lgkmcnt(2)\n"
    "s_and_b32 s48, s48, 3\n"
    "s_lshl_b32 s48, s48, 22\n"
    "v_mfma_f32_16x16x32_bf16 v[0:3], a[104:107], v[32:35], v[0:3]\n"
    "s_add_u32 s49, s46, 2\n"
    "ds_read_b128 v[48:51], v95 offset:4096\n"
    "ds_read_b128 v[52:55], v95 offset:5120\n"
    "v_mfma_f32_16x16x32_bf16 v[4:7], a[104:107], v[36:39], v[4:7]\n"
    "s_and_b32 s49, s49, 3\n"
    "s_lshl_b32 s49, s49, 22\n"
    "s_and_b32 s55, s46, 63\n"
    "v_mfma_f32_16x16x32_bf16 v[8:11], a[184:187], v[32:35], v[8:11]\n"
    "s_lshl_b32 s55, s55, 7\n"
    "s_mov_b32 s50, s48\n"
    "s_add_u32 s51, s24, s48\n"
    "v_mfma_f32_16x16x32_bf16 v[12:15], a[184:187], v[36:39], v[12:15]\n"
    "s_add_u32 s52, s25, s48\n"
    "s_add_u32 s56, s30, s55\n"
    "s_add_u32 s55, s31, s55\n"
    "v_mfma_f32_16x16x32_bf16 v[16:19], a[24:27], v[32:35], v[16:19]\n"
    "s_lshl_b32 s54, s60, 20\n"
    "s_lshl_b32 s45, s60, 12\n"
    "s_xor_b32 s45, s45, s23\n"
    "v_mfma_f32_16x16x32_bf16 v[20:23], a[24:27], v[36:39], v[20:23]\n"
    "s_and_b32 s45, s45, 0x2000\n"
    "s_add_u32 s54, s54, s45\n"
    "s_add_u32 s54, s54, s29\n"
    "v_mfma_f32_16x16x32_bf16 v[24:27], v[120:123], v[32:35], v[24:27]\n"
    "s_cmp_gt_i32 s60, 63\n"
    "s_cselect_b32 s53, 2130706432, s53\n"
    "s_cselect_b32 s54, 2130706432, s54\n"
    "v_mfma_f32_16x16x32_bf16 v[28:31], v[120:123], v[36:39], v[28:31]\n"
    "s_waitcnt lgkmcnt(2)\n"
    "s_waitcnt vmcnt(6) lgkmcnt(0)\n"
    "s_barrier\n"
    "v_mfma_f32_16x16x32_bf16 v[0:3], a[120:123], v[40:43], v[0:3]\n"
    "s_add_i32 s62, s62, s63\n"
    "ds_read_b128 v[32:35], v92 offset:6144\n"
    "ds_read_b128 v[36:39], v92 offset:7168\n"
    "v_mfma_f32_16x16x32_bf16 v[4:7], a[120:123], v[44:47], v[4:7]\n"
    "s_max_i32 s62, s62, 0\n"
    "s_min_i32 s62, s62, 20\n"
    "s_add_u32 s64, s62, 7\n"
    "v_mfma_f32_16x16x32_bf16 v[8:11], a[200:203], v[40:43], v[8:11]\n"
    "s_lshr_b32 s64, s64, 3\n"
    "s_min_u32 s64, s64, 2\n"
    "s_lshl_b32 s64, s64, 2\n"
    ".Lqk_mkL4_%=:\n"
    "v_mfma_f32_16x16x32_bf16 v[12:15], a[200:203], v[44:47], v[12:15]\n"
    "s_sub_u32 m0, s26, 8192\n"
    "v_mfma_f32_16x16x32_bf16 v[16:19], a[40:43], v[40:43], v[16:19]\n"
    "buffer_load_dwordx4 v96, s[0:3], s53 offen offset:0 sc1 lds\n"
    "v_mfma_f32_16x16x32_bf16 v[20:23], a[40:43], v[44:47], v[20:23]\n"
    "v_mfma_f32_16x16x32_bf16 v[24:27], v[136:139], v[40:43], v[24:27]\n"
    "v_mfma_f32_16x16x32_bf16 v[28:31], v[136:139], v[44:47], v[28:31]\n"
    "s_waitcnt lgkmcnt(2)\n"
    "v_mfma_f32_16x16x32_bf16 v[0:3], a[136:139], v[48:51], v[0:3]\n"
    "ds_read_b128 v[40:43], v93 offset:6144\n"
    "ds_read_b128 v[44:47], v93 offset:7168\n"
    "v_mfma_f32_16x16x32_bf16 v[4:7], a[136:139], v[52:55], v[4:7]\n"
    "buffer_load_dwordx4 v96, s[0:3], s53 offen offset:1024 sc1 lds\n"
    "v_mfma_f32_16x16x32_bf16 v[8:11], a[216:219], v[48:51], v[8:11]\n"
    "v_mfma_f32_16x16x32_bf16 v[12:15], a[216:219], v[52:55], v[12:15]\n"
    "v_mfma_f32_16x16x32_bf16 v[16:19], a[56:59], v[48:51], v[16:19]\n"
    "v_mfma_f32_16x16x32_bf16 v[20:23], a[56:59], v[52:55], v[20:23]\n"
    "buffer_load_dwordx4 v96, s[0:3], s53 offen offset:2048 sc1 lds\n"
    "v_mfma_f32_16x16x32_bf16 v[24:27], v[152:155], v[48:51], v[24:27]\n"
    "v_mfma_f32_16x16x32_bf16 v[28:31], v[152:155], v[52:55], v[28:31]\n"
    "s_waitcnt lgkmcnt(2)\n"
    "v_mfma_f32_16x16x32_bf16 v[0:3], a[92:95], v[32:35], v[0:3]\n"
    "ds_read_b128 v[48:51], v94 offset:6144\n"
    "ds_read_b128 v[52:55], v94 offset:7168\n"
    "v_mfma_f32_16x16x32_bf16 v[4:7], a[92:95], v[36:39], v[4:7]\n"
    "buffer_load_dwordx4 v96, s[0:3], s53 offen offset:3072 sc1 lds\n"
    "v_mfma_f32_16x16x32_bf16 v[8:11], a[172:175], v[32:35], v[8:11]\n"
    "s_sub_u32 m0, s26, 4096\n"
    "s_add_u32 s53, s53, 4096\n"
    "v_mfma_f32_16x16x32_bf16 v[12:15], a[172:175], v[36:39], v[12:15]\n"
    "v_mfma_f32_16x16x32_bf16 v[16:19], a[12:15], v[32:35], v[16:19]\n"
    "v_mfma_f32_16x16x32_bf16 v[20:23], a[12:15], v[36:39], v[20:23]\n"
    "buffer_load_dwordx4 v96, s[0:3], s53 offen offset:0 sc1 lds\n"
    "v_mfma_f32_16x16x32_bf16 v[24:27], a[252:255], v[32:35], v[24:27]\n"
    "v_mfma_f32_16x16x32_bf16 v[28:31], a[252:255], v[36:39], v[28:31]\n"
    "s_waitcnt lgkmcnt(2)\n"
    "v_mfma_f32_16x16x32_bf16 v[0:3], a[108:111], v[40:43], v[0:3]\n"
    "ds_read_b128 v[32:35], v95 offset:6144\n"
    "ds_read_b128 v[36:39], v95 offset:7168\n"
    "v_mfma_f32_16x16x32_bf16 v[4:7], a[108:111], v[44:47], v[4:7]\n"
    "buffer_load_dwordx4 v96, s[0:3], s53 offen offset:1024 sc1 lds\n"
    "v_mfma_f32_16x16x32_bf16 v[8:11], a[188:191], v[40:43], v[8:11]\n"
    ".Lqk_mkL5_%=:\n"
    "v_mfma_f32_16x16x32_bf16 v[12:15], a[188:191], v[44:47], v[12:15]\n"
    "v_mfma_f32_16x16x32_bf16 v[16:19], a[28:31], v[40:43], v[16:19]\n"
    "v_mfma_f32_16x16x32_bf16 v[20:23], a[28:31], v[44:47], v[20:23]\n"
    "v_mfma_f32_16x16x32_bf16 v[24:27], v[124:127], v[40:43], v[24:27]\n"
    "v_mfma_f32_16x16x32_bf16 v[28:31], v[124:127], v[44:47], v[28:31]\n"
    "s_waitcnt lgkmcnt(2)\n"
    "v_mfma_f32_16x16x32_bf16 v[0:3], a[124:127], v[48:51], v[0:3]\n"
    "ds_read_b128 v[40:43], v96 offset:45056\n"
    "ds_read_b128 v[44:47], v96 offset:46080\n"
    "v_mfma_f32_16x16x32_bf16 v[4:7], a[124:127], v[52:55], v[4:7]\n"
    "v_mfma_f32_16x16x32_bf16 v[8:11], a[204:207], v[48:51], v[8:11]\n"
    "v_mfma_f32_16x16x32_bf16 v[12:15], a[204:207], v[52:55], v[12:15]\n"
    "v_mfma_f32_16x16x32_bf16 v[16:19], a[44:47], v[48:51], v[16:19]\n"
    "v_mfma_f32_16x16x32_bf16 v[20:23], a[44:47], v[52:55], v[20:23]\n"
    "v_mfma_f32_16x16x32_bf16 v[24:27], v[140:143], v[48:51], v[24:27]\n"
    "v_mfma_f32_16x16x32_bf16 v[28:31], v[140:143], v[52:55], v[28:31]\n"
    "s_waitcnt lgkmcnt(2)\n"
    "v_mfma_f32_16x16x32_bf16 v[0:3], a[140:143], v[32:35], v[0:3]\n"
    "ds_read_b128 v[48:51], v96 offset:47104\n"
    "ds_read_b128 v[52:55], v96 offset:48128\n"
    "v_mfma_f32_16x16x32_bf16 v[4:7], a[140:143], v[36:39], v[4:7]\n"
    "v_mfma_f32_16x16x32_bf16 v[8:11], a[220:223], v[32:35], v[8:11]\n"
    "v_mfma_f32_16x16x32_bf16 v[12:15], a[220:223], v[36:39], v[12:15]\n"
    "v_mfma_f32_16x16x32_bf16 v[16:19], a[60:63], v[32:35], v[16:19]\n"
    "v_mfma_f32_16x16x32_bf16 v[20:23], a[60:63], v[36:39], v[20:23]\n"
    "v_mfma_f32_16x16x32_bf16 v[24:27], v[156:159], v[32:35], v[24:27]\n"
    "v_mfma_f32_16x16x32_bf16 v[28:31], v[156:159], v[36:39], v[28:31]\n"
    "s_waitcnt lgkmcnt(2)\n"
    "v_mfma_f32_16x16x32_bf16 v[0:3], a[144:147], v[40:43], v[0:3]\n"
    "s_waitcnt vmcnt(7)\n"
    "ds_read_b128 v[32:35], v96 offset:49152\n"
    "ds_read_b128 v[36:39], v96 offset:50176\n"
    "s_memrealtime s[74:75]\n"
    "v_mfma_f32_16x16x32_bf16 v[4:7], a[144:147], v[44:47], v[4:7]\n"
    "v_readfirstlane_b32 s44, v108\n"
    "v_readfirstlane_b32 s45, v109\n"
    "v_mfma_f32_16x16x32_bf16 v[8:11], a[224:227], v[40:43], v[8:11]\n"
    "v_mfma_f32_16x16x32_bf16 v[12:15], a[224:227], v[44:47], v[12:15]\n"
    "s_cmp_eq_u32 s45, s47\n"
    "s_cselect_b32 s57, s44, s57\n"
    "s_add_u32 s57, s57, 260\n"
    "v_mfma_f32_16x16x32_bf16 v[16:19], a[64:67], v[40:43], v[16:19]\n"
    "s_sub_u32 s58, s57, 59\n"
    "s_mov_b32 s47, s60\n"
    "s_mov_b32 m0, s26\n"
    "v_mfma_f32_16x16x32_bf16 v[20:23], a[64:67], v[44:47], v[20:23]\n"
    ".Lqk_mkL6_%=:\n"
    "v_mfma_f32_16x16x32_bf16 v[24:27], v[160:163], v[40:43], v[24:27]\n"
    "s_sub_u32 s76, s58, s74\n"
    "v_mfma_f32_16x16x32_bf16 v[28:31], v[160:163], v[44:47], v[28:31]\n"
    "s_waitcnt lgkmcnt(2)\n"
    ".Lqk_mk6x19_%=:\n"
    "s_cmp_gt_i32 s76, 4\n"
    "s_cbranch_scc1 .Lqk_early_%=\n"
    "buffer_load_dwordx4 v96, s[0:3], s51 offen sc1 lds\n"
    "buffer_load_dwordx4 v96, s[0:3], s51 offen offset:1024 sc1 lds\n"
    ".Lqk_asked_%=:\n"
    ".Lqk_mk7x21_%=:\n"
    "v_mfma_f32_16x16x32_bf16 v[0:3], a[148:151], v[48:51], v[0:3]\n"
    "ds_read_b128 v[40:43], v96 offset:51200\n"
    "ds_read_b128 v[44:47], v96 offset:52224\n"
    "v_mfma_f32_16x16x32_bf16 v[4:7], a[148:151], v[52:55], v[4:7]\n"
    ".Lqk_mkM0_%=:\n"
    "v_mfma_f32_16x16x32_bf16 v[8:11], a[228:231], v[48:51], v[8:11]\n"
    "v_mfma_f32_16x16x32_bf16 v[12:15], a[228:231], v[52:55], v[12:15]\n"
    "v_mfma_f32_16x16x32_bf16 v[16:19], a[68:71], v[48:51], v[16:19]\n"
    "v_mfma_f32_16x16x32_bf16 v[20:23], a[68:71], v[52:55], v[20:23]\n"
    ".Lqk_mkM1_%=:\n"
    "v_mfma_f32_16x16x32_bf16 v[24:27], v[164:167], v[48:51], v[24:27]\n"
    "v_mfma_f32_16x16x32_bf16 v[28:31], v[164:167], v[52:55], v[28:31]\n"
    "s_waitcnt lgkmcnt(2)\n"
    "v_mfma_f32_16x16x32_bf16 v[0:3], a[152:155], v[32:35], v[0:3]\n"
    "buffer_load_dwordx4 v96, s[0:3], s52 offen offset:2048 sc1 lds\n"
    "v_mfma_f32_16x16x32_bf16 v[4:7], a[152:155], v[36:39], v[4:7]\n"
    "v_mfma_f32_16x16x32_bf16 v[8:11], a[232:235], v[32:35], v[8:11]\n"
    ".Lqk_mkM2_%=:\n"
    "v_mfma_f32_16x16x32_bf16 v[12:15], a[232:235], v[36:39], v[12:15]\n"
    "v_mfma_f32_16x16x32_bf16 v[16:19], a[72:75], v[32:35], v[16:19]\n"
    "v_mfma_f32_16x16x32_bf16 v[20:23], a[72:75], v[36:39], v[20:23]\n"
    "v_mfma_f32_16x16x32_bf16 v[24:27], v[168:171], v[32:35], v[24:27]\n"
    ".Lqk_mkM3_%=:\n"
    "v_mfma_f32_16x16x32_bf16 v[28:31], v[168:171], v[36:39], v[28:31]\n"
    "s_waitcnt lgkmcnt(0)\n"
    "v_mfma_f32_16x16x32_bf16 v[0:3], a[156:159], v[40:43], v[0:3]\n"
    "v_mfma_f32_16x16x32_bf16 v[4:7], a[156:159], v[44:47], v[4:7]\n"
    "v_mfma_f32_16x16x32_bf16 v[8:11], a[236:239], v[40:43], v[8:11]\n"
    "v_mfma_f32_16x16x32_bf16 v[12:15], a[236:239], v[44:47], v[12:15]\n"
    "v_mfma_f32_16x16x32_bf16 v[16:19], a[76:79], v[40:43], v[16:19]\n"
    "v_mfma_f32_16x16x32_bf16 v[20:23], a[76:79], v[44:47], v[20:23]\n"
    "v_mfma_f32_16x16x32_bf16 v[24:27], v[172:175], v[40:43], v[24:27]\n"
    ".Lqk_mkM4_%=:\n"
    "v_mfma_f32_16x16x32_bf16 v[28:31], v[172:175], v[44:47], v[28:31]\n"
    "ds_read_b128 v[32:35], v97 offset:0\n"
    "ds_read_b128 v[36:39], v97 offset:1024\n"
    "ds_read_b128 v[40:43], v97 offset:2048\n"
    "ds_read_b128 v[44:47], v97 offset:3072\n"
    "ds_read_b128 v[48:51], v97 offset:4096\n"
    "ds_read_b128 v[52:55], v97 offset:5120\n"
    "v_cvt_pk_bf16_f32 v68, v0, v1\n"
    "v_cvt_pk_bf16_f32 v69, v2, v3\n"
    "v_cvt_pk_bf16_f32 v70, v8, v9\n"
    "v_cvt_pk_bf16_f32 v71, v10, v11\n"
    "v_cvt_pk_bf16_f32 v72, v4, v5\n"
    "v_cvt_pk_bf16_f32 v73, v6, v7\n"
    "v_cvt_pk_bf16_f32 v74, v12, v13\n"
    "v_cvt_pk_bf16_f32 v75, v14, v15\n"
    "v_mfma_f32_16x16x32_bf16 v[108:111], v[68:71], v[84:87], 0\n"
    "v_cvt_pk_bf16_f32 v88, v24, v25\n"
    "v_cvt_pk_bf16_f32 v89, v26, v27\n"
    "v_cvt_pk_bf16_f32 v90, v28, v29\n"
    "v_cvt_pk_bf16_f32 v91, v30, v31\n"
    "v_mfma_f32_16x16x32_bf16 v[24:27], v[72:75], v[84:87], 0\n"
    "v_cvt_pk_bf16_f32 v76, v16, v17\n"
    "v_cvt_pk_bf16_f32 v77, v18, v19\n"
    "v_cvt_pk_bf16_f32 v78, v20, v21\n"
    "v_cvt_pk_bf16_f32 v79, v22, v23\n"
    "v_mfma_f32_16x16x32_bf16 v[28:31], v[68:71], v[80:83], 0\n"
    "v_mov_b64 v[0:1], 0\n"
    "v_mov_b64 v[2:3], 0\n"
    "v_mfma_f32_16x16x16_bf16 v[108:111], v[76:77], v[90:91], v[108:111]\n"
    "v_mov_b64 v[4:5], 0\n"
    "v_mov_b64 v[6:7], 0\n"
    "v_mfma_f32_16x16x16_bf16 v[24:27], v[78:79], v[90:91], v[24:27]\n"
    "v_mov_b64 v[8:9], 0\n"
    "v_mov_b64 v[10:11], 0\n"
    "v_mfma_f32_16x16x16_bf16 v[28:31], v[76:77], v[88:89], v[28:31]\n"
    "v_mov_b64 v[12:13], 0\n"
    "v_mov_b64 v[14:15], 0\n"
    "v_mov_b64 v[16:17], 0\n"
    "v_mov_b64 v[18:19], 0\n"
    "v_mov_b64 v[20:21], 0\n"
    "v_mov_b64 v[22:23], 0\n"
    "ds_write_b128 v104, v[108:111]\n"
    "ds_write_b128 v104, v[24:27] offset:64\n"
    "ds_write_b128 v104, v[28:31] offset:160\n"
    ".Lqk_mk8x22_%=:\n"
    "s_branch .Lqk_take_%=\n"
    ".Lqk_tk0_%=:\n"
    ".Lqk_po3_%=:\n"
    "buffer_load_dword v109, v108, s[0:3], 0 offen sc1\n"
    "s_waitcnt vmcnt(0)\n"
    "v_cmp_gt_i32 vcc, 32, v109\n"
    "s_nop 0\n"
    "s_cbranch_vccz .Lqk_pd4_%=\n"
    "s_add_u32 s40, s40, 1\n"
    "s_cmp_lt_u32 s40, 0x40000\n"
    "s_cbranch_scc1 .Lqk_sp5_%=\n"
    "v_mov_b32 v109, 90\n"
    "s_mov_b32 s44, 33728\n"
    "buffer_store_dword v109, off, s[0:3], s44 sc1\n"
    "s_waitcnt vmcnt(0)\n"
    "s_branch .Lqk_pd4_%=\n"
    ".Lqk_sp5_%=:\n"
    "s_sleep 8\n"
    "s_branch .Lqk_po3_%=\n"
    ".Lqk_pd4_%=:\n"
    "s_branch .Lqk_tk1_%=\n"
    ".Lqk_early_%=:\n"
    ".Lqk_rt21_%=:\n"
    "s_memrealtime s[42:43]\n"
    "s_waitcnt lgkmcnt(0)\n"
    "s_sub_u32 s44, s58, s42\n"
    "s_cmp_lt_u32 s44, 600\n"
    "s_cbranch_scc1 .Lqk_rt21_%=\n"
    "buffer_load_dwordx4 v96, s[0:3], s51 offen sc1 lds\n"
    "buffer_load_dwordx4 v96, s[0:3], s51 offen offset:1024 sc1 lds\n"
    "s_branch .Lqk_asked_%=\n"
    ".Lqk_retry_%=:\n"
    "s_add_u32 s40, s40, 1\n"
    "s_cmp_lt_u32 s40, 0x40000\n"
    "s_cbranch_scc1 .Lqk_sp24_%=\n"
    "v_mov_b32 v108, 1\n"
    "s_mov_b32 s44, 33728\n"
    "buffer_store_dword v108, off, s[0:3], s44 sc1\n"
    "s_waitcnt vmcnt(0)\n"
    "s_branch .Lqk_go_%=\n"
    ".Lqk_sp24_%=:\n"
    "s_waitcnt lgkmcnt(0)\n"
    "s_nop 7\n"
    "ds_read_b128 v[32:35], v97 offset:0\n"
    "ds_read_b128 v[36:39], v97 offset:1024\n"
    "v_mov_b64 v[0:1], 0\n"
    "v_mov_b64 v[2:3], 0\n"
    "v_mov_b64 v[4:5], 0\n"
    "v_mov_b64 v[6:7], 0\n"
    "v_mov_b64 v[8:9], 0\n"
    "v_mov_b64 v[10:11], 0\n"
    "v_mov_b64 v[12:13], 0\n"
    "v_mov_b64 v[14:15], 0\n"
    "v_mov_b64 v[16:17], 0\n"
    "v_mov_b64 v[18:19], 0\n"
    "v_mov_b64 v[20:21], 0\n"
    "v_mov_b64 v[22:23], 0\n"
    "buffer_load_dwordx4 v96, s[0:3], s51 offen sc1 lds\n"
    "buffer_load_dwordx4 v96, s[0:3], s51 offen offset:1024 sc1 lds\n"
    "buffer_load_dwordx4 v96, s[0:3], s52 offen offset:2048 sc1 lds\n"
    ".Lqk_r1_%=:\n"
    "s_waitcnt vmcnt(0)\n"
    "s_branch .Lqk_take_%=\n"
    ".Lqk_r2_%=:\n"
    "s_add_u32 s40, s40, 1\n"
    "s_cmp_lt_u32 s40, 0x40000\n"
    "s_cbranch_scc1 .Lqk_sp25_%=\n"
    "v_mov_b32 v108, 5\n"
    "s_mov_b32 s44, 33728\n"
    "buffer_store_dword v108, off, s[0:3], s44 sc1\n"
    "s_waitcnt vmcnt(0)\n"
    "s_branch .Lqk_go2_%=\n"
    ".Lqk_sp25_%=:\n"
    "buffer_load_dwordx4 v96, s[0:3], s52 offen offset:2048 sc1 lds\n"
    ".Lqk_r3_%=:\n"
    "s_waitcnt vmcnt(0)\n"
    "ds_read_b128 v[64:67], v92 offset:10240\n"
    "s_waitcnt lgkmcnt(0)\n"
    "v_max_u32 v108, v64, v66\n"
    "v_cmp_eq_u32 vcc, -1, v108\n"
    "s_nop 0\n"
    "s_cbranch_vccnz .Lqk_r2_%=\n"
    "s_branch .Lqk_go2_%=\n"
    ".Lqk_end_%=:\n"
    "s_waitcnt vmcnt(0) lgkmcnt(0)\n"
    "s_endpgm\n"
    :: "{s[0:3]}"(s0_), "{s[4:7]}"(s4_), "{s[8:11]}"(s8_), "{s[12:15]}"(s12_), "{s[16:19]}"(s16_), "{s[20:23]}"(s20_), "{s[24:27]}"(s24_), "{s[28:31]}"(s28_), "{s[32:35]}"(s32_), "{s[36:39]}"(s36_), "{v[0:3]}"(v0_), "{v[4:7]}"(v4_), "{v[92:95]}"(v92_), "{v[96:99]}"(v96_), "{v[100:103]}"(v100_), "{v[104:107]}"(v104_) : "memory", "v255", "a255", "s95");
    __builtin_unreachable();
}
DEV void vo_role(const MegaArgs& A, const int L, const int h, const int rep) {
    ROLE_HEAD;
    const unsigned um = xl(RG_UM + ((8 * w + lq) * 4 + (h >> 3)) * 2048 + (h & 7) * 256 + l16 * 16, L);
#define XU 0
    const unsigned umx = XU ? OOB : xl(RG_UMX + ((4 * (2 * w + (lq & 1)) + (l16 >> 2)) * 4 + (h >> 3)) * 512 + (h & 7) * 64 + (l & 3) * 16 + (lq >> 1) * 8, L);
    const u32x4 s0_ = {RFL(rx[0]), RFL(rx[1]), RFL(rx[2]), RFL(rx[3])};
    const u32x4 s4_ = {RFL(lo(A.Wo + h * 192 * 640)), RFL(hi(A.Wo + h * 192 * 640)), RFL(192u * 1280u), RFL(0x20000u)};
    const u32x4 s8_ = {RFL(lo(A.Wv + h * 192)), RFL(hi(A.Wv + h * 192)), RFL(WBYTES - h * 384), RFL(0x20000u)};
    const u32x4 s12_ = {RFL(w), RFL(L << 13), RFL(L), RFL(xl(RG_MEM + rep * 16384, L) + w * 2048)};
    const u32x4 s16_ = {RFL(w < 2 ? xl(RG_MEM + rep * 16384 + 8192, L) + w * 1024 - 2048 : OOB), RFL(w * 13312 + 8192), RFL(REC0 + w * 16384), RFL(65536 - 4096 - w * 14336)};
    const u32x4 s20_ = {RFL(QT0 + h * 32768 + 16384 + w * 1024), RFL(146432 + w * 1024), RFL(xl(RG_UT + (32 * w + (h >> 3)) * 8192 + (h & 7) * 1024, L)), RFL(xl(Z_X + 8192, L))};
    const u32x4 s24_ = {RFL(xl(Z_CT + (h & 7) * 256, L)), RFL(w == 0 ? xl(Z_QV + (32 + h) * 4, L) : OOB), RFL(DBG0 + ((L * 128 + 32 + h) * 4 + w) * 2048), RFL(0u)};
    const u32x4 v0_ = {(unsigned)(((3 * w + 0) * 64 + l) / 24 * 12288 + ((((3 * w + 0) * 64 + l) % 24) ^ ((((3 * w + 0) * 64 + l) / 48) & 6)) * 16 - 0), (unsigned)(((3 * w + 1) * 64 + l) / 24 * 12288 + ((((3 * w + 1) * 64 + l) % 24) ^ ((((3 * w + 1) * 64 + l) / 48) & 6)) * 16 - 1024), (unsigned)(((3 * w + 2) * 64 + l) / 24 * 12288 + ((((3 * w + 2) * 64 + l) % 24) ^ ((((3 * w + 2) * 64 + l) / 48) & 6)) * 16 - 2048), (unsigned)((8 * lq + ((l >> 2) & 1) + ((l >> 1) & 4)) * 384 + (w ^ (((8 * lq + ((l >> 2) & 1) + ((l >> 1) & 4)) >> 2) & 3)) * 32 + (l & 3) * 8 + ((w + 0) & 3) * 12288)};
    const u32x4 v4_ = {(unsigned)((8 * lq + ((l >> 2) & 1) + ((l >> 1) & 4)) * 384 + (w ^ (((8 * lq + ((l >> 2) & 1) + ((l >> 1) & 4)) >> 2) & 3)) * 32 + (l & 3) * 8 + ((w + 1) & 3) * 12288), (unsigned)((8 * lq + ((l >> 2) & 1) + ((l >> 1) & 4)) * 384 + (w ^ (((8 * lq + ((l >> 2) & 1) + ((l >> 1) & 4)) >> 2) & 3)) * 32 + (l & 3) * 8 + ((w + 2) & 3) * 12288), (unsigned)((8 * lq + ((l >> 2) & 1) + ((l >> 1) & 4)) * 384 + (w ^ (((8 * lq + ((l >> 2) & 1) + ((l >> 1) & 4)) >> 2) & 3)) * 32 + (l & 3) * 8 + ((w + 3) & 3) * 12288), (unsigned)((8 * lq + ((l >> 2) & 1) + ((l >> 1) & 4)) * 384 + (w ^ (((8 * lq + ((l >> 2) & 1) + ((l >> 1) & 4)) >> 2) & 3)) * 32 + (l & 3) * 8)};
    const u32x4 v8_ = {(unsigned)(0), (unsigned)(0), (unsigned)(0), (unsigned)(0)};
    const u32x4 v12_ = {(unsigned)((4 * 0 + lq) * 1280 + 256 * w + ((l16 ^ (2 * ((4 * 0 + lq) & 7))) * 16) - 0), (unsigned)((4 * 1 + lq) * 1280 + 256 * w + ((l16 ^ (2 * ((4 * 1 + lq) & 7))) * 16) - 1024), (unsigned)((4 * 2 + lq) * 1280 + 256 * w + ((l16 ^ (2 * ((4 * 2 + lq) & 7))) * 16) - 2048), (unsigned)((4 * 3 + lq) * 1280 + 256 * w + ((l16 ^ (2 * ((4 * 3 + lq) & 7))) * 16) - 3072)};
    const u32x4 v16_ = {(unsigned)((4 * w + lq) * 1280 + 1024 + ((l16 ^ (2 * ((4 * w + lq) & 7))) * 16)), (unsigned)(w * 8192 + (l >> 2) * 256 + ((l >> 2) & 7) * 32 + (l & 3) * 8), (unsigned)(32768 + (l >> 2) * 256 + (((l >> 2) & 7) ^ (2 * w)) * 32 + (l & 3) * 8), (unsigned)(0u)};
    const u32x4 v92_ = {(unsigned)(l * 16 + ((w + 0) & 3) * 13312), (unsigned)(l * 16 + ((w + 1) & 3) * 13312), (unsigned)(l * 16 + ((w + 2) & 3) * 13312), (unsigned)(l * 16 + ((w + 3) & 3) * 13312)};
    const u32x4 v96_ = {(unsigned)(l * 16), (unsigned)(l * 16 + 64512 + w * 20480), (unsigned)((l & 2) ? 0xeeeeu : 0x4444u), (unsigned)(61440 + 512 * w + (l16 >> 2) * 64 + (l & 1) * 32 + sw2(lq) * 8 + (l & 2) * 2)};
    const u32x4 v100_ = {(unsigned)(61440 + 2048 + 256 * w + (l16 >> 2) * 64 + (l & 1) * 32 + sw2(lq) * 8 + (l & 2) * 2), (unsigned)(61440 + 512 * (lq >> 1) + 128 * (lq & 1) + (l & 4) * 8 + (l & 8) * 32 + (l & 3) * 8), (unsigned)(um), (unsigned)(umx)};
    const u32x4 v104_ = {(unsigned)(xl(RG_UTX + ((8 * w + (l16 >> 2)) * 4 + (h >> 3)) * 2048 + (h & 1) * 1024 + ((h & 7) >> 1) * 256 + lq * 64 + (l & 3) * 16, L)), (unsigned)(l < 4 || l > 8 ? xl(Z_QW + h * 128 + (l & 3) * 4, L) : (L ? xl(Z_XN + rep * 2048 + (l - 4) * 128, 0) : OOB)), (unsigned)(146432 + l * 16), (unsigned)(148480 + l * 16)};
    asm volatile(
    "s_mov_b32 s28, 0\n"
    "s_mov_b64 s[30:31], 0\n"
    "s_mov_b32 s33, 0\n"
    "s_cmp_eq_u32 s13, 0\n"
    "s_cselect_b32 s32, 0, 28\n"
    "s_sub_u32 s32, s32, 0\n"
    ".Lvo_ho1_%=:\n"
    "s_sub_u32 s32, s32, 1\n"
    "s_cbranch_scc1 .Lvo_hd2_%=\n"
    "s_sleep 17\n"
    "s_branch .Lvo_ho1_%=\n"
    ".Lvo_hd2_%=:\n"
    "s_mul_i32 s41, s12, 3072\n"
    "s_lshl_b32 s42, s12, 13\n"
    "s_lshl_b32 s43, s12, 10\n"
    "s_mov_b32 s49, 0\n"
    "s_add_u32 m0, s42, 0\n"
    "s_nop 0\n"
    "buffer_load_dwordx4 v12, s[4:7], s49 offen offset:0 nt lds\n"
    "buffer_load_dwordx4 v13, s[4:7], s49 offen offset:1024 nt lds\n"
    "buffer_load_dwordx4 v14, s[4:7], s49 offen offset:2048 nt lds\n"
    "buffer_load_dwordx4 v15, s[4:7], s49 offen offset:3072 nt lds\n"
    "s_add_u32 m0, s43, 32768\n"
    "s_nop 0\n"
    "buffer_load_dwordx4 v16, s[4:7], s49 offen nt lds\n"
    "s_mov_b32 s49, 81920\n"
    "s_add_u32 m0, s42, 4096\n"
    "s_nop 0\n"
    "buffer_load_dwordx4 v12, s[4:7], s49 offen offset:0 nt lds\n"
    "buffer_load_dwordx4 v13, s[4:7], s49 offen offset:1024 nt lds\n"
    "buffer_load_dwordx4 v14, s[4:7], s49 offen offset:2048 nt lds\n"
    "buffer_load_dwordx4 v15, s[4:7], s49 offen offset:3072 nt lds\n"
    "s_add_u32 m0, s43, 36864\n"
    "s_nop 0\n"
    "buffer_load_dwordx4 v16, s[4:7], s49 offen nt lds\n"
    "s_mov_b32 s49, 20480\n"
    "s_add_u32 m0, s42, 49152\n"
    "s_nop 0\n"
    "buffer_load_dwordx4 v12, s[4:7], s49 offen offset:0 nt lds\n"
    "buffer_load_dwordx4 v13, s[4:7], s49 offen offset:1024 nt lds\n"
    "buffer_load_dwordx4 v14, s[4:7], s49 offen offset:2048 nt lds\n"
    "buffer_load_dwordx4 v15, s[4:7], s49 offen offset:3072 nt lds\n"
    "s_add_u32 m0, s43, 81920\n"
    "s_nop 0\n"
    "buffer_load_dwordx4 v16, s[4:7], s49 offen nt lds\n"
    "s_mov_b32 s49, 102400\n"
    "s_add_u32 m0, s42, 53248\n"
    "s_nop 0\n"
    "buffer_load_dwordx4 v12, s[4:7], s49 offen offset:0 nt lds\n"
    "buffer_load_dwordx4 v13, s[4:7], s49 offen offset:1024 nt lds\n"
    "buffer_load_dwordx4 v14, s[4:7], s49 offen offset:2048 nt lds\n"
    "buffer_load_dwordx4 v15, s[4:7], s49 offen offset:3072 nt lds\n"
    "s_add_u32 m0, s43, 86016\n"
    "s_nop 0\n"
    "buffer_load_dwordx4 v16, s[4:7], s49 offen nt lds\n"
    "s_mov_b32 s49, 40960\n"
    "s_add_u32 m0, s42, 98304\n"
    "s_nop 0\n"
    "buffer_load_dwordx4 v12, s[4:7], s49 offen offset:0 nt lds\n"
    "buffer_load_dwordx4 v13, s[4:7], s49 offen offset:1024 nt lds\n"
    "buffer_load_dwordx4 v14, s[4:7], s49 offen offset:2048 nt lds\n"
    "buffer_load_dwordx4 v15, s[4:7], s49 offen offset:3072 nt lds\n"
    "s_add_u32 m0, s43, 131072\n"
    "s_nop 0\n"
    "buffer_load_dwordx4 v16, s[4:7], s49 offen nt lds\n"
    "s_mov_b32 s49, 122880\n"
    "s_add_u32 m0, s42, 102400\n"
    "s_nop 0\n"
    "buffer_load_dwordx4 v12, s[4:7], s49 offen offset:0 nt lds\n"
    "buffer_load_dwordx4 v13, s[4:7], s49 offen offset:1024 nt lds\n"
    "buffer_load_dwordx4 v14, s[4:7], s49 offen offset:2048 nt lds\n"
    "buffer_load_dwordx4 v15, s[4:7], s49 offen offset:3072 nt lds\n"
    "s_add_u32 m0, s43, 135168\n"
    "s_nop 0\n"
    "buffer_load_dwordx4 v16, s[4:7], s49 offen nt lds\n"
    "v_mov_b64 v[108:109], -1\n"
    "v_mov_b64 v[110:111], -1\n"
    "s_mov_b32 s49, 0x10000\n"
    "buffer_store_dwordx4 v[108:111], v102, s[0:3], 0 offen sc1\n"
    "buffer_store_dwordx4 v[108:111], v102, s[0:3], s49 offen sc1\n"
    "buffer_store_dwordx2 v[108:109], v103, s[0:3], 0 offen sc1\n"
    "s_waitcnt vmcnt(23)\n"
    "s_barrier\n"
    "v_add_u32 v24, 0, v17\n"
    "v_add_u32 v32, 0, v18\n"
    "v_xor_b32 v33, 32, v32\n"
    "v_xor_b32 v25, 32, v24\n"
    "v_xor_b32 v26, 64, v24\n"
    "v_xor_b32 v27, 96, v24\n"
    "v_xor_b32 v28, 128, v24\n"
    "v_xor_b32 v29, 160, v24\n"
    "v_xor_b32 v30, 192, v24\n"
    "v_xor_b32 v31, 224, v24\n"
    "ds_read_b64_tr_b16 a[0:1], v24 offset:0\n"
    "ds_read_b64_tr_b16 a[2:3], v24 offset:4096\n"
    "ds_read_b64_tr_b16 a[24:25], v25 offset:0\n"
    "ds_read_b64_tr_b16 a[26:27], v25 offset:4096\n"
    "ds_read_b64_tr_b16 a[48:49], v26 offset:0\n"
    "ds_read_b64_tr_b16 a[50:51], v26 offset:4096\n"
    "ds_read_b64_tr_b16 a[72:73], v27 offset:0\n"
    "ds_read_b64_tr_b16 a[74:75], v27 offset:4096\n"
    "ds_read_b64_tr_b16 a[96:97], v28 offset:0\n"
    "ds_read_b64_tr_b16 a[98:99], v28 offset:4096\n"
    "ds_read_b64_tr_b16 a[120:121], v29 offset:0\n"
    "ds_read_b64_tr_b16 a[122:123], v29 offset:4096\n"
    "ds_read_b64_tr_b16 a[144:145], v30 offset:0\n"
    "ds_read_b64_tr_b16 a[146:147], v30 offset:4096\n"
    "ds_read_b64_tr_b16 a[168:169], v31 offset:0\n"
    "ds_read_b64_tr_b16 a[170:171], v31 offset:4096\n"
    "ds_read_b64_tr_b16 a[192:193], v32 offset:0\n"
    "ds_read_b64_tr_b16 a[194:195], v32 offset:4096\n"
    "ds_read_b64_tr_b16 a[216:217], v33 offset:0\n"
    "ds_read_b64_tr_b16 a[218:219], v33 offset:4096\n"
    "s_waitcnt lgkmcnt(0)\n"
    "s_barrier\n"
    "s_mov_b32 s49, 61440\n"
    "s_add_u32 m0, s42, 0\n"
    "s_nop 0\n"
    "buffer_load_dwordx4 v12, s[4:7], s49 offen offset:0 nt lds\n"
    "buffer_load_dwordx4 v13, s[4:7], s49 offen offset:1024 nt lds\n"
    "buffer_load_dwordx4 v14, s[4:7], s49 offen offset:2048 nt lds\n"
    "buffer_load_dwordx4 v15, s[4:7], s49 offen offset:3072 nt lds\n"
    "s_add_u32 m0, s43, 32768\n"
    "s_nop 0\n"
    "buffer_load_dwordx4 v16, s[4:7], s49 offen nt lds\n"
    "s_mov_b32 s49, 143360\n"
    "s_add_u32 m0, s42, 4096\n"
    "s_nop 0\n"
    "buffer_load_dwordx4 v12, s[4:7], s49 offen offset:0 nt lds\n"
    "buffer_load_dwordx4 v13, s[4:7], s49 offen offset:1024 nt lds\n"
    "buffer_load_dwordx4 v14, s[4:7], s49 offen offset:2048 nt lds\n"
    "buffer_load_dwordx4 v15, s[4:7], s49 offen offset:3072 nt lds\n"
    "s_add_u32 m0, s43, 36864\n"
    "s_nop 0\n"
    "buffer_load_dwordx4 v16, s[4:7], s49 offen nt lds\n"
    "s_waitcnt vmcnt(23)\n"
    "s_barrier\n"
    "v_add_u32 v24, 49152, v17\n"
    "v_add_u32 v32, 49152, v18\n"
    "v_xor_b32 v33, 32, v32\n"
    "v_xor_b32 v25, 32, v24\n"
    "v_xor_b32 v26, 64, v24\n"
    "v_xor_b32 v27, 96, v24\n"
    "v_xor_b32 v28, 128, v24\n"
    "v_xor_b32 v29, 160, v24\n"
    "v_xor_b32 v30, 192, v24\n"
    "v_xor_b32 v31, 224, v24\n"
    "ds_read_b64_tr_b16 a[4:5], v24 offset:0\n"
    "ds_read_b64_tr_b16 a[6:7], v24 offset:4096\n"
    "ds_read_b64_tr_b16 a[28:29], v25 offset:0\n"
    "ds_read_b64_tr_b16 a[30:31], v25 offset:4096\n"
    "ds_read_b64_tr_b16 a[52:53], v26 offset:0\n"
    "ds_read_b64_tr_b16 a[54:55], v26 offset:4096\n"
    "ds_read_b64_tr_b16 a[76:77], v27 offset:0\n"
    "ds_read_b64_tr_b16 a[78:79], v27 offset:4096\n"
    "ds_read_b64_tr_b16 a[100:101], v28 offset:0\n"
    "ds_read_b64_tr_b16 a[102:103], v28 offset:4096\n"
    "ds_read_b64_tr_b16 a[124:125], v29 offset:0\n"
    "ds_read_b64_tr_b16 a[126:127], v29 offset:4096\n"
    "ds_read_b64_tr_b16 a[148:149], v30 offset:0\n"
    "ds_read_b64_tr_b16 a[150:151], v30 offset:4096\n"
    "ds_read_b64_tr_b16 a[172:173], v31 offset:0\n"
    "ds_read_b64_tr_b16 a[174:175], v31 offset:4096\n"
    "ds_read_b64_tr_b16 a[196:197], v32 offset:0\n"
    "ds_read_b64_tr_b16 a[198:199], v32 offset:4096\n"
    "ds_read_b64_tr_b16 a[220:221], v33 offset:0\n"
    "ds_read_b64_tr_b16 a[222:223], v33 offset:4096\n"
    "s_waitcnt lgkmcnt(0)\n"
    "s_barrier\n"
    "s_mov_b32 s49, 163840\n"
    "s_add_u32 m0, s42, 49152\n"
    "s_nop 0\n"
    "buffer_load_dwordx4 v12, s[4:7], s49 offen offset:0 nt lds\n"
    "buffer_load_dwordx4 v13, s[4:7], s49 offen offset:1024 nt lds\n"
    "buffer_load_dwordx4 v14, s[4:7], s49 offen offset:2048 nt lds\n"
    "buffer_load_dwordx4 v15, s[4:7], s49 offen offset:3072 nt lds\n"
    "s_add_u32 m0, s43, 81920\n"
    "s_nop 0\n"
    "buffer_load_dwordx4 v16, s[4:7], s49 offen nt lds\n"
    "s_mov_b32 s49, 184320\n"
    "s_add_u32 m0, s42, 53248\n"
    "s_nop 0\n"
    "buffer_load_dwordx4 v12, s[4:7], s49 offen offset:0 nt lds\n"
    "buffer_load_dwordx4 v13, s[4:7], s49 offen offset:1024 nt lds\n"
    "buffer_load_dwordx4 v14, s[4:7], s49 offen offset:2048 nt lds\n"
    "buffer_load_dwordx4 v15, s[4:7], s49 offen offset:3072 nt lds\n"
    "s_add_u32 m0, s43, 86016\n"
    "s_nop 0\n"
    "buffer_load_dwordx4 v16, s[4:7], s49 offen nt lds\n"
    "v_and_b32 v108, 0x70, v96\n"
    "v_lshlrev_b32 v108, 2, v108\n"
    "v_or_b32 v108, 0x8000, v108\n"
    "buffer_load_dword v109, v108, s[0:3], 0 offen sc1\n"
    "s_waitcnt vmcnt(24)\n"
    "s_barrier\n"
    "v_add_u32 v24, 98304, v17\n"
    "v_add_u32 v32, 98304, v18\n"
    "v_xor_b32 v33, 32, v32\n"
    "v_xor_b32 v25, 32, v24\n"
    "v_xor_b32 v26, 64, v24\n"
    "v_xor_b32 v27, 96, v24\n"
    "v_xor_b32 v28, 128, v24\n"
    "v_xor_b32 v29, 160, v24\n"
    "v_xor_b32 v30, 192, v24\n"
    "v_xor_b32 v31, 224, v24\n"
    "ds_read_b64_tr_b16 a[8:9], v24 offset:0\n"
    "ds_read_b64_tr_b16 a[10:11], v24 offset:4096\n"
    "ds_read_b64_tr_b16 a[32:33], v25 offset:0\n"
    "ds_read_b64_tr_b16 a[34:35], v25 offset:4096\n"
    "ds_read_b64_tr_b16 a[56:57], v26 offset:0\n"
    "ds_read_b64_tr_b16 a[58:59], v26 offset:4096\n"
    "ds_read_b64_tr_b16 a[80:81], v27 offset:0\n"
    "ds_read_b64_tr_b16 a[82:83], v27 offset:4096\n"
    "ds_read_b64_tr_b16 a[104:105], v28 offset:0\n"
    "ds_read_b64_tr_b16 a[106:107], v28 offset:4096\n"
    "ds_read_b64_tr_b16 a[128:129], v29 offset:0\n"
    "ds_read_b64_tr_b16 a[130:131], v29 offset:4096\n"
    "ds_read_b64_tr_b16 a[152:153], v30 offset:0\n"
    "ds_read_b64_tr_b16 a[154:155], v30 offset:4096\n"
    "ds_read_b64_tr_b16 a[176:177], v31 offset:0\n"
    "ds_read_b64_tr_b16 a[178:179], v31 offset:4096\n"
    "ds_read_b64_tr_b16 a[200:201], v32 offset:0\n"
    "ds_read_b64_tr_b16 a[202:203], v32 offset:4096\n"
    "ds_read_b64_tr_b16 a[224:225], v33 offset:0\n"
    "ds_read_b64_tr_b16 a[226:227], v33 offset:4096\n"
    "s_waitcnt lgkmcnt(0)\n"
    "s_barrier\n"
    "s_mov_b32 s49, 204800\n"
    "s_add_u32 m0, s42, 98304\n"
    "s_nop 0\n"
    "buffer_load_dwordx4 v12, s[4:7], s49 offen offset:0 nt lds\n"
    "buffer_load_dwordx4 v13, s[4:7], s49 offen offset:1024 nt lds\n"
    "buffer_load_dwordx4 v14, s[4:7], s49 offen offset:2048 nt lds\n"
    "buffer_load_dwordx4 v15, s[4:7], s49 offen offset:3072 nt lds\n"
    "s_add_u32 m0, s43, 131072\n"
    "s_nop 0\n"
    "buffer_load_dwordx4 v16, s[4:7], s49 offen nt lds\n"
    "s_mov_b32 s49, 225280\n"
    "s_add_u32 m0, s42, 102400\n"
    "s_nop 0\n"
    "buffer_load_dwordx4 v12, s[4:7], s49 offen offset:0 nt lds\n"
    "buffer_load_dwordx4 v13, s[4:7], s49 offen offset:1024 nt lds\n"
    "buffer_load_dwordx4 v14, s[4:7], s49 offen offset:2048 nt lds\n"
    "buffer_load_dwordx4 v15, s[4:7], s49 offen offset:3072 nt lds\n"
    "s_add_u32 m0, s43, 135168\n"
    "s_nop 0\n"
    "buffer_load_dwordx4 v16, s[4:7], s49 offen nt lds\n"
    "s_waitcnt vmcnt(21)\n"
    "s_barrier\n"
    "v_add_u32 v24, 0, v17\n"
    "v_add_u32 v32, 0, v18\n"
    "v_xor_b32 v33, 32, v32\n"
    "v_xor_b32 v25, 32, v24\n"
    "v_xor_b32 v26, 64, v24\n"
    "v_xor_b32 v27, 96, v24\n"
    "v_xor_b32 v28, 128, v24\n"
    "v_xor_b32 v29, 160, v24\n"
    "v_xor_b32 v30, 192, v24\n"
    "v_xor_b32 v31, 224, v24\n"
    "ds_read_b64_tr_b16 a[12:13], v24 offset:0\n"
    "ds_read_b64_tr_b16 a[14:15], v24 offset:4096\n"
    "ds_read_b64_tr_b16 a[36:37], v25 offset:0\n"
    "ds_read_b64_tr_b16 a[38:39], v25 offset:4096\n"
    "ds_read_b64_tr_b16 a[60:61], v26 offset:0\n"
    "ds_read_b64_tr_b16 a[62:63], v26 offset:4096\n"
    "ds_read_b64_tr_b16 a[84:85], v27 offset:0\n"
    "ds_read_b64_tr_b16 a[86:87], v27 offset:4096\n"
    "ds_read_b64_tr_b16 a[108:109], v28 offset:0\n"
    "ds_read_b64_tr_b16 a[110:111], v28 offset:4096\n"
    "ds_read_b64_tr_b16 a[132:133], v29 offset:0\n"
    "ds_read_b64_tr_b16 a[134:135], v29 offset:4096\n"
    "ds_read_b64_tr_b16 a[156:157], v30 offset:0\n"
    "ds_read_b64_tr_b16 a[158:159], v30 offset:4096\n"
    "ds_read_b64_tr_b16 a[180:181], v31 offset:0\n"
    "ds_read_b64_tr_b16 a[182:183], v31 offset:4096\n"
    "ds_read_b64_tr_b16 a[204:205], v32 offset:0\n"
    "ds_read_b64_tr_b16 a[206:207], v32 offset:4096\n"
    "ds_read_b64_tr_b16 a[228:229], v33 offset:0\n"
    "ds_read_b64_tr_b16 a[230:231], v33 offset:4096\n"
    "s_waitcnt lgkmcnt(0)\n"
    "s_barrier\n"
    "s_mov_b32 s49, 0\n"
    "s_add_u32 m0, s41, 0\n"
    "s_nop 0\n"
    "buffer_load_dwordx4 v0, s[8:11], s49 offen offset:0 nt lds\n"
    "buffer_load_dwordx4 v1, s[8:11], s49 offen offset:1024 nt lds\n"
    "buffer_load_dwordx4 v2, s[8:11], s49 offen offset:2048 nt lds\n"
    "s_mov_b32 s49, 1572864\n"
    "s_add_u32 m0, s41, 12288\n"
    "s_nop 0\n"
    "buffer_load_dwordx4 v0, s[8:11], s49 offen offset:0 nt lds\n"
    "buffer_load_dwordx4 v1, s[8:11], s49 offen offset:1024 nt lds\n"
    "buffer_load_dwordx4 v2, s[8:11], s49 offen offset:2048 nt lds\n"
    "s_mov_b32 s49, 3145728\n"
    "s_add_u32 m0, s41, 24576\n"
    "s_nop 0\n"
    "buffer_load_dwordx4 v0, s[8:11], s49 offen offset:0 nt lds\n"
    "buffer_load_dwordx4 v1, s[8:11], s49 offen offset:1024 nt lds\n"
    "buffer_load_dwordx4 v2, s[8:11], s49 offen offset:2048 nt lds\n"
    "s_mov_b32 s49, 4718592\n"
    "s_add_u32 m0, s41, 36864\n"
    "s_nop 0\n"
    "buffer_load_dwordx4 v0, s[8:11], s49 offen offset:0 nt lds\n"
    "buffer_load_dwordx4 v1, s[8:11], s49 offen offset:1024 nt lds\n"
    "buffer_load_dwordx4 v2, s[8:11], s49 offen offset:2048 nt lds\n"
    "s_waitcnt vmcnt(23)\n"
    "s_barrier\n"
    "v_add_u32 v24, 49152, v17\n"
    "v_add_u32 v32, 49152, v18\n"
    "v_xor_b32 v33, 32, v32\n"
    "v_xor_b32 v25, 32, v24\n"
    "v_xor_b32 v26, 64, v24\n"
    "v_xor_b32 v27, 96, v24\n"
    "v_xor_b32 v28, 128, v24\n"
    "v_xor_b32 v29, 160, v24\n"
    "v_xor_b32 v30, 192, v24\n"
    "v_xor_b32 v31, 224, v24\n"
    "ds_read_b64_tr_b16 a[16:17], v24 offset:0\n"
    "ds_read_b64_tr_b16 a[18:19], v24 offset:4096\n"
    "ds_read_b64_tr_b16 a[40:41], v25 offset:0\n"
    "ds_read_b64_tr_b16 a[42:43], v25 offset:4096\n"
    "ds_read_b64_tr_b16 a[64:65], v26 offset:0\n"
    "ds_read_b64_tr_b16 a[66:67], v26 offset:4096\n"
    "ds_read_b64_tr_b16 a[88:89], v27 offset:0\n"
    "ds_read_b64_tr_b16 a[90:91], v27 offset:4096\n"
    "ds_read_b64_tr_b16 a[112:113], v28 offset:0\n"
    "ds_read_b64_tr_b16 a[114:115], v28 offset:4096\n"
    "ds_read_b64_tr_b16 a[136:137], v29 offset:0\n"
    "ds_read_b64_tr_b16 a[138:139], v29 offset:4096\n"
    "ds_read_b64_tr_b16 a[160:161], v30 offset:0\n"
    "ds_read_b64_tr_b16 a[162:163], v30 offset:4096\n"
    "ds_read_b64_tr_b16 a[184:185], v31 offset:0\n"
    "ds_read_b64_tr_b16 a[186:187], v31 offset:4096\n"
    "ds_read_b64_tr_b16 a[208:209], v32 offset:0\n"
    "ds_read_b64_tr_b16 a[210:211], v32 offset:4096\n"
    "ds_read_b64_tr_b16 a[232:233], v33 offset:0\n"
    "ds_read_b64_tr_b16 a[234:235], v33 offset:4096\n"
    "s_waitcnt lgkmcnt(0)\n"
    "s_barrier\n"
    "s_mov_b32 s49, 393216\n"
    "s_add_u32 m0, s41, 49152\n"
    "s_nop 0\n"
    "buffer_load_dwordx4 v0, s[8:11], s49 offen offset:0 nt lds\n"
    "buffer_load_dwordx4 v1, s[8:11], s49 offen offset:1024 nt lds\n"
    "buffer_load_dwordx4 v2, s[8:11], s49 offen offset:2048 nt lds\n"
    "s_mov_b32 s49, 1966080\n"
    "s_add_u32 m0, s41, 61440\n"
    "s_nop 0\n"
    "buffer_load_dwordx4 v0, s[8:11], s49 offen offset:0 nt lds\n"
    "buffer_load_dwordx4 v1, s[8:11], s49 offen offset:1024 nt lds\n"
    "buffer_load_dwordx4 v2, s[8:11], s49 offen offset:2048 nt lds\n"
    "s_mov_b32 s49, 3538944\n"
    "s_add_u32 m0, s41, 73728\n"
    "s_nop 0\n"
    "buffer_load_dwordx4 v0, s[8:11], s49 offen offset:0 nt lds\n"
    "buffer_load_dwordx4 v1, s[8:11], s49 offen offset:1024 nt lds\n"
    "buffer_load_dwordx4 v2, s[8:11], s49 offen offset:2048 nt lds\n"
    "s_mov_b32 s49, 5111808\n"
    "s_add_u32 m0, s41, 86016\n"
    "s_nop 0\n"
    "buffer_load_dwordx4 v0, s[8:11], s49 offen offset:0 nt lds\n"
    "buffer_load_dwordx4 v1, s[8:11], s49 offen offset:1024 nt lds\n"
    "buffer_load_dwordx4 v2, s[8:11], s49 offen offset:2048 nt lds\n"
    "s_waitcnt vmcnt(24)\n"
    "s_barrier\n"
    "v_add_u32 v24, 98304, v17\n"
    "v_add_u32 v32, 98304, v18\n"
    "v_xor_b32 v33, 32, v32\n"
    "v_xor_b32 v25, 32, v24\n"
    "v_xor_b32 v26, 64, v24\n"
    "v_xor_b32 v27, 96, v24\n"
    "v_xor_b32 v28, 128, v24\n"
    "v_xor_b32 v29, 160, v24\n"
    "v_xor_b32 v30, 192, v24\n"
    "v_xor_b32 v31, 224, v24\n"
    "ds_read_b64_tr_b16 a[20:21], v24 offset:0\n"
    "ds_read_b64_tr_b16 a[22:23], v24 offset:4096\n"
    "ds_read_b64_tr_b16 a[44:45], v25 offset:0\n"
    "ds_read_b64_tr_b16 a[46:47], v25 offset:4096\n"
    "ds_read_b64_tr_b16 a[68:69], v26 offset:0\n"
    "ds_read_b64_tr_b16 a[70:71], v26 offset:4096\n"
    "ds_read_b64_tr_b16 a[92:93], v27 offset:0\n"
    "ds_read_b64_tr_b16 a[94:95], v27 offset:4096\n"
    "ds_read_b64_tr_b16 a[116:117], v28 offset:0\n"
    "ds_read_b64_tr_b16 a[118:119], v28 offset:4096\n"
    "ds_read_b64_tr_b16 a[140:141], v29 offset:0\n"
    "ds_read_b64_tr_b16 a[142:143], v29 offset:4096\n"
    "ds_read_b64_tr_b16 a[164:165], v30 offset:0\n"
    "ds_read_b64_tr_b16 a[166:167], v30 offset:4096\n"
    "ds_read_b64_tr_b16 a[188:189], v31 offset:0\n"
    "ds_read_b64_tr_b16 a[190:191], v31 offset:4096\n"
    "ds_read_b64_tr_b16 a[212:213], v32 offset:0\n"
    "ds_read_b64_tr_b16 a[214:215], v32 offset:4096\n"
    "ds_read_b64_tr_b16 a[236:237], v33 offset:0\n"
    "ds_read_b64_tr_b16 a[238:239], v33 offset:4096\n"
    "s_waitcnt lgkmcnt(0)\n"
    "s_barrier\n"
    "s_mov_b32 s49, 786432\n"
    "s_add_u32 m0, s41, 98304\n"
    "s_nop 0\n"
    "buffer_load_dwordx4 v0, s[8:11], s49 offen offset:0 nt lds\n"
    "buffer_load_dwordx4 v1, s[8:11], s49 offen offset:1024 nt lds\n"
    "buffer_load_dwordx4 v2, s[8:11], s49 offen offset:2048 nt lds\n"
    "s_mov_b32 s49, 2359296\n"
    "s_add_u32 m0, s41, 110592\n"
    "s_nop 0\n"
    "buffer_load_dwordx4 v0, s[8:11], s49 offen offset:0 nt lds\n"
    "buffer_load_dwordx4 v1, s[8:11], s49 offen offset:1024 nt lds\n"
    "buffer_load_dwordx4 v2, s[8:11], s49 offen offset:2048 nt lds\n"
    "s_mov_b32 s49, 3932160\n"
    "s_add_u32 m0, s41, 122880\n"
    "s_nop 0\n"
    "buffer_load_dwordx4 v0, s[8:11], s49 offen offset:0 nt lds\n"
    "buffer_load_dwordx4 v1, s[8:11], s49 offen offset:1024 nt lds\n"
    "buffer_load_dwordx4 v2, s[8:11], s49 offen offset:2048 nt lds\n"
    "s_mov_b32 s49, 5505024\n"
    "s_add_u32 m0, s41, 135168\n"
    "s_nop 0\n"
    "buffer_load_dwordx4 v0, s[8:11], s49 offen offset:0 nt lds\n"
    "buffer_load_dwordx4 v1, s[8:11], s49 offen offset:1024 nt lds\n"
    "buffer_load_dwordx4 v2, s[8:11], s49 offen offset:2048 nt lds\n"
    "s_waitcnt vmcnt(24)\n"
    "s_barrier\n"
    "v_add_u32 v8, 0, v3\n"
    "ds_read_b64_tr_b16 a[240:241], v8 offset:0\n"
    "ds_read_b64_tr_b16 a[242:243], v8 offset:768\n"
    "ds_read_b64_tr_b16 v[176:177], v8 offset:128\n"
    "ds_read_b64_tr_b16 v[178:179], v8 offset:896\n"
    "ds_read_b64_tr_b16 v[12:13], v8 offset:256\n"
    "ds_read_b64_tr_b16 v[14:15], v8 offset:1024\n"
    "v_add_u32 v9, 0, v4\n"
    "ds_read_b64_tr_b16 v[112:113], v9 offset:0\n"
    "ds_read_b64_tr_b16 v[114:115], v9 offset:768\n"
    "ds_read_b64_tr_b16 v[192:193], v9 offset:128\n"
    "ds_read_b64_tr_b16 v[194:195], v9 offset:896\n"
    "ds_read_b64_tr_b16 v[28:29], v9 offset:256\n"
    "ds_read_b64_tr_b16 v[30:31], v9 offset:1024\n"
    "v_add_u32 v10, 0, v5\n"
    "ds_read_b64_tr_b16 v[128:129], v10 offset:0\n"
    "ds_read_b64_tr_b16 v[130:131], v10 offset:768\n"
    "ds_read_b64_tr_b16 v[208:209], v10 offset:128\n"
    "ds_read_b64_tr_b16 v[210:211], v10 offset:896\n"
    "ds_read_b64_tr_b16 v[44:45], v10 offset:256\n"
    "ds_read_b64_tr_b16 v[46:47], v10 offset:1024\n"
    "v_add_u32 v11, 0, v6\n"
    "ds_read_b64_tr_b16 v[144:145], v11 offset:0\n"
    "ds_read_b64_tr_b16 v[146:147], v11 offset:768\n"
    "ds_read_b64_tr_b16 v[224:225], v11 offset:128\n"
    "ds_read_b64_tr_b16 v[226:227], v11 offset:896\n"
    "ds_read_b64_tr_b16 v[60:61], v11 offset:256\n"
    "ds_read_b64_tr_b16 v[62:63], v11 offset:1024\n"
    "s_waitcnt lgkmcnt(0)\n"
    "s_barrier\n"
    "s_mov_b32 s49, 1179648\n"
    "s_add_u32 m0, s41, 0\n"
    "s_nop 0\n"
    "buffer_load_dwordx4 v0, s[8:11], s49 offen offset:0 nt lds\n"
    "buffer_load_dwordx4 v1, s[8:11], s49 offen offset:1024 nt lds\n"
    "buffer_load_dwordx4 v2, s[8:11], s49 offen offset:2048 nt lds\n"
    "s_mov_b32 s49, 2752512\n"
    "s_add_u32 m0, s41, 12288\n"
    "s_nop 0\n"
    "buffer_load_dwordx4 v0, s[8:11], s49 offen offset:0 nt lds\n"
    "buffer_load_dwordx4 v1, s[8:11], s49 offen offset:1024 nt lds\n"
    "buffer_load_dwordx4 v2, s[8:11], s49 offen offset:2048 nt lds\n"
    "s_mov_b32 s49, 4325376\n"
    "s_add_u32 m0, s41, 24576\n"
    "s_nop 0\n"
    "buffer_load_dwordx4 v0, s[8:11], s49 offen offset:0 nt lds\n"
    "buffer_load_dwordx4 v1, s[8:11], s49 offen offset:1024 nt lds\n"
    "buffer_load_dwordx4 v2, s[8:11], s49 offen offset:2048 nt lds\n"
    "s_mov_b32 s49, 5898240\n"
    "s_add_u32 m0, s41, 36864\n"
    "s_nop 0\n"
    "buffer_load_dwordx4 v0, s[8:11], s49 offen offset:0 nt lds\n"
    "buffer_load_dwordx4 v1, s[8:11], s49 offen offset:1024 nt lds\n"
    "buffer_load_dwordx4 v2, s[8:11], s49 offen offset:2048 nt lds\n"
    "s_waitcnt vmcnt(24)\n"
    "s_barrier\n"
    "v_add_u32 v8, 49152, v3\n"
    "ds_read_b64_tr_b16 a[244:245], v8 offset:0\n"
    "ds_read_b64_tr_b16 a[246:247], v8 offset:768\n"
    "ds_read_b64_tr_b16 v[180:181], v8 offset:128\n"
    "ds_read_b64_tr_b16 v[182:183], v8 offset:896\n"
    "ds_read_b64_tr_b16 v[16:17], v8 offset:256\n"
    "ds_read_b64_tr_b16 v[18:19], v8 offset:1024\n"
    "v_add_u32 v9, 49152, v4\n"
    "ds_read_b64_tr_b16 v[116:117], v9 offset:0\n"
    "ds_read_b64_tr_b16 v[118:119], v9 offset:768\n"
    "ds_read_b64_tr_b16 v[196:197], v9 offset:128\n"
    "ds_read_b64_tr_b16 v[198:199], v9 offset:896\n"
    "ds_read_b64_tr_b16 v[32:33], v9 offset:256\n"
    "ds_read_b64_tr_b16 v[34:35], v9 offset:1024\n"
    "v_add_u32 v10, 49152, v5\n"
    "ds_read_b64_tr_b16 v[132:133], v10 offset:0\n"
    "ds_read_b64_tr_b16 v[134:135], v10 offset:768\n"
    "ds_read_b64_tr_b16 v[212:213], v10 offset:128\n"
    "ds_read_b64_tr_b16 v[214:215], v10 offset:896\n"
    "ds_read_b64_tr_b16 v[48:49], v10 offset:256\n"
    "ds_read_b64_tr_b16 v[50:51], v10 offset:1024\n"
    "v_add_u32 v11, 49152, v6\n"
    "ds_read_b64_tr_b16 v[148:149], v11 offset:0\n"
    "ds_read_b64_tr_b16 v[150:151], v11 offset:768\n"
    "ds_read_b64_tr_b16 v[228:229], v11 offset:128\n"
    "ds_read_b64_tr_b16 v[230:231], v11 offset:896\n"
    "ds_read_b64_tr_b16 v[64:65], v11 offset:256\n"
    "ds_read_b64_tr_b16 v[66:67], v11 offset:1024\n"
    "s_waitcnt lgkmcnt(0)\n"
    "s_barrier\n"
    "s_waitcnt vmcnt(58)\n"
    "v_cmp_gt_i32 vcc, 32, v109\n"
    "s_nop 0\n"
    "s_cbranch_vccnz .Lvo_tk0_%=\n"
    ".Lvo_tk1_%=:\n"
    "s_lshl_b32 s48, s12, 11\n"
    "s_add_u32 s48, s48, 53248\n"
    "v_lshrrev_b32 v108, 2, v96\n"
    "s_mov_b32 s50, 0\n"
    "s_mov_b32 s51, s14\n"
    "s_add_u32 s43, s18, s13\n"
    "v_mov_b32 v111, s50\n"
    "s_mov_b64 exec, 0x1f0\n"
    "v_mov_b32 v111, s51\n"
    "s_mov_b64 exec, -1\n"
    "buffer_load_dword v109, v108, s[0:3], s24 offen sc1\n"
    "buffer_load_dword v110, v105, s[0:3], 0 offen sc1\n"
    "s_mov_b32 s49, 6291456\n"
    "s_add_u32 m0, s41, 49152\n"
    "s_nop 0\n"
    "buffer_load_dwordx4 v0, s[8:11], s49 offen offset:0 nt lds\n"
    "buffer_load_dwordx4 v1, s[8:11], s49 offen offset:1024 nt lds\n"
    "buffer_load_dwordx4 v2, s[8:11], s49 offen offset:2048 nt lds\n"
    "s_mov_b32 s49, 6684672\n"
    "s_add_u32 m0, s41, 61440\n"
    "s_nop 0\n"
    "buffer_load_dwordx4 v0, s[8:11], s49 offen offset:0 nt lds\n"
    "buffer_load_dwordx4 v1, s[8:11], s49 offen offset:1024 nt lds\n"
    "buffer_load_dwordx4 v2, s[8:11], s49 offen offset:2048 nt lds\n"
    "s_mov_b32 s49, 7077888\n"
    "s_add_u32 m0, s41, 73728\n"
    "s_nop 0\n"
    "buffer_load_dwordx4 v0, s[8:11], s49 offen offset:0 nt lds\n"
    "buffer_load_dwordx4 v1, s[8:11], s49 offen offset:1024 nt lds\n"
    "buffer_load_dwordx4 v2, s[8:11], s49 offen offset:2048 nt lds\n"
    "s_mov_b32 s49, 7471104\n"
    "s_add_u32 m0, s41, 86016\n"
    "s_nop 0\n"
    "buffer_load_dwordx4 v0, s[8:11], s49 offen offset:0 nt lds\n"
    "buffer_load_dwordx4 v1, s[8:11], s49 offen offset:1024 nt lds\n"
    "buffer_load_dwordx4 v2, s[8:11], s49 offen offset:2048 nt lds\n"
    "s_waitcnt vmcnt(26)\n"
    "s_barrier\n"
    "v_add_u32 v8, 98304, v3\n"
    "ds_read_b64_tr_b16 a[248:249], v8 offset:0\n"
    "ds_read_b64_tr_b16 a[250:251], v8 offset:768\n"
    "ds_read_b64_tr_b16 v[184:185], v8 offset:128\n"
    "ds_read_b64_tr_b16 v[186:187], v8 offset:896\n"
    "ds_read_b64_tr_b16 v[20:21], v8 offset:256\n"
    "ds_read_b64_tr_b16 v[22:23], v8 offset:1024\n"
    "v_add_u32 v9, 98304, v4\n"
    "ds_read_b64_tr_b16 v[120:121], v9 offset:0\n"
    "ds_read_b64_tr_b16 v[122:123], v9 offset:768\n"
    "ds_read_b64_tr_b16 v[200:201], v9 offset:128\n"
    "ds_read_b64_tr_b16 v[202:203], v9 offset:896\n"
    "ds_read_b64_tr_b16 v[36:37], v9 offset:256\n"
    "ds_read_b64_tr_b16 v[38:39], v9 offset:1024\n"
    "v_add_u32 v10, 98304, v5\n"
    "ds_read_b64_tr_b16 v[136:137], v10 offset:0\n"
    "ds_read_b64_tr_b16 v[138:139], v10 offset:768\n"
    "ds_read_b64_tr_b16 v[216:217], v10 offset:128\n"
    "ds_read_b64_tr_b16 v[218:219], v10 offset:896\n"
    "ds_read_b64_tr_b16 v[52:53], v10 offset:256\n"
    "ds_read_b64_tr_b16 v[54:55], v10 offset:1024\n"
    "v_add_u32 v11, 98304, v6\n"
    "ds_read_b64_tr_b16 v[152:153], v11 offset:0\n"
    "ds_read_b64_tr_b16 v[154:155], v11 offset:768\n"
    "ds_read_b64_tr_b16 v[232:233], v11 offset:128\n"
    "ds_read_b64_tr_b16 v[234:235], v11 offset:896\n"
    "ds_read_b64_tr_b16 v[68:69], v11 offset:256\n"
    "ds_read_b64_tr_b16 v[70:71], v11 offset:1024\n"
    "s_waitcnt lgkmcnt(0)\n"
    "s_barrier\n"
    "s_waitcnt vmcnt(14)\n"
    "s_barrier\n"
    "v_add_u32 v8, 0, v3\n"
    "ds_read_b64_tr_b16 a[252:253], v8 offset:0\n"
    "ds_read_b64_tr_b16 a[254:255], v8 offset:768\n"
    "ds_read_b64_tr_b16 v[188:189], v8 offset:128\n"
    "ds_read_b64_tr_b16 v[190:191], v8 offset:896\n"
    "ds_read_b64_tr_b16 v[24:25], v8 offset:256\n"
    "ds_read_b64_tr_b16 v[26:27], v8 offset:1024\n"
    "v_add_u32 v9, 0, v4\n"
    "ds_read_b64_tr_b16 v[124:125], v9 offset:0\n"
    "ds_read_b64_tr_b16 v[126:127], v9 offset:768\n"
    "ds_read_b64_tr_b16 v[204:205], v9 offset:128\n"
    "ds_read_b64_tr_b16 v[206:207], v9 offset:896\n"
    "ds_read_b64_tr_b16 v[40:41], v9 offset:256\n"
    "ds_read_b64_tr_b16 v[42:43], v9 offset:1024\n"
    "v_add_u32 v10, 0, v5\n"
    "ds_read_b64_tr_b16 v[140:141], v10 offset:0\n"
    "ds_read_b64_tr_b16 v[142:143], v10 offset:768\n"
    "ds_read_b64_tr_b16 v[220:221], v10 offset:128\n"
    "ds_read_b64_tr_b16 v[222:223], v10 offset:896\n"
    "ds_read_b64_tr_b16 v[56:57], v10 offset:256\n"
    "ds_read_b64_tr_b16 v[58:59], v10 offset:1024\n"
    "v_add_u32 v11, 0, v6\n"
    "ds_read_b64_tr_b16 v[156:157], v11 offset:0\n"
    "ds_read_b64_tr_b16 v[158:159], v11 offset:768\n"
    "ds_read_b64_tr_b16 v[236:237], v11 offset:128\n"
    "ds_read_b64_tr_b16 v[238:239], v11 offset:896\n"
    "ds_read_b64_tr_b16 v[72:73], v11 offset:256\n"
    "ds_read_b64_tr_b16 v[74:75], v11 offset:1024\n"
    "s_waitcnt lgkmcnt(0)\n"
    "s_barrier\n"
    "s_waitcnt vmcnt(12)\n"
    "v_cmp_gt_i32 vcc, 1, v109\n"
    "s_nop 0\n"
    "s_cbranch_vccnz .Lvo_e0_%=\n"
    "v_cmp_gt_i32 vcc, v111, v110\n"
    "s_nop 0\n"
    "s_cbranch_vccnz .Lvo_e0_%=\n"
    ".Lvo_e1_%=:\n"
    "s_sub_u32 m0, s17, 8192\n"
    "s_nop 0\n"
    "buffer_load_dwordx4 v96, s[0:3], s43 offen offset:0 sc1 lds\n"
    "buffer_load_dwordx4 v96, s[0:3], s43 offen offset:1024 sc1 lds\n"
    "buffer_load_dwordx4 v96, s[0:3], s43 offen offset:2048 sc1 lds\n"
    "buffer_load_dwordx4 v96, s[0:3], s43 offen offset:3072 sc1 lds\n"
    "s_sub_u32 m0, s17, 4096\n"
    "s_add_u32 s43, s43, 4096\n"
    "buffer_load_dwordx4 v96, s[0:3], s43 offen offset:0 sc1 lds\n"
    "buffer_load_dwordx4 v96, s[0:3], s43 offen offset:1024 sc1 lds\n"
    "buffer_load_dwordx4 v96, s[0:3], s43 offen offset:2048 sc1 lds\n"
    "buffer_load_dwordx4 v96, s[0:3], s43 offen offset:3072 sc1 lds\n"
    "s_waitcnt vmcnt(8)\n"
    "s_barrier\n"
    "v_add_u32 v8, 49152, v7\n"
    "ds_read_b64_tr_b16 v[160:161], v8 offset:0\n"
    "ds_read_b64_tr_b16 v[162:163], v8 offset:768\n"
    "ds_read_b64_tr_b16 v[240:241], v8 offset:128\n"
    "ds_read_b64_tr_b16 v[242:243], v8 offset:896\n"
    "ds_read_b64_tr_b16 v[76:77], v8 offset:256\n"
    "ds_read_b64_tr_b16 v[78:79], v8 offset:1024\n"
    "v_add_u32 v9, 61440, v7\n"
    "ds_read_b64_tr_b16 v[164:165], v9 offset:0\n"
    "ds_read_b64_tr_b16 v[166:167], v9 offset:768\n"
    "ds_read_b64_tr_b16 v[244:245], v9 offset:128\n"
    "ds_read_b64_tr_b16 v[246:247], v9 offset:896\n"
    "ds_read_b64_tr_b16 v[80:81], v9 offset:256\n"
    "ds_read_b64_tr_b16 v[82:83], v9 offset:1024\n"
    "v_add_u32 v10, 73728, v7\n"
    "ds_read_b64_tr_b16 v[168:169], v10 offset:0\n"
    "ds_read_b64_tr_b16 v[170:171], v10 offset:768\n"
    "ds_read_b64_tr_b16 v[248:249], v10 offset:128\n"
    "ds_read_b64_tr_b16 v[250:251], v10 offset:896\n"
    "ds_read_b64_tr_b16 v[84:85], v10 offset:256\n"
    "ds_read_b64_tr_b16 v[86:87], v10 offset:1024\n"
    "v_add_u32 v11, 86016, v7\n"
    "ds_read_b64_tr_b16 v[172:173], v11 offset:0\n"
    "ds_read_b64_tr_b16 v[174:175], v11 offset:768\n"
    "ds_read_b64_tr_b16 v[252:253], v11 offset:128\n"
    "ds_read_b64_tr_b16 v[254:255], v11 offset:896\n"
    "ds_read_b64_tr_b16 v[88:89], v11 offset:256\n"
    "ds_read_b64_tr_b16 v[90:91], v11 offset:1024\n"
    "s_waitcnt lgkmcnt(0)\n"
    "s_barrier\n"
    "s_mov_b32 m0, s48\n"
    "s_add_u32 s43, s43, s19\n"
    "buffer_load_dwordx4 v96, s[0:3], s43 offen offset:0 sc1 lds\n"
    "buffer_load_dwordx4 v96, s[0:3], s43 offen offset:1024 sc1 lds\n"
    "ds_write_b128 v97, v[12:15] offset:0\n"
    "ds_write_b128 v97, v[16:19] offset:1024\n"
    "ds_write_b128 v97, v[20:23] offset:2048\n"
    "ds_write_b128 v97, v[24:27] offset:3072\n"
    "ds_write_b128 v97, v[28:31] offset:4096\n"
    "ds_write_b128 v97, v[32:35] offset:5120\n"
    "ds_write_b128 v97, v[36:39] offset:6144\n"
    "ds_write_b128 v97, v[40:43] offset:7168\n"
    "ds_write_b128 v97, v[44:47] offset:8192\n"
    "ds_write_b128 v97, v[48:51] offset:9216\n"
    "ds_write_b128 v97, v[52:55] offset:10240\n"
    "ds_write_b128 v97, v[56:59] offset:11264\n"
    "ds_write_b128 v97, v[60:63] offset:12288\n"
    "ds_write_b128 v97, v[64:67] offset:13312\n"
    "ds_write_b128 v97, v[68:71] offset:14336\n"
    "ds_write_b128 v97, v[72:75] offset:15360\n"
    "ds_write_b128 v97, v[76:79] offset:16384\n"
    "ds_write_b128 v97, v[80:83] offset:17408\n"
    "ds_write_b128 v97, v[84:87] offset:18432\n"
    "ds_write_b128 v97, v[88:91] offset:19456\n"
    "s_mov_b32 s34, -4\n"
    "s_memrealtime s[30:31]\n"
    "s_waitcnt vmcnt(0) lgkmcnt(0)\n"
    "s_barrier\n"
    "ds_read_b128 v[24:27], v92 offset:0\n"
    "ds_read_b128 v[28:31], v92 offset:1024\n"
    "ds_read_b128 v[32:35], v93 offset:0\n"
    "ds_read_b128 v[36:39], v93 offset:1024\n"
    "ds_read_b128 v[40:43], v94 offset:0\n"
    "ds_read_b128 v[44:47], v94 offset:1024\n"
    "s_sub_u32 s46, s30, 1300\n"
    "s_mov_b32 s34, -3\n"
    "s_mov_b32 s35, -5\n"
    "s_mov_b32 s45, s23\n"
    "s_mov_b32 s38, 2130706432\n"
    "s_mov_b32 s39, 2130706432\n"
    "s_mov_b32 s40, 2130706432\n"
    "s_waitcnt lgkmcnt(0)\n"
    "s_branch .Lvo_vph_%=\n"
    ".Lvo_take_%=:\n"
    "v_mfma_f32_16x16x32_bf16 v[84:87], v[248:251], v[24:27], v[84:87]\n"
    ".Lvo_mk8x5_%=:\n"
    "s_waitcnt vmcnt(3)\n"
    ".Lvo_mk0x6_%=:\n"
    "ds_read_b128 v[72:75], v92 offset:8192\n"
    "v_cvt_pk_bf16_f32 v40, v64, v65\n"
    "v_mfma_f32_16x16x32_bf16 v[84:87], v[252:255], v[32:35], v[84:87]\n"
    "s_waitcnt vmcnt(2)\n"
    "ds_read_b128 v[76:79], v92 offset:9216\n"
    "v_cvt_pk_bf16_f32 v41, v66, v67\n"
    "v_mfma_f32_16x16x32_bf16 v[68:71], v[164:167], v[44:47], v[68:71]\n"
    "v_mov_b64 v[64:65], 0\n"
    "v_mov_b64 v[66:67], 0\n"
    "v_mfma_f32_16x16x32_bf16 v[68:71], v[168:171], v[28:31], v[68:71]\n"
    "s_waitcnt lgkmcnt(2)\n"
    "ds_read_b128 v[24:27], v97 offset:6144\n"
    "v_cvt_pk_bf16_f32 v42, v84, v85\n"
    "v_mfma_f32_16x16x32_bf16 v[68:71], v[172:175], v[36:39], v[68:71]\n"
    "v_cvt_pk_bf16_f32 v43, v86, v87\n"
    "v_mov_b64 v[84:85], 0\n"
    "v_mfma_f32_16x16x32_bf16 v[88:91], v[244:247], v[44:47], v[88:91]\n"
    "ds_write_b128 v92, v[40:43] offset:11264\n"
    "v_mfma_f32_16x16x32_bf16 v[88:91], v[248:251], v[28:31], v[88:91]\n"
    "s_waitcnt lgkmcnt(2)\n"
    "v_max_u32 v108, v72, v74\n"
    "v_max3_u32 v108, v108, v76, v78\n"
    "v_mfma_f32_16x16x32_bf16 v[88:91], v[252:255], v[36:39], v[88:91]\n"
    "v_cmp_le_u32 vcc, s52, v108\n"
    "v_mov_b64 v[86:87], 0\n"
    "v_smfmac_f32_16x16x64_bf16 v[64:67], v[72:75], a[240:247], v98\n"
    "s_cbranch_vccnz .Lvo_retry_%=\n"
    ".Lvo_go_%=:\n"
    "s_barrier\n"
    ".Lvo_mk1x7_%=:\n"
    "ds_read_b128 v[32:35], v93 offset:8192\n"
    "v_cvt_pk_bf16_f32 v44, v68, v69\n"
    "v_smfmac_f32_16x16x64_bf16 v[64:67], v[76:79], a[248:255], v98\n"
    "ds_read_b128 v[36:39], v93 offset:9216\n"
    "v_cvt_pk_bf16_f32 v45, v70, v71\n"
    "v_smfmac_f32_16x16x64_bf16 v[84:87], v[72:75], v[0:7], v98\n"
    "s_waitcnt vmcnt(1)\n"
    "ds_read_b128 v[80:83], v92 offset:10240\n"
    "v_mov_b64 v[68:69], 0\n"
    "v_mov_b64 v[70:71], 0\n"
    "v_smfmac_f32_16x16x64_bf16 v[84:87], v[76:79], v[8:15], v98\n"
    "ds_read_b128 v[28:31], v97 offset:7168\n"
    "v_cvt_pk_bf16_f32 v46, v88, v89\n"
    "v_smfmac_f32_16x16x64_bf16 v[68:71], v[72:75], v[176:183], v98\n"
    "ds_read_b128 v[0:3], v97 offset:12288\n"
    "v_cvt_pk_bf16_f32 v47, v90, v91\n"
    "v_smfmac_f32_16x16x64_bf16 v[68:71], v[76:79], v[184:191], v98\n"
    "s_waitcnt lgkmcnt(4)\n"
    "ds_read_b128 v[88:91], v94 offset:8192\n"
    "ds_write_b128 v92, v[44:47] offset:12288\n"
    "v_smfmac_f32_16x16x64_bf16 v[84:87], v[32:35], v[16:23], v98\n"
    "ds_read_b128 v[40:43], v94 offset:9216\n"
    "v_smfmac_f32_16x16x64_bf16 v[64:67], v[32:35], v[112:119], v98\n"
    "s_waitcnt lgkmcnt(5)\n"
    "ds_read_b128 v[4:7], v97 offset:13312\n"
    "v_max_u32 v108, v80, v82\n"
    "v_smfmac_f32_16x16x64_bf16 v[68:71], v[32:35], v[192:199], v98\n"
    "s_waitcnt lgkmcnt(5)\n"
    "ds_read_b128 v[44:47], v95 offset:8192\n"
    "v_cmp_eq_u32 vcc, -1, v108\n"
    "v_smfmac_f32_16x16x64_bf16 v[84:87], v[36:39], v[24:31], v98\n"
    "ds_read_b128 v[8:11], v97 offset:14336\n"
    "ds_read_b128 v[12:15], v97 offset:15360\n"
    "v_smfmac_f32_16x16x64_bf16 v[64:67], v[36:39], v[120:127], v98\n"
    "ds_read_b128 v[16:19], v97 offset:16384\n"
    "v_smfmac_f32_16x16x64_bf16 v[68:71], v[36:39], v[200:207], v98\n"
    "s_waitcnt lgkmcnt(6)\n"
    "s_cbranch_vccnz .Lvo_r2_%=\n"
    ".Lvo_go2_%=:\n"
    "s_barrier\n"
    "ds_read_b128 v[72:75], v96 offset:10240\n"
    "v_smfmac_f32_16x16x64_bf16 v[84:87], v[88:91], v[48:55], v98\n"
    "s_mov_b32 m0, s21\n"
    "ds_read_b128 v[80:83], v95 offset:9216\n"
    "ds_read_b128 v[76:79], v96 offset:23552\n"
    "v_smfmac_f32_16x16x64_bf16 v[64:67], v[88:91], v[128:135], v98\n"
    "s_waitcnt vmcnt(0)\n"
    "ds_read_b128 v[20:23], v97 offset:17408\n"
    "v_cmp_gt_i32 vcc, v111, v110\n"
    "v_smfmac_f32_16x16x64_bf16 v[68:71], v[88:91], v[208:215], v98\n"
    "s_waitcnt lgkmcnt(7)\n"
    "s_cbranch_vccnz .Lvo_wf_%=\n"
    ".Lvo_wfb_%=:\n"
    "v_smfmac_f32_16x16x64_bf16 v[64:67], v[40:43], v[136:143], v98\n"
    "buffer_load_dwordx4 v96, s[0:3], s44 offen sc1 lds\n"
    "v_smfmac_f32_16x16x64_bf16 v[64:67], v[44:47], v[144:151], v98\n"
    "s_waitcnt lgkmcnt(3)\n"
    "ds_read_b128 v[24:27], v97 offset:18432\n"
    "v_mov_b64 v[32:33], 0\n"
    "v_smfmac_f32_16x16x64_bf16 v[64:67], v[72:75], v[160:167], v98\n"
    "s_waitcnt lgkmcnt(3)\n"
    "ds_read_b128 v[28:31], v97 offset:19456\n"
    "v_mov_b64 v[34:35], 0\n"
    "v_smfmac_f32_16x16x64_bf16 v[64:67], v[80:83], v[152:159], v98\n"
    "s_waitcnt lgkmcnt(3)\n"
    "v_mov_b64 v[36:37], 0\n"
    "v_smfmac_f32_16x16x64_bf16 v[64:67], v[76:79], v[168:175], v98\n"
    "s_sub_u32 m0, s17, 8192\n"
    "v_mov_b64 v[38:39], 0\n"
    "v_smfmac_f32_16x16x64_bf16 v[84:87], v[40:43], v[56:63], v98\n"
    "v_mov_b64 v[48:49], 0\n"
    "v_smfmac_f32_16x16x64_bf16 v[84:87], v[44:47], v[0:7], v98\n"
    "ds_read_b128 v[0:3], v96 offset:11264\n"
    "s_nop 0\n"
    "v_add_f32 v64, v64, v66\n"
    "v_smfmac_f32_16x16x64_bf16 v[84:87], v[80:83], v[8:15], v98\n"
    "s_waitcnt lgkmcnt(3)\n"
    "ds_read_b128 v[4:7], v96 offset:12288\n"
    "v_add_f32 v65, v65, v67\n"
    "v_smfmac_f32_16x16x64_bf16 v[84:87], v[72:75], v[16:23], v98\n"
    "s_waitcnt lgkmcnt(2)\n"
    "ds_read_b128 v[8:11], v96 offset:24576\n"
    "v_cvt_pk_bf16_f32 v64, v64, v65\n"
    "v_smfmac_f32_16x16x64_bf16 v[84:87], v[76:79], v[24:31], v98\n"
    "ds_write_b32 v99, v64 offset:0\n"
    "v_smfmac_f32_16x16x64_bf16 v[68:71], v[40:43], v[216:223], v98\n"
    "ds_read_b128 v[12:15], v96 offset:25600\n"
    "v_mov_b64 v[50:51], 0\n"
    "v_smfmac_f32_16x16x64_bf16 v[68:71], v[44:47], v[224:231], v98\n"
    "ds_read_b128 v[16:19], v96 offset:37888\n"
    "s_nop 0\n"
    "v_add_f32 v84, v84, v86\n"
    "v_smfmac_f32_16x16x64_bf16 v[68:71], v[80:83], v[232:239], v98\n"
    "ds_read_b128 v[20:23], v96 offset:38912\n"
    "v_add_f32 v85, v85, v87\n"
    "v_smfmac_f32_16x16x64_bf16 v[68:71], v[72:75], v[240:247], v98\n"
    "ds_read_b128 v[24:27], v96 offset:51200\n"
    "v_cvt_pk_bf16_f32 v84, v84, v85\n"
    "v_smfmac_f32_16x16x64_bf16 v[68:71], v[76:79], v[248:255], v98\n"
    "ds_write_b32 v100, v84 offset:0\n"
    "s_waitcnt lgkmcnt(7)\n"
    "v_mfma_f32_16x16x32_bf16 v[40:43], v[0:3], a[0:3], 0\n"
    "ds_read_b128 v[28:31], v96 offset:52224\n"
    "v_mov_b64 v[52:53], 0\n"
    "v_mfma_f32_16x16x32_bf16 v[44:47], v[4:7], a[0:3], 0\n"
    "s_nop 1\n"
    "v_add_f32 v68, v68, v70\n"
    "v_add_f32 v69, v69, v71\n"
    "v_cvt_pk_bf16_f32 v68, v68, v69\n"
    "ds_write_b32 v99, v68 offset:256\n"
    "s_waitcnt lgkmcnt(4)\n"
    "v_mfma_f32_16x16x32_bf16 v[40:43], v[8:11], a[4:7], v[40:43]\n"
    "v_mov_b64 v[54:55], 0\n"
    "v_mov_b64 v[56:57], 0\n"
    "v_mfma_f32_16x16x32_bf16 v[44:47], v[12:15], a[4:7], v[44:47]\n"
    "v_mov_b64 v[58:59], 0\n"
    "v_mov_b64 v[60:61], 0\n"
    "v_mfma_f32_16x16x32_bf16 v[40:43], v[16:19], a[8:11], v[40:43]\n"
    "v_mov_b64 v[62:63], 0\n"
    "v_mov_b64 v[64:65], 0\n"
    "v_mfma_f32_16x16x32_bf16 v[44:47], v[20:23], a[8:11], v[44:47]\n"
    "v_mov_b64 v[66:67], 0\n"
    "v_mov_b64 v[68:69], 0\n"
    "s_waitcnt lgkmcnt(0)\n"
    "s_barrier\n"
    ".Lvo_mk2x8_%=:\n"
    "ds_read_b64_tr_b16 v[72:73], v101 offset:0\n"
    "ds_read_b64_tr_b16 v[74:75], v101 offset:64\n"
    "v_mfma_f32_16x16x32_bf16 v[40:43], v[24:27], a[12:15], v[40:43]\n"
    "ds_read_b64_tr_b16 v[76:77], v101 offset:1024\n"
    "ds_read_b64_tr_b16 v[78:79], v101 offset:1088\n"
    "v_mfma_f32_16x16x32_bf16 v[44:47], v[28:31], a[12:15], v[44:47]\n"
    "ds_read_b64_tr_b16 v[80:81], v101 offset:2048\n"
    "ds_read_b64_tr_b16 v[82:83], v101 offset:2112\n"
    "v_mov_b64 v[70:71], 0\n"
    "v_mov_b64 v[84:85], 0\n"
    "v_mov_b64 v[86:87], 0\n"
    "v_mov_b64 v[88:89], 0\n"
    "v_mov_b64 v[90:91], 0\n"
    "s_waitcnt lgkmcnt(4)\n"
    "v_smfmac_f32_16x16x64_bf16 v[32:35], v[72:75], a[192:199], v98\n"
    "buffer_load_dwordx4 v96, s[0:3], s43 offen offset:0 sc1 lds\n"
    "s_waitcnt lgkmcnt(2)\n"
    "v_smfmac_f32_16x16x64_bf16 v[32:35], v[76:79], a[200:207], v98\n"
    "s_waitcnt lgkmcnt(0)\n"
    "v_smfmac_f32_16x16x64_bf16 v[32:35], v[80:83], a[208:215], v98\n"
    "v_smfmac_f32_16x16x64_bf16 v[36:39], v[72:75], a[216:223], v98\n"
    "buffer_load_dwordx4 v96, s[0:3], s43 offen offset:1024 sc1 lds\n"
    "v_smfmac_f32_16x16x64_bf16 v[36:39], v[76:79], a[224:231], v98\n"
    "s_nop 1\n"
    "v_add_f32 v32, v32, v34\n"
    "v_smfmac_f32_16x16x64_bf16 v[36:39], v[80:83], a[232:239], v98\n"
    "v_add_f32 v33, v33, v35\n"
    "v_smfmac_f32_16x16x64_bf16 v[48:51], v[72:75], a[0:7], v98\n"
    "v_cvt_pk_bf16_f32 v108, v32, v33\n"
    "v_smfmac_f32_16x16x64_bf16 v[48:51], v[76:79], a[8:15], v98\n"
    "s_nop 1\n"
    "v_add_f32 v36, v36, v38\n"
    "v_smfmac_f32_16x16x64_bf16 v[48:51], v[80:83], a[16:23], v98\n"
    "v_add_f32 v37, v37, v39\n"
    "v_smfmac_f32_16x16x64_bf16 v[52:55], v[72:75], a[24:31], v98\n"
    "v_cvt_pk_bf16_f32 v109, v36, v37\n"
    "v_smfmac_f32_16x16x64_bf16 v[52:55], v[76:79], a[32:39], v98\n"
    "s_nop 1\n"
    "v_add_f32 v48, v48, v50\n"
    "v_permlane16_swap_b32 v108, v109\n"
    "v_min_u32 v108, -2, v108\n"
    "v_smfmac_f32_16x16x64_bf16 v[52:55], v[80:83], a[40:47], v98\n"
    "buffer_store_dwordx2 v[108:109], v103, s[0:3], s36 offen sc1\n"
    "v_add_f32 v49, v49, v51\n"
    "v_smfmac_f32_16x16x64_bf16 v[56:59], v[72:75], a[48:55], v98\n"
    "v_cvt_pk_bf16_f32 v48, v48, v49\n"
    "v_smfmac_f32_16x16x64_bf16 v[56:59], v[76:79], a[56:63], v98\n"
    "s_nop 1\n"
    "v_add_f32 v52, v52, v54\n"
    "v_smfmac_f32_16x16x64_bf16 v[56:59], v[80:83], a[64:71], v98\n"
    "v_add_f32 v53, v53, v55\n"
    "v_smfmac_f32_16x16x64_bf16 v[60:63], v[72:75], a[72:79], v98\n"
    "v_cvt_pk_bf16_f32 v49, v52, v53\n"
    "v_smfmac_f32_16x16x64_bf16 v[60:63], v[76:79], a[80:87], v98\n"
    "s_waitcnt vmcnt(3)\n"
    "s_barrier\n"
    "v_add_f32 v56, v56, v58\n"
    "v_smfmac_f32_16x16x64_bf16 v[60:63], v[80:83], a[88:95], v98\n"
    "ds_read2_b64 v[32:35], v106 offset0:0 offset1:128\n"
    "v_add_f32 v57, v57, v59\n"
    "v_smfmac_f32_16x16x64_bf16 v[64:67], v[72:75], a[96:103], v98\n"
    "ds_read2_b64 v[36:39], v106 offset0:1 offset1:129\n"
    "v_cvt_pk_bf16_f32 v50, v56, v57\n"
    "v_smfmac_f32_16x16x64_bf16 v[64:67], v[76:79], a[104:111], v98\n"
    "s_nop 1\n"
    "v_add_f32 v60, v60, v62\n"
    "v_permlane32_swap_b32 v48, v50\n"
    "v_smfmac_f32_16x16x64_bf16 v[64:67], v[80:83], a[112:119], v98\n"
    "v_add_f32 v61, v61, v63\n"
    "v_smfmac_f32_16x16x64_bf16 v[68:71], v[72:75], a[120:127], v98\n"
    "v_cvt_pk_bf16_f32 v51, v60, v61\n"
    "v_smfmac_f32_16x16x64_bf16 v[68:71], v[76:79], a[128:135], v98\n"
    "s_nop 0\n"
    "v_permlane32_swap_b32 v49, v51\n"
    "v_smfmac_f32_16x16x64_bf16 v[68:71], v[80:83], a[136:143], v98\n"
    "s_nop 0\n"
    "v_permlane16_swap_b32 v50, v51\n"
    "v_permlane16_swap_b32 v48, v49\n"
    "v_min_u32 v48, -2, v48\n"
    "v_smfmac_f32_16x16x64_bf16 v[84:87], v[72:75], a[144:151], v98\n"
    "buffer_store_dwordx4 v[48:51], v102, s[0:3], s36 offen sc1\n"
    "v_add_f32 v64, v64, v66\n"
    "v_add_f32 v65, v65, v67\n"
    "v_cvt_pk_bf16_f32 v64, v64, v65\n"
    "v_smfmac_f32_16x16x64_bf16 v[84:87], v[76:79], a[152:159], v98\n"
    "v_add_f32 v68, v68, v70\n"
    "v_smfmac_f32_16x16x64_bf16 v[84:87], v[80:83], a[160:167], v98\n"
    "v_add_f32 v69, v69, v71\n"
    "v_smfmac_f32_16x16x64_bf16 v[88:91], v[72:75], a[168:175], v98\n"
    "v_cvt_pk_bf16_f32 v65, v68, v69\n"
    "v_smfmac_f32_16x16x64_bf16 v[88:91], v[76:79], a[176:183], v98\n"
    "s_nop 1\n"
    "v_add_f32 v84, v84, v86\n"
    "v_smfmac_f32_16x16x64_bf16 v[88:91], v[80:83], a[184:191], v98\n"
    ".Lvo_mk3x9_%=:\n"
    ".Lvo_uph_%=:\n"
    "s_waitcnt lgkmcnt(0)\n"
    "v_add_f32 v85, v85, v87\n"
    "v_mfma_f32_16x16x32_bf16 v[56:59], v[32:35], a[16:19], v[40:43]\n"
    "v_cvt_pk_bf16_f32 v66, v84, v85\n"
    "v_mfma_f32_16x16x32_bf16 v[60:63], v[36:39], a[16:19], v[44:47]\n"
    "s_nop 0\n"
    "v_permlane32_swap_b32 v64, v66\n"
    "v_add_f32 v88, v88, v90\n"
    "v_add_f32 v89, v89, v91\n"
    "v_cvt_pk_bf16_f32 v67, v88, v89\n"
    "s_nop 1\n"
    "v_permlane32_swap_b32 v65, v67\n"
    "s_nop 1\n"
    "v_permlane16_swap_b32 v66, v67\n"
    "v_permlane16_swap_b32 v64, v65\n"
    "v_min_u32 v64, -2, v64\n"
    "buffer_store_dwordx4 v[64:67], v102, s[0:3], s37 offen sc1\n"
    ".Lvo_mkum_%=:\n"
    "s_cmp_eq_u32 s34, 63\n"
    "s_cbranch_scc1 .Lvo_end_%=\n"
    "ds_read2_b64 v[40:43], v107 offset0:0 offset1:128\n"
    "ds_read2_b64 v[44:47], v107 offset0:1 offset1:129\n"
    "v_mfma_f32_16x16x32_bf16 v[48:51], v[0:3], a[24:27], 0\n"
    "v_mfma_f32_16x16x32_bf16 v[52:55], v[4:7], a[24:27], 0\n"
    ".Lvo_mkL0_%=:\n"
    "v_mfma_f32_16x16x32_bf16 v[48:51], v[8:11], a[28:31], v[48:51]\n"
    "v_mfma_f32_16x16x32_bf16 v[52:55], v[12:15], a[28:31], v[52:55]\n"
    "v_mfma_f32_16x16x32_bf16 v[48:51], v[16:19], a[32:35], v[48:51]\n"
    "v_mov_b64 v[88:89], -1\n"
    "v_mov_b64 v[90:91], -1\n"
    "v_mfma_f32_16x16x32_bf16 v[52:55], v[20:23], a[32:35], v[52:55]\n"
    "s_waitcnt lgkmcnt(0)\n"
    "v_mfma_f32_16x16x32_bf16 v[56:59], v[40:43], a[20:23], v[56:59]\n"
    "buffer_load_dwordx4 v96, s[0:3], s43 offen offset:2048 sc1 lds\n"
    "v_mfma_f32_16x16x32_bf16 v[60:63], v[44:47], a[20:23], v[60:63]\n"
    "v_mfma_f32_16x16x32_bf16 v[48:51], v[24:27], a[36:39], v[48:51]\n"
    "s_nop 1\n"
    "v_cvt_pk_bf16_f32 v72, v56, v57\n"
    "v_mfma_f32_16x16x32_bf16 v[52:55], v[28:31], a[36:39], v[52:55]\n"
    "v_cvt_pk_bf16_f32 v74, v58, v59\n"
    "v_cvt_pk_bf16_f32 v73, v60, v61\n"
    "v_mfma_f32_16x16x32_bf16 v[48:51], v[32:35], a[40:43], v[48:51]\n"
    "v_cvt_pk_bf16_f32 v75, v62, v63\n"
    "v_mfma_f32_16x16x32_bf16 v[52:55], v[36:39], a[40:43], v[52:55]\n"
    "buffer_load_dwordx4 v96, s[0:3], s43 offen offset:3072 sc1 lds\n"
    "v_mfma_f32_16x16x32_bf16 v[48:51], v[40:43], a[44:47], v[48:51]\n"
    "s_sub_u32 m0, s17, 4096\n"
    "s_add_u32 s43, s43, 4096\n"
    "v_mfma_f32_16x16x32_bf16 v[52:55], v[44:47], a[44:47], v[52:55]\n"
    "v_mfma_f32_16x16x32_bf16 v[56:59], v[0:3], a[48:51], 0\n"
    "s_nop 1\n"
    "v_cvt_pk_bf16_f32 v76, v48, v49\n"
    "v_mfma_f32_16x16x32_bf16 v[60:63], v[4:7], a[48:51], 0\n"
    "v_cvt_pk_bf16_f32 v78, v50, v51\n"
    "v_cvt_pk_bf16_f32 v77, v52, v53\n"
    "v_mfma_f32_16x16x32_bf16 v[56:59], v[8:11], a[52:55], v[56:59]\n"
    "v_cvt_pk_bf16_f32 v79, v54, v55\n"
    "v_mfma_f32_16x16x32_bf16 v[60:63], v[12:15], a[52:55], v[60:63]\n"
    "buffer_load_dwordx4 v96, s[0:3], s43 offen offset:0 sc1 lds\n"
    "v_mfma_f32_16x16x32_bf16 v[56:59], v[16:19], a[56:59], v[56:59]\n"
    "v_mfma_f32_16x16x32_bf16 v[60:63], v[20:23], a[56:59], v[60:63]\n"
    "v_mfma_f32_16x16x32_bf16 v[56:59], v[24:27], a[60:63], v[56:59]\n"
    "v_mfma_f32_16x16x32_bf16 v[60:63], v[28:31], a[60:63], v[60:63]\n"
    "buffer_load_dwordx4 v96, s[0:3], s43 offen offset:1024 sc1 lds\n"
    "v_mfma_f32_16x16x32_bf16 v[56:59], v[32:35], a[64:67], v[56:59]\n"
    "v_mfma_f32_16x16x32_bf16 v[60:63], v[36:39], a[64:67], v[60:63]\n"
    "v_mfma_f32_16x16x32_bf16 v[56:59], v[40:43], a[68:71], v[56:59]\n"
    "v_mfma_f32_16x16x32_bf16 v[60:63], v[44:47], a[68:71], v[60:63]\n"
    "buffer_load_dwordx4 v96, s[0:3], s43 offen offset:2048 sc1 lds\n"
    "v_mfma_f32_16x16x32_bf16 v[48:51], v[0:3], a[72:75], 0\n"
    "s_nop 1\n"
    "v_cvt_pk_bf16_f32 v80, v56, v57\n"
    "v_mfma_f32_16x16x32_bf16 v[52:55], v[4:7], a[72:75], 0\n"
    "v_cvt_pk_bf16_f32 v82, v58, v59\n"
    "v_cvt_pk_bf16_f32 v81, v60, v61\n"
    "v_mfma_f32_16x16x32_bf16 v[48:51], v[8:11], a[76:79], v[48:51]\n"
    "v_cvt_pk_bf16_f32 v83, v62, v63\n"
    "v_mfma_f32_16x16x32_bf16 v[52:55], v[12:15], a[76:79], v[52:55]\n"
    "buffer_store_dwordx4 v[72:75], v96, s[0:3], s40 offen sc1\n"
    "s_add_u32 s40, s40, 0x10000\n"
    "v_mfma_f32_16x16x32_bf16 v[48:51], v[16:19], a[80:83], v[48:51]\n"
    ".Lvo_mkL1_%=:\n"
    "v_mfma_f32_16x16x32_bf16 v[52:55], v[20:23], a[80:83], v[52:55]\n"
    "v_mfma_f32_16x16x32_bf16 v[48:51], v[24:27], a[84:87], v[48:51]\n"
    "v_mfma_f32_16x16x32_bf16 v[52:55], v[28:31], a[84:87], v[52:55]\n"
    "v_mfma_f32_16x16x32_bf16 v[48:51], v[32:35], a[88:91], v[48:51]\n"
    "v_mfma_f32_16x16x32_bf16 v[52:55], v[36:39], a[88:91], v[52:55]\n"
    "v_mfma_f32_16x16x32_bf16 v[48:51], v[40:43], a[92:95], v[48:51]\n"
    "buffer_load_dwordx4 v96, s[0:3], s43 offen offset:3072 sc1 lds\n"
    "v_mfma_f32_16x16x32_bf16 v[52:55], v[44:47], a[92:95], v[52:55]\n"
    "v_mfma_f32_16x16x32_bf16 v[56:59], v[0:3], a[96:99], 0\n"
    "s_nop 1\n"
    "v_cvt_pk_bf16_f32 v72, v48, v49\n"
    "v_mfma_f32_16x16x32_bf16 v[60:63], v[4:7], a[96:99], 0\n"
    "v_cvt_pk_bf16_f32 v74, v50, v51\n"
    "v_cvt_pk_bf16_f32 v73, v52, v53\n"
    "v_mfma_f32_16x16x32_bf16 v[56:59], v[8:11], a[100:103], v[56:59]\n"
    "buffer_store_dwordx4 v[76:79], v96, s[0:3], s40 offen sc1\n"
    "v_cvt_pk_bf16_f32 v75, v54, v55\n"
    "s_add_u32 s40, s40, 0x10000\n"
    "s_mov_b32 m0, s48\n"
    "v_mfma_f32_16x16x32_bf16 v[60:63], v[12:15], a[100:103], v[60:63]\n"
    "s_add_u32 s43, s43, s19\n"
    "v_mfma_f32_16x16x32_bf16 v[56:59], v[16:19], a[104:107], v[56:59]\n"
    "v_mfma_f32_16x16x32_bf16 v[60:63], v[20:23], a[104:107], v[60:63]\n"
    "v_mfma_f32_16x16x32_bf16 v[56:59], v[24:27], a[108:111], v[56:59]\n"
    "v_mfma_f32_16x16x32_bf16 v[60:63], v[28:31], a[108:111], v[60:63]\n"
    "v_mfma_f32_16x16x32_bf16 v[56:59], v[32:35], a[112:115], v[56:59]\n"
    "v_mfma_f32_16x16x32_bf16 v[60:63], v[36:39], a[112:115], v[60:63]\n"
    "buffer_load_dwordx4 v96, s[0:3], s43 offen offset:0 sc1 lds\n"
    "v_mfma_f32_16x16x32_bf16 v[56:59], v[40:43], a[116:119], v[56:59]\n"
    "v_mfma_f32_16x16x32_bf16 v[60:63], v[44:47], a[116:119], v[60:63]\n"
    "v_mfma_f32_16x16x32_bf16 v[48:51], v[0:3], a[120:123], 0\n"
    "s_nop 1\n"
    "v_cvt_pk_bf16_f32 v76, v56, v57\n"
    "v_mfma_f32_16x16x32_bf16 v[52:55], v[4:7], a[120:123], 0\n"
    "buffer_store_dwordx4 v[80:83], v96, s[0:3], s40 offen sc1\n"
    "v_cvt_pk_bf16_f32 v78, v58, v59\n"
    "v_cvt_pk_bf16_f32 v77, v60, v61\n"
    "s_add_u32 s40, s40, 0x10000\n"
    "v_mfma_f32_16x16x32_bf16 v[48:51], v[8:11], a[124:127], v[48:51]\n"
    "v_cvt_pk_bf16_f32 v79, v62, v63\n"
    "v_mfma_f32_16x16x32_bf16 v[52:55], v[12:15], a[124:127], v[52:55]\n"
    "v_mfma_f32_16x16x32_bf16 v[48:51], v[16:19], a[128:131], v[48:51]\n"
    "v_mfma_f32_16x16x32_bf16 v[52:55], v[20:23], a[128:131], v[52:55]\n"
    "v_mfma_f32_16x16x32_bf16 v[48:51], v[24:27], a[132:135], v[48:51]\n"
    "v_mfma_f32_16x16x32_bf16 v[52:55], v[28:31], a[132:135], v[52:55]\n"
    "v_mfma_f32_16x16x32_bf16 v[48:51], v[32:35], a[136:139], v[48:51]\n"
    "buffer_load_dwordx4 v96, s[0:3], s43 offen offset:1024 sc1 lds\n"
    "v_mfma_f32_16x16x32_bf16 v[52:55], v[36:39], a[136:139], v[52:55]\n"
    "v_mfma_f32_16x16x32_bf16 v[48:51], v[40:43], a[140:143], v[48:51]\n"
    ".Lvo_mkL2_%=:\n"
    "v_mfma_f32_16x16x32_bf16 v[52:55], v[44:47], a[140:143], v[52:55]\n"
    "v_mfma_f32_16x16x32_bf16 v[56:59], v[0:3], a[144:147], 0\n"
    "buffer_store_dwordx4 v[72:75], v96, s[0:3], s40 offen sc1\n"
    "s_nop 0\n"
    "v_cvt_pk_bf16_f32 v80, v48, v49\n"
    "s_add_u32 s40, s40, 0x10000\n"
    "v_mfma_f32_16x16x32_bf16 v[60:63], v[4:7], a[144:147], 0\n"
    "v_cvt_pk_bf16_f32 v82, v50, v51\n"
    "v_cvt_pk_bf16_f32 v81, v52, v53\n"
    "v_mfma_f32_16x16x32_bf16 v[56:59], v[8:11], a[148:151], v[56:59]\n"
    "v_cvt_pk_bf16_f32 v83, v54, v55\n"
    "v_mfma_f32_16x16x32_bf16 v[60:63], v[12:15], a[148:151], v[60:63]\n"
    "v_mfma_f32_16x16x32_bf16 v[56:59], v[16:19], a[152:155], v[56:59]\n"
    "v_mfma_f32_16x16x32_bf16 v[60:63], v[20:23], a[152:155], v[60:63]\n"
    "v_mfma_f32_16x16x32_bf16 v[56:59], v[24:27], a[156:159], v[56:59]\n"
    "v_mfma_f32_16x16x32_bf16 v[60:63], v[28:31], a[156:159], v[60:63]\n"
    "buffer_load_dwordx2 v[108:109], off, s[0:3], s45 sc1\n"
    "v_mfma_f32_16x16x32_bf16 v[56:59], v[32:35], a[160:163], v[56:59]\n"
    "v_mfma_f32_16x16x32_bf16 v[60:63], v[36:39], a[160:163], v[60:63]\n"
    "v_mfma_f32_16x16x32_bf16 v[56:59], v[40:43], a[164:167], v[56:59]\n"
    "v_mfma_f32_16x16x32_bf16 v[60:63], v[44:47], a[164:167], v[60:63]\n"
    "buffer_store_dwordx4 v[76:79], v96, s[0:3], s40 offen sc1\n"
    "s_add_u32 s40, s40, 0x10000\n"
    "v_mfma_f32_16x16x32_bf16 v[48:51], v[0:3], a[168:171], 0\n"
    "s_nop 1\n"
    "v_cvt_pk_bf16_f32 v72, v56, v57\n"
    "v_mfma_f32_16x16x32_bf16 v[52:55], v[4:7], a[168:171], 0\n"
    "v_cvt_pk_bf16_f32 v74, v58, v59\n"
    "v_cvt_pk_bf16_f32 v73, v60, v61\n"
    "v_mfma_f32_16x16x32_bf16 v[48:51], v[8:11], a[172:175], v[48:51]\n"
    "v_cvt_pk_bf16_f32 v75, v62, v63\n"
    "v_mfma_f32_16x16x32_bf16 v[52:55], v[12:15], a[172:175], v[52:55]\n"
    "v_mfma_f32_16x16x32_bf16 v[48:51], v[16:19], a[176:179], v[48:51]\n"
    "v_mfma_f32_16x16x32_bf16 v[52:55], v[20:23], a[176:179], v[52:55]\n"
    "v_mfma_f32_16x16x32_bf16 v[48:51], v[24:27], a[180:183], v[48:51]\n"
    "buffer_store_dwordx4 v[88:91], v102, s[0:3], s38 offen sc1\n"
    "v_mfma_f32_16x16x32_bf16 v[52:55], v[28:31], a[180:183], v[52:55]\n"
    "v_mfma_f32_16x16x32_bf16 v[48:51], v[32:35], a[184:187], v[48:51]\n"
    "v_mfma_f32_16x16x32_bf16 v[52:55], v[36:39], a[184:187], v[52:55]\n"
    "v_mfma_f32_16x16x32_bf16 v[48:51], v[40:43], a[188:191], v[48:51]\n"
    "v_mfma_f32_16x16x32_bf16 v[52:55], v[44:47], a[188:191], v[52:55]\n"
    "v_mfma_f32_16x16x32_bf16 v[56:59], v[0:3], a[192:195], 0\n"
    "s_nop 1\n"
    "v_cvt_pk_bf16_f32 v76, v48, v49\n"
    "v_mfma_f32_16x16x32_bf16 v[60:63], v[4:7], a[192:195], 0\n"
    "buffer_store_dwordx4 v[80:83], v96, s[0:3], s40 offen sc1\n"
    "v_cvt_pk_bf16_f32 v78, v50, v51\n"
    "v_cvt_pk_bf16_f32 v77, v52, v53\n"
    "s_add_u32 s40, s40, 0x10000\n"
    "v_mfma_f32_16x16x32_bf16 v[56:59], v[8:11], a[196:199], v[56:59]\n"
    "v_cvt_pk_bf16_f32 v79, v54, v55\n"
    "v_mfma_f32_16x16x32_bf16 v[60:63], v[12:15], a[196:199], v[60:63]\n"
    "v_mfma_f32_16x16x32_bf16 v[56:59], v[16:19], a[200:203], v[56:59]\n"
    ".Lvo_mkL3_%=:\n"
    "v_mfma_f32_16x16x32_bf16 v[60:63], v[20:23], a[200:203], v[60:63]\n"
    "v_mfma_f32_16x16x32_bf16 v[56:59], v[24:27], a[204:207], v[56:59]\n"
    "v_mfma_f32_16x16x32_bf16 v[60:63], v[28:31], a[204:207], v[60:63]\n"
    "v_mfma_f32_16x16x32_bf16 v[56:59], v[32:35], a[208:211], v[56:59]\n"
    "buffer_store_dwordx4 v[88:91], v102, s[0:3], s39 offen sc1\n"
    "v_mfma_f32_16x16x32_bf16 v[60:63], v[36:39], a[208:211], v[60:63]\n"
    "v_mfma_f32_16x16x32_bf16 v[56:59], v[40:43], a[212:215], v[56:59]\n"
    "v_mfma_f32_16x16x32_bf16 v[60:63], v[44:47], a[212:215], v[60:63]\n"
    "s_waitcnt vmcnt(15)\n"
    "s_barrier\n"
    "v_mfma_f32_16x16x32_bf16 v[48:51], v[24:27], a[228:231], 0\n"
    "s_nop 1\n"
    "v_cvt_pk_bf16_f32 v80, v56, v57\n"
    "v_mfma_f32_16x16x32_bf16 v[52:55], v[28:31], a[228:231], 0\n"
    "v_cvt_pk_bf16_f32 v82, v58, v59\n"
    "v_cvt_pk_bf16_f32 v81, v60, v61\n"
    "v_mfma_f32_16x16x32_bf16 v[48:51], v[32:35], a[232:235], v[48:51]\n"
    "ds_read_b128 v[24:27], v92 offset:0\n"
    "v_cvt_pk_bf16_f32 v83, v62, v63\n"
    "v_mfma_f32_16x16x32_bf16 v[52:55], v[36:39], a[232:235], v[52:55]\n"
    "buffer_store_dwordx4 v[72:75], v96, s[0:3], s40 offen sc1\n"
    "ds_read_b128 v[28:31], v92 offset:1024\n"
    "s_add_u32 s40, s40, 0x10000\n"
    "v_mfma_f32_16x16x32_bf16 v[48:51], v[40:43], a[236:239], v[48:51]\n"
    "ds_read_b128 v[32:35], v93 offset:0\n"
    "v_mfma_f32_16x16x32_bf16 v[52:55], v[44:47], a[236:239], v[52:55]\n"
    "ds_read_b128 v[36:39], v93 offset:1024\n"
    "v_mfma_f32_16x16x32_bf16 v[48:51], v[0:3], a[216:219], v[48:51]\n"
    "ds_read_b128 v[40:43], v94 offset:0\n"
    "v_mfma_f32_16x16x32_bf16 v[52:55], v[4:7], a[216:219], v[52:55]\n"
    "ds_read_b128 v[44:47], v94 offset:1024\n"
    "v_mfma_f32_16x16x32_bf16 v[48:51], v[8:11], a[220:223], v[48:51]\n"
    ".Lvo_mkL4_%=:\n"
    "v_mfma_f32_16x16x32_bf16 v[52:55], v[12:15], a[220:223], v[52:55]\n"
    "v_mfma_f32_16x16x32_bf16 v[48:51], v[16:19], a[224:227], v[48:51]\n"
    "buffer_store_dwordx2 v[88:89], v103, s[0:3], s38 offen sc1\n"
    "v_mfma_f32_16x16x32_bf16 v[52:55], v[20:23], a[224:227], v[52:55]\n"
    ".Lvo_mk4x10_%=:\n"
    ".Lvo_vph_%=:\n"
    "s_waitcnt lgkmcnt(4)\n"
    "v_mfma_f32_16x16x32_bf16 v[64:67], a[240:243], v[24:27], 0\n"
    "s_nop 1\n"
    "v_cvt_pk_bf16_f32 v72, v48, v49\n"
    "v_mfma_f32_16x16x32_bf16 v[84:87], v[176:179], v[24:27], 0\n"
    "s_add_u32 s34, s34, 1\n"
    "v_cvt_pk_bf16_f32 v74, v50, v51\n"
    "v_cvt_pk_bf16_f32 v73, v52, v53\n"
    "v_mfma_f32_16x16x32_bf16 v[68:71], a[240:243], v[28:31], 0\n"
    "s_add_u32 s36, s34, 0\n"
    "ds_read_b128 v[24:27], v95 offset:0\n"
    "v_cvt_pk_bf16_f32 v75, v54, v55\n"
    "v_mfma_f32_16x16x32_bf16 v[88:91], v[176:179], v[28:31], 0\n"
    "s_waitcnt lgkmcnt(3)\n"
    "ds_read_b128 v[28:31], v95 offset:1024\n"
    "s_and_b32 s36, s36, 3\n"
    "v_mfma_f32_16x16x32_bf16 v[64:67], v[112:115], v[32:35], v[64:67]\n"
    "s_lshl_b32 s36, s36, 22\n"
    "s_add_u32 s41, s15, s36\n"
    "s_add_u32 s42, s16, s36\n"
    "v_mfma_f32_16x16x32_bf16 v[84:87], v[192:195], v[32:35], v[84:87]\n"
    "s_add_u32 s37, s36, 0x10000\n"
    "s_add_u32 s50, s34, 3\n"
    "ds_read_b128 v[0:3], v97 offset:0\n"
    "v_mfma_f32_16x16x32_bf16 v[68:71], v[112:115], v[36:39], v[68:71]\n"
    "buffer_store_dwordx4 v[76:79], v96, s[0:3], s40 offen sc1\n"
    "ds_read_b128 v[32:35], v92 offset:2048\n"
    "s_add_u32 s40, s40, 0x10000\n"
    "s_cmp_gt_i32 s50, 64\n"
    "s_cselect_b32 s50, 0, s50\n"
    "v_mfma_f32_16x16x32_bf16 v[88:91], v[192:195], v[36:39], v[88:91]\n"
    "s_waitcnt lgkmcnt(4)\n"
    "ds_read_b128 v[36:39], v92 offset:3072\n"
    "s_add_u32 s51, s34, 4\n"
    "v_mfma_f32_16x16x32_bf16 v[64:67], v[128:131], v[40:43], v[64:67]\n"
    "s_cmp_gt_i32 s51, 64\n"
    "s_cselect_b32 s51, 0, s51\n"
    "s_mul_i32 s51, s51, s14\n"
    "v_mfma_f32_16x16x32_bf16 v[84:87], v[208:211], v[40:43], v[84:87]\n"
    "ds_read_b128 v[4:7], v97 offset:1024\n"
    "v_mov_b32 v111, s50\n"
    "v_mfma_f32_16x16x32_bf16 v[68:71], v[128:131], v[44:47], v[68:71]\n"
    "ds_read_b128 v[40:43], v93 offset:2048\n"
    "v_mfma_f32_16x16x32_bf16 v[88:91], v[208:211], v[44:47], v[88:91]\n"
    "s_waitcnt lgkmcnt(5)\n"
    "ds_read_b128 v[44:47], v93 offset:3072\n"
    "v_mfma_f32_16x16x32_bf16 v[64:67], v[144:147], v[24:27], v[64:67]\n"
    "s_mov_b64 exec, 0x1f0\n"
    "v_mov_b32 v111, s51\n"
    "s_mov_b64 exec, -1\n"
    "v_mfma_f32_16x16x32_bf16 v[84:87], v[224:227], v[24:27], v[84:87]\n"
    "buffer_store_dwordx4 v[80:83], v104, s[0:3], s38 offen sc1\n"
    "ds_read_b128 v[8:11], v97 offset:2048\n"
    "v_mfma_f32_16x16x32_bf16 v[68:71], v[144:147], v[28:31], v[68:71]\n"
    "ds_read_b128 v[24:27], v94 offset:2048\n"
    "v_mfma_f32_16x16x32_bf16 v[88:91], v[224:227], v[28:31], v[88:91]\n"
    "s_waitcnt lgkmcnt(5)\n"
    "ds_read_b128 v[28:31], v94 offset:3072\n"
    "v_mfma_f32_16x16x32_bf16 v[64:67], a[244:247], v[32:35], v[64:67]\n"
    "ds_read_b128 v[12:15], v97 offset:3072\n"
    "v_mfma_f32_16x16x32_bf16 v[84:87], v[180:183], v[32:35], v[84:87]\n"
    "ds_read_b128 v[16:19], v97 offset:4096\n"
    "v_mfma_f32_16x16x32_bf16 v[68:71], a[244:247], v[36:39], v[68:71]\n"
    "ds_read_b128 v[32:35], v95 offset:2048\n"
    "v_mfma_f32_16x16x32_bf16 v[88:91], v[180:183], v[36:39], v[88:91]\n"
    "s_waitcnt lgkmcnt(6)\n"
    "ds_read_b128 v[36:39], v95 offset:3072\n"
    "v_mfma_f32_16x16x32_bf16 v[64:67], v[116:119], v[40:43], v[64:67]\n"
    "buffer_store_dwordx4 v[72:75], v104, s[0:3], s39 offen sc1\n"
    "s_waitcnt vmcnt(11)\n"
    "ds_read_b128 v[20:23], v97 offset:5120\n"
    "v_mfma_f32_16x16x32_bf16 v[84:87], v[196:199], v[40:43], v[84:87]\n"
    "s_barrier\n"
    "ds_read_b128 v[48:51], v97 offset:8192\n"
    "v_mfma_f32_16x16x32_bf16 v[68:71], v[116:119], v[44:47], v[68:71]\n"
    "ds_read_b128 v[40:43], v92 offset:4096\n"
    "v_mfma_f32_16x16x32_bf16 v[88:91], v[196:199], v[44:47], v[88:91]\n"
    "s_waitcnt lgkmcnt(7)\n"
    "ds_read_b128 v[44:47], v92 offset:5120\n"
    "v_mfma_f32_16x16x32_bf16 v[64:67], v[132:135], v[24:27], v[64:67]\n"
    "ds_read_b128 v[52:55], v97 offset:9216\n"
    ".Lvo_mkL5_%=:\n"
    "v_mfma_f32_16x16x32_bf16 v[84:87], v[212:215], v[24:27], v[84:87]\n"
    "ds_read_b128 v[56:59], v97 offset:10240\n"
    "v_mfma_f32_16x16x32_bf16 v[68:71], v[132:135], v[28:31], v[68:71]\n"
    "ds_read_b128 v[24:27], v93 offset:4096\n"
    "v_mfma_f32_16x16x32_bf16 v[88:91], v[212:215], v[28:31], v[88:91]\n"
    "s_waitcnt lgkmcnt(7)\n"
    "ds_read_b128 v[28:31], v93 offset:5120\n"
    "s_waitcnt vmcnt(9)\n"
    "v_mfma_f32_16x16x32_bf16 v[64:67], v[148:151], v[32:35], v[64:67]\n"
    "s_memrealtime s[54:55]\n"
    "v_readfirstlane_b32 s32, v108\n"
    "v_readfirstlane_b32 s33, v109\n"
    "v_mfma_f32_16x16x32_bf16 v[84:87], v[228:231], v[32:35], v[84:87]\n"
    "ds_read_b128 v[60:63], v97 offset:11264\n"
    "v_mfma_f32_16x16x32_bf16 v[68:71], v[148:151], v[36:39], v[68:71]\n"
    "s_cmp_eq_u32 s33, s35\n"
    "s_cselect_b32 s46, s32, s46\n"
    "ds_read_b128 v[32:35], v94 offset:4096\n"
    "v_mfma_f32_16x16x32_bf16 v[88:91], v[228:231], v[36:39], v[88:91]\n"
    "s_waitcnt lgkmcnt(6)\n"
    "ds_read_b128 v[36:39], v94 offset:5120\n"
    "s_add_u32 s46, s46, 260\n"
    "v_mfma_f32_16x16x32_bf16 v[64:67], a[248:251], v[40:43], v[64:67]\n"
    "s_sub_u32 s47, s46, 59\n"
    "s_mov_b32 m0, s17\n"
    "v_mfma_f32_16x16x32_bf16 v[84:87], v[184:187], v[40:43], v[84:87]\n"
    ".Lvo_mkL6_%=:\n"
    "v_mfma_f32_16x16x32_bf16 v[68:71], a[248:251], v[44:47], v[68:71]\n"
    "ds_read_b128 v[40:43], v95 offset:4096\n"
    "s_sub_u32 s56, s47, s54\n"
    "v_mfma_f32_16x16x32_bf16 v[88:91], v[184:187], v[44:47], v[88:91]\n"
    "s_waitcnt lgkmcnt(4)\n"
    "ds_read_b128 v[44:47], v95 offset:5120\n"
    ".Lvo_mk6x11_%=:\n"
    "s_cmp_gt_i32 s56, 4\n"
    "s_cbranch_scc1 .Lvo_early_%=\n"
    "buffer_load_dwordx4 v96, s[0:3], s41 offen sc1 lds\n"
    "buffer_load_dwordx4 v96, s[0:3], s41 offen offset:1024 sc1 lds\n"
    ".Lvo_asked_%=:\n"
    ".Lvo_mk7x13_%=:\n"
    "v_mfma_f32_16x16x32_bf16 v[64:67], v[120:123], v[24:27], v[64:67]\n"
    "s_add_u32 s49, s34, 3\n"
    "s_lshl_b32 s43, s49, 19\n"
    "s_lshl_b32 s33, s49, 11\n"
    "v_mfma_f32_16x16x32_bf16 v[84:87], v[200:203], v[24:27], v[84:87]\n"
    "s_xor_b32 s33, s33, s13\n"
    "s_and_b32 s33, s33, 0x2000\n"
    "s_add_u32 s43, s43, s33\n"
    ".Lvo_mkM0_%=:\n"
    "v_mfma_f32_16x16x32_bf16 v[68:71], v[120:123], v[28:31], v[68:71]\n"
    "ds_read_b128 v[24:27], v92 offset:6144\n"
    "s_add_u32 s43, s43, s18\n"
    "v_mfma_f32_16x16x32_bf16 v[88:91], v[200:203], v[28:31], v[88:91]\n"
    "s_waitcnt lgkmcnt(3)\n"
    "ds_read_b128 v[28:31], v92 offset:7168\n"
    "v_mfma_f32_16x16x32_bf16 v[64:67], v[136:139], v[32:35], v[64:67]\n"
    "s_cmp_gt_i32 s49, 63\n"
    "s_cselect_b32 s43, 2130706432, s43\n"
    "s_add_u32 s49, s34, 2\n"
    "v_mfma_f32_16x16x32_bf16 v[84:87], v[216:219], v[32:35], v[84:87]\n"
    "s_lshl_b32 s44, s49, 20\n"
    "s_lshl_b32 s33, s49, 12\n"
    "s_xor_b32 s33, s33, s13\n"
    "v_mfma_f32_16x16x32_bf16 v[68:71], v[136:139], v[36:39], v[68:71]\n"
    "ds_read_b128 v[32:35], v93 offset:6144\n"
    "s_and_b32 s33, s33, 0x2000\n"
    "v_mfma_f32_16x16x32_bf16 v[88:91], v[216:219], v[36:39], v[88:91]\n"
    "s_waitcnt lgkmcnt(3)\n"
    "ds_read_b128 v[36:39], v93 offset:7168\n"
    "s_add_u32 s44, s44, s33\n"
    "v_mfma_f32_16x16x32_bf16 v[64:67], v[152:155], v[40:43], v[64:67]\n"
    "buffer_load_dwordx4 v96, s[0:3], s42 offen offset:2048 sc1 lds\n"
    "v_mfma_f32_16x16x32_bf16 v[84:87], v[232:235], v[40:43], v[84:87]\n"
    "s_add_u32 s44, s44, s20\n"
    "s_cmp_gt_i32 s49, 63\n"
    "s_cselect_b32 s44, 2130706432, s44\n"
    "v_mfma_f32_16x16x32_bf16 v[68:71], v[152:155], v[44:47], v[68:71]\n"
    "ds_read_b128 v[40:43], v94 offset:6144\n"
    "s_add_u32 s38, s34, 2\n"
    ".Lvo_mkM1_%=:\n"
    "v_mfma_f32_16x16x32_bf16 v[88:91], v[232:235], v[44:47], v[88:91]\n"
    "s_waitcnt lgkmcnt(3)\n"
    "ds_read_b128 v[44:47], v94 offset:7168\n"
    "s_and_b32 s38, s38, 3\n"
    "v_mfma_f32_16x16x32_bf16 v[64:67], a[252:255], v[24:27], v[64:67]\n"
    "s_lshl_b32 s38, s38, 22\n"
    "s_add_u32 s39, s38, 0x10000\n"
    "s_add_u32 s40, s22, s38\n"
    "v_mfma_f32_16x16x32_bf16 v[84:87], v[188:191], v[24:27], v[84:87]\n"
    "s_and_b32 s45, s34, 63\n"
    "s_lshl_b32 s45, s45, 7\n"
    "s_add_u32 s45, s23, s45\n"
    "v_mfma_f32_16x16x32_bf16 v[68:71], a[252:255], v[28:31], v[68:71]\n"
    "ds_read_b128 v[24:27], v95 offset:6144\n"
    "s_add_u32 s35, s34, 1\n"
    "v_mfma_f32_16x16x32_bf16 v[88:91], v[188:191], v[28:31], v[88:91]\n"
    "s_waitcnt lgkmcnt(3)\n"
    "ds_read_b128 v[28:31], v95 offset:7168\n"
    "v_mfma_f32_16x16x32_bf16 v[64:67], v[124:127], v[32:35], v[64:67]\n"
    "s_cmp_lt_i32 s34, 0\n"
    "s_cselect_b32 s35, -5, s35\n"
    "s_cselect_b32 s52, 0, -1\n"
    "v_mfma_f32_16x16x32_bf16 v[84:87], v[204:207], v[32:35], v[84:87]\n"
    "buffer_load_dword v110, v105, s[0:3], 0 offen sc1\n"
    "v_mfma_f32_16x16x32_bf16 v[68:71], v[124:127], v[36:39], v[68:71]\n"
    "ds_read_b128 v[32:35], v96 offset:53248\n"
    "v_mfma_f32_16x16x32_bf16 v[88:91], v[204:207], v[36:39], v[88:91]\n"
    "s_waitcnt lgkmcnt(3)\n"
    "ds_read_b128 v[36:39], v96 offset:54272\n"
    "v_mfma_f32_16x16x32_bf16 v[64:67], v[140:143], v[40:43], v[64:67]\n"
    ".Lvo_mkM2_%=:\n"
    "v_mfma_f32_16x16x32_bf16 v[84:87], v[220:223], v[40:43], v[84:87]\n"
    "v_mfma_f32_16x16x32_bf16 v[68:71], v[140:143], v[44:47], v[68:71]\n"
    "ds_read_b128 v[40:43], v96 offset:55296\n"
    "v_mfma_f32_16x16x32_bf16 v[88:91], v[220:223], v[44:47], v[88:91]\n"
    "s_waitcnt lgkmcnt(3)\n"
    "ds_read_b128 v[44:47], v96 offset:56320\n"
    "v_mfma_f32_16x16x32_bf16 v[64:67], v[156:159], v[24:27], v[64:67]\n"
    "v_mfma_f32_16x16x32_bf16 v[84:87], v[236:239], v[24:27], v[84:87]\n"
    "v_mfma_f32_16x16x32_bf16 v[68:71], v[156:159], v[28:31], v[68:71]\n"
    "ds_read_b128 v[24:27], v96 offset:57344\n"
    ".Lvo_mkM3_%=:\n"
    "v_mfma_f32_16x16x32_bf16 v[88:91], v[236:239], v[28:31], v[88:91]\n"
    "s_waitcnt lgkmcnt(3)\n"
    "ds_read_b128 v[28:31], v96 offset:58368\n"
    "v_mfma_f32_16x16x32_bf16 v[64:67], v[160:163], v[32:35], v[64:67]\n"
    "v_mfma_f32_16x16x32_bf16 v[84:87], v[240:243], v[32:35], v[84:87]\n"
    "ds_read_b128 v[32:35], v96 offset:59392\n"
    "v_mfma_f32_16x16x32_bf16 v[68:71], v[160:163], v[36:39], v[68:71]\n"
    "v_mfma_f32_16x16x32_bf16 v[88:91], v[240:243], v[36:39], v[88:91]\n"
    "s_waitcnt lgkmcnt(3)\n"
    "ds_read_b128 v[36:39], v96 offset:60416\n"
    "v_mfma_f32_16x16x32_bf16 v[64:67], v[164:167], v[40:43], v[64:67]\n"
    "s_waitcnt lgkmcnt(2)\n"
    "v_mfma_f32_16x16x32_bf16 v[64:67], v[168:171], v[24:27], v[64:67]\n"
    "s_waitcnt lgkmcnt(1)\n"
    "v_mfma_f32_16x16x32_bf16 v[64:67], v[172:175], v[32:35], v[64:67]\n"
    ".Lvo_mkM4_%=:\n"
    "v_mfma_f32_16x16x32_bf16 v[84:87], v[244:247], v[40:43], v[84:87]\n"
    "s_branch .Lvo_take_%=\n"
    ".Lvo_tk0_%=:\n"
    ".Lvo_po3_%=:\n"
    "buffer_load_dword v109, v108, s[0:3], 0 offen sc1\n"
    "s_waitcnt vmcnt(0)\n"
    "v_cmp_gt_i32 vcc, 32, v109\n"
    "s_nop 0\n"
    "s_cbranch_vccz .Lvo_pd4_%=\n"
    "s_add_u32 s28, s28, 1\n"
    "s_cmp_lt_u32 s28, 0x40000\n"
    "s_cbranch_scc1 .Lvo_sp5_%=\n"
    "v_mov_b32 v109, 90\n"
    "s_mov_b32 s32, 33728\n"
    "buffer_store_dword v109, off, s[0:3], s32 sc1\n"
    "s_waitcnt vmcnt(0)\n"
    "s_branch .Lvo_pd4_%=\n"
    ".Lvo_sp5_%=:\n"
    "s_sleep 8\n"
    "s_branch .Lvo_po3_%=\n"
    ".Lvo_pd4_%=:\n"
    "s_branch .Lvo_tk1_%=\n"
    ".Lvo_early_%=:\n"
    ".Lvo_rt13_%=:\n"
    "s_memrealtime s[30:31]\n"
    "s_waitcnt lgkmcnt(0)\n"
    "s_sub_u32 s32, s47, s30\n"
    "s_cmp_lt_u32 s32, 600\n"
    "s_cbranch_scc1 .Lvo_rt13_%=\n"
    "buffer_load_dwordx4 v96, s[0:3], s41 offen sc1 lds\n"
    "buffer_load_dwordx4 v96, s[0:3], s41 offen offset:1024 sc1 lds\n"
    "s_branch .Lvo_asked_%=\n"
    ".Lvo_retry_%=:\n"
    "s_nop 7\n"
    "s_nop 7\n"
    "s_cmp_lt_i32 s34, 0\n"
    "s_cbranch_scc0 .Lvo_again_%=\n"
    "v_cvt_pk_bf16_f32 v44, v68, v69\n"
    "v_cvt_pk_bf16_f32 v45, v70, v71\n"
    "v_cvt_pk_bf16_f32 v46, v88, v89\n"
    "v_cvt_pk_bf16_f32 v47, v90, v91\n"
    "ds_write_b128 v92, v[44:47] offset:12288\n"
    "s_waitcnt vmcnt(0) lgkmcnt(0)\n"
    "s_barrier\n"
    "v_cmp_gt_i32 vcc, v111, v110\n"
    "s_nop 0\n"
    "s_cbranch_vccz .Lvo_e2_%=\n"
    ".Lvo_po15_%=:\n"
    "buffer_load_dword v110, v105, s[0:3], 0 offen sc1\n"
    "s_waitcnt vmcnt(0)\n"
    "v_cmp_gt_i32 vcc, v111, v110\n"
    "s_nop 0\n"
    "s_cbranch_vccz .Lvo_pd16_%=\n"
    "s_add_u32 s28, s28, 1\n"
    "s_cmp_lt_u32 s28, 0x40000\n"
    "s_cbranch_scc1 .Lvo_sp17_%=\n"
    "v_mov_b32 v110, 32\n"
    "s_mov_b32 s32, 33728\n"
    "buffer_store_dword v110, off, s[0:3], s32 sc1\n"
    "s_waitcnt vmcnt(0)\n"
    "s_branch .Lvo_pd16_%=\n"
    ".Lvo_sp17_%=:\n"
    "s_sleep 4\n"
    "s_branch .Lvo_po15_%=\n"
    ".Lvo_pd16_%=:\n"
    ".Lvo_e2_%=:\n"
    "s_mov_b32 m0, s21\n"
    "s_nop 0\n"
    "buffer_load_dwordx4 v96, s[0:3], s44 offen sc1 lds\n"
    "s_sub_u32 m0, s17, 8192\n"
    "s_nop 0\n"
    "buffer_load_dwordx4 v96, s[0:3], s43 offen offset:0 sc1 lds\n"
    "buffer_load_dwordx4 v96, s[0:3], s43 offen offset:1024 sc1 lds\n"
    "s_add_u32 s49, s34, 3\n"
    "v_lshlrev_b32 v108, 4, v96\n"
    "v_mov_b32 v109, s49\n"
    "s_mov_b64 exec, 15\n"
    "buffer_store_dword v109, v108, s[0:3], s25 offen sc1\n"
    "s_mov_b64 exec, -1\n"
    "ds_read_b128 v[0:3], v96 offset:11264\n"
    "ds_read_b128 v[4:7], v96 offset:12288\n"
    "ds_read_b128 v[8:11], v96 offset:24576\n"
    "ds_read_b128 v[12:15], v96 offset:25600\n"
    "ds_read_b128 v[16:19], v96 offset:37888\n"
    "ds_read_b128 v[20:23], v96 offset:38912\n"
    "ds_read_b128 v[24:27], v96 offset:51200\n"
    "ds_read_b128 v[28:31], v96 offset:52224\n"
    "s_waitcnt vmcnt(3) lgkmcnt(0)\n"
    "s_barrier\n"
    "ds_read2_b64 v[32:35], v106 offset0:0 offset1:128\n"
    "ds_read2_b64 v[36:39], v106 offset0:1 offset1:129\n"
    "v_mfma_f32_16x16x32_bf16 v[40:43], v[0:3], a[0:3], 0\n"
    "v_mfma_f32_16x16x32_bf16 v[44:47], v[4:7], a[0:3], 0\n"
    "v_mfma_f32_16x16x32_bf16 v[40:43], v[8:11], a[4:7], v[40:43]\n"
    "v_mfma_f32_16x16x32_bf16 v[44:47], v[12:15], a[4:7], v[44:47]\n"
    "v_mfma_f32_16x16x32_bf16 v[40:43], v[16:19], a[8:11], v[40:43]\n"
    "v_mfma_f32_16x16x32_bf16 v[44:47], v[20:23], a[8:11], v[44:47]\n"
    "v_mfma_f32_16x16x32_bf16 v[40:43], v[24:27], a[12:15], v[40:43]\n"
    "v_mfma_f32_16x16x32_bf16 v[44:47], v[28:31], a[12:15], v[44:47]\n"
    "s_waitcnt lgkmcnt(0)\n"
    "s_mov_b32 s37, 2130706432\n"
    "s_mov_b32 s36, 2130706432\n"
    "s_nop 7\n"
    "s_branch .Lvo_uph_%=\n"
    ".Lvo_e0_%=:\n"
    ".Lvo_po18_%=:\n"
    "buffer_load_dword v109, v108, s[0:3], s24 offen sc1\n"
    "s_waitcnt vmcnt(0)\n"
    "v_cmp_gt_i32 vcc, 1, v109\n"
    "s_nop 0\n"
    "s_cbranch_vccz .Lvo_pd19_%=\n"
    "s_add_u32 s28, s28, 1\n"
    "s_cmp_lt_u32 s28, 0x40000\n"
    "s_cbranch_scc1 .Lvo_sp20_%=\n"
    "v_mov_b32 v109, 30\n"
    "s_mov_b32 s32, 33728\n"
    "buffer_store_dword v109, off, s[0:3], s32 sc1\n"
    "s_waitcnt vmcnt(0)\n"
    "s_branch .Lvo_pd19_%=\n"
    ".Lvo_sp20_%=:\n"
    "s_sleep 4\n"
    "s_branch .Lvo_po18_%=\n"
    ".Lvo_pd19_%=:\n"
    ".Lvo_po21_%=:\n"
    "buffer_load_dword v110, v105, s[0:3], 0 offen sc1\n"
    "s_waitcnt vmcnt(0)\n"
    "v_cmp_gt_i32 vcc, v111, v110\n"
    "s_nop 0\n"
    "s_cbranch_vccz .Lvo_pd22_%=\n"
    "s_add_u32 s28, s28, 1\n"
    "s_cmp_lt_u32 s28, 0x40000\n"
    "s_cbranch_scc1 .Lvo_sp23_%=\n"
    "v_mov_b32 v110, 31\n"
    "s_mov_b32 s32, 33728\n"
    "buffer_store_dword v110, off, s[0:3], s32 sc1\n"
    "s_waitcnt vmcnt(0)\n"
    "s_branch .Lvo_pd22_%=\n"
    ".Lvo_sp23_%=:\n"
    "s_sleep 4\n"
    "s_branch .Lvo_po21_%=\n"
    ".Lvo_pd22_%=:\n"
    "s_branch .Lvo_e1_%=\n"
    ".Lvo_again_%=:\n"
    "s_add_u32 s28, s28, 1\n"
    "s_cmp_lt_u32 s28, 0x40000\n"
    "s_cbranch_scc1 .Lvo_sp24_%=\n"
    "v_mov_b32 v108, 2\n"
    "s_mov_b32 s32, 33728\n"
    "buffer_store_dword v108, off, s[0:3], s32 sc1\n"
    "s_waitcnt vmcnt(0)\n"
    "s_branch .Lvo_go_%=\n"
    ".Lvo_sp24_%=:\n"
    "buffer_load_dwordx4 v96, s[0:3], s41 offen sc1 lds\n"
    "buffer_load_dwordx4 v96, s[0:3], s41 offen offset:1024 sc1 lds\n"
    "buffer_load_dwordx4 v96, s[0:3], s42 offen offset:2048 sc1 lds\n"
    ".Lvo_ag1_%=:\n"
    "s_waitcnt vmcnt(0)\n"
    "ds_read_b128 v[72:75], v92 offset:8192\n"
    "ds_read_b128 v[76:79], v92 offset:9216\n"
    "s_waitcnt lgkmcnt(0)\n"
    "v_mov_b64 v[64:65], 0\n"
    "v_mov_b64 v[66:67], 0\n"
    "v_max_u32 v108, v72, v74\n"
    "v_max3_u32 v108, v108, v76, v78\n"
    "v_cmp_le_u32 vcc, s52, v108\n"
    "s_nop 0\n"
    "s_cbranch_vccnz .Lvo_again_%=\n"
    "v_smfmac_f32_16x16x64_bf16 v[64:67], v[72:75], a[240:247], v98\n"
    "s_branch .Lvo_go_%=\n"
    ".Lvo_r2_%=:\n"
    "s_add_u32 s28, s28, 1\n"
    "s_cmp_lt_u32 s28, 0x40000\n"
    "s_cbranch_scc1 .Lvo_sp25_%=\n"
    "v_mov_b32 v108, 6\n"
    "s_mov_b32 s32, 33728\n"
    "buffer_store_dword v108, off, s[0:3], s32 sc1\n"
    "s_waitcnt vmcnt(0)\n"
    "s_branch .Lvo_go2_%=\n"
    ".Lvo_sp25_%=:\n"
    "buffer_load_dwordx4 v96, s[0:3], s42 offen offset:2048 sc1 lds\n"
    ".Lvo_r3_%=:\n"
    "s_waitcnt vmcnt(0)\n"
    "ds_read_b128 v[80:83], v92 offset:10240\n"
    "s_waitcnt lgkmcnt(0)\n"
    "v_max_u32 v108, v80, v82\n"
    "v_cmp_eq_u32 vcc, -1, v108\n"
    "s_nop 0\n"
    "s_cbranch_vccnz .Lvo_r2_%=\n"
    "s_branch .Lvo_go2_%=\n"
    ".Lvo_wf_%=:\n"
    ".Lvo_po26_%=:\n"
    "buffer_load_dword v110, v105, s[0:3], 0 offen sc1\n"
    "s_waitcnt vmcnt(0)\n"
    "v_cmp_gt_i32 vcc, v111, v110\n"
    "s_nop 0\n"
    "s_cbranch_vccz .Lvo_pd27_%=\n"
    "s_add_u32 s28, s28, 1\n"
    "s_cmp_lt_u32 s28, 0x40000\n"
    "s_cbranch_scc1 .Lvo_sp28_%=\n"
    "v_mov_b32 v110, 33\n"
    "s_mov_b32 s32, 33728\n"
    "buffer_store_dword v110, off, s[0:3], s32 sc1\n"
    "s_waitcnt vmcnt(0)\n"
    "s_branch .Lvo_pd27_%=\n"
    ".Lvo_sp28_%=:\n"
    "s_sleep 4\n"
    "s_branch .Lvo_po26_%=\n"
    ".Lvo_pd27_%=:\n"
    "s_branch .Lvo_wfb_%=\n"
    ".Lvo_end_%=:\n"
    "s_waitcnt vmcnt(0) lgkmcnt(0)\n"
    "s_endpgm\n"
    :: "{s[0:3]}"(s0_), "{s[4:7]}"(s4_), "{s[8:11]}"(s8_), "{s[12:15]}"(s12_), "{s[16:19]}"(s16_), "{s[20:23]}"(s20_), "{s[24:27]}"(s24_), "{v[0:3]}"(v0_), "{v[4:7]}"(v4_), "{v[8:11]}"(v8_), "{v[12:15]}"(v12_), "{v[16:19]}"(v16_), "{v[92:95]}"(v92_), "{v[96:99]}"(v96_), "{v[100:103]}"(v100_), "{v[104:107]}"(v104_) : "memory", "v255", "a255", "s95");
    __builtin_unreachable();
}
DEV void ct_role(const MegaArgs& A, const int L, const int u, char* smem) {
    ROLE_HEAD;
    const int r5 = l / 5, q5 = l - 5 * r5, c0 = q5 < 4 ? 16 * u + 4 * q5 : 512 + 4 * u, tk = 8 * w + r5;
    u32x2 m0;
    {
        const int sg = 2 * u + L, rt = w & 1, j0 = 10 * (w >> 1);
        float* ex = (float*)(smem + 1024);
        u32x4 xa[10];
        const unsigned xo = ((32 * sg + 16 * rt + l16) * D_ + 32 * j0 + 8 * lq) * 2;
#pragma unroll
        for (int j = 0; j < 10; ++j) asm volatile("global_load_dwordx4 %0, %1, %2 offset:%3" : "=&v"(xa[j]) : "v"(xo), "s"(A.x0), "n"(64 * j) : "memory");
        asm volatile("global_load_dwordx2 %0, %1, %2" : "=&v"(m0) : "v"(((r5 & 7) * D_ + c0) * 2), "s"(A.mem0) : "memory");
        asm volatile("s_waitcnt vmcnt(0)" ::: "memory");
#pragma unroll
        for (int j = 0; j < 10; ++j) asm volatile("" : "+v"(xa[j]));
        asm volatile("" : "+v"(m0));
        float s = 0.f, q = 0.f;
#pragma unroll
        for (int j = 0; j < 10; ++j)
#pragma unroll
            for (int e = 0; e < 4; ++e) s += lo16(xa[j][e]) + hi16(xa[j][e]);
        s += __shfl_xor(s, 16); s += __shfl_xor(s, 32);
        ex[threadIdx.x] = s;
        asm volatile("s_waitcnt lgkmcnt(0)\n\ts_barrier" ::: "memory");
        const float mean = (ex[64 * rt + l16] + ex[128 + 64 * rt + l16]) * (1.0f / D_);
#pragma unroll
        for (int j = 0; j < 10; ++j)
#pragma unroll
            for (int e = 0; e < 4; ++e) { const float d = lo16(xa[j][e]) - mean, d2 = hi16(xa[j][e]) - mean; q += d * d; q += d2 * d2; }
        q += __shfl_xor(q, 16); q += __shfl_xor(q, 32);
        ex[256 + threadIdx.x] = q;
        asm volatile("s_waitcnt lgkmcnt(0)\n\ts_barrier" ::: "memory");
        q = ex[256 + 64 * rt + l16] + ex[384 + 64 * rt + l16];
        const float mb = rb(mean), rd = __builtin_amdgcn_rcpf(rb(sqrtf(rb(rb(q * (1.0f / D_)) + 1e-5f))));
#pragma unroll
        for (int j = 0; j < 10; ++j) {
            u32x4 o;
#pragma unroll
            for (int e = 0; e < 4; ++e) { const unsigned d = pk2(lo16(xa[j][e]) - mb, hi16(xa[j][e]) - mb); o[sw2(e)] = pk2(lo16(d) * rd, hi16(d) * rd); }
            bst16(rr, rec(sg, 0) + ((j0 + j) >> 2) * 16384 + ((j0 + j) & 3) * 2048 + rt * 1024 + l * 16, o);
        }
    }
#define XM 5
    const unsigned mpos = l < 40 && q5 < XM ? xl(RG_MEM + w * 16384 + (c0 >> 6) * 1024 + (((c0 >> 4) & 3) * 16 + 4 * (r5 >> 1) + ((c0 >> 1) & 2) + (r5 & 1)) * 16 + (c0 & 8), L) : OOB;
    const unsigned xpos = l < 40 ? (c0 >> 7) * 16384 + ((c0 >> 5) & 3) * 2048 + (tk >> 4) * 1024 + (((c0 >> 3) & 3) * 16 + (tk & 15)) * 16 + (c0 & 4) * 2 : OOB;
    const unsigned rpos = (L || l >= 40) ? xpos + RC_X2 : tk * 1280 + c0 * 2;
    const u32x4 rs = L ? rx : u32x4{lo(A.x0), hi(A.x0), 2048u * 1280u, 0x20000u};
#pragma unroll
    for (int k = 0; k < 4; ++k) bst16(rr, rec(2 * u + (k >> 1), L) + RC_X1 + 65536 + w * 2048 + (k & 1) * 1024 + l * 16, ones);
    bst8(rr, mpos, u32x2{nosent(m0[0]), m0[1]}); bst8(rr, mpos + SLOT, ones2); bst8(rr, mpos + 2 * SLOT, ones2);
    wait_drawn(A.ar);
    DRAIN_BARRIER();
    bst4(rr, threadIdx.x < 16 ? xl(Z_CT + (l & 7) * 256 + (32 * L + u) * 4, l >> 3) : OOB, 1u);
    if (!((A.layers >> L) & 1)) return;
    const u32x4 s0_ = {RFL(rx[0]), RFL(rx[1]), RFL(rx[2]), RFL(rx[3])};
    const u32x4 s4_ = {RFL(rs[0]), RFL(rs[1]), RFL(rs[2]), RFL(rs[3])};
    const u32x4 s8_ = {RFL(L << 13), RFL(REC0), RFL(xl(RG_UT + (u * 4 + w) * 8192, L)), RFL(xl(RG_UTX + (u * 4 + w) * 2048, L))};
    const u32x4 s12_ = {RFL(xl(RG_UM + (u * 4 + w) * 2048, L)), RFL(xl(RG_UMX + (u * 4 + w) * 512, L)), RFL(xl(RG_PT + w * 16384, L)), RFL(xl(RG_PM + w * 8192, L))};
    const u32x4 s16_ = {RFL(xl(Z_X + 16384 + (u & 1) * 8192, L)), RFL(xl(Z_QV + (u & 3) * 256, L)), RFL(DBG0 + ((L * 128 + 64 + u) * 4 + w) * 2048), RFL(DBG0 + (2 << 20) + ((L * 32 + u) * 4 + w) * 8192)};
    const u32x4 v192_ = {(unsigned)(l * 16), (unsigned)(l * 16 + 4096), (unsigned)((l & 2) ? 0xeeeeu : 0x4444u), (unsigned)(0x4444u)};
    const u32x4 v196_ = {(unsigned)((l16 >> 2) * 256 + lq * 64 + (l & 3) * 16), (unsigned)(lq * 64 + (l & 3) * 16), (unsigned)((lq >> 1) * 2048 + 1024 + (lq & 1) * 256 + l16 * 16), (unsigned)(lq * 2048 + 1536 + l16 * 16)};
    const u32x4 v200_ = {(unsigned)(0 + w * 640 + lq * 160 + l16 * 4), (unsigned)(l16 < 4 ? 0 + w * 640 + lq * 160 + 64 + l16 * 4 : 12800), (unsigned)(0 + (l < 40 ? l : 0) * 16), (unsigned)(mpos)};
    const u32x4 v204_ = {(unsigned)(2560 + w * 2560 + lq * 320 + l16 * 4), (unsigned)(l16 < 4 ? 2560 + w * 2560 + lq * 320 + 64 + l16 * 4 : 12800), (unsigned)(3840 + w * 2560 + lq * 320 + l16 * 4), (unsigned)(l16 < 4 ? 3840 + w * 2560 + lq * 320 + 64 + l16 * 4 : 12800)};
    const u32x4 v208_ = {(unsigned)(2560 + w * 640 + (l < 40 ? l : 0) * 16), (unsigned)(xpos + RC_X1), (unsigned)(rpos), (unsigned)(l < 4 && w == 1 ? xl(Z_X1 + l * 4096 + u * 128, L) : OOB)};
    const u32x4 v212_ = {(unsigned)(l < 16 ? ((u >> 2) & 1) * 16384 + (u & 3) * 2048 + l * 128 : OOB), (unsigned)(0u), (unsigned)(0u), (unsigned)(0u)};
    asm volatile(
    "s_mov_b32 s20, 0\n"
    "s_mov_b64 s[22:23], 0\n"
    "s_mov_b32 s25, 0\n"
    "s_mov_b32 s28, -2\n"
    "v_lshrrev_b32 v4, 2, v192\n"
    "v_lshrrev_b32 v5, 9, v192\n"
    "v_add_u32 v5, 1, v5\n"
    ".Lct_po1_%=:\n"
    "buffer_load_dword v8, v4, s[0:3], s17 offen sc1\n"
    "s_waitcnt vmcnt(0)\n"
    "v_cmp_gt_i32 vcc, v5, v8\n"
    "s_nop 0\n"
    "s_cbranch_vccz .Lct_pd2_%=\n"
    "s_add_u32 s20, s20, 1\n"
    "s_cmp_lt_u32 s20, 0x40000\n"
    "s_cbranch_scc1 .Lct_sp3_%=\n"
    "v_mov_b32 v8, 40\n"
    "s_mov_b32 s24, 33728\n"
    "buffer_store_dword v8, off, s[0:3], s24 sc1\n"
    "s_waitcnt vmcnt(0)\n"
    "s_branch .Lct_pd2_%=\n"
    ".Lct_sp3_%=:\n"
    "s_sleep 16\n"
    "s_branch .Lct_po1_%=\n"
    ".Lct_pd2_%=:\n"
    "s_memrealtime s[22:23]\n"
    "s_waitcnt lgkmcnt(0)\n"
    "s_sub_u32 s26, s22, 780\n"
    "s_mov_b32 s28, -1\n"
    "s_add_u32 s28, s28, 1\n"
    "s_add_u32 s27, s28, 1\n"
    "s_add_u32 s29, s28, 0\n"
    "s_and_b32 s29, s29, 3\n"
    "s_lshl_b32 s29, s29, 22\n"
    "s_add_u32 s30, s28, 1\n"
    "s_and_b32 s30, s30, 3\n"
    "s_lshl_b32 s30, s30, 22\n"
    "s_add_u32 s31, s28, 3\n"
    "s_and_b32 s31, s31, 3\n"
    "s_lshl_b32 s31, s31, 22\n"
    "s_and_b32 s41, s28, 63\n"
    "s_lshl_b32 s41, s41, 7\n"
    "s_add_u32 s32, s12, s29\n"
    "s_add_u32 s33, s13, s29\n"
    "s_add_u32 s34, s15, s29\n"
    "s_add_u32 s35, s14, s29\n"
    "s_add_u32 s39, s10, s30\n"
    "s_add_u32 s40, s11, s30\n"
    "s_add_u32 s41, s16, s41\n"
    "s_add_u32 s36, s35, 4096\n"
    "s_add_u32 s37, s35, 16384\n"
    "s_add_u32 s38, s35, 20480\n"
    "s_add_u32 s43, s26, 304\n"
    "s_lshl_b32 s45, s28, 19\n"
    "s_lshl_b32 s25, s28, 11\n"
    "s_xor_b32 s25, s25, s8\n"
    "s_and_b32 s25, s25, 0x2000\n"
    "s_add_u32 s45, s45, s25\n"
    "s_add_u32 s45, s45, s9\n"
    "s_mul_i32 s46, s28, 40960\n"
    "s_cmp_eq_u32 s8, 0\n"
    "s_cselect_b32 s46, s46, s45\n"
    "s_cmp_eq_u32 s28, 63\n"
    "s_cselect_b32 s39, 2130706432, s39\n"
    "s_cselect_b32 s40, 2130706432, s40\n"
    "v_mov_b64 v[76:77], 0\n"
    "v_mov_b64 v[78:79], 0\n"
    "v_mov_b64 v[80:81], 0\n"
    "v_mov_b64 v[82:83], 0\n"
    "s_mov_b32 s39, s10\n"
    "s_mov_b32 s40, s11\n"
    "buffer_load_dwordx4 a[0:3], v192, s[0:3], s39 offen offset:0 sc1\n"
    "buffer_load_dwordx4 a[4:7], v192, s[0:3], s39 offen offset:1024 sc1\n"
    "buffer_load_dwordx4 a[8:11], v192, s[0:3], s39 offen offset:2048 sc1\n"
    "buffer_load_dwordx4 a[12:15], v192, s[0:3], s39 offen offset:3072 sc1\n"
    "buffer_load_dwordx4 a[16:19], v193, s[0:3], s39 offen offset:0 sc1\n"
    "buffer_load_dwordx4 a[20:23], v193, s[0:3], s39 offen offset:1024 sc1\n"
    "buffer_load_dwordx4 a[24:27], v193, s[0:3], s39 offen offset:2048 sc1\n"
    "buffer_load_dwordx4 a[28:31], v193, s[0:3], s39 offen offset:3072 sc1\n"
    "buffer_load_dwordx4 v[0:3], v196, s[0:3], s40 offen offset:0 sc1\n"
    "buffer_load_dwordx4 v[4:7], v196, s[0:3], s40 offen offset:1024 sc1\n"
    "s_add_u32 s39, s10, s30\n"
    "s_add_u32 s40, s11, s30\n"
    "buffer_load_dwordx2 v[190:191], off, s[0:3], s41 sc1\n"
    "s_waitcnt vmcnt(0)\n"
    ".Lct_top_%=:\n"
    "s_waitcnt vmcnt(2)\n"
    "v_mov_b64 v[8:9], v[0:1]\n"
    "v_mov_b64 v[10:11], v[2:3]\n"
    "v_mov_b64 v[12:13], v[4:5]\n"
    "v_mov_b64 v[14:15], v[6:7]\n"
    "v_mov_b32_dpp v16, v0 row_shl:4 row_mask:0xf bank_mask:0xf bound_ctrl:1\n"
    "v_mov_b32_dpp v17, v1 row_shl:4 row_mask:0xf bank_mask:0xf bound_ctrl:1\n"
    "v_mov_b32_dpp v18, v2 row_shl:4 row_mask:0xf bank_mask:0xf bound_ctrl:1\n"
    "v_mov_b32_dpp v19, v3 row_shl:4 row_mask:0xf bank_mask:0xf bound_ctrl:1\n"
    "v_mov_b32_dpp v20, v4 row_shl:4 row_mask:0xf bank_mask:0xf bound_ctrl:1\n"
    "v_mov_b32_dpp v21, v5 row_shl:4 row_mask:0xf bank_mask:0xf bound_ctrl:1\n"
    "v_mov_b32_dpp v22, v6 row_shl:4 row_mask:0xf bank_mask:0xf bound_ctrl:1\n"
    "v_mov_b32_dpp v23, v7 row_shl:4 row_mask:0xf bank_mask:0xf bound_ctrl:1\n"
    "v_mov_b32_dpp v24, v0 row_shl:8 row_mask:0xf bank_mask:0xf bound_ctrl:1\n"
    "v_mov_b32_dpp v25, v1 row_shl:8 row_mask:0xf bank_mask:0xf bound_ctrl:1\n"
    "v_mov_b32_dpp v26, v2 row_shl:8 row_mask:0xf bank_mask:0xf bound_ctrl:1\n"
    "v_mov_b32_dpp v27, v3 row_shl:8 row_mask:0xf bank_mask:0xf bound_ctrl:1\n"
    "v_mov_b32_dpp v28, v4 row_shl:8 row_mask:0xf bank_mask:0xf bound_ctrl:1\n"
    "v_mov_b32_dpp v29, v5 row_shl:8 row_mask:0xf bank_mask:0xf bound_ctrl:1\n"
    "v_mov_b32_dpp v30, v6 row_shl:8 row_mask:0xf bank_mask:0xf bound_ctrl:1\n"
    "v_mov_b32_dpp v31, v7 row_shl:8 row_mask:0xf bank_mask:0xf bound_ctrl:1\n"
    "v_mov_b32_dpp v32, v0 row_shl:12 row_mask:0xf bank_mask:0xf bound_ctrl:1\n"
    "v_mov_b32_dpp v33, v1 row_shl:12 row_mask:0xf bank_mask:0xf bound_ctrl:1\n"
    "v_mov_b32_dpp v34, v2 row_shl:12 row_mask:0xf bank_mask:0xf bound_ctrl:1\n"
    "v_mov_b32_dpp v35, v3 row_shl:12 row_mask:0xf bank_mask:0xf bound_ctrl:1\n"
    "v_mov_b32_dpp v36, v4 row_shl:12 row_mask:0xf bank_mask:0xf bound_ctrl:1\n"
    "v_mov_b32_dpp v37, v5 row_shl:12 row_mask:0xf bank_mask:0xf bound_ctrl:1\n"
    "v_mov_b32_dpp v38, v6 row_shl:12 row_mask:0xf bank_mask:0xf bound_ctrl:1\n"
    "v_mov_b32_dpp v39, v7 row_shl:12 row_mask:0xf bank_mask:0xf bound_ctrl:1\n"
    "s_waitcnt vmcnt(1)\n"
    ".Lct_x0_%=:\n"
    "v_readfirstlane_b32 s25, v191\n"
    "v_readfirstlane_b32 s24, v190\n"
    "s_cmp_eq_u32 s25, s27\n"
    "s_cselect_b32 s26, s24, s26\n"
    "s_cbranch_scc0 .Lct_xr0_%=\n"
    ".Lct_xg0_%=:\n"
    "s_add_u32 s43, s26, 93\n"
    "s_add_u32 s44, s26, 101\n"
    ".Lct_mk0x3_%=:\n"
    ".Lct_rt5_%=:\n"
    "s_memrealtime s[22:23]\n"
    "s_waitcnt lgkmcnt(0)\n"
    "s_sub_u32 s24, s43, s22\n"
    "s_cmp_lt_u32 s24, 600\n"
    "s_cbranch_scc1 .Lct_rt5_%=\n"
    "buffer_load_dwordx4 v[40:43], v192, s[0:3], s32 offen offset:0 sc1\n"
    "buffer_load_dwordx4 v[44:47], v192, s[0:3], s32 offen offset:1024 sc1\n"
    "buffer_load_dwordx4 v[48:51], v197, s[0:3], s33 offen offset:0 sc1\n"
    "buffer_load_dwordx4 v[52:55], v197, s[0:3], s33 offen offset:256 sc1\n"
    ".Lct_rt6_%=:\n"
    "s_memrealtime s[22:23]\n"
    "s_waitcnt lgkmcnt(0)\n"
    "s_sub_u32 s24, s44, s22\n"
    "s_cmp_lt_u32 s24, 600\n"
    "s_cbranch_scc1 .Lct_rt6_%=\n"
    "buffer_load_dwordx4 v[72:75], v193, s[0:3], s34 offen sc1\n"
    "buffer_load_dwordx4 v[56:59], v192, s[0:3], s34 offen offset:0 sc1\n"
    "buffer_load_dwordx4 v[60:63], v192, s[0:3], s34 offen offset:1024 sc1\n"
    "buffer_load_dwordx4 v[64:67], v192, s[0:3], s34 offen offset:2048 sc1\n"
    "buffer_load_dwordx4 v[68:71], v192, s[0:3], s34 offen offset:3072 sc1\n"
    "v_mov_b32 v148, s28\n"
    "v_mov_b32 v149, s28\n"
    "v_mov_b32 v150, s28\n"
    "v_mov_b32 v151, s28\n"
    "s_waitcnt vmcnt(9)\n"
    "s_barrier\n"
    "buffer_store_dwordx4 v[148:151], v211, s[0:3], 0 offen sc1\n"
    ".Lct_mk1x6_%=:\n"
    ".Lct_take0_%=:\n"
    "s_waitcnt vmcnt(6)\n"
    "v_max_u32 v184, v40, v44\n"
    "v_max3_u32 v184, v48, v50, v184\n"
    "v_max3_u32 v184, v52, v54, v184\n"
    "s_waitcnt vmcnt(5)\n"
    "v_smfmac_f32_16x16x64_bf16 v[76:79], v[72:75], v[40:47], v194\n"
    "v_max3_u32 v184, v72, v74, v184\n"
    "v_smfmac_f32_16x16x64_bf16 v[80:83], v[72:75], v[48:55], v194\n"
    "s_waitcnt vmcnt(4)\n"
    "v_smfmac_f32_16x16x64_bf16 v[80:83], v[56:59], v[8:15], v194\n"
    "v_max3_u32 v184, v56, v58, v184\n"
    "v_smfmac_f32_16x16x64_bf16 v[76:79], v[56:59], a[0:7], v194\n"
    "s_waitcnt vmcnt(3)\n"
    "v_smfmac_f32_16x16x64_bf16 v[80:83], v[60:63], v[16:23], v194\n"
    "v_max3_u32 v184, v60, v62, v184\n"
    "v_smfmac_f32_16x16x64_bf16 v[76:79], v[60:63], a[8:15], v194\n"
    "s_waitcnt vmcnt(2)\n"
    "v_smfmac_f32_16x16x64_bf16 v[80:83], v[64:67], v[24:31], v194\n"
    "v_max3_u32 v184, v64, v66, v184\n"
    "v_smfmac_f32_16x16x64_bf16 v[76:79], v[64:67], a[16:23], v194\n"
    "s_waitcnt vmcnt(1)\n"
    "v_smfmac_f32_16x16x64_bf16 v[80:83], v[68:71], v[32:39], v194\n"
    "v_max3_u32 v184, v68, v70, v184\n"
    "v_smfmac_f32_16x16x64_bf16 v[76:79], v[68:71], a[24:31], v194\n"
    ".Lct_mk2x7_%=:\n"
    "s_nop 1\n"
    "v_cmp_eq_u32 vcc, -1, v184\n"
    "s_nop 1\n"
    "v_add_f32 v80, v80, v82\n"
    "v_add_f32 v81, v81, v83\n"
    "s_nop 0\n"
    "v_pk_add_f32 v[76:77], v[76:77], v[78:79]\n"
    "ds_write2_b32 v201, v80, v81 offset1:20\n"
    "ds_write2_b32 v200, v76, v77 offset1:20\n"
    "s_cbranch_vccnz .Lct_retry0_%=\n"
    ".Lct_go0_%=:\n"
    "s_waitcnt lgkmcnt(0)\n"
    "s_barrier\n"
    "ds_read_b128 v[164:167], v202 offset:0\n"
    "ds_read_b128 v[168:171], v202 offset:640\n"
    "ds_read_b128 v[172:175], v202 offset:1280\n"
    "ds_read_b128 v[176:179], v202 offset:1920\n"
    "s_waitcnt lgkmcnt(2)\n"
    "v_pk_add_f32 v[164:165], v[164:165], v[168:169]\n"
    "v_pk_add_f32 v[166:167], v[166:167], v[170:171]\n"
    "s_waitcnt lgkmcnt(1)\n"
    "v_pk_add_f32 v[164:165], v[164:165], v[172:173]\n"
    "v_pk_add_f32 v[166:167], v[166:167], v[174:175]\n"
    "s_waitcnt lgkmcnt(0)\n"
    "v_pk_add_f32 v[164:165], v[164:165], v[176:177]\n"
    "v_pk_add_f32 v[166:167], v[166:167], v[178:179]\n"
    "v_cvt_pk_bf16_f32 v164, v164, v165\n"
    "v_cvt_pk_bf16_f32 v165, v166, v167\n"
    "v_min_u32 v164, -2, v164\n"
    "buffer_store_dwordx2 v[164:165], v203, s[0:3], s30 offen sc1\n"
    ".Lct_mk3x8_%=:\n"
    "s_sleep 2\n"
    "v_mov_b64 v[166:167], -1\n"
    "buffer_load_dwordx4 v[132:135], v199, s[0:3], s35 offen offset:0 sc1\n"
    "buffer_load_dwordx4 v[136:139], v199, s[0:3], s37 offen offset:0 sc1\n"
    "buffer_load_dwordx4 v[140:143], v199, s[0:3], s35 offen offset:256 sc1\n"
    "buffer_load_dwordx4 v[144:147], v199, s[0:3], s37 offen offset:256 sc1\n"
    "buffer_load_dwordx4 v[116:119], v198, s[0:3], s35 offen sc1\n"
    "buffer_load_dwordx4 v[84:87], v192, s[0:3], s35 offen offset:0 sc1\n"
    "buffer_load_dwordx4 v[88:91], v192, s[0:3], s35 offen offset:2048 sc1\n"
    "buffer_load_dwordx4 v[120:123], v198, s[0:3], s36 offen sc1\n"
    "buffer_load_dwordx4 v[92:95], v192, s[0:3], s36 offen offset:0 sc1\n"
    "buffer_load_dwordx4 v[96:99], v192, s[0:3], s36 offen offset:2048 sc1\n"
    ".Lct_mk7x9_%=:\n"
    "buffer_load_dwordx4 v[124:127], v198, s[0:3], s37 offen sc1\n"
    "s_waitcnt vmcnt(10)\n"
    "v_max3_u32 v184, v132, v134, v132\n"
    "v_swap_b32 v133, v134\n"
    "s_nop 1\n"
    "v_mfma_f32_16x16x32_bf16 v[148:151], v[132:135], v[40:43], 0\n"
    "v_mfma_f32_16x16x32_bf16 v[152:155], v[132:135], v[48:51], 0\n"
    "buffer_load_dwordx4 v[100:103], v192, s[0:3], s37 offen offset:0 sc1\n"
    "s_waitcnt vmcnt(10)\n"
    "v_max3_u32 v184, v136, v138, v184\n"
    "v_swap_b32 v137, v138\n"
    "s_nop 1\n"
    "v_mfma_f32_16x16x32_bf16 v[148:151], v[136:139], v[44:47], v[148:151]\n"
    "v_mfma_f32_16x16x32_bf16 v[152:155], v[136:139], v[52:55], v[152:155]\n"
    "buffer_load_dwordx4 v[104:107], v192, s[0:3], s37 offen offset:2048 sc1\n"
    "s_waitcnt vmcnt(10)\n"
    "v_max3_u32 v184, v140, v142, v184\n"
    "v_swap_b32 v141, v142\n"
    "s_nop 1\n"
    "v_mfma_f32_16x16x32_bf16 v[156:159], v[140:143], v[40:43], 0\n"
    "v_mfma_f32_16x16x32_bf16 v[160:163], v[140:143], v[48:51], 0\n"
    "buffer_load_dwordx4 v[128:131], v198, s[0:3], s38 offen sc1\n"
    "s_waitcnt vmcnt(10)\n"
    "v_max3_u32 v184, v144, v146, v184\n"
    "v_swap_b32 v145, v146\n"
    "s_nop 1\n"
    "v_mfma_f32_16x16x32_bf16 v[156:159], v[144:147], v[44:47], v[156:159]\n"
    "v_mfma_f32_16x16x32_bf16 v[160:163], v[144:147], v[52:55], v[160:163]\n"
    "buffer_load_dwordx4 v[108:111], v192, s[0:3], s38 offen offset:0 sc1\n"
    "s_waitcnt vmcnt(10)\n"
    "v_max3_u32 v184, v116, v118, v184\n"
    "v_smfmac_f32_16x16x64_bf16 v[148:151], v[116:119], a[0:7], v195\n"
    "v_smfmac_f32_16x16x64_bf16 v[152:155], v[116:119], v[8:15], v195\n"
    "buffer_load_dwordx4 v[112:115], v192, s[0:3], s38 offen offset:2048 sc1\n"
    "s_waitcnt vmcnt(10)\n"
    "v_max3_u32 v184, v84, v86, v184\n"
    "v_swap_b32 v85, v86\n"
    "s_nop 1\n"
    "v_mfma_f32_16x16x32_bf16 v[156:159], v[84:87], a[0:3], v[156:159]\n"
    "v_mfma_f32_16x16x32_bf16 v[160:163], v[84:87], v[8:11], v[160:163]\n"
    "buffer_load_dwordx2 v[186:187], v210, s[4:7], s46 offen sc1\n"
    "s_waitcnt vmcnt(10)\n"
    "v_max3_u32 v184, v88, v90, v184\n"
    "v_swap_b32 v89, v90\n"
    "s_nop 1\n"
    "v_mfma_f32_16x16x32_bf16 v[156:159], v[88:91], a[4:7], v[156:159]\n"
    "v_mfma_f32_16x16x32_bf16 v[160:163], v[88:91], v[12:15], v[160:163]\n"
    "buffer_store_dwordx2 v[166:167], v203, s[0:3], s31 offen sc1\n"
    "s_waitcnt vmcnt(10)\n"
    "v_max3_u32 v184, v120, v122, v184\n"
    "v_smfmac_f32_16x16x64_bf16 v[148:151], v[120:123], a[8:15], v195\n"
    "v_smfmac_f32_16x16x64_bf16 v[152:155], v[120:123], v[16:23], v195\n"
    "buffer_load_dwordx4 a[32:35], v192, s[0:3], s39 offen offset:0 sc1\n"
    "s_waitcnt vmcnt(10)\n"
    "v_max3_u32 v184, v92, v94, v184\n"
    "v_swap_b32 v93, v94\n"
    "s_nop 1\n"
    "v_mfma_f32_16x16x32_bf16 v[156:159], v[92:95], a[8:11], v[156:159]\n"
    "v_mfma_f32_16x16x32_bf16 v[160:163], v[92:95], v[16:19], v[160:163]\n"
    "buffer_load_dwordx4 a[36:39], v192, s[0:3], s39 offen offset:1024 sc1\n"
    "s_waitcnt vmcnt(10)\n"
    "v_max3_u32 v184, v96, v98, v184\n"
    "v_swap_b32 v97, v98\n"
    "s_nop 1\n"
    "v_mfma_f32_16x16x32_bf16 v[156:159], v[96:99], a[12:15], v[156:159]\n"
    "v_mfma_f32_16x16x32_bf16 v[160:163], v[96:99], v[20:23], v[160:163]\n"
    "buffer_load_dwordx4 a[40:43], v192, s[0:3], s39 offen offset:2048 sc1\n"
    "s_waitcnt vmcnt(10)\n"
    "v_max3_u32 v184, v124, v126, v184\n"
    "v_smfmac_f32_16x16x64_bf16 v[148:151], v[124:127], a[16:23], v195\n"
    "v_smfmac_f32_16x16x64_bf16 v[152:155], v[124:127], v[24:31], v195\n"
    "buffer_load_dwordx4 a[44:47], v192, s[0:3], s39 offen offset:3072 sc1\n"
    "s_waitcnt vmcnt(10)\n"
    "v_max3_u32 v184, v100, v102, v184\n"
    "v_swap_b32 v101, v102\n"
    "s_nop 1\n"
    "v_mfma_f32_16x16x32_bf16 v[156:159], v[100:103], a[16:19], v[156:159]\n"
    "v_mfma_f32_16x16x32_bf16 v[160:163], v[100:103], v[24:27], v[160:163]\n"
    "buffer_load_dwordx4 a[48:51], v193, s[0:3], s39 offen offset:0 sc1\n"
    "s_waitcnt vmcnt(10)\n"
    "v_max3_u32 v184, v104, v106, v184\n"
    "v_swap_b32 v105, v106\n"
    "s_nop 1\n"
    "v_mfma_f32_16x16x32_bf16 v[156:159], v[104:107], a[20:23], v[156:159]\n"
    "v_mfma_f32_16x16x32_bf16 v[160:163], v[104:107], v[28:31], v[160:163]\n"
    "buffer_load_dwordx4 a[52:55], v193, s[0:3], s39 offen offset:1024 sc1\n"
    "s_waitcnt vmcnt(10)\n"
    "v_max3_u32 v184, v128, v130, v184\n"
    "v_smfmac_f32_16x16x64_bf16 v[148:151], v[128:131], a[24:31], v195\n"
    "v_smfmac_f32_16x16x64_bf16 v[152:155], v[128:131], v[32:39], v195\n"
    "buffer_load_dwordx4 a[56:59], v193, s[0:3], s39 offen offset:2048 sc1\n"
    "s_waitcnt vmcnt(10)\n"
    "v_max3_u32 v184, v108, v110, v184\n"
    "v_swap_b32 v109, v110\n"
    "s_nop 1\n"
    "v_mfma_f32_16x16x32_bf16 v[156:159], v[108:111], a[24:27], v[156:159]\n"
    "v_mfma_f32_16x16x32_bf16 v[160:163], v[108:111], v[32:35], v[160:163]\n"
    "buffer_load_dwordx4 a[60:63], v193, s[0:3], s39 offen offset:3072 sc1\n"
    "s_waitcnt vmcnt(10)\n"
    "v_max3_u32 v184, v112, v114, v184\n"
    "v_swap_b32 v113, v114\n"
    "s_nop 1\n"
    "v_mfma_f32_16x16x32_bf16 v[156:159], v[112:115], a[28:31], v[156:159]\n"
    "v_mfma_f32_16x16x32_bf16 v[160:163], v[112:115], v[36:39], v[160:163]\n"
    "v_cmp_eq_u32 vcc, -1, v184\n"
    "s_nop 0\n"
    "s_cbranch_vccnz .Lct_tr0_%=\n"
    ".Lct_tg0_%=:\n"
    ".Lct_mk4x10_%=:\n"
    "ds_write2_b32 v204, v148, v149 offset0:0 offset1:20\n"
    "ds_write2_b32 v204, v150, v151 offset0:40 offset1:60\n"
    "ds_write2_b32 v205, v152, v153 offset0:0 offset1:20\n"
    "ds_write2_b32 v205, v154, v155 offset0:40 offset1:60\n"
    "ds_write2_b32 v206, v156, v157 offset0:0 offset1:20\n"
    "ds_write2_b32 v206, v158, v159 offset0:40 offset1:60\n"
    "ds_write2_b32 v207, v160, v161 offset0:0 offset1:20\n"
    "ds_write2_b32 v207, v162, v163 offset0:40 offset1:60\n"
    "s_mov_b32 s42, s45\n"
    "buffer_load_dwordx4 v[0:3], v196, s[0:3], s40 offen offset:0 sc1\n"
    "buffer_load_dwordx4 v[4:7], v196, s[0:3], s40 offen offset:1024 sc1\n"
    "s_add_u32 s28, s28, 1\n"
    "s_add_u32 s27, s28, 1\n"
    "s_add_u32 s29, s28, 0\n"
    "s_and_b32 s29, s29, 3\n"
    "s_lshl_b32 s29, s29, 22\n"
    "s_add_u32 s30, s28, 1\n"
    "s_and_b32 s30, s30, 3\n"
    "s_lshl_b32 s30, s30, 22\n"
    "s_add_u32 s31, s28, 3\n"
    "s_and_b32 s31, s31, 3\n"
    "s_lshl_b32 s31, s31, 22\n"
    "s_and_b32 s41, s28, 63\n"
    "s_lshl_b32 s41, s41, 7\n"
    "s_add_u32 s32, s12, s29\n"
    "s_add_u32 s33, s13, s29\n"
    "s_add_u32 s34, s15, s29\n"
    "s_add_u32 s35, s14, s29\n"
    "s_add_u32 s39, s10, s30\n"
    "s_add_u32 s40, s11, s30\n"
    "s_add_u32 s41, s16, s41\n"
    "s_add_u32 s36, s35, 4096\n"
    "s_add_u32 s37, s35, 16384\n"
    "s_add_u32 s38, s35, 20480\n"
    "s_add_u32 s43, s26, 304\n"
    "s_lshl_b32 s45, s28, 19\n"
    "s_lshl_b32 s25, s28, 11\n"
    "s_xor_b32 s25, s25, s8\n"
    "s_and_b32 s25, s25, 0x2000\n"
    "s_add_u32 s45, s45, s25\n"
    "s_add_u32 s45, s45, s9\n"
    "s_mul_i32 s46, s28, 40960\n"
    "s_cmp_eq_u32 s8, 0\n"
    "s_cselect_b32 s46, s46, s45\n"
    "s_cmp_eq_u32 s28, 63\n"
    "s_cselect_b32 s39, 2130706432, s39\n"
    "s_cselect_b32 s40, 2130706432, s40\n"
    "v_mov_b64 v[76:77], 0\n"
    "v_mov_b64 v[78:79], 0\n"
    "v_mov_b64 v[80:81], 0\n"
    "v_mov_b64 v[82:83], 0\n"
    "s_waitcnt vmcnt(11)\n"
    "v_lshlrev_b32 v148, 16, v186\n"
    "v_and_b32 v149, 0xffff0000, v186\n"
    "v_lshlrev_b32 v150, 16, v187\n"
    "v_and_b32 v151, 0xffff0000, v187\n"
    "s_memrealtime s[22:23]\n"
    "s_waitcnt lgkmcnt(0)\n"
    "s_barrier\n"
    "s_sub_u32 s24, s43, s22\n"
    "s_cmp_gt_i32 s24, 14\n"
    "s_cselect_b32 s24, 2130706432, s41\n"
    "buffer_load_dwordx2 v[190:191], off, s[0:3], s24 sc1\n"
    ".Lct_mk6x11_%=:\n"
    "ds_read_b128 v[164:167], v208 offset:0\n"
    "ds_read_b128 v[168:171], v208 offset:2560\n"
    "ds_read_b128 v[172:175], v208 offset:5120\n"
    "ds_read_b128 v[176:179], v208 offset:7680\n"
    "s_waitcnt lgkmcnt(2)\n"
    "v_pk_add_f32 v[164:165], v[164:165], v[168:169]\n"
    "v_pk_add_f32 v[166:167], v[166:167], v[170:171]\n"
    "s_waitcnt lgkmcnt(1)\n"
    "v_pk_add_f32 v[164:165], v[164:165], v[172:173]\n"
    "v_pk_add_f32 v[166:167], v[166:167], v[174:175]\n"
    "s_waitcnt lgkmcnt(0)\n"
    "v_pk_add_f32 v[164:165], v[164:165], v[176:177]\n"
    "v_pk_add_f32 v[166:167], v[166:167], v[178:179]\n"
    "v_cvt_pk_bf16_f32 v164, v164, v165\n"
    "v_cvt_pk_bf16_f32 v165, v166, v167\n"
    "v_lshlrev_b32 v168, 16, v164\n"
    "v_and_b32 v169, 0xffff0000, v164\n"
    "v_lshlrev_b32 v170, 16, v165\n"
    "v_and_b32 v171, 0xffff0000, v165\n"
    "v_pk_add_f32 v[168:169], v[168:169], v[148:149]\n"
    "v_pk_add_f32 v[170:171], v[170:171], v[150:151]\n"
    "v_cvt_pk_bf16_f32 v164, v168, v169\n"
    "v_cvt_pk_bf16_f32 v165, v170, v171\n"
    "buffer_store_dwordx2 v[164:165], v209, s[0:3], s42 offen sc1\n"
    ".Lct_mk5x12_%=:\n"
    "s_waitcnt vmcnt(2)\n"
    "v_mov_b64 v[8:9], v[0:1]\n"
    "v_mov_b64 v[10:11], v[2:3]\n"
    "v_mov_b64 v[12:13], v[4:5]\n"
    "v_mov_b64 v[14:15], v[6:7]\n"
    "v_mov_b32_dpp v16, v0 row_shl:4 row_mask:0xf bank_mask:0xf bound_ctrl:1\n"
    "v_mov_b32_dpp v17, v1 row_shl:4 row_mask:0xf bank_mask:0xf bound_ctrl:1\n"
    "v_mov_b32_dpp v18, v2 row_shl:4 row_mask:0xf bank_mask:0xf bound_ctrl:1\n"
    "v_mov_b32_dpp v19, v3 row_shl:4 row_mask:0xf bank_mask:0xf bound_ctrl:1\n"
    "v_mov_b32_dpp v20, v4 row_shl:4 row_mask:0xf bank_mask:0xf bound_ctrl:1\n"
    "v_mov_b32_dpp v21, v5 row_shl:4 row_mask:0xf bank_mask:0xf bound_ctrl:1\n"
    "v_mov_b32_dpp v22, v6 row_shl:4 row_mask:0xf bank_mask:0xf bound_ctrl:1\n"
    "v_mov_b32_dpp v23, v7 row_shl:4 row_mask:0xf bank_mask:0xf bound_ctrl:1\n"
    "v_mov_b32_dpp v24, v0 row_shl:8 row_mask:0xf bank_mask:0xf bound_ctrl:1\n"
    "v_mov_b32_dpp v25, v1 row_shl:8 row_mask:0xf bank_mask:0xf bound_ctrl:1\n"
    "v_mov_b32_dpp v26, v2 row_shl:8 row_mask:0xf bank_mask:0xf bound_ctrl:1\n"
    "v_mov_b32_dpp v27, v3 row_shl:8 row_mask:0xf bank_mask:0xf bound_ctrl:1\n"
    "v_mov_b32_dpp v28, v4 row_shl:8 row_mask:0xf bank_mask:0xf bound_ctrl:1\n"
    "v_mov_b32_dpp v29, v5 row_shl:8 row_mask:0xf bank_mask:0xf bound_ctrl:1\n"
    "v_mov_b32_dpp v30, v6 row_shl:8 row_mask:0xf bank_mask:0xf bound_ctrl:1\n"
    "v_mov_b32_dpp v31, v7 row_shl:8 row_mask:0xf bank_mask:0xf bound_ctrl:1\n"
    "v_mov_b32_dpp v32, v0 row_shl:12 row_mask:0xf bank_mask:0xf bound_ctrl:1\n"
    "v_mov_b32_dpp v33, v1 row_shl:12 row_mask:0xf bank_mask:0xf bound_ctrl:1\n"
    "v_mov_b32_dpp v34, v2 row_shl:12 row_mask:0xf bank_mask:0xf bound_ctrl:1\n"
    "v_mov_b32_dpp v35, v3 row_shl:12 row_mask:0xf bank_mask:0xf bound_ctrl:1\n"
    "v_mov_b32_dpp v36, v4 row_shl:12 row_mask:0xf bank_mask:0xf bound_ctrl:1\n"
    "v_mov_b32_dpp v37, v5 row_shl:12 row_mask:0xf bank_mask:0xf bound_ctrl:1\n"
    "v_mov_b32_dpp v38, v6 row_shl:12 row_mask:0xf bank_mask:0xf bound_ctrl:1\n"
    "v_mov_b32_dpp v39, v7 row_shl:12 row_mask:0xf bank_mask:0xf bound_ctrl:1\n"
    "s_waitcnt vmcnt(1)\n"
    ".Lct_x1_%=:\n"
    "v_readfirstlane_b32 s25, v191\n"
    "v_readfirstlane_b32 s24, v190\n"
    "s_cmp_eq_u32 s25, s27\n"
    "s_cselect_b32 s26, s24, s26\n"
    "s_cbranch_scc0 .Lct_xr1_%=\n"
    ".Lct_xg1_%=:\n"
    "s_add_u32 s43, s26, 93\n"
    "s_add_u32 s44, s26, 101\n"
    ".Lct_mk0x17_%=:\n"
    ".Lct_rt19_%=:\n"
    "s_memrealtime s[22:23]\n"
    "s_waitcnt lgkmcnt(0)\n"
    "s_sub_u32 s24, s43, s22\n"
    "s_cmp_lt_u32 s24, 600\n"
    "s_cbranch_scc1 .Lct_rt19_%=\n"
    "buffer_load_dwordx4 v[40:43], v192, s[0:3], s32 offen offset:0 sc1\n"
    "buffer_load_dwordx4 v[44:47], v192, s[0:3], s32 offen offset:1024 sc1\n"
    "buffer_load_dwordx4 v[48:51], v197, s[0:3], s33 offen offset:0 sc1\n"
    "buffer_load_dwordx4 v[52:55], v197, s[0:3], s33 offen offset:256 sc1\n"
    ".Lct_rt20_%=:\n"
    "s_memrealtime s[22:23]\n"
    "s_waitcnt lgkmcnt(0)\n"
    "s_sub_u32 s24, s44, s22\n"
    "s_cmp_lt_u32 s24, 600\n"
    "s_cbranch_scc1 .Lct_rt20_%=\n"
    "buffer_load_dwordx4 v[72:75], v193, s[0:3], s34 offen sc1\n"
    "buffer_load_dwordx4 v[56:59], v192, s[0:3], s34 offen offset:0 sc1\n"
    "buffer_load_dwordx4 v[60:63], v192, s[0:3], s34 offen offset:1024 sc1\n"
    "buffer_load_dwordx4 v[64:67], v192, s[0:3], s34 offen offset:2048 sc1\n"
    "buffer_load_dwordx4 v[68:71], v192, s[0:3], s34 offen offset:3072 sc1\n"
    "v_mov_b32 v148, s28\n"
    "v_mov_b32 v149, s28\n"
    "v_mov_b32 v150, s28\n"
    "v_mov_b32 v151, s28\n"
    "s_waitcnt vmcnt(9)\n"
    "s_barrier\n"
    "buffer_store_dwordx4 v[148:151], v211, s[0:3], 0 offen sc1\n"
    ".Lct_mk1x20_%=:\n"
    ".Lct_take1_%=:\n"
    "s_waitcnt vmcnt(6)\n"
    "v_max_u32 v184, v40, v44\n"
    "v_max3_u32 v184, v48, v50, v184\n"
    "v_max3_u32 v184, v52, v54, v184\n"
    "s_waitcnt vmcnt(5)\n"
    "v_smfmac_f32_16x16x64_bf16 v[76:79], v[72:75], v[40:47], v194\n"
    "v_max3_u32 v184, v72, v74, v184\n"
    "v_smfmac_f32_16x16x64_bf16 v[80:83], v[72:75], v[48:55], v194\n"
    "s_waitcnt vmcnt(4)\n"
    "v_smfmac_f32_16x16x64_bf16 v[80:83], v[56:59], v[8:15], v194\n"
    "v_max3_u32 v184, v56, v58, v184\n"
    "v_smfmac_f32_16x16x64_bf16 v[76:79], v[56:59], a[32:39], v194\n"
    "s_waitcnt vmcnt(3)\n"
    "v_smfmac_f32_16x16x64_bf16 v[80:83], v[60:63], v[16:23], v194\n"
    "v_max3_u32 v184, v60, v62, v184\n"
    "v_smfmac_f32_16x16x64_bf16 v[76:79], v[60:63], a[40:47], v194\n"
    "s_waitcnt vmcnt(2)\n"
    "v_smfmac_f32_16x16x64_bf16 v[80:83], v[64:67], v[24:31], v194\n"
    "v_max3_u32 v184, v64, v66, v184\n"
    "v_smfmac_f32_16x16x64_bf16 v[76:79], v[64:67], a[48:55], v194\n"
    "s_waitcnt vmcnt(1)\n"
    "v_smfmac_f32_16x16x64_bf16 v[80:83], v[68:71], v[32:39], v194\n"
    "v_max3_u32 v184, v68, v70, v184\n"
    "v_smfmac_f32_16x16x64_bf16 v[76:79], v[68:71], a[56:63], v194\n"
    ".Lct_mk2x21_%=:\n"
    "s_nop 1\n"
    "v_cmp_eq_u32 vcc, -1, v184\n"
    "s_nop 1\n"
    "v_add_f32 v80, v80, v82\n"
    "v_add_f32 v81, v81, v83\n"
    "s_nop 0\n"
    "v_pk_add_f32 v[76:77], v[76:77], v[78:79]\n"
    "ds_write2_b32 v201, v80, v81 offset1:20\n"
    "ds_write2_b32 v200, v76, v77 offset1:20\n"
    "s_cbranch_vccnz .Lct_retry1_%=\n"
    ".Lct_go1_%=:\n"
    "s_waitcnt lgkmcnt(0)\n"
    "s_barrier\n"
    "ds_read_b128 v[164:167], v202 offset:0\n"
    "ds_read_b128 v[168:171], v202 offset:640\n"
    "ds_read_b128 v[172:175], v202 offset:1280\n"
    "ds_read_b128 v[176:179], v202 offset:1920\n"
    "s_waitcnt lgkmcnt(2)\n"
    "v_pk_add_f32 v[164:165], v[164:165], v[168:169]\n"
    "v_pk_add_f32 v[166:167], v[166:167], v[170:171]\n"
    "s_waitcnt lgkmcnt(1)\n"
    "v_pk_add_f32 v[164:165], v[164:165], v[172:173]\n"
    "v_pk_add_f32 v[166:167], v[166:167], v[174:175]\n"
    "s_waitcnt lgkmcnt(0)\n"
    "v_pk_add_f32 v[164:165], v[164:165], v[176:177]\n"
    "v_pk_add_f32 v[166:167], v[166:167], v[178:179]\n"
    "v_cvt_pk_bf16_f32 v164, v164, v165\n"
    "v_cvt_pk_bf16_f32 v165, v166, v167\n"
    "v_min_u32 v164, -2, v164\n"
    "buffer_store_dwordx2 v[164:165], v203, s[0:3], s30 offen sc1\n"
    ".Lct_mk3x22_%=:\n"
    "s_sleep 2\n"
    "v_mov_b64 v[166:167], -1\n"
    "buffer_load_dwordx4 v[132:135], v199, s[0:3], s35 offen offset:0 sc1\n"
    "buffer_load_dwordx4 v[136:139], v199, s[0:3], s37 offen offset:0 sc1\n"
    "buffer_load_dwordx4 v[140:143], v199, s[0:3], s35 offen offset:256 sc1\n"
    "buffer_load_dwordx4 v[144:147], v199, s[0:3], s37 offen offset:256 sc1\n"
    "buffer_load_dwordx4 v[116:119], v198, s[0:3], s35 offen sc1\n"
    "buffer_load_dwordx4 v[84:87], v192, s[0:3], s35 offen offset:0 sc1\n"
    "buffer_load_dwordx4 v[88:91], v192, s[0:3], s35 offen offset:2048 sc1\n"
    "buffer_load_dwordx4 v[120:123], v198, s[0:3], s36 offen sc1\n"
    "buffer_load_dwordx4 v[92:95], v192, s[0:3], s36 offen offset:0 sc1\n"
    "buffer_load_dwordx4 v[96:99], v192, s[0:3], s36 offen offset:2048 sc1\n"
    ".Lct_mk7x23_%=:\n"
    "buffer_load_dwordx4 v[124:127], v198, s[0:3], s37 offen sc1\n"
    "s_waitcnt vmcnt(10)\n"
    "v_max3_u32 v184, v132, v134, v132\n"
    "v_swap_b32 v133, v134\n"
    "s_nop 1\n"
    "v_mfma_f32_16x16x32_bf16 v[148:151], v[132:135], v[40:43], 0\n"
    "v_mfma_f32_16x16x32_bf16 v[152:155], v[132:135], v[48:51], 0\n"
    "buffer_load_dwordx4 v[100:103], v192, s[0:3], s37 offen offset:0 sc1\n"
    "s_waitcnt vmcnt(10)\n"
    "v_max3_u32 v184, v136, v138, v184\n"
    "v_swap_b32 v137, v138\n"
    "s_nop 1\n"
    "v_mfma_f32_16x16x32_bf16 v[148:151], v[136:139], v[44:47], v[148:151]\n"
    "v_mfma_f32_16x16x32_bf16 v[152:155], v[136:139], v[52:55], v[152:155]\n"
    "buffer_load_dwordx4 v[104:107], v192, s[0:3], s37 offen offset:2048 sc1\n"
    "s_waitcnt vmcnt(10)\n"
    "v_max3_u32 v184, v140, v142, v184\n"
    "v_swap_b32 v141, v142\n"
    "s_nop 1\n"
    "v_mfma_f32_16x16x32_bf16 v[156:159], v[140:143], v[40:43], 0\n"
    "v_mfma_f32_16x16x32_bf16 v[160:163], v[140:143], v[48:51], 0\n"
    "buffer_load_dwordx4 v[128:131], v198, s[0:3], s38 offen sc1\n"
    "s_waitcnt vmcnt(10)\n"
    "v_max3_u32 v184, v144, v146, v184\n"
    "v_swap_b32 v145, v146\n"
    "s_nop 1\n"
    "v_mfma_f32_16x16x32_bf16 v[156:159], v[144:147], v[44:47], v[156:159]\n"
    "v_mfma_f32_16x16x32_bf16 v[160:163], v[144:147], v[52:55], v[160:163]\n"
    "buffer_load_dwordx4 v[108:111], v192, s[0:3], s38 offen offset:0 sc1\n"
    "s_waitcnt vmcnt(10)\n"
    "v_max3_u32 v184, v116, v118, v184\n"
    "v_smfmac_f32_16x16x64_bf16 v[148:151], v[116:119], a[32:39], v195\n"
    "v_smfmac_f32_16x16x64_bf16 v[152:155], v[116:119], v[8:15], v195\n"
    "buffer_load_dwordx4 v[112:115], v192, s[0:3], s38 offen offset:2048 sc1\n"
    "s_waitcnt vmcnt(10)\n"
    "v_max3_u32 v184, v84, v86, v184\n"
    "v_swap_b32 v85, v86\n"
    "s_nop 1\n"
    "v_mfma_f32_16x16x32_bf16 v[156:159], v[84:87], a[32:35], v[156:159]\n"
    "v_mfma_f32_16x16x32_bf16 v[160:163], v[84:87], v[8:11], v[160:163]\n"
    "buffer_load_dwordx2 v[186:187], v210, s[4:7], s46 offen sc1\n"
    "s_waitcnt vmcnt(10)\n"
    "v_max3_u32 v184, v88, v90, v184\n"
    "v_swap_b32 v89, v90\n"
    "s_nop 1\n"
    "v_mfma_f32_16x16x32_bf16 v[156:159], v[88:91], a[36:39], v[156:159]\n"
    "v_mfma_f32_16x16x32_bf16 v[160:163], v[88:91], v[12:15], v[160:163]\n"
    "buffer_store_dwordx2 v[166:167], v203, s[0:3], s31 offen sc1\n"
    "s_waitcnt vmcnt(10)\n"
    "v_max3_u32 v184, v120, v122, v184\n"
    "v_smfmac_f32_16x16x64_bf16 v[148:151], v[120:123], a[40:47], v195\n"
    "v_smfmac_f32_16x16x64_bf16 v[152:155], v[120:123], v[16:23], v195\n"
    "buffer_load_dwordx4 a[0:3], v192, s[0:3], s39 offen offset:0 sc1\n"
    "s_waitcnt vmcnt(10)\n"
    "v_max3_u32 v184, v92, v94, v184\n"
    "v_swap_b32 v93, v94\n"
    "s_nop 1\n"
    "v_mfma_f32_16x16x32_bf16 v[156:159], v[92:95], a[40:43], v[156:159]\n"
    "v_mfma_f32_16x16x32_bf16 v[160:163], v[92:95], v[16:19], v[160:163]\n"
    "buffer_load_dwordx4 a[4:7], v192, s[0:3], s39 offen offset:1024 sc1\n"
    "s_waitcnt vmcnt(10)\n"
    "v_max3_u32 v184, v96, v98, v184\n"
    "v_swap_b32 v97, v98\n"
    "s_nop 1\n"
    "v_mfma_f32_16x16x32_bf16 v[156:159], v[96:99], a[44:47], v[156:159]\n"
    "v_mfma_f32_16x16x32_bf16 v[160:163], v[96:99], v[20:23], v[160:163]\n"
    "buffer_load_dwordx4 a[8:11], v192, s[0:3], s39 offen offset:2048 sc1\n"
    "s_waitcnt vmcnt(10)\n"
    "v_max3_u32 v184, v124, v126, v184\n"
    "v_smfmac_f32_16x16x64_bf16 v[148:151], v[124:127], a[48:55], v195\n"
    "v_smfmac_f32_16x16x64_bf16 v[152:155], v[124:127], v[24:31], v195\n"
    "buffer_load_dwordx4 a[12:15], v192, s[0:3], s39 offen offset:3072 sc1\n"
    "s_waitcnt vmcnt(10)\n"
    "v_max3_u32 v184, v100, v102, v184\n"
    "v_swap_b32 v101, v102\n"
    "s_nop 1\n"
    "v_mfma_f32_16x16x32_bf16 v[156:159], v[100:103], a[48:51], v[156:159]\n"
    "v_mfma_f32_16x16x32_bf16 v[160:163], v[100:103], v[24:27], v[160:163]\n"
    "buffer_load_dwordx4 a[16:19], v193, s[0:3], s39 offen offset:0 sc1\n"
    "s_waitcnt vmcnt(10)\n"
    "v_max3_u32 v184, v104, v106, v184\n"
    "v_swap_b32 v105, v106\n"
    "s_nop 1\n"
    "v_mfma_f32_16x16x32_bf16 v[156:159], v[104:107], a[52:55], v[156:159]\n"
    "v_mfma_f32_16x16x32_bf16 v[160:163], v[104:107], v[28:31], v[160:163]\n"
    "buffer_load_dwordx4 a[20:23], v193, s[0:3], s39 offen offset:1024 sc1\n"
    "s_waitcnt vmcnt(10)\n"
    "v_max3_u32 v184, v128, v130, v184\n"
    "v_smfmac_f32_16x16x64_bf16 v[148:151], v[128:131], a[56:63], v195\n"
    "v_smfmac_f32_16x16x64_bf16 v[152:155], v[128:131], v[32:39], v195\n"
    "buffer_load_dwordx4 a[24:27], v193, s[0:3], s39 offen offset:2048 sc1\n"
    "s_waitcnt vmcnt(10)\n"
    "v_max3_u32 v184, v108, v110, v184\n"
    "v_swap_b32 v109, v110\n"
    "s_nop 1\n"
    "v_mfma_f32_16x16x32_bf16 v[156:159], v[108:111], a[56:59], v[156:159]\n"
    "v_mfma_f32_16x16x32_bf16 v[160:163], v[108:111], v[32:35], v[160:163]\n"
    "buffer_load_dwordx4 a[28:31], v193, s[0:3], s39 offen offset:3072 sc1\n"
    "s_waitcnt vmcnt(10)\n"
    "v_max3_u32 v184, v112, v114, v184\n"
    "v_swap_b32 v113, v114\n"
    "s_nop 1\n"
    "v_mfma_f32_16x16x32_bf16 v[156:159], v[112:115], a[60:63], v[156:159]\n"
    "v_mfma_f32_16x16x32_bf16 v[160:163], v[112:115], v[36:39], v[160:163]\n"
    "v_cmp_eq_u32 vcc, -1, v184\n"
    "s_nop 0\n"
    "s_cbranch_vccnz .Lct_tr1_%=\n"
    ".Lct_tg1_%=:\n"
    ".Lct_mk4x24_%=:\n"
    "ds_write2_b32 v204, v148, v149 offset0:0 offset1:20\n"
    "ds_write2_b32 v204, v150, v151 offset0:40 offset1:60\n"
    "ds_write2_b32 v205, v152, v153 offset0:0 offset1:20\n"
    "ds_write2_b32 v205, v154, v155 offset0:40 offset1:60\n"
    "ds_write2_b32 v206, v156, v157 offset0:0 offset1:20\n"
    "ds_write2_b32 v206, v158, v159 offset0:40 offset1:60\n"
    "ds_write2_b32 v207, v160, v161 offset0:0 offset1:20\n"
    "ds_write2_b32 v207, v162, v163 offset0:40 offset1:60\n"
    "s_mov_b32 s42, s45\n"
    "buffer_load_dwordx4 v[0:3], v196, s[0:3], s40 offen offset:0 sc1\n"
    "buffer_load_dwordx4 v[4:7], v196, s[0:3], s40 offen offset:1024 sc1\n"
    "s_add_u32 s28, s28, 1\n"
    "s_add_u32 s27, s28, 1\n"
    "s_add_u32 s29, s28, 0\n"
    "s_and_b32 s29, s29, 3\n"
    "s_lshl_b32 s29, s29, 22\n"
    "s_add_u32 s30, s28, 1\n"
    "s_and_b32 s30, s30, 3\n"
    "s_lshl_b32 s30, s30, 22\n"
    "s_add_u32 s31, s28, 3\n"
    "s_and_b32 s31, s31, 3\n"
    "s_lshl_b32 s31, s31, 22\n"
    "s_and_b32 s41, s28, 63\n"
    "s_lshl_b32 s41, s41, 7\n"
    "s_add_u32 s32, s12, s29\n"
    "s_add_u32 s33, s13, s29\n"
    "s_add_u32 s34, s15, s29\n"
    "s_add_u32 s35, s14, s29\n"
    "s_add_u32 s39, s10, s30\n"
    "s_add_u32 s40, s11, s30\n"
    "s_add_u32 s41, s16, s41\n"
    "s_add_u32 s36, s35, 4096\n"
    "s_add_u32 s37, s35, 16384\n"
    "s_add_u32 s38, s35, 20480\n"
    "s_add_u32 s43, s26, 304\n"
    "s_lshl_b32 s45, s28, 19\n"
    "s_lshl_b32 s25, s28, 11\n"
    "s_xor_b32 s25, s25, s8\n"
    "s_and_b32 s25, s25, 0x2000\n"
    "s_add_u32 s45, s45, s25\n"
    "s_add_u32 s45, s45, s9\n"
    "s_mul_i32 s46, s28, 40960\n"
    "s_cmp_eq_u32 s8, 0\n"
    "s_cselect_b32 s46, s46, s45\n"
    "s_cmp_eq_u32 s28, 63\n"
    "s_cselect_b32 s39, 2130706432, s39\n"
    "s_cselect_b32 s40, 2130706432, s40\n"
    "v_mov_b64 v[76:77], 0\n"
    "v_mov_b64 v[78:79], 0\n"
    "v_mov_b64 v[80:81], 0\n"
    "v_mov_b64 v[82:83], 0\n"
    "s_waitcnt vmcnt(11)\n"
    "v_lshlrev_b32 v148, 16, v186\n"
    "v_and_b32 v149, 0xffff0000, v186\n"
    "v_lshlrev_b32 v150, 16, v187\n"
    "v_and_b32 v151, 0xffff0000, v187\n"
    "s_memrealtime s[22:23]\n"
    "s_waitcnt lgkmcnt(0)\n"
    "s_barrier\n"
    "s_sub_u32 s24, s43, s22\n"
    "s_cmp_gt_i32 s24, 14\n"
    "s_cselect_b32 s24, 2130706432, s41\n"
    "buffer_load_dwordx2 v[190:191], off, s[0:3], s24 sc1\n"
    ".Lct_mk6x25_%=:\n"
    "ds_read_b128 v[164:167], v208 offset:0\n"
    "ds_read_b128 v[168:171], v208 offset:2560\n"
    "ds_read_b128 v[172:175], v208 offset:5120\n"
    "ds_read_b128 v[176:179], v208 offset:7680\n"
    "s_waitcnt lgkmcnt(2)\n"
    "v_pk_add_f32 v[164:165], v[164:165], v[168:169]\n"
    "v_pk_add_f32 v[166:167], v[166:167], v[170:171]\n"
    "s_waitcnt lgkmcnt(1)\n"
    "v_pk_add_f32 v[164:165], v[164:165], v[172:173]\n"
    "v_pk_add_f32 v[166:167], v[166:167], v[174:175]\n"
    "s_waitcnt lgkmcnt(0)\n"
    "v_pk_add_f32 v[164:165], v[164:165], v[176:177]\n"
    "v_pk_add_f32 v[166:167], v[166:167], v[178:179]\n"
    "v_cvt_pk_bf16_f32 v164, v164, v165\n"
    "v_cvt_pk_bf16_f32 v165, v166, v167\n"
    "v_lshlrev_b32 v168, 16, v164\n"
    "v_and_b32 v169, 0xffff0000, v164\n"
    "v_lshlrev_b32 v170, 16, v165\n"
    "v_and_b32 v171, 0xffff0000, v165\n"
    "v_pk_add_f32 v[168:169], v[168:169], v[148:149]\n"
    "v_pk_add_f32 v[170:171], v[170:171], v[150:151]\n"
    "v_cvt_pk_bf16_f32 v164, v168, v169\n"
    "v_cvt_pk_bf16_f32 v165, v170, v171\n"
    "buffer_store_dwordx2 v[164:165], v209, s[0:3], s42 offen sc1\n"
    ".Lct_mk5x26_%=:\n"
    "s_cmp_lt_u32 s28, 64\n"
    "s_cbranch_scc1 .Lct_top_%=\n"
    "v_mov_b32 v148, s28\n"
    "v_mov_b32 v149, s28\n"
    "v_mov_b32 v150, s28\n"
    "v_mov_b32 v151, s28\n"
    "s_waitcnt vmcnt(0)\n"
    "s_barrier\n"
    "buffer_store_dwordx4 v[148:151], v211, s[0:3], 0 offen sc1\n"
    "s_waitcnt vmcnt(0) lgkmcnt(0)\n"
    "s_endpgm\n"
    ".Lct_xr0_%=:\n"
    "s_add_u32 s20, s20, 1\n"
    "s_cmp_lt_u32 s20, 0x40000\n"
    "s_cbranch_scc1 .Lct_sp14_%=\n"
    "v_mov_b32 v184, 42\n"
    "s_mov_b32 s24, 33728\n"
    "buffer_store_dword v184, off, s[0:3], s24 sc1\n"
    "s_waitcnt vmcnt(0)\n"
    "s_branch .Lct_xg0_%=\n"
    ".Lct_sp14_%=:\n"
    ".Lct_rt15_%=:\n"
    "s_memrealtime s[22:23]\n"
    "s_waitcnt lgkmcnt(0)\n"
    "s_sub_u32 s24, s43, s22\n"
    "s_cmp_lt_u32 s24, 600\n"
    "s_cbranch_scc1 .Lct_rt15_%=\n"
    "s_add_u32 s43, s22, 44\n"
    "buffer_load_dwordx2 v[190:191], off, s[0:3], s41 sc1\n"
    "s_waitcnt vmcnt(0)\n"
    "s_branch .Lct_x0_%=\n"
    ".Lct_retry0_%=:\n"
    "s_add_u32 s20, s20, 1\n"
    "s_cmp_lt_u32 s20, 0x40000\n"
    "s_cbranch_scc1 .Lct_sp16_%=\n"
    "v_mov_b32 v184, 3\n"
    "s_mov_b32 s24, 33728\n"
    "buffer_store_dword v184, off, s[0:3], s24 sc1\n"
    "s_waitcnt vmcnt(0)\n"
    "s_branch .Lct_go0_%=\n"
    ".Lct_sp16_%=:\n"
    "s_waitcnt vmcnt(0) lgkmcnt(0)\n"
    "v_mov_b64 v[76:77], 0\n"
    "v_mov_b64 v[78:79], 0\n"
    "v_mov_b64 v[80:81], 0\n"
    "v_mov_b64 v[82:83], 0\n"
    "buffer_load_dwordx4 v[40:43], v192, s[0:3], s32 offen offset:0 sc1\n"
    "buffer_load_dwordx4 v[44:47], v192, s[0:3], s32 offen offset:1024 sc1\n"
    "buffer_load_dwordx4 v[48:51], v197, s[0:3], s33 offen offset:0 sc1\n"
    "buffer_load_dwordx4 v[52:55], v197, s[0:3], s33 offen offset:256 sc1\n"
    "buffer_load_dwordx4 v[72:75], v193, s[0:3], s34 offen sc1\n"
    "buffer_load_dwordx4 v[56:59], v192, s[0:3], s34 offen offset:0 sc1\n"
    "buffer_load_dwordx4 v[60:63], v192, s[0:3], s34 offen offset:1024 sc1\n"
    "buffer_load_dwordx4 v[64:67], v192, s[0:3], s34 offen offset:2048 sc1\n"
    "buffer_load_dwordx4 v[68:71], v192, s[0:3], s34 offen offset:3072 sc1\n"
    "buffer_store_dwordx4 v[148:151], v211, s[0:3], 0 offen sc1\n"
    "s_waitcnt vmcnt(0)\n"
    "s_branch .Lct_take0_%=\n"
    ".Lct_tr0_%=:\n"
    "s_add_u32 s20, s20, 1\n"
    "s_cmp_lt_u32 s20, 0x40000\n"
    "s_cbranch_scc1 .Lct_sp17_%=\n"
    "v_mov_b32 v184, 4\n"
    "s_mov_b32 s24, 33728\n"
    "buffer_store_dword v184, off, s[0:3], s24 sc1\n"
    "s_waitcnt vmcnt(0)\n"
    "s_branch .Lct_tg0_%=\n"
    ".Lct_sp17_%=:\n"
    "buffer_load_dwordx4 v[132:135], v199, s[0:3], s35 offen offset:0 sc1\n"
    "buffer_load_dwordx4 v[136:139], v199, s[0:3], s37 offen offset:0 sc1\n"
    "buffer_load_dwordx4 v[140:143], v199, s[0:3], s35 offen offset:256 sc1\n"
    "buffer_load_dwordx4 v[144:147], v199, s[0:3], s37 offen offset:256 sc1\n"
    "buffer_load_dwordx4 v[116:119], v198, s[0:3], s35 offen sc1\n"
    "buffer_load_dwordx4 v[84:87], v192, s[0:3], s35 offen offset:0 sc1\n"
    "buffer_load_dwordx4 v[88:91], v192, s[0:3], s35 offen offset:2048 sc1\n"
    "buffer_load_dwordx4 v[120:123], v198, s[0:3], s36 offen sc1\n"
    "buffer_load_dwordx4 v[92:95], v192, s[0:3], s36 offen offset:0 sc1\n"
    "buffer_load_dwordx4 v[96:99], v192, s[0:3], s36 offen offset:2048 sc1\n"
    "buffer_load_dwordx4 v[124:127], v198, s[0:3], s37 offen sc1\n"
    "buffer_load_dwordx4 v[100:103], v192, s[0:3], s37 offen offset:0 sc1\n"
    "buffer_load_dwordx4 v[104:107], v192, s[0:3], s37 offen offset:2048 sc1\n"
    "buffer_load_dwordx4 v[128:131], v198, s[0:3], s38 offen sc1\n"
    "buffer_load_dwordx4 v[108:111], v192, s[0:3], s38 offen offset:0 sc1\n"
    "buffer_load_dwordx4 v[112:115], v192, s[0:3], s38 offen offset:2048 sc1\n"
    "s_waitcnt vmcnt(0)\n"
    "v_max3_u32 v184, v132, v134, v132\n"
    "v_swap_b32 v133, v134\n"
    "s_nop 1\n"
    "v_mfma_f32_16x16x32_bf16 v[148:151], v[132:135], v[40:43], 0\n"
    "v_mfma_f32_16x16x32_bf16 v[152:155], v[132:135], v[48:51], 0\n"
    "v_max3_u32 v184, v136, v138, v184\n"
    "v_swap_b32 v137, v138\n"
    "s_nop 1\n"
    "v_mfma_f32_16x16x32_bf16 v[148:151], v[136:139], v[44:47], v[148:151]\n"
    "v_mfma_f32_16x16x32_bf16 v[152:155], v[136:139], v[52:55], v[152:155]\n"
    "v_max3_u32 v184, v140, v142, v184\n"
    "v_swap_b32 v141, v142\n"
    "s_nop 1\n"
    "v_mfma_f32_16x16x32_bf16 v[156:159], v[140:143], v[40:43], 0\n"
    "v_mfma_f32_16x16x32_bf16 v[160:163], v[140:143], v[48:51], 0\n"
    "v_max3_u32 v184, v144, v146, v184\n"
    "v_swap_b32 v145, v146\n"
    "s_nop 1\n"
    "v_mfma_f32_16x16x32_bf16 v[156:159], v[144:147], v[44:47], v[156:159]\n"
    "v_mfma_f32_16x16x32_bf16 v[160:163], v[144:147], v[52:55], v[160:163]\n"
    "v_max3_u32 v184, v116, v118, v184\n"
    "v_smfmac_f32_16x16x64_bf16 v[148:151], v[116:119], a[0:7], v195\n"
    "v_smfmac_f32_16x16x64_bf16 v[152:155], v[116:119], v[8:15], v195\n"
    "v_max3_u32 v184, v84, v86, v184\n"
    "v_swap_b32 v85, v86\n"
    "s_nop 1\n"
    "v_mfma_f32_16x16x32_bf16 v[156:159], v[84:87], a[0:3], v[156:159]\n"
    "v_mfma_f32_16x16x32_bf16 v[160:163], v[84:87], v[8:11], v[160:163]\n"
    "v_max3_u32 v184, v88, v90, v184\n"
    "v_swap_b32 v89, v90\n"
    "s_nop 1\n"
    "v_mfma_f32_16x16x32_bf16 v[156:159], v[88:91], a[4:7], v[156:159]\n"
    "v_mfma_f32_16x16x32_bf16 v[160:163], v[88:91], v[12:15], v[160:163]\n"
    "v_max3_u32 v184, v120, v122, v184\n"
    "v_smfmac_f32_16x16x64_bf16 v[148:151], v[120:123], a[8:15], v195\n"
    "v_smfmac_f32_16x16x64_bf16 v[152:155], v[120:123], v[16:23], v195\n"
    "v_max3_u32 v184, v92, v94, v184\n"
    "v_swap_b32 v93, v94\n"
    "s_nop 1\n"
    "v_mfma_f32_16x16x32_bf16 v[156:159], v[92:95], a[8:11], v[156:159]\n"
    "v_mfma_f32_16x16x32_bf16 v[160:163], v[92:95], v[16:19], v[160:163]\n"
    "v_max3_u32 v184, v96, v98, v184\n"
    "v_swap_b32 v97, v98\n"
    "s_nop 1\n"
    "v_mfma_f32_16x16x32_bf16 v[156:159], v[96:99], a[12:15], v[156:159]\n"
    "v_mfma_f32_16x16x32_bf16 v[160:163], v[96:99], v[20:23], v[160:163]\n"
    "v_max3_u32 v184, v124, v126, v184\n"
    "v_smfmac_f32_16x16x64_bf16 v[148:151], v[124:127], a[16:23], v195\n"
    "v_smfmac_f32_16x16x64_bf16 v[152:155], v[124:127], v[24:31], v195\n"
    "v_max3_u32 v184, v100, v102, v184\n"
    "v_swap_b32 v101, v102\n"
    "s_nop 1\n"
    "v_mfma_f32_16x16x32_bf16 v[156:159], v[100:103], a[16:19], v[156:159]\n"
    "v_mfma_f32_16x16x32_bf16 v[160:163], v[100:103], v[24:27], v[160:163]\n"
    "v_max3_u32 v184, v104, v106, v184\n"
    "v_swap_b32 v105, v106\n"
    "s_nop 1\n"
    "v_mfma_f32_16x16x32_bf16 v[156:159], v[104:107], a[20:23], v[156:159]\n"
    "v_mfma_f32_16x16x32_bf16 v[160:163], v[104:107], v[28:31], v[160:163]\n"
    "v_max3_u32 v184, v128, v130, v184\n"
    "v_smfmac_f32_16x16x64_bf16 v[148:151], v[128:131], a[24:31], v195\n"
    "v_smfmac_f32_16x16x64_bf16 v[152:155], v[128:131], v[32:39], v195\n"
    "v_max3_u32 v184, v108, v110, v184\n"
    "v_swap_b32 v109, v110\n"
    "s_nop 1\n"
    "v_mfma_f32_16x16x32_bf16 v[156:159], v[108:111], a[24:27], v[156:159]\n"
    "v_mfma_f32_16x16x32_bf16 v[160:163], v[108:111], v[32:35], v[160:163]\n"
    "v_max3_u32 v184, v112, v114, v184\n"
    "v_swap_b32 v113, v114\n"
    "s_nop 1\n"
    "v_mfma_f32_16x16x32_bf16 v[156:159], v[112:115], a[28:31], v[156:159]\n"
    "v_mfma_f32_16x16x32_bf16 v[160:163], v[112:115], v[36:39], v[160:163]\n"
    "v_cmp_eq_u32 vcc, -1, v184\n"
    "s_nop 0\n"
    "s_cbranch_vccnz .Lct_tr0_%=\n"
    "s_nop 7\n"
    "s_branch .Lct_tg0_%=\n"
    ".Lct_xr1_%=:\n"
    "s_add_u32 s20, s20, 1\n"
    "s_cmp_lt_u32 s20, 0x40000\n"
    "s_cbranch_scc1 .Lct_sp28_%=\n"
    "v_mov_b32 v184, 42\n"
    "s_mov_b32 s24, 33728\n"
    "buffer_store_dword v184, off, s[0:3], s24 sc1\n"
    "s_waitcnt vmcnt(0)\n"
    "s_branch .Lct_xg1_%=\n"
    ".Lct_sp28_%=:\n"
    ".Lct_rt29_%=:\n"
    "s_memrealtime s[22:23]\n"
    "s_waitcnt lgkmcnt(0)\n"
    "s_sub_u32 s24, s43, s22\n"
    "s_cmp_lt_u32 s24, 600\n"
    "s_cbranch_scc1 .Lct_rt29_%=\n"
    "s_add_u32 s43, s22, 44\n"
    "buffer_load_dwordx2 v[190:191], off, s[0:3], s41 sc1\n"
    "s_waitcnt vmcnt(0)\n"
    "s_branch .Lct_x1_%=\n"
    ".Lct_retry1_%=:\n"
    "s_add_u32 s20, s20, 1\n"
    "s_cmp_lt_u32 s20, 0x40000\n"
    "s_cbranch_scc1 .Lct_sp30_%=\n"
    "v_mov_b32 v184, 3\n"
    "s_mov_b32 s24, 33728\n"
    "buffer_store_dword v184, off, s[0:3], s24 sc1\n"
    "s_waitcnt vmcnt(0)\n"
    "s_branch .Lct_go1_%=\n"
    ".Lct_sp30_%=:\n"
    "s_waitcnt vmcnt(0) lgkmcnt(0)\n"
    "v_mov_b64 v[76:77], 0\n"
    "v_mov_b64 v[78:79], 0\n"
    "v_mov_b64 v[80:81], 0\n"
    "v_mov_b64 v[82:83], 0\n"
    "buffer_load_dwordx4 v[40:43], v192, s[0:3], s32 offen offset:0 sc1\n"
    "buffer_load_dwordx4 v[44:47], v192, s[0:3], s32 offen offset:1024 sc1\n"
    "buffer_load_dwordx4 v[48:51], v197, s[0:3], s33 offen offset:0 sc1\n"
    "buffer_load_dwordx4 v[52:55], v197, s[0:3], s33 offen offset:256 sc1\n"
    "buffer_load_dwordx4 v[72:75], v193, s[0:3], s34 offen sc1\n"
    "buffer_load_dwordx4 v[56:59], v192, s[0:3], s34 offen offset:0 sc1\n"
    "buffer_load_dwordx4 v[60:63], v192, s[0:3], s34 offen offset:1024 sc1\n"
    "buffer_load_dwordx4 v[64:67], v192, s[0:3], s34 offen offset:2048 sc1\n"
    "buffer_load_dwordx4 v[68:71], v192, s[0:3], s34 offen offset:3072 sc1\n"
    "buffer_store_dwordx4 v[148:151], v211, s[0:3], 0 offen sc1\n"
    "s_waitcnt vmcnt(0)\n"
    "s_branch .Lct_take1_%=\n"
    ".Lct_tr1_%=:\n"
    "s_add_u32 s20, s20, 1\n"
    "s_cmp_lt_u32 s20, 0x40000\n"
    "s_cbranch_scc1 .Lct_sp31_%=\n"
    "v_mov_b32 v184, 4\n"
    "s_mov_b32 s24, 33728\n"
    "buffer_store_dword v184, off, s[0:3], s24 sc1\n"
    "s_waitcnt vmcnt(0)\n"
    "s_branch .Lct_tg1_%=\n"
    ".Lct_sp31_%=:\n"
    "buffer_load_dwordx4 v[132:135], v199, s[0:3], s35 offen offset:0 sc1\n"
    "buffer_load_dwordx4 v[136:139], v199, s[0:3], s37 offen offset:0 sc1\n"
    "buffer_load_dwordx4 v[140:143], v199, s[0:3], s35 offen offset:256 sc1\n"
    "buffer_load_dwordx4 v[144:147], v199, s[0:3], s37 offen offset:256 sc1\n"
    "buffer_load_dwordx4 v[116:119], v198, s[0:3], s35 offen sc1\n"
    "buffer_load_dwordx4 v[84:87], v192, s[0:3], s35 offen offset:0 sc1\n"
    "buffer_load_dwordx4 v[88:91], v192, s[0:3], s35 offen offset:2048 sc1\n"
    "buffer_load_dwordx4 v[120:123], v198, s[0:3], s36 offen sc1\n"
    "buffer_load_dwordx4 v[92:95], v192, s[0:3], s36 offen offset:0 sc1\n"
    "buffer_load_dwordx4 v[96:99], v192, s[0:3], s36 offen offset:2048 sc1\n"
    "buffer_load_dwordx4 v[124:127], v198, s[0:3], s37 offen sc1\n"
    "buffer_load_dwordx4 v[100:103], v192, s[0:3], s37 offen offset:0 sc1\n"
    "buffer_load_dwordx4 v[104:107], v192, s[0:3], s37 offen offset:2048 sc1\n"
    "buffer_load_dwordx4 v[128:131], v198, s[0:3], s38 offen sc1\n"
    "buffer_load_dwordx4 v[108:111], v192, s[0:3], s38 offen offset:0 sc1\n"
    "buffer_load_dwordx4 v[112:115], v192, s[0:3], s38 offen offset:2048 sc1\n"
    "s_waitcnt vmcnt(0)\n"
    "v_max3_u32 v184, v132, v134, v132\n"
    "v_swap_b32 v133, v134\n"
    "s_nop 1\n"
    "v_mfma_f32_16x16x32_bf16 v[148:151], v[132:135], v[40:43], 0\n"
    "v_mfma_f32_16x16x32_bf16 v[152:155], v[132:135], v[48:51], 0\n"
    "v_max3_u32 v184, v136, v138, v184\n"
    "v_swap_b32 v137, v138\n"
    "s_nop 1\n"
    "v_mfma_f32_16x16x32_bf16 v[148:151], v[136:139], v[44:47], v[148:151]\n"
    "v_mfma_f32_16x16x32_bf16 v[152:155], v[136:139], v[52:55], v[152:155]\n"
    "v_max3_u32 v184, v140, v142, v184\n"
    "v_swap_b32 v141, v142\n"
    "s_nop 1\n"
    "v_mfma_f32_16x16x32_bf16 v[156:159], v[140:143], v[40:43], 0\n"
    "v_mfma_f32_16x16x32_bf16 v[160:163], v[140:143], v[48:51], 0\n"
    "v_max3_u32 v184, v144, v146, v184\n"
    "v_swap_b32 v145, v146\n"
    "s_nop 1\n"
    "v_mfma_f32_16x16x32_bf16 v[156:159], v[144:147], v[44:47], v[156:159]\n"
    "v_mfma_f32_16x16x32_bf16 v[160:163], v[144:147], v[52:55], v[160:163]\n"
    "v_max3_u32 v184, v116, v118, v184\n"
    "v_smfmac_f32_16x16x64_bf16 v[148:151], v[116:119], a[32:39], v195\n"
    "v_smfmac_f32_16x16x64_bf16 v[152:155], v[116:119], v[8:15], v195\n"
    "v_max3_u32 v184, v84, v86, v184\n"
    "v_swap_b32 v85, v86\n"
    "s_nop 1\n"
    "v_mfma_f32_16x16x32_bf16 v[156:159], v[84:87], a[32:35], v[156:159]\n"
    "v_mfma_f32_16x16x32_bf16 v[160:163], v[84:87], v[8:11], v[160:163]\n"
    "v_max3_u32 v184, v88, v90, v184\n"
    "v_swap_b32 v89, v90\n"
    "s_nop 1\n"
    "v_mfma_f32_16x16x32_bf16 v[156:159], v[88:91], a[36:39], v[156:159]\n"
    "v_mfma_f32_16x16x32_bf16 v[160:163], v[88:91], v[12:15], v[160:163]\n"
    "v_max3_u32 v184, v120, v122, v184\n"
    "v_smfmac_f32_16x16x64_bf16 v[148:151], v[120:123], a[40:47], v195\n"
    "v_smfmac_f32_16x16x64_bf16 v[152:155], v[120:123], v[16:23], v195\n"
    "v_max3_u32 v184, v92, v94, v184\n"
    "v_swap_b32 v93, v94\n"
    "s_nop 1\n"
    "v_mfma_f32_16x16x32_bf16 v[156:159], v[92:95], a[40:43], v[156:159]\n"
    "v_mfma_f32_16x16x32_bf16 v[160:163], v[92:95], v[16:19], v[160:163]\n"
    "v_max3_u32 v184, v96, v98, v184\n"
    "v_swap_b32 v97, v98\n"
    "s_nop 1\n"
    "v_mfma_f32_16x16x32_bf16 v[156:159], v[96:99], a[44:47], v[156:159]\n"
    "v_mfma_f32_16x16x32_bf16 v[160:163], v[96:99], v[20:23], v[160:163]\n"
    "v_max3_u32 v184, v124, v126, v184\n"
    "v_smfmac_f32_16x16x64_bf16 v[148:151], v[124:127], a[48:55], v195\n"
    "v_smfmac_f32_16x16x64_bf16 v[152:155], v[124:127], v[24:31], v195\n"
    "v_max3_u32 v184, v100, v102, v184\n"
    "v_swap_b32 v101, v102\n"
    "s_nop 1\n"
    "v_mfma_f32_16x16x32_bf16 v[156:159], v[100:103], a[48:51], v[156:159]\n"
    "v_mfma_f32_16x16x32_bf16 v[160:163], v[100:103], v[24:27], v[160:163]\n"
    "v_max3_u32 v184, v104, v106, v184\n"
    "v_swap_b32 v105, v106\n"
    "s_nop 1\n"
    "v_mfma_f32_16x16x32_bf16 v[156:159], v[104:107], a[52:55], v[156:159]\n"
    "v_mfma_f32_16x16x32_bf16 v[160:163], v[104:107], v[28:31], v[160:163]\n"
    "v_max3_u32 v184, v128, v130, v184\n"
    "v_smfmac_f32_16x16x64_bf16 v[148:151], v[128:131], a[56:63], v195\n"
    "v_smfmac_f32_16x16x64_bf16 v[152:155], v[128:131], v[32:39], v195\n"
    "v_max3_u32 v184, v108, v110, v184\n"
    "v_swap_b32 v109, v110\n"
    "s_nop 1\n"
    "v_mfma_f32_16x16x32_bf16 v[156:159], v[108:111], a[56:59], v[156:159]\n"
    "v_mfma_f32_16x16x32_bf16 v[160:163], v[108:111], v[32:35], v[160:163]\n"
    "v_max3_u32 v184, v112, v114, v184\n"
    "v_swap_b32 v113, v114\n"
    "s_nop 1\n"
    "v_mfma_f32_16x16x32_bf16 v[156:159], v[112:115], a[60:63], v[156:159]\n"
    "v_mfma_f32_16x16x32_bf16 v[160:163], v[112:115], v[36:39], v[160:163]\n"
    "v_cmp_eq_u32 vcc, -1, v184\n"
    "s_nop 0\n"
    "s_cbranch_vccnz .Lct_tr1_%=\n"
    "s_nop 7\n"
    "s_branch .Lct_tg1_%=\n"
    :: "{s[0:3]}"(s0_), "{s[4:7]}"(s4_), "{s[8:11]}"(s8_), "{s[12:15]}"(s12_), "{s[16:19]}"(s16_), "{v[192:195]}"(v192_), "{v[196:199]}"(v196_), "{v[200:203]}"(v200_), "{v[204:207]}"(v204_), "{v[208:211]}"(v208_), "{v[212:215]}"(v212_) : "memory", "v255", "a255", "s95");
    __builtin_unreachable();
}
DEV void qw_role(const MegaArgs& A, const int L, const int h, const int rep) {
    ROLE_HEAD;
    const u32x4 s0_ = {RFL(rx[0]), RFL(rx[1]), RFL(rx[2]), RFL(rx[3])};
    const u32x4 s4_ = {RFL(lo(A.Wq + h * 192 + 64)), RFL(hi(A.Wq + h * 192 + 64)), RFL(WBYTES - h * 384 - 128), RFL(0x20000u)};
    const u32x4 s8_ = {RFL(lo(A.Wv + h * 192 + 128)), RFL(hi(A.Wv + h * 192 + 128)), RFL(WBYTES - h * 384 - 256), RFL(0x20000u)};
    const u32x4 s12_ = {RFL(w), RFL(REC0 + w * 2048), RFL(QT0 + h * 32768), RFL((A.layers & 2) ? 64 : 0)};
    const u32x4 s16_ = {RFL(xl(Z_CT + (h & 7) * 256, L)), RFL(DBG0 + ((L * 128 + 112 + (h >> 3) * 4 + (h & 3)) * 4 + w) * 2048), RFL(0u), RFL(0u)};
    const u32x4 v0_ = {(unsigned)((8 * w + (l >> 3)) * 12288 + ((l & 7) ^ (((8 * w + (l >> 3)) >> 1) & 6)) * 16), (unsigned)((8 * w + (l >> 3)) * 12288 + ((l & 7) ^ (((8 * w + (l >> 3)) >> 1) & 6)) * 16 + 128), (unsigned)((8 * lq + ((l >> 2) & 1) + ((l >> 1) & 4)) * 128 + (w ^ (((8 * lq + ((l >> 2) & 1) + ((l >> 1) & 4)) >> 2) & 3)) * 32 + (l & 3) * 8), (unsigned)(0u)};
    const u32x4 v64_ = {(unsigned)(l * 16), (unsigned)(l * 16 + 40960), (unsigned)(l * 16 + w * 2048), (unsigned)(l * 16 + w * 1024 + 16384)};
    const u32x4 v68_ = {(unsigned)(l == 99 ? xl(Z_X1 + rep * 4096 + h * 128, 1) : xl(Z_XN + rep * 2048, 0) + (l < 20 ? (l >> 2) * 128 + (l & 3) * 4 : 0)), (unsigned)(l == 0 ? xl(Z_QW + h * 128 + w * 4, 0) : OOB), (unsigned)(l == 20 ? 64 : 0), (unsigned)(0u)};
    asm volatile(
    "s_mov_b32 s20, 0\n"
    "s_mov_b64 s[22:23], 0\n"
    "s_mov_b32 s25, 0\n"
    "s_lshl_b32 s32, s12, 10\n"
    "s_mov_b32 s38, 0\n"
    "s_add_u32 m0, s32, 0\n"
    "s_nop 0\n"
    "buffer_load_dwordx4 v0, s[4:7], s38 offen nt lds\n"
    "s_add_u32 m0, s32, 4096\n"
    "s_nop 0\n"
    "buffer_load_dwordx4 v1, s[4:7], s38 offen nt lds\n"
    "s_add_u32 m0, s32, 8192\n"
    "s_nop 0\n"
    "buffer_load_dwordx4 v0, s[8:11], s38 offen nt lds\n"
    "s_mov_b32 s38, 1572864\n"
    "s_add_u32 m0, s32, 12288\n"
    "s_nop 0\n"
    "buffer_load_dwordx4 v0, s[4:7], s38 offen nt lds\n"
    "s_add_u32 m0, s32, 16384\n"
    "s_nop 0\n"
    "buffer_load_dwordx4 v1, s[4:7], s38 offen nt lds\n"
    "s_add_u32 m0, s32, 20480\n"
    "s_nop 0\n"
    "buffer_load_dwordx4 v0, s[8:11], s38 offen nt lds\n"
    "s_mov_b32 s38, 3145728\n"
    "s_add_u32 m0, s32, 24576\n"
    "s_nop 0\n"
    "buffer_load_dwordx4 v0, s[4:7], s38 offen nt lds\n"
    "s_add_u32 m0, s32, 28672\n"
    "s_nop 0\n"
    "buffer_load_dwordx4 v1, s[4:7], s38 offen nt lds\n"
    "s_add_u32 m0, s32, 32768\n"
    "s_nop 0\n"
    "buffer_load_dwordx4 v0, s[8:11], s38 offen nt lds\n"
    "s_mov_b32 s38, 4718592\n"
    "s_add_u32 m0, s32, 36864\n"
    "s_nop 0\n"
    "buffer_load_dwordx4 v0, s[4:7], s38 offen nt lds\n"
    "s_add_u32 m0, s32, 40960\n"
    "s_nop 0\n"
    "buffer_load_dwordx4 v1, s[4:7], s38 offen nt lds\n"
    "s_add_u32 m0, s32, 45056\n"
    "s_nop 0\n"
    "buffer_load_dwordx4 v0, s[8:11], s38 offen nt lds\n"
    "s_mov_b32 s38, 393216\n"
    "s_add_u32 m0, s32, 49152\n"
    "s_nop 0\n"
    "buffer_load_dwordx4 v0, s[4:7], s38 offen nt lds\n"
    "s_add_u32 m0, s32, 53248\n"
    "s_nop 0\n"
    "buffer_load_dwordx4 v1, s[4:7], s38 offen nt lds\n"
    "s_add_u32 m0, s32, 57344\n"
    "s_nop 0\n"
    "buffer_load_dwordx4 v0, s[8:11], s38 offen nt lds\n"
    "s_mov_b32 s38, 1966080\n"
    "s_add_u32 m0, s32, 61440\n"
    "s_nop 0\n"
    "buffer_load_dwordx4 v0, s[4:7], s38 offen nt lds\n"
    "s_add_u32 m0, s32, 65536\n"
    "s_nop 0\n"
    "buffer_load_dwordx4 v1, s[4:7], s38 offen nt lds\n"
    "s_add_u32 m0, s32, 69632\n"
    "s_nop 0\n"
    "buffer_load_dwordx4 v0, s[8:11], s38 offen nt lds\n"
    "s_mov_b32 s38, 3538944\n"
    "s_add_u32 m0, s32, 73728\n"
    "s_nop 0\n"
    "buffer_load_dwordx4 v0, s[4:7], s38 offen nt lds\n"
    "s_add_u32 m0, s32, 77824\n"
    "s_nop 0\n"
    "buffer_load_dwordx4 v1, s[4:7], s38 offen nt lds\n"
    "s_add_u32 m0, s32, 81920\n"
    "s_nop 0\n"
    "buffer_load_dwordx4 v0, s[8:11], s38 offen nt lds\n"
    "s_mov_b32 s38, 5111808\n"
    "s_add_u32 m0, s32, 86016\n"
    "s_nop 0\n"
    "buffer_load_dwordx4 v0, s[4:7], s38 offen nt lds\n"
    "s_add_u32 m0, s32, 90112\n"
    "s_nop 0\n"
    "buffer_load_dwordx4 v1, s[4:7], s38 offen nt lds\n"
    "s_add_u32 m0, s32, 94208\n"
    "s_nop 0\n"
    "buffer_load_dwordx4 v0, s[8:11], s38 offen nt lds\n"
    "s_mov_b32 s38, 786432\n"
    "s_add_u32 m0, s32, 98304\n"
    "s_nop 0\n"
    "buffer_load_dwordx4 v0, s[4:7], s38 offen nt lds\n"
    "s_add_u32 m0, s32, 102400\n"
    "s_nop 0\n"
    "buffer_load_dwordx4 v1, s[4:7], s38 offen nt lds\n"
    "s_add_u32 m0, s32, 106496\n"
    "s_nop 0\n"
    "buffer_load_dwordx4 v0, s[8:11], s38 offen nt lds\n"
    "s_mov_b32 s38, 2359296\n"
    "s_add_u32 m0, s32, 110592\n"
    "s_nop 0\n"
    "buffer_load_dwordx4 v0, s[4:7], s38 offen nt lds\n"
    "s_add_u32 m0, s32, 114688\n"
    "s_nop 0\n"
    "buffer_load_dwordx4 v1, s[4:7], s38 offen nt lds\n"
    "s_add_u32 m0, s32, 118784\n"
    "s_nop 0\n"
    "buffer_load_dwordx4 v0, s[8:11], s38 offen nt lds\n"
    "s_mov_b32 s38, 3932160\n"
    "s_add_u32 m0, s32, 122880\n"
    "s_nop 0\n"
    "buffer_load_dwordx4 v0, s[4:7], s38 offen nt lds\n"
    "s_add_u32 m0, s32, 126976\n"
    "s_nop 0\n"
    "buffer_load_dwordx4 v1, s[4:7], s38 offen nt lds\n"
    "s_add_u32 m0, s32, 131072\n"
    "s_nop 0\n"
    "buffer_load_dwordx4 v0, s[8:11], s38 offen nt lds\n"
    "s_mov_b32 s38, 5505024\n"
    "s_add_u32 m0, s32, 135168\n"
    "s_nop 0\n"
    "buffer_load_dwordx4 v0, s[4:7], s38 offen nt lds\n"
    "s_add_u32 m0, s32, 139264\n"
    "s_nop 0\n"
    "buffer_load_dwordx4 v1, s[4:7], s38 offen nt lds\n"
    "s_add_u32 m0, s32, 143360\n"
    "s_nop 0\n"
    "buffer_load_dwordx4 v0, s[8:11], s38 offen nt lds\n"
    "s_waitcnt vmcnt(24)\n"
    "s_barrier\n"
    "v_add_u32 v8, 0, v2\n"
    "ds_read_b64_tr_b16 a[0:1], v8 offset:0\n"
    "ds_read_b64_tr_b16 a[2:3], v8 offset:256\n"
    "ds_read_b64_tr_b16 a[80:81], v8 offset:4096\n"
    "ds_read_b64_tr_b16 a[82:83], v8 offset:4352\n"
    "ds_read_b64_tr_b16 a[160:161], v8 offset:8192\n"
    "ds_read_b64_tr_b16 a[162:163], v8 offset:8448\n"
    "ds_read_b64_tr_b16 a[16:17], v8 offset:12288\n"
    "ds_read_b64_tr_b16 a[18:19], v8 offset:12544\n"
    "ds_read_b64_tr_b16 a[96:97], v8 offset:16384\n"
    "ds_read_b64_tr_b16 a[98:99], v8 offset:16640\n"
    "ds_read_b64_tr_b16 a[176:177], v8 offset:20480\n"
    "ds_read_b64_tr_b16 a[178:179], v8 offset:20736\n"
    "ds_read_b64_tr_b16 a[32:33], v8 offset:24576\n"
    "ds_read_b64_tr_b16 a[34:35], v8 offset:24832\n"
    "ds_read_b64_tr_b16 a[112:113], v8 offset:28672\n"
    "ds_read_b64_tr_b16 a[114:115], v8 offset:28928\n"
    "ds_read_b64_tr_b16 a[192:193], v8 offset:32768\n"
    "ds_read_b64_tr_b16 a[194:195], v8 offset:33024\n"
    "ds_read_b64_tr_b16 a[48:49], v8 offset:36864\n"
    "ds_read_b64_tr_b16 a[50:51], v8 offset:37120\n"
    "ds_read_b64_tr_b16 a[128:129], v8 offset:40960\n"
    "ds_read_b64_tr_b16 a[130:131], v8 offset:41216\n"
    "ds_read_b64_tr_b16 a[208:209], v8 offset:45056\n"
    "ds_read_b64_tr_b16 a[210:211], v8 offset:45312\n"
    "s_waitcnt lgkmcnt(0)\n"
    "s_barrier\n"
    "s_mov_b32 s38, 1179648\n"
    "s_add_u32 m0, s32, 0\n"
    "s_nop 0\n"
    "buffer_load_dwordx4 v0, s[4:7], s38 offen nt lds\n"
    "s_add_u32 m0, s32, 4096\n"
    "s_nop 0\n"
    "buffer_load_dwordx4 v1, s[4:7], s38 offen nt lds\n"
    "s_add_u32 m0, s32, 8192\n"
    "s_nop 0\n"
    "buffer_load_dwordx4 v0, s[8:11], s38 offen nt lds\n"
    "s_mov_b32 s38, 2752512\n"
    "s_add_u32 m0, s32, 12288\n"
    "s_nop 0\n"
    "buffer_load_dwordx4 v0, s[4:7], s38 offen nt lds\n"
    "s_add_u32 m0, s32, 16384\n"
    "s_nop 0\n"
    "buffer_load_dwordx4 v1, s[4:7], s38 offen nt lds\n"
    "s_add_u32 m0, s32, 20480\n"
    "s_nop 0\n"
    "buffer_load_dwordx4 v0, s[8:11], s38 offen nt lds\n"
    "s_mov_b32 s38, 4325376\n"
    "s_add_u32 m0, s32, 24576\n"
    "s_nop 0\n"
    "buffer_load_dwordx4 v0, s[4:7], s38 offen nt lds\n"
    "s_add_u32 m0, s32, 28672\n"
    "s_nop 0\n"
    "buffer_load_dwordx4 v1, s[4:7], s38 offen nt lds\n"
    "s_add_u32 m0, s32, 32768\n"
    "s_nop 0\n"
    "buffer_load_dwordx4 v0, s[8:11], s38 offen nt lds\n"
    "s_mov_b32 s38, 5898240\n"
    "s_add_u32 m0, s32, 36864\n"
    "s_nop 0\n"
    "buffer_load_dwordx4 v0, s[4:7], s38 offen nt lds\n"
    "s_add_u32 m0, s32, 40960\n"
    "s_nop 0\n"
    "buffer_load_dwordx4 v1, s[4:7], s38 offen nt lds\n"
    "s_add_u32 m0, s32, 45056\n"
    "s_nop 0\n"
    "buffer_load_dwordx4 v0, s[8:11], s38 offen nt lds\n"
    "v_and_b32 v4, 0x70, v64\n"
    "v_lshlrev_b32 v4, 2, v4\n"
    "v_or_b32 v4, 0x8000, v4\n"
    "buffer_load_dword v5, v4, s[0:3], 0 offen sc1\n"
    "s_waitcnt vmcnt(25)\n"
    "s_barrier\n"
    "v_add_u32 v8, 49152, v2\n"
    "ds_read_b64_tr_b16 a[4:5], v8 offset:0\n"
    "ds_read_b64_tr_b16 a[6:7], v8 offset:256\n"
    "ds_read_b64_tr_b16 a[84:85], v8 offset:4096\n"
    "ds_read_b64_tr_b16 a[86:87], v8 offset:4352\n"
    "ds_read_b64_tr_b16 a[164:165], v8 offset:8192\n"
    "ds_read_b64_tr_b16 a[166:167], v8 offset:8448\n"
    "ds_read_b64_tr_b16 a[20:21], v8 offset:12288\n"
    "ds_read_b64_tr_b16 a[22:23], v8 offset:12544\n"
    "ds_read_b64_tr_b16 a[100:101], v8 offset:16384\n"
    "ds_read_b64_tr_b16 a[102:103], v8 offset:16640\n"
    "ds_read_b64_tr_b16 a[180:181], v8 offset:20480\n"
    "ds_read_b64_tr_b16 a[182:183], v8 offset:20736\n"
    "ds_read_b64_tr_b16 a[36:37], v8 offset:24576\n"
    "ds_read_b64_tr_b16 a[38:39], v8 offset:24832\n"
    "ds_read_b64_tr_b16 a[116:117], v8 offset:28672\n"
    "ds_read_b64_tr_b16 a[118:119], v8 offset:28928\n"
    "ds_read_b64_tr_b16 a[196:197], v8 offset:32768\n"
    "ds_read_b64_tr_b16 a[198:199], v8 offset:33024\n"
    "ds_read_b64_tr_b16 a[52:53], v8 offset:36864\n"
    "ds_read_b64_tr_b16 a[54:55], v8 offset:37120\n"
    "ds_read_b64_tr_b16 a[132:133], v8 offset:40960\n"
    "ds_read_b64_tr_b16 a[134:135], v8 offset:41216\n"
    "ds_read_b64_tr_b16 a[212:213], v8 offset:45056\n"
    "ds_read_b64_tr_b16 a[214:215], v8 offset:45312\n"
    "s_waitcnt lgkmcnt(0)\n"
    "s_barrier\n"
    "s_mov_b32 s38, 6291456\n"
    "s_add_u32 m0, s32, 49152\n"
    "s_nop 0\n"
    "buffer_load_dwordx4 v0, s[4:7], s38 offen nt lds\n"
    "s_add_u32 m0, s32, 53248\n"
    "s_nop 0\n"
    "buffer_load_dwordx4 v1, s[4:7], s38 offen nt lds\n"
    "s_add_u32 m0, s32, 57344\n"
    "s_nop 0\n"
    "buffer_load_dwordx4 v0, s[8:11], s38 offen nt lds\n"
    "s_mov_b32 s38, 6684672\n"
    "s_add_u32 m0, s32, 61440\n"
    "s_nop 0\n"
    "buffer_load_dwordx4 v0, s[4:7], s38 offen nt lds\n"
    "s_add_u32 m0, s32, 65536\n"
    "s_nop 0\n"
    "buffer_load_dwordx4 v1, s[4:7], s38 offen nt lds\n"
    "s_add_u32 m0, s32, 69632\n"
    "s_nop 0\n"
    "buffer_load_dwordx4 v0, s[8:11], s38 offen nt lds\n"
    "s_mov_b32 s38, 7077888\n"
    "s_add_u32 m0, s32, 73728\n"
    "s_nop 0\n"
    "buffer_load_dwordx4 v0, s[4:7], s38 offen nt lds\n"
    "s_add_u32 m0, s32, 77824\n"
    "s_nop 0\n"
    "buffer_load_dwordx4 v1, s[4:7], s38 offen nt lds\n"
    "s_add_u32 m0, s32, 81920\n"
    "s_nop 0\n"
    "buffer_load_dwordx4 v0, s[8:11], s38 offen nt lds\n"
    "s_mov_b32 s38, 7471104\n"
    "s_add_u32 m0, s32, 86016\n"
    "s_nop 0\n"
    "buffer_load_dwordx4 v0, s[4:7], s38 offen nt lds\n"
    "s_add_u32 m0, s32, 90112\n"
    "s_nop 0\n"
    "buffer_load_dwordx4 v1, s[4:7], s38 offen nt lds\n"
    "s_add_u32 m0, s32, 94208\n"
    "s_nop 0\n"
    "buffer_load_dwordx4 v0, s[8:11], s38 offen nt lds\n"
    "s_waitcnt vmcnt(25)\n"
    "s_barrier\n"
    "v_add_u32 v8, 98304, v2\n"
    "ds_read_b64_tr_b16 a[8:9], v8 offset:0\n"
    "ds_read_b64_tr_b16 a[10:11], v8 offset:256\n"
    "ds_read_b64_tr_b16 a[88:89], v8 offset:4096\n"
    "ds_read_b64_tr_b16 a[90:91], v8 offset:4352\n"
    "ds_read_b64_tr_b16 a[168:169], v8 offset:8192\n"
    "ds_read_b64_tr_b16 a[170:171], v8 offset:8448\n"
    "ds_read_b64_tr_b16 a[24:25], v8 offset:12288\n"
    "ds_read_b64_tr_b16 a[26:27], v8 offset:12544\n"
    "ds_read_b64_tr_b16 a[104:105], v8 offset:16384\n"
    "ds_read_b64_tr_b16 a[106:107], v8 offset:16640\n"
    "ds_read_b64_tr_b16 a[184:185], v8 offset:20480\n"
    "ds_read_b64_tr_b16 a[186:187], v8 offset:20736\n"
    "ds_read_b64_tr_b16 a[40:41], v8 offset:24576\n"
    "ds_read_b64_tr_b16 a[42:43], v8 offset:24832\n"
    "ds_read_b64_tr_b16 a[120:121], v8 offset:28672\n"
    "ds_read_b64_tr_b16 a[122:123], v8 offset:28928\n"
    "ds_read_b64_tr_b16 a[200:201], v8 offset:32768\n"
    "ds_read_b64_tr_b16 a[202:203], v8 offset:33024\n"
    "ds_read_b64_tr_b16 a[56:57], v8 offset:36864\n"
    "ds_read_b64_tr_b16 a[58:59], v8 offset:37120\n"
    "ds_read_b64_tr_b16 a[136:137], v8 offset:40960\n"
    "ds_read_b64_tr_b16 a[138:139], v8 offset:41216\n"
    "ds_read_b64_tr_b16 a[216:217], v8 offset:45056\n"
    "ds_read_b64_tr_b16 a[218:219], v8 offset:45312\n"
    "s_waitcnt lgkmcnt(0)\n"
    "s_barrier\n"
    "s_waitcnt vmcnt(13)\n"
    "s_barrier\n"
    "v_add_u32 v8, 0, v2\n"
    "ds_read_b64_tr_b16 a[12:13], v8 offset:0\n"
    "ds_read_b64_tr_b16 a[14:15], v8 offset:256\n"
    "ds_read_b64_tr_b16 a[92:93], v8 offset:4096\n"
    "ds_read_b64_tr_b16 a[94:95], v8 offset:4352\n"
    "ds_read_b64_tr_b16 a[172:173], v8 offset:8192\n"
    "ds_read_b64_tr_b16 a[174:175], v8 offset:8448\n"
    "ds_read_b64_tr_b16 a[28:29], v8 offset:12288\n"
    "ds_read_b64_tr_b16 a[30:31], v8 offset:12544\n"
    "ds_read_b64_tr_b16 a[108:109], v8 offset:16384\n"
    "ds_read_b64_tr_b16 a[110:111], v8 offset:16640\n"
    "ds_read_b64_tr_b16 a[188:189], v8 offset:20480\n"
    "ds_read_b64_tr_b16 a[190:191], v8 offset:20736\n"
    "ds_read_b64_tr_b16 a[44:45], v8 offset:24576\n"
    "ds_read_b64_tr_b16 a[46:47], v8 offset:24832\n"
    "ds_read_b64_tr_b16 a[124:125], v8 offset:28672\n"
    "ds_read_b64_tr_b16 a[126:127], v8 offset:28928\n"
    "ds_read_b64_tr_b16 a[204:205], v8 offset:32768\n"
    "ds_read_b64_tr_b16 a[206:207], v8 offset:33024\n"
    "ds_read_b64_tr_b16 a[60:61], v8 offset:36864\n"
    "ds_read_b64_tr_b16 a[62:63], v8 offset:37120\n"
    "ds_read_b64_tr_b16 a[140:141], v8 offset:40960\n"
    "ds_read_b64_tr_b16 a[142:143], v8 offset:41216\n"
    "ds_read_b64_tr_b16 a[220:221], v8 offset:45056\n"
    "ds_read_b64_tr_b16 a[222:223], v8 offset:45312\n"
    "s_waitcnt lgkmcnt(0)\n"
    "s_barrier\n"
    "s_waitcnt vmcnt(0)\n"
    "s_barrier\n"
    "v_add_u32 v8, 49152, v2\n"
    "ds_read_b64_tr_b16 a[64:65], v8 offset:0\n"
    "ds_read_b64_tr_b16 a[66:67], v8 offset:256\n"
    "ds_read_b64_tr_b16 a[144:145], v8 offset:4096\n"
    "ds_read_b64_tr_b16 a[146:147], v8 offset:4352\n"
    "ds_read_b64_tr_b16 a[224:225], v8 offset:8192\n"
    "ds_read_b64_tr_b16 a[226:227], v8 offset:8448\n"
    "ds_read_b64_tr_b16 a[68:69], v8 offset:12288\n"
    "ds_read_b64_tr_b16 a[70:71], v8 offset:12544\n"
    "ds_read_b64_tr_b16 a[148:149], v8 offset:16384\n"
    "ds_read_b64_tr_b16 a[150:151], v8 offset:16640\n"
    "ds_read_b64_tr_b16 a[228:229], v8 offset:20480\n"
    "ds_read_b64_tr_b16 a[230:231], v8 offset:20736\n"
    "ds_read_b64_tr_b16 a[72:73], v8 offset:24576\n"
    "ds_read_b64_tr_b16 a[74:75], v8 offset:24832\n"
    "ds_read_b64_tr_b16 a[152:153], v8 offset:28672\n"
    "ds_read_b64_tr_b16 a[154:155], v8 offset:28928\n"
    "ds_read_b64_tr_b16 a[232:233], v8 offset:32768\n"
    "ds_read_b64_tr_b16 a[234:235], v8 offset:33024\n"
    "ds_read_b64_tr_b16 a[76:77], v8 offset:36864\n"
    "ds_read_b64_tr_b16 a[78:79], v8 offset:37120\n"
    "ds_read_b64_tr_b16 a[156:157], v8 offset:40960\n"
    "ds_read_b64_tr_b16 a[158:159], v8 offset:41216\n"
    "ds_read_b64_tr_b16 a[236:237], v8 offset:45056\n"
    "ds_read_b64_tr_b16 a[238:239], v8 offset:45312\n"
    "s_waitcnt lgkmcnt(0)\n"
    "s_barrier\n"
    "s_waitcnt vmcnt(12)\n"
    "v_cmp_gt_i32 vcc, 32, v5\n"
    "s_nop 0\n"
    "s_cbranch_vccnz .Lqw_tk0_%=\n"
    ".Lqw_tk1_%=:\n"
    "s_mov_b32 s26, -2\n"
    "v_lshrrev_b32 v4, 2, v64\n"
    ".Lqw_po4_%=:\n"
    "buffer_load_dword v8, v4, s[0:3], s16 offen sc1\n"
    "s_waitcnt vmcnt(0)\n"
    "v_cmp_gt_i32 vcc, 1, v8\n"
    "s_nop 0\n"
    "s_cbranch_vccz .Lqw_pd5_%=\n"
    "s_add_u32 s20, s20, 1\n"
    "s_cmp_lt_u32 s20, 0x40000\n"
    "s_cbranch_scc1 .Lqw_sp6_%=\n"
    "v_mov_b32 v8, 50\n"
    "s_mov_b32 s24, 33728\n"
    "buffer_store_dword v8, off, s[0:3], s24 sc1\n"
    "s_waitcnt vmcnt(0)\n"
    "s_branch .Lqw_pd5_%=\n"
    ".Lqw_sp6_%=:\n"
    "s_sleep 4\n"
    "s_branch .Lqw_po4_%=\n"
    ".Lqw_pd5_%=:\n"
    "s_mov_b32 s26, -1\n"
    "s_mov_b32 s35, 0\n"
    "s_lshl_b32 s37, s12, 11\n"
    "s_mov_b32 s45, 0\n"
    "v_mov_b32 v60, 0\n"
    "s_mov_b32 s43, 2130706432\n"
    "s_mov_b32 s41, 2130706432\n"
    "s_mov_b32 s46, 2130706432\n"
    "s_mov_b32 s34, 2130706432\n"
    "s_add_u32 s26, s26, 1\n"
    "s_sub_u32 s27, s26, 14\n"
    "s_ashr_i32 s28, s27, 1\n"
    "s_and_b32 s29, s27, 1\n"
    "s_add_u32 s30, s28, 14\n"
    "s_cmp_lt_i32 s27, 0\n"
    "s_cselect_b32 s29, 0, s29\n"
    "s_cselect_b32 s30, s26, s30\n"
    "s_cmp_eq_u32 s29, 1\n"
    "s_cselect_b32 s30, s28, s30\n"
    "s_cselect_b32 s27, s15, 64\n"
    "s_lshl_b32 s31, s29, 13\n"
    "s_add_u32 s40, s30, 1\n"
    "s_lshl_b32 s32, s30, 19\n"
    "s_lshl_b32 s25, s30, 11\n"
    "s_xor_b32 s25, s25, s31\n"
    "s_and_b32 s25, s25, 0x2000\n"
    "s_add_u32 s32, s32, s25\n"
    "s_add_u32 s32, s32, s13\n"
    "s_lshl_b32 s33, s30, 20\n"
    "s_lshl_b32 s25, s30, 12\n"
    "s_xor_b32 s25, s25, s31\n"
    "s_and_b32 s25, s25, 0x2000\n"
    "s_add_u32 s33, s33, s25\n"
    "s_add_u32 s33, s33, s14\n"
    "s_mul_i32 s47, s40, s29\n"
    "s_cmp_lt_u32 s30, s27\n"
    "s_cselect_b32 s36, 1, 0\n"
    "s_cselect_b32 s32, s32, 2130706432\n"
    "s_cmp_lt_u32 s30, s27\n"
    "s_cselect_b32 s39, s31, 2130706432\n"
    "s_cselect_b32 s47, s47, 0\n"
    "s_cmp_gt_u32 s47, 0\n"
    "s_cselect_b32 s38, 0, 2130706432\n"
    ".Lqw_top_%=:\n"
    "ds_read_b128 v[24:27], v65 offset:0\n"
    "ds_read_b128 v[28:31], v65 offset:1024\n"
    "ds_read_b128 v[32:35], v65 offset:2048\n"
    "ds_read_b128 v[36:39], v65 offset:3072\n"
    "ds_read_b128 v[40:43], v65 offset:4096\n"
    "ds_read_b128 v[44:47], v65 offset:5120\n"
    "v_add_u32 v60, v60, v70\n"
    "v_cmp_gt_i32 vcc, s47, v60\n"
    "s_nop 0\n"
    "s_cbranch_vccnz .Lqw_wx_%=\n"
    ".Lqw_wxb_%=:\n"
    "s_cmp_eq_u32 s35, 0\n"
    "s_cbranch_scc1 .Lqw_skip_%=\n"
    ".Lqw_mk0x6_%=:\n"
    "s_waitcnt lgkmcnt(4)\n"
    "v_mfma_f32_16x16x32_bf16 v[0:3], a[0:3], v[24:27], 0\n"
    "buffer_store_dwordx4 v[48:51], v66, s[0:3], s46 offen offset:0 sc1\n"
    "v_mfma_f32_16x16x32_bf16 v[4:7], a[0:3], v[28:31], 0\n"
    "v_mfma_f32_16x16x32_bf16 v[8:11], a[80:83], v[24:27], 0\n"
    "v_mfma_f32_16x16x32_bf16 v[12:15], a[80:83], v[28:31], 0\n"
    "v_mfma_f32_16x16x32_bf16 v[16:19], a[160:163], v[24:27], 0\n"
    "ds_read_b128 v[24:27], v65 offset:6144\n"
    "v_mfma_f32_16x16x32_bf16 v[20:23], a[160:163], v[28:31], 0\n"
    "ds_read_b128 v[28:31], v65 offset:7168\n"
    "s_waitcnt lgkmcnt(4)\n"
    "v_mfma_f32_16x16x32_bf16 v[0:3], a[4:7], v[32:35], v[0:3]\n"
    "v_mfma_f32_16x16x32_bf16 v[4:7], a[4:7], v[36:39], v[4:7]\n"
    "buffer_store_dwordx4 v[52:55], v66, s[0:3], s46 offen offset:1024 sc1\n"
    "v_mfma_f32_16x16x32_bf16 v[8:11], a[84:87], v[32:35], v[8:11]\n"
    "v_mfma_f32_16x16x32_bf16 v[12:15], a[84:87], v[36:39], v[12:15]\n"
    "v_mfma_f32_16x16x32_bf16 v[16:19], a[164:167], v[32:35], v[16:19]\n"
    "ds_read_b128 v[32:35], v65 offset:8192\n"
    "v_mfma_f32_16x16x32_bf16 v[20:23], a[164:167], v[36:39], v[20:23]\n"
    "ds_read_b128 v[36:39], v65 offset:9216\n"
    "s_waitcnt lgkmcnt(4)\n"
    "v_mfma_f32_16x16x32_bf16 v[0:3], a[8:11], v[40:43], v[0:3]\n"
    "v_mfma_f32_16x16x32_bf16 v[4:7], a[8:11], v[44:47], v[4:7]\n"
    "v_mfma_f32_16x16x32_bf16 v[8:11], a[88:91], v[40:43], v[8:11]\n"
    "buffer_store_dwordx4 v[56:59], v67, s[0:3], s46 offen sc1\n"
    "s_add_u32 m0, s37, 0\n"
    "v_mfma_f32_16x16x32_bf16 v[12:15], a[88:91], v[44:47], v[12:15]\n"
    "v_mfma_f32_16x16x32_bf16 v[16:19], a[168:171], v[40:43], v[16:19]\n"
    "ds_read_b128 v[40:43], v65 offset:10240\n"
    "v_mfma_f32_16x16x32_bf16 v[20:23], a[168:171], v[44:47], v[20:23]\n"
    "ds_read_b128 v[44:47], v65 offset:11264\n"
    "s_waitcnt lgkmcnt(4)\n"
    "v_mfma_f32_16x16x32_bf16 v[0:3], a[12:15], v[24:27], v[0:3]\n"
    "v_mfma_f32_16x16x32_bf16 v[4:7], a[12:15], v[28:31], v[4:7]\n"
    "v_mfma_f32_16x16x32_bf16 v[8:11], a[92:95], v[24:27], v[8:11]\n"
    "v_mfma_f32_16x16x32_bf16 v[12:15], a[92:95], v[28:31], v[12:15]\n"
    "buffer_load_dwordx4 v64, s[0:3], s32 offen sc1 lds\n"
    "v_mfma_f32_16x16x32_bf16 v[16:19], a[172:175], v[24:27], v[16:19]\n"
    "ds_read_b128 v[24:27], v65 offset:12288\n"
    "v_mfma_f32_16x16x32_bf16 v[20:23], a[172:175], v[28:31], v[20:23]\n"
    "ds_read_b128 v[28:31], v65 offset:13312\n"
    "s_waitcnt lgkmcnt(4)\n"
    "v_mfma_f32_16x16x32_bf16 v[0:3], a[16:19], v[32:35], v[0:3]\n"
    "v_mfma_f32_16x16x32_bf16 v[4:7], a[16:19], v[36:39], v[4:7]\n"
    "buffer_load_dwordx4 v64, s[0:3], s32 offen offset:1024 sc1 lds\n"
    "v_mfma_f32_16x16x32_bf16 v[8:11], a[96:99], v[32:35], v[8:11]\n"
    "s_add_u32 m0, s37, 8192\n"
    "s_add_u32 s32, s32, 0x4000\n"
    "v_mfma_f32_16x16x32_bf16 v[12:15], a[96:99], v[36:39], v[12:15]\n"
    "v_mfma_f32_16x16x32_bf16 v[16:19], a[176:179], v[32:35], v[16:19]\n"
    "ds_read_b128 v[32:35], v65 offset:14336\n"
    "v_mfma_f32_16x16x32_bf16 v[20:23], a[176:179], v[36:39], v[20:23]\n"
    "ds_read_b128 v[36:39], v65 offset:15360\n"
    "s_waitcnt lgkmcnt(4)\n"
    "v_mfma_f32_16x16x32_bf16 v[0:3], a[20:23], v[40:43], v[0:3]\n"
    "buffer_load_dwordx4 v64, s[0:3], s32 offen sc1 lds\n"
    "v_mfma_f32_16x16x32_bf16 v[4:7], a[20:23], v[44:47], v[4:7]\n"
    "v_mfma_f32_16x16x32_bf16 v[8:11], a[100:103], v[40:43], v[8:11]\n"
    "v_mfma_f32_16x16x32_bf16 v[12:15], a[100:103], v[44:47], v[12:15]\n"
    "v_mfma_f32_16x16x32_bf16 v[16:19], a[180:183], v[40:43], v[16:19]\n"
    "ds_read_b128 v[40:43], v65 offset:16384\n"
    "v_mfma_f32_16x16x32_bf16 v[20:23], a[180:183], v[44:47], v[20:23]\n"
    "ds_read_b128 v[44:47], v65 offset:17408\n"
    "s_waitcnt lgkmcnt(4)\n"
    "v_mfma_f32_16x16x32_bf16 v[0:3], a[24:27], v[24:27], v[0:3]\n"
    "buffer_load_dwordx4 v64, s[0:3], s32 offen offset:1024 sc1 lds\n"
    "v_mfma_f32_16x16x32_bf16 v[4:7], a[24:27], v[28:31], v[4:7]\n"
    "s_add_u32 m0, s37, 16384\n"
    "s_add_u32 s32, s32, 0x4000\n"
    "v_mfma_f32_16x16x32_bf16 v[8:11], a[104:107], v[24:27], v[8:11]\n"
    "v_mfma_f32_16x16x32_bf16 v[12:15], a[104:107], v[28:31], v[12:15]\n"
    "v_mfma_f32_16x16x32_bf16 v[16:19], a[184:187], v[24:27], v[16:19]\n"
    "ds_read_b128 v[24:27], v65 offset:18432\n"
    "v_mfma_f32_16x16x32_bf16 v[20:23], a[184:187], v[28:31], v[20:23]\n"
    "ds_read_b128 v[28:31], v65 offset:19456\n"
    "s_waitcnt lgkmcnt(4)\n"
    "v_mfma_f32_16x16x32_bf16 v[0:3], a[28:31], v[32:35], v[0:3]\n"
    "buffer_load_dwordx4 v64, s[0:3], s32 offen sc1 lds\n"
    "v_mfma_f32_16x16x32_bf16 v[4:7], a[28:31], v[36:39], v[4:7]\n"
    "v_mfma_f32_16x16x32_bf16 v[8:11], a[108:111], v[32:35], v[8:11]\n"
    "v_mfma_f32_16x16x32_bf16 v[12:15], a[108:111], v[36:39], v[12:15]\n"
    "v_mfma_f32_16x16x32_bf16 v[16:19], a[188:191], v[32:35], v[16:19]\n"
    "ds_read_b128 v[32:35], v65 offset:20480\n"
    "v_mfma_f32_16x16x32_bf16 v[20:23], a[188:191], v[36:39], v[20:23]\n"
    "ds_read_b128 v[36:39], v65 offset:21504\n"
    "s_waitcnt lgkmcnt(4)\n"
    "v_mfma_f32_16x16x32_bf16 v[0:3], a[32:35], v[40:43], v[0:3]\n"
    "buffer_load_dwordx4 v64, s[0:3], s32 offen offset:1024 sc1 lds\n"
    "v_mfma_f32_16x16x32_bf16 v[4:7], a[32:35], v[44:47], v[4:7]\n"
    "s_add_u32 m0, s37, 24576\n"
    "s_add_u32 s32, s32, 0x4000\n"
    "v_mfma_f32_16x16x32_bf16 v[8:11], a[112:115], v[40:43], v[8:11]\n"
    "v_mfma_f32_16x16x32_bf16 v[12:15], a[112:115], v[44:47], v[12:15]\n"
    "v_mfma_f32_16x16x32_bf16 v[16:19], a[192:195], v[40:43], v[16:19]\n"
    "ds_read_b128 v[40:43], v65 offset:22528\n"
    "v_mfma_f32_16x16x32_bf16 v[20:23], a[192:195], v[44:47], v[20:23]\n"
    "ds_read_b128 v[44:47], v65 offset:23552\n"
    "s_waitcnt lgkmcnt(4)\n"
    "v_mfma_f32_16x16x32_bf16 v[0:3], a[36:39], v[24:27], v[0:3]\n"
    "buffer_load_dwordx4 v64, s[0:3], s32 offen sc1 lds\n"
    "v_mfma_f32_16x16x32_bf16 v[4:7], a[36:39], v[28:31], v[4:7]\n"
    "v_mfma_f32_16x16x32_bf16 v[8:11], a[116:119], v[24:27], v[8:11]\n"
    "v_mfma_f32_16x16x32_bf16 v[12:15], a[116:119], v[28:31], v[12:15]\n"
    "v_mfma_f32_16x16x32_bf16 v[16:19], a[196:199], v[24:27], v[16:19]\n"
    "ds_read_b128 v[24:27], v65 offset:24576\n"
    "v_mfma_f32_16x16x32_bf16 v[20:23], a[196:199], v[28:31], v[20:23]\n"
    "ds_read_b128 v[28:31], v65 offset:25600\n"
    "s_waitcnt lgkmcnt(4)\n"
    "v_mfma_f32_16x16x32_bf16 v[0:3], a[40:43], v[32:35], v[0:3]\n"
    "buffer_load_dwordx4 v64, s[0:3], s32 offen offset:1024 sc1 lds\n"
    "v_mfma_f32_16x16x32_bf16 v[4:7], a[40:43], v[36:39], v[4:7]\n"
    "s_add_u32 m0, s37, 32768\n"
    "s_add_u32 s32, s32, 0x4000\n"
    "v_mfma_f32_16x16x32_bf16 v[8:11], a[120:123], v[32:35], v[8:11]\n"
    "v_mfma_f32_16x16x32_bf16 v[12:15], a[120:123], v[36:39], v[12:15]\n"
    "v_mfma_f32_16x16x32_bf16 v[16:19], a[200:203], v[32:35], v[16:19]\n"
    "ds_read_b128 v[32:35], v65 offset:26624\n"
    "v_mfma_f32_16x16x32_bf16 v[20:23], a[200:203], v[36:39], v[20:23]\n"
    "ds_read_b128 v[36:39], v65 offset:27648\n"
    "s_waitcnt lgkmcnt(4)\n"
    "v_mfma_f32_16x16x32_bf16 v[0:3], a[44:47], v[40:43], v[0:3]\n"
    "buffer_load_dwordx4 v64, s[0:3], s32 offen sc1 lds\n"
    "v_mfma_f32_16x16x32_bf16 v[4:7], a[44:47], v[44:47], v[4:7]\n"
    "v_mfma_f32_16x16x32_bf16 v[8:11], a[124:127], v[40:43], v[8:11]\n"
    "v_mfma_f32_16x16x32_bf16 v[12:15], a[124:127], v[44:47], v[12:15]\n"
    "v_mfma_f32_16x16x32_bf16 v[16:19], a[204:207], v[40:43], v[16:19]\n"
    "ds_read_b128 v[40:43], v65 offset:28672\n"
    "v_mfma_f32_16x16x32_bf16 v[20:23], a[204:207], v[44:47], v[20:23]\n"
    "ds_read_b128 v[44:47], v65 offset:29696\n"
    "s_waitcnt lgkmcnt(4)\n"
    "v_mfma_f32_16x16x32_bf16 v[0:3], a[48:51], v[24:27], v[0:3]\n"
    "buffer_load_dwordx4 v64, s[0:3], s32 offen offset:1024 sc1 lds\n"
    "v_mfma_f32_16x16x32_bf16 v[4:7], a[48:51], v[28:31], v[4:7]\n"
    "s_waitcnt vmcnt(10)\n"
    "v_mov_b32 v61, s44\n"
    "v_mfma_f32_16x16x32_bf16 v[8:11], a[128:131], v[24:27], v[8:11]\n"
    "v_mfma_f32_16x16x32_bf16 v[12:15], a[128:131], v[28:31], v[12:15]\n"
    "v_mfma_f32_16x16x32_bf16 v[16:19], a[208:211], v[24:27], v[16:19]\n"
    "buffer_store_dword v61, v69, s[0:3], s43 offen sc1\n"
    "ds_read_b128 v[24:27], v65 offset:30720\n"
    "v_mfma_f32_16x16x32_bf16 v[20:23], a[208:211], v[28:31], v[20:23]\n"
    "s_mov_b32 s46, s34\n"
    "ds_read_b128 v[28:31], v65 offset:31744\n"
    "s_waitcnt lgkmcnt(4)\n"
    "v_mfma_f32_16x16x32_bf16 v[0:3], a[52:55], v[32:35], v[0:3]\n"
    "s_mov_b32 s43, s41\n"
    "s_mov_b32 s44, s42\n"
    "s_mov_b32 s41, s39\n"
    "v_mfma_f32_16x16x32_bf16 v[4:7], a[52:55], v[36:39], v[4:7]\n"
    "s_mov_b32 s42, s40\n"
    "s_mov_b32 s34, s33\n"
    "s_mov_b32 s35, s36\n"
    "v_mfma_f32_16x16x32_bf16 v[8:11], a[132:135], v[32:35], v[8:11]\n"
    "s_xor_b32 s37, s37, 40960\n"
    "s_add_u32 s26, s26, 1\n"
    "s_sub_u32 s27, s26, 14\n"
    "v_mfma_f32_16x16x32_bf16 v[12:15], a[132:135], v[36:39], v[12:15]\n"
    "s_ashr_i32 s28, s27, 1\n"
    "s_and_b32 s29, s27, 1\n"
    "s_add_u32 s30, s28, 14\n"
    "v_mfma_f32_16x16x32_bf16 v[16:19], a[212:215], v[32:35], v[16:19]\n"
    "ds_read_b128 v[32:35], v65 offset:32768\n"
    "v_mfma_f32_16x16x32_bf16 v[20:23], a[212:215], v[36:39], v[20:23]\n"
    "ds_read_b128 v[36:39], v65 offset:33792\n"
    "s_waitcnt lgkmcnt(4)\n"
    "v_mfma_f32_16x16x32_bf16 v[0:3], a[56:59], v[40:43], v[0:3]\n"
    "s_cmp_lt_i32 s27, 0\n"
    "s_cselect_b32 s29, 0, s29\n"
    "s_cselect_b32 s30, s26, s30\n"
    "v_mfma_f32_16x16x32_bf16 v[4:7], a[56:59], v[44:47], v[4:7]\n"
    "s_cmp_eq_u32 s29, 1\n"
    "s_cselect_b32 s30, s28, s30\n"
    "s_cselect_b32 s27, s15, 64\n"
    "v_mfma_f32_16x16x32_bf16 v[8:11], a[136:139], v[40:43], v[8:11]\n"
    "s_lshl_b32 s31, s29, 13\n"
    "s_add_u32 s40, s30, 1\n"
    "s_lshl_b32 s32, s30, 19\n"
    "v_mfma_f32_16x16x32_bf16 v[12:15], a[136:139], v[44:47], v[12:15]\n"
    "s_lshl_b32 s25, s30, 11\n"
    "s_xor_b32 s25, s25, s31\n"
    "s_and_b32 s25, s25, 0x2000\n"
    "v_mfma_f32_16x16x32_bf16 v[16:19], a[216:219], v[40:43], v[16:19]\n"
    "ds_read_b128 v[40:43], v65 offset:34816\n"
    "s_add_u32 s32, s32, s25\n"
    "v_mfma_f32_16x16x32_bf16 v[20:23], a[216:219], v[44:47], v[20:23]\n"
    "s_add_u32 s32, s32, s13\n"
    "ds_read_b128 v[44:47], v65 offset:35840\n"
    "s_waitcnt lgkmcnt(4)\n"
    "v_mfma_f32_16x16x32_bf16 v[0:3], a[60:63], v[24:27], v[0:3]\n"
    "s_lshl_b32 s33, s30, 20\n"
    "s_lshl_b32 s25, s30, 12\n"
    "s_xor_b32 s25, s25, s31\n"
    "v_mfma_f32_16x16x32_bf16 v[4:7], a[60:63], v[28:31], v[4:7]\n"
    "s_and_b32 s25, s25, 0x2000\n"
    "s_add_u32 s33, s33, s25\n"
    "s_add_u32 s33, s33, s14\n"
    "v_mfma_f32_16x16x32_bf16 v[8:11], a[140:143], v[24:27], v[8:11]\n"
    "s_mul_i32 s47, s40, s29\n"
    "v_mfma_f32_16x16x32_bf16 v[12:15], a[140:143], v[28:31], v[12:15]\n"
    "s_cmp_lt_u32 s30, s27\n"
    "s_cselect_b32 s36, 1, 0\n"
    "s_cselect_b32 s32, s32, 2130706432\n"
    "v_mfma_f32_16x16x32_bf16 v[16:19], a[220:223], v[24:27], v[16:19]\n"
    "ds_read_b128 v[24:27], v65 offset:36864\n"
    "v_mfma_f32_16x16x32_bf16 v[20:23], a[220:223], v[28:31], v[20:23]\n"
    "ds_read_b128 v[28:31], v65 offset:37888\n"
    "s_waitcnt lgkmcnt(4)\n"
    "v_mfma_f32_16x16x32_bf16 v[0:3], a[64:67], v[32:35], v[0:3]\n"
    "s_cmp_lt_u32 s30, s27\n"
    "s_cselect_b32 s39, s31, 2130706432\n"
    "s_cselect_b32 s47, s47, 0\n"
    "v_mfma_f32_16x16x32_bf16 v[4:7], a[64:67], v[36:39], v[4:7]\n"
    "s_cmp_gt_u32 s47, 0\n"
    "s_cselect_b32 s38, 0, 2130706432\n"
    "v_mfma_f32_16x16x32_bf16 v[8:11], a[144:147], v[32:35], v[8:11]\n"
    "buffer_load_dword v60, v68, s[0:3], s38 offen sc1\n"
    "v_mfma_f32_16x16x32_bf16 v[12:15], a[144:147], v[36:39], v[12:15]\n"
    "v_mfma_f32_16x16x32_bf16 v[16:19], a[224:227], v[32:35], v[16:19]\n"
    "ds_read_b128 v[32:35], v65 offset:38912\n"
    "v_mfma_f32_16x16x32_bf16 v[20:23], a[224:227], v[36:39], v[20:23]\n"
    "ds_read_b128 v[36:39], v65 offset:39936\n"
    "s_waitcnt lgkmcnt(4)\n"
    "v_mfma_f32_16x16x32_bf16 v[0:3], a[68:71], v[40:43], v[0:3]\n"
    "v_mfma_f32_16x16x32_bf16 v[4:7], a[68:71], v[44:47], v[4:7]\n"
    "v_mfma_f32_16x16x32_bf16 v[8:11], a[148:151], v[40:43], v[8:11]\n"
    "v_mfma_f32_16x16x32_bf16 v[12:15], a[148:151], v[44:47], v[12:15]\n"
    "v_mfma_f32_16x16x32_bf16 v[16:19], a[228:231], v[40:43], v[16:19]\n"
    "v_mfma_f32_16x16x32_bf16 v[20:23], a[228:231], v[44:47], v[20:23]\n"
    "s_waitcnt lgkmcnt(2)\n"
    "v_mfma_f32_16x16x32_bf16 v[0:3], a[72:75], v[24:27], v[0:3]\n"
    "v_mfma_f32_16x16x32_bf16 v[4:7], a[72:75], v[28:31], v[4:7]\n"
    "v_mfma_f32_16x16x32_bf16 v[8:11], a[152:155], v[24:27], v[8:11]\n"
    "v_mfma_f32_16x16x32_bf16 v[12:15], a[152:155], v[28:31], v[12:15]\n"
    "v_mfma_f32_16x16x32_bf16 v[16:19], a[232:235], v[24:27], v[16:19]\n"
    "v_mfma_f32_16x16x32_bf16 v[20:23], a[232:235], v[28:31], v[20:23]\n"
    "s_waitcnt lgkmcnt(0)\n"
    "v_mfma_f32_16x16x32_bf16 v[0:3], a[76:79], v[32:35], v[0:3]\n"
    "v_mfma_f32_16x16x32_bf16 v[4:7], a[76:79], v[36:39], v[4:7]\n"
    "v_mfma_f32_16x16x32_bf16 v[8:11], a[156:159], v[32:35], v[8:11]\n"
    "v_mfma_f32_16x16x32_bf16 v[12:15], a[156:159], v[36:39], v[12:15]\n"
    "v_mfma_f32_16x16x32_bf16 v[16:19], a[236:239], v[32:35], v[16:19]\n"
    "v_mfma_f32_16x16x32_bf16 v[20:23], a[236:239], v[36:39], v[20:23]\n"
    "v_cvt_pk_bf16_f32 v48, v0, v1\n"
    "v_cvt_pk_bf16_f32 v49, v2, v3\n"
    "v_cvt_pk_bf16_f32 v50, v8, v9\n"
    "v_cvt_pk_bf16_f32 v51, v10, v11\n"
    "v_cvt_pk_bf16_f32 v52, v4, v5\n"
    "v_cvt_pk_bf16_f32 v53, v6, v7\n"
    "v_cvt_pk_bf16_f32 v54, v12, v13\n"
    "v_cvt_pk_bf16_f32 v55, v14, v15\n"
    "v_cvt_pk_bf16_f32 v56, v16, v17\n"
    "v_cvt_pk_bf16_f32 v57, v18, v19\n"
    "v_cvt_pk_bf16_f32 v58, v20, v21\n"
    "v_cvt_pk_bf16_f32 v59, v22, v23\n"
    ".Lqw_mk1x7_%=:\n"
    ".Lqw_join_%=:\n"
    "v_xor_b32 v65, 40960, v65\n"
    "s_waitcnt vmcnt(0) lgkmcnt(0)\n"
    "s_barrier\n"
    ".Lqw_mk2x8_%=:\n"
    "s_cmp_le_u32 s26, 142\n"
    "s_cbranch_scc1 .Lqw_top_%=\n"
    "buffer_store_dwordx4 v[48:51], v66, s[0:3], s46 offen offset:0 sc1\n"
    "buffer_store_dwordx4 v[52:55], v66, s[0:3], s46 offen offset:1024 sc1\n"
    "buffer_store_dwordx4 v[56:59], v67, s[0:3], s46 offen sc1\n"
    "s_waitcnt vmcnt(0)\n"
    "v_mov_b32 v61, s44\n"
    "buffer_store_dword v61, v69, s[0:3], s43 offen sc1\n"
    "s_waitcnt vmcnt(0) lgkmcnt(0)\n"
    "s_endpgm\n"
    ".Lqw_tk0_%=:\n"
    ".Lqw_po1_%=:\n"
    "buffer_load_dword v5, v4, s[0:3], 0 offen sc1\n"
    "s_waitcnt vmcnt(0)\n"
    "v_cmp_gt_i32 vcc, 32, v5\n"
    "s_nop 0\n"
    "s_cbranch_vccz .Lqw_pd2_%=\n"
    "s_add_u32 s20, s20, 1\n"
    "s_cmp_lt_u32 s20, 0x40000\n"
    "s_cbranch_scc1 .Lqw_sp3_%=\n"
    "v_mov_b32 v5, 90\n"
    "s_mov_b32 s24, 33728\n"
    "buffer_store_dword v5, off, s[0:3], s24 sc1\n"
    "s_waitcnt vmcnt(0)\n"
    "s_branch .Lqw_pd2_%=\n"
    ".Lqw_sp3_%=:\n"
    "s_sleep 8\n"
    "s_branch .Lqw_po1_%=\n"
    ".Lqw_pd2_%=:\n"
    "s_branch .Lqw_tk1_%=\n"
    ".Lqw_wx_%=:\n"
    "buffer_store_dwordx4 v[48:51], v66, s[0:3], s46 offen offset:0 sc1\n"
    "buffer_store_dwordx4 v[52:55], v66, s[0:3], s46 offen offset:1024 sc1\n"
    "buffer_store_dwordx4 v[56:59], v67, s[0:3], s46 offen sc1\n"
    "s_waitcnt vmcnt(0)\n"
    "v_mov_b32 v61, s44\n"
    "buffer_store_dword v61, v69, s[0:3], s43 offen sc1\n"
    "s_mov_b32 s43, 2130706432\n"
    "s_mov_b32 s46, 2130706432\n"
    ".Lqw_rt10_%=:\n"
    "s_memrealtime s[22:23]\n"
    "s_waitcnt lgkmcnt(0)\n"
    "s_sub_u32 s24, s45, s22\n"
    "s_cmp_lt_u32 s24, 600\n"
    "s_cbranch_scc1 .Lqw_rt10_%=\n"
    ".Lqw_po11_%=:\n"
    "buffer_load_dword v60, v68, s[0:3], 0 offen sc1\n"
    "s_waitcnt vmcnt(0)\n"
    "v_add_u32 v60, v60, v70\n"
    "s_memrealtime s[22:23]\n"
    "s_waitcnt lgkmcnt(0)\n"
    "s_add_u32 s45, s22, 228\n"
    "v_cmp_gt_i32 vcc, s47, v60\n"
    "s_nop 0\n"
    "s_cbranch_vccz .Lqw_pd12_%=\n"
    "s_add_u32 s20, s20, 1\n"
    "s_cmp_lt_u32 s20, 0x40000\n"
    "s_cbranch_scc1 .Lqw_sp13_%=\n"
    "v_mov_b32 v60, 51\n"
    "s_mov_b32 s24, 33728\n"
    "buffer_store_dword v60, off, s[0:3], s24 sc1\n"
    "s_waitcnt vmcnt(0)\n"
    "s_branch .Lqw_pd12_%=\n"
    ".Lqw_sp13_%=:\n"
    "s_sleep 10\n"
    "s_branch .Lqw_po11_%=\n"
    ".Lqw_pd12_%=:\n"
    "s_branch .Lqw_wxb_%=\n"
    ".Lqw_skip_%=:\n"
    "buffer_store_dwordx4 v[48:51], v66, s[0:3], s46 offen offset:0 sc1\n"
    "buffer_store_dwordx4 v[52:55], v66, s[0:3], s46 offen offset:1024 sc1\n"
    "buffer_store_dwordx4 v[56:59], v67, s[0:3], s46 offen sc1\n"
    "s_add_u32 m0, s37, 0\n"
    "s_nop 0\n"
    "buffer_load_dwordx4 v64, s[0:3], s32 offen sc1 lds\n"
    "buffer_load_dwordx4 v64, s[0:3], s32 offen offset:1024 sc1 lds\n"
    "s_add_u32 m0, s37, 8192\n"
    "s_add_u32 s32, s32, 0x4000\n"
    "buffer_load_dwordx4 v64, s[0:3], s32 offen sc1 lds\n"
    "buffer_load_dwordx4 v64, s[0:3], s32 offen offset:1024 sc1 lds\n"
    "s_add_u32 m0, s37, 16384\n"
    "s_add_u32 s32, s32, 0x4000\n"
    "buffer_load_dwordx4 v64, s[0:3], s32 offen sc1 lds\n"
    "buffer_load_dwordx4 v64, s[0:3], s32 offen offset:1024 sc1 lds\n"
    "s_add_u32 m0, s37, 24576\n"
    "s_add_u32 s32, s32, 0x4000\n"
    "buffer_load_dwordx4 v64, s[0:3], s32 offen sc1 lds\n"
    "buffer_load_dwordx4 v64, s[0:3], s32 offen offset:1024 sc1 lds\n"
    "s_add_u32 m0, s37, 32768\n"
    "s_add_u32 s32, s32, 0x4000\n"
    "buffer_load_dwordx4 v64, s[0:3], s32 offen sc1 lds\n"
    "buffer_load_dwordx4 v64, s[0:3], s32 offen offset:1024 sc1 lds\n"
    "s_waitcnt vmcnt(0)\n"
    "v_mov_b32 v61, s44\n"
    "buffer_store_dword v61, v69, s[0:3], s43 offen sc1\n"
    "s_mov_b32 s34, 2130706432\n"
    "s_mov_b32 s46, s34\n"
    "s_mov_b32 s43, s41\n"
    "s_mov_b32 s44, s42\n"
    "s_mov_b32 s41, s39\n"
    "s_mov_b32 s42, s40\n"
    "s_mov_b32 s34, s33\n"
    "s_mov_b32 s35, s36\n"
    "s_xor_b32 s37, s37, 40960\n"
    "s_add_u32 s26, s26, 1\n"
    "s_sub_u32 s27, s26, 14\n"
    "s_ashr_i32 s28, s27, 1\n"
    "s_and_b32 s29, s27, 1\n"
    "s_add_u32 s30, s28, 14\n"
    "s_cmp_lt_i32 s27, 0\n"
    "s_cselect_b32 s29, 0, s29\n"
    "s_cselect_b32 s30, s26, s30\n"
    "s_cmp_eq_u32 s29, 1\n"
    "s_cselect_b32 s30, s28, s30\n"
    "s_cselect_b32 s27, s15, 64\n"
    "s_lshl_b32 s31, s29, 13\n"
    "s_add_u32 s40, s30, 1\n"
    "s_lshl_b32 s32, s30, 19\n"
    "s_lshl_b32 s25, s30, 11\n"
    "s_xor_b32 s25, s25, s31\n"
    "s_and_b32 s25, s25, 0x2000\n"
    "s_add_u32 s32, s32, s25\n"
    "s_add_u32 s32, s32, s13\n"
    "s_lshl_b32 s33, s30, 20\n"
    "s_lshl_b32 s25, s30, 12\n"
    "s_xor_b32 s25, s25, s31\n"
    "s_and_b32 s25, s25, 0x2000\n"
    "s_add_u32 s33, s33, s25\n"
    "s_add_u32 s33, s33, s14\n"
    "s_mul_i32 s47, s40, s29\n"
    "s_cmp_lt_u32 s30, s27\n"
    "s_cselect_b32 s36, 1, 0\n"
    "s_cselect_b32 s32, s32, 2130706432\n"
    "s_cmp_lt_u32 s30, s27\n"
    "s_cselect_b32 s39, s31, 2130706432\n"
    "s_cselect_b32 s47, s47, 0\n"
    "s_cmp_gt_u32 s47, 0\n"
    "s_cselect_b32 s38, 0, 2130706432\n"
    "buffer_load_dword v60, v68, s[0:3], s38 offen sc1\n"
    "s_branch .Lqw_join_%=\n"
    :: "{s[0:3]}"(s0_), "{s[4:7]}"(s4_), "{s[8:11]}"(s8_), "{s[12:15]}"(s12_), "{s[16:19]}"(s16_), "{v[0:3]}"(v0_), "{v[64:67]}"(v64_), "{v[68:71]}"(v68_) : "memory", "v255", "a255", "s95");
    __builtin_unreachable();
}
DEV void b1_role(const MegaArgs& A, const int L, const int i, const int rep) {
    ROLE_HEAD;
    const int qd = 4 * w + lq, sw = (l16 & 6) << 3, hw = w < 3 ? w : 2, hd = 3 * i + w;
    const bool live = w < 3 && hd < 32;
    const unsigned om = live ? RC_O - RC_X1 - w * 2048 + (hd >> 3) * 16384 + ((hd >> 1) & 3) * 2048 + (lq & 1) * 1024 + (2 * (hd & 1) + (lq >> 1)) * 256 + l16 * 16 : OOB;
    const unsigned ox = live && l < 16 ? RC_O - RC_X1 - w * 2048 + 65536 + hd * 256 + l16 * 16 : OOB;
    u64 wm[4];
#pragma unroll
    for (int e = 0; e < 4; ++e) { const int d = l16 + 16 - 16 * (e >> 1) - 4 * lq - 2 * (e & 1) - (l16 & 1); wm[e] = __ballot(d >= 0 && d <= 14); }
    const u32x4 s0_ = {RFL(rx[0]), RFL(rx[1]), RFL(rx[2]), RFL(rx[3])};
    const u32x4 s4_ = {RFL(lo(A.wqkv + 60 * i + 16 * w)), RFL(hi(A.wqkv + 60 * i + 16 * w)), RFL((640 * 1920 - 60 * i - 16 * w) * 2), RFL(0x20000u)};
    const u32x4 s8_ = {RFL((unsigned)wm[0]), RFL((unsigned)(wm[0] >> 32)), RFL((unsigned)wm[1]), RFL((unsigned)(wm[1] >> 32))};
    const u32x4 s12_ = {RFL((unsigned)wm[2]), RFL((unsigned)(wm[2] >> 32)), RFL((unsigned)wm[3]), RFL((unsigned)(wm[3] >> 32))};
    const u32x4 s16_ = {RFL(w == 3 ? 0xf000f000u : 0u), RFL(w == 3 ? 0xf000f000u : 0u), RFL(w), RFL(L << 13)};
    const u32x4 s20_ = {RFL(REC0 + RC_X1 + w * 2048), RFL(xl(Z_X1 + rep * 4096, L)), RFL(DBG0 + ((L * 128 + 96 + i) * 4 + w) * 2048), RFL(blockIdx.x == 0 ? 6 : 0)};
    const u32x4 v0_ = {(unsigned)((l >> 1) * 3840 + (l & 1) * 16 + 0), (unsigned)((l >> 1) * 3840 + (l & 1) * 16 + 256), (unsigned)((l >> 1) * 3840 + (l & 1) * 16 + 512), (unsigned)(w * 40960 + (8 * lq + (l16 >> 2)) * 32 + (l & 3) * 8)};
    const u32x4 v224_ = {(unsigned)(l * 16 + ((w + 0) & 3) * 2048), (unsigned)(l * 16 + ((w + 1) & 3) * 2048), (unsigned)(l * 16 + ((w + 2) & 3) * 2048), (unsigned)(l * 16 + ((w + 3) & 3) * 2048)};
    const u32x4 v228_ = {(unsigned)(l * 16), (unsigned)((l & 31) * 128), (unsigned)(l < 4 && w == 3 ? xl(Z_O + l * 2048 + i * 128, L) : OOB), (unsigned)(qd == 15 ? 86016 : 40960 + (qd / 5) * 2048 + l16 * 64 + (((qd % 5) * 8) ^ sw))};
    const u32x4 v232_ = {(unsigned)(qd == 15 ? 86016 : 49152 + (qd / 5) * 4096 + l16 * 64 + (((qd % 5) * 8) ^ sw)), (unsigned)(qd == 15 ? 86016 : 61440 + (qd / 5) * 4096 + l16 * 64 + (((qd % 5) * 8) ^ sw)), (unsigned)(40960 + hw * 2048 + l16 * 64 + ((lq * 16) ^ sw)), (unsigned)(49152 + hw * 4096 + l16 * 64 + ((lq * 16) ^ sw))};
    const u32x4 v236_ = {(unsigned)(51200 + hw * 4096 + l16 * 64 + ((lq * 16) ^ sw)), (unsigned)(61440 + hw * 4096 + (l >> 2) * 64 + ((0 + (l & 3) * 8) ^ (((l >> 2) & 6) << 3))), (unsigned)(63488 + hw * 4096 + (l >> 2) * 64 + ((0 + (l & 3) * 8) ^ (((l >> 2) & 6) << 3))), (unsigned)(61440 + hw * 4096 + (l >> 2) * 64 + ((32 + (l & 3) * 8) ^ (((l >> 2) & 6) << 3)))};
    const u32x4 v240_ = {(unsigned)(63488 + hw * 4096 + (l >> 2) * 64 + ((32 + (l & 3) * 8) ^ (((l >> 2) & 6) << 3))), (unsigned)(73728 + w * 2048 + l * 16), (unsigned)(73728 + (l >> 5) * 1024 + ((l16 >> 2) * 16 + l16) * 16 + (l & 3) * 4), (unsigned)(81920 + w * 512 + l * 4)};
    const u32x4 v244_ = {(unsigned)(81920 + 3 * 512 + (l >> 5) * 256 + (48 + l16) * 4), (unsigned)(om), (unsigned)(ox), (unsigned)((l & 1) ? 0x01000c0cu : 0x0c0c0100u)};
    const u32x4 v248_ = {(unsigned)((l & 1) ? 0x03020c0cu : 0x0c0c0302u), (unsigned)(0xff800000u), (unsigned)(0u), (unsigned)(0u)};
    asm volatile(
    "s_mov_b32 s24, 0\n"
    "s_mov_b64 s[26:27], 0\n"
    "s_mov_b32 s29, 0\n"
    "s_cmp_eq_u32 s19, 0\n"
    "s_cselect_b32 s28, 12, 50\n"
    "s_sub_u32 s28, s28, s23\n"
    ".Lb1_ho1_%=:\n"
    "s_sub_u32 s28, s28, 1\n"
    "s_cbranch_scc1 .Lb1_hd2_%=\n"
    "s_sleep 17\n"
    "s_branch .Lb1_ho1_%=\n"
    ".Lb1_hd2_%=:\n"
    "v_and_b32 v30, 0x70, v228\n"
    "v_lshlrev_b32 v30, 2, v30\n"
    "v_or_b32 v30, 0x8000, v30\n"
    "buffer_load_dword v31, v30, s[0:3], 0 offen sc1\n"
    "s_waitcnt vmcnt(0)\n"
    "v_cmp_gt_i32 vcc, 32, v31\n"
    "s_nop 0\n"
    "s_cbranch_vccnz .Lb1_tk0_%=\n"
    ".Lb1_tk1_%=:\n"
    "s_mul_i32 s32, s18, 40960\n"
    "s_add_u32 s35, s18, 0\n"
    "s_and_b32 s35, s35, 3\n"
    "s_add_u32 s35, s35, 0\n"
    "s_mul_i32 s35, s35, 122880\n"
    "s_add_u32 m0, s32, 0\n"
    "s_nop 0\n"
    "buffer_load_dwordx4 v0, s[4:7], s35 offen offset:0 lds\n"
    "buffer_load_dwordx4 v1, s[4:7], s35 offen offset:1024 lds\n"
    "buffer_load_dwordx4 v2, s[4:7], s35 offen offset:2048 lds\n"
    "s_add_u32 s35, s18, 1\n"
    "s_and_b32 s35, s35, 3\n"
    "s_add_u32 s35, s35, 0\n"
    "s_mul_i32 s35, s35, 122880\n"
    "s_add_u32 m0, s32, 3072\n"
    "s_nop 0\n"
    "buffer_load_dwordx4 v0, s[4:7], s35 offen offset:0 lds\n"
    "buffer_load_dwordx4 v1, s[4:7], s35 offen offset:1024 lds\n"
    "buffer_load_dwordx4 v2, s[4:7], s35 offen offset:2048 lds\n"
    "s_add_u32 s35, s18, 2\n"
    "s_and_b32 s35, s35, 3\n"
    "s_add_u32 s35, s35, 0\n"
    "s_mul_i32 s35, s35, 122880\n"
    "s_add_u32 m0, s32, 6144\n"
    "s_nop 0\n"
    "buffer_load_dwordx4 v0, s[4:7], s35 offen offset:0 lds\n"
    "buffer_load_dwordx4 v1, s[4:7], s35 offen offset:1024 lds\n"
    "buffer_load_dwordx4 v2, s[4:7], s35 offen offset:2048 lds\n"
    "s_add_u32 s35, s18, 3\n"
    "s_and_b32 s35, s35, 3\n"
    "s_add_u32 s35, s35, 0\n"
    "s_mul_i32 s35, s35, 122880\n"
    "s_add_u32 m0, s32, 9216\n"
    "s_nop 0\n"
    "buffer_load_dwordx4 v0, s[4:7], s35 offen offset:0 lds\n"
    "buffer_load_dwordx4 v1, s[4:7], s35 offen offset:1024 lds\n"
    "buffer_load_dwordx4 v2, s[4:7], s35 offen offset:2048 lds\n"
    "s_add_u32 s35, s18, 0\n"
    "s_and_b32 s35, s35, 3\n"
    "s_add_u32 s35, s35, 4\n"
    "s_mul_i32 s35, s35, 122880\n"
    "s_add_u32 m0, s32, 12288\n"
    "s_nop 0\n"
    "buffer_load_dwordx4 v0, s[4:7], s35 offen offset:0 lds\n"
    "buffer_load_dwordx4 v1, s[4:7], s35 offen offset:1024 lds\n"
    "buffer_load_dwordx4 v2, s[4:7], s35 offen offset:2048 lds\n"
    "s_add_u32 s35, s18, 1\n"
    "s_and_b32 s35, s35, 3\n"
    "s_add_u32 s35, s35, 4\n"
    "s_mul_i32 s35, s35, 122880\n"
    "s_add_u32 m0, s32, 15360\n"
    "s_nop 0\n"
    "buffer_load_dwordx4 v0, s[4:7], s35 offen offset:0 lds\n"
    "buffer_load_dwordx4 v1, s[4:7], s35 offen offset:1024 lds\n"
    "buffer_load_dwordx4 v2, s[4:7], s35 offen offset:2048 lds\n"
    "s_add_u32 s35, s18, 2\n"
    "s_and_b32 s35, s35, 3\n"
    "s_add_u32 s35, s35, 4\n"
    "s_mul_i32 s35, s35, 122880\n"
    "s_add_u32 m0, s32, 18432\n"
    "s_nop 0\n"
    "buffer_load_dwordx4 v0, s[4:7], s35 offen offset:0 lds\n"
    "buffer_load_dwordx4 v1, s[4:7], s35 offen offset:1024 lds\n"
    "buffer_load_dwordx4 v2, s[4:7], s35 offen offset:2048 lds\n"
    "s_add_u32 s35, s18, 3\n"
    "s_and_b32 s35, s35, 3\n"
    "s_add_u32 s35, s35, 4\n"
    "s_mul_i32 s35, s35, 122880\n"
    "s_add_u32 m0, s32, 21504\n"
    "s_nop 0\n"
    "buffer_load_dwordx4 v0, s[4:7], s35 offen offset:0 lds\n"
    "buffer_load_dwordx4 v1, s[4:7], s35 offen offset:1024 lds\n"
    "buffer_load_dwordx4 v2, s[4:7], s35 offen offset:2048 lds\n"
    "s_add_u32 s35, s18, 0\n"
    "s_and_b32 s35, s35, 3\n"
    "s_add_u32 s35, s35, 8\n"
    "s_mul_i32 s35, s35, 122880\n"
    "s_add_u32 m0, s32, 24576\n"
    "s_nop 0\n"
    "buffer_load_dwordx4 v0, s[4:7], s35 offen offset:0 lds\n"
    "buffer_load_dwordx4 v1, s[4:7], s35 offen offset:1024 lds\n"
    "buffer_load_dwordx4 v2, s[4:7], s35 offen offset:2048 lds\n"
    "s_add_u32 s35, s18, 1\n"
    "s_and_b32 s35, s35, 3\n"
    "s_add_u32 s35, s35, 8\n"
    "s_mul_i32 s35, s35, 122880\n"
    "s_add_u32 m0, s32, 27648\n"
    "s_nop 0\n"
    "buffer_load_dwordx4 v0, s[4:7], s35 offen offset:0 lds\n"
    "buffer_load_dwordx4 v1, s[4:7], s35 offen offset:1024 lds\n"
    "buffer_load_dwordx4 v2, s[4:7], s35 offen offset:2048 lds\n"
    "s_add_u32 s35, s18, 2\n"
    "s_and_b32 s35, s35, 3\n"
    "s_add_u32 s35, s35, 8\n"
    "s_mul_i32 s35, s35, 122880\n"
    "s_add_u32 m0, s32, 30720\n"
    "s_nop 0\n"
    "buffer_load_dwordx4 v0, s[4:7], s35 offen offset:0 lds\n"
    "buffer_load_dwordx4 v1, s[4:7], s35 offen offset:1024 lds\n"
    "buffer_load_dwordx4 v2, s[4:7], s35 offen offset:2048 lds\n"
    "s_add_u32 s35, s18, 3\n"
    "s_and_b32 s35, s35, 3\n"
    "s_add_u32 s35, s35, 8\n"
    "s_mul_i32 s35, s35, 122880\n"
    "s_add_u32 m0, s32, 33792\n"
    "s_nop 0\n"
    "buffer_load_dwordx4 v0, s[4:7], s35 offen offset:0 lds\n"
    "buffer_load_dwordx4 v1, s[4:7], s35 offen offset:1024 lds\n"
    "buffer_load_dwordx4 v2, s[4:7], s35 offen offset:2048 lds\n"
    "s_add_u32 s35, s18, 0\n"
    "s_and_b32 s35, s35, 3\n"
    "s_add_u32 s35, s35, 12\n"
    "s_mul_i32 s35, s35, 122880\n"
    "s_add_u32 m0, s32, 36864\n"
    "s_nop 0\n"
    "buffer_load_dwordx4 v0, s[4:7], s35 offen offset:0 lds\n"
    "buffer_load_dwordx4 v1, s[4:7], s35 offen offset:1024 lds\n"
    "buffer_load_dwordx4 v2, s[4:7], s35 offen offset:2048 lds\n"
    "s_waitcnt vmcnt(36)\n"
    "ds_read_b64_tr_b16 a[0:1], v3 offset:0\n"
    "ds_read_b64_tr_b16 a[2:3], v3 offset:128\n"
    "ds_read_b64_tr_b16 a[80:81], v3 offset:1024\n"
    "ds_read_b64_tr_b16 a[82:83], v3 offset:1152\n"
    "ds_read_b64_tr_b16 a[160:161], v3 offset:2048\n"
    "ds_read_b64_tr_b16 a[162:163], v3 offset:2176\n"
    "s_waitcnt lgkmcnt(0)\n"
    "s_add_u32 s35, s18, 1\n"
    "s_and_b32 s35, s35, 3\n"
    "s_add_u32 s35, s35, 12\n"
    "s_mul_i32 s35, s35, 122880\n"
    "s_add_u32 m0, s32, 0\n"
    "s_nop 0\n"
    "buffer_load_dwordx4 v0, s[4:7], s35 offen offset:0 lds\n"
    "buffer_load_dwordx4 v1, s[4:7], s35 offen offset:1024 lds\n"
    "buffer_load_dwordx4 v2, s[4:7], s35 offen offset:2048 lds\n"
    "s_waitcnt vmcnt(36)\n"
    "ds_read_b64_tr_b16 a[4:5], v3 offset:3072\n"
    "ds_read_b64_tr_b16 a[6:7], v3 offset:3200\n"
    "ds_read_b64_tr_b16 a[84:85], v3 offset:4096\n"
    "ds_read_b64_tr_b16 a[86:87], v3 offset:4224\n"
    "ds_read_b64_tr_b16 a[164:165], v3 offset:5120\n"
    "ds_read_b64_tr_b16 a[166:167], v3 offset:5248\n"
    "s_waitcnt lgkmcnt(0)\n"
    "s_add_u32 s35, s18, 2\n"
    "s_and_b32 s35, s35, 3\n"
    "s_add_u32 s35, s35, 12\n"
    "s_mul_i32 s35, s35, 122880\n"
    "s_add_u32 m0, s32, 3072\n"
    "s_nop 0\n"
    "buffer_load_dwordx4 v0, s[4:7], s35 offen offset:0 lds\n"
    "buffer_load_dwordx4 v1, s[4:7], s35 offen offset:1024 lds\n"
    "buffer_load_dwordx4 v2, s[4:7], s35 offen offset:2048 lds\n"
    "s_waitcnt vmcnt(36)\n"
    "ds_read_b64_tr_b16 a[8:9], v3 offset:6144\n"
    "ds_read_b64_tr_b16 a[10:11], v3 offset:6272\n"
    "ds_read_b64_tr_b16 a[88:89], v3 offset:7168\n"
    "ds_read_b64_tr_b16 a[90:91], v3 offset:7296\n"
    "ds_read_b64_tr_b16 a[168:169], v3 offset:8192\n"
    "ds_read_b64_tr_b16 a[170:171], v3 offset:8320\n"
    "s_waitcnt lgkmcnt(0)\n"
    "s_add_u32 s35, s18, 3\n"
    "s_and_b32 s35, s35, 3\n"
    "s_add_u32 s35, s35, 12\n"
    "s_mul_i32 s35, s35, 122880\n"
    "s_add_u32 m0, s32, 6144\n"
    "s_nop 0\n"
    "buffer_load_dwordx4 v0, s[4:7], s35 offen offset:0 lds\n"
    "buffer_load_dwordx4 v1, s[4:7], s35 offen offset:1024 lds\n"
    "buffer_load_dwordx4 v2, s[4:7], s35 offen offset:2048 lds\n"
    "s_waitcnt vmcnt(36)\n"
    "ds_read_b64_tr_b16 a[12:13], v3 offset:9216\n"
    "ds_read_b64_tr_b16 a[14:15], v3 offset:9344\n"
    "ds_read_b64_tr_b16 a[92:93], v3 offset:10240\n"
    "ds_read_b64_tr_b16 a[94:95], v3 offset:10368\n"
    "ds_read_b64_tr_b16 a[172:173], v3 offset:11264\n"
    "ds_read_b64_tr_b16 a[174:175], v3 offset:11392\n"
    "s_waitcnt lgkmcnt(0)\n"
    "s_add_u32 s35, s18, 0\n"
    "s_and_b32 s35, s35, 3\n"
    "s_add_u32 s35, s35, 16\n"
    "s_mul_i32 s35, s35, 122880\n"
    "s_add_u32 m0, s32, 9216\n"
    "s_nop 0\n"
    "buffer_load_dwordx4 v0, s[4:7], s35 offen offset:0 lds\n"
    "buffer_load_dwordx4 v1, s[4:7], s35 offen offset:1024 lds\n"
    "buffer_load_dwordx4 v2, s[4:7], s35 offen offset:2048 lds\n"
    "s_waitcnt vmcnt(36)\n"
    "ds_read_b64_tr_b16 a[16:17], v3 offset:12288\n"
    "ds_read_b64_tr_b16 a[18:19], v3 offset:12416\n"
    "ds_read_b64_tr_b16 a[96:97], v3 offset:13312\n"
    "ds_read_b64_tr_b16 a[98:99], v3 offset:13440\n"
    "ds_read_b64_tr_b16 a[176:177], v3 offset:14336\n"
    "ds_read_b64_tr_b16 a[178:179], v3 offset:14464\n"
    "s_waitcnt lgkmcnt(0)\n"
    "s_add_u32 s35, s18, 1\n"
    "s_and_b32 s35, s35, 3\n"
    "s_add_u32 s35, s35, 16\n"
    "s_mul_i32 s35, s35, 122880\n"
    "s_add_u32 m0, s32, 12288\n"
    "s_nop 0\n"
    "buffer_load_dwordx4 v0, s[4:7], s35 offen offset:0 lds\n"
    "buffer_load_dwordx4 v1, s[4:7], s35 offen offset:1024 lds\n"
    "buffer_load_dwordx4 v2, s[4:7], s35 offen offset:2048 lds\n"
    "s_waitcnt vmcnt(36)\n"
    "ds_read_b64_tr_b16 a[20:21], v3 offset:15360\n"
    "ds_read_b64_tr_b16 a[22:23], v3 offset:15488\n"
    "ds_read_b64_tr_b16 a[100:101], v3 offset:16384\n"
    "ds_read_b64_tr_b16 a[102:103], v3 offset:16512\n"
    "ds_read_b64_tr_b16 a[180:181], v3 offset:17408\n"
    "ds_read_b64_tr_b16 a[182:183], v3 offset:17536\n"
    "s_waitcnt lgkmcnt(0)\n"
    "s_add_u32 s35, s18, 2\n"
    "s_and_b32 s35, s35, 3\n"
    "s_add_u32 s35, s35, 16\n"
    "s_mul_i32 s35, s35, 122880\n"
    "s_add_u32 m0, s32, 15360\n"
    "s_nop 0\n"
    "buffer_load_dwordx4 v0, s[4:7], s35 offen offset:0 lds\n"
    "buffer_load_dwordx4 v1, s[4:7], s35 offen offset:1024 lds\n"
    "buffer_load_dwordx4 v2, s[4:7], s35 offen offset:2048 lds\n"
    "s_waitcnt vmcnt(36)\n"
    "ds_read_b64_tr_b16 a[24:25], v3 offset:18432\n"
    "ds_read_b64_tr_b16 a[26:27], v3 offset:18560\n"
    "ds_read_b64_tr_b16 a[104:105], v3 offset:19456\n"
    "ds_read_b64_tr_b16 a[106:107], v3 offset:19584\n"
    "ds_read_b64_tr_b16 a[184:185], v3 offset:20480\n"
    "ds_read_b64_tr_b16 a[186:187], v3 offset:20608\n"
    "s_waitcnt lgkmcnt(0)\n"
    "s_add_u32 s35, s18, 3\n"
    "s_and_b32 s35, s35, 3\n"
    "s_add_u32 s35, s35, 16\n"
    "s_mul_i32 s35, s35, 122880\n"
    "s_add_u32 m0, s32, 18432\n"
    "s_nop 0\n"
    "buffer_load_dwordx4 v0, s[4:7], s35 offen offset:0 lds\n"
    "buffer_load_dwordx4 v1, s[4:7], s35 offen offset:1024 lds\n"
    "buffer_load_dwordx4 v2, s[4:7], s35 offen offset:2048 lds\n"
    "s_waitcnt vmcnt(36)\n"
    "ds_read_b64_tr_b16 a[28:29], v3 offset:21504\n"
    "ds_read_b64_tr_b16 a[30:31], v3 offset:21632\n"
    "ds_read_b64_tr_b16 a[108:109], v3 offset:22528\n"
    "ds_read_b64_tr_b16 a[110:111], v3 offset:22656\n"
    "ds_read_b64_tr_b16 a[188:189], v3 offset:23552\n"
    "ds_read_b64_tr_b16 a[190:191], v3 offset:23680\n"
    "s_waitcnt lgkmcnt(0)\n"
    "s_waitcnt vmcnt(33)\n"
    "ds_read_b64_tr_b16 a[32:33], v3 offset:24576\n"
    "ds_read_b64_tr_b16 a[34:35], v3 offset:24704\n"
    "ds_read_b64_tr_b16 a[112:113], v3 offset:25600\n"
    "ds_read_b64_tr_b16 a[114:115], v3 offset:25728\n"
    "ds_read_b64_tr_b16 a[192:193], v3 offset:26624\n"
    "ds_read_b64_tr_b16 a[194:195], v3 offset:26752\n"
    "s_waitcnt lgkmcnt(0)\n"
    "s_waitcnt vmcnt(30)\n"
    "ds_read_b64_tr_b16 a[36:37], v3 offset:27648\n"
    "ds_read_b64_tr_b16 a[38:39], v3 offset:27776\n"
    "ds_read_b64_tr_b16 a[116:117], v3 offset:28672\n"
    "ds_read_b64_tr_b16 a[118:119], v3 offset:28800\n"
    "ds_read_b64_tr_b16 a[196:197], v3 offset:29696\n"
    "ds_read_b64_tr_b16 a[198:199], v3 offset:29824\n"
    "s_waitcnt lgkmcnt(0)\n"
    "s_waitcnt vmcnt(27)\n"
    "ds_read_b64_tr_b16 a[40:41], v3 offset:30720\n"
    "ds_read_b64_tr_b16 a[42:43], v3 offset:30848\n"
    "ds_read_b64_tr_b16 a[120:121], v3 offset:31744\n"
    "ds_read_b64_tr_b16 a[122:123], v3 offset:31872\n"
    "ds_read_b64_tr_b16 a[200:201], v3 offset:32768\n"
    "ds_read_b64_tr_b16 a[202:203], v3 offset:32896\n"
    "s_waitcnt lgkmcnt(0)\n"
    "s_waitcnt vmcnt(24)\n"
    "ds_read_b64_tr_b16 a[44:45], v3 offset:33792\n"
    "ds_read_b64_tr_b16 a[46:47], v3 offset:33920\n"
    "ds_read_b64_tr_b16 a[124:125], v3 offset:34816\n"
    "ds_read_b64_tr_b16 a[126:127], v3 offset:34944\n"
    "ds_read_b64_tr_b16 a[204:205], v3 offset:35840\n"
    "ds_read_b64_tr_b16 a[206:207], v3 offset:35968\n"
    "s_waitcnt lgkmcnt(0)\n"
    "s_waitcnt vmcnt(21)\n"
    "ds_read_b64_tr_b16 a[48:49], v3 offset:36864\n"
    "ds_read_b64_tr_b16 a[50:51], v3 offset:36992\n"
    "ds_read_b64_tr_b16 a[128:129], v3 offset:37888\n"
    "ds_read_b64_tr_b16 a[130:131], v3 offset:38016\n"
    "ds_read_b64_tr_b16 a[208:209], v3 offset:38912\n"
    "ds_read_b64_tr_b16 a[210:211], v3 offset:39040\n"
    "s_waitcnt lgkmcnt(0)\n"
    "s_waitcnt vmcnt(18)\n"
    "ds_read_b64_tr_b16 a[52:53], v3 offset:0\n"
    "ds_read_b64_tr_b16 a[54:55], v3 offset:128\n"
    "ds_read_b64_tr_b16 a[132:133], v3 offset:1024\n"
    "ds_read_b64_tr_b16 a[134:135], v3 offset:1152\n"
    "ds_read_b64_tr_b16 a[212:213], v3 offset:2048\n"
    "ds_read_b64_tr_b16 a[214:215], v3 offset:2176\n"
    "s_waitcnt lgkmcnt(0)\n"
    "s_waitcnt vmcnt(15)\n"
    "ds_read_b64_tr_b16 a[56:57], v3 offset:3072\n"
    "ds_read_b64_tr_b16 a[58:59], v3 offset:3200\n"
    "ds_read_b64_tr_b16 a[136:137], v3 offset:4096\n"
    "ds_read_b64_tr_b16 a[138:139], v3 offset:4224\n"
    "ds_read_b64_tr_b16 a[216:217], v3 offset:5120\n"
    "ds_read_b64_tr_b16 a[218:219], v3 offset:5248\n"
    "s_waitcnt lgkmcnt(0)\n"
    "s_waitcnt vmcnt(12)\n"
    "ds_read_b64_tr_b16 a[60:61], v3 offset:6144\n"
    "ds_read_b64_tr_b16 a[62:63], v3 offset:6272\n"
    "ds_read_b64_tr_b16 a[140:141], v3 offset:7168\n"
    "ds_read_b64_tr_b16 a[142:143], v3 offset:7296\n"
    "ds_read_b64_tr_b16 a[220:221], v3 offset:8192\n"
    "ds_read_b64_tr_b16 a[222:223], v3 offset:8320\n"
    "s_waitcnt lgkmcnt(0)\n"
    "s_waitcnt vmcnt(9)\n"
    "ds_read_b64_tr_b16 a[64:65], v3 offset:9216\n"
    "ds_read_b64_tr_b16 a[66:67], v3 offset:9344\n"
    "ds_read_b64_tr_b16 a[144:145], v3 offset:10240\n"
    "ds_read_b64_tr_b16 a[146:147], v3 offset:10368\n"
    "ds_read_b64_tr_b16 a[224:225], v3 offset:11264\n"
    "ds_read_b64_tr_b16 a[226:227], v3 offset:11392\n"
    "s_waitcnt lgkmcnt(0)\n"
    "s_waitcnt vmcnt(6)\n"
    "ds_read_b64_tr_b16 a[68:69], v3 offset:12288\n"
    "ds_read_b64_tr_b16 a[70:71], v3 offset:12416\n"
    "ds_read_b64_tr_b16 a[148:149], v3 offset:13312\n"
    "ds_read_b64_tr_b16 a[150:151], v3 offset:13440\n"
    "ds_read_b64_tr_b16 a[228:229], v3 offset:14336\n"
    "ds_read_b64_tr_b16 a[230:231], v3 offset:14464\n"
    "s_waitcnt lgkmcnt(0)\n"
    "s_waitcnt vmcnt(3)\n"
    "ds_read_b64_tr_b16 a[72:73], v3 offset:15360\n"
    "ds_read_b64_tr_b16 a[74:75], v3 offset:15488\n"
    "ds_read_b64_tr_b16 a[152:153], v3 offset:16384\n"
    "ds_read_b64_tr_b16 a[154:155], v3 offset:16512\n"
    "ds_read_b64_tr_b16 a[232:233], v3 offset:17408\n"
    "ds_read_b64_tr_b16 a[234:235], v3 offset:17536\n"
    "s_waitcnt lgkmcnt(0)\n"
    "s_waitcnt vmcnt(0)\n"
    "ds_read_b64_tr_b16 a[76:77], v3 offset:18432\n"
    "ds_read_b64_tr_b16 a[78:79], v3 offset:18560\n"
    "ds_read_b64_tr_b16 a[156:157], v3 offset:19456\n"
    "ds_read_b64_tr_b16 a[158:159], v3 offset:19584\n"
    "ds_read_b64_tr_b16 a[236:237], v3 offset:20480\n"
    "ds_read_b64_tr_b16 a[238:239], v3 offset:20608\n"
    "s_waitcnt lgkmcnt(0)\n"
    "s_waitcnt lgkmcnt(0)\n"
    "s_barrier\n"
    "v_mov_b32 v32, 0x3f803f80\n"
    "v_mov_b32 v36, 0\n"
    "v_mov_b32 v33, 0x3f803f80\n"
    "v_mov_b32 v37, 0\n"
    "v_mov_b32 v34, 0x3f803f80\n"
    "v_mov_b32 v38, 0\n"
    "v_mov_b32 v35, 0x3f803f80\n"
    "v_mov_b32 v39, 0\n"
    "s_nop 0\n"
    "v_mfma_f32_16x16x32_bf16 v[96:99], a[0:3], v[32:35], 0\n"
    "v_mfma_f32_16x16x32_bf16 v[100:103], a[80:83], v[32:35], 0\n"
    "v_mfma_f32_16x16x32_bf16 v[104:107], a[160:163], v[32:35], 0\n"
    "v_mfma_f32_16x16x32_bf16 v[96:99], a[4:7], v[32:35], v[96:99]\n"
    "v_mfma_f32_16x16x32_bf16 v[100:103], a[84:87], v[32:35], v[100:103]\n"
    "v_mfma_f32_16x16x32_bf16 v[104:107], a[164:167], v[32:35], v[104:107]\n"
    "v_mfma_f32_16x16x32_bf16 v[96:99], a[8:11], v[32:35], v[96:99]\n"
    "v_mfma_f32_16x16x32_bf16 v[100:103], a[88:91], v[32:35], v[100:103]\n"
    "v_mfma_f32_16x16x32_bf16 v[104:107], a[168:171], v[32:35], v[104:107]\n"
    "v_mfma_f32_16x16x32_bf16 v[96:99], a[12:15], v[32:35], v[96:99]\n"
    "v_mfma_f32_16x16x32_bf16 v[100:103], a[92:95], v[32:35], v[100:103]\n"
    "v_mfma_f32_16x16x32_bf16 v[104:107], a[172:175], v[32:35], v[104:107]\n"
    "v_mfma_f32_16x16x32_bf16 v[96:99], a[16:19], v[32:35], v[96:99]\n"
    "v_mfma_f32_16x16x32_bf16 v[100:103], a[96:99], v[32:35], v[100:103]\n"
    "v_mfma_f32_16x16x32_bf16 v[104:107], a[176:179], v[32:35], v[104:107]\n"
    "v_mfma_f32_16x16x32_bf16 v[96:99], a[20:23], v[32:35], v[96:99]\n"
    "v_mfma_f32_16x16x32_bf16 v[100:103], a[100:103], v[32:35], v[100:103]\n"
    "v_mfma_f32_16x16x32_bf16 v[104:107], a[180:183], v[32:35], v[104:107]\n"
    "v_mfma_f32_16x16x32_bf16 v[96:99], a[24:27], v[32:35], v[96:99]\n"
    "v_mfma_f32_16x16x32_bf16 v[100:103], a[104:107], v[32:35], v[100:103]\n"
    "v_mfma_f32_16x16x32_bf16 v[104:107], a[184:187], v[32:35], v[104:107]\n"
    "v_mfma_f32_16x16x32_bf16 v[96:99], a[28:31], v[32:35], v[96:99]\n"
    "v_mfma_f32_16x16x32_bf16 v[100:103], a[108:111], v[32:35], v[100:103]\n"
    "v_mfma_f32_16x16x32_bf16 v[104:107], a[188:191], v[32:35], v[104:107]\n"
    "v_mfma_f32_16x16x32_bf16 v[96:99], a[32:35], v[32:35], v[96:99]\n"
    "v_mfma_f32_16x16x32_bf16 v[100:103], a[112:115], v[32:35], v[100:103]\n"
    "v_mfma_f32_16x16x32_bf16 v[104:107], a[192:195], v[32:35], v[104:107]\n"
    "v_mfma_f32_16x16x32_bf16 v[96:99], a[36:39], v[32:35], v[96:99]\n"
    "v_mfma_f32_16x16x32_bf16 v[100:103], a[116:119], v[32:35], v[100:103]\n"
    "v_mfma_f32_16x16x32_bf16 v[104:107], a[196:199], v[32:35], v[104:107]\n"
    "v_mfma_f32_16x16x32_bf16 v[96:99], a[40:43], v[32:35], v[96:99]\n"
    "v_mfma_f32_16x16x32_bf16 v[100:103], a[120:123], v[32:35], v[100:103]\n"
    "v_mfma_f32_16x16x32_bf16 v[104:107], a[200:203], v[32:35], v[104:107]\n"
    "v_mfma_f32_16x16x32_bf16 v[96:99], a[44:47], v[32:35], v[96:99]\n"
    "v_mfma_f32_16x16x32_bf16 v[100:103], a[124:127], v[32:35], v[100:103]\n"
    "v_mfma_f32_16x16x32_bf16 v[104:107], a[204:207], v[32:35], v[104:107]\n"
    "v_mfma_f32_16x16x32_bf16 v[96:99], a[48:51], v[32:35], v[96:99]\n"
    "v_mfma_f32_16x16x32_bf16 v[100:103], a[128:131], v[32:35], v[100:103]\n"
    "v_mfma_f32_16x16x32_bf16 v[104:107], a[208:211], v[32:35], v[104:107]\n"
    "v_mfma_f32_16x16x32_bf16 v[96:99], a[52:55], v[32:35], v[96:99]\n"
    "v_mfma_f32_16x16x32_bf16 v[100:103], a[132:135], v[32:35], v[100:103]\n"
    "v_mfma_f32_16x16x32_bf16 v[104:107], a[212:215], v[32:35], v[104:107]\n"
    "v_mfma_f32_16x16x32_bf16 v[96:99], a[56:59], v[32:35], v[96:99]\n"
    "v_mfma_f32_16x16x32_bf16 v[100:103], a[136:139], v[32:35], v[100:103]\n"
    "v_mfma_f32_16x16x32_bf16 v[104:107], a[216:219], v[32:35], v[104:107]\n"
    "v_mfma_f32_16x16x32_bf16 v[96:99], a[60:63], v[32:35], v[96:99]\n"
    "v_mfma_f32_16x16x32_bf16 v[100:103], a[140:143], v[32:35], v[100:103]\n"
    "v_mfma_f32_16x16x32_bf16 v[104:107], a[220:223], v[32:35], v[104:107]\n"
    "v_mfma_f32_16x16x32_bf16 v[96:99], a[64:67], v[32:35], v[96:99]\n"
    "v_mfma_f32_16x16x32_bf16 v[100:103], a[144:147], v[32:35], v[100:103]\n"
    "v_mfma_f32_16x16x32_bf16 v[104:107], a[224:227], v[32:35], v[104:107]\n"
    "v_mfma_f32_16x16x32_bf16 v[96:99], a[68:71], v[32:35], v[96:99]\n"
    "v_mfma_f32_16x16x32_bf16 v[100:103], a[148:151], v[32:35], v[100:103]\n"
    "v_mfma_f32_16x16x32_bf16 v[104:107], a[228:231], v[32:35], v[104:107]\n"
    "v_mfma_f32_16x16x32_bf16 v[96:99], a[72:75], v[32:35], v[96:99]\n"
    "v_mfma_f32_16x16x32_bf16 v[100:103], a[152:155], v[32:35], v[100:103]\n"
    "v_mfma_f32_16x16x32_bf16 v[104:107], a[232:235], v[32:35], v[104:107]\n"
    "v_mfma_f32_16x16x32_bf16 v[96:99], a[76:79], v[32:35], v[96:99]\n"
    "v_mfma_f32_16x16x32_bf16 v[100:103], a[156:159], v[32:35], v[100:103]\n"
    "v_mfma_f32_16x16x32_bf16 v[104:107], a[236:239], v[32:35], v[104:107]\n"
    "s_lshl_b32 s35, s18, 13\n"
    "v_add_u32 v8, s35, v228\n"
    "ds_write_b128 v8, v[36:39] offset:40960\n"
    "ds_write_b128 v8, v[36:39] offset:41984\n"
    "ds_write_b128 v8, v[36:39] offset:43008\n"
    "ds_write_b128 v8, v[36:39] offset:44032\n"
    "ds_write_b128 v8, v[36:39] offset:45056\n"
    "ds_write_b128 v8, v[36:39] offset:46080\n"
    "ds_write_b128 v8, v[36:39] offset:47104\n"
    "ds_write_b128 v8, v[36:39] offset:48128\n"
    "s_nop 7\n"
    "s_nop 7\n"
    "s_mov_b64 exec, s[16:17]\n"
    "v_accvgpr_write_b32 a0, v32\n"
    "v_accvgpr_write_b32 a1, v32\n"
    "v_accvgpr_write_b32 a2, v32\n"
    "v_accvgpr_write_b32 a3, v32\n"
    "v_accvgpr_write_b32 a4, v32\n"
    "v_accvgpr_write_b32 a5, v32\n"
    "v_accvgpr_write_b32 a6, v32\n"
    "v_accvgpr_write_b32 a7, v32\n"
    "v_accvgpr_write_b32 a8, v32\n"
    "v_accvgpr_write_b32 a9, v32\n"
    "v_accvgpr_write_b32 a10, v32\n"
    "v_accvgpr_write_b32 a11, v32\n"
    "v_accvgpr_write_b32 a12, v32\n"
    "v_accvgpr_write_b32 a13, v32\n"
    "v_accvgpr_write_b32 a14, v32\n"
    "v_accvgpr_write_b32 a15, v32\n"
    "v_accvgpr_write_b32 a16, v32\n"
    "v_accvgpr_write_b32 a17, v32\n"
    "v_accvgpr_write_b32 a18, v32\n"
    "v_accvgpr_write_b32 a19, v32\n"
    "v_accvgpr_write_b32 a20, v32\n"
    "v_accvgpr_write_b32 a21, v32\n"
    "v_accvgpr_write_b32 a22, v32\n"
    "v_accvgpr_write_b32 a23, v32\n"
    "v_accvgpr_write_b32 a24, v32\n"
    "v_accvgpr_write_b32 a25, v32\n"
    "v_accvgpr_write_b32 a26, v32\n"
    "v_accvgpr_write_b32 a27, v32\n"
    "v_accvgpr_write_b32 a28, v32\n"
    "v_accvgpr_write_b32 a29, v32\n"
    "v_accvgpr_write_b32 a30, v32\n"
    "v_accvgpr_write_b32 a31, v32\n"
    "v_accvgpr_write_b32 a32, v32\n"
    "v_accvgpr_write_b32 a33, v32\n"
    "v_accvgpr_write_b32 a34, v32\n"
    "v_accvgpr_write_b32 a35, v32\n"
    "v_accvgpr_write_b32 a36, v32\n"
    "v_accvgpr_write_b32 a37, v32\n"
    "v_accvgpr_write_b32 a38, v32\n"
    "v_accvgpr_write_b32 a39, v32\n"
    "v_accvgpr_write_b32 a40, v32\n"
    "v_accvgpr_write_b32 a41, v32\n"
    "v_accvgpr_write_b32 a42, v32\n"
    "v_accvgpr_write_b32 a43, v32\n"
    "v_accvgpr_write_b32 a44, v32\n"
    "v_accvgpr_write_b32 a45, v32\n"
    "v_accvgpr_write_b32 a46, v32\n"
    "v_accvgpr_write_b32 a47, v32\n"
    "v_accvgpr_write_b32 a48, v32\n"
    "v_accvgpr_write_b32 a49, v32\n"
    "v_accvgpr_write_b32 a50, v32\n"
    "v_accvgpr_write_b32 a51, v32\n"
    "v_accvgpr_write_b32 a52, v32\n"
    "v_accvgpr_write_b32 a53, v32\n"
    "v_accvgpr_write_b32 a54, v32\n"
    "v_accvgpr_write_b32 a55, v32\n"
    "v_accvgpr_write_b32 a56, v32\n"
    "v_accvgpr_write_b32 a57, v32\n"
    "v_accvgpr_write_b32 a58, v32\n"
    "v_accvgpr_write_b32 a59, v32\n"
    "v_accvgpr_write_b32 a60, v32\n"
    "v_accvgpr_write_b32 a61, v32\n"
    "v_accvgpr_write_b32 a62, v32\n"
    "v_accvgpr_write_b32 a63, v32\n"
    "v_accvgpr_write_b32 a64, v32\n"
    "v_accvgpr_write_b32 a65, v32\n"
    "v_accvgpr_write_b32 a66, v32\n"
    "v_accvgpr_write_b32 a67, v32\n"
    "v_accvgpr_write_b32 a68, v32\n"
    "v_accvgpr_write_b32 a69, v32\n"
    "v_accvgpr_write_b32 a70, v32\n"
    "v_accvgpr_write_b32 a71, v32\n"
    "v_accvgpr_write_b32 a72, v32\n"
    "v_accvgpr_write_b32 a73, v32\n"
    "v_accvgpr_write_b32 a74, v32\n"
    "v_accvgpr_write_b32 a75, v32\n"
    "v_accvgpr_write_b32 a76, v32\n"
    "v_accvgpr_write_b32 a77, v32\n"
    "v_accvgpr_write_b32 a78, v32\n"
    "v_accvgpr_write_b32 a79, v32\n"
    "s_mov_b64 exec, -1\n"
    "s_mov_b32 s30, -2\n"
    "s_mov_b32 s30, 0\n"
    "s_mov_b32 s31, 1\n"
    "s_lshl_b32 s34, s18, 11\n"
    "s_add_u32 s32, s20, s19\n"
    "s_mov_b32 s37, 0x3ea52b5c\n"
    "s_mov_b64 s[38:39], 0\n"
    "s_mov_b64 s[40:41], 0\n"
    "s_mov_b32 s36, 0\n"
    "v_mov_b32 v108, 0\n"
    "s_mov_b32 s42, 0xaaaaaaaa\n"
    "s_mov_b32 s43, 0xaaaaaaaa\n"
    "s_waitcnt lgkmcnt(0)\n"
    ".Lb1_top_%=:\n"
    "s_cmp_lg_u32 s18, 0\n"
    "s_cbranch_scc1 .Lb1_pd7_%=\n"
    "s_add_u32 s29, s26, 244\n"
    "v_min3_i32 v108, v108, v109, v110\n"
    "v_min_i32 v108, v108, v111\n"
    "v_cmp_gt_i32 vcc, s31, v108\n"
    "s_nop 0\n"
    "s_cbranch_vccz .Lb1_pd7_%=\n"
    ".Lb1_rt8_%=:\n"
    "s_memrealtime s[26:27]\n"
    "s_waitcnt lgkmcnt(0)\n"
    "s_sub_u32 s28, s36, s26\n"
    "s_cmp_lt_u32 s28, 600\n"
    "s_cbranch_scc1 .Lb1_rt8_%=\n"
    ".Lb1_po6_%=:\n"
    "buffer_load_dwordx4 v[108:111], v229, s[0:3], s21 offen sc1\n"
    "s_memrealtime s[26:27]\n"
    "s_waitcnt vmcnt(0) lgkmcnt(0)\n"
    "s_add_u32 s29, s26, 244\n"
    "v_min3_i32 v108, v108, v109, v110\n"
    "v_min_i32 v108, v108, v111\n"
    "v_cmp_gt_i32 vcc, s31, v108\n"
    "s_nop 0\n"
    "s_cbranch_vccz .Lb1_pd7_%=\n"
    "s_add_u32 s24, s24, 1\n"
    "s_cmp_lt_u32 s24, 0x40000\n"
    "s_cbranch_scc1 .Lb1_sp9_%=\n"
    "v_mov_b32 v108, 60\n"
    "s_mov_b32 s28, 33728\n"
    "buffer_store_dword v108, off, s[0:3], s28 sc1\n"
    "s_waitcnt vmcnt(0)\n"
    "s_branch .Lb1_pd7_%=\n"
    ".Lb1_sp9_%=:\n"
    "s_sleep 6\n"
    "s_branch .Lb1_po6_%=\n"
    ".Lb1_pd7_%=:\n"
    "s_barrier\n"
    "s_cmp_eq_u32 s31, 63\n"
    "s_cselect_b32 s36, s26, s29\n"
    ".Lb1_mk0x9_%=:\n"
    "s_mov_b32 s33, s32\n"
    "s_add_u32 m0, s34, 0\n"
    "s_nop 0\n"
    "buffer_load_dwordx4 v228, s[0:3], s32 offen sc1 lds\n"
    "buffer_load_dwordx4 v228, s[0:3], s32 offen offset:1024 sc1 lds\n"
    "s_add_u32 m0, s34, 8192\n"
    "s_add_u32 s32, s32, 0x4000\n"
    "buffer_load_dwordx4 v228, s[0:3], s32 offen sc1 lds\n"
    "buffer_load_dwordx4 v228, s[0:3], s32 offen offset:1024 sc1 lds\n"
    "s_add_u32 m0, s34, 16384\n"
    "s_add_u32 s32, s32, 0x4000\n"
    "buffer_load_dwordx4 v228, s[0:3], s32 offen sc1 lds\n"
    "buffer_load_dwordx4 v228, s[0:3], s32 offen offset:1024 sc1 lds\n"
    "s_add_u32 m0, s34, 24576\n"
    "s_add_u32 s32, s32, 0x4000\n"
    "buffer_load_dwordx4 v228, s[0:3], s32 offen sc1 lds\n"
    "buffer_load_dwordx4 v228, s[0:3], s32 offen offset:1024 sc1 lds\n"
    "s_add_u32 m0, s34, 32768\n"
    "s_add_u32 s32, s32, 0x4000\n"
    "buffer_load_dwordx4 v228, s[0:3], s32 offen sc1 lds\n"
    "buffer_load_dwordx4 v228, s[0:3], s32 offen offset:1024 sc1 lds\n"
    "s_waitcnt vmcnt(8)\n"
    "s_barrier\n"
    ".Lb1_mk1x10_%=:\n"
    "ds_read_b128 v[32:35], v224 offset:0\n"
    "ds_read_b128 v[36:39], v224 offset:1024\n"
    "ds_read_b128 v[40:43], v225 offset:0\n"
    "ds_read_b128 v[44:47], v225 offset:1024\n"
    "s_waitcnt lgkmcnt(2)\n"
    "v_mfma_f32_16x16x32_bf16 v[24:27], v[32:35], v[32:35], 0\n"
    "ds_read_b128 v[48:51], v226 offset:0\n"
    "ds_read_b128 v[52:55], v226 offset:1024\n"
    "v_mfma_f32_16x16x32_bf16 v[28:31], v[36:39], v[36:39], 0\n"
    "v_mfma_f32_16x16x32_bf16 v[0:3], a[0:3], v[32:35], 0\n"
    "v_mfma_f32_16x16x32_bf16 v[4:7], a[0:3], v[36:39], 0\n"
    "v_mfma_f32_16x16x32_bf16 v[8:11], a[80:83], v[32:35], 0\n"
    "s_add_u32 s35, s30, 1\n"
    "s_lshl_b32 s32, s35, 19\n"
    "s_lshl_b32 s29, s35, 11\n"
    "v_mfma_f32_16x16x32_bf16 v[12:15], a[80:83], v[36:39], 0\n"
    "s_xor_b32 s29, s29, s19\n"
    "s_and_b32 s29, s29, 0x2000\n"
    "s_add_u32 s32, s32, s29\n"
    "v_mfma_f32_16x16x32_bf16 v[16:19], a[160:163], v[32:35], 0\n"
    "s_add_u32 s32, s32, s20\n"
    "v_xor_b32 v232, 0x800, v232\n"
    "v_xor_b32 v233, 0x800, v233\n"
    "v_mfma_f32_16x16x32_bf16 v[20:23], a[160:163], v[36:39], 0\n"
    "s_waitcnt lgkmcnt(2)\n"
    "v_xor_b32 v235, 0x800, v235\n"
    "v_xor_b32 v236, 0x800, v236\n"
    "v_mfma_f32_16x16x32_bf16 v[0:3], a[4:7], v[40:43], v[0:3]\n"
    "ds_read_b128 v[56:59], v227 offset:0\n"
    "ds_read_b128 v[60:63], v227 offset:1024\n"
    "v_mfma_f32_16x16x32_bf16 v[4:7], a[4:7], v[44:47], v[4:7]\n"
    "v_xor_b32 v237, 0x800, v237\n"
    "v_xor_b32 v238, 0x800, v238\n"
    "v_mfma_f32_16x16x32_bf16 v[8:11], a[84:87], v[40:43], v[8:11]\n"
    "v_xor_b32 v239, 0x800, v239\n"
    "v_xor_b32 v240, 0x800, v240\n"
    "v_mfma_f32_16x16x32_bf16 v[12:15], a[84:87], v[44:47], v[12:15]\n"
    "v_mov_b32 v116, s31\n"
    "v_mov_b32 v117, s31\n"
    "v_mfma_f32_16x16x32_bf16 v[16:19], a[164:167], v[40:43], v[16:19]\n"
    "v_mov_b32 v118, s31\n"
    "v_mov_b32 v119, s31\n"
    "v_mfma_f32_16x16x32_bf16 v[20:23], a[164:167], v[44:47], v[20:23]\n"
    "s_waitcnt lgkmcnt(2)\n"
    "v_mfma_f32_16x16x32_bf16 v[0:3], a[8:11], v[48:51], v[0:3]\n"
    "s_waitcnt vmcnt(6)\n"
    "s_barrier\n"
    "ds_read_b128 v[64:67], v224 offset:8192\n"
    "v_mfma_f32_16x16x32_bf16 v[4:7], a[8:11], v[52:55], v[4:7]\n"
    "ds_read_b128 v[68:71], v224 offset:9216\n"
    "v_mfma_f32_16x16x32_bf16 v[8:11], a[88:91], v[48:51], v[8:11]\n"
    "v_mfma_f32_16x16x32_bf16 v[12:15], a[88:91], v[52:55], v[12:15]\n"
    "v_mfma_f32_16x16x32_bf16 v[16:19], a[168:171], v[48:51], v[16:19]\n"
    "v_mfma_f32_16x16x32_bf16 v[20:23], a[168:171], v[52:55], v[20:23]\n"
    "s_waitcnt lgkmcnt(2)\n"
    "v_mfma_f32_16x16x32_bf16 v[0:3], a[12:15], v[56:59], v[0:3]\n"
    "ds_read_b128 v[72:75], v225 offset:8192\n"
    "ds_read_b128 v[76:79], v225 offset:9216\n"
    "v_mfma_f32_16x16x32_bf16 v[4:7], a[12:15], v[60:63], v[4:7]\n"
    "v_mfma_f32_16x16x32_bf16 v[8:11], a[92:95], v[56:59], v[8:11]\n"
    "v_mfma_f32_16x16x32_bf16 v[12:15], a[92:95], v[60:63], v[12:15]\n"
    "v_mfma_f32_16x16x32_bf16 v[16:19], a[172:175], v[56:59], v[16:19]\n"
    "v_mfma_f32_16x16x32_bf16 v[20:23], a[172:175], v[60:63], v[20:23]\n"
    "s_waitcnt lgkmcnt(2)\n"
    "v_mfma_f32_16x16x32_bf16 v[24:27], v[64:67], v[64:67], v[24:27]\n"
    "ds_read_b128 v[80:83], v226 offset:8192\n"
    "ds_read_b128 v[84:87], v226 offset:9216\n"
    "v_mfma_f32_16x16x32_bf16 v[28:31], v[68:71], v[68:71], v[28:31]\n"
    "v_mfma_f32_16x16x32_bf16 v[0:3], a[16:19], v[64:67], v[0:3]\n"
    "v_mfma_f32_16x16x32_bf16 v[4:7], a[16:19], v[68:71], v[4:7]\n"
    "v_mfma_f32_16x16x32_bf16 v[8:11], a[96:99], v[64:67], v[8:11]\n"
    "v_mfma_f32_16x16x32_bf16 v[12:15], a[96:99], v[68:71], v[12:15]\n"
    "v_mfma_f32_16x16x32_bf16 v[16:19], a[176:179], v[64:67], v[16:19]\n"
    "v_mfma_f32_16x16x32_bf16 v[20:23], a[176:179], v[68:71], v[20:23]\n"
    "s_waitcnt lgkmcnt(2)\n"
    "v_mfma_f32_16x16x32_bf16 v[0:3], a[20:23], v[72:75], v[0:3]\n"
    "ds_read_b128 v[88:91], v227 offset:8192\n"
    "ds_read_b128 v[92:95], v227 offset:9216\n"
    "v_mfma_f32_16x16x32_bf16 v[4:7], a[20:23], v[76:79], v[4:7]\n"
    "v_mfma_f32_16x16x32_bf16 v[8:11], a[100:103], v[72:75], v[8:11]\n"
    "v_mfma_f32_16x16x32_bf16 v[12:15], a[100:103], v[76:79], v[12:15]\n"
    "v_mfma_f32_16x16x32_bf16 v[16:19], a[180:183], v[72:75], v[16:19]\n"
    "v_mfma_f32_16x16x32_bf16 v[20:23], a[180:183], v[76:79], v[20:23]\n"
    "s_waitcnt lgkmcnt(2)\n"
    "v_mfma_f32_16x16x32_bf16 v[0:3], a[24:27], v[80:83], v[0:3]\n"
    "s_waitcnt vmcnt(4)\n"
    "s_barrier\n"
    "ds_read_b128 v[32:35], v224 offset:16384\n"
    "v_mfma_f32_16x16x32_bf16 v[4:7], a[24:27], v[84:87], v[4:7]\n"
    "ds_read_b128 v[36:39], v224 offset:17408\n"
    "v_mfma_f32_16x16x32_bf16 v[8:11], a[104:107], v[80:83], v[8:11]\n"
    "v_mfma_f32_16x16x32_bf16 v[12:15], a[104:107], v[84:87], v[12:15]\n"
    "v_mfma_f32_16x16x32_bf16 v[16:19], a[184:187], v[80:83], v[16:19]\n"
    "v_mfma_f32_16x16x32_bf16 v[20:23], a[184:187], v[84:87], v[20:23]\n"
    "s_waitcnt lgkmcnt(2)\n"
    "v_mfma_f32_16x16x32_bf16 v[0:3], a[28:31], v[88:91], v[0:3]\n"
    "ds_read_b128 v[40:43], v225 offset:16384\n"
    "ds_read_b128 v[44:47], v225 offset:17408\n"
    "v_mfma_f32_16x16x32_bf16 v[4:7], a[28:31], v[92:95], v[4:7]\n"
    "v_mfma_f32_16x16x32_bf16 v[8:11], a[108:111], v[88:91], v[8:11]\n"
    "v_mfma_f32_16x16x32_bf16 v[12:15], a[108:111], v[92:95], v[12:15]\n"
    "v_mfma_f32_16x16x32_bf16 v[16:19], a[188:191], v[88:91], v[16:19]\n"
    "v_mfma_f32_16x16x32_bf16 v[20:23], a[188:191], v[92:95], v[20:23]\n"
    "s_waitcnt lgkmcnt(2)\n"
    "v_mfma_f32_16x16x32_bf16 v[24:27], v[32:35], v[32:35], v[24:27]\n"
    "ds_read_b128 v[48:51], v226 offset:16384\n"
    "ds_read_b128 v[52:55], v226 offset:17408\n"
    "v_mfma_f32_16x16x32_bf16 v[28:31], v[36:39], v[36:39], v[28:31]\n"
    "v_mfma_f32_16x16x32_bf16 v[0:3], a[32:35], v[32:35], v[0:3]\n"
    "v_mfma_f32_16x16x32_bf16 v[4:7], a[32:35], v[36:39], v[4:7]\n"
    "v_mfma_f32_16x16x32_bf16 v[8:11], a[112:115], v[32:35], v[8:11]\n"
    "v_mfma_f32_16x16x32_bf16 v[12:15], a[112:115], v[36:39], v[12:15]\n"
    "v_mfma_f32_16x16x32_bf16 v[16:19], a[192:195], v[32:35], v[16:19]\n"
    "v_mfma_f32_16x16x32_bf16 v[20:23], a[192:195], v[36:39], v[20:23]\n"
    "s_waitcnt lgkmcnt(2)\n"
    "v_mfma_f32_16x16x32_bf16 v[0:3], a[36:39], v[40:43], v[0:3]\n"
    "ds_read_b128 v[56:59], v227 offset:16384\n"
    "ds_read_b128 v[60:63], v227 offset:17408\n"
    "v_mfma_f32_16x16x32_bf16 v[4:7], a[36:39], v[44:47], v[4:7]\n"
    "v_mfma_f32_16x16x32_bf16 v[8:11], a[116:119], v[40:43], v[8:11]\n"
    "v_mfma_f32_16x16x32_bf16 v[12:15], a[116:119], v[44:47], v[12:15]\n"
    "v_mfma_f32_16x16x32_bf16 v[16:19], a[196:199], v[40:43], v[16:19]\n"
    "v_mfma_f32_16x16x32_bf16 v[20:23], a[196:199], v[44:47], v[20:23]\n"
    "s_waitcnt lgkmcnt(2)\n"
    "v_mfma_f32_16x16x32_bf16 v[0:3], a[40:43], v[48:51], v[0:3]\n"
    "s_waitcnt vmcnt(2)\n"
    "s_barrier\n"
    "ds_read_b128 v[64:67], v224 offset:24576\n"
    "v_mfma_f32_16x16x32_bf16 v[4:7], a[40:43], v[52:55], v[4:7]\n"
    "ds_read_b128 v[68:71], v224 offset:25600\n"
    "ds_read_b128 v[72:75], v225 offset:24576\n"
    "v_mfma_f32_16x16x32_bf16 v[8:11], a[120:123], v[48:51], v[8:11]\n"
    "v_mfma_f32_16x16x32_bf16 v[12:15], a[120:123], v[52:55], v[12:15]\n"
    "ds_read_b128 v[76:79], v225 offset:25600\n"
    "v_mfma_f32_16x16x32_bf16 v[16:19], a[200:203], v[48:51], v[16:19]\n"
    "ds_read_b128 v[80:83], v226 offset:24576\n"
    "ds_read_b128 v[84:87], v226 offset:25600\n"
    "v_mfma_f32_16x16x32_bf16 v[20:23], a[200:203], v[52:55], v[20:23]\n"
    "s_waitcnt lgkmcnt(6)\n"
    "v_mfma_f32_16x16x32_bf16 v[0:3], a[44:47], v[56:59], v[0:3]\n"
    "ds_read_b128 v[88:91], v227 offset:24576\n"
    "v_mfma_f32_16x16x32_bf16 v[4:7], a[44:47], v[60:63], v[4:7]\n"
    "ds_read_b128 v[92:95], v227 offset:25600\n"
    "s_waitcnt vmcnt(0)\n"
    "v_mfma_f32_16x16x32_bf16 v[8:11], a[124:127], v[56:59], v[8:11]\n"
    "s_barrier\n"
    "ds_read_b128 v[32:35], v224 offset:32768\n"
    "v_mfma_f32_16x16x32_bf16 v[12:15], a[124:127], v[60:63], v[12:15]\n"
    "ds_read_b128 v[36:39], v224 offset:33792\n"
    "v_mfma_f32_16x16x32_bf16 v[16:19], a[204:207], v[56:59], v[16:19]\n"
    "ds_read_b128 v[40:43], v225 offset:32768\n"
    "v_mfma_f32_16x16x32_bf16 v[20:23], a[204:207], v[60:63], v[20:23]\n"
    "s_waitcnt lgkmcnt(9)\n"
    "ds_read_b128 v[44:47], v225 offset:33792\n"
    "v_mfma_f32_16x16x32_bf16 v[24:27], v[64:67], v[64:67], v[24:27]\n"
    "ds_read_b128 v[48:51], v226 offset:32768\n"
    "v_mfma_f32_16x16x32_bf16 v[28:31], v[68:71], v[68:71], v[28:31]\n"
    "ds_read_b128 v[52:55], v226 offset:33792\n"
    "v_mfma_f32_16x16x32_bf16 v[0:3], a[48:51], v[64:67], v[0:3]\n"
    "ds_read_b128 v[56:59], v227 offset:32768\n"
    "v_mfma_f32_16x16x32_bf16 v[4:7], a[48:51], v[68:71], v[4:7]\n"
    "s_waitcnt lgkmcnt(11)\n"
    "ds_read_b128 v[60:63], v227 offset:33792\n"
    "v_mfma_f32_16x16x32_bf16 v[0:3], a[52:55], v[72:75], v[0:3]\n"
    "v_mfma_f32_16x16x32_bf16 v[4:7], a[52:55], v[76:79], v[4:7]\n"
    "s_waitcnt lgkmcnt(10)\n"
    "v_mfma_f32_16x16x32_bf16 v[0:3], a[56:59], v[80:83], v[0:3]\n"
    "v_mfma_f32_16x16x32_bf16 v[4:7], a[56:59], v[84:87], v[4:7]\n"
    "s_waitcnt lgkmcnt(8)\n"
    "v_mfma_f32_16x16x32_bf16 v[0:3], a[60:63], v[88:91], v[0:3]\n"
    "v_mfma_f32_16x16x32_bf16 v[4:7], a[60:63], v[92:95], v[4:7]\n"
    "s_waitcnt lgkmcnt(6)\n"
    "v_mfma_f32_16x16x32_bf16 v[24:27], v[32:35], v[32:35], v[24:27]\n"
    "v_mfma_f32_16x16x32_bf16 v[28:31], v[36:39], v[36:39], v[28:31]\n"
    "v_mfma_f32_16x16x32_bf16 v[0:3], a[64:67], v[32:35], v[0:3]\n"
    "v_mfma_f32_16x16x32_bf16 v[4:7], a[64:67], v[36:39], v[4:7]\n"
    "s_waitcnt lgkmcnt(4)\n"
    "v_mfma_f32_16x16x32_bf16 v[0:3], a[68:71], v[40:43], v[0:3]\n"
    "v_mfma_f32_16x16x32_bf16 v[4:7], a[68:71], v[44:47], v[4:7]\n"
    "s_waitcnt lgkmcnt(2)\n"
    "v_mfma_f32_16x16x32_bf16 v[0:3], a[72:75], v[48:51], v[0:3]\n"
    "v_mfma_f32_16x16x32_bf16 v[4:7], a[72:75], v[52:55], v[4:7]\n"
    "s_waitcnt lgkmcnt(0)\n"
    "v_mfma_f32_16x16x32_bf16 v[0:3], a[76:79], v[56:59], v[0:3]\n"
    "v_mfma_f32_16x16x32_bf16 v[4:7], a[76:79], v[60:63], v[4:7]\n"
    "ds_write_b128 v241, v[24:27]\n"
    "v_mfma_f32_16x16x32_bf16 v[8:11], a[128:131], v[64:67], v[8:11]\n"
    "ds_write_b128 v241, v[28:31] offset:1024\n"
    "v_mfma_f32_16x16x32_bf16 v[12:15], a[128:131], v[68:71], v[12:15]\n"
    "ds_write2st64_b32 v243, v0, v4 offset1:1\n"
    "v_mfma_f32_16x16x32_bf16 v[8:11], a[132:135], v[72:75], v[8:11]\n"
    "s_waitcnt lgkmcnt(0)\n"
    "s_barrier\n"
    "ds_read2st64_b32 v[24:25], v242 offset1:8\n"
    "v_mfma_f32_16x16x32_bf16 v[12:15], a[132:135], v[76:79], v[12:15]\n"
    "ds_read2st64_b32 v[26:27], v242 offset0:16 offset1:24\n"
    "ds_read_b32 v108, v244\n"
    "v_mfma_f32_16x16x32_bf16 v[8:11], a[136:139], v[80:83], v[8:11]\n"
    "v_mfma_f32_16x16x32_bf16 v[12:15], a[136:139], v[84:87], v[12:15]\n"
    "v_mfma_f32_16x16x32_bf16 v[8:11], a[140:143], v[88:91], v[8:11]\n"
    "v_mfma_f32_16x16x32_bf16 v[12:15], a[140:143], v[92:95], v[12:15]\n"
    "v_mfma_f32_16x16x32_bf16 v[8:11], a[144:147], v[32:35], v[8:11]\n"
    "s_waitcnt lgkmcnt(0)\n"
    "v_add_f32 v24, v24, v26\n"
    "v_add_f32 v25, v25, v27\n"
    "v_mfma_f32_16x16x32_bf16 v[12:15], a[144:147], v[36:39], v[12:15]\n"
    "v_add_f32 v112, v24, v25\n"
    "v_mul_f32 v108, 0x3acccccd, v108\n"
    "v_mfma_f32_16x16x32_bf16 v[8:11], a[148:151], v[40:43], v[8:11]\n"
    "v_mul_f32 v112, 0x3acccccd, v112\n"
    "v_fma_f32 v112, -v108, v108, v112\n"
    "v_mfma_f32_16x16x32_bf16 v[12:15], a[148:151], v[44:47], v[12:15]\n"
    "v_max_f32 v112, 0, v112\n"
    "v_cvt_pk_bf16_f32 v108, v108, v112\n"
    "v_mfma_f32_16x16x32_bf16 v[8:11], a[152:155], v[48:51], v[8:11]\n"
    "v_and_b32 v112, 0xffff0000, v108\n"
    "v_lshlrev_b32 v108, 16, v108\n"
    "v_mfma_f32_16x16x32_bf16 v[12:15], a[152:155], v[52:55], v[12:15]\n"
    "v_add_f32 v112, 0x3727c5ac, v112\n"
    "v_mov_b32 v110, v108\n"
    "v_mfma_f32_16x16x32_bf16 v[8:11], a[156:159], v[56:59], v[8:11]\n"
    "v_cvt_pk_bf16_f32 v112, v112, v112\n"
    "v_lshlrev_b32 v112, 16, v112\n"
    "v_mfma_f32_16x16x32_bf16 v[12:15], a[156:159], v[60:63], v[12:15]\n"
    "v_sqrt_f32 v112, v112\n"
    "v_mfma_f32_16x16x32_bf16 v[16:19], a[208:211], v[64:67], v[16:19]\n"
    "v_permlane32_swap_b32 v108, v110\n"
    "v_mfma_f32_16x16x32_bf16 v[20:23], a[208:211], v[68:71], v[20:23]\n"
    "v_cvt_pk_bf16_f32 v112, v112, v112\n"
    "v_lshlrev_b32 v112, 16, v112\n"
    "v_mfma_f32_16x16x32_bf16 v[16:19], a[212:215], v[72:75], v[16:19]\n"
    "v_rcp_f32 v112, v112\n"
    "v_mfma_f32_16x16x32_bf16 v[20:23], a[212:215], v[76:79], v[20:23]\n"
    "v_mov_b32 v114, v112\n"
    "v_mfma_f32_16x16x32_bf16 v[16:19], a[216:219], v[80:83], v[16:19]\n"
    "s_nop 0\n"
    "v_permlane32_swap_b32 v112, v114\n"
    "v_mfma_f32_16x16x32_bf16 v[20:23], a[216:219], v[84:87], v[20:23]\n"
    "v_fma_f32 v0, v96, -v108, v0\n"
    "v_fma_f32 v1, v97, -v108, v1\n"
    "v_mfma_f32_16x16x32_bf16 v[16:19], a[220:223], v[88:91], v[16:19]\n"
    "v_fma_f32 v2, v98, -v108, v2\n"
    "v_fma_f32 v3, v99, -v108, v3\n"
    "v_mfma_f32_16x16x32_bf16 v[20:23], a[220:223], v[92:95], v[20:23]\n"
    "v_fma_f32 v4, v96, -v110, v4\n"
    "v_fma_f32 v5, v97, -v110, v5\n"
    "v_mfma_f32_16x16x32_bf16 v[16:19], a[224:227], v[32:35], v[16:19]\n"
    "v_fma_f32 v6, v98, -v110, v6\n"
    "v_fma_f32 v7, v99, -v110, v7\n"
    "v_mfma_f32_16x16x32_bf16 v[20:23], a[224:227], v[36:39], v[20:23]\n"
    "v_mul_f32 v0, v0, v112\n"
    "v_mul_f32 v1, v1, v112\n"
    "v_mfma_f32_16x16x32_bf16 v[16:19], a[228:231], v[40:43], v[16:19]\n"
    "v_mul_f32 v2, v2, v112\n"
    "v_mul_f32 v3, v3, v112\n"
    "v_mfma_f32_16x16x32_bf16 v[20:23], a[228:231], v[44:47], v[20:23]\n"
    "v_mul_f32 v4, v4, v114\n"
    "v_mul_f32 v5, v5, v114\n"
    "v_mfma_f32_16x16x32_bf16 v[16:19], a[232:235], v[48:51], v[16:19]\n"
    "v_mul_f32 v6, v6, v114\n"
    "v_mul_f32 v7, v7, v114\n"
    "v_mfma_f32_16x16x32_bf16 v[20:23], a[232:235], v[52:55], v[20:23]\n"
    "v_cvt_pk_bf16_f32 v0, v0, v1\n"
    "v_cvt_pk_bf16_f32 v1, v2, v3\n"
    "v_mfma_f32_16x16x32_bf16 v[16:19], a[236:239], v[56:59], v[16:19]\n"
    "ds_write_b64 v231, v[0:1] offset:0\n"
    "v_mfma_f32_16x16x32_bf16 v[20:23], a[236:239], v[60:63], v[20:23]\n"
    "v_cvt_pk_bf16_f32 v4, v4, v5\n"
    "v_cvt_pk_bf16_f32 v5, v6, v7\n"
    ".Lb1_mk2x11_%=:\n"
    "ds_write_b64 v231, v[4:5] offset:1024\n"
    "v_pk_fma_f32 v[8:9], v[100:101], v[108:109], v[8:9] op_sel_hi:[1,0,1] neg_lo:[0,1,0] neg_hi:[0,1,0]\n"
    "v_pk_fma_f32 v[10:11], v[102:103], v[108:109], v[10:11] op_sel_hi:[1,0,1] neg_lo:[0,1,0] neg_hi:[0,1,0]\n"
    "v_pk_fma_f32 v[12:13], v[100:101], v[110:111], v[12:13] op_sel_hi:[1,0,1] neg_lo:[0,1,0] neg_hi:[0,1,0]\n"
    "v_pk_fma_f32 v[14:15], v[102:103], v[110:111], v[14:15] op_sel_hi:[1,0,1] neg_lo:[0,1,0] neg_hi:[0,1,0]\n"
    "v_pk_mul_f32 v[8:9], v[8:9], v[112:113] op_sel_hi:[1,0]\n"
    "v_pk_mul_f32 v[10:11], v[10:11], v[112:113] op_sel_hi:[1,0]\n"
    "v_pk_mul_f32 v[12:13], v[12:13], v[114:115] op_sel_hi:[1,0]\n"
    "v_pk_mul_f32 v[14:15], v[14:15], v[114:115] op_sel_hi:[1,0]\n"
    "v_cvt_pk_bf16_f32 v8, v8, v9\n"
    "v_cvt_pk_bf16_f32 v9, v10, v11\n"
    "ds_write_b64 v232, v[8:9] offset:0\n"
    "v_cvt_pk_bf16_f32 v12, v12, v13\n"
    "v_cvt_pk_bf16_f32 v13, v14, v15\n"
    "ds_write_b64 v232, v[12:13] offset:1024\n"
    "v_pk_fma_f32 v[16:17], v[104:105], v[108:109], v[16:17] op_sel_hi:[1,0,1] neg_lo:[0,1,0] neg_hi:[0,1,0]\n"
    "v_pk_fma_f32 v[18:19], v[106:107], v[108:109], v[18:19] op_sel_hi:[1,0,1] neg_lo:[0,1,0] neg_hi:[0,1,0]\n"
    "v_pk_fma_f32 v[20:21], v[104:105], v[110:111], v[20:21] op_sel_hi:[1,0,1] neg_lo:[0,1,0] neg_hi:[0,1,0]\n"
    "v_pk_fma_f32 v[22:23], v[106:107], v[110:111], v[22:23] op_sel_hi:[1,0,1] neg_lo:[0,1,0] neg_hi:[0,1,0]\n"
    "v_pk_mul_f32 v[16:17], v[16:17], v[112:113] op_sel_hi:[1,0]\n"
    "v_pk_mul_f32 v[18:19], v[18:19], v[112:113] op_sel_hi:[1,0]\n"
    "v_pk_mul_f32 v[20:21], v[20:21], v[114:115] op_sel_hi:[1,0]\n"
    "v_pk_mul_f32 v[22:23], v[22:23], v[114:115] op_sel_hi:[1,0]\n"
    "v_cvt_pk_bf16_f32 v16, v16, v17\n"
    "v_cvt_pk_bf16_f32 v17, v18, v19\n"
    "ds_write_b64 v233, v[16:17] offset:0\n"
    "v_cvt_pk_bf16_f32 v20, v20, v21\n"
    "v_cvt_pk_bf16_f32 v21, v22, v23\n"
    "ds_write_b64 v233, v[20:21] offset:1024\n"
    "s_waitcnt lgkmcnt(0)\n"
    "s_barrier\n"
    ".Lb1_mk3x12_%=:\n"
    "ds_read_b128 v[32:35], v236 offset:1024\n"
    "ds_read_b128 v[44:47], v234 offset:0\n"
    "ds_read_b128 v[36:39], v235 offset:0\n"
    "ds_read_b128 v[48:51], v234 offset:1024\n"
    "ds_read_b128 v[40:43], v235 offset:1024\n"
    "ds_read_b64_tr_b16 v[52:53], v238 offset:1024\n"
    "ds_read_b64_tr_b16 v[54:55], v237 offset:0\n"
    "ds_read_b64_tr_b16 v[56:57], v237 offset:1024\n"
    "ds_read_b64_tr_b16 v[58:59], v240 offset:1024\n"
    "ds_read_b64_tr_b16 v[60:61], v239 offset:0\n"
    "ds_read_b64_tr_b16 v[62:63], v239 offset:1024\n"
    "s_waitcnt lgkmcnt(9)\n"
    "v_mfma_f32_16x16x32_bf16 v[64:67], v[32:35], v[44:47], 0\n"
    "s_waitcnt lgkmcnt(8)\n"
    "v_mfma_f32_16x16x32_bf16 v[68:71], v[36:39], v[44:47], 0\n"
    "s_waitcnt lgkmcnt(7)\n"
    "v_mfma_f32_16x16x32_bf16 v[72:75], v[36:39], v[48:51], 0\n"
    "s_waitcnt lgkmcnt(6)\n"
    "v_mfma_f32_16x16x32_bf16 v[76:79], v[40:43], v[48:51], 0\n"
    "v_cndmask_b32 v64, v64, v65, s[42:43]\n"
    "v_cndmask_b32 v65, v66, v67, s[42:43]\n"
    "v_cndmask_b32 v66, v68, v69, s[42:43]\n"
    "v_cndmask_b32 v67, v70, v71, s[42:43]\n"
    "s_nop 0\n"
    "v_cndmask_b32 v68, v72, v73, s[42:43]\n"
    "v_cndmask_b32 v69, v74, v75, s[42:43]\n"
    "s_nop 0\n"
    "v_cndmask_b32 v70, v76, v77, s[42:43]\n"
    "v_cndmask_b32 v71, v78, v79, s[42:43]\n"
    "v_cndmask_b32 v64, v249, v64, s[38:39]\n"
    "v_cndmask_b32 v65, v249, v65, s[40:41]\n"
    "v_cndmask_b32 v66, v249, v66, s[12:13]\n"
    "v_cndmask_b32 v67, v249, v67, s[14:15]\n"
    "v_cndmask_b32 v68, v249, v68, s[8:9]\n"
    "v_cndmask_b32 v69, v249, v69, s[10:11]\n"
    "v_cndmask_b32 v70, v249, v70, s[12:13]\n"
    "v_cndmask_b32 v71, v249, v71, s[14:15]\n"
    "v_max3_f32 v72, v64, v65, v66\n"
    "v_max3_f32 v74, v68, v69, v70\n"
    "v_max_f32 v72, v72, v67\n"
    "v_max_f32 v74, v74, v71\n"
    "s_nop 1\n"
    "v_permlane16_swap_b32 v72, v74\n"
    "v_max_f32 v72, v72, v74\n"
    "v_mov_b32 v74, v72\n"
    "s_nop 1\n"
    "v_permlane32_swap_b32 v72, v74\n"
    "v_max_f32 v72, v72, v74\n"
    "v_mul_f32 v72, s37, v72\n"
    "v_mov_b32 v74, v72\n"
    "s_nop 1\n"
    "v_permlane16_swap_b32 v72, v74\n"
    "v_fma_f32 v64, v64, s37, -v72\n"
    "v_fma_f32 v65, v65, s37, -v72\n"
    "v_fma_f32 v66, v66, s37, -v72\n"
    "v_fma_f32 v67, v67, s37, -v72\n"
    "v_fma_f32 v68, v68, s37, -v74\n"
    "v_fma_f32 v69, v69, s37, -v74\n"
    "v_fma_f32 v70, v70, s37, -v74\n"
    "v_fma_f32 v71, v71, s37, -v74\n"
    "v_exp_f32 v64, v64\n"
    "v_exp_f32 v65, v65\n"
    "v_exp_f32 v66, v66\n"
    "v_exp_f32 v67, v67\n"
    "v_exp_f32 v68, v68\n"
    "v_exp_f32 v69, v69\n"
    "v_exp_f32 v70, v70\n"
    "v_exp_f32 v71, v71\n"
    "v_pk_add_f32 v[72:73], v[64:65], v[66:67]\n"
    "v_pk_add_f32 v[74:75], v[68:69], v[70:71]\n"
    "v_add_f32 v72, v72, v73\n"
    "v_add_f32 v74, v74, v75\n"
    "s_nop 1\n"
    "v_permlane16_swap_b32 v72, v74\n"
    "v_add_f32 v72, v72, v74\n"
    "v_mov_b32 v74, v72\n"
    "s_nop 1\n"
    "v_permlane32_swap_b32 v72, v74\n"
    "v_add_f32 v72, v72, v74\n"
    "v_rcp_f32 v72, v72\n"
    "s_nop 0\n"
    "v_mov_b32 v74, v72\n"
    "s_nop 1\n"
    "v_permlane16_swap_b32 v72, v74\n"
    "v_pk_mul_f32 v[64:65], v[64:65], v[72:73] op_sel_hi:[1,0]\n"
    "v_pk_mul_f32 v[66:67], v[66:67], v[72:73] op_sel_hi:[1,0]\n"
    "v_pk_mul_f32 v[68:69], v[68:69], v[74:75] op_sel_hi:[1,0]\n"
    "v_pk_mul_f32 v[70:71], v[70:71], v[74:75] op_sel_hi:[1,0]\n"
    "v_cvt_pk_bf16_f32 v64, v64, v65\n"
    "v_cvt_pk_bf16_f32 v66, v66, v67\n"
    "v_cvt_pk_bf16_f32 v68, v68, v69\n"
    "v_cvt_pk_bf16_f32 v70, v70, v71\n"
    "v_perm_b32 v80, v64, v64, v247\n"
    "v_perm_b32 v81, v64, v64, v248\n"
    "v_perm_b32 v82, v66, v66, v247\n"
    "v_perm_b32 v83, v66, v66, v248\n"
    "v_perm_b32 v84, v68, v68, v247\n"
    "v_perm_b32 v85, v68, v68, v248\n"
    "v_perm_b32 v86, v70, v70, v247\n"
    "v_perm_b32 v87, v70, v70, v248\n"
    "s_waitcnt lgkmcnt(0)\n"
    "v_mfma_f32_16x16x32_bf16 v[0:3], v[52:55], v[80:83], 0\n"
    "v_mfma_f32_16x16x32_bf16 v[4:7], v[54:57], v[84:87], 0\n"
    "v_mfma_f32_16x16x32_bf16 v[8:11], v[58:61], v[80:83], 0\n"
    "v_mfma_f32_16x16x32_bf16 v[12:15], v[60:63], v[84:87], 0\n"
    "s_mov_b64 s[38:39], s[8:9]\n"
    "s_mov_b64 s[40:41], s[10:11]\n"
    "v_cvt_pk_bf16_f32 v16, v0, v1\n"
    "v_cvt_pk_bf16_f32 v17, v2, v3\n"
    "v_cvt_pk_bf16_f32 v18, v4, v5\n"
    "v_cvt_pk_bf16_f32 v19, v6, v7\n"
    "s_nop 0\n"
    "v_permlane16_swap_b32 v16, v18\n"
    "v_permlane16_swap_b32 v17, v19\n"
    "buffer_store_dwordx4 v[16:19], v245, s[0:3], s33 offen sc1\n"
    "v_cvt_pk_bf16_f32 v20, v8, v9\n"
    "v_cvt_pk_bf16_f32 v21, v10, v11\n"
    "v_cvt_pk_bf16_f32 v22, v12, v13\n"
    "v_cvt_pk_bf16_f32 v23, v14, v15\n"
    "buffer_store_dwordx4 v[20:23], v246, s[0:3], s33 offen sc1\n"
    ".Lb1_mk4x13_%=:\n"
    "v_mov_b32 v108, 0\n"
    "s_memrealtime s[26:27]\n"
    "s_cmp_lg_u32 s18, 0\n"
    "s_waitcnt lgkmcnt(0)\n"
    "s_cbranch_scc1 .Lb1_pl15_%=\n"
    "s_sub_u32 s28, s36, s26\n"
    "s_cmp_gt_i32 s28, 40\n"
    "s_cbranch_scc1 .Lb1_pl15_%=\n"
    "buffer_load_dwordx4 v[108:111], v229, s[0:3], s21 offen sc1\n"
    ".Lb1_pl15_%=:\n"
    "s_waitcnt vmcnt(0)\n"
    "s_barrier\n"
    "buffer_store_dwordx4 v[116:119], v230, s[0:3], 0 offen sc1\n"
    ".Lb1_mk5x15_%=:\n"
    "s_mov_b32 s30, s31\n"
    "s_add_u32 s31, s31, 1\n"
    "s_cmp_lt_u32 s30, 64\n"
    "s_cbranch_scc1 .Lb1_top_%=\n"
    "s_waitcnt vmcnt(0) lgkmcnt(0)\n"
    "s_endpgm\n"
    ".Lb1_tk0_%=:\n"
    ".Lb1_po3_%=:\n"
    "buffer_load_dword v31, v30, s[0:3], 0 offen sc1\n"
    "s_waitcnt vmcnt(0)\n"
    "v_cmp_gt_i32 vcc, 32, v31\n"
    "s_nop 0\n"
    "s_cbranch_vccz .Lb1_pd4_%=\n"
    "s_add_u32 s24, s24, 1\n"
    "s_cmp_lt_u32 s24, 0x40000\n"
    "s_cbranch_scc1 .Lb1_sp5_%=\n"
    "v_mov_b32 v31, 90\n"
    "s_mov_b32 s28, 33728\n"
    "buffer_store_dword v31, off, s[0:3], s28 sc1\n"
    "s_waitcnt vmcnt(0)\n"
    "s_branch .Lb1_pd4_%=\n"
    ".Lb1_sp5_%=:\n"
    "s_sleep 8\n"
    "s_branch .Lb1_po3_%=\n"
    ".Lb1_pd4_%=:\n"
    "s_branch .Lb1_tk1_%=\n"
    :: "{s[0:3]}"(s0_), "{s[4:7]}"(s4_), "{s[8:11]}"(s8_), "{s[12:15]}"(s12_), "{s[16:19]}"(s16_), "{s[20:23]}"(s20_), "{v[0:3]}"(v0_), "{v[224:227]}"(v224_), "{v[228:231]}"(v228_), "{v[232:235]}"(v232_), "{v[236:239]}"(v236_), "{v[240:243]}"(v240_), "{v[244:247]}"(v244_), "{v[248:251]}"(v248_) : "memory", "v255", "a255", "s95");
    __builtin_unreachable();
}
DEV void b2_role(const MegaArgs& A, const int L, const int c, const int rep) {
    ROLE_HEAD;
    const int b = 4 * c + w;
    const u32x4 ro = L ? u32x4{lo(A.out), hi(A.out), 2048u * 1280u, 0x20000u} : rx;
    if (L == 0) {
#pragma unroll
        for (int k = 0; k < 16; ++k) bst16(rr, ST0 + b * 32768 + (k >> 3) * 16384 + (k & 7) * 1024 + l * 16, ones);
        DRAIN();
        wait_drawn(A.ar);
        DRAIN_BARRIER();
        bst4(rr, threadIdx.x == 0 ? xl(Z_ST + c * 128, 0) : OOB, 1u);
    }
    const u32x4 s0_ = {RFL(rx[0]), RFL(rx[1]), RFL(rx[2]), RFL(rx[3])};
    const u32x4 s4_ = {RFL(lo(A.wo + 32 * b)), RFL(hi(A.wo + 32 * b)), RFL((640 * 640 - 32 * b) * 2), RFL(0x20000u)};
    const u32x4 s8_ = {RFL(ro[0]), RFL(ro[1]), RFL(ro[2]), RFL(ro[3])};
    const u32x4 s12_ = {RFL(w), RFL(L << 13), RFL(REC0 + RC_O + w * 2048), RFL(xl(Z_O + rep * 2048, L))};
    const u32x4 s16_ = {RFL(xl(Z_ST, 0)), RFL(ST0), RFL(DBG0 + ((L * 128 + 107 + c) * 4 + w) * 2048), RFL(blockIdx.x == 0 ? 6 : 0)};
    const u32x4 v0_ = {(unsigned)((0 + (l >> 2)) * 1280 + (l & 3) * 16 - 0), (unsigned)((16 + (l >> 2)) * 1280 + (l & 3) * 16 - 1024), (unsigned)((32 + (l >> 2)) * 1280 + (l & 3) * 16 - 2048), (unsigned)((48 + (l >> 2)) * 1280 + (l & 3) * 16 - 3072)};
    const u32x4 v4_ = {(unsigned)(w * 40960 + (8 * (lq & 1) + 20 * (lq >> 1) + (l16 >> 2)) * 64 + (l & 3) * 16), (unsigned)(w * 40960 + (20 * lq + 16 + (l16 >> 2)) * 64 + (l & 3) * 16), (unsigned)((l < 5 ? l : 0) * 128), (unsigned)(0u)};
    const u32x4 v224_ = {(unsigned)(l * 16), (unsigned)((l < 11 ? l : 0) * 128), (unsigned)(l < 4 && w == 1 ? xl(Z_XN + l * 2048 + c * 128, 0) : OOB), (unsigned)(c * 16384 + l * 16)};
    const u32x4 v228_ = {(unsigned)(L ? (l16 + 0) * 1280 + b * 64 + lq * 16 : RC_X2 + c * 16384 + 0 + l * 16), (unsigned)(L ? (l16 + 16) * 1280 + b * 64 + lq * 16 : RC_X2 + c * 16384 + 1024 + l * 16), (unsigned)(l < 16 ? b * 32768 + l * 16 : OOB), (unsigned)((0 + lq) * 32768 + l16 * 16)};
    const u32x4 v232_ = {(unsigned)((4 + lq) * 32768 + l16 * 16), (unsigned)((8 + lq) * 32768 + l16 * 16), (unsigned)((12 + lq) * 32768 + l16 * 16), (unsigned)((16 + lq) * 32768 + l16 * 16)};
    asm volatile(
    "s_mov_b32 s20, 0\n"
    "s_mov_b64 s[22:23], 0\n"
    "s_mov_b32 s25, 0\n"
    "s_cmp_eq_u32 s13, 0\n"
    "s_cselect_b32 s24, 24, 60\n"
    "s_sub_u32 s24, s24, s19\n"
    ".Lb2_ho1_%=:\n"
    "s_sub_u32 s24, s24, 1\n"
    "s_cbranch_scc1 .Lb2_hd2_%=\n"
    "s_sleep 17\n"
    "s_branch .Lb2_ho1_%=\n"
    ".Lb2_hd2_%=:\n"
    "v_and_b32 v30, 0x70, v224\n"
    "v_lshlrev_b32 v30, 2, v30\n"
    "v_or_b32 v30, 0x8000, v30\n"
    "buffer_load_dword v31, v30, s[0:3], 0 offen sc1\n"
    "s_waitcnt vmcnt(0)\n"
    "v_cmp_gt_i32 vcc, 32, v31\n"
    "s_nop 0\n"
    "s_cbranch_vccnz .Lb2_tk0_%=\n"
    ".Lb2_tk1_%=:\n"
    "s_mul_i32 s28, s12, 40960\n"
    "s_mov_b32 s34, 0\n"
    "s_add_u32 m0, s28, 0\n"
    "s_nop 0\n"
    "buffer_load_dwordx4 v0, s[4:7], s34 offen offset:0 lds\n"
    "buffer_load_dwordx4 v1, s[4:7], s34 offen offset:1024 lds\n"
    "buffer_load_dwordx4 v2, s[4:7], s34 offen offset:2048 lds\n"
    "buffer_load_dwordx4 v3, s[4:7], s34 offen offset:3072 lds\n"
    "s_mov_b32 s34, 81920\n"
    "s_add_u32 m0, s28, 4096\n"
    "s_nop 0\n"
    "buffer_load_dwordx4 v0, s[4:7], s34 offen offset:0 lds\n"
    "buffer_load_dwordx4 v1, s[4:7], s34 offen offset:1024 lds\n"
    "buffer_load_dwordx4 v2, s[4:7], s34 offen offset:2048 lds\n"
    "buffer_load_dwordx4 v3, s[4:7], s34 offen offset:3072 lds\n"
    "s_mov_b32 s34, 163840\n"
    "s_add_u32 m0, s28, 8192\n"
    "s_nop 0\n"
    "buffer_load_dwordx4 v0, s[4:7], s34 offen offset:0 lds\n"
    "buffer_load_dwordx4 v1, s[4:7], s34 offen offset:1024 lds\n"
    "buffer_load_dwordx4 v2, s[4:7], s34 offen offset:2048 lds\n"
    "buffer_load_dwordx4 v3, s[4:7], s34 offen offset:3072 lds\n"
    "s_mov_b32 s34, 245760\n"
    "s_add_u32 m0, s28, 12288\n"
    "s_nop 0\n"
    "buffer_load_dwordx4 v0, s[4:7], s34 offen offset:0 lds\n"
    "buffer_load_dwordx4 v1, s[4:7], s34 offen offset:1024 lds\n"
    "buffer_load_dwordx4 v2, s[4:7], s34 offen offset:2048 lds\n"
    "buffer_load_dwordx4 v3, s[4:7], s34 offen offset:3072 lds\n"
    "s_mov_b32 s34, 327680\n"
    "s_add_u32 m0, s28, 16384\n"
    "s_nop 0\n"
    "buffer_load_dwordx4 v0, s[4:7], s34 offen offset:0 lds\n"
    "buffer_load_dwordx4 v1, s[4:7], s34 offen offset:1024 lds\n"
    "buffer_load_dwordx4 v2, s[4:7], s34 offen offset:2048 lds\n"
    "buffer_load_dwordx4 v3, s[4:7], s34 offen offset:3072 lds\n"
    "s_mov_b32 s34, 409600\n"
    "s_add_u32 m0, s28, 20480\n"
    "s_nop 0\n"
    "buffer_load_dwordx4 v0, s[4:7], s34 offen offset:0 lds\n"
    "buffer_load_dwordx4 v1, s[4:7], s34 offen offset:1024 lds\n"
    "buffer_load_dwordx4 v2, s[4:7], s34 offen offset:2048 lds\n"
    "buffer_load_dwordx4 v3, s[4:7], s34 offen offset:3072 lds\n"
    "s_mov_b32 s34, 491520\n"
    "s_add_u32 m0, s28, 24576\n"
    "s_nop 0\n"
    "buffer_load_dwordx4 v0, s[4:7], s34 offen offset:0 lds\n"
    "buffer_load_dwordx4 v1, s[4:7], s34 offen offset:1024 lds\n"
    "buffer_load_dwordx4 v2, s[4:7], s34 offen offset:2048 lds\n"
    "buffer_load_dwordx4 v3, s[4:7], s34 offen offset:3072 lds\n"
    "s_mov_b32 s34, 573440\n"
    "s_add_u32 m0, s28, 28672\n"
    "s_nop 0\n"
    "buffer_load_dwordx4 v0, s[4:7], s34 offen offset:0 lds\n"
    "buffer_load_dwordx4 v1, s[4:7], s34 offen offset:1024 lds\n"
    "buffer_load_dwordx4 v2, s[4:7], s34 offen offset:2048 lds\n"
    "buffer_load_dwordx4 v3, s[4:7], s34 offen offset:3072 lds\n"
    "s_mov_b32 s34, 655360\n"
    "s_add_u32 m0, s28, 32768\n"
    "s_nop 0\n"
    "buffer_load_dwordx4 v0, s[4:7], s34 offen offset:0 lds\n"
    "buffer_load_dwordx4 v1, s[4:7], s34 offen offset:1024 lds\n"
    "buffer_load_dwordx4 v2, s[4:7], s34 offen offset:2048 lds\n"
    "buffer_load_dwordx4 v3, s[4:7], s34 offen offset:3072 lds\n"
    "s_mov_b32 s34, 737280\n"
    "s_add_u32 m0, s28, 36864\n"
    "s_nop 0\n"
    "buffer_load_dwordx4 v0, s[4:7], s34 offen offset:0 lds\n"
    "buffer_load_dwordx4 v1, s[4:7], s34 offen offset:1024 lds\n"
    "buffer_load_dwordx4 v2, s[4:7], s34 offen offset:2048 lds\n"
    "buffer_load_dwordx4 v3, s[4:7], s34 offen offset:3072 lds\n"
    "s_waitcnt vmcnt(0)\n"
    "ds_read_b64_tr_b16 a[0:1], v4 offset:0\n"
    "ds_read_b64_tr_b16 a[2:3], v4 offset:256\n"
    "ds_read_b64_tr_b16 a[80:81], v4 offset:8\n"
    "ds_read_b64_tr_b16 a[82:83], v4 offset:264\n"
    "s_waitcnt lgkmcnt(0)\n"
    "ds_read_b64_tr_b16 a[4:5], v4 offset:2560\n"
    "ds_read_b64_tr_b16 a[6:7], v4 offset:2816\n"
    "ds_read_b64_tr_b16 a[84:85], v4 offset:2568\n"
    "ds_read_b64_tr_b16 a[86:87], v4 offset:2824\n"
    "s_waitcnt lgkmcnt(0)\n"
    "ds_read_b64_tr_b16 a[8:9], v4 offset:5120\n"
    "ds_read_b64_tr_b16 a[10:11], v4 offset:5376\n"
    "ds_read_b64_tr_b16 a[88:89], v4 offset:5128\n"
    "ds_read_b64_tr_b16 a[90:91], v4 offset:5384\n"
    "s_waitcnt lgkmcnt(0)\n"
    "ds_read_b64_tr_b16 a[12:13], v4 offset:7680\n"
    "ds_read_b64_tr_b16 a[14:15], v4 offset:7936\n"
    "ds_read_b64_tr_b16 a[92:93], v4 offset:7688\n"
    "ds_read_b64_tr_b16 a[94:95], v4 offset:7944\n"
    "s_waitcnt lgkmcnt(0)\n"
    "ds_read_b64_tr_b16 a[16:17], v4 offset:10240\n"
    "ds_read_b64_tr_b16 a[18:19], v4 offset:10496\n"
    "ds_read_b64_tr_b16 a[96:97], v4 offset:10248\n"
    "ds_read_b64_tr_b16 a[98:99], v4 offset:10504\n"
    "s_waitcnt lgkmcnt(0)\n"
    "ds_read_b64_tr_b16 a[20:21], v4 offset:12800\n"
    "ds_read_b64_tr_b16 a[22:23], v4 offset:13056\n"
    "ds_read_b64_tr_b16 a[100:101], v4 offset:12808\n"
    "ds_read_b64_tr_b16 a[102:103], v4 offset:13064\n"
    "s_waitcnt lgkmcnt(0)\n"
    "ds_read_b64_tr_b16 a[24:25], v4 offset:15360\n"
    "ds_read_b64_tr_b16 a[26:27], v4 offset:15616\n"
    "ds_read_b64_tr_b16 a[104:105], v4 offset:15368\n"
    "ds_read_b64_tr_b16 a[106:107], v4 offset:15624\n"
    "s_waitcnt lgkmcnt(0)\n"
    "ds_read_b64_tr_b16 a[28:29], v4 offset:17920\n"
    "ds_read_b64_tr_b16 a[30:31], v4 offset:18176\n"
    "ds_read_b64_tr_b16 a[108:109], v4 offset:17928\n"
    "ds_read_b64_tr_b16 a[110:111], v4 offset:18184\n"
    "s_waitcnt lgkmcnt(0)\n"
    "ds_read_b64_tr_b16 a[32:33], v4 offset:20480\n"
    "ds_read_b64_tr_b16 a[34:35], v4 offset:20736\n"
    "ds_read_b64_tr_b16 a[112:113], v4 offset:20488\n"
    "ds_read_b64_tr_b16 a[114:115], v4 offset:20744\n"
    "s_waitcnt lgkmcnt(0)\n"
    "ds_read_b64_tr_b16 a[36:37], v4 offset:23040\n"
    "ds_read_b64_tr_b16 a[38:39], v4 offset:23296\n"
    "ds_read_b64_tr_b16 a[116:117], v4 offset:23048\n"
    "ds_read_b64_tr_b16 a[118:119], v4 offset:23304\n"
    "s_waitcnt lgkmcnt(0)\n"
    "ds_read_b64_tr_b16 a[40:41], v4 offset:25600\n"
    "ds_read_b64_tr_b16 a[42:43], v4 offset:25856\n"
    "ds_read_b64_tr_b16 a[120:121], v4 offset:25608\n"
    "ds_read_b64_tr_b16 a[122:123], v4 offset:25864\n"
    "s_waitcnt lgkmcnt(0)\n"
    "ds_read_b64_tr_b16 a[44:45], v4 offset:28160\n"
    "ds_read_b64_tr_b16 a[46:47], v4 offset:28416\n"
    "ds_read_b64_tr_b16 a[124:125], v4 offset:28168\n"
    "ds_read_b64_tr_b16 a[126:127], v4 offset:28424\n"
    "s_waitcnt lgkmcnt(0)\n"
    "ds_read_b64_tr_b16 a[48:49], v4 offset:30720\n"
    "ds_read_b64_tr_b16 a[50:51], v4 offset:30976\n"
    "ds_read_b64_tr_b16 a[128:129], v4 offset:30728\n"
    "ds_read_b64_tr_b16 a[130:131], v4 offset:30984\n"
    "s_waitcnt lgkmcnt(0)\n"
    "ds_read_b64_tr_b16 a[52:53], v4 offset:33280\n"
    "ds_read_b64_tr_b16 a[54:55], v4 offset:33536\n"
    "ds_read_b64_tr_b16 a[132:133], v4 offset:33288\n"
    "ds_read_b64_tr_b16 a[134:135], v4 offset:33544\n"
    "s_waitcnt lgkmcnt(0)\n"
    "ds_read_b64_tr_b16 a[56:57], v4 offset:35840\n"
    "ds_read_b64_tr_b16 a[58:59], v4 offset:36096\n"
    "ds_read_b64_tr_b16 a[136:137], v4 offset:35848\n"
    "ds_read_b64_tr_b16 a[138:139], v4 offset:36104\n"
    "s_waitcnt lgkmcnt(0)\n"
    "ds_read_b64_tr_b16 a[60:61], v4 offset:38400\n"
    "ds_read_b64_tr_b16 a[62:63], v4 offset:38656\n"
    "ds_read_b64_tr_b16 a[140:141], v4 offset:38408\n"
    "ds_read_b64_tr_b16 a[142:143], v4 offset:38664\n"
    "s_waitcnt lgkmcnt(0)\n"
    "ds_read_b64_tr_b16 a[64:65], v5 offset:0\n"
    "ds_read_b64_tr_b16 a[66:67], v5 offset:5120\n"
    "ds_read_b64_tr_b16 a[144:145], v5 offset:8\n"
    "ds_read_b64_tr_b16 a[146:147], v5 offset:5128\n"
    "s_waitcnt lgkmcnt(0)\n"
    "ds_read_b64_tr_b16 a[68:69], v5 offset:10240\n"
    "ds_read_b64_tr_b16 a[70:71], v5 offset:15360\n"
    "ds_read_b64_tr_b16 a[148:149], v5 offset:10248\n"
    "ds_read_b64_tr_b16 a[150:151], v5 offset:15368\n"
    "s_waitcnt lgkmcnt(0)\n"
    "ds_read_b64_tr_b16 a[72:73], v5 offset:20480\n"
    "ds_read_b64_tr_b16 a[74:75], v5 offset:25600\n"
    "ds_read_b64_tr_b16 a[152:153], v5 offset:20488\n"
    "ds_read_b64_tr_b16 a[154:155], v5 offset:25608\n"
    "s_waitcnt lgkmcnt(0)\n"
    "ds_read_b64_tr_b16 a[76:77], v5 offset:30720\n"
    "ds_read_b64_tr_b16 a[78:79], v5 offset:35840\n"
    "ds_read_b64_tr_b16 a[156:157], v5 offset:30728\n"
    "ds_read_b64_tr_b16 a[158:159], v5 offset:35848\n"
    "s_waitcnt lgkmcnt(0)\n"
    "s_mov_b32 s26, -2\n"
    ".Lb2_po6_%=:\n"
    "buffer_load_dword v8, v6, s[0:3], s16 offen sc1\n"
    "s_waitcnt vmcnt(0)\n"
    "v_cmp_gt_i32 vcc, 1, v8\n"
    "s_nop 0\n"
    "s_cbranch_vccz .Lb2_pd7_%=\n"
    "s_add_u32 s20, s20, 1\n"
    "s_cmp_lt_u32 s20, 0x40000\n"
    "s_cbranch_scc1 .Lb2_sp8_%=\n"
    "v_mov_b32 v8, 70\n"
    "s_mov_b32 s24, 33728\n"
    "buffer_store_dword v8, off, s[0:3], s24 sc1\n"
    "s_waitcnt vmcnt(0)\n"
    "s_branch .Lb2_pd7_%=\n"
    ".Lb2_sp8_%=:\n"
    "s_sleep 4\n"
    "s_branch .Lb2_po6_%=\n"
    ".Lb2_pd7_%=:\n"
    "s_mov_b32 s26, 0\n"
    "s_lshl_b32 s33, s12, 11\n"
    "s_mov_b32 s35, 0\n"
    "v_mov_b32 v84, 0\n"
    ".Lb2_top_%=:\n"
    "s_lshl_b32 s28, s26, 19\n"
    "s_lshl_b32 s25, s26, 11\n"
    "s_xor_b32 s25, s25, s13\n"
    "s_and_b32 s25, s25, 0x2000\n"
    "s_add_u32 s28, s28, s25\n"
    "s_add_u32 s28, s28, s14\n"
    "s_add_u32 s27, s26, 1\n"
    "s_sub_u32 s29, s28, 81920\n"
    "s_sub_u32 s31, s28, 163840\n"
    "s_xor_b32 s31, s31, 0x2000\n"
    "s_mul_i32 s30, s26, 40960\n"
    "s_cmp_eq_u32 s13, 0\n"
    "s_cselect_b32 s30, s31, s30\n"
    "s_and_b32 s32, s26, 32\n"
    "s_add_u32 s32, s32, s26\n"
    "s_lshl_b32 s32, s32, 8\n"
    "s_add_u32 s32, s32, s17\n"
    "s_cmp_lg_u32 s12, 0\n"
    "s_cbranch_scc1 .Lb2_pd10_%=\n"
    "s_add_u32 s25, s22, 244\n"
    "v_min3_i32 v84, v84, v85, v86\n"
    "v_min_i32 v84, v84, v87\n"
    "v_cmp_gt_i32 vcc, s27, v84\n"
    "s_nop 0\n"
    "s_cbranch_vccz .Lb2_pd10_%=\n"
    ".Lb2_rt11_%=:\n"
    "s_memrealtime s[22:23]\n"
    "s_waitcnt lgkmcnt(0)\n"
    "s_sub_u32 s24, s35, s22\n"
    "s_cmp_lt_u32 s24, 600\n"
    "s_cbranch_scc1 .Lb2_rt11_%=\n"
    ".Lb2_po9_%=:\n"
    "buffer_load_dwordx4 v[84:87], v225, s[0:3], s15 offen sc1\n"
    "s_memrealtime s[22:23]\n"
    "s_waitcnt vmcnt(0) lgkmcnt(0)\n"
    "s_add_u32 s25, s22, 244\n"
    "v_min3_i32 v84, v84, v85, v86\n"
    "v_min_i32 v84, v84, v87\n"
    "v_cmp_gt_i32 vcc, s27, v84\n"
    "s_nop 0\n"
    "s_cbranch_vccz .Lb2_pd10_%=\n"
    "s_add_u32 s20, s20, 1\n"
    "s_cmp_lt_u32 s20, 0x40000\n"
    "s_cbranch_scc1 .Lb2_sp12_%=\n"
    "v_mov_b32 v84, 71\n"
    "s_mov_b32 s24, 33728\n"
    "buffer_store_dword v84, off, s[0:3], s24 sc1\n"
    "s_waitcnt vmcnt(0)\n"
    "s_branch .Lb2_pd10_%=\n"
    ".Lb2_sp12_%=:\n"
    "s_sleep 6\n"
    "s_branch .Lb2_po9_%=\n"
    ".Lb2_pd10_%=:\n"
    "s_barrier\n"
    "s_cmp_eq_u32 s27, 63\n"
    "s_cselect_b32 s35, s22, s25\n"
    ".Lb2_mk0x12_%=:\n"
    "s_add_u32 m0, s33, 0\n"
    "s_nop 0\n"
    "buffer_load_dwordx4 v224, s[0:3], s28 offen sc1 lds\n"
    "buffer_load_dwordx4 v224, s[0:3], s28 offen offset:1024 sc1 lds\n"
    "s_add_u32 m0, s33, 8192\n"
    "s_add_u32 s28, s28, 0x4000\n"
    "buffer_load_dwordx4 v224, s[0:3], s28 offen sc1 lds\n"
    "buffer_load_dwordx4 v224, s[0:3], s28 offen offset:1024 sc1 lds\n"
    "s_add_u32 m0, s33, 16384\n"
    "s_add_u32 s28, s28, 0x4000\n"
    "buffer_load_dwordx4 v224, s[0:3], s28 offen sc1 lds\n"
    "buffer_load_dwordx4 v224, s[0:3], s28 offen offset:1024 sc1 lds\n"
    "s_add_u32 m0, s33, 24576\n"
    "s_add_u32 s28, s28, 0x4000\n"
    "buffer_load_dwordx4 v224, s[0:3], s28 offen sc1 lds\n"
    "buffer_load_dwordx4 v224, s[0:3], s28 offen offset:1024 sc1 lds\n"
    "s_add_u32 m0, s33, 32768\n"
    "s_add_u32 s28, s28, 0x4000\n"
    "buffer_load_dwordx4 v224, s[0:3], s28 offen sc1 lds\n"
    "buffer_load_dwordx4 v224, s[0:3], s28 offen offset:1024 sc1 lds\n"
    "buffer_load_dwordx4 v[48:51], v227, s[0:3], s29 offen offset:0 sc1\n"
    "buffer_load_dwordx4 v[52:55], v227, s[0:3], s29 offen offset:1024 sc1\n"
    "s_waitcnt vmcnt(10)\n"
    "s_barrier\n"
    ".Lb2_mk1x13_%=:\n"
    "ds_read_b128 v[16:19], v224 offset:0\n"
    "ds_read_b128 v[20:23], v224 offset:1024\n"
    "ds_read_b128 v[24:27], v224 offset:2048\n"
    "ds_read_b128 v[28:31], v224 offset:3072\n"
    "ds_read_b128 v[32:35], v224 offset:4096\n"
    "ds_read_b128 v[36:39], v224 offset:5120\n"
    "s_waitcnt lgkmcnt(4)\n"
    "v_mfma_f32_16x16x32_bf16 v[0:3], a[0:3], v[16:19], 0\n"
    "ds_read_b128 v[40:43], v224 offset:6144\n"
    "ds_read_b128 v[44:47], v224 offset:7168\n"
    "v_mfma_f32_16x16x32_bf16 v[8:11], a[0:3], v[20:23], 0\n"
    "v_mfma_f32_16x16x32_bf16 v[4:7], a[80:83], v[16:19], 0\n"
    "v_mfma_f32_16x16x32_bf16 v[12:15], a[80:83], v[20:23], 0\n"
    "s_waitcnt lgkmcnt(4)\n"
    "v_mfma_f32_16x16x32_bf16 v[0:3], a[4:7], v[24:27], v[0:3]\n"
    "s_waitcnt vmcnt(8)\n"
    "s_barrier\n"
    "ds_read_b128 v[16:19], v224 offset:8192\n"
    "v_mfma_f32_16x16x32_bf16 v[8:11], a[4:7], v[28:31], v[8:11]\n"
    "ds_read_b128 v[20:23], v224 offset:9216\n"
    "v_mfma_f32_16x16x32_bf16 v[4:7], a[84:87], v[24:27], v[4:7]\n"
    "v_mfma_f32_16x16x32_bf16 v[12:15], a[84:87], v[28:31], v[12:15]\n"
    "s_waitcnt lgkmcnt(4)\n"
    "v_mfma_f32_16x16x32_bf16 v[0:3], a[8:11], v[32:35], v[0:3]\n"
    "ds_read_b128 v[24:27], v224 offset:10240\n"
    "ds_read_b128 v[28:31], v224 offset:11264\n"
    "v_mfma_f32_16x16x32_bf16 v[8:11], a[8:11], v[36:39], v[8:11]\n"
    "v_mfma_f32_16x16x32_bf16 v[4:7], a[88:91], v[32:35], v[4:7]\n"
    "v_mfma_f32_16x16x32_bf16 v[12:15], a[88:91], v[36:39], v[12:15]\n"
    "s_waitcnt lgkmcnt(4)\n"
    "v_mfma_f32_16x16x32_bf16 v[0:3], a[12:15], v[40:43], v[0:3]\n"
    "ds_read_b128 v[32:35], v224 offset:12288\n"
    "ds_read_b128 v[36:39], v224 offset:13312\n"
    "v_mfma_f32_16x16x32_bf16 v[8:11], a[12:15], v[44:47], v[8:11]\n"
    "v_mfma_f32_16x16x32_bf16 v[4:7], a[92:95], v[40:43], v[4:7]\n"
    "v_mfma_f32_16x16x32_bf16 v[12:15], a[92:95], v[44:47], v[12:15]\n"
    "s_waitcnt lgkmcnt(4)\n"
    "v_mfma_f32_16x16x32_bf16 v[0:3], a[16:19], v[16:19], v[0:3]\n"
    "ds_read_b128 v[40:43], v224 offset:14336\n"
    "ds_read_b128 v[44:47], v224 offset:15360\n"
    "v_mfma_f32_16x16x32_bf16 v[8:11], a[16:19], v[20:23], v[8:11]\n"
    "v_mfma_f32_16x16x32_bf16 v[4:7], a[96:99], v[16:19], v[4:7]\n"
    "v_mfma_f32_16x16x32_bf16 v[12:15], a[96:99], v[20:23], v[12:15]\n"
    "s_waitcnt lgkmcnt(4)\n"
    "v_mfma_f32_16x16x32_bf16 v[0:3], a[20:23], v[24:27], v[0:3]\n"
    "s_waitcnt vmcnt(6)\n"
    "s_barrier\n"
    "ds_read_b128 v[16:19], v224 offset:16384\n"
    "v_mfma_f32_16x16x32_bf16 v[8:11], a[20:23], v[28:31], v[8:11]\n"
    "ds_read_b128 v[20:23], v224 offset:17408\n"
    "v_mfma_f32_16x16x32_bf16 v[4:7], a[100:103], v[24:27], v[4:7]\n"
    "v_mfma_f32_16x16x32_bf16 v[12:15], a[100:103], v[28:31], v[12:15]\n"
    "s_waitcnt lgkmcnt(4)\n"
    "v_mfma_f32_16x16x32_bf16 v[0:3], a[24:27], v[32:35], v[0:3]\n"
    "ds_read_b128 v[24:27], v224 offset:18432\n"
    "ds_read_b128 v[28:31], v224 offset:19456\n"
    "v_mfma_f32_16x16x32_bf16 v[8:11], a[24:27], v[36:39], v[8:11]\n"
    "v_mfma_f32_16x16x32_bf16 v[4:7], a[104:107], v[32:35], v[4:7]\n"
    "v_mfma_f32_16x16x32_bf16 v[12:15], a[104:107], v[36:39], v[12:15]\n"
    "s_waitcnt lgkmcnt(4)\n"
    "v_mfma_f32_16x16x32_bf16 v[0:3], a[28:31], v[40:43], v[0:3]\n"
    "ds_read_b128 v[32:35], v224 offset:20480\n"
    "ds_read_b128 v[36:39], v224 offset:21504\n"
    "v_mfma_f32_16x16x32_bf16 v[8:11], a[28:31], v[44:47], v[8:11]\n"
    "v_mfma_f32_16x16x32_bf16 v[4:7], a[108:111], v[40:43], v[4:7]\n"
    "v_mfma_f32_16x16x32_bf16 v[12:15], a[108:111], v[44:47], v[12:15]\n"
    "s_waitcnt lgkmcnt(4)\n"
    "v_mfma_f32_16x16x32_bf16 v[0:3], a[32:35], v[16:19], v[0:3]\n"
    "ds_read_b128 v[40:43], v224 offset:22528\n"
    "ds_read_b128 v[44:47], v224 offset:23552\n"
    "v_mfma_f32_16x16x32_bf16 v[8:11], a[32:35], v[20:23], v[8:11]\n"
    "v_mfma_f32_16x16x32_bf16 v[4:7], a[112:115], v[16:19], v[4:7]\n"
    "v_mfma_f32_16x16x32_bf16 v[12:15], a[112:115], v[20:23], v[12:15]\n"
    "s_waitcnt lgkmcnt(4)\n"
    "v_mfma_f32_16x16x32_bf16 v[0:3], a[36:39], v[24:27], v[0:3]\n"
    "s_waitcnt vmcnt(4)\n"
    "s_barrier\n"
    "ds_read_b128 v[16:19], v224 offset:24576\n"
    "v_mfma_f32_16x16x32_bf16 v[8:11], a[36:39], v[28:31], v[8:11]\n"
    "ds_read_b128 v[20:23], v224 offset:25600\n"
    "v_mfma_f32_16x16x32_bf16 v[4:7], a[116:119], v[24:27], v[4:7]\n"
    "v_mfma_f32_16x16x32_bf16 v[12:15], a[116:119], v[28:31], v[12:15]\n"
    "s_waitcnt lgkmcnt(4)\n"
    "v_mfma_f32_16x16x32_bf16 v[0:3], a[40:43], v[32:35], v[0:3]\n"
    "ds_read_b128 v[24:27], v224 offset:26624\n"
    "ds_read_b128 v[28:31], v224 offset:27648\n"
    "v_mfma_f32_16x16x32_bf16 v[8:11], a[40:43], v[36:39], v[8:11]\n"
    "v_mfma_f32_16x16x32_bf16 v[4:7], a[120:123], v[32:35], v[4:7]\n"
    "v_mfma_f32_16x16x32_bf16 v[12:15], a[120:123], v[36:39], v[12:15]\n"
    "s_waitcnt lgkmcnt(4)\n"
    "v_mfma_f32_16x16x32_bf16 v[0:3], a[44:47], v[40:43], v[0:3]\n"
    "ds_read_b128 v[32:35], v224 offset:28672\n"
    "ds_read_b128 v[36:39], v224 offset:29696\n"
    "v_mfma_f32_16x16x32_bf16 v[8:11], a[44:47], v[44:47], v[8:11]\n"
    "v_mfma_f32_16x16x32_bf16 v[4:7], a[124:127], v[40:43], v[4:7]\n"
    "v_mfma_f32_16x16x32_bf16 v[12:15], a[124:127], v[44:47], v[12:15]\n"
    "s_waitcnt lgkmcnt(4)\n"
    "v_mfma_f32_16x16x32_bf16 v[0:3], a[48:51], v[16:19], v[0:3]\n"
    "ds_read_b128 v[40:43], v224 offset:30720\n"
    "ds_read_b128 v[44:47], v224 offset:31744\n"
    "v_mfma_f32_16x16x32_bf16 v[8:11], a[48:51], v[20:23], v[8:11]\n"
    "v_mfma_f32_16x16x32_bf16 v[4:7], a[128:131], v[16:19], v[4:7]\n"
    "v_mfma_f32_16x16x32_bf16 v[12:15], a[128:131], v[20:23], v[12:15]\n"
    "s_waitcnt lgkmcnt(4)\n"
    "v_mfma_f32_16x16x32_bf16 v[0:3], a[52:55], v[24:27], v[0:3]\n"
    "s_waitcnt vmcnt(2)\n"
    "s_barrier\n"
    "ds_read_b128 v[16:19], v224 offset:32768\n"
    "v_mfma_f32_16x16x32_bf16 v[8:11], a[52:55], v[28:31], v[8:11]\n"
    "ds_read_b128 v[20:23], v224 offset:33792\n"
    "v_mfma_f32_16x16x32_bf16 v[4:7], a[132:135], v[24:27], v[4:7]\n"
    "v_mfma_f32_16x16x32_bf16 v[12:15], a[132:135], v[28:31], v[12:15]\n"
    "s_waitcnt lgkmcnt(4)\n"
    "v_mfma_f32_16x16x32_bf16 v[0:3], a[56:59], v[32:35], v[0:3]\n"
    "ds_read_b128 v[24:27], v224 offset:34816\n"
    "ds_read_b128 v[28:31], v224 offset:35840\n"
    "v_mfma_f32_16x16x32_bf16 v[8:11], a[56:59], v[36:39], v[8:11]\n"
    "v_mfma_f32_16x16x32_bf16 v[4:7], a[136:139], v[32:35], v[4:7]\n"
    "s_waitcnt vmcnt(0)\n"
    "v_lshlrev_b32 v96, 16, v48\n"
    "v_and_b32 v97, 0xffff0000, v48\n"
    "v_mfma_f32_16x16x32_bf16 v[12:15], a[136:139], v[36:39], v[12:15]\n"
    "s_waitcnt lgkmcnt(4)\n"
    "v_lshlrev_b32 v98, 16, v49\n"
    "v_and_b32 v99, 0xffff0000, v49\n"
    "v_mfma_f32_16x16x32_bf16 v[0:3], a[60:63], v[40:43], v[0:3]\n"
    "s_waitcnt lgkmcnt(2)\n"
    "ds_read_b128 v[32:35], v224 offset:36864\n"
    "ds_read_b128 v[36:39], v224 offset:37888\n"
    "v_mfma_f32_16x16x32_bf16 v[8:11], a[60:63], v[44:47], v[8:11]\n"
    "v_swap_b32 v18, v20\n"
    "v_mfma_f32_16x16x32_bf16 v[4:7], a[140:143], v[40:43], v[4:7]\n"
    "v_swap_b32 v19, v21\n"
    "v_mfma_f32_16x16x32_bf16 v[12:15], a[140:143], v[44:47], v[12:15]\n"
    "v_lshlrev_b32 v100, 16, v50\n"
    "v_and_b32 v101, 0xffff0000, v50\n"
    "v_mfma_f32_16x16x32_bf16 v[0:3], a[64:67], v[16:19], v[0:3]\n"
    "s_waitcnt lgkmcnt(2)\n"
    "ds_read_b128 v[40:43], v224 offset:38912\n"
    "ds_read_b128 v[44:47], v224 offset:39936\n"
    "v_mfma_f32_16x16x32_bf16 v[8:11], a[64:67], v[20:23], v[8:11]\n"
    "v_swap_b32 v26, v28\n"
    "v_mfma_f32_16x16x32_bf16 v[4:7], a[144:147], v[16:19], v[4:7]\n"
    "v_swap_b32 v27, v29\n"
    "v_mfma_f32_16x16x32_bf16 v[12:15], a[144:147], v[20:23], v[12:15]\n"
    "v_lshlrev_b32 v102, 16, v51\n"
    "v_and_b32 v103, 0xffff0000, v51\n"
    "v_mfma_f32_16x16x32_bf16 v[0:3], a[68:71], v[24:27], v[0:3]\n"
    "s_waitcnt lgkmcnt(2)\n"
    "v_lshlrev_b32 v104, 16, v52\n"
    "v_and_b32 v105, 0xffff0000, v52\n"
    "v_mfma_f32_16x16x32_bf16 v[8:11], a[68:71], v[28:31], v[8:11]\n"
    "v_swap_b32 v34, v36\n"
    "v_mfma_f32_16x16x32_bf16 v[4:7], a[148:151], v[24:27], v[4:7]\n"
    "v_swap_b32 v35, v37\n"
    "v_mfma_f32_16x16x32_bf16 v[12:15], a[148:151], v[28:31], v[12:15]\n"
    "v_lshlrev_b32 v106, 16, v53\n"
    "v_and_b32 v107, 0xffff0000, v53\n"
    "v_mfma_f32_16x16x32_bf16 v[0:3], a[72:75], v[32:35], v[0:3]\n"
    "s_waitcnt lgkmcnt(0)\n"
    "v_lshlrev_b32 v108, 16, v54\n"
    "v_and_b32 v109, 0xffff0000, v54\n"
    "v_mfma_f32_16x16x32_bf16 v[8:11], a[72:75], v[36:39], v[8:11]\n"
    "v_swap_b32 v42, v44\n"
    "v_mfma_f32_16x16x32_bf16 v[4:7], a[152:155], v[32:35], v[4:7]\n"
    "v_swap_b32 v43, v45\n"
    "v_mfma_f32_16x16x32_bf16 v[12:15], a[152:155], v[36:39], v[12:15]\n"
    "v_lshlrev_b32 v110, 16, v55\n"
    "v_and_b32 v111, 0xffff0000, v55\n"
    "v_mfma_f32_16x16x32_bf16 v[0:3], a[76:79], v[40:43], v[0:3]\n"
    "v_mfma_f32_16x16x32_bf16 v[8:11], a[76:79], v[44:47], v[8:11]\n"
    "v_mfma_f32_16x16x32_bf16 v[4:7], a[156:159], v[40:43], v[4:7]\n"
    "v_mfma_f32_16x16x32_bf16 v[12:15], a[156:159], v[44:47], v[12:15]\n"
    ".Lb2_mk2x14_%=:\n"
    "v_pk_add_f32 v[0:1], v[0:1], v[96:97]\n"
    "v_pk_add_f32 v[2:3], v[2:3], v[98:99]\n"
    "v_pk_add_f32 v[8:9], v[8:9], v[104:105]\n"
    "v_pk_add_f32 v[10:11], v[10:11], v[106:107]\n"
    "s_nop 0\n"
    "v_pk_add_f32 v[4:5], v[4:5], v[100:101]\n"
    "v_pk_add_f32 v[6:7], v[6:7], v[102:103]\n"
    "s_nop 0\n"
    "v_pk_add_f32 v[12:13], v[12:13], v[108:109]\n"
    "v_pk_add_f32 v[14:15], v[14:15], v[110:111]\n"
    "s_cmp_lg_u32 s13, 0\n"
    "s_cbranch_scc1 .Lb2_out_%=\n"
    "v_pk_add_f32 v[64:65], v[0:1], v[2:3]\n"
    "v_pk_add_f32 v[68:69], v[8:9], v[10:11]\n"
    "v_pk_mul_f32 v[66:67], v[0:1], v[0:1]\n"
    "v_pk_mul_f32 v[70:71], v[8:9], v[8:9]\n"
    "v_pk_add_f32 v[96:97], v[4:5], v[6:7]\n"
    "v_pk_add_f32 v[98:99], v[12:13], v[14:15]\n"
    "v_pk_fma_f32 v[66:67], v[2:3], v[2:3], v[66:67]\n"
    "v_pk_fma_f32 v[70:71], v[10:11], v[10:11], v[70:71]\n"
    "v_pk_add_f32 v[64:65], v[64:65], v[96:97]\n"
    "v_pk_add_f32 v[68:69], v[68:69], v[98:99]\n"
    "v_pk_fma_f32 v[66:67], v[4:5], v[4:5], v[66:67]\n"
    "v_pk_fma_f32 v[70:71], v[12:13], v[12:13], v[70:71]\n"
    "v_add_f32 v64, v64, v65\n"
    "v_add_f32 v68, v68, v69\n"
    "v_pk_fma_f32 v[66:67], v[6:7], v[6:7], v[66:67]\n"
    "v_pk_fma_f32 v[70:71], v[14:15], v[14:15], v[70:71]\n"
    "v_add_f32 v66, v66, v67\n"
    "v_add_f32 v70, v70, v71\n"
    "s_nop 0\n"
    "v_permlane16_swap_b32 v64, v66\n"
    "v_permlane16_swap_b32 v68, v70\n"
    "v_add_f32 v64, v64, v66\n"
    "v_add_f32 v68, v68, v70\n"
    "s_nop 1\n"
    "v_permlane32_swap_b32 v64, v68\n"
    "v_add_f32 v64, v64, v68\n"
    "v_min_u32 v64, -2, v64\n"
    "v_mov_b32 v65, v64\n"
    "v_cvt_pk_bf16_f32 v56, v0, v1\n"
    "v_cvt_pk_bf16_f32 v57, v2, v3\n"
    "v_permlane16_swap_b32 v64, v65\n"
    "v_mov_b32 v66, v64\n"
    "v_mov_b32 v67, v65\n"
    "v_cvt_pk_bf16_f32 v58, v4, v5\n"
    "v_cvt_pk_bf16_f32 v59, v6, v7\n"
    "v_permlane32_swap_b32 v64, v66\n"
    "v_permlane32_swap_b32 v65, v67\n"
    "buffer_store_dwordx4 v[64:67], v230, s[0:3], s32 offen sc1\n"
    "v_cvt_pk_bf16_f32 v60, v8, v9\n"
    "v_cvt_pk_bf16_f32 v61, v10, v11\n"
    "v_cvt_pk_bf16_f32 v62, v12, v13\n"
    "v_cvt_pk_bf16_f32 v63, v14, v15\n"
    ".Lb2_mk3x15_%=:\n"
    "v_lshlrev_b32 v0, 16, v56\n"
    "v_and_b32 v1, 0xffff0000, v56\n"
    "v_lshlrev_b32 v2, 16, v57\n"
    "v_and_b32 v3, 0xffff0000, v57\n"
    "v_lshlrev_b32 v4, 16, v58\n"
    "v_and_b32 v5, 0xffff0000, v58\n"
    "v_lshlrev_b32 v6, 16, v59\n"
    "v_and_b32 v7, 0xffff0000, v59\n"
    "v_lshlrev_b32 v8, 16, v60\n"
    "v_and_b32 v9, 0xffff0000, v60\n"
    "v_lshlrev_b32 v10, 16, v61\n"
    "v_and_b32 v11, 0xffff0000, v61\n"
    "v_lshlrev_b32 v12, 16, v62\n"
    "v_and_b32 v13, 0xffff0000, v62\n"
    "v_lshlrev_b32 v14, 16, v63\n"
    "v_and_b32 v15, 0xffff0000, v63\n"
    "s_sleep 6\n"
    "buffer_load_dwordx4 v[64:67], v231, s[0:3], s32 offen sc1\n"
    "buffer_load_dwordx4 v[68:71], v232, s[0:3], s32 offen sc1\n"
    "buffer_load_dwordx4 v[72:75], v233, s[0:3], s32 offen sc1\n"
    "buffer_load_dwordx4 v[76:79], v234, s[0:3], s32 offen sc1\n"
    "buffer_load_dwordx4 v[80:83], v235, s[0:3], s32 offen sc1\n"
    "buffer_store_dwordx4 v[56:59], v228, s[8:11], s30 offen sc1\n"
    "buffer_store_dwordx4 v[60:63], v229, s[8:11], s30 offen sc1\n"
    ".Lb2_sl_%=:\n"
    "s_waitcnt vmcnt(6)\n"
    "v_max_u32 v84, v64, v64\n"
    "s_waitcnt vmcnt(5)\n"
    "v_max_u32 v84, v68, v84\n"
    "v_pk_add_f32 v[64:65], v[64:65], v[68:69]\n"
    "v_pk_add_f32 v[66:67], v[66:67], v[70:71]\n"
    "s_waitcnt vmcnt(4)\n"
    "v_max_u32 v84, v72, v84\n"
    "v_pk_add_f32 v[64:65], v[64:65], v[72:73]\n"
    "v_pk_add_f32 v[66:67], v[66:67], v[74:75]\n"
    "s_waitcnt vmcnt(3)\n"
    "v_max_u32 v84, v76, v84\n"
    "v_pk_add_f32 v[64:65], v[64:65], v[76:77]\n"
    "v_pk_add_f32 v[66:67], v[66:67], v[78:79]\n"
    "s_waitcnt vmcnt(2)\n"
    "v_max_u32 v84, v80, v84\n"
    "v_pk_add_f32 v[64:65], v[64:65], v[80:81]\n"
    "v_pk_add_f32 v[66:67], v[66:67], v[82:83]\n"
    "v_cmp_eq_u32 vcc, -1, v84\n"
    "s_nop 0\n"
    "s_cbranch_vccnz .Lb2_sr_%=\n"
    ".Lb2_sg_%=:\n"
    ".Lb2_mk4x16_%=:\n"
    "v_permlane16_swap_b32 v64, v65\n"
    "v_permlane16_swap_b32 v66, v67\n"
    "v_add_f32 v64, v64, v65\n"
    "v_add_f32 v66, v66, v67\n"
    "s_nop 1\n"
    "v_permlane32_swap_b32 v64, v66\n"
    "v_add_f32 v64, v64, v66\n"
    "v_mov_b32 v66, v64\n"
    "s_nop 1\n"
    "v_permlane16_swap_b32 v64, v66\n"
    "v_mul_f32 v64, 0x3acccccd, v64\n"
    "v_mul_f32 v66, 0x3acccccd, v66\n"
    "v_fma_f32 v66, -v64, v64, v66\n"
    "v_max_f32 v66, 0, v66\n"
    "v_cvt_pk_bf16_f32 v64, v64, v66\n"
    "v_and_b32 v66, 0xffff0000, v64\n"
    "v_lshlrev_b32 v64, 16, v64\n"
    "v_add_f32 v66, 0x3727c5ac, v66\n"
    "v_mov_b32 v68, v64\n"
    "v_cvt_pk_bf16_f32 v66, v66, v66\n"
    "v_lshlrev_b32 v66, 16, v66\n"
    "v_sqrt_f32 v66, v66\n"
    "v_permlane32_swap_b32 v64, v68\n"
    "v_cvt_pk_bf16_f32 v66, v66, v66\n"
    "v_lshlrev_b32 v66, 16, v66\n"
    "v_rcp_f32 v66, v66\n"
    "s_nop 0\n"
    "v_mov_b32 v70, v66\n"
    "s_nop 1\n"
    "v_permlane32_swap_b32 v66, v70\n"
    "v_mul_f32 v64, v64, v66\n"
    "v_mul_f32 v68, v68, v70\n"
    "v_pk_fma_f32 v[0:1], v[0:1], v[66:67], v[64:65] op_sel_hi:[1,0,0] neg_lo:[0,0,1] neg_hi:[0,0,1]\n"
    "v_pk_fma_f32 v[2:3], v[2:3], v[66:67], v[64:65] op_sel_hi:[1,0,0] neg_lo:[0,0,1] neg_hi:[0,0,1]\n"
    "v_pk_fma_f32 v[4:5], v[4:5], v[66:67], v[64:65] op_sel_hi:[1,0,0] neg_lo:[0,0,1] neg_hi:[0,0,1]\n"
    "v_pk_fma_f32 v[6:7], v[6:7], v[66:67], v[64:65] op_sel_hi:[1,0,0] neg_lo:[0,0,1] neg_hi:[0,0,1]\n"
    "v_cvt_pk_bf16_f32 v96, v0, v1\n"
    "v_cvt_pk_bf16_f32 v98, v2, v3\n"
    "v_cvt_pk_bf16_f32 v97, v4, v5\n"
    "v_cvt_pk_bf16_f32 v99, v6, v7\n"
    "buffer_store_dwordx4 v[96:99], v227, s[0:3], s31 offen offset:0 sc1\n"
    "v_pk_fma_f32 v[8:9], v[8:9], v[70:71], v[68:69] op_sel_hi:[1,0,0] neg_lo:[0,0,1] neg_hi:[0,0,1]\n"
    "v_pk_fma_f32 v[10:11], v[10:11], v[70:71], v[68:69] op_sel_hi:[1,0,0] neg_lo:[0,0,1] neg_hi:[0,0,1]\n"
    "v_pk_fma_f32 v[12:13], v[12:13], v[70:71], v[68:69] op_sel_hi:[1,0,0] neg_lo:[0,0,1] neg_hi:[0,0,1]\n"
    "v_pk_fma_f32 v[14:15], v[14:15], v[70:71], v[68:69] op_sel_hi:[1,0,0] neg_lo:[0,0,1] neg_hi:[0,0,1]\n"
    "v_cvt_pk_bf16_f32 v100, v8, v9\n"
    "v_cvt_pk_bf16_f32 v102, v10, v11\n"
    "v_cvt_pk_bf16_f32 v101, v12, v13\n"
    "v_cvt_pk_bf16_f32 v103, v14, v15\n"
    "buffer_store_dwordx4 v[100:103], v227, s[0:3], s31 offen offset:1024 sc1\n"
    "v_mov_b32 v88, s27\n"
    "v_mov_b32 v89, s27\n"
    "v_mov_b32 v90, s27\n"
    "v_mov_b32 v91, s27\n"
    ".Lb2_mk5x17_%=:\n"
    "v_mov_b32 v84, 0\n"
    "s_memrealtime s[22:23]\n"
    "s_cmp_lg_u32 s12, 0\n"
    "s_waitcnt lgkmcnt(0)\n"
    "s_cbranch_scc1 .Lb2_pl19_%=\n"
    "s_sub_u32 s24, s35, s22\n"
    "s_cmp_gt_i32 s24, 40\n"
    "s_cbranch_scc1 .Lb2_pl19_%=\n"
    "buffer_load_dwordx4 v[84:87], v225, s[0:3], s15 offen sc1\n"
    ".Lb2_pl19_%=:\n"
    "s_waitcnt vmcnt(0)\n"
    "s_barrier\n"
    "buffer_store_dwordx4 v[88:91], v226, s[0:3], 0 offen sc1\n"
    ".Lb2_mk6x19_%=:\n"
    ".Lb2_nx_%=:\n"
    "s_mov_b32 s26, s27\n"
    "s_cmp_lt_u32 s26, 64\n"
    "s_cbranch_scc1 .Lb2_top_%=\n"
    "s_waitcnt vmcnt(0) lgkmcnt(0)\n"
    "s_endpgm\n"
    ".Lb2_tk0_%=:\n"
    ".Lb2_po3_%=:\n"
    "buffer_load_dword v31, v30, s[0:3], 0 offen sc1\n"
    "s_waitcnt vmcnt(0)\n"
    "v_cmp_gt_i32 vcc, 32, v31\n"
    "s_nop 0\n"
    "s_cbranch_vccz .Lb2_pd4_%=\n"
    "s_add_u32 s20, s20, 1\n"
    "s_cmp_lt_u32 s20, 0x40000\n"
    "s_cbranch_scc1 .Lb2_sp5_%=\n"
    "v_mov_b32 v31, 90\n"
    "s_mov_b32 s24, 33728\n"
    "buffer_store_dword v31, off, s[0:3], s24 sc1\n"
    "s_waitcnt vmcnt(0)\n"
    "s_branch .Lb2_pd4_%=\n"
    ".Lb2_sp5_%=:\n"
    "s_sleep 8\n"
    "s_branch .Lb2_po3_%=\n"
    ".Lb2_pd4_%=:\n"
    "s_branch .Lb2_tk1_%=\n"
    ".Lb2_out_%=:\n"
    "v_cvt_pk_bf16_f32 v56, v0, v1\n"
    "v_cvt_pk_bf16_f32 v57, v2, v3\n"
    "v_cvt_pk_bf16_f32 v58, v4, v5\n"
    "v_cvt_pk_bf16_f32 v59, v6, v7\n"
    "v_cvt_pk_bf16_f32 v60, v8, v9\n"
    "v_cvt_pk_bf16_f32 v61, v10, v11\n"
    "v_cvt_pk_bf16_f32 v62, v12, v13\n"
    "v_cvt_pk_bf16_f32 v63, v14, v15\n"
    "buffer_store_dwordx4 v[56:59], v228, s[8:11], s30 offen\n"
    "buffer_store_dwordx4 v[60:63], v229, s[8:11], s30 offen\n"
    "v_mov_b32 v84, 0\n"
    "s_branch .Lb2_nx_%=\n"
    ".Lb2_sr_%=:\n"
    "s_add_u32 s20, s20, 1\n"
    "s_cmp_lt_u32 s20, 0x40000\n"
    "s_cbranch_scc1 .Lb2_sp21_%=\n"
    "v_mov_b32 v84, 72\n"
    "s_mov_b32 s24, 33728\n"
    "buffer_store_dword v84, off, s[0:3], s24 sc1\n"
    "s_waitcnt vmcnt(0)\n"
    "s_branch .Lb2_sg_%=\n"
    ".Lb2_sp21_%=:\n"
    "s_sleep 2\n"
    "buffer_load_dwordx4 v[64:67], v231, s[0:3], s32 offen sc1\n"
    "buffer_load_dwordx4 v[68:71], v232, s[0:3], s32 offen sc1\n"
    "buffer_load_dwordx4 v[72:75], v233, s[0:3], s32 offen sc1\n"
    "buffer_load_dwordx4 v[76:79], v234, s[0:3], s32 offen sc1\n"
    "buffer_load_dwordx4 v[80:83], v235, s[0:3], s32 offen sc1\n"
    "s_waitcnt vmcnt(0)\n"
    "s_branch .Lb2_sl_%=\n"
    :: "{s[0:3]}"(s0_), "{s[4:7]}"(s4_), "{s[8:11]}"(s8_), "{s[12:15]}"(s12_), "{s[16:19]}"(s16_), "{v[0:3]}"(v0_), "{v[4:7]}"(v4_), "{v[224:227]}"(v224_), "{v[228:231]}"(v228_), "{v[232:235]}"(v232_) : "memory", "v255", "a255", "s95");
    __builtin_unreachable();
}
#define SWP 112 - ((k - 24) >> 2) * 16
#define BOX A.ar - 262144
#define NP 4
constexpr int MEGA_LDS = 163840;
__global__ void __launch_bounds__(256, 1) mega_kernel(MegaArgs A) {
    extern __shared__ __attribute__((aligned(16))) char smem[];
    int* sRole = (int*)smem;
    asm volatile("" :: "s"(A.x0), "s"(A.mem0), "s"(A.Wq), "s"(A.Wk), "s"(A.Wv), "s"(A.Wo), "s"(A.wqkv), "s"(A.wo));
    const int x4 = (int)(__builtin_amdgcn_s_getreg((3 << 11) | 20) & 3u);
    const rsrc_t rf = mk_rsrc(arena_base(A.ar), ARENA);
    unsigned* const cnt = (unsigned*)(arena_base(A.ar) + xl(Z_TK, 0));
    u64* const box = (u64*)(BOX);
    const int ln = threadIdx.x & 63;
    static_assert(2 * (Z_END - Z_TK) == 256 * 640, "");
    if (threadIdx.x < 64) {
        bst16(rf, ln < 40 ? 2 * Z_TK + blockIdx.x * 640 + ln * 16 : OOB, u32x4{0u, 0u, 0u, 0u});
        int want;
        if (blockIdx.x == 0) {
            DRAIN();
            u64 sent[4];
            for (int n = 0;; ++n) {
                u64 v[4];
#pragma unroll
                for (int k = 0; k < 4; ++k) v[k] = __hip_atomic_load(box + 8 * (((64 * k + ln) & 31) * 8 + ((64 * k + ln) >> 5)), RLX_AGENT);
                const unsigned c = n < 2 ? 0u : __hip_atomic_load(cnt + 16 * (ln & 7), RLX_AGENT);
                const unsigned open = (unsigned)__ballot(c < 32u) & 255u;
                want = __ffs(open) - 1;
                if ((open & (open - 1)) == 0 && __all(c >= 31u)) break;
#pragma unroll
                for (int k = 0; k < 4; ++k) { if (n == 0 || v[k] != sent[k]) __hip_atomic_store(box + 2048 + 64 * k + ln, v[k], RLX_AGENT); sent[k] = v[k]; }
                if (n > 0) __builtin_amdgcn_s_sleep(16);
            }
        } else {
            const u64 mine = __builtin_amdgcn_s_memrealtime();
            if (threadIdx.x == 0) __hip_atomic_store(box + 8 * blockIdx.x, mine, RLX_AGENT);
            unsigned tm[2] = {~0u, ~0u};
            u64* const in = box + 2048 + (blockIdx.x & 7) * 32 + (blockIdx.x >> 3);
            u64 back;
            const unsigned pr = (blockIdx.x >> 7) * 16384 + (blockIdx.x & 127) * 64;
            if (threadIdx.x < 2) bst4(rf, pr + threadIdx.x * 8192, 0u);
            DRAIN();
#pragma unroll
            for (int k = 0; k < NP; ++k) {
                const unsigned long long t0 = __builtin_amdgcn_s_memtime();
                if (threadIdx.x == 0) bst4(rf, pr + (k & 1) * 8192, 0u);
                if (k < NP - 1) DRAIN();
                else asm volatile("global_load_dwordx2 %0, %1, %2 sc1\n\ts_waitcnt vmcnt(1)" : "=&v"(back) : "v"(0u), "s"(in) : "memory");
                tm[k & 1] = min(tm[k & 1], (unsigned)(__builtin_amdgcn_s_memtime() - t0));
            }
            asm volatile("s_waitcnt vmcnt(0)" : "+v"(back) :: "memory");
            while (back != mine) { __builtin_amdgcn_s_sleep(2); back = __hip_atomic_load(in, RLX_AGENT); }
            want = (tm[1] < tm[0] ? 4 : 0) + x4;
        }
        asm volatile("" ::: "memory");
        if (threadIdx.x == 0) {
            int role = -1;
            for (int p = -1; role < 0; ++p) {
                const int pool = p < 0 ? want : p, x = pool & 3;
                const int k = (int)__hip_atomic_fetch_add(cnt + 16 * pool, 1u, RLX_AGENT);
                if (k < 32) role = (pool >> 2) * 128 + (k < 24 ? (k >> 3) * 32 + 8 * x + (k & 7) : SWP + 4 * x + (k & 3));
            }
            *sRole = role;
        }
    }
    __syncthreads();
    const int role = RFL(*sRole), L = role >> 7, r = role & 127;
    __syncthreads();
    const bool on = (A.layers >> L) & 1;
    if (r < 32) { if (on) qk_role(A, L, r, x4); }
    else if (r < 64) { if (on) vo_role(A, L, r - 32, x4); }
    else if (r < 96) ct_role(A, L, r - 64, smem);
    else if (r < 112) { if (on) { if (r < 107) b1_role(A, L, r - 96, x4); else b2_role(A, L, r - 107, x4); } }
    else qw_role(A, L, 8 * ((r - 112) >> 2) + 4 * L + (r & 3), x4);
}

#define LAYERS 3
extern "C" void kernel_launch(void* const* d_in, const int* in_sizes, int n_in, void* d_out, int out_size, void* d_ws, size_t ws_size, hipStream_t stream) {
    MegaArgs A;
    A.x0 = (const bf16_t*)d_in[0]; A.mem0 = (const bf16_t*)d_in[1]; A.Wq = (const bf16_t*)d_in[2]; A.Wk = (const bf16_t*)d_in[3]; A.Wv = (const bf16_t*)d_in[4];
    A.Wo = (const bf16_t*)d_in[5]; A.wqkv = (const bf16_t*)d_in[8]; A.wo = (const bf16_t*)d_in[9];
    A.ar = (char*)d_ws + 266240;
    A.out = (bf16_t*)d_out; A.layers = LAYERS; A.pad = 0;
    (void)hipFuncSetAttribute((const void*)mega_kernel, hipFuncAttributeMaxDynamicSharedMemorySize, MEGA_LDS);
    mega_kernel<<<256, 256, MEGA_LDS, stream>>>(A);
}
```

```cpp
#include <hip/hip_runtime.h>
#include <hip/hip_bf16.h>
#include <stdint.h>

typedef unsigned short bf16_t;
typedef short bf16x8 __attribute__((ext_vector_type(8)));
typedef short bf16x4 __attribute__((ext_vector_type(4)));
typedef float f32x4 __attribute__((ext_vector_type(4)));
typedef unsigned u32x4 __attribute__((ext_vector_type(4)));
typedef unsigned u32x2 __attribute__((ext_vector_type(2)));
typedef unsigned long long u64;
typedef __bf16 bf16v2 __attribute__((ext_vector_type(2)));
typedef float f32v2 __attribute__((ext_vector_type(2)));
#define DEV __device__ __forceinline__
DEV unsigned pk2(float a, float b) { f32v2 f = {a, b}; bf16v2 r = __builtin_convertvector(f, bf16v2); return __builtin_bit_cast(unsigned, r); }
DEV float rb(float a) { return __uint_as_float((pk2(a, 0.f) & 0xffffu) << 16); }
DEV float lo16(unsigned u) { return __uint_as_float(u << 16); }
DEV float hi16(unsigned u) { return __uint_as_float(u & 0xffff0000u); }
constexpr int D_ = 640;
#define RLX_AGENT __ATOMIC_RELAXED, __HIP_MEMORY_SCOPE_AGENT
#define AUX_SC1 16
typedef __amdgpu_buffer_rsrc_t rsrc_t;
DEV rsrc_t mk_rsrc(const void* p, unsigned bytes) { return __builtin_amdgcn_make_buffer_rsrc((void*)p, 0, (int)bytes, 0x00020000); }
DEV void bst16(rsrc_t r, unsigned off, u32x4 v) { __builtin_amdgcn_raw_buffer_store_b128(v, r, off, 0, AUX_SC1); }
DEV void bst8(rsrc_t r, unsigned off, u32x2 v) { __builtin_amdgcn_raw_buffer_store_b64(v, r, off, 0, AUX_SC1); }
DEV void bst4(rsrc_t r, unsigned off, unsigned v) { __builtin_amdgcn_raw_buffer_store_b32(v, r, off, 0, AUX_SC1); }
#define DRAIN() asm volatile("s_waitcnt vmcnt(0)" ::: "memory")
#define DRAIN_BARRIER() asm volatile("s_waitcnt vmcnt(0) lgkmcnt(0)\n\ts_barrier" ::: "memory")
#define RFL(x) ((unsigned)__builtin_amdgcn_readfirstlane((int)(x)))
DEV unsigned lo(const void* p) { return (unsigned)(size_t)p; }
DEV unsigned hi(const void* p) { return (unsigned)((size_t)p >> 32) & 0xffffu; }
DEV unsigned nosent(unsigned x) { return min(x, 0xfffffffeu); }
DEV int sw2(int x) { return ((x & 1) << 1) | (x >> 1); }

DEV unsigned xl(unsigned o, int side) { const unsigned ph = ((o >> 13) << 14) | (o & 8191u); return ph | ((((ph >> 21) ^ side) & 1u) << 13); }
constexpr unsigned Z_TK = 16384, Z_QV = Z_TK + 1024, Z_CT = Z_QV + 1024, Z_QW = Z_CT + 2048, Z_X = 3 * 8192, Z_X1 = 7 * 8192, Z_XN = 9 * 8192, Z_O = 10 * 8192, Z_ST = 11 * 8192, Z_END = 12 * 8192;
constexpr unsigned RG_UT = 1u << 21, RG_UTX = RG_UT + (1u << 20), RG_UM = RG_UTX + 262144, RG_UMX = RG_UM + 262144, RG_PT = RG_UMX + 65536, RG_PM = RG_PT + 65536, RG_MEM = RG_PM + 32768;
constexpr unsigned SLOT = 1u << 22;
constexpr unsigned REC0 = 20u << 20, RC_X1 = 5 * 16384, RC_O = 10 * 16384, RC_X2 = 15 * 16384, ST0 = 1u << 20, QT0 = 52u << 20, DBG0 = 116u << 20, ARENA = 120u << 20;
DEV unsigned rec(int s, int L) { return REC0 + (s << 19) + ((((s >> 2) ^ L) & 1) << 13); }
constexpr unsigned OOB = 0x7f000000u, WBYTES = 640u * 6144u * 2u;
DEV char* arena_base(char* p) { return p + ((0 - (size_t)p) & 0x3fffff); }

struct MegaArgs {
    const bf16_t* x0; const bf16_t* mem0; const bf16_t* Wq; const bf16_t* Wk; const bf16_t* Wv; const bf16_t* Wo; const bf16_t* wqkv; const bf16_t* wo;
    char* ar; bf16_t* out; int layers, pad;
};
DEV bool all_drawn(char* ar) { return __all(__hip_atomic_load((unsigned*)(arena_base(ar) + xl(Z_TK, 0)) + 16 * (threadIdx.x & 7), RLX_AGENT) >= 32u); }
DEV void wait_drawn(char* ar) { while (!all_drawn(ar)) __builtin_amdgcn_s_sleep(8); asm volatile("" ::: "memory"); }
#define ROLE_HEAD const int l = threadIdx.x & 63, l16 = l & 15, lq = l >> 4, w = RFL(threadIdx.x >> 6); char* const ab = arena_base(A.ar); \
    const rsrc_t rr = mk_rsrc(ab, ARENA); const u32x4 rx = {lo(ab), hi(ab), ARENA, 0x20000u}; const u32x4 ones = {~0u, ~0u, ~0u, ~0u}; const u32x2 ones2 = {~0u, ~0u}
DEV void qk_role(const MegaArgs& A, const int L, const int h, const int rep) {
    ROLE_HEAD;
#define XP 10
    const int i = 4 * (w & 1) + lq, p = l16 < 8 ? l16 >> 2 : l16 & 1, rho = 4 * (i >> 1) + 2 * p + (i & 1), tp = 4 * w + lq;
    const unsigned pm = (w >= 2 || l16 >= XP) ? OOB : xl(RG_PM + (h >> 3) * 8192 +
        (l16 < 8 ? ((h & 7) >> 1) * 1024 + ((2 * (h & 1) + ((l16 >> 1) & 1)) * 16 + rho) * 16 + (l16 & 1) * 8 : 4096 + (((h & 7) >> 1) * 16 + rho) * 16 + (h & 1) * 8), L);
    const unsigned pt = xl(RG_PT + h * 2048 + tp * 16 + (l16 < 8 ? (l16 & 3) * 256 + (l16 >> 2) * 8 : (l16 & 1) * 8 + (l16 < 10 ? 1792 : (l16 < 14 ? 1024 + ((l16 - 10) >> 1) * 256 : 1536))), L);
    u64 cm[4];
    const int k0 = (l16 >= 4 && l16 < 8) ? 4 * (l16 - 4) : ((l16 >= 10 && l16 < 14) ? 4 * (l16 - 10) : -4);
#pragma unroll
    for (int r = 0; r < 4; ++r) cm[r] = __ballot(k0 + r <= tp);
    const u32x4 s0_ = {RFL(rx[0]), RFL(rx[1]), RFL(rx[2]), RFL(rx[3])};
    const u32x4 s4_ = {RFL(lo(A.Wk + h * 192)), RFL(hi(A.Wk + h * 192)), RFL(WBYTES - h * 384), RFL(0x20000u)};
    const u32x4 s8_ = {RFL(lo(A.Wq + h * 192)), RFL(hi(A.Wq + h * 192)), RFL(WBYTES - h * 384), RFL(0x20000u)};
    const u32x4 s12_ = {RFL((unsigned)cm[0]), RFL((unsigned)(cm[0] >> 32)), RFL((unsigned)cm[1]), RFL((unsigned)(cm[1] >> 32))};
    const u32x4 s16_ = {RFL((unsigned)cm[2]), RFL((unsigned)(cm[2] >> 32)), RFL((unsigned)cm[3]), RFL((unsigned)(cm[3] >> 32))};
    const u32x4 s20_ = {RFL(w < 2 ? ~0u : 1u), RFL(w < 2 ? ~0u : 0u), RFL(w), RFL(L << 13)};
    const u32x4 s24_ = {RFL(xl(RG_MEM + rep * 16384, L) + w * 2048), RFL(w < 2 ? xl(RG_MEM + rep * 16384 + 8192, L) + w * 1024 - 2048 : OOB), RFL(w * 11264 + 8192), RFL(REC0 + w * 16384)};
    const u32x4 s28_ = {RFL(65536 - 4096 - w * 14336), RFL(QT0 + h * 32768 + w * 2048), RFL(h == 0 && w == 0 ? xl(Z_X, L) : OOB), RFL(xl(Z_X, L))};
    const u32x4 s32_ = {RFL(xl(Z_CT + (h & 7) * 256, L)), RFL(xl(Z_XN + rep * 2048, 0)), RFL(xl(Z_QW + h * 128, L)), RFL(w == 0 ? xl(Z_QV + h * 4, L) : OOB)};
    const u32x4 s36_ = {RFL(DBG0 + ((L * 128 + h) * 4 + w) * 2048), RFL(0u), RFL(0u), RFL(0u)};
    const u32x4 v0_ = {(unsigned)(((3 * w + 0) * 64 + l) / 24 * 12288 + ((((3 * w + 0) * 64 + l) % 24) ^ ((((3 * w + 0) * 64 + l) / 48) & 6)) * 16 - 0), (unsigned)(((3 * w + 1) * 64 + l) / 24 * 12288 + ((((3 * w + 1) * 64 + l) % 24) ^ ((((3 * w + 1) * 64 + l) / 48) & 6)) * 16 - 1024), (unsigned)(((3 * w + 2) * 64 + l) / 24 * 12288 + ((((3 * w + 2) * 64 + l) % 24) ^ ((((3 * w + 2) * 64 + l) / 48) & 6)) * 16 - 2048), (unsigned)((8 * lq + ((l >> 2) & 1) + ((l >> 1) & 4)) * 384 + (w ^ (((8 * lq + ((l >> 2) & 1) + ((l >> 1) & 4)) >> 2) & 3)) * 32 + (l & 3) * 8 + ((w + 0) & 3) * 12288)};
    const u32x4 v4_ = {(unsigned)((8 * lq + ((l >> 2) & 1) + ((l >> 1) & 4)) * 384 + (w ^ (((8 * lq + ((l >> 2) & 1) + ((l >> 1) & 4)) >> 2) & 3)) * 32 + (l & 3) * 8 + ((w + 1) & 3) * 12288), (unsigned)((8 * lq + ((l >> 2) & 1) + ((l >> 1) & 4)) * 384 + (w ^ (((8 * lq + ((l >> 2) & 1) + ((l >> 1) & 4)) >> 2) & 3)) * 32 + (l & 3) * 8 + ((w + 2) & 3) * 12288), (unsigned)((8 * lq + ((l >> 2) & 1) + ((l >> 1) & 4)) * 384 + (w ^ (((8 * lq + ((l >> 2) & 1) + ((l >> 1) & 4)) >> 2) & 3)) * 32 + (l & 3) * 8 + ((w + 3) & 3) * 12288), (unsigned)((8 * lq + ((l >> 2) & 1) + ((l >> 1) & 4)) * 384 + (w ^ (((8 * lq + ((l >> 2) & 1) + ((l >> 1) & 4)) >> 2) & 3)) * 32 + (l & 3) * 8)};
    const u32x4 v92_ = {(unsigned)(l * 16 + ((w + 0) & 3) * 11264), (unsigned)(l * 16 + ((w + 1) & 3) * 11264), (unsigned)(l * 16 + ((w + 2) & 3) * 11264), (unsigned)(l * 16 + ((w + 3) & 3) * 11264)};
    const u32x4 v96_ = {(unsigned)(l * 16), (unsigned)(l * 16 + 53248 + w * 20480), (unsigned)((l & 2) ? 0xeeeeu : 0x4444u), (unsigned)(0xff800000u)};
    const u32x4 v100_ = {(unsigned)(l16 == 0 + 4 * lq ? 0x3f80u : (l16 == 0 + 4 * lq + 1 ? 0x3f800000u : 0u)), (unsigned)(l16 == 2 + 4 * lq ? 0x3f80u : (l16 == 2 + 4 * lq + 1 ? 0x3f800000u : 0u)), (unsigned)(135168 + w * 1936 + (2 * (l16 >> 2) + (l & 1)) * 240 + (l & 2) * 32 + lq * 16), (unsigned)(135168 + (l16 < 10 ? (4 * (w & 1) + lq) * 240 + l16 * 16 : 1920))};
    const u32x4 v104_ = {(unsigned)(142912 + w * 4352 + l16 * 272 + lq * 16), (unsigned)(142912 + (4 * w + lq) * 272 + l16 * 16), (unsigned)(pm), (unsigned)(pt)};
    asm volatile(
    "s_mov_b32 s40, 0\n"
    "s_mov_b64 s[42:43], 0\n"
    "s_mov_b32 s45, 0\n"
    "s_cmp_eq_u32 s23, 0\n"
    "s_cselect_b32 s44, 0, 28\n"
    "s_sub_u32 s44, s44, 0\n"
    ".Lqk_ho1_%=:\n"
    "s_sub_u32 s44, s44, 1\n"
    "s_cbranch_scc1 .Lqk_hd2_%=\n"
    "s_sleep 17\n"
    "s_branch .Lqk_ho1_%=\n"
    ".Lqk_hd2_%=:\n"
    "s_mul_i32 s51, s22, 3072\n"
    "s_mov_b32 s60, 0\n"
    "s_add_u32 m0, s51, 0\n"
    "s_nop 0\n"
    "buffer_load_dwordx4 v0, s[8:11], s60 offen offset:0 nt lds\n"
    "buffer_load_dwordx4 v1, s[8:11], s60 offen offset:1024 nt lds\n"
    "buffer_load_dwordx4 v2, s[8:11], s60 offen offset:2048 nt lds\n"
    "s_mov_b32 s60, 1572864\n"
    "s_add_u32 m0, s51, 12288\n"
    "s_nop 0\n"
    "buffer_load_dwordx4 v0, s[8:11], s60 offen offset:0 nt lds\n"
    "buffer_load_dwordx4 v1, s[8:11], s60 offen offset:1024 nt lds\n"
    "buffer_load_dwordx4 v2, s[8:11], s60 offen offset:2048 nt lds\n"
    "s_mov_b32 s60, 3145728\n"
    "s_add_u32 m0, s51, 24576\n"
    "s_nop 0\n"
    "buffer_load_dwordx4 v0, s[8:11], s60 offen offset:0 nt lds\n"
    "buffer_load_dwordx4 v1, s[8:11], s60 offen offset:1024 nt lds\n"
    "buffer_load_dwordx4 v2, s[8:11], s60 offen offset:2048 nt lds\n"
    "s_mov_b32 s60, 4718592\n"
    "s_add_u32 m0, s51, 36864\n"
    "s_nop 0\n"
    "buffer_load_dwordx4 v0, s[8:11], s60 offen offset:0 nt lds\n"
    "buffer_load_dwordx4 v1, s[8:11], s60 offen offset:1024 nt lds\n"
    "buffer_load_dwordx4 v2, s[8:11], s60 offen offset:2048 nt lds\n"
    "s_mov_b32 s60, 393216\n"
    "s_add_u32 m0, s51, 49152\n"
    "s_nop 0\n"
    "buffer_load_dwordx4 v0, s[8:11], s60 offen offset:0 nt lds\n"
    "buffer_load_dwordx4 v1, s[8:11], s60 offen offset:1024 nt lds\n"
    "buffer_load_dwordx4 v2, s[8:11], s60 offen offset:2048 nt lds\n"
    "s_mov_b32 s60, 1966080\n"
    "s_add_u32 m0, s51, 61440\n"
    "s_nop 0\n"
    "buffer_load_dwordx4 v0, s[8:11], s60 offen offset:0 nt lds\n"
    "buffer_load_dwordx4 v1, s[8:11], s60 offen offset:1024 nt lds\n"
    "buffer_load_dwordx4 v2, s[8:11], s60 offen offset:2048 nt lds\n"
    "s_mov_b32 s60, 3538944\n"
    "s_add_u32 m0, s51, 73728\n"
    "s_nop 0\n"
    "buffer_load_dwordx4 v0, s[8:11], s60 offen offset:0 nt lds\n"
    "buffer_load_dwordx4 v1, s[8:11], s60 offen offset:1024 nt lds\n"
    "buffer_load_dwordx4 v2, s[8:11], s60 offen offset:2048 nt lds\n"
    "s_mov_b32 s60, 5111808\n"
    "s_add_u32 m0, s51, 86016\n"
    "s_nop 0\n"
    "buffer_load_dwordx4 v0, s[8:11], s60 offen offset:0 nt lds\n"
    "buffer_load_dwordx4 v1, s[8:11], s60 offen offset:1024 nt lds\n"
    "buffer_load_dwordx4 v2, s[8:11], s60 offen offset:2048 nt lds\n"
    "s_mov_b32 s60, 786432\n"
    "s_add_u32 m0, s51, 98304\n"
    "s_nop 0\n"
    "buffer_load_dwordx4 v0, s[8:11], s60 offen offset:0 nt lds\n"
    "buffer_load_dwordx4 v1, s[8:11], s60 offen offset:1024 nt lds\n"
    "buffer_load_dwordx4 v2, s[8:11], s60 offen offset:2048 nt lds\n"
    "s_mov_b32 s60, 2359296\n"
    "s_add_u32 m0, s51, 110592\n"
    "s_nop 0\n"
    "buffer_load_dwordx4 v0, s[8:11], s60 offen offset:0 nt lds\n"
    "buffer_load_dwordx4 v1, s[8:11], s60 offen offset:1024 nt lds\n"
    "buffer_load_dwordx4 v2, s[8:11], s60 offen offset:2048 nt lds\n"
    "s_mov_b32 s60, 3932160\n"
    "s_add_u32 m0, s51, 122880\n"
    "s_nop 0\n"
    "buffer_load_dwordx4 v0, s[8:11], s60 offen offset:0 nt lds\n"
    "buffer_load_dwordx4 v1, s[8:11], s60 offen offset:1024 nt lds\n"
    "buffer_load_dwordx4 v2, s[8:11], s60 offen offset:2048 nt lds\n"
    "s_mov_b32 s60, 5505024\n"
    "s_add_u32 m0, s51, 135168\n"
    "s_nop 0\n"
    "buffer_load_dwordx4 v0, s[8:11], s60 offen offset:0 nt lds\n"
    "buffer_load_dwordx4 v1, s[8:11], s60 offen offset:1024 nt lds\n"
    "buffer_load_dwordx4 v2, s[8:11], s60 offen offset:2048 nt lds\n"
    "v_mov_b64 v[108:109], -1\n"
    "buffer_store_dwordx2 v[108:109], v106, s[0:3], 0 offen sc1\n"
    "buffer_store_dwordx2 v[108:109], v107, s[0:3], 0 offen sc1\n"
    "s_waitcnt vmcnt(26)\n"
    "s_barrier\n"
    "v_add_u32 v8, 0, v3\n"
    "ds_read_b64_tr_b16 a[240:241], v8 offset:0\n"
    "ds_read_b64_tr_b16 a[242:243], v8 offset:768\n"
    "ds_read_b64_tr_b16 v[176:177], v8 offset:128\n"
    "ds_read_b64_tr_b16 v[178:179], v8 offset:896\n"
    "ds_read_b64_tr_b16 v[12:13], v8 offset:256\n"
    "ds_read_b64_tr_b16 v[14:15], v8 offset:1024\n"
    "v_add_u32 v9, 0, v4\n"
    "ds_read_b64_tr_b16 v[112:113], v9 offset:0\n"
    "ds_read_b64_tr_b16 v[114:115], v9 offset:768\n"
    "ds_read_b64_tr_b16 v[192:193], v9 offset:128\n"
    "ds_read_b64_tr_b16 v[194:195], v9 offset:896\n"
    "ds_read_b64_tr_b16 v[28:29], v9 offset:256\n"
    "ds_read_b64_tr_b16 v[30:31], v9 offset:1024\n"
    "v_add_u32 v10, 0, v5\n"
    "ds_read_b64_tr_b16 v[128:129], v10 offset:0\n"
    "ds_read_b64_tr_b16 v[130:131], v10 offset:768\n"
    "ds_read_b64_tr_b16 v[208:209], v10 offset:128\n"
    "ds_read_b64_tr_b16 v[210:211], v10 offset:896\n"
    "ds_read_b64_tr_b16 v[44:45], v10 offset:256\n"
    "ds_read_b64_tr_b16 v[46:47], v10 offset:1024\n"
    "v_add_u32 v11, 0, v6\n"
    "ds_read_b64_tr_b16 v[144:145], v11 offset:0\n"
    "ds_read_b64_tr_b16 v[146:147], v11 offset:768\n"
    "ds_read_b64_tr_b16 v[224:225], v11 offset:128\n"
    "ds_read_b64_tr_b16 v[226:227], v11 offset:896\n"
    "ds_read_b64_tr_b16 v[60:61], v11 offset:256\n"
    "ds_read_b64_tr_b16 v[62:63], v11 offset:1024\n"
    "s_waitcnt lgkmcnt(0)\n"
    "s_barrier\n"
    "s_mov_b32 s60, 1179648\n"
    "s_add_u32 m0, s51, 0\n"
    "s_nop 0\n"
    "buffer_load_dwordx4 v0, s[8:11], s60 offen offset:0 nt lds\n"
    "buffer_load_dwordx4 v1, s[8:11], s60 offen offset:1024 nt lds\n"
    "buffer_load_dwordx4 v2, s[8:11], s60 offen offset:2048 nt lds\n"
    "s_mov_b32 s60, 2752512\n"
    "s_add_u32 m0, s51, 12288\n"
    "s_nop 0\n"
    "buffer_load_dwordx4 v0, s[8:11], s60 offen offset:0 nt lds\n"
    "buffer_load_dwordx4 v1, s[8:11], s60 offen offset:1024 nt lds\n"
    "buffer_load_dwordx4 v2, s[8:11], s60 offen offset:2048 nt lds\n"
    "s_mov_b32 s60, 4325376\n"
    "s_add_u32 m0, s51, 24576\n"
    "s_nop 0\n"
    "buffer_load_dwordx4 v0, s[8:11], s60 offen offset:0 nt lds\n"
    "buffer_load_dwordx4 v1, s[8:11], s60 offen offset:1024 nt lds\n"
    "buffer_load_dwordx4 v2, s[8:11], s60 offen offset:2048 nt lds\n"
    "s_mov_b32 s60, 5898240\n"
    "s_add_u32 m0, s51, 36864\n"
    "s_nop 0\n"
    "buffer_load_dwordx4 v0, s[8:11], s60 offen offset:0 nt lds\n"
    "buffer_load_dwordx4 v1, s[8:11], s60 offen offset:1024 nt lds\n"
    "buffer_load_dwordx4 v2, s[8:11], s60 offen offset:2048 nt lds\n"
    "v_and_b32 v108, 0x70, v96\n"
    "v_lshlrev_b32 v108, 2, v108\n"
    "v_or_b32 v108, 0x8000, v108\n"
    "buffer_load_dword v109, v108, s[0:3], 0 offen sc1\n"
    "s_waitcnt vmcnt(27)\n"
    "s_barrier\n"
    "v_add_u32 v8, 49152, v3\n"
    "ds_read_b64_tr_b16 a[244:245], v8 offset:0\n"
    "ds_read_b64_tr_b16 a[246:247], v8 offset:768\n"
    "ds_read_b64_tr_b16 v[180:181], v8 offset:128\n"
    "ds_read_b64_tr_b16 v[182:183], v8 offset:896\n"
    "ds_read_b64_tr_b16 v[16:17], v8 offset:256\n"
    "ds_read_b64_tr_b16 v[18:19], v8 offset:1024\n"
    "v_add_u32 v9, 49152, v4\n"
    "ds_read_b64_tr_b16 v[116:117], v9 offset:0\n"
    "ds_read_b64_tr_b16 v[118:119], v9 offset:768\n"
    "ds_read_b64_tr_b16 v[196:197], v9 offset:128\n"
    "ds_read_b64_tr_b16 v[198:199], v9 offset:896\n"
    "ds_read_b64_tr_b16 v[32:33], v9 offset:256\n"
    "ds_read_b64_tr_b16 v[34:35], v9 offset:1024\n"
    "v_add_u32 v10, 49152, v5\n"
    "ds_read_b64_tr_b16 v[132:133], v10 offset:0\n"
    "ds_read_b64_tr_b16 v[134:135], v10 offset:768\n"
    "ds_read_b64_tr_b16 v[212:213], v10 offset:128\n"
    "ds_read_b64_tr_b16 v[214:215], v10 offset:896\n"
    "ds_read_b64_tr_b16 v[48:49], v10 offset:256\n"
    "ds_read_b64_tr_b16 v[50:51], v10 offset:1024\n"
    "v_add_u32 v11, 49152, v6\n"
    "ds_read_b64_tr_b16 v[148:149], v11 offset:0\n"
    "ds_read_b64_tr_b16 v[150:151], v11 offset:768\n"
    "ds_read_b64_tr_b16 v[228:229], v11 offset:128\n"
    "ds_read_b64_tr_b16 v[230:231], v11 offset:896\n"
    "ds_read_b64_tr_b16 v[64:65], v11 offset:256\n"
    "ds_read_b64_tr_b16 v[66:67], v11 offset:1024\n"
    "s_waitcnt lgkmcnt(0)\n"
    "s_barrier\n"
    "s_mov_b32 s60, 6291456\n"
    "s_add_u32 m0, s51, 49152\n"
    "s_nop 0\n"
    "buffer_load_dwordx4 v0, s[8:11], s60 offen offset:0 nt lds\n"
    "buffer_load_dwordx4 v1, s[8:11], s60 offen offset:1024 nt lds\n"
    "buffer_load_dwordx4 v2, s[8:11], s60 offen offset:2048 nt lds\n"
    "s_mov_b32 s60, 6684672\n"
    "s_add_u32 m0, s51, 61440\n"
    "s_nop 0\n"
    "buffer_load_dwordx4 v0, s[8:11], s60 offen offset:0 nt lds\n"
    "buffer_load_dwordx4 v1, s[8:11], s60 offen offset:1024 nt lds\n"
    "buffer_load_dwordx4 v2, s[8:11], s60 offen offset:2048 nt lds\n"
    "s_mov_b32 s60, 7077888\n"
    "s_add_u32 m0, s51, 73728\n"
    "s_nop 0\n"
    "buffer_load_dwordx4 v0, s[8:11], s60 offen offset:0 nt lds\n"
    "buffer_load_dwordx4 v1, s[8:11], s60 offen offset:1024 nt lds\n"
    "buffer_load_dwordx4 v2, s[8:11], s60 offen offset:2048 nt lds\n"
    "s_mov_b32 s60, 7471104\n"
    "s_add_u32 m0, s51, 86016\n"
    "s_nop 0\n"
    "buffer_load_dwordx4 v0, s[8:11], s60 offen offset:0 nt lds\n"
    "buffer_load_dwordx4 v1, s[8:11], s60 offen offset:1024 nt lds\n"
    "buffer_load_dwordx4 v2, s[8:11], s60 offen offset:2048 nt lds\n"
    "s_waitcnt vmcnt(27)\n"
    "s_barrier\n"
    "v_add_u32 v8, 98304, v3\n"
    "ds_read_b64_tr_b16 a[248:249], v8 offset:0\n"
    "ds_read_b64_tr_b16 a[250:251], v8 offset:768\n"
    "ds_read_b64_tr_b16 v[184:185], v8 offset:128\n"
    "ds_read_b64_tr_b16 v[186:187], v8 offset:896\n"
    "ds_read_b64_tr_b16 v[20:21], v8 offset:256\n"
    "ds_read_b64_tr_b16 v[22:23], v8 offset:1024\n"
    "v_add_u32 v9, 98304, v4\n"
    "ds_read_b64_tr_b16 v[120:121], v9 offset:0\n"
    "ds_read_b64_tr_b16 v[122:123], v9 offset:768\n"
    "ds_read_b64_tr_b16 v[200:201], v9 offset:128\n"
    "ds_read_b64_tr_b16 v[202:203], v9 offset:896\n"
    "ds_read_b64_tr_b16 v[36:37], v9 offset:256\n"
    "ds_read_b64_tr_b16 v[38:39], v9 offset:1024\n"
    "v_add_u32 v10, 98304, v5\n"
    "ds_read_b64_tr_b16 v[136:137], v10 offset:0\n"
    "ds_read_b64_tr_b16 v[138:139], v10 offset:768\n"
    "ds_read_b64_tr_b16 v[216:217], v10 offset:128\n"
    "ds_read_b64_tr_b16 v[218:219], v10 offset:896\n"
    "ds_read_b64_tr_b16 v[52:53], v10 offset:256\n"
    "ds_read_b64_tr_b16 v[54:55], v10 offset:1024\n"
    "v_add_u32 v11, 98304, v6\n"
    "ds_read_b64_tr_b16 v[152:153], v11 offset:0\n"
    "ds_read_b64_tr_b16 v[154:155], v11 offset:768\n"
    "ds_read_b64_tr_b16 v[232:233], v11 offset:128\n"
    "ds_read_b64_tr_b16 v[234:235], v11 offset:896\n"
    "ds_read_b64_tr_b16 v[68:69], v11 offset:256\n"
    "ds_read_b64_tr_b16 v[70:71], v11 offset:1024\n"
    "s_waitcnt lgkmcnt(0)\n"
    "s_barrier\n"
    "s_mov_b32 s60, 0\n"
    "s_add_u32 m0, s51, 98304\n"
    "s_nop 0\n"
    "buffer_load_dwordx4 v0, s[4:7], s60 offen offset:0 nt lds\n"
    "buffer_load_dwordx4 v1, s[4:7], s60 offen offset:1024 nt lds\n"
    "buffer_load_dwordx4 v2, s[4:7], s60 offen offset:2048 nt lds\n"
    "s_mov_b32 s60, 1572864\n"
    "s_add_u32 m0, s51, 110592\n"
    "s_nop 0\n"
    "buffer_load_dwordx4 v0, s[4:7], s60 offen offset:0 nt lds\n"
    "buffer_load_dwordx4 v1, s[4:7], s60 offen offset:1024 nt lds\n"
    "buffer_load_dwordx4 v2, s[4:7], s60 offen offset:2048 nt lds\n"
    "s_mov_b32 s60, 3145728\n"
    "s_add_u32 m0, s51, 122880\n"
    "s_nop 0\n"
    "buffer_load_dwordx4 v0, s[4:7], s60 offen offset:0 nt lds\n"
    "buffer_load_dwordx4 v1, s[4:7], s60 offen offset:1024 nt lds\n"
    "buffer_load_dwordx4 v2, s[4:7], s60 offen offset:2048 nt lds\n"
    "s_mov_b32 s60, 4718592\n"
    "s_add_u32 m0, s51, 135168\n"
    "s_nop 0\n"
    "buffer_load_dwordx4 v0, s[4:7], s60 offen offset:0 nt lds\n"
    "buffer_load_dwordx4 v1, s[4:7], s60 offen offset:1024 nt lds\n"
    "buffer_load_dwordx4 v2, s[4:7], s60 offen offset:2048 nt lds\n"
    "s_waitcnt vmcnt(25)\n"
    "s_barrier\n"
    "v_add_u32 v8, 0, v3\n"
    "ds_read_b64_tr_b16 a[252:253], v8 offset:0\n"
    "ds_read_b64_tr_b16 a[254:255], v8 offset:768\n"
    "ds_read_b64_tr_b16 v[188:189], v8 offset:128\n"
    "ds_read_b64_tr_b16 v[190:191], v8 offset:896\n"
    "ds_read_b64_tr_b16 v[24:25], v8 offset:256\n"
    "ds_read_b64_tr_b16 v[26:27], v8 offset:1024\n"
    "v_add_u32 v9, 0, v4\n"
    "ds_read_b64_tr_b16 v[124:125], v9 offset:0\n"
    "ds_read_b64_tr_b16 v[126:127], v9 offset:768\n"
    "ds_read_b64_tr_b16 v[204:205], v9 offset:128\n"
    "ds_read_b64_tr_b16 v[206:207], v9 offset:896\n"
    "ds_read_b64_tr_b16 v[40:41], v9 offset:256\n"
    "ds_read_b64_tr_b16 v[42:43], v9 offset:1024\n"
    "v_add_u32 v10, 0, v5\n"
    "ds_read_b64_tr_b16 v[140:141], v10 offset:0\n"
    "ds_read_b64_tr_b16 v[142:143], v10 offset:768\n"
    "ds_read_b64_tr_b16 v[220:221], v10 offset:128\n"
    "ds_read_b64_tr_b16 v[222:223], v10 offset:896\n"
    "ds_read_b64_tr_b16 v[56:57], v10 offset:256\n"
    "ds_read_b64_tr_b16 v[58:59], v10 offset:1024\n"
    "v_add_u32 v11, 0, v6\n"
    "ds_read_b64_tr_b16 v[156:157], v11 offset:0\n"
    "ds_read_b64_tr_b16 v[158:159], v11 offset:768\n"
    "ds_read_b64_tr_b16 v[236:237], v11 offset:128\n"
    "ds_read_b64_tr_b16 v[238:239], v11 offset:896\n"
    "ds_read_b64_tr_b16 v[72:73], v11 offset:256\n"
    "ds_read_b64_tr_b16 v[74:75], v11 offset:1024\n"
    "s_waitcnt lgkmcnt(0)\n"
    "s_barrier\n"
    "s_mov_b32 s60, 393216\n"
    "s_add_u32 m0, s51, 0\n"
    "s_nop 0\n"
    "buffer_load_dwordx4 v0, s[4:7], s60 offen offset:0 nt lds\n"
    "buffer_load_dwordx4 v1, s[4:7], s60 offen offset:1024 nt lds\n"
    "buffer_load_dwordx4 v2, s[4:7], s60 offen offset:2048 nt lds\n"
    "s_mov_b32 s60, 1966080\n"
    "s_add_u32 m0, s51, 12288\n"
    "s_nop 0\n"
    "buffer_load_dwordx4 v0, s[4:7], s60 offen offset:0 nt lds\n"
    "buffer_load_dwordx4 v1, s[4:7], s60 offen offset:1024 nt lds\n"
    "buffer_load_dwordx4 v2, s[4:7], s60 offen offset:2048 nt lds\n"
    "s_mov_b32 s60, 3538944\n"
    "s_add_u32 m0, s51, 24576\n"
    "s_nop 0\n"
    "buffer_load_dwordx4 v0, s[4:7], s60 offen offset:0 nt lds\n"
    "buffer_load_dwordx4 v1, s[4:7], s60 offen offset:1024 nt lds\n"
    "buffer_load_dwordx4 v2, s[4:7], s60 offen offset:2048 nt lds\n"
    "s_mov_b32 s60, 5111808\n"
    "s_add_u32 m0, s51, 36864\n"
    "s_nop 0\n"
    "buffer_load_dwordx4 v0, s[4:7], s60 offen offset:0 nt lds\n"
    "buffer_load_dwordx4 v1, s[4:7], s60 offen offset:1024 nt lds\n"
    "buffer_load_dwordx4 v2, s[4:7], s60 offen offset:2048 nt lds\n"
    "s_waitcnt vmcnt(24)\n"
    "s_barrier\n"
    "v_add_u32 v8, 49152, v7\n"
    "ds_read_b64_tr_b16 v[160:161], v8 offset:0\n"
    "ds_read_b64_tr_b16 v[162:163], v8 offset:768\n"
    "ds_read_b64_tr_b16 v[240:241], v8 offset:128\n"
    "ds_read_b64_tr_b16 v[242:243], v8 offset:896\n"
    "ds_read_b64_tr_b16 v[76:77], v8 offset:256\n"
    "ds_read_b64_tr_b16 v[78:79], v8 offset:1024\n"
    "v_add_u32 v9, 61440, v7\n"
    "ds_read_b64_tr_b16 v[164:165], v9 offset:0\n"
    "ds_read_b64_tr_b16 v[166:167], v9 offset:768\n"
    "ds_read_b64_tr_b16 v[244:245], v9 offset:128\n"
    "ds_read_b64_tr_b16 v[246:247], v9 offset:896\n"
    "ds_read_b64_tr_b16 v[80:81], v9 offset:256\n"
    "ds_read_b64_tr_b16 v[82:83], v9 offset:1024\n"
    "v_add_u32 v10, 73728, v7\n"
    "ds_read_b64_tr_b16 v[168:169], v10 offset:0\n"
    "ds_read_b64_tr_b16 v[170:171], v10 offset:768\n"
    "ds_read_b64_tr_b16 v[248:249], v10 offset:128\n"
    "ds_read_b64_tr_b16 v[250:251], v10 offset:896\n"
    "ds_read_b64_tr_b16 v[84:85], v10 offset:256\n"
    "ds_read_b64_tr_b16 v[86:87], v10 offset:1024\n"
    "v_add_u32 v11, 86016, v7\n"
    "ds_read_b64_tr_b16 v[172:173], v11 offset:0\n"
    "ds_read_b64_tr_b16 v[174:175], v11 offset:768\n"
    "ds_read_b64_tr_b16 v[252:253], v11 offset:128\n"
    "ds_read_b64_tr_b16 v[254:255], v11 offset:896\n"
    "ds_read_b64_tr_b16 v[88:89], v11 offset:256\n"
    "ds_read_b64_tr_b16 v[90:91], v11 offset:1024\n"
    "s_waitcnt lgkmcnt(0)\n"
    "s_barrier\n"
    "s_waitcnt vmcnt(36)\n"
    "v_cmp_gt_i32 vcc, 32, v109\n"
    "s_nop 0\n"
    "s_cbranch_vccnz .Lqk_tk0_%=\n"
    ".Lqk_tk1_%=:\n"
    "s_mov_b32 s60, 786432\n"
    "s_add_u32 m0, s51, 49152\n"
    "s_nop 0\n"
    "buffer_load_dwordx4 v0, s[4:7], s60 offen offset:0 nt lds\n"
    "buffer_load_dwordx4 v1, s[4:7], s60 offen offset:1024 nt lds\n"
    "buffer_load_dwordx4 v2, s[4:7], s60 offen offset:2048 nt lds\n"
    "s_mov_b32 s60, 2359296\n"
    "s_add_u32 m0, s51, 61440\n"
    "s_nop 0\n"
    "buffer_load_dwordx4 v0, s[4:7], s60 offen offset:0 nt lds\n"
    "buffer_load_dwordx4 v1, s[4:7], s60 offen offset:1024 nt lds\n"
    "buffer_load_dwordx4 v2, s[4:7], s60 offen offset:2048 nt lds\n"
    "s_mov_b32 s60, 3932160\n"
    "s_add_u32 m0, s51, 73728\n"
    "s_nop 0\n"
    "buffer_load_dwordx4 v0, s[4:7], s60 offen offset:0 nt lds\n"
    "buffer_load_dwordx4 v1, s[4:7], s60 offen offset:1024 nt lds\n"
    "buffer_load_dwordx4 v2, s[4:7], s60 offen offset:2048 nt lds\n"
    "s_mov_b32 s60, 5505024\n"
    "s_add_u32 m0, s51, 86016\n"
    "s_nop 0\n"
    "buffer_load_dwordx4 v0, s[4:7], s60 offen offset:0 nt lds\n"
    "buffer_load_dwordx4 v1, s[4:7], s60 offen offset:1024 nt lds\n"
    "buffer_load_dwordx4 v2, s[4:7], s60 offen offset:2048 nt lds\n"
    "v_lshlrev_b32 v108, 4, v96\n"
    "v_mov_b32 v109, 1\n"
    "s_mov_b64 exec, 15\n"
    "buffer_store_dword v109, v108, s[0:3], s35 offen sc1\n"
    "s_mov_b64 exec, -1\n"
    "s_waitcnt vmcnt(25)\n"
    "s_barrier\n"
    "v_add_u32 v8, 98304, v3\n"
    "ds_read_b64_tr_b16 a[0:1], v8 offset:0\n"
    "ds_read_b64_tr_b16 a[2:3], v8 offset:768\n"
    "ds_read_b64_tr_b16 a[80:81], v8 offset:128\n"
    "ds_read_b64_tr_b16 a[82:83], v8 offset:896\n"
    "ds_read_b64_tr_b16 a[160:161], v8 offset:256\n"
    "ds_read_b64_tr_b16 a[162:163], v8 offset:1024\n"
    "v_add_u32 v9, 98304, v4\n"
    "ds_read_b64_tr_b16 a[16:17], v9 offset:0\n"
    "ds_read_b64_tr_b16 a[18:19], v9 offset:768\n"
    "ds_read_b64_tr_b16 a[96:97], v9 offset:128\n"
    "ds_read_b64_tr_b16 a[98:99], v9 offset:896\n"
    "ds_read_b64_tr_b16 a[176:177], v9 offset:256\n"
    "ds_read_b64_tr_b16 a[178:179], v9 offset:1024\n"
    "v_add_u32 v10, 98304, v5\n"
    "ds_read_b64_tr_b16 a[32:33], v10 offset:0\n"
    "ds_read_b64_tr_b16 a[34:35], v10 offset:768\n"
    "ds_read_b64_tr_b16 a[112:113], v10 offset:128\n"
    "ds_read_b64_tr_b16 a[114:115], v10 offset:896\n"
    "ds_read_b64_tr_b16 a[192:193], v10 offset:256\n"
    "ds_read_b64_tr_b16 a[194:195], v10 offset:1024\n"
    "v_add_u32 v11, 98304, v6\n"
    "ds_read_b64_tr_b16 a[48:49], v11 offset:0\n"
    "ds_read_b64_tr_b16 a[50:51], v11 offset:768\n"
    "ds_read_b64_tr_b16 a[128:129], v11 offset:128\n"
    "ds_read_b64_tr_b16 a[130:131], v11 offset:896\n"
    "ds_read_b64_tr_b16 a[208:209], v11 offset:256\n"
    "ds_read_b64_tr_b16 a[210:211], v11 offset:1024\n"
    "s_waitcnt lgkmcnt(0)\n"
    "s_barrier\n"
    "s_mov_b32 s60, 1179648\n"
    "s_add_u32 m0, s51, 98304\n"
    "s_nop 0\n"
    "buffer_load_dwordx4 v0, s[4:7], s60 offen offset:0 nt lds\n"
    "buffer_load_dwordx4 v1, s[4:7], s60 offen offset:1024 nt lds\n"
    "buffer_load_dwordx4 v2, s[4:7], s60 offen offset:2048 nt lds\n"
    "s_mov_b32 s60, 2752512\n"
    "s_add_u32 m0, s51, 110592\n"
    "s_nop 0\n"
    "buffer_load_dwordx4 v0, s[4:7], s60 offen offset:0 nt lds\n"
    "buffer_load_dwordx4 v1, s[4:7], s60 offen offset:1024 nt lds\n"
    "buffer_load_dwordx4 v2, s[4:7], s60 offen offset:2048 nt lds\n"
    "s_mov_b32 s60, 4325376\n"
    "s_add_u32 m0, s51, 122880\n"
    "s_nop 0\n"
    "buffer_load_dwordx4 v0, s[4:7], s60 offen offset:0 nt lds\n"
    "buffer_load_dwordx4 v1, s[4:7], s60 offen offset:1024 nt lds\n"
    "buffer_load_dwordx4 v2, s[4:7], s60 offen offset:2048 nt lds\n"
    "s_mov_b32 s60, 5898240\n"
    "s_add_u32 m0, s51, 135168\n"
    "s_nop 0\n"
    "buffer_load_dwordx4 v0, s[4:7], s60 offen offset:0 nt lds\n"
    "buffer_load_dwordx4 v1, s[4:7], s60 offen offset:1024 nt lds\n"
    "buffer_load_dwordx4 v2, s[4:7], s60 offen offset:2048 nt lds\n"
    "s_waitcnt vmcnt(25)\n"
    "s_barrier\n"
    "v_add_u32 v8, 0, v3\n"
    "ds_read_b64_tr_b16 a[4:5], v8 offset:0\n"
    "ds_read_b64_tr_b16 a[6:7], v8 offset:768\n"
    "ds_read_b64_tr_b16 a[84:85], v8 offset:128\n"
    "ds_read_b64_tr_b16 a[86:87], v8 offset:896\n"
    "ds_read_b64_tr_b16 a[164:165], v8 offset:256\n"
    "ds_read_b64_tr_b16 a[166:167], v8 offset:1024\n"
    "v_add_u32 v9, 0, v4\n"
    "ds_read_b64_tr_b16 a[20:21], v9 offset:0\n"
    "ds_read_b64_tr_b16 a[22:23], v9 offset:768\n"
    "ds_read_b64_tr_b16 a[100:101], v9 offset:128\n"
    "ds_read_b64_tr_b16 a[102:103], v9 offset:896\n"
    "ds_read_b64_tr_b16 a[180:181], v9 offset:256\n"
    "ds_read_b64_tr_b16 a[182:183], v9 offset:1024\n"
    "v_add_u32 v10, 0, v5\n"
    "ds_read_b64_tr_b16 a[36:37], v10 offset:0\n"
    "ds_read_b64_tr_b16 a[38:39], v10 offset:768\n"
    "ds_read_b64_tr_b16 a[116:117], v10 offset:128\n"
    "ds_read_b64_tr_b16 a[118:119], v10 offset:896\n"
    "ds_read_b64_tr_b16 a[196:197], v10 offset:256\n"
    "ds_read_b64_tr_b16 a[198:199], v10 offset:1024\n"
    "v_add_u32 v11, 0, v6\n"
    "ds_read_b64_tr_b16 a[52:53], v11 offset:0\n"
    "ds_read_b64_tr_b16 a[54:55], v11 offset:768\n"
    "ds_read_b64_tr_b16 a[132:133], v11 offset:128\n"
    "ds_read_b64_tr_b16 a[134:135], v11 offset:896\n"
    "ds_read_b64_tr_b16 a[212:213], v11 offset:256\n"
    "ds_read_b64_tr_b16 a[214:215], v11 offset:1024\n"
    "s_waitcnt lgkmcnt(0)\n"
    "s_barrier\n"
    "s_mov_b32 s60, 6291456\n"
    "s_add_u32 m0, s51, 0\n"
    "s_nop 0\n"
    "buffer_load_dwordx4 v0, s[4:7], s60 offen offset:0 nt lds\n"
    "buffer_load_dwordx4 v1, s[4:7], s60 offen offset:1024 nt lds\n"
    "buffer_load_dwordx4 v2, s[4:7], s60 offen offset:2048 nt lds\n"
    "s_mov_b32 s60, 6684672\n"
    "s_add_u32 m0, s51, 12288\n"
    "s_nop 0\n"
    "buffer_load_dwordx4 v0, s[4:7], s60 offen offset:0 nt lds\n"
    "buffer_load_dwordx4 v1, s[4:7], s60 offen offset:1024 nt lds\n"
    "buffer_load_dwordx4 v2, s[4:7], s60 offen offset:2048 nt lds\n"
    "s_mov_b32 s60, 7077888\n"
    "s_add_u32 m0, s51, 24576\n"
    "s_nop 0\n"
    "buffer_load_dwordx4 v0, s[4:7], s60 offen offset:0 nt lds\n"
    "buffer_load_dwordx4 v1, s[4:7], s60 offen offset:1024 nt lds\n"
    "buffer_load_dwordx4 v2, s[4:7], s60 offen offset:2048 nt lds\n"
    "s_mov_b32 s60, 7471104\n"
    "s_add_u32 m0, s51, 36864\n"
    "s_nop 0\n"
    "buffer_load_dwordx4 v0, s[4:7], s60 offen offset:0 nt lds\n"
    "buffer_load_dwordx4 v1, s[4:7], s60 offen offset:1024 nt lds\n"
    "buffer_load_dwordx4 v2, s[4:7], s60 offen offset:2048 nt lds\n"
    "s_waitcnt vmcnt(25)\n"
    "s_barrier\n"
    "v_add_u32 v8, 49152, v3\n"
    "ds_read_b64_tr_b16 a[8:9], v8 offset:0\n"
    "ds_read_b64_tr_b16 a[10:11], v8 offset:768\n"
    "ds_read_b64_tr_b16 a[88:89], v8 offset:128\n"
    "ds_read_b64_tr_b16 a[90:91], v8 offset:896\n"
    "ds_read_b64_tr_b16 a[168:169], v8 offset:256\n"
    "ds_read_b64_tr_b16 a[170:171], v8 offset:1024\n"
    "v_add_u32 v9, 49152, v4\n"
    "ds_read_b64_tr_b16 a[24:25], v9 offset:0\n"
    "ds_read_b64_tr_b16 a[26:27], v9 offset:768\n"
    "ds_read_b64_tr_b16 a[104:105], v9 offset:128\n"
    "ds_read_b64_tr_b16 a[106:107], v9 offset:896\n"
    "ds_read_b64_tr_b16 a[184:185], v9 offset:256\n"
    "ds_read_b64_tr_b16 a[186:187], v9 offset:1024\n"
    "v_add_u32 v10, 49152, v5\n"
    "ds_read_b64_tr_b16 a[40:41], v10 offset:0\n"
    "ds_read_b64_tr_b16 a[42:43], v10 offset:768\n"
    "ds_read_b64_tr_b16 a[120:121], v10 offset:128\n"
    "ds_read_b64_tr_b16 a[122:123], v10 offset:896\n"
    "ds_read_b64_tr_b16 a[200:201], v10 offset:256\n"
    "ds_read_b64_tr_b16 a[202:203], v10 offset:1024\n"
    "v_add_u32 v11, 49152, v6\n"
    "ds_read_b64_tr_b16 a[56:57], v11 offset:0\n"
    "ds_read_b64_tr_b16 a[58:59], v11 offset:768\n"
    "ds_read_b64_tr_b16 a[136:137], v11 offset:128\n"
    "ds_read_b64_tr_b16 a[138:139], v11 offset:896\n"
    "ds_read_b64_tr_b16 a[216:217], v11 offset:256\n"
    "ds_read_b64_tr_b16 a[218:219], v11 offset:1024\n"
    "s_waitcnt lgkmcnt(0)\n"
    "s_barrier\n"
    "s_waitcnt vmcnt(12)\n"
    "s_barrier\n"
    "v_add_u32 v8, 98304, v3\n"
    "ds_read_b64_tr_b16 a[12:13], v8 offset:0\n"
    "ds_read_b64_tr_b16 a[14:15], v8 offset:768\n"
    "ds_read_b64_tr_b16 a[92:93], v8 offset:128\n"
    "ds_read_b64_tr_b16 a[94:95], v8 offset:896\n"
    "ds_read_b64_tr_b16 a[172:173], v8 offset:256\n"
    "ds_read_b64_tr_b16 a[174:175], v8 offset:1024\n"
    "v_add_u32 v9, 98304, v4\n"
    "ds_read_b64_tr_b16 a[28:29], v9 offset:0\n"
    "ds_read_b64_tr_b16 a[30:31], v9 offset:768\n"
    "ds_read_b64_tr_b16 a[108:109], v9 offset:128\n"
    "ds_read_b64_tr_b16 a[110:111], v9 offset:896\n"
    "ds_read_b64_tr_b16 a[188:189], v9 offset:256\n"
    "ds_read_b64_tr_b16 a[190:191], v9 offset:1024\n"
    "v_add_u32 v10, 98304, v5\n"
    "ds_read_b64_tr_b16 a[44:45], v10 offset:0\n"
    "ds_read_b64_tr_b16 a[46:47], v10 offset:768\n"
    "ds_read_b64_tr_b16 a[124:125], v10 offset:128\n"
    "ds_read_b64_tr_b16 a[126:127], v10 offset:896\n"
    "ds_read_b64_tr_b16 a[204:205], v10 offset:256\n"
    "ds_read_b64_tr_b16 a[206:207], v10 offset:1024\n"
    "v_add_u32 v11, 98304, v6\n"
    "ds_read_b64_tr_b16 a[60:61], v11 offset:0\n"
    "ds_read_b64_tr_b16 a[62:63], v11 offset:768\n"
    "ds_read_b64_tr_b16 a[140:141], v11 offset:128\n"
    "ds_read_b64_tr_b16 a[142:143], v11 offset:896\n"
    "ds_read_b64_tr_b16 a[220:221], v11 offset:256\n"
    "ds_read_b64_tr_b16 a[222:223], v11 offset:1024\n"
    "s_waitcnt lgkmcnt(0)\n"
    "s_barrier\n"
    "s_waitcnt vmcnt(0)\n"
    "s_barrier\n"
    "v_add_u32 v8, 0, v7\n"
    "ds_read_b64_tr_b16 a[64:65], v8 offset:0\n"
    "ds_read_b64_tr_b16 a[66:67], v8 offset:768\n"
    "ds_read_b64_tr_b16 a[144:145], v8 offset:128\n"
    "ds_read_b64_tr_b16 a[146:147], v8 offset:896\n"
    "ds_read_b64_tr_b16 a[224:225], v8 offset:256\n"
    "ds_read_b64_tr_b16 a[226:227], v8 offset:1024\n"
    "v_add_u32 v9, 12288, v7\n"
    "ds_read_b64_tr_b16 a[68:69], v9 offset:0\n"
    "ds_read_b64_tr_b16 a[70:71], v9 offset:768\n"
    "ds_read_b64_tr_b16 a[148:149], v9 offset:128\n"
    "ds_read_b64_tr_b16 a[150:151], v9 offset:896\n"
    "ds_read_b64_tr_b16 a[228:229], v9 offset:256\n"
    "ds_read_b64_tr_b16 a[230:231], v9 offset:1024\n"
    "v_add_u32 v10, 24576, v7\n"
    "ds_read_b64_tr_b16 a[72:73], v10 offset:0\n"
    "ds_read_b64_tr_b16 a[74:75], v10 offset:768\n"
    "ds_read_b64_tr_b16 a[152:153], v10 offset:128\n"
    "ds_read_b64_tr_b16 a[154:155], v10 offset:896\n"
    "ds_read_b64_tr_b16 a[232:233], v10 offset:256\n"
    "ds_read_b64_tr_b16 a[234:235], v10 offset:1024\n"
    "v_add_u32 v11, 36864, v7\n"
    "ds_read_b64_tr_b16 a[76:77], v11 offset:0\n"
    "ds_read_b64_tr_b16 a[78:79], v11 offset:768\n"
    "ds_read_b64_tr_b16 a[156:157], v11 offset:128\n"
    "ds_read_b64_tr_b16 a[158:159], v11 offset:896\n"
    "ds_read_b64_tr_b16 a[236:237], v11 offset:256\n"
    "ds_read_b64_tr_b16 a[238:239], v11 offset:1024\n"
    "s_waitcnt lgkmcnt(0)\n"
    "s_barrier\n"
    "ds_write_b128 v97, v[12:15] offset:0\n"
    "ds_write_b128 v97, v[16:19] offset:1024\n"
    "ds_write_b128 v97, v[20:23] offset:2048\n"
    "ds_write_b128 v97, v[24:27] offset:3072\n"
    "ds_write_b128 v97, v[28:31] offset:4096\n"
    "ds_write_b128 v97, v[32:35] offset:5120\n"
    "ds_write_b128 v97, v[36:39] offset:6144\n"
    "ds_write_b128 v97, v[40:43] offset:7168\n"
    "ds_write_b128 v97, v[44:47] offset:8192\n"
    "ds_write_b128 v97, v[48:51] offset:9216\n"
    "ds_write_b128 v97, v[52:55] offset:10240\n"
    "ds_write_b128 v97, v[56:59] offset:11264\n"
    "ds_write_b128 v97, v[60:63] offset:12288\n"
    "ds_write_b128 v97, v[64:67] offset:13312\n"
    "ds_write_b128 v97, v[68:71] offset:14336\n"
    "ds_write_b128 v97, v[72:75] offset:15360\n"
    "ds_write_b128 v97, v[76:79] offset:16384\n"
    "ds_write_b128 v97, v[80:83] offset:17408\n"
    "ds_write_b128 v97, v[84:87] offset:18432\n"
    "ds_write_b128 v97, v[88:91] offset:19456\n"
    "s_mov_b32 s46, -2\n"
    "s_mov_b32 s66, 1037384597\n"
    "s_mov_b32 s67, 1037384597\n"
    "s_mov_b32 s68, 858993459\n"
    "s_mov_b32 s69, 858993459\n"
    "s_mov_b32 s70, 67044351\n"
    "s_mov_b32 s71, 67044351\n"
    "v_mov_b32 v8, v99\n"
    "v_mov_b32 v9, v99\n"
    "v_mov_b32 v10, 137088\n"
    "ds_write_b64 v10, v[8:9] offset:0\n"
    "ds_write_b64 v10, v[8:9] offset:8\n"
    "ds_write_b64 v10, v[8:9] offset:1936\n"
    "ds_write_b64 v10, v[8:9] offset:1944\n"
    "ds_write_b64 v10, v[8:9] offset:3872\n"
    "ds_write_b64 v10, v[8:9] offset:3880\n"
    "ds_write_b64 v10, v[8:9] offset:5808\n"
    "ds_write_b64 v10, v[8:9] offset:5816\n"
    "s_lshl_b32 s59, s22, 11\n"
    "s_add_u32 s59, s59, 45056\n"
    "v_lshrrev_b32 v4, 2, v96\n"
    "v_and_b32 v6, 12, v4\n"
    "v_lshlrev_b32 v5, 1, v96\n"
    "v_and_b32 v5, 0x380, v5\n"
    "v_min_u32 v5, 0x200, v5\n"
    "v_add_u32 v5, v5, v6\n"
    "s_lshr_b32 s60, s23, 13\n"
    "s_mul_i32 s60, s60, 3\n"
    ".Lqk_po6_%=:\n"
    "buffer_load_dword v8, v4, s[0:3], s32 offen sc1\n"
    "s_waitcnt vmcnt(0)\n"
    "v_cmp_gt_i32 vcc, 1, v8\n"
    "s_nop 0\n"
    "s_cbranch_vccz .Lqk_pd7_%=\n"
    "s_add_u32 s40, s40, 1\n"
    "s_cmp_lt_u32 s40, 0x40000\n"
    "s_cbranch_scc1 .Lqk_sp8_%=\n"
    "v_mov_b32 v8, 20\n"
    "s_mov_b32 s44, 33728\n"
    "buffer_store_dword v8, off, s[0:3], s44 sc1\n"
    "s_waitcnt vmcnt(0)\n"
    "s_branch .Lqk_pd7_%=\n"
    ".Lqk_sp8_%=:\n"
    "s_sleep 4\n"
    "s_branch .Lqk_po6_%=\n"
    ".Lqk_pd7_%=:\n"
    ".Lqk_po9_%=:\n"
    "buffer_load_dword v8, v5, s[0:3], s33 offen sc1\n"
    "s_waitcnt vmcnt(0)\n"
    "v_cmp_gt_i32 vcc, s60, v8\n"
    "s_nop 0\n"
    "s_cbranch_vccz .Lqk_pd10_%=\n"
    "s_add_u32 s40, s40, 1\n"
    "s_cmp_lt_u32 s40, 0x40000\n"
    "s_cbranch_scc1 .Lqk_sp11_%=\n"
    "v_mov_b32 v8, 21\n"
    "s_mov_b32 s44, 33728\n"
    "buffer_store_dword v8, off, s[0:3], s44 sc1\n"
    "s_waitcnt vmcnt(0)\n"
    "s_branch .Lqk_pd10_%=\n"
    ".Lqk_sp11_%=:\n"
    "s_sleep 4\n"
    "s_branch .Lqk_po9_%=\n"
    ".Lqk_pd10_%=:\n"
    "s_add_u32 s53, s27, s23\n"
    "s_sub_u32 m0, s26, 8192\n"
    "s_nop 0\n"
    "buffer_load_dwordx4 v96, s[0:3], s53 offen offset:0 sc1 lds\n"
    "buffer_load_dwordx4 v96, s[0:3], s53 offen offset:1024 sc1 lds\n"
    "buffer_load_dwordx4 v96, s[0:3], s53 offen offset:2048 sc1 lds\n"
    "buffer_load_dwordx4 v96, s[0:3], s53 offen offset:3072 sc1 lds\n"
    "s_sub_u32 m0, s26, 4096\n"
    "s_add_u32 s53, s53, 4096\n"
    "buffer_load_dwordx4 v96, s[0:3], s53 offen offset:0 sc1 lds\n"
    "buffer_load_dwordx4 v96, s[0:3], s53 offen offset:1024 sc1 lds\n"
    ".Lqk_po12_%=:\n"
    "buffer_load_dword v8, v6, s[0:3], s34 offen sc1\n"
    "s_waitcnt vmcnt(0)\n"
    "v_cmp_gt_i32 vcc, 2, v8\n"
    "s_nop 0\n"
    "s_cbranch_vccz .Lqk_pd13_%=\n"
    "s_add_u32 s40, s40, 1\n"
    "s_cmp_lt_u32 s40, 0x40000\n"
    "s_cbranch_scc1 .Lqk_sp14_%=\n"
    "v_mov_b32 v8, 22\n"
    "s_mov_b32 s44, 33728\n"
    "buffer_store_dword v8, off, s[0:3], s44 sc1\n"
    "s_waitcnt vmcnt(0)\n"
    "s_branch .Lqk_pd13_%=\n"
    ".Lqk_sp14_%=:\n"
    "s_sleep 4\n"
    "s_branch .Lqk_po12_%=\n"
    ".Lqk_pd13_%=:\n"
    "s_memrealtime s[42:43]\n"
    "s_waitcnt lgkmcnt(0)\n"
    "s_sub_u32 s57, s42, 768\n"
    "s_mov_b32 s46, -1\n"
    "s_mov_b32 s47, -5\n"
    "s_mov_b32 s50, 2130706432\n"
    "s_mov_b32 s48, 2130706432\n"
    "s_mov_b32 s49, 4194304\n"
    "s_mov_b32 s55, s31\n"
    "s_add_u32 s54, s29, s23\n"
    "s_mov_b32 s62, -12\n"
    "s_mov_b32 s63, 0\n"
    "s_barrier\n"
    "v_mov_b32 v110, v104\n"
    "ds_read_b128 v[32:35], v92 offset:0\n"
    "ds_read_b128 v[36:39], v92 offset:1024\n"
    "ds_read_b128 v[40:43], v93 offset:0\n"
    "ds_read_b128 v[44:47], v93 offset:1024\n"
    "ds_read_b128 v[48:51], v94 offset:0\n"
    "ds_read_b128 v[52:55], v94 offset:1024\n"
    "s_waitcnt lgkmcnt(0)\n"
    "s_branch .Lqk_bulk_%=\n"
    ".Lqk_take_%=:\n"
    "s_waitcnt vmcnt(2)\n"
    ".Lqk_mk0x14_%=:\n"
    "ds_read_b128 v[56:59], v92 offset:8192\n"
    "s_waitcnt vmcnt(1)\n"
    "ds_read_b128 v[60:63], v92 offset:9216\n"
    "s_waitcnt lgkmcnt(1)\n"
    "v_smfmac_f32_16x16x64_bf16 v[8:11], v[56:59], v[32:39], v98\n"
    "v_max_u32 v108, v56, v58\n"
    "v_smfmac_f32_16x16x64_bf16 v[12:15], v[56:59], a[160:167], v98\n"
    "s_waitcnt lgkmcnt(0)\n"
    "ds_read_b128 v[32:35], v97 offset:6144\n"
    "v_max3_u32 v108, v108, v60, v62\n"
    "v_smfmac_f32_16x16x64_bf16 v[0:3], v[56:59], v[176:183], v98\n"
    "ds_read_b128 v[36:39], v97 offset:7168\n"
    "v_cmp_eq_u32 vcc, -1, v108\n"
    "v_smfmac_f32_16x16x64_bf16 v[4:7], v[56:59], a[80:87], v98\n"
    "s_cbranch_vccnz .Lqk_retry_%=\n"
    ".Lqk_go_%=:\n"
    "s_barrier\n"
    ".Lqk_mk1x15_%=:\n"
    "v_smfmac_f32_16x16x64_bf16 v[16:19], v[56:59], a[240:247], v98\n"
    "ds_read_b128 v[24:27], v93 offset:8192\n"
    "ds_read_b128 v[28:31], v93 offset:9216\n"
    "v_smfmac_f32_16x16x64_bf16 v[20:23], v[56:59], a[0:7], v98\n"
    "s_waitcnt vmcnt(0)\n"
    "ds_read_b128 v[64:67], v92 offset:10240\n"
    "v_smfmac_f32_16x16x64_bf16 v[8:11], v[60:63], v[40:47], v98\n"
    "ds_read_b128 v[56:59], v94 offset:9216\n"
    "v_smfmac_f32_16x16x64_bf16 v[12:15], v[60:63], a[168:175], v98\n"
    "ds_read_b128 v[40:43], v97 offset:8192\n"
    "ds_read_b128 v[44:47], v97 offset:9216\n"
    "v_smfmac_f32_16x16x64_bf16 v[0:3], v[60:63], v[184:191], v98\n"
    "v_smfmac_f32_16x16x64_bf16 v[4:7], v[60:63], a[88:95], v98\n"
    "v_smfmac_f32_16x16x64_bf16 v[16:19], v[60:63], a[248:255], v98\n"
    "s_waitcnt lgkmcnt(3)\n"
    "v_max_u32 v108, v64, v66\n"
    "v_smfmac_f32_16x16x64_bf16 v[20:23], v[60:63], a[8:15], v98\n"
    "v_cmp_eq_u32 vcc, -1, v108\n"
    "v_smfmac_f32_16x16x64_bf16 v[8:11], v[24:27], v[48:55], v98\n"
    "s_cbranch_vccnz .Lqk_r2_%=\n"
    ".Lqk_go2_%=:\n"
    "s_barrier\n"
    "v_smfmac_f32_16x16x64_bf16 v[12:15], v[24:27], a[176:183], v98\n"
    "s_memrealtime s[72:73]\n"
    "ds_read_b128 v[64:67], v94 offset:8192\n"
    "ds_read_b128 v[60:63], v95 offset:8192\n"
    "v_smfmac_f32_16x16x64_bf16 v[0:3], v[24:27], v[192:199], v98\n"
    "ds_read_b128 v[48:51], v97 offset:10240\n"
    "ds_read_b128 v[52:55], v97 offset:11264\n"
    "v_smfmac_f32_16x16x64_bf16 v[4:7], v[24:27], a[96:103], v98\n"
    "v_mov_b32 v109, s47\n"
    "v_smfmac_f32_16x16x64_bf16 v[16:19], v[24:27], v[112:119], v98\n"
    "v_lshlrev_b32 v110, 10, v96\n"
    "v_smfmac_f32_16x16x64_bf16 v[20:23], v[24:27], a[16:23], v98\n"
    "ds_read_b128 v[24:27], v95 offset:9216\n"
    "s_mov_b64 vcc, s[68:69]\n"
    "v_smfmac_f32_16x16x64_bf16 v[8:11], v[28:31], v[32:39], v98\n"
    "s_add_u32 s64, s64, s57\n"
    "ds_read_b128 v[32:35], v97 offset:12288\n"
    "ds_read_b128 v[36:39], v97 offset:13312\n"
    "v_smfmac_f32_16x16x64_bf16 v[12:15], v[28:31], a[184:191], v98\n"
    "v_smfmac_f32_16x16x64_bf16 v[0:3], v[28:31], v[200:207], v98\n"
    "v_smfmac_f32_16x16x64_bf16 v[4:7], v[28:31], a[104:111], v98\n"
    "v_smfmac_f32_16x16x64_bf16 v[16:19], v[28:31], v[120:127], v98\n"
    "v_smfmac_f32_16x16x64_bf16 v[20:23], v[28:31], a[24:31], v98\n"
    "s_waitcnt lgkmcnt(0)\n"
    "v_smfmac_f32_16x16x64_bf16 v[8:11], v[64:67], v[40:47], v98\n"
    "s_sub_u32 s44, s72, s57\n"
    "ds_read_b128 v[40:43], v97 offset:14336\n"
    "ds_read_b128 v[44:47], v97 offset:15360\n"
    "v_smfmac_f32_16x16x64_bf16 v[12:15], v[64:67], a[192:199], v98\n"
    "ds_read_b128 v[28:31], v96 offset:10240\n"
    "s_add_u32 s45, s72, 18\n"
    "v_smfmac_f32_16x16x64_bf16 v[0:3], v[64:67], v[208:215], v98\n"
    "s_cmp_gt_i32 s44, 6\n"
    "s_cselect_b32 s57, s45, s64\n"
    "s_cselect_b32 s63, 12, -1\n"
    "v_smfmac_f32_16x16x64_bf16 v[4:7], v[64:67], a[112:119], v98\n"
    "v_mov_b32 v108, s57\n"
    "v_smfmac_f32_16x16x64_bf16 v[16:19], v[64:67], v[128:135], v98\n"
    "s_mov_b64 exec, 15\n"
    "buffer_store_dwordx2 v[108:109], v110, s[0:3], s56 offen sc1\n"
    "s_mov_b64 exec, -1\n"
    "v_smfmac_f32_16x16x64_bf16 v[20:23], v[64:67], a[32:39], v98\n"
    "v_smfmac_f32_16x16x64_bf16 v[8:11], v[56:59], v[48:55], v98\n"
    "ds_read_b128 v[48:51], v97 offset:16384\n"
    "ds_read_b128 v[52:55], v97 offset:17408\n"
    "v_smfmac_f32_16x16x64_bf16 v[12:15], v[56:59], a[200:207], v98\n"
    "ds_read_b128 v[64:67], v96 offset:21504\n"
    "v_bfe_u32 v111, v96, 8, 2\n"
    "v_smfmac_f32_16x16x64_bf16 v[8:11], v[60:63], v[32:39], v98\n"
    "ds_read_b128 v[32:35], v97 offset:18432\n"
    "ds_read_b128 v[36:39], v97 offset:19456\n"
    "v_smfmac_f32_16x16x64_bf16 v[12:15], v[60:63], a[208:215], v98\n"
    "s_waitcnt lgkmcnt(5)\n"
    "v_mad_i32_i24 v110, v111, -8, v104\n"
    "v_smfmac_f32_16x16x64_bf16 v[8:11], v[24:27], v[40:47], v98\n"
    "v_mad_i32_i24 v111, v111, -8, v102\n"
    "v_smfmac_f32_16x16x64_bf16 v[12:15], v[24:27], a[216:223], v98\n"
    "v_smfmac_f32_16x16x64_bf16 v[0:3], v[56:59], v[216:223], v98\n"
    "v_smfmac_f32_16x16x64_bf16 v[4:7], v[56:59], a[120:127], v98\n"
    "s_waitcnt lgkmcnt(3)\n"
    "v_smfmac_f32_16x16x64_bf16 v[8:11], v[28:31], v[48:55], v98\n"
    "v_smfmac_f32_16x16x64_bf16 v[12:15], v[28:31], a[224:231], v98\n"
    "s_waitcnt lgkmcnt(0)\n"
    "v_smfmac_f32_16x16x64_bf16 v[8:11], v[64:67], v[32:39], v98\n"
    "v_smfmac_f32_16x16x64_bf16 v[12:15], v[64:67], a[232:239], v98\n"
    "ds_read_b128 v[32:35], v92 offset:0\n"
    "ds_read_b128 v[36:39], v92 offset:1024\n"
    "v_smfmac_f32_16x16x64_bf16 v[0:3], v[60:63], v[224:231], v98\n"
    "s_nop 1\n"
    "v_add_f32 v8, v8, v10\n"
    "v_smfmac_f32_16x16x64_bf16 v[4:7], v[60:63], a[128:135], v98\n"
    "ds_read_b128 v[40:43], v93 offset:0\n"
    "v_add_f32 v9, v9, v11\n"
    "v_smfmac_f32_16x16x64_bf16 v[0:3], v[24:27], v[232:239], v98\n"
    "ds_read_b128 v[44:47], v93 offset:1024\n"
    "v_cvt_pk_bf16_f32 v8, v8, v9\n"
    "v_smfmac_f32_16x16x64_bf16 v[0:3], v[28:31], v[240:247], v98\n"
    "ds_read_b128 v[48:51], v94 offset:0\n"
    "v_add_f32 v12, v12, v14\n"
    "v_smfmac_f32_16x16x64_bf16 v[0:3], v[64:67], v[248:255], v98\n"
    "ds_read_b128 v[52:55], v94 offset:1024\n"
    "v_add_f32 v13, v13, v15\n"
    "v_smfmac_f32_16x16x64_bf16 v[4:7], v[24:27], a[136:143], v98\n"
    "v_cvt_pk_bf16_f32 v9, v12, v13\n"
    "v_smfmac_f32_16x16x64_bf16 v[4:7], v[28:31], a[144:151], v98\n"
    "s_nop 0\n"
    "v_mfma_f32_16x16x16_bf16 v[12:15], v[8:9], v[100:101], 0\n"
    "v_add_f32 v0, v0, v2\n"
    "v_add_f32 v1, v1, v3\n"
    "v_smfmac_f32_16x16x64_bf16 v[4:7], v[64:67], a[152:159], v98\n"
    "v_cvt_pk_bf16_f32 v0, v0, v1\n"
    "v_smfmac_f32_16x16x64_bf16 v[16:19], v[56:59], v[136:143], v98\n"
    "s_nop 1\n"
    "v_cvt_pk_bf16_f32 v15, v14, v15\n"
    "v_smfmac_f32_16x16x64_bf16 v[20:23], v[56:59], a[40:47], v98\n"
    "s_nop 0\n"
    "v_add_f32 v4, v4, v6\n"
    "v_smfmac_f32_16x16x64_bf16 v[16:19], v[60:63], v[144:151], v98\n"
    "v_add_f32 v5, v5, v7\n"
    "v_smfmac_f32_16x16x64_bf16 v[20:23], v[60:63], a[48:55], v98\n"
    "v_cvt_pk_bf16_f32 v1, v4, v5\n"
    "v_smfmac_f32_16x16x64_bf16 v[16:19], v[24:27], v[152:159], v98\n"
    "s_nop 0\n"
    "v_mfma_f32_16x16x16_bf16 v[4:7], v[0:1], v[100:101], 0\n"
    "v_cvt_pk_bf16_f32 v14, v12, v13\n"
    "v_smfmac_f32_16x16x64_bf16 v[20:23], v[24:27], a[56:63], v98\n"
    "v_smfmac_f32_16x16x64_bf16 v[16:19], v[28:31], v[160:167], v98\n"
    "s_nop 1\n"
    "v_cvt_pk_bf16_f32 v13, v6, v7\n"
    "v_smfmac_f32_16x16x64_bf16 v[20:23], v[28:31], a[64:71], v98\n"
    "v_cvt_pk_bf16_f32 v12, v4, v5\n"
    "v_smfmac_f32_16x16x64_bf16 v[16:19], v[64:67], v[168:175], v98\n"
    "v_smfmac_f32_16x16x64_bf16 v[20:23], v[64:67], a[72:79], v98\n"
    ".Lqk_mk2x16_%=:\n"
    "v_mfma_f32_16x16x32_bf16 v[24:27], v[12:15], v[12:15], 0\n"
    "s_nop 1\n"
    "v_add_f32 v16, v16, v18\n"
    "v_add_f32 v17, v17, v19\n"
    "v_cvt_pk_bf16_f32 v16, v16, v17\n"
    "v_add_f32 v20, v20, v22\n"
    "v_add_f32 v21, v21, v23\n"
    "v_cvt_pk_bf16_f32 v17, v20, v21\n"
    "s_nop 1\n"
    "v_mfma_f32_16x16x16_bf16 v[20:23], v[16:17], v[100:101], 0\n"
    "v_mfma_f32_16x16x32_bf16 v[0:3], v[72:75], v[12:15], 0\n"
    "v_mfma_f32_16x16x32_bf16 v[8:11], v[68:71], v[12:15], 0\n"
    "s_nop 1\n"
    "v_cvt_pk_bf16_f32 v17, v22, v23\n"
    "v_cvt_pk_bf16_f32 v16, v20, v21\n"
    "s_nop 1\n"
    "v_mfma_f32_16x16x16_bf16 v[24:27], v[16:17], v[16:17], v[24:27]\n"
    "v_mfma_f32_16x16x16_bf16 v[0:3], v[78:79], v[16:17], v[0:3]\n"
    "v_mfma_f32_16x16x16_bf16 v[8:11], v[76:77], v[16:17], v[8:11]\n"
    "s_nop 1\n"
    "ds_write_b64 v111, v[26:27] offset:128\n"
    "v_mfma_f32_16x16x32_bf16 v[56:59], v[12:15], v[80:83], 0\n"
    "s_nop 3\n"
    "v_cndmask_b32_dpp v8, v0, v8, vcc quad_perm:[0,1,0,1] row_mask:0xf bank_mask:0xf\n"
    "v_cndmask_b32_dpp v9, v1, v9, vcc quad_perm:[0,1,0,1] row_mask:0xf bank_mask:0xf\n"
    "v_cndmask_b32_dpp v10, v2, v10, vcc quad_perm:[0,1,0,1] row_mask:0xf bank_mask:0xf\n"
    "v_cndmask_b32_dpp v11, v3, v11, vcc quad_perm:[0,1,0,1] row_mask:0xf bank_mask:0xf\n"
    "ds_write_b128 v102, v[8:11]\n"
    "v_mfma_f32_16x16x32_bf16 v[60:63], v[12:15], v[84:87], 0\n"
    "s_mov_b64 exec, s[20:21]\n"
    "s_waitcnt lgkmcnt(0)\n"
    "s_barrier\n"
    ".Lqk_mk3x17_%=:\n"
    "ds_read_b128 v[72:75], v103 offset:0\n"
    "ds_read_b128 v[76:79], v103 offset:1936\n"
    "ds_read_b128 v[80:83], v103 offset:3872\n"
    "ds_read_b128 v[84:87], v103 offset:5808\n"
    "s_mov_b64 exec, -1\n"
    "v_mfma_f32_16x16x16_bf16 v[56:59], v[16:17], v[88:89], v[56:59]\n"
    "v_mfma_f32_16x16x16_bf16 v[60:63], v[16:17], v[90:91], v[60:63]\n"
    "s_waitcnt lgkmcnt(2)\n"
    "v_pk_add_f32 v[72:73], v[72:73], v[76:77]\n"
    "v_pk_add_f32 v[74:75], v[74:75], v[78:79]\n"
    "s_waitcnt lgkmcnt(1)\n"
    "v_pk_add_f32 v[72:73], v[72:73], v[80:81]\n"
    "v_pk_add_f32 v[74:75], v[74:75], v[82:83]\n"
    "s_waitcnt lgkmcnt(0)\n"
    "v_pk_add_f32 v[72:73], v[72:73], v[84:85]\n"
    "v_pk_add_f32 v[74:75], v[74:75], v[86:87]\n"
    "v_max3_f32 v76, v72, v73, v74\n"
    "v_max_f32 v76, v76, v75\n"
    "v_pk_mul_f32 v[72:73], v[72:73], s[66:67]\n"
    "v_pk_mul_f32 v[74:75], v[74:75], s[66:67]\n"
    "v_max_f32_dpp v76, v76, v76 row_ror:8 row_mask:0xf bank_mask:0xf\n"
    "s_nop 1\n"
    "v_max_f32_dpp v76, v76, v76 row_ror:4 row_mask:0xf bank_mask:0xf\n"
    "s_nop 1\n"
    "v_max_f32_dpp v76, v76, v76 row_ror:2 row_mask:0xf bank_mask:0xf\n"
    "s_nop 1\n"
    "v_max_f32_dpp v76, v76, v76 row_ror:1 row_mask:0xf bank_mask:0xf\n"
    "v_pk_fma_f32 v[72:73], v[76:77], s[66:67], v[72:73] op_sel_hi:[0,1,1] neg_lo:[1,0,0] neg_hi:[1,0,0]\n"
    "v_pk_fma_f32 v[74:75], v[76:77], s[66:67], v[74:75] op_sel_hi:[0,1,1] neg_lo:[1,0,0] neg_hi:[1,0,0]\n"
    "v_exp_f32 v72, v72\n"
    "v_exp_f32 v74, v74\n"
    "v_exp_f32 v73, v73\n"
    "v_exp_f32 v75, v75\n"
    "v_add_f32 v76, v72, v74\n"
    "v_add_f32 v77, v73, v75\n"
    "v_add_f32 v76, v76, v77\n"
    "s_nop 1\n"
    "v_add_f32_dpp v76, v76, v76 row_ror:8 row_mask:0xf bank_mask:0xf\n"
    "s_nop 1\n"
    "v_add_f32_dpp v76, v76, v76 row_ror:4 row_mask:0xf bank_mask:0xf\n"
    "s_nop 1\n"
    "v_add_f32_dpp v76, v76, v76 row_ror:2 row_mask:0xf bank_mask:0xf\n"
    "s_nop 1\n"
    "v_add_f32_dpp v76, v76, v76 row_ror:1 row_mask:0xf bank_mask:0xf\n"
    "v_rcp_f32 v76, v76\n"
    "s_nop 0\n"
    "v_pk_mul_f32 v[72:73], v[72:73], v[76:77] op_sel_hi:[1,0] clamp\n"
    "v_pk_mul_f32 v[74:75], v[74:75], v[76:77] op_sel_hi:[1,0] clamp\n"
    "v_cvt_pk_bf16_f32 v72, v72, v73\n"
    "v_cvt_pk_bf16_f32 v73, v74, v75\n"
    "buffer_store_dwordx2 v[72:73], v106, s[0:3], s48 offen sc1\n"
    ".Lqk_mk4x18_%=:\n"
    ".Lqk_bulk_%=:\n"
    "v_mfma_f32_16x16x32_bf16 v[0:3], a[80:83], v[32:35], 0\n"
    "ds_write_b64 v110, v[58:59] offset:224\n"
    "v_mfma_f32_16x16x32_bf16 v[4:7], a[80:83], v[36:39], 0\n"
    "ds_write_b64 v110, v[62:63] offset:128\n"
    ".Lqk_mkL0_%=:\n"
    "v_mfma_f32_16x16x32_bf16 v[8:11], a[160:163], v[32:35], 0\n"
    "v_mfma_f32_16x16x32_bf16 v[12:15], a[160:163], v[36:39], 0\n"
    "v_mfma_f32_16x16x32_bf16 v[16:19], a[0:3], v[32:35], 0\n"
    "v_mfma_f32_16x16x32_bf16 v[20:23], a[0:3], v[36:39], 0\n"
    "s_waitcnt lgkmcnt(0)\n"
    "s_barrier\n"
    "ds_read_b128 v[56:59], v105 offset:0\n"
    "v_mfma_f32_16x16x32_bf16 v[24:27], a[240:243], v[32:35], 0\n"
    "ds_read_b128 v[60:63], v105 offset:4352\n"
    "v_mfma_f32_16x16x32_bf16 v[28:31], a[240:243], v[36:39], 0\n"
    "v_mfma_f32_16x16x32_bf16 v[0:3], a[96:99], v[40:43], v[0:3]\n"
    "s_sub_u32 m0, s26, 4096\n"
    "ds_read_b128 v[32:35], v95 offset:0\n"
    "ds_read_b128 v[36:39], v95 offset:1024\n"
    "v_mfma_f32_16x16x32_bf16 v[4:7], a[96:99], v[44:47], v[4:7]\n"
    "ds_read_b128 v[64:67], v105 offset:8704\n"
    "v_mfma_f32_16x16x32_bf16 v[8:11], a[176:179], v[40:43], v[8:11]\n"
    "buffer_load_dwordx4 v96, s[0:3], s53 offen offset:2048 sc1 lds\n"
    "v_mfma_f32_16x16x32_bf16 v[12:15], a[176:179], v[44:47], v[12:15]\n"
    "ds_read_b128 v[68:71], v105 offset:13056\n"
    "v_mfma_f32_16x16x32_bf16 v[16:19], a[16:19], v[40:43], v[16:19]\n"
    "v_mfma_f32_16x16x32_bf16 v[20:23], a[16:19], v[44:47], v[20:23]\n"
    "v_mfma_f32_16x16x32_bf16 v[24:27], v[112:115], v[40:43], v[24:27]\n"
    "buffer_load_dwordx4 v96, s[0:3], s53 offen offset:3072 sc1 lds\n"
    "v_mfma_f32_16x16x32_bf16 v[28:31], v[112:115], v[44:47], v[28:31]\n"
    "s_mov_b32 m0, s59\n"
    "s_add_u32 s53, s53, s28\n"
    "s_waitcnt lgkmcnt(4)\n"
    "v_mfma_f32_16x16x32_bf16 v[0:3], a[112:115], v[48:51], v[0:3]\n"
    "ds_read_b128 v[40:43], v92 offset:2048\n"
    "ds_read_b128 v[44:47], v92 offset:3072\n"
    "v_mfma_f32_16x16x32_bf16 v[4:7], a[112:115], v[52:55], v[4:7]\n"
    "v_add_f32 v56, v56, v60\n"
    "v_mfma_f32_16x16x32_bf16 v[8:11], a[192:195], v[48:51], v[8:11]\n"
    "buffer_load_dwordx4 v96, s[0:3], s53 offen offset:0 sc1 lds\n"
    "v_mfma_f32_16x16x32_bf16 v[12:15], a[192:195], v[52:55], v[12:15]\n"
    "v_add_f32 v57, v57, v61\n"
    "v_add_f32 v58, v58, v62\n"
    "v_mfma_f32_16x16x32_bf16 v[16:19], a[32:35], v[48:51], v[16:19]\n"
    "v_add_f32 v59, v59, v63\n"
    "s_waitcnt lgkmcnt(3)\n"
    "v_mfma_f32_16x16x32_bf16 v[20:23], a[32:35], v[52:55], v[20:23]\n"
    "v_add_f32 v56, v56, v64\n"
    ".Lqk_mkL1_%=:\n"
    "v_mfma_f32_16x16x32_bf16 v[24:27], v[128:131], v[48:51], v[24:27]\n"
    "buffer_load_dwordx4 v96, s[0:3], s53 offen offset:1024 sc1 lds\n"
    "v_mfma_f32_16x16x32_bf16 v[28:31], v[128:131], v[52:55], v[28:31]\n"
    "s_waitcnt lgkmcnt(4)\n"
    "v_mov_b64 v[110:111], -1\n"
    "v_add_f32 v57, v57, v65\n"
    "v_mfma_f32_16x16x32_bf16 v[0:3], a[128:131], v[32:35], v[0:3]\n"
    "ds_read_b128 v[48:51], v93 offset:2048\n"
    "ds_read_b128 v[52:55], v93 offset:3072\n"
    "v_mfma_f32_16x16x32_bf16 v[4:7], a[128:131], v[36:39], v[4:7]\n"
    "v_add_f32 v58, v58, v66\n"
    "v_mfma_f32_16x16x32_bf16 v[8:11], a[208:211], v[32:35], v[8:11]\n"
    "buffer_load_dwordx4 v[80:83], v96, s[0:3], s54 offen sc1\n"
    "v_mfma_f32_16x16x32_bf16 v[12:15], a[208:211], v[36:39], v[12:15]\n"
    "v_add_f32 v59, v59, v67\n"
    "s_waitcnt lgkmcnt(4)\n"
    "v_mfma_f32_16x16x32_bf16 v[16:19], a[48:51], v[32:35], v[16:19]\n"
    "v_add_f32 v56, v56, v68\n"
    "v_add_f32 v57, v57, v69\n"
    "v_mfma_f32_16x16x32_bf16 v[20:23], a[48:51], v[36:39], v[20:23]\n"
    "v_add_f32 v58, v58, v70\n"
    "v_mfma_f32_16x16x32_bf16 v[24:27], v[144:147], v[32:35], v[24:27]\n"
    "buffer_load_dwordx4 v[84:87], v96, s[0:3], s54 offen offset:1024 sc1\n"
    "v_mfma_f32_16x16x32_bf16 v[28:31], v[144:147], v[36:39], v[28:31]\n"
    "s_waitcnt lgkmcnt(2)\n"
    "v_add_f32 v59, v59, v71\n"
    "s_barrier\n"
    "v_mfma_f32_16x16x32_bf16 v[0:3], a[84:87], v[40:43], v[0:3]\n"
    "ds_read_b128 v[32:35], v94 offset:2048\n"
    "ds_read_b128 v[36:39], v94 offset:3072\n"
    "v_mfma_f32_16x16x32_bf16 v[4:7], a[84:87], v[44:47], v[4:7]\n"
    "v_cndmask_b32 v56, v99, v56, s[12:13]\n"
    "v_cndmask_b32 v57, v99, v57, s[14:15]\n"
    "v_mfma_f32_16x16x32_bf16 v[8:11], a[164:167], v[40:43], v[8:11]\n"
    "buffer_store_dwordx2 v[110:111], v106, s[0:3], s49 offen sc1\n"
    "v_cndmask_b32 v58, v99, v58, s[16:17]\n"
    "v_mfma_f32_16x16x32_bf16 v[12:15], a[164:167], v[44:47], v[12:15]\n"
    "v_cndmask_b32 v59, v99, v59, s[18:19]\n"
    "v_max3_f32 v72, v56, v57, v58\n"
    "v_mfma_f32_16x16x32_bf16 v[16:19], a[4:7], v[40:43], v[16:19]\n"
    "v_max_f32 v72, v72, v59\n"
    "v_cndmask_b32 v74, v99, v72, s[70:71]\n"
    "v_mfma_f32_16x16x32_bf16 v[20:23], a[4:7], v[44:47], v[20:23]\n"
    "v_cndmask_b32 v75, v72, v99, s[70:71]\n"
    "v_max_f32_dpp v74, v74, v74 row_ror:8 row_mask:0xf bank_mask:0xf\n"
    "v_mfma_f32_16x16x32_bf16 v[24:27], a[244:247], v[40:43], v[24:27]\n"
    "v_max_f32_dpp v75, v75, v75 row_ror:8 row_mask:0xf bank_mask:0xf\n"
    "v_max_f32_dpp v74, v74, v74 row_ror:4 row_mask:0xf bank_mask:0xf\n"
    "v_mfma_f32_16x16x32_bf16 v[28:31], a[244:247], v[44:47], v[28:31]\n"
    "s_waitcnt lgkmcnt(2)\n"
    "v_max_f32_dpp v75, v75, v75 row_ror:4 row_mask:0xf bank_mask:0xf\n"
    "v_max_f32_dpp v74, v74, v74 row_ror:2 row_mask:0xf bank_mask:0xf\n"
    "v_mfma_f32_16x16x32_bf16 v[0:3], a[100:103], v[48:51], v[0:3]\n"
    "ds_read_b128 v[40:43], v95 offset:2048\n"
    "ds_read_b128 v[44:47], v95 offset:3072\n"
    "v_mfma_f32_16x16x32_bf16 v[4:7], a[100:103], v[52:55], v[4:7]\n"
    "v_max_f32_dpp v75, v75, v75 row_ror:2 row_mask:0xf bank_mask:0xf\n"
    "v_max_f32_dpp v74, v74, v74 row_ror:1 row_mask:0xf bank_mask:0xf\n"
    "v_mfma_f32_16x16x32_bf16 v[8:11], a[180:183], v[48:51], v[8:11]\n"
    "buffer_store_dwordx2 v[110:111], v107, s[0:3], s49 offen sc1\n"
    "v_max_f32_dpp v75, v75, v75 row_ror:1 row_mask:0xf bank_mask:0xf\n"
    ".Lqk_mkL2_%=:\n"
    "v_mfma_f32_16x16x32_bf16 v[12:15], a[180:183], v[52:55], v[12:15]\n"
    "v_cndmask_b32 v72, v75, v74, s[70:71]\n"
    "v_mul_f32 v72, s66, v72\n"
    "v_mfma_f32_16x16x32_bf16 v[16:19], a[20:23], v[48:51], v[16:19]\n"
    "v_fma_f32 v56, v56, s66, -v72\n"
    "v_fma_f32 v57, v57, s66, -v72\n"
    "v_mfma_f32_16x16x32_bf16 v[20:23], a[20:23], v[52:55], v[20:23]\n"
    "v_fma_f32 v58, v58, s66, -v72\n"
    "v_fma_f32 v59, v59, s66, -v72\n"
    "v_mfma_f32_16x16x32_bf16 v[24:27], v[116:119], v[48:51], v[24:27]\n"
    "v_exp_f32 v56, v56\n"
    "v_mfma_f32_16x16x32_bf16 v[28:31], v[116:119], v[52:55], v[28:31]\n"
    "s_waitcnt lgkmcnt(2)\n"
    "v_exp_f32 v58, v58\n"
    "v_mfma_f32_16x16x32_bf16 v[0:3], a[116:119], v[32:35], v[0:3]\n"
    "ds_read_b128 v[48:51], v92 offset:4096\n"
    "ds_read_b128 v[52:55], v92 offset:5120\n"
    "v_mfma_f32_16x16x32_bf16 v[4:7], a[116:119], v[36:39], v[4:7]\n"
    "v_mfma_f32_16x16x32_bf16 v[8:11], a[196:199], v[32:35], v[8:11]\n"
    "buffer_load_dwordx2 v[108:109], off, s[0:3], s55 sc1\n"
    "v_mfma_f32_16x16x32_bf16 v[12:15], a[196:199], v[36:39], v[12:15]\n"
    "v_exp_f32 v57, v57\n"
    "v_mfma_f32_16x16x32_bf16 v[16:19], a[36:39], v[32:35], v[16:19]\n"
    "v_exp_f32 v59, v59\n"
    "v_mfma_f32_16x16x32_bf16 v[20:23], a[36:39], v[36:39], v[20:23]\n"
    "v_add_f32 v72, v56, v58\n"
    "v_add_f32 v73, v57, v59\n"
    "v_mfma_f32_16x16x32_bf16 v[24:27], v[132:135], v[32:35], v[24:27]\n"
    "v_add_f32 v72, v72, v73\n"
    "v_cndmask_b32 v74, 0, v72, s[70:71]\n"
    "v_mfma_f32_16x16x32_bf16 v[28:31], v[132:135], v[36:39], v[28:31]\n"
    "s_waitcnt lgkmcnt(2)\n"
    "v_cndmask_b32 v75, v72, 0, s[70:71]\n"
    "v_add_f32_dpp v74, v74, v74 row_ror:8 row_mask:0xf bank_mask:0xf\n"
    "v_mfma_f32_16x16x32_bf16 v[0:3], a[132:135], v[40:43], v[0:3]\n"
    "ds_read_b128 v[32:35], v93 offset:4096\n"
    "ds_read_b128 v[36:39], v93 offset:5120\n"
    "v_mfma_f32_16x16x32_bf16 v[4:7], a[132:135], v[44:47], v[4:7]\n"
    "v_add_f32_dpp v75, v75, v75 row_ror:8 row_mask:0xf bank_mask:0xf\n"
    "v_add_f32_dpp v74, v74, v74 row_ror:4 row_mask:0xf bank_mask:0xf\n"
    "v_mfma_f32_16x16x32_bf16 v[8:11], a[212:215], v[40:43], v[8:11]\n"
    "v_add_f32_dpp v75, v75, v75 row_ror:4 row_mask:0xf bank_mask:0xf\n"
    "v_add_f32_dpp v74, v74, v74 row_ror:2 row_mask:0xf bank_mask:0xf\n"
    "v_mfma_f32_16x16x32_bf16 v[12:15], a[212:215], v[44:47], v[12:15]\n"
    "v_add_f32_dpp v75, v75, v75 row_ror:2 row_mask:0xf bank_mask:0xf\n"
    "v_add_f32_dpp v74, v74, v74 row_ror:1 row_mask:0xf bank_mask:0xf\n"
    "v_mfma_f32_16x16x32_bf16 v[16:19], a[52:55], v[40:43], v[16:19]\n"
    "v_add_f32_dpp v75, v75, v75 row_ror:1 row_mask:0xf bank_mask:0xf\n"
    "v_cndmask_b32 v72, v75, v74, s[70:71]\n"
    "v_mfma_f32_16x16x32_bf16 v[20:23], a[52:55], v[44:47], v[20:23]\n"
    "v_rcp_f32 v72, v72\n"
    "v_mfma_f32_16x16x32_bf16 v[24:27], v[148:151], v[40:43], v[24:27]\n"
    "v_mul_f32 v56, v56, v72 clamp\n"
    "v_mul_f32 v57, v57, v72 clamp\n"
    "v_mfma_f32_16x16x32_bf16 v[28:31], v[148:151], v[44:47], v[28:31]\n"
    "s_waitcnt lgkmcnt(2)\n"
    "v_mul_f32 v58, v58, v72 clamp\n"
    "v_mul_f32 v59, v59, v72 clamp\n"
    "v_mfma_f32_16x16x32_bf16 v[0:3], a[88:91], v[48:51], v[0:3]\n"
    "ds_read_b128 v[40:43], v94 offset:4096\n"
    "ds_read_b128 v[44:47], v94 offset:5120\n"
    "v_mfma_f32_16x16x32_bf16 v[4:7], a[88:91], v[52:55], v[4:7]\n"
    "v_cvt_pk_bf16_f32 v56, v56, v57\n"
    "v_cvt_pk_bf16_f32 v57, v58, v59\n"
    "v_mfma_f32_16x16x32_bf16 v[8:11], a[168:171], v[48:51], v[8:11]\n"
    "buffer_store_dwordx2 v[56:57], v107, s[0:3], s50 offen sc1\n"
    ".Lqk_mkL3_%=:\n"
    "v_mfma_f32_16x16x32_bf16 v[12:15], a[168:171], v[52:55], v[12:15]\n"
    "s_cmp_eq_u32 s46, 63\n"
    "s_cbranch_scc1 .Lqk_end_%=\n"
    "v_mfma_f32_16x16x32_bf16 v[16:19], a[8:11], v[48:51], v[16:19]\n"
    "s_add_u32 s46, s46, 1\n"
    "s_add_u32 s60, s46, 1\n"
    "s_lshl_b32 s53, s60, 19\n"
    "v_mfma_f32_16x16x32_bf16 v[20:23], a[8:11], v[52:55], v[20:23]\n"
    "s_lshl_b32 s45, s60, 11\n"
    "s_xor_b32 s45, s45, s23\n"
    "s_and_b32 s45, s45, 0x2000\n"
    "v_mfma_f32_16x16x32_bf16 v[24:27], a[248:251], v[48:51], v[24:27]\n"
    "s_add_u32 s53, s53, s45\n"
    "s_add_u32 s53, s53, s27\n"
    "s_add_u32 s48, s46, 0\n"
    "v_mfma_f32_16x16x32_bf16 v[28:31], a[248:251], v[52:55], v[28:31]\n"
    "s_waitcnt lgkmcnt(2)\n"
    "s_and_b32 s48, s48, 3\n"
    "s_lshl_b32 s48, s48, 22\n"
    "v_mfma_f32_16x16x32_bf16 v[0:3], a[104:107], v[32:35], v[0:3]\n"
    "s_add_u32 s49, s46, 2\n"
    "ds_read_b128 v[48:51], v95 offset:4096\n"
    "ds_read_b128 v[52:55], v95 offset:5120\n"
    "v_mfma_f32_16x16x32_bf16 v[4:7], a[104:107], v[36:39], v[4:7]\n"
    "s_and_b32 s49, s49, 3\n"
    "s_lshl_b32 s49, s49, 22\n"
    "s_and_b32 s55, s46, 63\n"
    "v_mfma_f32_16x16x32_bf16 v[8:11], a[184:187], v[32:35], v[8:11]\n"
    "s_lshl_b32 s55, s55, 7\n"
    "s_mov_b32 s50, s48\n"
    "s_add_u32 s51, s24, s48\n"
    "v_mfma_f32_16x16x32_bf16 v[12:15], a[184:187], v[36:39], v[12:15]\n"
    "s_add_u32 s52, s25, s48\n"
    "s_add_u32 s56, s30, s55\n"
    "s_add_u32 s55, s31, s55\n"
    "v_mfma_f32_16x16x32_bf16 v[16:19], a[24:27], v[32:35], v[16:19]\n"
    "s_lshl_b32 s54, s60, 20\n"
    "s_lshl_b32 s45, s60, 12\n"
    "s_xor_b32 s45, s45, s23\n"
    "v_mfma_f32_16x16x32_bf16 v[20:23], a[24:27], v[36:39], v[20:23]\n"
    "s_and_b32 s45, s45, 0x2000\n"
    "s_add_u32 s54, s54, s45\n"
    "s_add_u32 s54, s54, s29\n"
    "v_mfma_f32_16x16x32_bf16 v[24:27], v[120:123], v[32:35], v[24:27]\n"
    "s_cmp_gt_i32 s60, 63\n"
    "s_cselect_b32 s53, 2130706432, s53\n"
    "s_cselect_b32 s54, 2130706432, s54\n"
    "v_mfma_f32_16x16x32_bf16 v[28:31], v[120:123], v[36:39], v[28:31]\n"
    "s_waitcnt lgkmcnt(2)\n"
    "s_waitcnt vmcnt(6) lgkmcnt(0)\n"
    "s_barrier\n"
    "v_mfma_f32_16x16x32_bf16 v[0:3], a[120:123], v[40:43], v[0:3]\n"
    "s_add_i32 s62, s62, s63\n"
    "ds_read_b128 v[32:35], v92 offset:6144\n"
    "ds_read_b128 v[36:39], v92 offset:7168\n"
    "v_mfma_f32_16x16x32_bf16 v[4:7], a[120:123], v[44:47], v[4:7]\n"
    "s_max_i32 s62, s62, 0\n"
    "s_min_i32 s62, s62, 28\n"
    "v_mfma_f32_16x16x32_bf16 v[8:11], a[200:203], v[40:43], v[8:11]\n"
    "s_cmp_lt_i32 s46, 3\n"
    "s_cselect_b32 s62, 16, s62\n"
    "s_add_u32 s64, s62, 7\n"
    ".Lqk_mkL4_%=:\n"
    "v_mfma_f32_16x16x32_bf16 v[12:15], a[200:203], v[44:47], v[12:15]\n"
    "s_lshr_b32 s64, s64, 3\n"
    "s_min_u32 s64, s64, 3\n"
    "s_lshl_b32 s64, s64, 2\n"
    "v_mfma_f32_16x16x32_bf16 v[16:19], a[40:43], v[40:43], v[16:19]\n"
    "s_sub_u32 m0, s26, 8192\n"
    "v_mfma_f32_16x16x32_bf16 v[20:23], a[40:43], v[44:47], v[20:23]\n"
    "buffer_load_dwordx4 v96, s[0:3], s53 offen offset:0 sc1 lds\n"
    "v_mfma_f32_16x16x32_bf16 v[24:27], v[136:139], v[40:43], v[24:27]\n"
    "v_mfma_f32_16x16x32_bf16 v[28:31], v[136:139], v[44:47], v[28:31]\n"
    "s_waitcnt lgkmcnt(2)\n"
    "v_mfma_f32_16x16x32_bf16 v[0:3], a[136:139], v[48:51], v[0:3]\n"
    "ds_read_b128 v[40:43], v93 offset:6144\n"
    "ds_read_b128 v[44:47], v93 offset:7168\n"
    "v_mfma_f32_16x16x32_bf16 v[4:7], a[136:139], v[52:55], v[4:7]\n"
    "buffer_load_dwordx4 v96, s[0:3], s53 offen offset:1024 sc1 lds\n"
    "v_mfma_f32_16x16x32_bf16 v[8:11], a[216:219], v[48:51], v[8:11]\n"
    "v_mfma_f32_16x16x32_bf16 v[12:15], a[216:219], v[52:55], v[12:15]\n"
    "v_mfma_f32_16x16x32_bf16 v[16:19], a[56:59], v[48:51], v[16:19]\n"
    "v_mfma_f32_16x16x32_bf16 v[20:23], a[56:59], v[52:55], v[20:23]\n"
    "buffer_load_dwordx4 v96, s[0:3], s53 offen offset:2048 sc1 lds\n"
    "v_mfma_f32_16x16x32_bf16 v[24:27], v[152:155], v[48:51], v[24:27]\n"
    "v_mfma_f32_16x16x32_bf16 v[28:31], v[152:155], v[52:55], v[28:31]\n"
    "s_waitcnt lgkmcnt(2)\n"
    "v_mfma_f32_16x16x32_bf16 v[0:3], a[92:95], v[32:35], v[0:3]\n"
    "ds_read_b128 v[48:51], v94 offset:6144\n"
    "ds_read_b128 v[52:55], v94 offset:7168\n"
    "v_mfma_f32_16x16x32_bf16 v[4:7], a[92:95], v[36:39], v[4:7]\n"
    "buffer_load_dwordx4 v96, s[0:3], s53 offen offset:3072 sc1 lds\n"
    "v_mfma_f32_16x16x32_bf16 v[8:11], a[172:175], v[32:35], v[8:11]\n"
    "s_sub_u32 m0, s26, 4096\n"
    "s_add_u32 s53, s53, 4096\n"
    "v_mfma_f32_16x16x32_bf16 v[12:15], a[172:175], v[36:39], v[12:15]\n"
    "v_mfma_f32_16x16x32_bf16 v[16:19], a[12:15], v[32:35], v[16:19]\n"
    "v_mfma_f32_16x16x32_bf16 v[20:23], a[12:15], v[36:39], v[20:23]\n"
    "buffer_load_dwordx4 v96, s[0:3], s53 offen offset:0 sc1 lds\n"
    "v_mfma_f32_16x16x32_bf16 v[24:27], a[252:255], v[32:35], v[24:27]\n"
    "v_mfma_f32_16x16x32_bf16 v[28:31], a[252:255], v[36:39], v[28:31]\n"
    "s_waitcnt lgkmcnt(2)\n"
    "v_mfma_f32_16x16x32_bf16 v[0:3], a[108:111], v[40:43], v[0:3]\n"
    "ds_read_b128 v[32:35], v95 offset:6144\n"
    "ds_read_b128 v[36:39], v95 offset:7168\n"
    "v_mfma_f32_16x16x32_bf16 v[4:7], a[108:111], v[44:47], v[4:7]\n"
    "buffer_load_dwordx4 v96, s[0:3], s53 offen offset:1024 sc1 lds\n"
    "v_mfma_f32_16x16x32_bf16 v[8:11], a[188:191], v[40:43], v[8:11]\n"
    ".Lqk_mkL5_%=:\n"
    "v_mfma_f32_16x16x32_bf16 v[12:15], a[188:191], v[44:47], v[12:15]\n"
    "v_mfma_f32_16x16x32_bf16 v[16:19], a[28:31], v[40:43], v[16:19]\n"
    "v_mfma_f32_16x16x32_bf16 v[20:23], a[28:31], v[44:47], v[20:23]\n"
    "v_mfma_f32_16x16x32_bf16 v[24:27], v[124:127], v[40:43], v[24:27]\n"
    "v_mfma_f32_16x16x32_bf16 v[28:31], v[124:127], v[44:47], v[28:31]\n"
    "s_waitcnt lgkmcnt(2)\n"
    "v_mfma_f32_16x16x32_bf16 v[0:3], a[124:127], v[48:51], v[0:3]\n"
    "ds_read_b128 v[40:43], v96 offset:45056\n"
    "ds_read_b128 v[44:47], v96 offset:46080\n"
    "v_mfma_f32_16x16x32_bf16 v[4:7], a[124:127], v[52:55], v[4:7]\n"
    "v_mfma_f32_16x16x32_bf16 v[8:11], a[204:207], v[48:51], v[8:11]\n"
    "v_mfma_f32_16x16x32_bf16 v[12:15], a[204:207], v[52:55], v[12:15]\n"
    "v_mfma_f32_16x16x32_bf16 v[16:19], a[44:47], v[48:51], v[16:19]\n"
    "v_mfma_f32_16x16x32_bf16 v[20:23], a[44:47], v[52:55], v[20:23]\n"
    "v_mfma_f32_16x16x32_bf16 v[24:27], v[140:143], v[48:51], v[24:27]\n"
    "v_mfma_f32_16x16x32_bf16 v[28:31], v[140:143], v[52:55], v[28:31]\n"
    "s_waitcnt lgkmcnt(2)\n"
    "v_mfma_f32_16x16x32_bf16 v[0:3], a[140:143], v[32:35], v[0:3]\n"
    "ds_read_b128 v[48:51], v96 offset:47104\n"
    "ds_read_b128 v[52:55], v96 offset:48128\n"
    "v_mfma_f32_16x16x32_bf16 v[4:7], a[140:143], v[36:39], v[4:7]\n"
    "v_mfma_f32_16x16x32_bf16 v[8:11], a[220:223], v[32:35], v[8:11]\n"
    "v_mfma_f32_16x16x32_bf16 v[12:15], a[220:223], v[36:39], v[12:15]\n"
    "v_mfma_f32_16x16x32_bf16 v[16:19], a[60:63], v[32:35], v[16:19]\n"
    "v_mfma_f32_16x16x32_bf16 v[20:23], a[60:63], v[36:39], v[20:23]\n"
    "v_mfma_f32_16x16x32_bf16 v[24:27], v[156:159], v[32:35], v[24:27]\n"
    "v_mfma_f32_16x16x32_bf16 v[28:31], v[156:159], v[36:39], v[28:31]\n"
    "s_waitcnt lgkmcnt(2)\n"
    "v_mfma_f32_16x16x32_bf16 v[0:3], a[144:147], v[40:43], v[0:3]\n"
    "s_waitcnt vmcnt(7)\n"
    "ds_read_b128 v[32:35], v96 offset:49152\n"
    "ds_read_b128 v[36:39], v96 offset:50176\n"
    "s_memrealtime s[74:75]\n"
    "v_mfma_f32_16x16x32_bf16 v[4:7], a[144:147], v[44:47], v[4:7]\n"
    "v_readfirstlane_b32 s44, v108\n"
    "v_readfirstlane_b32 s45, v109\n"
    "v_mfma_f32_16x16x32_bf16 v[8:11], a[224:227], v[40:43], v[8:11]\n"
    "v_mfma_f32_16x16x32_bf16 v[12:15], a[224:227], v[44:47], v[12:15]\n"
    "s_cmp_eq_u32 s45, s47\n"
    "s_cselect_b32 s57, s44, s57\n"
    "s_add_u32 s57, s57, 256\n"
    "v_mfma_f32_16x16x32_bf16 v[16:19], a[64:67], v[40:43], v[16:19]\n"
    "s_sub_u32 s58, s57, 59\n"
    "s_mov_b32 s47, s60\n"
    "s_mov_b32 m0, s26\n"
    "v_mfma_f32_16x16x32_bf16 v[20:23], a[64:67], v[44:47], v[20:23]\n"
    ".Lqk_mkL6_%=:\n"
    "v_mfma_f32_16x16x32_bf16 v[24:27], v[160:163], v[40:43], v[24:27]\n"
    "s_sub_u32 s76, s58, s74\n"
    "v_mfma_f32_16x16x32_bf16 v[28:31], v[160:163], v[44:47], v[28:31]\n"
    "s_waitcnt lgkmcnt(2)\n"
    ".Lqk_mk6x19_%=:\n"
    "s_cmp_gt_i32 s76, 4\n"
    "s_cbranch_scc1 .Lqk_early_%=\n"
    "buffer_load_dwordx4 v96, s[0:3], s51 offen sc1 lds\n"
    "buffer_load_dwordx4 v96, s[0:3], s51 offen offset:1024 sc1 lds\n"
    ".Lqk_asked_%=:\n"
    ".Lqk_mk7x21_%=:\n"
    "v_mfma_f32_16x16x32_bf16 v[0:3], a[148:151], v[48:51], v[0:3]\n"
    "ds_read_b128 v[40:43], v96 offset:51200\n"
    "ds_read_b128 v[44:47], v96 offset:52224\n"
    "v_mfma_f32_16x16x32_bf16 v[4:7], a[148:151], v[52:55], v[4:7]\n"
    ".Lqk_mkM0_%=:\n"
    "v_mfma_f32_16x16x32_bf16 v[8:11], a[228:231], v[48:51], v[8:11]\n"
    "v_mfma_f32_16x16x32_bf16 v[12:15], a[228:231], v[52:55], v[12:15]\n"
    "v_mfma_f32_16x16x32_bf16 v[16:19], a[68:71], v[48:51], v[16:19]\n"
    "v_mfma_f32_16x16x32_bf16 v[20:23], a[68:71], v[52:55], v[20:23]\n"
    ".Lqk_mkM1_%=:\n"
    "v_mfma_f32_16x16x32_bf16 v[24:27], v[164:167], v[48:51], v[24:27]\n"
    "v_mfma_f32_16x16x32_bf16 v[28:31], v[164:167], v[52:55], v[28:31]\n"
    "s_waitcnt lgkmcnt(2)\n"
    "v_mfma_f32_16x16x32_bf16 v[0:3], a[152:155], v[32:35], v[0:3]\n"
    "buffer_load_dwordx4 v96, s[0:3], s52 offen offset:2048 sc1 lds\n"
    "v_mfma_f32_16x16x32_bf16 v[4:7], a[152:155], v[36:39], v[4:7]\n"
    "v_mfma_f32_16x16x32_bf16 v[8:11], a[232:235], v[32:35], v[8:11]\n"
    ".Lqk_mkM2_%=:\n"
    "v_mfma_f32_16x16x32_bf16 v[12:15], a[232:235], v[36:39], v[12:15]\n"
    "v_mfma_f32_16x16x32_bf16 v[16:19], a[72:75], v[32:35], v[16:19]\n"
    "v_mfma_f32_16x16x32_bf16 v[20:23], a[72:75], v[36:39], v[20:23]\n"
    "v_mfma_f32_16x16x32_bf16 v[24:27], v[168:171], v[32:35], v[24:27]\n"
    ".Lqk_mkM3_%=:\n"
    "v_mfma_f32_16x16x32_bf16 v[28:31], v[168:171], v[36:39], v[28:31]\n"
    "s_waitcnt lgkmcnt(0)\n"
    "v_mfma_f32_16x16x32_bf16 v[0:3], a[156:159], v[40:43], v[0:3]\n"
    "v_mfma_f32_16x16x32_bf16 v[4:7], a[156:159], v[44:47], v[4:7]\n"
    "v_mfma_f32_16x16x32_bf16 v[8:11], a[236:239], v[40:43], v[8:11]\n"
    "v_mfma_f32_16x16x32_bf16 v[12:15], a[236:239], v[44:47], v[12:15]\n"
    "v_mfma_f32_16x16x32_bf16 v[16:19], a[76:79], v[40:43], v[16:19]\n"
    "v_mfma_f32_16x16x32_bf16 v[20:23], a[76:79], v[44:47], v[20:23]\n"
    "v_mfma_f32_16x16x32_bf16 v[24:27], v[172:175], v[40:43], v[24:27]\n"
    ".Lqk_mkM4_%=:\n"
    "v_mfma_f32_16x16x32_bf16 v[28:31], v[172:175], v[44:47], v[28:31]\n"
    "ds_read_b128 v[32:35], v97 offset:0\n"
    "ds_read_b128 v[36:39], v97 offset:1024\n"
    "ds_read_b128 v[40:43], v97 offset:2048\n"
    "ds_read_b128 v[44:47], v97 offset:3072\n"
    "ds_read_b128 v[48:51], v97 offset:4096\n"
    "ds_read_b128 v[52:55], v97 offset:5120\n"
    "v_cvt_pk_bf16_f32 v68, v0, v1\n"
    "v_cvt_pk_bf16_f32 v69, v2, v3\n"
    "v_cvt_pk_bf16_f32 v70, v8, v9\n"
    "v_cvt_pk_bf16_f32 v71, v10, v11\n"
    "v_cvt_pk_bf16_f32 v72, v4, v5\n"
    "v_cvt_pk_bf16_f32 v73, v6, v7\n"
    "v_cvt_pk_bf16_f32 v74, v12, v13\n"
    "v_cvt_pk_bf16_f32 v75, v14, v15\n"
    "v_mfma_f32_16x16x32_bf16 v[108:111], v[68:71], v[84:87], 0\n"
    "v_cvt_pk_bf16_f32 v88, v24, v25\n"
    "v_cvt_pk_bf16_f32 v89, v26, v27\n"
    "v_cvt_pk_bf16_f32 v90, v28, v29\n"
    "v_cvt_pk_bf16_f32 v91, v30, v31\n"
    "v_mfma_f32_16x16x32_bf16 v[24:27], v[72:75], v[84:87], 0\n"
    "v_cvt_pk_bf16_f32 v76, v16, v17\n"
    "v_cvt_pk_bf16_f32 v77, v18, v19\n"
    "v_cvt_pk_bf16_f32 v78, v20, v21\n"
    "v_cvt_pk_bf16_f32 v79, v22, v23\n"
    "v_mfma_f32_16x16x32_bf16 v[28:31], v[68:71], v[80:83], 0\n"
    "v_mov_b64 v[0:1], 0\n"
    "v_mov_b64 v[2:3], 0\n"
    "v_mfma_f32_16x16x16_bf16 v[108:111], v[76:77], v[90:91], v[108:111]\n"
    "v_mov_b64 v[4:5], 0\n"
    "v_mov_b64 v[6:7], 0\n"
    "v_mfma_f32_16x16x16_bf16 v[24:27], v[78:79], v[90:91], v[24:27]\n"
    "v_mov_b64 v[8:9], 0\n"
    "v_mov_b64 v[10:11], 0\n"
    "v_mfma_f32_16x16x16_bf16 v[28:31], v[76:77], v[88:89], v[28:31]\n"
    "v_mov_b64 v[12:13], 0\n"
    "v_mov_b64 v[14:15], 0\n"
    "v_mov_b64 v[16:17], 0\n"
    "v_mov_b64 v[18:19], 0\n"
    "v_mov_b64 v[20:21], 0\n"
    "v_mov_b64 v[22:23], 0\n"
    "ds_write_b128 v104, v[108:111]\n"
    "ds_write_b128 v104, v[24:27] offset:64\n"
    "ds_write_b128 v104, v[28:31] offset:160\n"
    ".Lqk_mk8x22_%=:\n"
    "s_branch .Lqk_take_%=\n"
    ".Lqk_tk0_%=:\n"
    ".Lqk_po3_%=:\n"
    "buffer_load_dword v109, v108, s[0:3], 0 offen sc1\n"
    "s_waitcnt vmcnt(0)\n"
    "v_cmp_gt_i32 vcc, 32, v109\n"
    "s_nop 0\n"
    "s_cbranch_vccz .Lqk_pd4_%=\n"
    "s_add_u32 s40, s40, 1\n"
    "s_cmp_lt_u32 s40, 0x40000\n"
    "s_cbranch_scc1 .Lqk_sp5_%=\n"
    "v_mov_b32 v109, 90\n"
    "s_mov_b32 s44, 33728\n"
    "buffer_store_dword v109, off, s[0:3], s44 sc1\n"
    "s_waitcnt vmcnt(0)\n"
    "s_branch .Lqk_pd4_%=\n"
    ".Lqk_sp5_%=:\n"
    "s_sleep 8\n"
    "s_branch .Lqk_po3_%=\n"
    ".Lqk_pd4_%=:\n"
    "s_branch .Lqk_tk1_%=\n"
    ".Lqk_early_%=:\n"
    ".Lqk_rt21_%=:\n"
    "s_memrealtime s[42:43]\n"
    "s_waitcnt lgkmcnt(0)\n"
    "s_sub_u32 s44, s58, s42\n"
    "s_cmp_lt_u32 s44, 600\n"
    "s_cbranch_scc1 .Lqk_rt21_%=\n"
    "buffer_load_dwordx4 v96, s[0:3], s51 offen sc1 lds\n"
    "buffer_load_dwordx4 v96, s[0:3], s51 offen offset:1024 sc1 lds\n"
    "s_branch .Lqk_asked_%=\n"
    ".Lqk_retry_%=:\n"
    "s_add_u32 s40, s40, 1\n"
    "s_cmp_lt_u32 s40, 0x40000\n"
    "s_cbranch_scc1 .Lqk_sp24_%=\n"
    "v_mov_b32 v108, 1\n"
    "s_mov_b32 s44, 33728\n"
    "buffer_store_dword v108, off, s[0:3], s44 sc1\n"
    "s_waitcnt vmcnt(0)\n"
    "s_branch .Lqk_go_%=\n"
    ".Lqk_sp24_%=:\n"
    "s_waitcnt lgkmcnt(0)\n"
    "s_nop 7\n"
    "ds_read_b128 v[32:35], v97 offset:0\n"
    "ds_read_b128 v[36:39], v97 offset:1024\n"
    "v_mov_b64 v[0:1], 0\n"
    "v_mov_b64 v[2:3], 0\n"
    "v_mov_b64 v[4:5], 0\n"
    "v_mov_b64 v[6:7], 0\n"
    "v_mov_b64 v[8:9], 0\n"
    "v_mov_b64 v[10:11], 0\n"
    "v_mov_b64 v[12:13], 0\n"
    "v_mov_b64 v[14:15], 0\n"
    "v_mov_b64 v[16:17], 0\n"
    "v_mov_b64 v[18:19], 0\n"
    "v_mov_b64 v[20:21], 0\n"
    "v_mov_b64 v[22:23], 0\n"
    "buffer_load_dwordx4 v96, s[0:3], s51 offen sc1 lds\n"
    "buffer_load_dwordx4 v96, s[0:3], s51 offen offset:1024 sc1 lds\n"
    "buffer_load_dwordx4 v96, s[0:3], s52 offen offset:2048 sc1 lds\n"
    ".Lqk_r1_%=:\n"
    "s_waitcnt vmcnt(0)\n"
    "s_branch .Lqk_take_%=\n"
    ".Lqk_r2_%=:\n"
    "s_add_u32 s40, s40, 1\n"
    "s_cmp_lt_u32 s40, 0x40000\n"
    "s_cbranch_scc1 .Lqk_sp25_%=\n"
    "v_mov_b32 v108, 5\n"
    "s_mov_b32 s44, 33728\n"
    "buffer_store_dword v108, off, s[0:3], s44 sc1\n"
    "s_waitcnt vmcnt(0)\n"
    "s_branch .Lqk_go2_%=\n"
    ".Lqk_sp25_%=:\n"
    "buffer_load_dwordx4 v96, s[0:3], s52 offen offset:2048 sc1 lds\n"
    ".Lqk_r3_%=:\n"
    "s_waitcnt vmcnt(0)\n"
    "ds_read_b128 v[64:67], v92 offset:10240\n"
    "s_waitcnt lgkmcnt(0)\n"
    "v_max_u32 v108, v64, v66\n"
    "v_cmp_eq_u32 vcc, -1, v108\n"
    "s_nop 0\n"
    "s_cbranch_vccnz .Lqk_r2_%=\n"
    "s_branch .Lqk_go2_%=\n"
    ".Lqk_end_%=:\n"
    "s_waitcnt vmcnt(0) lgkmcnt(0)\n"
    "s_endpgm\n"
    :: "{s[0:3]}"(s0_), "{s[4:7]}"(s4_), "{s[8:11]}"(s8_), "{s[12:15]}"(s12_), "{s[16:19]}"(s16_), "{s[20:23]}"(s20_), "{s[24:27]}"(s24_), "{s[28:31]}"(s28_), "{s[32:35]}"(s32_), "{s[36:39]}"(s36_), "{v[0:3]}"(v0_), "{v[4:7]}"(v4_), "{v[92:95]}"(v92_), "{v[96:99]}"(v96_), "{v[100:103]}"(v100_), "{v[104:107]}"(v104_) : "memory", "v255", "a255", "s95");
    __builtin_unreachable();
}
DEV void vo_role(const MegaArgs& A, const int L, const int h, const int rep) {
    ROLE_HEAD;
    const unsigned um = xl(RG_UM + ((8 * w + lq) * 4 + (h >> 3)) * 2048 + (h & 7) * 256 + l16 * 16, L);
#define XU 0
    const unsigned umx = XU ? OOB : xl(RG_UMX + ((4 * (2 * w + (lq & 1)) + (l16 >> 2)) * 4 + (h >> 3)) * 512 + (h & 7) * 64 + (l & 3) * 16 + (lq >> 1) * 8, L);
    const u32x4 s0_ = {RFL(rx[0]), RFL(rx[1]), RFL(rx[2]), RFL(rx[3])};
    const u32x4 s4_ = {RFL(lo(A.Wo + h * 192 * 640)), RFL(hi(A.Wo + h * 192 * 640)), RFL(192u * 1280u), RFL(0x20000u)};
    const u32x4 s8_ = {RFL(lo(A.Wv + h * 192)), RFL(hi(A.Wv + h * 192)), RFL(WBYTES - h * 384), RFL(0x20000u)};
    const u32x4 s12_ = {RFL(w), RFL(L << 13), RFL(L), RFL(xl(RG_MEM + rep * 16384, L) + w * 2048)};
    const u32x4 s16_ = {RFL(w < 2 ? xl(RG_MEM + rep * 16384 + 8192, L) + w * 1024 - 2048 : OOB), RFL(w * 13312 + 8192), RFL(REC0 + w * 16384), RFL(65536 - 4096 - w * 14336)};
    const u32x4 s20_ = {RFL(QT0 + h * 32768 + 16384 + w * 1024), RFL(146432 + w * 1024), RFL(xl(RG_UT + (32 * w + (h >> 3)) * 8192 + (h & 7) * 1024, L)), RFL(xl(Z_X + 8192, L))};
    const u32x4 s24_ = {RFL(xl(Z_CT + (h & 7) * 256, L)), RFL(w == 0 ? xl(Z_QV + (32 + h) * 4, L) : OOB), RFL(DBG0 + ((L * 128 + 32 + h) * 4 + w) * 2048), RFL(0u)};
    const u32x4 v0_ = {(unsigned)(((3 * w + 0) * 64 + l) / 24 * 12288 + ((((3 * w + 0) * 64 + l) % 24) ^ ((((3 * w + 0) * 64 + l) / 48) & 6)) * 16 - 0), (unsigned)(((3 * w + 1) * 64 + l) / 24 * 12288 + ((((3 * w + 1) * 64 + l) % 24) ^ ((((3 * w + 1) * 64 + l) / 48) & 6)) * 16 - 1024), (unsigned)(((3 * w + 2) * 64 + l) / 24 * 12288 + ((((3 * w + 2) * 64 + l) % 24) ^ ((((3 * w + 2) * 64 + l) / 48) & 6)) * 16 - 2048), (unsigned)((8 * lq + ((l >> 2) & 1) + ((l >> 1) & 4)) * 384 + (w ^ (((8 * lq + ((l >> 2) & 1) + ((l >> 1) & 4)) >> 2) & 3)) * 32 + (l & 3) * 8 + ((w + 0) & 3) * 12288)};
    const u32x4 v4_ = {(unsigned)((8 * lq + ((l >> 2) & 1) + ((l >> 1) & 4)) * 384 + (w ^ (((8 * lq + ((l >> 2) & 1) + ((l >> 1) & 4)) >> 2) & 3)) * 32 + (l & 3) * 8 + ((w + 1) & 3) * 12288), (unsigned)((8 * lq + ((l >> 2) & 1) + ((l >> 1) & 4)) * 384 + (w ^ (((8 * lq + ((l >> 2) & 1) + ((l >> 1) & 4)) >> 2) & 3)) * 32 + (l & 3) * 8 + ((w + 2) & 3) * 12288), (unsigned)((8 * lq + ((l >> 2) & 1) + ((l >> 1) & 4)) * 384 + (w ^ (((8 * lq + ((l >> 2) & 1) + ((l >> 1) & 4)) >> 2) & 3)) * 32 + (l & 3) * 8 + ((w + 3) & 3) * 12288), (unsigned)((8 * lq + ((l >> 2) & 1) + ((l >> 1) & 4)) * 384 + (w ^ (((8 * lq + ((l >> 2) & 1) + ((l >> 1) & 4)) >> 2) & 3)) * 32 + (l & 3) * 8)};
    const u32x4 v8_ = {(unsigned)(0), (unsigned)(0), (unsigned)(0), (unsigned)(0)};
    const u32x4 v12_ = {(unsigned)((4 * 0 + lq) * 1280 + 256 * w + ((l16 ^ (2 * ((4 * 0 + lq) & 7))) * 16) - 0), (unsigned)((4 * 1 + lq) * 1280 + 256 * w + ((l16 ^ (2 * ((4 * 1 + lq) & 7))) * 16) - 1024), (unsigned)((4 * 2 + lq) * 1280 + 256 * w + ((l16 ^ (2 * ((4 * 2 + lq) & 7))) * 16) - 2048), (unsigned)((4 * 3 + lq) * 1280 + 256 * w + ((l16 ^ (2 * ((4 * 3 + lq) & 7))) * 16) - 3072)};
    const u32x4 v16_ = {(unsigned)((4 * w + lq) * 1280 + 1024 + ((l16 ^ (2 * ((4 * w + lq) & 7))) * 16)), (unsigned)(w * 8192 + (l >> 2) * 256 + ((l >> 2) & 7) * 32 + (l & 3) * 8), (unsigned)(32768 + (l >> 2) * 256 + (((l >> 2) & 7) ^ (2 * w)) * 32 + (l & 3) * 8), (unsigned)(0u)};
    const u32x4 v92_ = {(unsigned)(l * 16 + ((w + 0) & 3) * 13312), (unsigned)(l * 16 + ((w + 1) & 3) * 13312), (unsigned)(l * 16 + ((w + 2) & 3) * 13312), (unsigned)(l * 16 + ((w + 3) & 3) * 13312)};
    const u32x4 v96_ = {(unsigned)(l * 16), (unsigned)(l * 16 + 64512 + w * 20480), (unsigned)((l & 2) ? 0xeeeeu : 0x4444u), (unsigned)(61440 + 512 * w + (l16 >> 2) * 64 + (l & 1) * 32 + sw2(lq) * 8 + (l & 2) * 2)};
    const u32x4 v100_ = {(unsigned)(61440 + 2048 + 256 * w + (l16 >> 2) * 64 + (l & 1) * 32 + sw2(lq) * 8 + (l & 2) * 2), (unsigned)(61440 + 512 * (lq >> 1) + 128 * (lq & 1) + (l & 4) * 8 + (l & 8) * 32 + (l & 3) * 8), (unsigned)(um), (unsigned)(umx)};
    const u32x4 v104_ = {(unsigned)(xl(RG_UTX + ((8 * w + (l16 >> 2)) * 4 + (h >> 3)) * 2048 + (h & 1) * 1024 + ((h & 7) >> 1) * 256 + lq * 64 + (l & 3) * 16, L)), (unsigned)(l < 4 || l > 8 ? xl(Z_QW + h * 128 + (l & 3) * 4, L) : (L ? xl(Z_XN + rep * 2048 + (l - 4) * 128, 0) : OOB)), (unsigned)(146432 + l * 16), (unsigned)(148480 + l * 16)};
    asm volatile(
    "s_mov_b32 s28, 0\n"
    "s_mov_b64 s[30:31], 0\n"
    "s_mov_b32 s33, 0\n"
    "s_cmp_eq_u32 s13, 0\n"
    "s_cselect_b32 s32, 0, 28\n"
    "s_sub_u32 s32, s32, 0\n"
    ".Lvo_ho1_%=:\n"
    "s_sub_u32 s32, s32, 1\n"
    "s_cbranch_scc1 .Lvo_hd2_%=\n"
    "s_sleep 17\n"
    "s_branch .Lvo_ho1_%=\n"
    ".Lvo_hd2_%=:\n"
    "s_mul_i32 s41, s12, 3072\n"
    "s_lshl_b32 s42, s12, 13\n"
    "s_lshl_b32 s43, s12, 10\n"
    "s_mov_b32 s49, 0\n"
    "s_add_u32 m0, s42, 0\n"
    "s_nop 0\n"
    "buffer_load_dwordx4 v12, s[4:7], s49 offen offset:0 nt lds\n"
    "buffer_load_dwordx4 v13, s[4:7], s49 offen offset:1024 nt lds\n"
    "buffer_load_dwordx4 v14, s[4:7], s49 offen offset:2048 nt lds\n"
    "buffer_load_dwordx4 v15, s[4:7], s49 offen offset:3072 nt lds\n"
    "s_add_u32 m0, s43, 32768\n"
    "s_nop 0\n"
    "buffer_load_dwordx4 v16, s[4:7], s49 offen nt lds\n"
    "s_mov_b32 s49, 81920\n"
    "s_add_u32 m0, s42, 4096\n"
    "s_nop 0\n"
    "buffer_load_dwordx4 v12, s[4:7], s49 offen offset:0 nt lds\n"
    "buffer_load_dwordx4 v13, s[4:7], s49 offen offset:1024 nt lds\n"
    "buffer_load_dwordx4 v14, s[4:7], s49 offen offset:2048 nt lds\n"
    "buffer_load_dwordx4 v15, s[4:7], s49 offen offset:3072 nt lds\n"
    "s_add_u32 m0, s43, 36864\n"
    "s_nop 0\n"
    "buffer_load_dwordx4 v16, s[4:7], s49 offen nt lds\n"
    "s_mov_b32 s49, 20480\n"
    "s_add_u32 m0, s42, 49152\n"
    "s_nop 0\n"
    "buffer_load_dwordx4 v12, s[4:7], s49 offen offset:0 nt lds\n"
    "buffer_load_dwordx4 v13, s[4:7], s49 offen offset:1024 nt lds\n"
    "buffer_load_dwordx4 v14, s[4:7], s49 offen offset:2048 nt lds\n"
    "buffer_load_dwordx4 v15, s[4:7], s49 offen offset:3072 nt lds\n"
    "s_add_u32 m0, s43, 81920\n"
    "s_nop 0\n"
    "buffer_load_dwordx4 v16, s[4:7], s49 offen nt lds\n"
    "s_mov_b32 s49, 102400\n"
    "s_add_u32 m0, s42, 53248\n"
    "s_nop 0\n"
    "buffer_load_dwordx4 v12, s[4:7], s49 offen offset:0 nt lds\n"
    "buffer_load_dwordx4 v13, s[4:7], s49 offen offset:1024 nt lds\n"
    "buffer_load_dwordx4 v14, s[4:7], s49 offen offset:2048 nt lds\n"
    "buffer_load_dwordx4 v15, s[4:7], s49 offen offset:3072 nt lds\n"
    "s_add_u32 m0, s43, 86016\n"
    "s_nop 0\n"
    "buffer_load_dwordx4 v16, s[4:7], s49 offen nt lds\n"
    "s_mov_b32 s49, 40960\n"
    "s_add_u32 m0, s42, 98304\n"
    "s_nop 0\n"
    "buffer_load_dwordx4 v12, s[4:7], s49 offen offset:0 nt lds\n"
    "buffer_load_dwordx4 v13, s[4:7], s49 offen offset:1024 nt lds\n"
    "buffer_load_dwordx4 v14, s[4:7], s49 offen offset:2048 nt lds\n"
    "buffer_load_dwordx4 v15, s[4:7], s49 offen offset:3072 nt lds\n"
    "s_add_u32 m0, s43, 131072\n"
    "s_nop 0\n"
    "buffer_load_dwordx4 v16, s[4:7], s49 offen nt lds\n"
    "s_mov_b32 s49, 122880\n"
    "s_add_u32 m0, s42, 102400\n"
    "s_nop 0\n"
    "buffer_load_dwordx4 v12, s[4:7], s49 offen offset:0 nt lds\n"
    "buffer_load_dwordx4 v13, s[4:7], s49 offen offset:1024 nt lds\n"
    "buffer_load_dwordx4 v14, s[4:7], s49 offen offset:2048 nt lds\n"
    "buffer_load_dwordx4 v15, s[4:7], s49 offen offset:3072 nt lds\n"
    "s_add_u32 m0, s43, 135168\n"
    "s_nop 0\n"
    "buffer_load_dwordx4 v16, s[4:7], s49 offen nt lds\n"
    "v_mov_b64 v[108:109], -1\n"
    "v_mov_b64 v[110:111], -1\n"
    "s_mov_b32 s49, 0x10000\n"
    "buffer_store_dwordx4 v[108:111], v102, s[0:3], 0 offen sc1\n"
    "buffer_store_dwordx4 v[108:111], v102, s[0:3], s49 offen sc1\n"
    "buffer_store_dwordx2 v[108:109], v103, s[0:3], 0 offen sc1\n"
    "s_waitcnt vmcnt(23)\n"
    "s_barrier\n"
    "v_add_u32 v24, 0, v17\n"
    "v_add_u32 v32, 0, v18\n"
    "v_xor_b32 v33, 32, v32\n"
    "v_xor_b32 v25, 32, v24\n"
    "v_xor_b32 v26, 64, v24\n"
    "v_xor_b32 v27, 96, v24\n"
    "v_xor_b32 v28, 128, v24\n"
    "v_xor_b32 v29, 160, v24\n"
    "v_xor_b32 v30, 192, v24\n"
    "v_xor_b32 v31, 224, v24\n"
    "ds_read_b64_tr_b16 a[0:1], v24 offset:0\n"
    "ds_read_b64_tr_b16 a[2:3], v24 offset:4096\n"
    "ds_read_b64_tr_b16 a[24:25], v25 offset:0\n"
    "ds_read_b64_tr_b16 a[26:27], v25 offset:4096\n"
    "ds_read_b64_tr_b16 a[48:49], v26 offset:0\n"
    "ds_read_b64_tr_b16 a[50:51], v26 offset:4096\n"
    "ds_read_b64_tr_b16 a[72:73], v27 offset:0\n"
    "ds_read_b64_tr_b16 a[74:75], v27 offset:4096\n"
    "ds_read_b64_tr_b16 a[96:97], v28 offset:0\n"
    "ds_read_b64_tr_b16 a[98:99], v28 offset:4096\n"
    "ds_read_b64_tr_b16 a[120:121], v29 offset:0\n"
    "ds_read_b64_tr_b16 a[122:123], v29 offset:4096\n"
    "ds_read_b64_tr_b16 a[144:145], v30 offset:0\n"
    "ds_read_b64_tr_b16 a[146:147], v30 offset:4096\n"
    "ds_read_b64_tr_b16 a[168:169], v31 offset:0\n"
    "ds_read_b64_tr_b16 a[170:171], v31 offset:4096\n"
    "ds_read_b64_tr_b16 a[192:193], v32 offset:0\n"
    "ds_read_b64_tr_b16 a[194:195], v32 offset:4096\n"
    "ds_read_b64_tr_b16 a[216:217], v33 offset:0\n"
    "ds_read_b64_tr_b16 a[218:219], v33 offset:4096\n"
    "s_waitcnt lgkmcnt(0)\n"
    "s_barrier\n"
    "s_mov_b32 s49, 61440\n"
    "s_add_u32 m0, s42, 0\n"
    "s_nop 0\n"
    "buffer_load_dwordx4 v12, s[4:7], s49 offen offset:0 nt lds\n"
    "buffer_load_dwordx4 v13, s[4:7], s49 offen offset:1024 nt lds\n"
    "buffer_load_dwordx4 v14, s[4:7], s49 offen offset:2048 nt lds\n"
    "buffer_load_dwordx4 v15, s[4:7], s49 offen offset:3072 nt lds\n"
    "s_add_u32 m0, s43, 32768\n"
    "s_nop 0\n"
    "buffer_load_dwordx4 v16, s[4:7], s49 offen nt lds\n"
    "s_mov_b32 s49, 143360\n"
    "s_add_u32 m0, s42, 4096\n"
    "s_nop 0\n"
    "buffer_load_dwordx4 v12, s[4:7], s49 offen offset:0 nt lds\n"
    "buffer_load_dwordx4 v13, s[4:7], s49 offen offset:1024 nt lds\n"
    "buffer_load_dwordx4 v14, s[4:7], s49 offen offset:2048 nt lds\n"
    "buffer_load_dwordx4 v15, s[4:7], s49 offen offset:3072 nt lds\n"
    "s_add_u32 m0, s43, 36864\n"
    "s_nop 0\n"
    "buffer_load_dwordx4 v16, s[4:7], s49 offen nt lds\n"
    "s_waitcnt vmcnt(23)\n"
    "s_barrier\n"
    "v_add_u32 v24, 49152, v17\n"
    "v_add_u32 v32, 49152, v18\n"
    "v_xor_b32 v33, 32, v32\n"
    "v_xor_b32 v25, 32, v24\n"
    "v_xor_b32 v26, 64, v24\n"
    "v_xor_b32 v27, 96, v24\n"
    "v_xor_b32 v28, 128, v24\n"
    "v_xor_b32 v29, 160, v24\n"
    "v_xor_b32 v30, 192, v24\n"
    "v_xor_b32 v31, 224, v24\n"
    "ds_read_b64_tr_b16 a[4:5], v24 offset:0\n"
    "ds_read_b64_tr_b16 a[6:7], v24 offset:4096\n"
    "ds_read_b64_tr_b16 a[28:29], v25 offset:0\n"
    "ds_read_b64_tr_b16 a[30:31], v25 offset:4096\n"
    "ds_read_b64_tr_b16 a[52:53], v26 offset:0\n"
    "ds_read_b64_tr_b16 a[54:55], v26 offset:4096\n"
    "ds_read_b64_tr_b16 a[76:77], v27 offset:0\n"
    "ds_read_b64_tr_b16 a[78:79], v27 offset:4096\n"
    "ds_read_b64_tr_b16 a[100:101], v28 offset:0\n"
    "ds_read_b64_tr_b16 a[102:103], v28 offset:4096\n"
    "ds_read_b64_tr_b16 a[124:125], v29 offset:0\n"
    "ds_read_b64_tr_b16 a[126:127], v29 offset:4096\n"
    "ds_read_b64_tr_b16 a[148:149], v30 offset:0\n"
    "ds_read_b64_tr_b16 a[150:151], v30 offset:4096\n"
    "ds_read_b64_tr_b16 a[172:173], v31 offset:0\n"
    "ds_read_b64_tr_b16 a[174:175], v31 offset:4096\n"
    "ds_read_b64_tr_b16 a[196:197], v32 offset:0\n"
    "ds_read_b64_tr_b16 a[198:199], v32 offset:4096\n"
    "ds_read_b64_tr_b16 a[220:221], v33 offset:0\n"
    "ds_read_b64_tr_b16 a[222:223], v33 offset:4096\n"
    "s_waitcnt lgkmcnt(0)\n"
    "s_barrier\n"
    "s_mov_b32 s49, 163840\n"
    "s_add_u32 m0, s42, 49152\n"
    "s_nop 0\n"
    "buffer_load_dwordx4 v12, s[4:7], s49 offen offset:0 nt lds\n"
    "buffer_load_dwordx4 v13, s[4:7], s49 offen offset:1024 nt lds\n"
    "buffer_load_dwordx4 v14, s[4:7], s49 offen offset:2048 nt lds\n"
    "buffer_load_dwordx4 v15, s[4:7], s49 offen offset:3072 nt lds\n"
    "s_add_u32 m0, s43, 81920\n"
    "s_nop 0\n"
    "buffer_load_dwordx4 v16, s[4:7], s49 offen nt lds\n"
    "s_mov_b32 s49, 184320\n"
    "s_add_u32 m0, s42, 53248\n"
    "s_nop 0\n"
    "buffer_load_dwordx4 v12, s[4:7], s49 offen offset:0 nt lds\n"
    "buffer_load_dwordx4 v13, s[4:7], s49 offen offset:1024 nt lds\n"
    "buffer_load_dwordx4 v14, s[4:7], s49 offen offset:2048 nt lds\n"
    "buffer_load_dwordx4 v15, s[4:7], s49 offen offset:3072 nt lds\n"
    "s_add_u32 m0, s43, 86016\n"
    "s_nop 0\n"
    "buffer_load_dwordx4 v16, s[4:7], s49 offen nt lds\n"
    "v_and_b32 v108, 0x70, v96\n"
    "v_lshlrev_b32 v108, 2, v108\n"
    "v_or_b32 v108, 0x8000, v108\n"
    "buffer_load_dword v109, v108, s[0:3], 0 offen sc1\n"
    "s_waitcnt vmcnt(24)\n"
    "s_barrier\n"
    "v_add_u32 v24, 98304, v17\n"
    "v_add_u32 v32, 98304, v18\n"
    "v_xor_b32 v33, 32, v32\n"
    "v_xor_b32 v25, 32, v24\n"
    "v_xor_b32 v26, 64, v24\n"
    "v_xor_b32 v27, 96, v24\n"
    "v_xor_b32 v28, 128, v24\n"
    "v_xor_b32 v29, 160, v24\n"
    "v_xor_b32 v30, 192, v24\n"
    "v_xor_b32 v31, 224, v24\n"
    "ds_read_b64_tr_b16 a[8:9], v24 offset:0\n"
    "ds_read_b64_tr_b16 a[10:11], v24 offset:4096\n"
    "ds_read_b64_tr_b16 a[32:33], v25 offset:0\n"
    "ds_read_b64_tr_b16 a[34:35], v25 offset:4096\n"
    "ds_read_b64_tr_b16 a[56:57], v26 offset:0\n"
    "ds_read_b64_tr_b16 a[58:59], v26 offset:4096\n"
    "ds_read_b64_tr_b16 a[80:81], v27 offset:0\n"
    "ds_read_b64_tr_b16 a[82:83], v27 offset:4096\n"
    "ds_read_b64_tr_b16 a[104:105], v28 offset:0\n"
    "ds_read_b64_tr_b16 a[106:107], v28 offset:4096\n"
    "ds_read_b64_tr_b16 a[128:129], v29 offset:0\n"
    "ds_read_b64_tr_b16 a[130:131], v29 offset:4096\n"
    "ds_read_b64_tr_b16 a[152:153], v30 offset:0\n"
    "ds_read_b64_tr_b16 a[154:155], v30 offset:4096\n"
    "ds_read_b64_tr_b16 a[176:177], v31 offset:0\n"
    "ds_read_b64_tr_b16 a[178:179], v31 offset:4096\n"
    "ds_read_b64_tr_b16 a[200:201], v32 offset:0\n"
    "ds_read_b64_tr_b16 a[202:203], v32 offset:4096\n"
    "ds_read_b64_tr_b16 a[224:225], v33 offset:0\n"
    "ds_read_b64_tr_b16 a[226:227], v33 offset:4096\n"
    "s_waitcnt lgkmcnt(0)\n"
    "s_barrier\n"
    "s_mov_b32 s49, 204800\n"
    "s_add_u32 m0, s42, 98304\n"
    "s_nop 0\n"
    "buffer_load_dwordx4 v12, s[4:7], s49 offen offset:0 nt lds\n"
    "buffer_load_dwordx4 v13, s[4:7], s49 offen offset:1024 nt lds\n"
    "buffer_load_dwordx4 v14, s[4:7], s49 offen offset:2048 nt lds\n"
    "buffer_load_dwordx4 v15, s[4:7], s49 offen offset:3072 nt lds\n"
    "s_add_u32 m0, s43, 131072\n"
    "s_nop 0\n"
    "buffer_load_dwordx4 v16, s[4:7], s49 offen nt lds\n"
    "s_mov_b32 s49, 225280\n"
    "s_add_u32 m0, s42, 102400\n"
    "s_nop 0\n"
    "buffer_load_dwordx4 v12, s[4:7], s49 offen offset:0 nt lds\n"
    "buffer_load_dwordx4 v13, s[4:7], s49 offen offset:1024 nt lds\n"
    "buffer_load_dwordx4 v14, s[4:7], s49 offen offset:2048 nt lds\n"
    "buffer_load_dwordx4 v15, s[4:7], s49 offen offset:3072 nt lds\n"
    "s_add_u32 m0, s43, 135168\n"
    "s_nop 0\n"
    "buffer_load_dwordx4 v16, s[4:7], s49 offen nt lds\n"
    "s_waitcnt vmcnt(21)\n"
    "s_barrier\n"
    "v_add_u32 v24, 0, v17\n"
    "v_add_u32 v32, 0, v18\n"
    "v_xor_b32 v33, 32, v32\n"
    "v_xor_b32 v25, 32, v24\n"
    "v_xor_b32 v26, 64, v24\n"
    "v_xor_b32 v27, 96, v24\n"
    "v_xor_b32 v28, 128, v24\n"
    "v_xor_b32 v29, 160, v24\n"
    "v_xor_b32 v30, 192, v24\n"
    "v_xor_b32 v31, 224, v24\n"
    "ds_read_b64_tr_b16 a[12:13], v24 offset:0\n"
    "ds_read_b64_tr_b16 a[14:15], v24 offset:4096\n"
    "ds_read_b64_tr_b16 a[36:37], v25 offset:0\n"
    "ds_read_b64_tr_b16 a[38:39], v25 offset:4096\n"
    "ds_read_b64_tr_b16 a[60:61], v26 offset:0\n"
    "ds_read_b64_tr_b16 a[62:63], v26 offset:4096\n"
    "ds_read_b64_tr_b16 a[84:85], v27 offset:0\n"
    "ds_read_b64_tr_b16 a[86:87], v27 offset:4096\n"
    "ds_read_b64_tr_b16 a[108:109], v28 offset:0\n"
    "ds_read_b64_tr_b16 a[110:111], v28 offset:4096\n"
    "ds_read_b64_tr_b16 a[132:133], v29 offset:0\n"
    "ds_read_b64_tr_b16 a[134:135], v29 offset:4096\n"
    "ds_read_b64_tr_b16 a[156:157], v30 offset:0\n"
    "ds_read_b64_tr_b16 a[158:159], v30 offset:4096\n"
    "ds_read_b64_tr_b16 a[180:181], v31 offset:0\n"
    "ds_read_b64_tr_b16 a[182:183], v31 offset:4096\n"
    "ds_read_b64_tr_b16 a[204:205], v32 offset:0\n"
    "ds_read_b64_tr_b16 a[206:207], v32 offset:4096\n"
    "ds_read_b64_tr_b16 a[228:229], v33 offset:0\n"
    "ds_read_b64_tr_b16 a[230:231], v33 offset:4096\n"
    "s_waitcnt lgkmcnt(0)\n"
    "s_barrier\n"
    "s_mov_b32 s49, 0\n"
    "s_add_u32 m0, s41, 0\n"
    "s_nop 0\n"
    "buffer_load_dwordx4 v0, s[8:11], s49 offen offset:0 nt lds\n"
    "buffer_load_dwordx4 v1, s[8:11], s49 offen offset:1024 nt lds\n"
    "buffer_load_dwordx4 v2, s[8:11], s49 offen offset:2048 nt lds\n"
    "s_mov_b32 s49, 1572864\n"
    "s_add_u32 m0, s41, 12288\n"
    "s_nop 0\n"
    "buffer_load_dwordx4 v0, s[8:11], s49 offen offset:0 nt lds\n"
    "buffer_load_dwordx4 v1, s[8:11], s49 offen offset:1024 nt lds\n"
    "buffer_load_dwordx4 v2, s[8:11], s49 offen offset:2048 nt lds\n"
    "s_mov_b32 s49, 3145728\n"
    "s_add_u32 m0, s41, 24576\n"
    "s_nop 0\n"
    "buffer_load_dwordx4 v0, s[8:11], s49 offen offset:0 nt lds\n"
    "buffer_load_dwordx4 v1, s[8:11], s49 offen offset:1024 nt lds\n"
    "buffer_load_dwordx4 v2, s[8:11], s49 offen offset:2048 nt lds\n"
    "s_mov_b32 s49, 4718592\n"
    "s_add_u32 m0, s41, 36864\n"
    "s_nop 0\n"
    "buffer_load_dwordx4 v0, s[8:11], s49 offen offset:0 nt lds\n"
    "buffer_load_dwordx4 v1, s[8:11], s49 offen offset:1024 nt lds\n"
    "buffer_load_dwordx4 v2, s[8:11], s49 offen offset:2048 nt lds\n"
    "s_waitcnt vmcnt(23)\n"
    "s_barrier\n"
    "v_add_u32 v24, 49152, v17\n"
    "v_add_u32 v32, 49152, v18\n"
    "v_xor_b32 v33, 32, v32\n"
    "v_xor_b32 v25, 32, v24\n"
    "v_xor_b32 v26, 64, v24\n"
    "v_xor_b32 v27, 96, v24\n"
    "v_xor_b32 v28, 128, v24\n"
    "v_xor_b32 v29, 160, v24\n"
    "v_xor_b32 v30, 192, v24\n"
    "v_xor_b32 v31, 224, v24\n"
    "ds_read_b64_tr_b16 a[16:17], v24 offset:0\n"
    "ds_read_b64_tr_b16 a[18:19], v24 offset:4096\n"
    "ds_read_b64_tr_b16 a[40:41], v25 offset:0\n"
    "ds_read_b64_tr_b16 a[42:43], v25 offset:4096\n"
    "ds_read_b64_tr_b16 a[64:65], v26 offset:0\n"
    "ds_read_b64_tr_b16 a[66:67], v26 offset:4096\n"
    "ds_read_b64_tr_b16 a[88:89], v27 offset:0\n"
    "ds_read_b64_tr_b16 a[90:91], v27 offset:4096\n"
    "ds_read_b64_tr_b16 a[112:113], v28 offset:0\n"
    "ds_read_b64_tr_b16 a[114:115], v28 offset:4096\n"
    "ds_read_b64_tr_b16 a[136:137], v29 offset:0\n"
    "ds_read_b64_tr_b16 a[138:139], v29 offset:4096\n"
    "ds_read_b64_tr_b16 a[160:161], v30 offset:0\n"
    "ds_read_b64_tr_b16 a[162:163], v30 offset:4096\n"
    "ds_read_b64_tr_b16 a[184:185], v31 offset:0\n"
    "ds_read_b64_tr_b16 a[186:187], v31 offset:4096\n"
    "ds_read_b64_tr_b16 a[208:209], v32 offset:0\n"
    "ds_read_b64_tr_b16 a[210:211], v32 offset:4096\n"
    "ds_read_b64_tr_b16 a[232:233], v33 offset:0\n"
    "ds_read_b64_tr_b16 a[234:235], v33 offset:4096\n"
    "s_waitcnt lgkmcnt(0)\n"
    "s_barrier\n"
    "s_mov_b32 s49, 393216\n"
    "s_add_u32 m0, s41, 49152\n"
    "s_nop 0\n"
    "buffer_load_dwordx4 v0, s[8:11], s49 offen offset:0 nt lds\n"
    "buffer_load_dwordx4 v1, s[8:11], s49 offen offset:1024 nt lds\n"
    "buffer_load_dwordx4 v2, s[8:11], s49 offen offset:2048 nt lds\n"
    "s_mov_b32 s49, 1966080\n"
    "s_add_u32 m0, s41, 61440\n"
    "s_nop 0\n"
    "buffer_load_dwordx4 v0, s[8:11], s49 offen offset:0 nt lds\n"
    "buffer_load_dwordx4 v1, s[8:11], s49 offen offset:1024 nt lds\n"
    "buffer_load_dwordx4 v2, s[8:11], s49 offen offset:2048 nt lds\n"
    "s_mov_b32 s49, 3538944\n"
    "s_add_u32 m0, s41, 73728\n"
    "s_nop 0\n"
    "buffer_load_dwordx4 v0, s[8:11], s49 offen offset:0 nt lds\n"
    "buffer_load_dwordx4 v1, s[8:11], s49 offen offset:1024 nt lds\n"
    "buffer_load_dwordx4 v2, s[8:11], s49 offen offset:2048 nt lds\n"
    "s_mov_b32 s49, 5111808\n"
    "s_add_u32 m0, s41, 86016\n"
    "s_nop 0\n"
    "buffer_load_dwordx4 v0, s[8:11], s49 offen offset:0 nt lds\n"
    "buffer_load_dwordx4 v1, s[8:11], s49 offen offset:1024 nt lds\n"
    "buffer_load_dwordx4 v2, s[8:11], s49 offen offset:2048 nt lds\n"
    "s_waitcnt vmcnt(24)\n"
    "s_barrier\n"
    "v_add_u32 v24, 98304, v17\n"
    "v_add_u32 v32, 98304, v18\n"
    "v_xor_b32 v33, 32, v32\n"
    "v_xor_b32 v25, 32, v24\n"
    "v_xor_b32 v26, 64, v24\n"
    "v_xor_b32 v27, 96, v24\n"
    "v_xor_b32 v28, 128, v24\n"
    "v_xor_b32 v29, 160, v24\n"
    "v_xor_b32 v30, 192, v24\n"
    "v_xor_b32 v31, 224, v24\n"
    "ds_read_b64_tr_b16 a[20:21], v24 offset:0\n"
    "ds_read_b64_tr_b16 a[22:23], v24 offset:4096\n"
    "ds_read_b64_tr_b16 a[44:45], v25 offset:0\n"
    "ds_read_b64_tr_b16 a[46:47], v25 offset:4096\n"
    "ds_read_b64_tr_b16 a[68:69], v26 offset:0\n"
    "ds_read_b64_tr_b16 a[70:71], v26 offset:4096\n"
    "ds_read_b64_tr_b16 a[92:93], v27 offset:0\n"
    "ds_read_b64_tr_b16 a[94:95], v27 offset:4096\n"
    "ds_read_b64_tr_b16 a[116:117], v28 offset:0\n"
    "ds_read_b64_tr_b16 a[118:119], v28 offset:4096\n"
    "ds_read_b64_tr_b16 a[140:141], v29 offset:0\n"
    "ds_read_b64_tr_b16 a[142:143], v29 offset:4096\n"
    "ds_read_b64_tr_b16 a[164:165], v30 offset:0\n"
    "ds_read_b64_tr_b16 a[166:167], v30 offset:4096\n"
    "ds_read_b64_tr_b16 a[188:189], v31 offset:0\n"
    "ds_read_b64_tr_b16 a[190:191], v31 offset:4096\n"
    "ds_read_b64_tr_b16 a[212:213], v32 offset:0\n"
    "ds_read_b64_tr_b16 a[214:215], v32 offset:4096\n"
    "ds_read_b64_tr_b16 a[236:237], v33 offset:0\n"
    "ds_read_b64_tr_b16 a[238:239], v33 offset:4096\n"
    "s_waitcnt lgkmcnt(0)\n"
    "s_barrier\n"
    "s_mov_b32 s49, 786432\n"
    "s_add_u32 m0, s41, 98304\n"
    "s_nop 0\n"
    "buffer_load_dwordx4 v0, s[8:11], s49 offen offset:0 nt lds\n"
    "buffer_load_dwordx4 v1, s[8:11], s49 offen offset:1024 nt lds\n"
    "buffer_load_dwordx4 v2, s[8:11], s49 offen offset:2048 nt lds\n"
    "s_mov_b32 s49, 2359296\n"
    "s_add_u32 m0, s41, 110592\n"
    "s_nop 0\n"
    "buffer_load_dwordx4 v0, s[8:11], s49 offen offset:0 nt lds\n"
    "buffer_load_dwordx4 v1, s[8:11], s49 offen offset:1024 nt lds\n"
    "buffer_load_dwordx4 v2, s[8:11], s49 offen offset:2048 nt lds\n"
    "s_mov_b32 s49, 3932160\n"
    "s_add_u32 m0, s41, 122880\n"
    "s_nop 0\n"
    "buffer_load_dwordx4 v0, s[8:11], s49 offen offset:0 nt lds\n"
    "buffer_load_dwordx4 v1, s[8:11], s49 offen offset:1024 nt lds\n"
    "buffer_load_dwordx4 v2, s[8:11], s49 offen offset:2048 nt lds\n"
    "s_mov_b32 s49, 5505024\n"
    "s_add_u32 m0, s41, 135168\n"
    "s_nop 0\n"
    "buffer_load_dwordx4 v0, s[8:11], s49 offen offset:0 nt lds\n"
    "buffer_load_dwordx4 v1, s[8:11], s49 offen offset:1024 nt lds\n"
    "buffer_load_dwordx4 v2, s[8:11], s49 offen offset:2048 nt lds\n"
    "s_waitcnt vmcnt(24)\n"
    "s_barrier\n"
    "v_add_u32 v8, 0, v3\n"
    "ds_read_b64_tr_b16 a[240:241], v8 offset:0\n"
    "ds_read_b64_tr_b16 a[242:243], v8 offset:768\n"
    "ds_read_b64_tr_b16 v[176:177], v8 offset:128\n"
    "ds_read_b64_tr_b16 v[178:179], v8 offset:896\n"
    "ds_read_b64_tr_b16 v[12:13], v8 offset:256\n"
    "ds_read_b64_tr_b16 v[14:15], v8 offset:1024\n"
    "v_add_u32 v9, 0, v4\n"
    "ds_read_b64_tr_b16 v[112:113], v9 offset:0\n"
    "ds_read_b64_tr_b16 v[114:115], v9 offset:768\n"
    "ds_read_b64_tr_b16 v[192:193], v9 offset:128\n"
    "ds_read_b64_tr_b16 v[194:195], v9 offset:896\n"
    "ds_read_b64_tr_b16 v[28:29], v9 offset:256\n"
    "ds_read_b64_tr_b16 v[30:31], v9 offset:1024\n"
    "v_add_u32 v10, 0, v5\n"
    "ds_read_b64_tr_b16 v[128:129], v10 offset:0\n"
    "ds_read_b64_tr_b16 v[130:131], v10 offset:768\n"
    "ds_read_b64_tr_b16 v[208:209], v10 offset:128\n"
    "ds_read_b64_tr_b16 v[210:211], v10 offset:896\n"
    "ds_read_b64_tr_b16 v[44:45], v10 offset:256\n"
    "ds_read_b64_tr_b16 v[46:47], v10 offset:1024\n"
    "v_add_u32 v11, 0, v6\n"
    "ds_read_b64_tr_b16 v[144:145], v11 offset:0\n"
    "ds_read_b64_tr_b16 v[146:147], v11 offset:768\n"
    "ds_read_b64_tr_b16 v[224:225], v11 offset:128\n"
    "ds_read_b64_tr_b16 v[226:227], v11 offset:896\n"
    "ds_read_b64_tr_b16 v[60:61], v11 offset:256\n"
    "ds_read_b64_tr_b16 v[62:63], v11 offset:1024\n"
    "s_waitcnt lgkmcnt(0)\n"
    "s_barrier\n"
    "s_mov_b32 s49, 1179648\n"
    "s_add_u32 m0, s41, 0\n"
    "s_nop 0\n"
    "buffer_load_dwordx4 v0, s[8:11], s49 offen offset:0 nt lds\n"
    "buffer_load_dwordx4 v1, s[8:11], s49 offen offset:1024 nt lds\n"
    "buffer_load_dwordx4 v2, s[8:11], s49 offen offset:2048 nt lds\n"
    "s_mov_b32 s49, 2752512\n"
    "s_add_u32 m0, s41, 12288\n"
    "s_nop 0\n"
    "buffer_load_dwordx4 v0, s[8:11], s49 offen offset:0 nt lds\n"
    "buffer_load_dwordx4 v1, s[8:11], s49 offen offset:1024 nt lds\n"
    "buffer_load_dwordx4 v2, s[8:11], s49 offen offset:2048 nt lds\n"
    "s_mov_b32 s49, 4325376\n"
    "s_add_u32 m0, s41, 24576\n"
    "s_nop 0\n"
    "buffer_load_dwordx4 v0, s[8:11], s49 offen offset:0 nt lds\n"
    "buffer_load_dwordx4 v1, s[8:11], s49 offen offset:1024 nt lds\n"
    "buffer_load_dwordx4 v2, s[8:11], s49 offen offset:2048 nt lds\n"
    "s_mov_b32 s49, 5898240\n"
    "s_add_u32 m0, s41, 36864\n"
    "s_nop 0\n"
    "buffer_load_dwordx4 v0, s[8:11], s49 offen offset:0 nt lds\n"
    "buffer_load_dwordx4 v1, s[8:11], s49 offen offset:1024 nt lds\n"
    "buffer_load_dwordx4 v2, s[8:11], s49 offen offset:2048 nt lds\n"
    "s_waitcnt vmcnt(24)\n"
    "s_barrier\n"
    "v_add_u32 v8, 49152, v3\n"
    "ds_read_b64_tr_b16 a[244:245], v8 offset:0\n"
    "ds_read_b64_tr_b16 a[246:247], v8 offset:768\n"
    "ds_read_b64_tr_b16 v[180:181], v8 offset:128\n"
    "ds_read_b64_tr_b16 v[182:183], v8 offset:896\n"
    "ds_read_b64_tr_b16 v[16:17], v8 offset:256\n"
    "ds_read_b64_tr_b16 v[18:19], v8 offset:1024\n"
    "v_add_u32 v9, 49152, v4\n"
    "ds_read_b64_tr_b16 v[116:117], v9 offset:0\n"
    "ds_read_b64_tr_b16 v[118:119], v9 offset:768\n"
    "ds_read_b64_tr_b16 v[196:197], v9 offset:128\n"
    "ds_read_b64_tr_b16 v[198:199], v9 offset:896\n"
    "ds_read_b64_tr_b16 v[32:33], v9 offset:256\n"
    "ds_read_b64_tr_b16 v[34:35], v9 offset:1024\n"
    "v_add_u32 v10, 49152, v5\n"
    "ds_read_b64_tr_b16 v[132:133], v10 offset:0\n"
    "ds_read_b64_tr_b16 v[134:135], v10 offset:768\n"
    "ds_read_b64_tr_b16 v[212:213], v10 offset:128\n"
    "ds_read_b64_tr_b16 v[214:215], v10 offset:896\n"
    "ds_read_b64_tr_b16 v[48:49], v10 offset:256\n"
    "ds_read_b64_tr_b16 v[50:51], v10 offset:1024\n"
    "v_add_u32 v11, 49152, v6\n"
    "ds_read_b64_tr_b16 v[148:149], v11 offset:0\n"
    "ds_read_b64_tr_b16 v[150:151], v11 offset:768\n"
    "ds_read_b64_tr_b16 v[228:229], v11 offset:128\n"
    "ds_read_b64_tr_b16 v[230:231], v11 offset:896\n"
    "ds_read_b64_tr_b16 v[64:65], v11 offset:256\n"
    "ds_read_b64_tr_b16 v[66:67], v11 offset:1024\n"
    "s_waitcnt lgkmcnt(0)\n"
    "s_barrier\n"
    "s_waitcnt vmcnt(58)\n"
    "v_cmp_gt_i32 vcc, 32, v109\n"
    "s_nop 0\n"
    "s_cbranch_vccnz .Lvo_tk0_%=\n"
    ".Lvo_tk1_%=:\n"
    "s_lshl_b32 s48, s12, 11\n"
    "s_add_u32 s48, s48, 53248\n"
    "v_lshrrev_b32 v108, 2, v96\n"
    "s_mov_b32 s50, 0\n"
    "s_mov_b32 s51, s14\n"
    "s_add_u32 s43, s18, s13\n"
    "v_mov_b32 v111, s50\n"
    "s_mov_b64 exec, 0x1f0\n"
    "v_mov_b32 v111, s51\n"
    "s_mov_b64 exec, -1\n"
    "buffer_load_dword v109, v108, s[0:3], s24 offen sc1\n"
    "buffer_load_dword v110, v105, s[0:3], 0 offen sc1\n"
    "s_mov_b32 s49, 6291456\n"
    "s_add_u32 m0, s41, 49152\n"
    "s_nop 0\n"
    "buffer_load_dwordx4 v0, s[8:11], s49 offen offset:0 nt lds\n"
    "buffer_load_dwordx4 v1, s[8:11], s49 offen offset:1024 nt lds\n"
    "buffer_load_dwordx4 v2, s[8:11], s49 offen offset:2048 nt lds\n"
    "s_mov_b32 s49, 6684672\n"
    "s_add_u32 m0, s41, 61440\n"
    "s_nop 0\n"
    "buffer_load_dwordx4 v0, s[8:11], s49 offen offset:0 nt lds\n"
    "buffer_load_dwordx4 v1, s[8:11], s49 offen offset:1024 nt lds\n"
    "buffer_load_dwordx4 v2, s[8:11], s49 offen offset:2048 nt lds\n"
    "s_mov_b32 s49, 7077888\n"
    "s_add_u32 m0, s41, 73728\n"
    "s_nop 0\n"
    "buffer_load_dwordx4 v0, s[8:11], s49 offen offset:0 nt lds\n"
    "buffer_load_dwordx4 v1, s[8:11], s49 offen offset:1024 nt lds\n"
    "buffer_load_dwordx4 v2, s[8:11], s49 offen offset:2048 nt lds\n"
    "s_mov_b32 s49, 7471104\n"
    "s_add_u32 m0, s41, 86016\n"
    "s_nop 0\n"
    "buffer_load_dwordx4 v0, s[8:11], s49 offen offset:0 nt lds\n"
    "buffer_load_dwordx4 v1, s[8:11], s49 offen offset:1024 nt lds\n"
    "buffer_load_dwordx4 v2, s[8:11], s49 offen offset:2048 nt lds\n"
    "s_waitcnt vmcnt(26)\n"
    "s_barrier\n"
    "v_add_u32 v8, 98304, v3\n"
    "ds_read_b64_tr_b16 a[248:249], v8 offset:0\n"
    "ds_read_b64_tr_b16 a[250:251], v8 offset:768\n"
    "ds_read_b64_tr_b16 v[184:185], v8 offset:128\n"
    "ds_read_b64_tr_b16 v[186:187], v8 offset:896\n"
    "ds_read_b64_tr_b16 v[20:21], v8 offset:256\n"
    "ds_read_b64_tr_b16 v[22:23], v8 offset:1024\n"
    "v_add_u32 v9, 98304, v4\n"
    "ds_read_b64_tr_b16 v[120:121], v9 offset:0\n"
    "ds_read_b64_tr_b16 v[122:123], v9 offset:768\n"
    "ds_read_b64_tr_b16 v[200:201], v9 offset:128\n"
    "ds_read_b64_tr_b16 v[202:203], v9 offset:896\n"
    "ds_read_b64_tr_b16 v[36:37], v9 offset:256\n"
    "ds_read_b64_tr_b16 v[38:39], v9 offset:1024\n"
    "v_add_u32 v10, 98304, v5\n"
    "ds_read_b64_tr_b16 v[136:137], v10 offset:0\n"
    "ds_read_b64_tr_b16 v[138:139], v10 offset:768\n"
    "ds_read_b64_tr_b16 v[216:217], v10 offset:128\n"
    "ds_read_b64_tr_b16 v[218:219], v10 offset:896\n"
    "ds_read_b64_tr_b16 v[52:53], v10 offset:256\n"
    "ds_read_b64_tr_b16 v[54:55], v10 offset:1024\n"
    "v_add_u32 v11, 98304, v6\n"
    "ds_read_b64_tr_b16 v[152:153], v11 offset:0\n"
    "ds_read_b64_tr_b16 v[154:155], v11 offset:768\n"
    "ds_read_b64_tr_b16 v[232:233], v11 offset:128\n"
    "ds_read_b64_tr_b16 v[234:235], v11 offset:896\n"
    "ds_read_b64_tr_b16 v[68:69], v11 offset:256\n"
    "ds_read_b64_tr_b16 v[70:71], v11 offset:1024\n"
    "s_waitcnt lgkmcnt(0)\n"
    "s_barrier\n"
    "s_waitcnt vmcnt(14)\n"
    "s_barrier\n"
    "v_add_u32 v8, 0, v3\n"
    "ds_read_b64_tr_b16 a[252:253], v8 offset:0\n"
    "ds_read_b64_tr_b16 a[254:255], v8 offset:768\n"
    "ds_read_b64_tr_b16 v[188:189], v8 offset:128\n"
    "ds_read_b64_tr_b16 v[190:191], v8 offset:896\n"
    "ds_read_b64_tr_b16 v[24:25], v8 offset:256\n"
    "ds_read_b64_tr_b16 v[26:27], v8 offset:1024\n"
    "v_add_u32 v9, 0, v4\n"
    "ds_read_b64_tr_b16 v[124:125], v9 offset:0\n"
    "ds_read_b64_tr_b16 v[126:127], v9 offset:768\n"
    "ds_read_b64_tr_b16 v[204:205], v9 offset:128\n"
    "ds_read_b64_tr_b16 v[206:207], v9 offset:896\n"
    "ds_read_b64_tr_b16 v[40:41], v9 offset:256\n"
    "ds_read_b64_tr_b16 v[42:43], v9 offset:1024\n"
    "v_add_u32 v10, 0, v5\n"
    "ds_read_b64_tr_b16 v[140:141], v10 offset:0\n"
    "ds_read_b64_tr_b16 v[142:143], v10 offset:768\n"
    "ds_read_b64_tr_b16 v[220:221], v10 offset:128\n"
    "ds_read_b64_tr_b16 v[222:223], v10 offset:896\n"
    "ds_read_b64_tr_b16 v[56:57], v10 offset:256\n"
    "ds_read_b64_tr_b16 v[58:59], v10 offset:1024\n"
    "v_add_u32 v11, 0, v6\n"
    "ds_read_b64_tr_b16 v[156:157], v11 offset:0\n"
    "ds_read_b64_tr_b16 v[158:159], v11 offset:768\n"
    "ds_read_b64_tr_b16 v[236:237], v11 offset:128\n"
    "ds_read_b64_tr_b16 v[238:239], v11 offset:896\n"
    "ds_read_b64_tr_b16 v[72:73], v11 offset:256\n"
    "ds_read_b64_tr_b16 v[74:75], v11 offset:1024\n"
    "s_waitcnt lgkmcnt(0)\n"
    "s_barrier\n"
    "s_waitcnt vmcnt(12)\n"
    "v_cmp_gt_i32 vcc, 1, v109\n"
    "s_nop 0\n"
    "s_cbranch_vccnz .Lvo_e0_%=\n"
    "v_cmp_gt_i32 vcc, v111, v110\n"
    "s_nop 0\n"
    "s_cbranch_vccnz .Lvo_e0_%=\n"
    ".Lvo_e1_%=:\n"
    "s_sub_u32 m0, s17, 8192\n"
    "s_nop 0\n"
    "buffer_load_dwordx4 v96, s[0:3], s43 offen offset:0 sc1 lds\n"
    "buffer_load_dwordx4 v96, s[0:3], s43 offen offset:1024 sc1 lds\n"
    "buffer_load_dwordx4 v96, s[0:3], s43 offen offset:2048 sc1 lds\n"
    "buffer_load_dwordx4 v96, s[0:3], s43 offen offset:3072 sc1 lds\n"
    "s_sub_u32 m0, s17, 4096\n"
    "s_add_u32 s43, s43, 4096\n"
    "buffer_load_dwordx4 v96, s[0:3], s43 offen offset:0 sc1 lds\n"
    "buffer_load_dwordx4 v96, s[0:3], s43 offen offset:1024 sc1 lds\n"
    "buffer_load_dwordx4 v96, s[0:3], s43 offen offset:2048 sc1 lds\n"
    "buffer_load_dwordx4 v96, s[0:3], s43 offen offset:3072 sc1 lds\n"
    "s_waitcnt vmcnt(8)\n"
    "s_barrier\n"
    "v_add_u32 v8, 49152, v7\n"
    "ds_read_b64_tr_b16 v[160:161], v8 offset:0\n"
    "ds_read_b64_tr_b16 v[162:163], v8 offset:768\n"
    "ds_read_b64_tr_b16 v[240:241], v8 offset:128\n"
    "ds_read_b64_tr_b16 v[242:243], v8 offset:896\n"
    "ds_read_b64_tr_b16 v[76:77], v8 offset:256\n"
    "ds_read_b64_tr_b16 v[78:79], v8 offset:1024\n"
    "v_add_u32 v9, 61440, v7\n"
    "ds_read_b64_tr_b16 v[164:165], v9 offset:0\n"
    "ds_read_b64_tr_b16 v[166:167], v9 offset:768\n"
    "ds_read_b64_tr_b16 v[244:245], v9 offset:128\n"
    "ds_read_b64_tr_b16 v[246:247], v9 offset:896\n"
    "ds_read_b64_tr_b16 v[80:81], v9 offset:256\n"
    "ds_read_b64_tr_b16 v[82:83], v9 offset:1024\n"
    "v_add_u32 v10, 73728, v7\n"
    "ds_read_b64_tr_b16 v[168:169], v10 offset:0\n"
    "ds_read_b64_tr_b16 v[170:171], v10 offset:768\n"
    "ds_read_b64_tr_b16 v[248:249], v10 offset:128\n"
    "ds_read_b64_tr_b16 v[250:251], v10 offset:896\n"
    "ds_read_b64_tr_b16 v[84:85], v10 offset:256\n"
    "ds_read_b64_tr_b16 v[86:87], v10 offset:1024\n"
    "v_add_u32 v11, 86016, v7\n"
    "ds_read_b64_tr_b16 v[172:173], v11 offset:0\n"
    "ds_read_b64_tr_b16 v[174:175], v11 offset:768\n"
    "ds_read_b64_tr_b16 v[252:253], v11 offset:128\n"
    "ds_read_b64_tr_b16 v[254:255], v11 offset:896\n"
    "ds_read_b64_tr_b16 v[88:89], v11 offset:256\n"
    "ds_read_b64_tr_b16 v[90:91], v11 offset:1024\n"
    "s_waitcnt lgkmcnt(0)\n"
    "s_barrier\n"
    "s_mov_b32 m0, s48\n"
    "s_add_u32 s43, s43, s19\n"
    "buffer_load_dwordx4 v96, s[0:3], s43 offen offset:0 sc1 lds\n"
    "buffer_load_dwordx4 v96, s[0:3], s43 offen offset:1024 sc1 lds\n"
    "ds_write_b128 v97, v[12:15] offset:0\n"
    "ds_write_b128 v97, v[16:19] offset:1024\n"
    "ds_write_b128 v97, v[20:23] offset:2048\n"
    "ds_write_b128 v97, v[24:27] offset:3072\n"
    "ds_write_b128 v97, v[28:31] offset:4096\n"
    "ds_write_b128 v97, v[32:35] offset:5120\n"
    "ds_write_b128 v97, v[36:39] offset:6144\n"
    "ds_write_b128 v97, v[40:43] offset:7168\n"
    "ds_write_b128 v97, v[44:47] offset:8192\n"
    "ds_write_b128 v97, v[48:51] offset:9216\n"
    "ds_write_b128 v97, v[52:55] offset:10240\n"
    "ds_write_b128 v97, v[56:59] offset:11264\n"
    "ds_write_b128 v97, v[60:63] offset:12288\n"
    "ds_write_b128 v97, v[64:67] offset:13312\n"
    "ds_write_b128 v97, v[68:71] offset:14336\n"
    "ds_write_b128 v97, v[72:75] offset:15360\n"
    "ds_write_b128 v97, v[76:79] offset:16384\n"
    "ds_write_b128 v97, v[80:83] offset:17408\n"
    "ds_write_b128 v97, v[84:87] offset:18432\n"
    "ds_write_b128 v97, v[88:91] offset:19456\n"
    "s_mov_b32 s34, -4\n"
    "s_memrealtime s[30:31]\n"
    "s_waitcnt vmcnt(0) lgkmcnt(0)\n"
    "s_barrier\n"
    "ds_read_b128 v[24:27], v92 offset:0\n"
    "ds_read_b128 v[28:31], v92 offset:1024\n"
    "ds_read_b128 v[32:35], v93 offset:0\n"
    "ds_read_b128 v[36:39], v93 offset:1024\n"
    "ds_read_b128 v[40:43], v94 offset:0\n"
    "ds_read_b128 v[44:47], v94 offset:1024\n"
    "s_sub_u32 s46, s30, 1280\n"
    "s_mov_b32 s34, -3\n"
    "s_mov_b32 s35, -5\n"
    "s_mov_b32 s45, s23\n"
    "s_mov_b32 s38, 2130706432\n"
    "s_mov_b32 s39, 2130706432\n"
    "s_mov_b32 s40, 2130706432\n"
    "s_waitcnt lgkmcnt(0)\n"
    "s_branch .Lvo_vph_%=\n"
    ".Lvo_take_%=:\n"
    "v_mfma_f32_16x16x32_bf16 v[84:87], v[248:251], v[24:27], v[84:87]\n"
    ".Lvo_mk8x5_%=:\n"
    "s_waitcnt vmcnt(3)\n"
    ".Lvo_mk0x6_%=:\n"
    "ds_read_b128 v[72:75], v92 offset:8192\n"
    "v_cvt_pk_bf16_f32 v40, v64, v65\n"
    "v_mfma_f32_16x16x32_bf16 v[84:87], v[252:255], v[32:35], v[84:87]\n"
    "s_waitcnt vmcnt(2)\n"
    "ds_read_b128 v[76:79], v92 offset:9216\n"
    "v_cvt_pk_bf16_f32 v41, v66, v67\n"
    "v_mfma_f32_16x16x32_bf16 v[68:71], v[164:167], v[44:47], v[68:71]\n"
    "v_mov_b64 v[64:65], 0\n"
    "v_mov_b64 v[66:67], 0\n"
    "v_mfma_f32_16x16x32_bf16 v[68:71], v[168:171], v[28:31], v[68:71]\n"
    "s_waitcnt lgkmcnt(2)\n"
    "ds_read_b128 v[24:27], v97 offset:6144\n"
    "v_cvt_pk_bf16_f32 v42, v84, v85\n"
    "v_mfma_f32_16x16x32_bf16 v[68:71], v[172:175], v[36:39], v[68:71]\n"
    "v_cvt_pk_bf16_f32 v43, v86, v87\n"
    "v_mov_b64 v[84:85], 0\n"
    "v_mfma_f32_16x16x32_bf16 v[88:91], v[244:247], v[44:47], v[88:91]\n"
    "ds_write_b128 v92, v[40:43] offset:11264\n"
    "v_mfma_f32_16x16x32_bf16 v[88:91], v[248:251], v[28:31], v[88:91]\n"
    "s_waitcnt lgkmcnt(2)\n"
    "v_max_u32 v108, v72, v74\n"
    "v_max3_u32 v108, v108, v76, v78\n"
    "v_mfma_f32_16x16x32_bf16 v[88:91], v[252:255], v[36:39], v[88:91]\n"
    "v_cmp_le_u32 vcc, s52, v108\n"
    "v_mov_b64 v[86:87], 0\n"
    "v_smfmac_f32_16x16x64_bf16 v[64:67], v[72:75], a[240:247], v98\n"
    "s_cbranch_vccnz .Lvo_retry_%=\n"
    ".Lvo_go_%=:\n"
    "s_barrier\n"
    ".Lvo_mk1x7_%=:\n"
    "ds_read_b128 v[32:35], v93 offset:8192\n"
    "v_cvt_pk_bf16_f32 v44, v68, v69\n"
    "v_smfmac_f32_16x16x64_bf16 v[64:67], v[76:79], a[248:255], v98\n"
    "ds_read_b128 v[36:39], v93 offset:9216\n"
    "v_cvt_pk_bf16_f32 v45, v70, v71\n"
    "v_smfmac_f32_16x16x64_bf16 v[84:87], v[72:75], v[0:7], v98\n"
    "s_waitcnt vmcnt(1)\n"
    "ds_read_b128 v[80:83], v92 offset:10240\n"
    "v_mov_b64 v[68:69], 0\n"
    "v_mov_b64 v[70:71], 0\n"
    "v_smfmac_f32_16x16x64_bf16 v[84:87], v[76:79], v[8:15], v98\n"
    "ds_read_b128 v[28:31], v97 offset:7168\n"
    "v_cvt_pk_bf16_f32 v46, v88, v89\n"
    "v_smfmac_f32_16x16x64_bf16 v[68:71], v[72:75], v[176:183], v98\n"
    "ds_read_b128 v[0:3], v97 offset:12288\n"
    "v_cvt_pk_bf16_f32 v47, v90, v91\n"
    "v_smfmac_f32_16x16x64_bf16 v[68:71], v[76:79], v[184:191], v98\n"
    "s_waitcnt lgkmcnt(4)\n"
    "ds_read_b128 v[88:91], v94 offset:8192\n"
    "ds_write_b128 v92, v[44:47] offset:12288\n"
    "v_smfmac_f32_16x16x64_bf16 v[84:87], v[32:35], v[16:23], v98\n"
    "ds_read_b128 v[40:43], v94 offset:9216\n"
    "v_smfmac_f32_16x16x64_bf16 v[64:67], v[32:35], v[112:119], v98\n"
    "s_waitcnt lgkmcnt(5)\n"
    "ds_read_b128 v[4:7], v97 offset:13312\n"
    "v_max_u32 v108, v80, v82\n"
    "v_smfmac_f32_16x16x64_bf16 v[68:71], v[32:35], v[192:199], v98\n"
    "s_waitcnt lgkmcnt(5)\n"
    "ds_read_b128 v[44:47], v95 offset:8192\n"
    "v_cmp_eq_u32 vcc, -1, v108\n"
    "v_smfmac_f32_16x16x64_bf16 v[84:87], v[36:39], v[24:31], v98\n"
    "ds_read_b128 v[8:11], v97 offset:14336\n"
    "ds_read_b128 v[12:15], v97 offset:15360\n"
    "v_smfmac_f32_16x16x64_bf16 v[64:67], v[36:39], v[120:127], v98\n"
    "ds_read_b128 v[16:19], v97 offset:16384\n"
    "v_smfmac_f32_16x16x64_bf16 v[68:71], v[36:39], v[200:207], v98\n"
    "s_waitcnt lgkmcnt(6)\n"
    "s_cbranch_vccnz .Lvo_r2_%=\n"
    ".Lvo_go2_%=:\n"
    "s_barrier\n"
    "ds_read_b128 v[72:75], v96 offset:10240\n"
    "v_smfmac_f32_16x16x64_bf16 v[84:87], v[88:91], v[48:55], v98\n"
    "s_mov_b32 m0, s21\n"
    "ds_read_b128 v[80:83], v95 offset:9216\n"
    "ds_read_b128 v[76:79], v96 offset:23552\n"
    "v_smfmac_f32_16x16x64_bf16 v[64:67], v[88:91], v[128:135], v98\n"
    "s_waitcnt vmcnt(0)\n"
    "ds_read_b128 v[20:23], v97 offset:17408\n"
    "v_cmp_gt_i32 vcc, v111, v110\n"
    "v_smfmac_f32_16x16x64_bf16 v[68:71], v[88:91], v[208:215], v98\n"
    "s_waitcnt lgkmcnt(7)\n"
    "s_cbranch_vccnz .Lvo_wf_%=\n"
    ".Lvo_wfb_%=:\n"
    "v_smfmac_f32_16x16x64_bf16 v[64:67], v[40:43], v[136:143], v98\n"
    "buffer_load_dwordx4 v96, s[0:3], s44 offen sc1 lds\n"
    "v_smfmac_f32_16x16x64_bf16 v[64:67], v[44:47], v[144:151], v98\n"
    "s_waitcnt lgkmcnt(3)\n"
    "ds_read_b128 v[24:27], v97 offset:18432\n"
    "v_mov_b64 v[32:33], 0\n"
    "v_smfmac_f32_16x16x64_bf16 v[64:67], v[72:75], v[160:167], v98\n"
    "s_waitcnt lgkmcnt(3)\n"
    "ds_read_b128 v[28:31], v97 offset:19456\n"
    "v_mov_b64 v[34:35], 0\n"
    "v_smfmac_f32_16x16x64_bf16 v[64:67], v[80:83], v[152:159], v98\n"
    "s_waitcnt lgkmcnt(3)\n"
    "v_mov_b64 v[36:37], 0\n"
    "v_smfmac_f32_16x16x64_bf16 v[64:67], v[76:79], v[168:175], v98\n"
    "s_sub_u32 m0, s17, 8192\n"
    "v_mov_b64 v[38:39], 0\n"
    "v_smfmac_f32_16x16x64_bf16 v[84:87], v[40:43], v[56:63], v98\n"
    "v_mov_b64 v[48:49], 0\n"
    "v_smfmac_f32_16x16x64_bf16 v[84:87], v[44:47], v[0:7], v98\n"
    "ds_read_b128 v[0:3], v96 offset:11264\n"
    "s_nop 0\n"
    "v_add_f32 v64, v64, v66\n"
    "v_smfmac_f32_16x16x64_bf16 v[84:87], v[80:83], v[8:15], v98\n"
    "s_waitcnt lgkmcnt(3)\n"
    "ds_read_b128 v[4:7], v96 offset:12288\n"
    "v_add_f32 v65, v65, v67\n"
    "v_smfmac_f32_16x16x64_bf16 v[84:87], v[72:75], v[16:23], v98\n"
    "s_waitcnt lgkmcnt(2)\n"
    "ds_read_b128 v[8:11], v96 offset:24576\n"
    "v_cvt_pk_bf16_f32 v64, v64, v65\n"
    "v_smfmac_f32_16x16x64_bf16 v[84:87], v[76:79], v[24:31], v98\n"
    "ds_write_b32 v99, v64 offset:0\n"
    "v_smfmac_f32_16x16x64_bf16 v[68:71], v[40:43], v[216:223], v98\n"
    "ds_read_b128 v[12:15], v96 offset:25600\n"
    "v_mov_b64 v[50:51], 0\n"
    "v_smfmac_f32_16x16x64_bf16 v[68:71], v[44:47], v[224:231], v98\n"
    "ds_read_b128 v[16:19], v96 offset:37888\n"
    "s_nop 0\n"
    "v_add_f32 v84, v84, v86\n"
    "v_smfmac_f32_16x16x64_bf16 v[68:71], v[80:83], v[232:239], v98\n"
    "ds_read_b128 v[20:23], v96 offset:38912\n"
    "v_add_f32 v85, v85, v87\n"
    "v_smfmac_f32_16x16x64_bf16 v[68:71], v[72:75], v[240:247], v98\n"
    "ds_read_b128 v[24:27], v96 offset:51200\n"
    "v_cvt_pk_bf16_f32 v84, v84, v85\n"
    "v_smfmac_f32_16x16x64_bf16 v[68:71], v[76:79], v[248:255], v98\n"
    "ds_write_b32 v100, v84 offset:0\n"
    "s_waitcnt lgkmcnt(7)\n"
    "v_mfma_f32_16x16x32_bf16 v[40:43], v[0:3], a[0:3], 0\n"
    "ds_read_b128 v[28:31], v96 offset:52224\n"
    "v_mov_b64 v[52:53], 0\n"
    "v_mfma_f32_16x16x32_bf16 v[44:47], v[4:7], a[0:3], 0\n"
    "s_nop 1\n"
    "v_add_f32 v68, v68, v70\n"
    "v_add_f32 v69, v69, v71\n"
    "v_cvt_pk_bf16_f32 v68, v68, v69\n"
    "ds_write_b32 v99, v68 offset:256\n"
    "s_waitcnt lgkmcnt(4)\n"
    "v_mfma_f32_16x16x32_bf16 v[40:43], v[8:11], a[4:7], v[40:43]\n"
    "v_mov_b64 v[54:55], 0\n"
    "v_mov_b64 v[56:57], 0\n"
    "v_mfma_f32_16x16x32_bf16 v[44:47], v[12:15], a[4:7], v[44:47]\n"
    "v_mov_b64 v[58:59], 0\n"
    "v_mov_b64 v[60:61], 0\n"
    "v_mfma_f32_16x16x32_bf16 v[40:43], v[16:19], a[8:11], v[40:43]\n"
    "v_mov_b64 v[62:63], 0\n"
    "v_mov_b64 v[64:65], 0\n"
    "v_mfma_f32_16x16x32_bf16 v[44:47], v[20:23], a[8:11], v[44:47]\n"
    "v_mov_b64 v[66:67], 0\n"
    "v_mov_b64 v[68:69], 0\n"
    "s_waitcnt lgkmcnt(0)\n"
    "s_barrier\n"
    ".Lvo_mk2x8_%=:\n"
    "ds_read_b64_tr_b16 v[72:73], v101 offset:0\n"
    "ds_read_b64_tr_b16 v[74:75], v101 offset:64\n"
    "v_mfma_f32_16x16x32_bf16 v[40:43], v[24:27], a[12:15], v[40:43]\n"
    "ds_read_b64_tr_b16 v[76:77], v101 offset:1024\n"
    "ds_read_b64_tr_b16 v[78:79], v101 offset:1088\n"
    "v_mfma_f32_16x16x32_bf16 v[44:47], v[28:31], a[12:15], v[44:47]\n"
    "ds_read_b64_tr_b16 v[80:81], v101 offset:2048\n"
    "ds_read_b64_tr_b16 v[82:83], v101 offset:2112\n"
    "v_mov_b64 v[70:71], 0\n"
    "v_mov_b64 v[84:85], 0\n"
    "v_mov_b64 v[86:87], 0\n"
    "v_mov_b64 v[88:89], 0\n"
    "v_mov_b64 v[90:91], 0\n"
    "s_waitcnt lgkmcnt(4)\n"
    "v_smfmac_f32_16x16x64_bf16 v[32:35], v[72:75], a[192:199], v98\n"
    "buffer_load_dwordx4 v96, s[0:3], s43 offen offset:0 sc1 lds\n"
    "s_waitcnt lgkmcnt(2)\n"
    "v_smfmac_f32_16x16x64_bf16 v[32:35], v[76:79], a[200:207], v98\n"
    "s_waitcnt lgkmcnt(0)\n"
    "v_smfmac_f32_16x16x64_bf16 v[32:35], v[80:83], a[208:215], v98\n"
    "v_smfmac_f32_16x16x64_bf16 v[36:39], v[72:75], a[216:223], v98\n"
    "buffer_load_dwordx4 v96, s[0:3], s43 offen offset:1024 sc1 lds\n"
    "v_smfmac_f32_16x16x64_bf16 v[36:39], v[76:79], a[224:231], v98\n"
    "s_nop 1\n"
    "v_add_f32 v32, v32, v34\n"
    "v_smfmac_f32_16x16x64_bf16 v[36:39], v[80:83], a[232:239], v98\n"
    "v_add_f32 v33, v33, v35\n"
    "v_smfmac_f32_16x16x64_bf16 v[48:51], v[72:75], a[0:7], v98\n"
    "v_cvt_pk_bf16_f32 v108, v32, v33\n"
    "v_smfmac_f32_16x16x64_bf16 v[48:51], v[76:79], a[8:15], v98\n"
    "s_nop 1\n"
    "v_add_f32 v36, v36, v38\n"
    "v_smfmac_f32_16x16x64_bf16 v[48:51], v[80:83], a[16:23], v98\n"
    "v_add_f32 v37, v37, v39\n"
    "v_smfmac_f32_16x16x64_bf16 v[52:55], v[72:75], a[24:31], v98\n"
    "v_cvt_pk_bf16_f32 v109, v36, v37\n"
    "v_smfmac_f32_16x16x64_bf16 v[52:55], v[76:79], a[32:39], v98\n"
    "s_nop 1\n"
    "v_add_f32 v48, v48, v50\n"
    "v_permlane16_swap_b32 v108, v109\n"
    "v_min_u32 v108, -2, v108\n"
    "v_smfmac_f32_16x16x64_bf16 v[52:55], v[80:83], a[40:47], v98\n"
    "buffer_store_dwordx2 v[108:109], v103, s[0:3], s36 offen sc1\n"
    "v_add_f32 v49, v49, v51\n"
    "v_smfmac_f32_16x16x64_bf16 v[56:59], v[72:75], a[48:55], v98\n"
    "v_cvt_pk_bf16_f32 v48, v48, v49\n"
    "v_smfmac_f32_16x16x64_bf16 v[56:59], v[76:79], a[56:63], v98\n"
    "s_nop 1\n"
    "v_add_f32 v52, v52, v54\n"
    "v_smfmac_f32_16x16x64_bf16 v[56:59], v[80:83], a[64:71], v98\n"
    "v_add_f32 v53, v53, v55\n"
    "v_smfmac_f32_16x16x64_bf16 v[60:63], v[72:75], a[72:79], v98\n"
    "v_cvt_pk_bf16_f32 v49, v52, v53\n"
    "v_smfmac_f32_16x16x64_bf16 v[60:63], v[76:79], a[80:87], v98\n"
    "s_waitcnt vmcnt(3)\n"
    "s_barrier\n"
    "v_add_f32 v56, v56, v58\n"
    "v_smfmac_f32_16x16x64_bf16 v[60:63], v[80:83], a[88:95], v98\n"
    "ds_read2_b64 v[32:35], v106 offset0:0 offset1:128\n"
    "v_add_f32 v57, v57, v59\n"
    "v_smfmac_f32_16x16x64_bf16 v[64:67], v[72:75], a[96:103], v98\n"
    "ds_read2_b64 v[36:39], v106 offset0:1 offset1:129\n"
    "v_cvt_pk_bf16_f32 v50, v56, v57\n"
    "v_smfmac_f32_16x16x64_bf16 v[64:67], v[76:79], a[104:111], v98\n"
    "s_nop 1\n"
    "v_add_f32 v60, v60, v62\n"
    "v_permlane32_swap_b32 v48, v50\n"
    "v_smfmac_f32_16x16x64_bf16 v[64:67], v[80:83], a[112:119], v98\n"
    "v_add_f32 v61, v61, v63\n"
    "v_smfmac_f32_16x16x64_bf16 v[68:71], v[72:75], a[120:127], v98\n"
    "v_cvt_pk_bf16_f32 v51, v60, v61\n"
    "v_smfmac_f32_16x16x64_bf16 v[68:71], v[76:79], a[128:135], v98\n"
    "s_nop 0\n"
    "v_permlane32_swap_b32 v49, v51\n"
    "v_smfmac_f32_16x16x64_bf16 v[68:71], v[80:83], a[136:143], v98\n"
    "s_nop 0\n"
    "v_permlane16_swap_b32 v50, v51\n"
    "v_permlane16_swap_b32 v48, v49\n"
    "v_min_u32 v48, -2, v48\n"
    "v_smfmac_f32_16x16x64_bf16 v[84:87], v[72:75], a[144:151], v98\n"
    "buffer_store_dwordx4 v[48:51], v102, s[0:3], s36 offen sc1\n"
    "v_add_f32 v64, v64, v66\n"
    "v_add_f32 v65, v65, v67\n"
    "v_cvt_pk_bf16_f32 v64, v64, v65\n"
    "v_smfmac_f32_16x16x64_bf16 v[84:87], v[76:79], a[152:159], v98\n"
    "v_add_f32 v68, v68, v70\n"
    "v_smfmac_f32_16x16x64_bf16 v[84:87], v[80:83], a[160:167], v98\n"
    "v_add_f32 v69, v69, v71\n"
    "v_smfmac_f32_16x16x64_bf16 v[88:91], v[72:75], a[168:175], v98\n"
    "v_cvt_pk_bf16_f32 v65, v68, v69\n"
    "v_smfmac_f32_16x16x64_bf16 v[88:91], v[76:79], a[176:183], v98\n"
    "s_nop 1\n"
    "v_add_f32 v84, v84, v86\n"
    "v_smfmac_f32_16x16x64_bf16 v[88:91], v[80:83], a[184:191], v98\n"
    ".Lvo_mk3x9_%=:\n"
    ".Lvo_uph_%=:\n"
    "s_waitcnt lgkmcnt(0)\n"
    "v_add_f32 v85, v85, v87\n"
    "v_mfma_f32_16x16x32_bf16 v[56:59], v[32:35], a[16:19], v[40:43]\n"
    "v_cvt_pk_bf16_f32 v66, v84, v85\n"
    "v_mfma_f32_16x16x32_bf16 v[60:63], v[36:39], a[16:19], v[44:47]\n"
    "s_nop 0\n"
    "v_permlane32_swap_b32 v64, v66\n"
    "v_add_f32 v88, v88, v90\n"
    "v_add_f32 v89, v89, v91\n"
    "v_cvt_pk_bf16_f32 v67, v88, v89\n"
    "s_nop 1\n"
    "v_permlane32_swap_b32 v65, v67\n"
    "s_nop 1\n"
    "v_permlane16_swap_b32 v66, v67\n"
    "v_permlane16_swap_b32 v64, v65\n"
    "v_min_u32 v64, -2, v64\n"
    "buffer_store_dwordx4 v[64:67], v102, s[0:3], s37 offen sc1\n"
    ".Lvo_mkum_%=:\n"
    "s_cmp_eq_u32 s34, 63\n"
    "s_cbranch_scc1 .Lvo_end_%=\n"
    "ds_read2_b64 v[40:43], v107 offset0:0 offset1:128\n"
    "ds_read2_b64 v[44:47], v107 offset0:1 offset1:129\n"
    "v_mfma_f32_16x16x32_bf16 v[48:51], v[0:3], a[24:27], 0\n"
    "v_mfma_f32_16x16x32_bf16 v[52:55], v[4:7], a[24:27], 0\n"
    ".Lvo_mkL0_%=:\n"
    "v_mfma_f32_16x16x32_bf16 v[48:51], v[8:11], a[28:31], v[48:51]\n"
    "v_mfma_f32_16x16x32_bf16 v[52:55], v[12:15], a[28:31], v[52:55]\n"
    "v_mfma_f32_16x16x32_bf16 v[48:51], v[16:19], a[32:35], v[48:51]\n"
    "v_mov_b64 v[88:89], -1\n"
    "v_mov_b64 v[90:91], -1\n"
    "v_mfma_f32_16x16x32_bf16 v[52:55], v[20:23], a[32:35], v[52:55]\n"
    "s_waitcnt lgkmcnt(0)\n"
    "v_mfma_f32_16x16x32_bf16 v[56:59], v[40:43], a[20:23], v[56:59]\n"
    "buffer_load_dwordx4 v96, s[0:3], s43 offen offset:2048 sc1 lds\n"
    "v_mfma_f32_16x16x32_bf16 v[60:63], v[44:47], a[20:23], v[60:63]\n"
    "v_mfma_f32_16x16x32_bf16 v[48:51], v[24:27], a[36:39], v[48:51]\n"
    "s_nop 1\n"
    "v_cvt_pk_bf16_f32 v72, v56, v57\n"
    "v_mfma_f32_16x16x32_bf16 v[52:55], v[28:31], a[36:39], v[52:55]\n"
    "v_cvt_pk_bf16_f32 v74, v58, v59\n"
    "v_cvt_pk_bf16_f32 v73, v60, v61\n"
    "v_mfma_f32_16x16x32_bf16 v[48:51], v[32:35], a[40:43], v[48:51]\n"
    "v_cvt_pk_bf16_f32 v75, v62, v63\n"
    "v_mfma_f32_16x16x32_bf16 v[52:55], v[36:39], a[40:43], v[52:55]\n"
    "buffer_load_dwordx4 v96, s[0:3], s43 offen offset:3072 sc1 lds\n"
    "v_mfma_f32_16x16x32_bf16 v[48:51], v[40:43], a[44:47], v[48:51]\n"
    "s_sub_u32 m0, s17, 4096\n"
    "s_add_u32 s43, s43, 4096\n"
    "v_mfma_f32_16x16x32_bf16 v[52:55], v[44:47], a[44:47], v[52:55]\n"
    "v_mfma_f32_16x16x32_bf16 v[56:59], v[0:3], a[48:51], 0\n"
    "s_nop 1\n"
    "v_cvt_pk_bf16_f32 v76, v48, v49\n"
    "v_mfma_f32_16x16x32_bf16 v[60:63], v[4:7], a[48:51], 0\n"
    "v_cvt_pk_bf16_f32 v78, v50, v51\n"
    "v_cvt_pk_bf16_f32 v77, v52, v53\n"
    "v_mfma_f32_16x16x32_bf16 v[56:59], v[8:11], a[52:55], v[56:59]\n"
    "v_cvt_pk_bf16_f32 v79, v54, v55\n"
    "v_mfma_f32_16x16x32_bf16 v[60:63], v[12:15], a[52:55], v[60:63]\n"
    "buffer_load_dwordx4 v96, s[0:3], s43 offen offset:0 sc1 lds\n"
    "v_mfma_f32_16x16x32_bf16 v[56:59], v[16:19], a[56:59], v[56:59]\n"
    "v_mfma_f32_16x16x32_bf16 v[60:63], v[20:23], a[56:59], v[60:63]\n"
    "v_mfma_f32_16x16x32_bf16 v[56:59], v[24:27], a[60:63], v[56:59]\n"
    "v_mfma_f32_16x16x32_bf16 v[60:63], v[28:31], a[60:63], v[60:63]\n"
    "buffer_load_dwordx4 v96, s[0:3], s43 offen offset:1024 sc1 lds\n"
    "v_mfma_f32_16x16x32_bf16 v[56:59], v[32:35], a[64:67], v[56:59]\n"
    "v_mfma_f32_16x16x32_bf16 v[60:63], v[36:39], a[64:67], v[60:63]\n"
    "v_mfma_f32_16x16x32_bf16 v[56:59], v[40:43], a[68:71], v[56:59]\n"
    "v_mfma_f32_16x16x32_bf16 v[60:63], v[44:47], a[68:71], v[60:63]\n"
    "buffer_load_dwordx4 v96, s[0:3], s43 offen offset:2048 sc1 lds\n"
    "v_mfma_f32_16x16x32_bf16 v[48:51], v[0:3], a[72:75], 0\n"
    "s_nop 1\n"
    "v_cvt_pk_bf16_f32 v80, v56, v57\n"
    "v_mfma_f32_16x16x32_bf16 v[52:55], v[4:7], a[72:75], 0\n"
    "v_cvt_pk_bf16_f32 v82, v58, v59\n"
    "v_cvt_pk_bf16_f32 v81, v60, v61\n"
    "v_mfma_f32_16x16x32_bf16 v[48:51], v[8:11], a[76:79], v[48:51]\n"
    "v_cvt_pk_bf16_f32 v83, v62, v63\n"
    "v_mfma_f32_16x16x32_bf16 v[52:55], v[12:15], a[76:79], v[52:55]\n"
    "buffer_store_dwordx4 v[72:75], v96, s[0:3], s40 offen sc1\n"
    "s_add_u32 s40, s40, 0x10000\n"
    "v_mfma_f32_16x16x32_bf16 v[48:51], v[16:19], a[80:83], v[48:51]\n"
    ".Lvo_mkL1_%=:\n"
    "v_mfma_f32_16x16x32_bf16 v[52:55], v[20:23], a[80:83], v[52:55]\n"
    "v_mfma_f32_16x16x32_bf16 v[48:51], v[24:27], a[84:87], v[48:51]\n"
    "v_mfma_f32_16x16x32_bf16 v[52:55], v[28:31], a[84:87], v[52:55]\n"
    "v_mfma_f32_16x16x32_bf16 v[48:51], v[32:35], a[88:91], v[48:51]\n"
    "v_mfma_f32_16x16x32_bf16 v[52:55], v[36:39], a[88:91], v[52:55]\n"
    "v_mfma_f32_16x16x32_bf16 v[48:51], v[40:43], a[92:95], v[48:51]\n"
    "buffer_load_dwordx4 v96, s[0:3], s43 offen offset:3072 sc1 lds\n"
    "v_mfma_f32_16x16x32_bf16 v[52:55], v[44:47], a[92:95], v[52:55]\n"
    "v_mfma_f32_16x16x32_bf16 v[56:59], v[0:3], a[96:99], 0\n"
    "s_nop 1\n"
    "v_cvt_pk_bf16_f32 v72, v48, v49\n"
    "v_mfma_f32_16x16x32_bf16 v[60:63], v[4:7], a[96:99], 0\n"
    "v_cvt_pk_bf16_f32 v74, v50, v51\n"
    "v_cvt_pk_bf16_f32 v73, v52, v53\n"
    "v_mfma_f32_16x16x32_bf16 v[56:59], v[8:11], a[100:103], v[56:59]\n"
    "buffer_store_dwordx4 v[76:79], v96, s[0:3], s40 offen sc1\n"
    "v_cvt_pk_bf16_f32 v75, v54, v55\n"
    "s_add_u32 s40, s40, 0x10000\n"
    "s_mov_b32 m0, s48\n"
    "v_mfma_f32_16x16x32_bf16 v[60:63], v[12:15], a[100:103], v[60:63]\n"
    "s_add_u32 s43, s43, s19\n"
    "v_mfma_f32_16x16x32_bf16 v[56:59], v[16:19], a[104:107], v[56:59]\n"
    "v_mfma_f32_16x16x32_bf16 v[60:63], v[20:23], a[104:107], v[60:63]\n"
    "v_mfma_f32_16x16x32_bf16 v[56:59], v[24:27], a[108:111], v[56:59]\n"
    "v_mfma_f32_16x16x32_bf16 v[60:63], v[28:31], a[108:111], v[60:63]\n"
    "v_mfma_f32_16x16x32_bf16 v[56:59], v[32:35], a[112:115], v[56:59]\n"
    "v_mfma_f32_16x16x32_bf16 v[60:63], v[36:39], a[112:115], v[60:63]\n"
    "buffer_load_dwordx4 v96, s[0:3], s43 offen offset:0 sc1 lds\n"
    "v_mfma_f32_16x16x32_bf16 v[56:59], v[40:43], a[116:119], v[56:59]\n"
    "v_mfma_f32_16x16x32_bf16 v[60:63], v[44:47], a[116:119], v[60:63]\n"
    "v_mfma_f32_16x16x32_bf16 v[48:51], v[0:3], a[120:123], 0\n"
    "s_nop 1\n"
    "v_cvt_pk_bf16_f32 v76, v56, v57\n"
    "v_mfma_f32_16x16x32_bf16 v[52:55], v[4:7], a[120:123], 0\n"
    "buffer_store_dwordx4 v[80:83], v96, s[0:3], s40 offen sc1\n"
    "v_cvt_pk_bf16_f32 v78, v58, v59\n"
    "v_cvt_pk_bf16_f32 v77, v60, v61\n"
    "s_add_u32 s40, s40, 0x10000\n"
    "v_mfma_f32_16x16x32_bf16 v[48:51], v[8:11], a[124:127], v[48:51]\n"
    "v_cvt_pk_bf16_f32 v79, v62, v63\n"
    "v_mfma_f32_16x16x32_bf16 v[52:55], v[12:15], a[124:127], v[52:55]\n"
    "v_mfma_f32_16x16x32_bf16 v[48:51], v[16:19], a[128:131], v[48:51]\n"
    "v_mfma_f32_16x16x32_bf16 v[52:55], v[20:23], a[128:131], v[52:55]\n"
    "v_mfma_f32_16x16x32_bf16 v[48:51], v[24:27], a[132:135], v[48:51]\n"
    "v_mfma_f32_16x16x32_bf16 v[52:55], v[28:31], a[132:135], v[52:55]\n"
    "v_mfma_f32_16x16x32_bf16 v[48:51], v[32:35], a[136:139], v[48:51]\n"
    "buffer_load_dwordx4 v96, s[0:3], s43 offen offset:1024 sc1 lds\n"
    "v_mfma_f32_16x16x32_bf16 v[52:55], v[36:39], a[136:139], v[52:55]\n"
    "v_mfma_f32_16x16x32_bf16 v[48:51], v[40:43], a[140:143], v[48:51]\n"
    ".Lvo_mkL2_%=:\n"
    "v_mfma_f32_16x16x32_bf16 v[52:55], v[44:47], a[140:143], v[52:55]\n"
    "v_mfma_f32_16x16x32_bf16 v[56:59], v[0:3], a[144:147], 0\n"
    "buffer_store_dwordx4 v[72:75], v96, s[0:3], s40 offen sc1\n"
    "s_nop 0\n"
    "v_cvt_pk_bf16_f32 v80, v48, v49\n"
    "s_add_u32 s40, s40, 0x10000\n"
    "v_mfma_f32_16x16x32_bf16 v[60:63], v[4:7], a[144:147], 0\n"
    "v_cvt_pk_bf16_f32 v82, v50, v51\n"
    "v_cvt_pk_bf16_f32 v81, v52, v53\n"
    "v_mfma_f32_16x16x32_bf16 v[56:59], v[8:11], a[148:151], v[56:59]\n"
    "v_cvt_pk_bf16_f32 v83, v54, v55\n"
    "v_mfma_f32_16x16x32_bf16 v[60:63], v[12:15], a[148:151], v[60:63]\n"
    "v_mfma_f32_16x16x32_bf16 v[56:59], v[16:19], a[152:155], v[56:59]\n"
    "v_mfma_f32_16x16x32_bf16 v[60:63], v[20:23], a[152:155], v[60:63]\n"
    "v_mfma_f32_16x16x32_bf16 v[56:59], v[24:27], a[156:159], v[56:59]\n"
    "v_mfma_f32_16x16x32_bf16 v[60:63], v[28:31], a[156:159], v[60:63]\n"
    "buffer_load_dwordx2 v[108:109], off, s[0:3], s45 sc1\n"
    "v_mfma_f32_16x16x32_bf16 v[56:59], v[32:35], a[160:163], v[56:59]\n"
    "v_mfma_f32_16x16x32_bf16 v[60:63], v[36:39], a[160:163], v[60:63]\n"
    "v_mfma_f32_16x16x32_bf16 v[56:59], v[40:43], a[164:167], v[56:59]\n"
    "v_mfma_f32_16x16x32_bf16 v[60:63], v[44:47], a[164:167], v[60:63]\n"
    "buffer_store_dwordx4 v[76:79], v96, s[0:3], s40 offen sc1\n"
    "s_add_u32 s40, s40, 0x10000\n"
    "v_mfma_f32_16x16x32_bf16 v[48:51], v[0:3], a[168:171], 0\n"
    "s_nop 1\n"
    "v_cvt_pk_bf16_f32 v72, v56, v57\n"
    "v_mfma_f32_16x16x32_bf16 v[52:55], v[4:7], a[168:171], 0\n"
    "v_cvt_pk_bf16_f32 v74, v58, v59\n"
    "v_cvt_pk_bf16_f32 v73, v60, v61\n"
    "v_mfma_f32_16x16x32_bf16 v[48:51], v[8:11], a[172:175], v[48:51]\n"
    "v_cvt_pk_bf16_f32 v75, v62, v63\n"
    "v_mfma_f32_16x16x32_bf16 v[52:55], v[12:15], a[172:175], v[52:55]\n"
    "v_mfma_f32_16x16x32_bf16 v[48:51], v[16:19], a[176:179], v[48:51]\n"
    "v_mfma_f32_16x16x32_bf16 v[52:55], v[20:23], a[176:179], v[52:55]\n"
    "v_mfma_f32_16x16x32_bf16 v[48:51], v[24:27], a[180:183], v[48:51]\n"
    "buffer_store_dwordx4 v[88:91], v102, s[0:3], s38 offen sc1\n"
    "v_mfma_f32_16x16x32_bf16 v[52:55], v[28:31], a[180:183], v[52:55]\n"
    "v_mfma_f32_16x16x32_bf16 v[48:51], v[32:35], a[184:187], v[48:51]\n"
    "v_mfma_f32_16x16x32_bf16 v[52:55], v[36:39], a[184:187], v[52:55]\n"
    "v_mfma_f32_16x16x32_bf16 v[48:51], v[40:43], a[188:191], v[48:51]\n"
    "v_mfma_f32_16x16x32_bf16 v[52:55], v[44:47], a[188:191], v[52:55]\n"
    "v_mfma_f32_16x16x32_bf16 v[56:59], v[0:3], a[192:195], 0\n"
    "s_nop 1\n"
    "v_cvt_pk_bf16_f32 v76, v48, v49\n"
    "v_mfma_f32_16x16x32_bf16 v[60:63], v[4:7], a[192:195], 0\n"
    "buffer_store_dwordx4 v[80:83], v96, s[0:3], s40 offen sc1\n"
    "v_cvt_pk_bf16_f32 v78, v50, v51\n"
    "v_cvt_pk_bf16_f32 v77, v52, v53\n"
    "s_add_u32 s40, s40, 0x10000\n"
    "v_mfma_f32_16x16x32_bf16 v[56:59], v[8:11], a[196:199], v[56:59]\n"
    "v_cvt_pk_bf16_f32 v79, v54, v55\n"
    "v_mfma_f32_16x16x32_bf16 v[60:63], v[12:15], a[196:199], v[60:63]\n"
    "v_mfma_f32_16x16x32_bf16 v[56:59], v[16:19], a[200:203], v[56:59]\n"
    ".Lvo_mkL3_%=:\n"
    "v_mfma_f32_16x16x32_bf16 v[60:63], v[20:23], a[200:203], v[60:63]\n"
    "v_mfma_f32_16x16x32_bf16 v[56:59], v[24:27], a[204:207], v[56:59]\n"
    "v_mfma_f32_16x16x32_bf16 v[60:63], v[28:31], a[204:207], v[60:63]\n"
    "v_mfma_f32_16x16x32_bf16 v[56:59], v[32:35], a[208:211], v[56:59]\n"
    "buffer_store_dwordx4 v[88:91], v102, s[0:3], s39 offen sc1\n"
    "v_mfma_f32_16x16x32_bf16 v[60:63], v[36:39], a[208:211], v[60:63]\n"
    "v_mfma_f32_16x16x32_bf16 v[56:59], v[40:43], a[212:215], v[56:59]\n"
    "v_mfma_f32_16x16x32_bf16 v[60:63], v[44:47], a[212:215], v[60:63]\n"
    "s_waitcnt vmcnt(15)\n"
    "s_barrier\n"
    "v_mfma_f32_16x16x32_bf16 v[48:51], v[24:27], a[228:231], 0\n"
    "s_nop 1\n"
    "v_cvt_pk_bf16_f32 v80, v56, v57\n"
    "v_mfma_f32_16x16x32_bf16 v[52:55], v[28:31], a[228:231], 0\n"
    "v_cvt_pk_bf16_f32 v82, v58, v59\n"
    "v_cvt_pk_bf16_f32 v81, v60, v61\n"
    "v_mfma_f32_16x16x32_bf16 v[48:51], v[32:35], a[232:235], v[48:51]\n"
    "ds_read_b128 v[24:27], v92 offset:0\n"
    "v_cvt_pk_bf16_f32 v83, v62, v63\n"
    "v_mfma_f32_16x16x32_bf16 v[52:55], v[36:39], a[232:235], v[52:55]\n"
    "buffer_store_dwordx4 v[72:75], v96, s[0:3], s40 offen sc1\n"
    "ds_read_b128 v[28:31], v92 offset:1024\n"
    "s_add_u32 s40, s40, 0x10000\n"
    "v_mfma_f32_16x16x32_bf16 v[48:51], v[40:43], a[236:239], v[48:51]\n"
    "ds_read_b128 v[32:35], v93 offset:0\n"
    "v_mfma_f32_16x16x32_bf16 v[52:55], v[44:47], a[236:239], v[52:55]\n"
    "ds_read_b128 v[36:39], v93 offset:1024\n"
    "v_mfma_f32_16x16x32_bf16 v[48:51], v[0:3], a[216:219], v[48:51]\n"
    "ds_read_b128 v[40:43], v94 offset:0\n"
    "v_mfma_f32_16x16x32_bf16 v[52:55], v[4:7], a[216:219], v[52:55]\n"
    "ds_read_b128 v[44:47], v94 offset:1024\n"
    "v_mfma_f32_16x16x32_bf16 v[48:51], v[8:11], a[220:223], v[48:51]\n"
    ".Lvo_mkL4_%=:\n"
    "v_mfma_f32_16x16x32_bf16 v[52:55], v[12:15], a[220:223], v[52:55]\n"
    "v_mfma_f32_16x16x32_bf16 v[48:51], v[16:19], a[224:227], v[48:51]\n"
    "buffer_store_dwordx2 v[88:89], v103, s[0:3], s38 offen sc1\n"
    "v_mfma_f32_16x16x32_bf16 v[52:55], v[20:23], a[224:227], v[52:55]\n"
    ".Lvo_mk4x10_%=:\n"
    ".Lvo_vph_%=:\n"
    "s_waitcnt lgkmcnt(4)\n"
    "v_mfma_f32_16x16x32_bf16 v[64:67], a[240:243], v[24:27], 0\n"
    "s_nop 1\n"
    "v_cvt_pk_bf16_f32 v72, v48, v49\n"
    "v_mfma_f32_16x16x32_bf16 v[84:87], v[176:179], v[24:27], 0\n"
    "s_add_u32 s34, s34, 1\n"
    "v_cvt_pk_bf16_f32 v74, v50, v51\n"
    "v_cvt_pk_bf16_f32 v73, v52, v53\n"
    "v_mfma_f32_16x16x32_bf16 v[68:71], a[240:243], v[28:31], 0\n"
    "s_add_u32 s36, s34, 0\n"
    "ds_read_b128 v[24:27], v95 offset:0\n"
    "v_cvt_pk_bf16_f32 v75, v54, v55\n"
    "v_mfma_f32_16x16x32_bf16 v[88:91], v[176:179], v[28:31], 0\n"
    "s_waitcnt lgkmcnt(3)\n"
    "ds_read_b128 v[28:31], v95 offset:1024\n"
    "s_and_b32 s36, s36, 3\n"
    "v_mfma_f32_16x16x32_bf16 v[64:67], v[112:115], v[32:35], v[64:67]\n"
    "s_lshl_b32 s36, s36, 22\n"
    "s_add_u32 s41, s15, s36\n"
    "s_add_u32 s42, s16, s36\n"
    "v_mfma_f32_16x16x32_bf16 v[84:87], v[192:195], v[32:35], v[84:87]\n"
    "s_add_u32 s37, s36, 0x10000\n"
    "s_add_u32 s50, s34, 3\n"
    "ds_read_b128 v[0:3], v97 offset:0\n"
    "v_mfma_f32_16x16x32_bf16 v[68:71], v[112:115], v[36:39], v[68:71]\n"
    "buffer_store_dwordx4 v[76:79], v96, s[0:3], s40 offen sc1\n"
    "ds_read_b128 v[32:35], v92 offset:2048\n"
    "s_add_u32 s40, s40, 0x10000\n"
    "s_cmp_gt_i32 s50, 64\n"
    "s_cselect_b32 s50, 0, s50\n"
    "v_mfma_f32_16x16x32_bf16 v[88:91], v[192:195], v[36:39], v[88:91]\n"
    "s_waitcnt lgkmcnt(4)\n"
    "ds_read_b128 v[36:39], v92 offset:3072\n"
    "s_add_u32 s51, s34, 4\n"
    "v_mfma_f32_16x16x32_bf16 v[64:67], v[128:131], v[40:43], v[64:67]\n"
    "s_cmp_gt_i32 s51, 64\n"
    "s_cselect_b32 s51, 0, s51\n"
    "s_mul_i32 s51, s51, s14\n"
    "v_mfma_f32_16x16x32_bf16 v[84:87], v[208:211], v[40:43], v[84:87]\n"
    "ds_read_b128 v[4:7], v97 offset:1024\n"
    "v_mov_b32 v111, s50\n"
    "v_mfma_f32_16x16x32_bf16 v[68:71], v[128:131], v[44:47], v[68:71]\n"
    "ds_read_b128 v[40:43], v93 offset:2048\n"
    "v_mfma_f32_16x16x32_bf16 v[88:91], v[208:211], v[44:47], v[88:91]\n"
    "s_waitcnt lgkmcnt(5)\n"
    "ds_read_b128 v[44:47], v93 offset:3072\n"
    "v_mfma_f32_16x16x32_bf16 v[64:67], v[144:147], v[24:27], v[64:67]\n"
    "s_mov_b64 exec, 0x1f0\n"
    "v_mov_b32 v111, s51\n"
    "s_mov_b64 exec, -1\n"
    "v_mfma_f32_16x16x32_bf16 v[84:87], v[224:227], v[24:27], v[84:87]\n"
    "buffer_store_dwordx4 v[80:83], v104, s[0:3], s38 offen sc1\n"
    "ds_read_b128 v[8:11], v97 offset:2048\n"
    "v_mfma_f32_16x16x32_bf16 v[68:71], v[144:147], v[28:31], v[68:71]\n"
    "ds_read_b128 v[24:27], v94 offset:2048\n"
    "v_mfma_f32_16x16x32_bf16 v[88:91], v[224:227], v[28:31], v[88:91]\n"
    "s_waitcnt lgkmcnt(5)\n"
    "ds_read_b128 v[28:31], v94 offset:3072\n"
    "v_mfma_f32_16x16x32_bf16 v[64:67], a[244:247], v[32:35], v[64:67]\n"
    "ds_read_b128 v[12:15], v97 offset:3072\n"
    "v_mfma_f32_16x16x32_bf16 v[84:87], v[180:183], v[32:35], v[84:87]\n"
    "ds_read_b128 v[16:19], v97 offset:4096\n"
    "v_mfma_f32_16x16x32_bf16 v[68:71], a[244:247], v[36:39], v[68:71]\n"
    "ds_read_b128 v[32:35], v95 offset:2048\n"
    "v_mfma_f32_16x16x32_bf16 v[88:91], v[180:183], v[36:39], v[88:91]\n"
    "s_waitcnt lgkmcnt(6)\n"
    "ds_read_b128 v[36:39], v95 offset:3072\n"
    "v_mfma_f32_16x16x32_bf16 v[64:67], v[116:119], v[40:43], v[64:67]\n"
    "buffer_store_dwordx4 v[72:75], v104, s[0:3], s39 offen sc1\n"
    "s_waitcnt vmcnt(11)\n"
    "ds_read_b128 v[20:23], v97 offset:5120\n"
    "v_mfma_f32_16x16x32_bf16 v[84:87], v[196:199], v[40:43], v[84:87]\n"
    "s_barrier\n"
    "ds_read_b128 v[48:51], v97 offset:8192\n"
    "v_mfma_f32_16x16x32_bf16 v[68:71], v[116:119], v[44:47], v[68:71]\n"
    "ds_read_b128 v[40:43], v92 offset:4096\n"
    "v_mfma_f32_16x16x32_bf16 v[88:91], v[196:199], v[44:47], v[88:91]\n"
    "s_waitcnt lgkmcnt(7)\n"
    "ds_read_b128 v[44:47], v92 offset:5120\n"
    "v_mfma_f32_16x16x32_bf16 v[64:67], v[132:135], v[24:27], v[64:67]\n"
    "ds_read_b128 v[52:55], v97 offset:9216\n"
    ".Lvo_mkL5_%=:\n"
    "v_mfma_f32_16x16x32_bf16 v[84:87], v[212:215], v[24:27], v[84:87]\n"
    "ds_read_b128 v[56:59], v97 offset:10240\n"
    "v_mfma_f32_16x16x32_bf16 v[68:71], v[132:135], v[28:31], v[68:71]\n"
    "ds_read_b128 v[24:27], v93 offset:4096\n"
    "v_mfma_f32_16x16x32_bf16 v[88:91], v[212:215], v[28:31], v[88:91]\n"
    "s_waitcnt lgkmcnt(7)\n"
    "ds_read_b128 v[28:31], v93 offset:5120\n"
    "s_waitcnt vmcnt(9)\n"
    "v_mfma_f32_16x16x32_bf16 v[64:67], v[148:151], v[32:35], v[64:67]\n"
    "s_memrealtime s[54:55]\n"
    "v_readfirstlane_b32 s32, v108\n"
    "v_readfirstlane_b32 s33, v109\n"
    "v_mfma_f32_16x16x32_bf16 v[84:87], v[228:231], v[32:35], v[84:87]\n"
    "ds_read_b128 v[60:63], v97 offset:11264\n"
    "v_mfma_f32_16x16x32_bf16 v[68:71], v[148:151], v[36:39], v[68:71]\n"
    "s_cmp_eq_u32 s33, s35\n"
    "s_cselect_b32 s46, s32, s46\n"
    "ds_read_b128 v[32:35], v94 offset:4096\n"
    "v_mfma_f32_16x16x32_bf16 v[88:91], v[228:231], v[36:39], v[88:91]\n"
    "s_waitcnt lgkmcnt(6)\n"
    "ds_read_b128 v[36:39], v94 offset:5120\n"
    "s_add_u32 s46, s46, 256\n"
    "v_mfma_f32_16x16x32_bf16 v[64:67], a[248:251], v[40:43], v[64:67]\n"
    "s_sub_u32 s47, s46, 59\n"
    "s_mov_b32 m0, s17\n"
    "v_mfma_f32_16x16x32_bf16 v[84:87], v[184:187], v[40:43], v[84:87]\n"
    ".Lvo_mkL6_%=:\n"
    "v_mfma_f32_16x16x32_bf16 v[68:71], a[248:251], v[44:47], v[68:71]\n"
    "ds_read_b128 v[40:43], v95 offset:4096\n"
    "s_sub_u32 s56, s47, s54\n"
    "v_mfma_f32_16x16x32_bf16 v[88:91], v[184:187], v[44:47], v[88:91]\n"
    "s_waitcnt lgkmcnt(4)\n"
    "ds_read_b128 v[44:47], v95 offset:5120\n"
    ".Lvo_mk6x11_%=:\n"
    "s_cmp_gt_i32 s56, 4\n"
    "s_cbranch_scc1 .Lvo_early_%=\n"
    "buffer_load_dwordx4 v96, s[0:3], s41 offen sc1 lds\n"
    "buffer_load_dwordx4 v96, s[0:3], s41 offen offset:1024 sc1 lds\n"
    ".Lvo_asked_%=:\n"
    ".Lvo_mk7x13_%=:\n"
    "v_mfma_f32_16x16x32_bf16 v[64:67], v[120:123], v[24:27], v[64:67]\n"
    "s_add_u32 s49, s34, 3\n"
    "s_lshl_b32 s43, s49, 19\n"
    "s_lshl_b32 s33, s49, 11\n"
    "v_mfma_f32_16x16x32_bf16 v[84:87], v[200:203], v[24:27], v[84:87]\n"
    "s_xor_b32 s33, s33, s13\n"
    "s_and_b32 s33, s33, 0x2000\n"
    "s_add_u32 s43, s43, s33\n"
    ".Lvo_mkM0_%=:\n"
    "v_mfma_f32_16x16x32_bf16 v[68:71], v[120:123], v[28:31], v[68:71]\n"
    "ds_read_b128 v[24:27], v92 offset:6144\n"
    "s_add_u32 s43, s43, s18\n"
    "v_mfma_f32_16x16x32_bf16 v[88:91], v[200:203], v[28:31], v[88:91]\n"
    "s_waitcnt lgkmcnt(3)\n"
    "ds_read_b128 v[28:31], v92 offset:7168\n"
    "v_mfma_f32_16x16x32_bf16 v[64:67], v[136:139], v[32:35], v[64:67]\n"
    "s_cmp_gt_i32 s49, 63\n"
    "s_cselect_b32 s43, 2130706432, s43\n"
    "s_add_u32 s49, s34, 2\n"
    "v_mfma_f32_16x16x32_bf16 v[84:87], v[216:219], v[32:35], v[84:87]\n"
    "s_lshl_b32 s44, s49, 20\n"
    "s_lshl_b32 s33, s49, 12\n"
    "s_xor_b32 s33, s33, s13\n"
    "v_mfma_f32_16x16x32_bf16 v[68:71], v[136:139], v[36:39], v[68:71]\n"
    "ds_read_b128 v[32:35], v93 offset:6144\n"
    "s_and_b32 s33, s33, 0x2000\n"
    "v_mfma_f32_16x16x32_bf16 v[88:91], v[216:219], v[36:39], v[88:91]\n"
    "s_waitcnt lgkmcnt(3)\n"
    "ds_read_b128 v[36:39], v93 offset:7168\n"
    "s_add_u32 s44, s44, s33\n"
    "v_mfma_f32_16x16x32_bf16 v[64:67], v[152:155], v[40:43], v[64:67]\n"
    "buffer_load_dwordx4 v96, s[0:3], s42 offen offset:2048 sc1 lds\n"
    "v_mfma_f32_16x16x32_bf16 v[84:87], v[232:235], v[40:43], v[84:87]\n"
    "s_add_u32 s44, s44, s20\n"
    "s_cmp_gt_i32 s49, 63\n"
    "s_cselect_b32 s44, 2130706432, s44\n"
    "v_mfma_f32_16x16x32_bf16 v[68:71], v[152:155], v[44:47], v[68:71]\n"
    "ds_read_b128 v[40:43], v94 offset:6144\n"
    "s_add_u32 s38, s34, 2\n"
    ".Lvo_mkM1_%=:\n"
    "v_mfma_f32_16x16x32_bf16 v[88:91], v[232:235], v[44:47], v[88:91]\n"
    "s_waitcnt lgkmcnt(3)\n"
    "ds_read_b128 v[44:47], v94 offset:7168\n"
    "s_and_b32 s38, s38, 3\n"
    "v_mfma_f32_16x16x32_bf16 v[64:67], a[252:255], v[24:27], v[64:67]\n"
    "s_lshl_b32 s38, s38, 22\n"
    "s_add_u32 s39, s38, 0x10000\n"
    "s_add_u32 s40, s22, s38\n"
    "v_mfma_f32_16x16x32_bf16 v[84:87], v[188:191], v[24:27], v[84:87]\n"
    "s_and_b32 s45, s34, 63\n"
    "s_lshl_b32 s45, s45, 7\n"
    "s_add_u32 s45, s23, s45\n"
    "v_mfma_f32_16x16x32_bf16 v[68:71], a[252:255], v[28:31], v[68:71]\n"
    "ds_read_b128 v[24:27], v95 offset:6144\n"
    "s_add_u32 s35, s34, 1\n"
    "v_mfma_f32_16x16x32_bf16 v[88:91], v[188:191], v[28:31], v[88:91]\n"
    "s_waitcnt lgkmcnt(3)\n"
    "ds_read_b128 v[28:31], v95 offset:7168\n"
    "v_mfma_f32_16x16x32_bf16 v[64:67], v[124:127], v[32:35], v[64:67]\n"
    "s_cmp_lt_i32 s34, 0\n"
    "s_cselect_b32 s35, -5, s35\n"
    "s_cselect_b32 s52, 0, -1\n"
    "v_mfma_f32_16x16x32_bf16 v[84:87], v[204:207], v[32:35], v[84:87]\n"
    "buffer_load_dword v110, v105, s[0:3], 0 offen sc1\n"
    "v_mfma_f32_16x16x32_bf16 v[68:71], v[124:127], v[36:39], v[68:71]\n"
    "ds_read_b128 v[32:35], v96 offset:53248\n"
    "v_mfma_f32_16x16x32_bf16 v[88:91], v[204:207], v[36:39], v[88:91]\n"
    "s_waitcnt lgkmcnt(3)\n"
    "ds_read_b128 v[36:39], v96 offset:54272\n"
    "v_mfma_f32_16x16x32_bf16 v[64:67], v[140:143], v[40:43], v[64:67]\n"
    ".Lvo_mkM2_%=:\n"
    "v_mfma_f32_16x16x32_bf16 v[84:87], v[220:223], v[40:43], v[84:87]\n"
    "v_mfma_f32_16x16x32_bf16 v[68:71], v[140:143], v[44:47], v[68:71]\n"
    "ds_read_b128 v[40:43], v96 offset:55296\n"
    "v_mfma_f32_16x16x32_bf16 v[88:91], v[220:223], v[44:47], v[88:91]\n"
    "s_waitcnt lgkmcnt(3)\n"
    "ds_read_b128 v[44:47], v96 offset:56320\n"
    "v_mfma_f32_16x16x32_bf16 v[64:67], v[156:159], v[24:27], v[64:67]\n"
    "v_mfma_f32_16x16x32_bf16 v[84:87], v[236:239], v[24:27], v[84:87]\n"
    "v_mfma_f32_16x16x32_bf16 v[68:71], v[156:159], v[28:31], v[68:71]\n"
    "ds_read_b128 v[24:27], v96 offset:57344\n"
    ".Lvo_mkM3_%=:\n"
    "v_mfma_f32_16x16x32_bf16 v[88:91], v[236:239], v[28:31], v[88:91]\n"
    "s_waitcnt lgkmcnt(3)\n"
    "ds_read_b128 v[28:31], v96 offset:58368\n"
    "v_mfma_f32_16x16x32_bf16 v[64:67], v[160:163], v[32:35], v[64:67]\n"
    "v_mfma_f32_16x16x32_bf16 v[84:87], v[240:243], v[32:35], v[84:87]\n"
    "ds_read_b128 v[32:35], v96 offset:59392\n"
    "v_mfma_f32_16x16x32_bf16 v[68:71], v[160:163], v[36:39], v[68:71]\n"
    "v_mfma_f32_16x16x32_bf16 v[88:91], v[240:243], v[36:39], v[88:91]\n"
    "s_waitcnt lgkmcnt(3)\n"
    "ds_read_b128 v[36:39], v96 offset:60416\n"
    "v_mfma_f32_16x16x32_bf16 v[64:67], v[164:167], v[40:43], v[64:67]\n"
    "s_waitcnt lgkmcnt(2)\n"
    "v_mfma_f32_16x16x32_bf16 v[64:67], v[168:171], v[24:27], v[64:67]\n"
    "s_waitcnt lgkmcnt(1)\n"
    "v_mfma_f32_16x16x32_bf16 v[64:67], v[172:175], v[32:35], v[64:67]\n"
    ".Lvo_mkM4_%=:\n"
    "v_mfma_f32_16x16x32_bf16 v[84:87], v[244:247], v[40:43], v[84:87]\n"
    "s_branch .Lvo_take_%=\n"
    ".Lvo_tk0_%=:\n"
    ".Lvo_po3_%=:\n"
    "buffer_load_dword v109, v108, s[0:3], 0 offen sc1\n"
    "s_waitcnt vmcnt(0)\n"
    "v_cmp_gt_i32 vcc, 32, v109\n"
    "s_nop 0\n"
    "s_cbranch_vccz .Lvo_pd4_%=\n"
    "s_add_u32 s28, s28, 1\n"
    "s_cmp_lt_u32 s28, 0x40000\n"
    "s_cbranch_scc1 .Lvo_sp5_%=\n"
    "v_mov_b32 v109, 90\n"
    "s_mov_b32 s32, 33728\n"
    "buffer_store_dword v109, off, s[0:3], s32 sc1\n"
    "s_waitcnt vmcnt(0)\n"
    "s_branch .Lvo_pd4_%=\n"
    ".Lvo_sp5_%=:\n"
    "s_sleep 8\n"
    "s_branch .Lvo_po3_%=\n"
    ".Lvo_pd4_%=:\n"
    "s_branch .Lvo_tk1_%=\n"
    ".Lvo_early_%=:\n"
    ".Lvo_rt13_%=:\n"
    "s_memrealtime s[30:31]\n"
    "s_waitcnt lgkmcnt(0)\n"
    "s_sub_u32 s32, s47, s30\n"
    "s_cmp_lt_u32 s32, 600\n"
    "s_cbranch_scc1 .Lvo_rt13_%=\n"
    "buffer_load_dwordx4 v96, s[0:3], s41 offen sc1 lds\n"
    "buffer_load_dwordx4 v96, s[0:3], s41 offen offset:1024 sc1 lds\n"
    "s_branch .Lvo_asked_%=\n"
    ".Lvo_retry_%=:\n"
    "s_nop 7\n"
    "s_nop 7\n"
    "s_cmp_lt_i32 s34, 0\n"
    "s_cbranch_scc0 .Lvo_again_%=\n"
    "v_cvt_pk_bf16_f32 v44, v68, v69\n"
    "v_cvt_pk_bf16_f32 v45, v70, v71\n"
    "v_cvt_pk_bf16_f32 v46, v88, v89\n"
    "v_cvt_pk_bf16_f32 v47, v90, v91\n"
    "ds_write_b128 v92, v[44:47] offset:12288\n"
    "s_waitcnt vmcnt(0) lgkmcnt(0)\n"
    "s_barrier\n"
    "v_cmp_gt_i32 vcc, v111, v110\n"
    "s_nop 0\n"
    "s_cbranch_vccz .Lvo_e2_%=\n"
    ".Lvo_po15_%=:\n"
    "buffer_load_dword v110, v105, s[0:3], 0 offen sc1\n"
    "s_waitcnt vmcnt(0)\n"
    "v_cmp_gt_i32 vcc, v111, v110\n"
    "s_nop 0\n"
    "s_cbranch_vccz .Lvo_pd16_%=\n"
    "s_add_u32 s28, s28, 1\n"
    "s_cmp_lt_u32 s28, 0x40000\n"
    "s_cbranch_scc1 .Lvo_sp17_%=\n"
    "v_mov_b32 v110, 32\n"
    "s_mov_b32 s32, 33728\n"
    "buffer_store_dword v110, off, s[0:3], s32 sc1\n"
    "s_waitcnt vmcnt(0)\n"
    "s_branch .Lvo_pd16_%=\n"
    ".Lvo_sp17_%=:\n"
    "s_sleep 4\n"
    "s_branch .Lvo_po15_%=\n"
    ".Lvo_pd16_%=:\n"
    ".Lvo_e2_%=:\n"
    "s_mov_b32 m0, s21\n"
    "s_nop 0\n"
    "buffer_load_dwordx4 v96, s[0:3], s44 offen sc1 lds\n"
    "s_sub_u32 m0, s17, 8192\n"
    "s_nop 0\n"
    "buffer_load_dwordx4 v96, s[0:3], s43 offen offset:0 sc1 lds\n"
    "buffer_load_dwordx4 v96, s[0:3], s43 offen offset:1024 sc1 lds\n"
    "s_add_u32 s49, s34, 3\n"
    "v_lshlrev_b32 v108, 4, v96\n"
    "v_mov_b32 v109, s49\n"
    "s_mov_b64 exec, 15\n"
    "buffer_store_dword v109, v108, s[0:3], s25 offen sc1\n"
    "s_mov_b64 exec, -1\n"
    "ds_read_b128 v[0:3], v96 offset:11264\n"
    "ds_read_b128 v[4:7], v96 offset:12288\n"
    "ds_read_b128 v[8:11], v96 offset:24576\n"
    "ds_read_b128 v[12:15], v96 offset:25600\n"
    "ds_read_b128 v[16:19], v96 offset:37888\n"
    "ds_read_b128 v[20:23], v96 offset:38912\n"
    "ds_read_b128 v[24:27], v96 offset:51200\n"
    "ds_read_b128 v[28:31], v96 offset:52224\n"
    "s_waitcnt vmcnt(3) lgkmcnt(0)\n"
    "s_barrier\n"
    "ds_read2_b64 v[32:35], v106 offset0:0 offset1:128\n"
    "ds_read2_b64 v[36:39], v106 offset0:1 offset1:129\n"
    "v_mfma_f32_16x16x32_bf16 v[40:43], v[0:3], a[0:3], 0\n"
    "v_mfma_f32_16x16x32_bf16 v[44:47], v[4:7], a[0:3], 0\n"
    "v_mfma_f32_16x16x32_bf16 v[40:43], v[8:11], a[4:7], v[40:43]\n"
    "v_mfma_f32_16x16x32_bf16 v[44:47], v[12:15], a[4:7], v[44:47]\n"
    "v_mfma_f32_16x16x32_bf16 v[40:43], v[16:19], a[8:11], v[40:43]\n"
    "v_mfma_f32_16x16x32_bf16 v[44:47], v[20:23], a[8:11], v[44:47]\n"
    "v_mfma_f32_16x16x32_bf16 v[40:43], v[24:27], a[12:15], v[40:43]\n"
    "v_mfma_f32_16x16x32_bf16 v[44:47], v[28:31], a[12:15], v[44:47]\n"
    "s_waitcnt lgkmcnt(0)\n"
    "s_mov_b32 s37, 2130706432\n"
    "s_mov_b32 s36, 2130706432\n"
    "s_nop 7\n"
    "s_branch .Lvo_uph_%=\n"
    ".Lvo_e0_%=:\n"
    ".Lvo_po18_%=:\n"
    "buffer_load_dword v109, v108, s[0:3], s24 offen sc1\n"
    "s_waitcnt vmcnt(0)\n"
    "v_cmp_gt_i32 vcc, 1, v109\n"
    "s_nop 0\n"
    "s_cbranch_vccz .Lvo_pd19_%=\n"
    "s_add_u32 s28, s28, 1\n"
    "s_cmp_lt_u32 s28, 0x40000\n"
    "s_cbranch_scc1 .Lvo_sp20_%=\n"
    "v_mov_b32 v109, 30\n"
    "s_mov_b32 s32, 33728\n"
    "buffer_store_dword v109, off, s[0:3], s32 sc1\n"
    "s_waitcnt vmcnt(0)\n"
    "s_branch .Lvo_pd19_%=\n"
    ".Lvo_sp20_%=:\n"
    "s_sleep 4\n"
    "s_branch .Lvo_po18_%=\n"
    ".Lvo_pd19_%=:\n"
    ".Lvo_po21_%=:\n"
    "buffer_load_dword v110, v105, s[0:3], 0 offen sc1\n"
    "s_waitcnt vmcnt(0)\n"
    "v_cmp_gt_i32 vcc, v111, v110\n"
    "s_nop 0\n"
    "s_cbranch_vccz .Lvo_pd22_%=\n"
    "s_add_u32 s28, s28, 1\n"
    "s_cmp_lt_u32 s28, 0x40000\n"
    "s_cbranch_scc1 .Lvo_sp23_%=\n"
    "v_mov_b32 v110, 31\n"
    "s_mov_b32 s32, 33728\n"
    "buffer_store_dword v110, off, s[0:3], s32 sc1\n"
    "s_waitcnt vmcnt(0)\n"
    "s_branch .Lvo_pd22_%=\n"
    ".Lvo_sp23_%=:\n"
    "s_sleep 4\n"
    "s_branch .Lvo_po21_%=\n"
    ".Lvo_pd22_%=:\n"
    "s_branch .Lvo_e1_%=\n"
    ".Lvo_again_%=:\n"
    "s_add_u32 s28, s28, 1\n"
    "s_cmp_lt_u32 s28, 0x40000\n"
    "s_cbranch_scc1 .Lvo_sp24_%=\n"
    "v_mov_b32 v108, 2\n"
    "s_mov_b32 s32, 33728\n"
    "buffer_store_dword v108, off, s[0:3], s32 sc1\n"
    "s_waitcnt vmcnt(0)\n"
    "s_branch .Lvo_go_%=\n"
    ".Lvo_sp24_%=:\n"
    "buffer_load_dwordx4 v96, s[0:3], s41 offen sc1 lds\n"
    "buffer_load_dwordx4 v96, s[0:3], s41 offen offset:1024 sc1 lds\n"
    "buffer_load_dwordx4 v96, s[0:3], s42 offen offset:2048 sc1 lds\n"
    ".Lvo_ag1_%=:\n"
    "s_waitcnt vmcnt(0)\n"
    "ds_read_b128 v[72:75], v92 offset:8192\n"
    "ds_read_b128 v[76:79], v92 offset:9216\n"
    "s_waitcnt lgkmcnt(0)\n"
    "v_mov_b64 v[64:65], 0\n"
    "v_mov_b64 v[66:67], 0\n"
    "v_max_u32 v108, v72, v74\n"
    "v_max3_u32 v108, v108, v76, v78\n"
    "v_cmp_le_u32 vcc, s52, v108\n"
    "s_nop 0\n"
    "s_cbranch_vccnz .Lvo_again_%=\n"
    "v_smfmac_f32_16x16x64_bf16 v[64:67], v[72:75], a[240:247], v98\n"
    "s_branch .Lvo_go_%=\n"
    ".Lvo_r2_%=:\n"
    "s_add_u32 s28, s28, 1\n"
    "s_cmp_lt_u32 s28, 0x40000\n"
    "s_cbranch_scc1 .Lvo_sp25_%=\n"
    "v_mov_b32 v108, 6\n"
    "s_mov_b32 s32, 33728\n"
    "buffer_store_dword v108, off, s[0:3], s32 sc1\n"
    "s_waitcnt vmcnt(0)\n"
    "s_branch .Lvo_go2_%=\n"
    ".Lvo_sp25_%=:\n"
    "buffer_load_dwordx4 v96, s[0:3], s42 offen offset:2048 sc1 lds\n"
    ".Lvo_r3_%=:\n"
    "s_waitcnt vmcnt(0)\n"
    "ds_read_b128 v[80:83], v92 offset:10240\n"
    "s_waitcnt lgkmcnt(0)\n"
    "v_max_u32 v108, v80, v82\n"
    "v_cmp_eq_u32 vcc, -1, v108\n"
    "s_nop 0\n"
    "s_cbranch_vccnz .Lvo_r2_%=\n"
    "s_branch .Lvo_go2_%=\n"
    ".Lvo_wf_%=:\n"
    ".Lvo_po26_%=:\n"
    "buffer_load_dword v110, v105, s[0:3], 0 offen sc1\n"
    "s_waitcnt vmcnt(0)\n"
    "v_cmp_gt_i32 vcc, v111, v110\n"
    "s_nop 0\n"
    "s_cbranch_vccz .Lvo_pd27_%=\n"
    "s_add_u32 s28, s28, 1\n"
    "s_cmp_lt_u32 s28, 0x40000\n"
    "s_cbranch_scc1 .Lvo_sp28_%=\n"
    "v_mov_b32 v110, 33\n"
    "s_mov_b32 s32, 33728\n"
    "buffer_store_dword v110, off, s[0:3], s32 sc1\n"
    "s_waitcnt vmcnt(0)\n"
    "s_branch .Lvo_pd27_%=\n"
    ".Lvo_sp28_%=:\n"
    "s_sleep 4\n"
    "s_branch .Lvo_po26_%=\n"
    ".Lvo_pd27_%=:\n"
    "s_branch .Lvo_wfb_%=\n"
    ".Lvo_end_%=:\n"
    "s_waitcnt vmcnt(0) lgkmcnt(0)\n"
    "s_endpgm\n"
    :: "{s[0:3]}"(s0_), "{s[4:7]}"(s4_), "{s[8:11]}"(s8_), "{s[12:15]}"(s12_), "{s[16:19]}"(s16_), "{s[20:23]}"(s20_), "{s[24:27]}"(s24_), "{v[0:3]}"(v0_), "{v[4:7]}"(v4_), "{v[8:11]}"(v8_), "{v[12:15]}"(v12_), "{v[16:19]}"(v16_), "{v[92:95]}"(v92_), "{v[96:99]}"(v96_), "{v[100:103]}"(v100_), "{v[104:107]}"(v104_) : "memory", "v255", "a255", "s95");
    __builtin_unreachable();
}
DEV void ct_role(const MegaArgs& A, const int L, const int u, char* smem) {
    ROLE_HEAD;
    const int r5 = l / 5, q5 = l - 5 * r5, c0 = q5 < 4 ? 16 * u + 4 * q5 : 512 + 4 * u, tk = 8 * w + r5;
    u32x2 m0;
    {
        const int sg = 2 * u + L, rt = w & 1, j0 = 10 * (w >> 1);
        float* ex = (float*)(smem + 1024);
        u32x4 xa[10];
        const unsigned xo = ((32 * sg + 16 * rt + l16) * D_ + 32 * j0 + 8 * lq) * 2;
#pragma unroll
        for (int j = 0; j < 10; ++j) asm volatile("global_load_dwordx4 %0, %1, %2 offset:%3" : "=&v"(xa[j]) : "v"(xo), "s"(A.x0), "n"(64 * j) : "memory");
        asm volatile("global_load_dwordx2 %0, %1, %2" : "=&v"(m0) : "v"(((r5 & 7) * D_ + c0) * 2), "s"(A.mem0) : "memory");
        asm volatile("s_waitcnt vmcnt(0)" ::: "memory");
#pragma unroll
        for (int j = 0; j < 10; ++j) asm volatile("" : "+v"(xa[j]));
        asm volatile("" : "+v"(m0));
        float s = 0.f, q = 0.f;
#pragma unroll
        for (int j = 0; j < 10; ++j)
#pragma unroll
            for (int e = 0; e < 4; ++e) s += lo16(xa[j][e]) + hi16(xa[j][e]);
        s += __shfl_xor(s, 16); s += __shfl_xor(s, 32);
        ex[threadIdx.x] = s;
        asm volatile("s_waitcnt lgkmcnt(0)\n\ts_barrier" ::: "memory");
        const float mean = (ex[64 * rt + l16] + ex[128 + 64 * rt + l16]) * (1.0f / D_);
#pragma unroll
        for (int j = 0; j < 10; ++j)
#pragma unroll
            for (int e = 0; e < 4; ++e) { const float d = lo16(xa[j][e]) - mean, d2 = hi16(xa[j][e]) - mean; q += d * d; q += d2 * d2; }
        q += __shfl_xor(q, 16); q += __shfl_xor(q, 32);
        ex[256 + threadIdx.x] = q;
        asm volatile("s_waitcnt lgkmcnt(0)\n\ts_barrier" ::: "memory");
        q = ex[256 + 64 * rt + l16] + ex[384 + 64 * rt + l16];
        const float mb = rb(mean), rd = __builtin_amdgcn_rcpf(rb(sqrtf(rb(rb(q * (1.0f / D_)) + 1e-5f))));
#pragma unroll
        for (int j = 0; j < 10; ++j) {
            u32x4 o;
#pragma unroll
            for (int e = 0; e < 4; ++e) { const unsigned d = pk2(lo16(xa[j][e]) - mb, hi16(xa[j][e]) - mb); o[sw2(e)] = pk2(lo16(d) * rd, hi16(d) * rd); }
            bst16(rr, rec(sg, 0) + ((j0 + j) >> 2) * 16384 + ((j0 + j) & 3) * 2048 + rt * 1024 + l * 16, o);
        }
    }
#define XM 5
    const unsigned mpos = l < 40 && q5 < XM ? xl(RG_MEM + w * 16384 + (c0 >> 6) * 1024 + (((c0 >> 4) & 3) * 16 + 4 * (r5 >> 1) + ((c0 >> 1) & 2) + (r5 & 1)) * 16 + (c0 & 8), L) : OOB;
    const unsigned xpos = l < 40 ? (c0 >> 7) * 16384 + ((c0 >> 5) & 3) * 2048 + (tk >> 4) * 1024 + (((c0 >> 3) & 3) * 16 + (tk & 15)) * 16 + (c0 & 4) * 2 : OOB;
    const unsigned rpos = (L || l >= 40) ? xpos + RC_X2 : tk * 1280 + c0 * 2;
    const u32x4 rs = L ? rx : u32x4{lo(A.x0), hi(A.x0), 2048u * 1280u, 0x20000u};
#pragma unroll
    for (int k = 0; k < 4; ++k) bst16(rr, rec(2 * u + (k >> 1), L) + RC_X1 + 65536 + w * 2048 + (k & 1) * 1024 + l * 16, ones);
    bst8(rr, mpos, u32x2{nosent(m0[0]), m0[1]}); bst8(rr, mpos + SLOT, ones2); bst8(rr, mpos + 2 * SLOT, ones2);
    wait_drawn(A.ar);
    DRAIN_BARRIER();
    bst4(rr, threadIdx.x < 16 ? xl(Z_CT + (l & 7) * 256 + (32 * L + u) * 4, l >> 3) : OOB, 1u);
    if (!((A.layers >> L) & 1)) return;
    const u32x4 s0_ = {RFL(rx[0]), RFL(rx[1]), RFL(rx[2]), RFL(rx[3])};
    const u32x4 s4_ = {RFL(rs[0]), RFL(rs[1]), RFL(rs[2]), RFL(rs[3])};
    const u32x4 s8_ = {RFL(L << 13), RFL(REC0), RFL(xl(RG_UT + (u * 4 + w) * 8192, L)), RFL(xl(RG_UTX + (u * 4 + w) * 2048, L))};
    const u32x4 s12_ = {RFL(xl(RG_UM + (u * 4 + w) * 2048, L)), RFL(xl(RG_UMX + (u * 4 + w) * 512, L)), RFL(xl(RG_PT + w * 16384, L)), RFL(xl(RG_PM + w * 8192, L))};
    const u32x4 s16_ = {RFL(xl(Z_X + 16384 + (u & 1) * 8192, L)), RFL(xl(Z_QV + (u & 3) * 256, L)), RFL(DBG0 + ((L * 128 + 64 + u) * 4 + w) * 2048), RFL(DBG0 + (2 << 20) + ((L * 32 + u) * 4 + w) * 8192)};
    const u32x4 v192_ = {(unsigned)(l * 16), (unsigned)(l * 16 + 4096), (unsigned)((l & 2) ? 0xeeeeu : 0x4444u), (unsigned)(0x4444u)};
    const u32x4 v196_ = {(unsigned)((l16 >> 2) * 256 + lq * 64 + (l & 3) * 16), (unsigned)(lq * 64 + (l & 3) * 16), (unsigned)((lq >> 1) * 2048 + 1024 + (lq & 1) * 256 + l16 * 16), (unsigned)(lq * 2048 + 1536 + l16 * 16)};
    const u32x4 v200_ = {(unsigned)(0 + w * 640 + lq * 160 + l16 * 4), (unsigned)(l16 < 4 ? 0 + w * 640 + lq * 160 + 64 + l16 * 4 : 12800), (unsigned)(0 + (l < 40 ? l : 0) * 16), (unsigned)(mpos)};
    const u32x4 v204_ = {(unsigned)(2560 + w * 2560 + lq * 320 + l16 * 4), (unsigned)(l16 < 4 ? 2560 + w * 2560 + lq * 320 + 64 + l16 * 4 : 12800), (unsigned)(3840 + w * 2560 + lq * 320 + l16 * 4), (unsigned)(l16 < 4 ? 3840 + w * 2560 + lq * 320 + 64 + l16 * 4 : 12800)};
    const u32x4 v208_ = {(unsigned)(2560 + w * 640 + (l < 40 ? l : 0) * 16), (unsigned)(xpos + RC_X1), (unsigned)(rpos), (unsigned)(l < 4 && w == 1 ? xl(Z_X1 + l * 4096 + u * 128, L) : OOB)};
    const u32x4 v212_ = {(unsigned)(l < 16 ? ((u >> 2) & 1) * 16384 + (u & 3) * 2048 + l * 128 : OOB), (unsigned)(0u), (unsigned)(0u), (unsigned)(0u)};
    asm volatile(
    "s_mov_b32 s20, 0\n"
    "s_mov_b64 s[22:23], 0\n"
    "s_mov_b32 s25, 0\n"
    "s_mov_b32 s28, -2\n"
    "v_lshrrev_b32 v4, 2, v192\n"
    "v_lshrrev_b32 v5, 9, v192\n"
    "v_add_u32 v5, 1, v5\n"
    ".Lct_po1_%=:\n"
    "buffer_load_dword v8, v4, s[0:3], s17 offen sc1\n"
    "s_waitcnt vmcnt(0)\n"
    "v_cmp_gt_i32 vcc, v5, v8\n"
    "s_nop 0\n"
    "s_cbranch_vccz .Lct_pd2_%=\n"
    "s_add_u32 s20, s20, 1\n"
    "s_cmp_lt_u32 s20, 0x40000\n"
    "s_cbranch_scc1 .Lct_sp3_%=\n"
    "v_mov_b32 v8, 40\n"
    "s_mov_b32 s24, 33728\n"
    "buffer_store_dword v8, off, s[0:3], s24 sc1\n"
    "s_waitcnt vmcnt(0)\n"
    "s_branch .Lct_pd2_%=\n"
    ".Lct_sp3_%=:\n"
    "s_sleep 16\n"
    "s_branch .Lct_po1_%=\n"
    ".Lct_pd2_%=:\n"
    "s_memrealtime s[22:23]\n"
    "s_waitcnt lgkmcnt(0)\n"
    "s_sub_u32 s26, s22, 768\n"
    "s_mov_b32 s28, -1\n"
    "s_add_u32 s28, s28, 1\n"
    "s_add_u32 s27, s28, 1\n"
    "s_add_u32 s29, s28, 0\n"
    "s_and_b32 s29, s29, 3\n"
    "s_lshl_b32 s29, s29, 22\n"
    "s_add_u32 s30, s28, 1\n"
    "s_and_b32 s30, s30, 3\n"
    "s_lshl_b32 s30, s30, 22\n"
    "s_add_u32 s31, s28, 3\n"
    "s_and_b32 s31, s31, 3\n"
    "s_lshl_b32 s31, s31, 22\n"
    "s_and_b32 s41, s28, 63\n"
    "s_lshl_b32 s41, s41, 7\n"
    "s_add_u32 s32, s12, s29\n"
    "s_add_u32 s33, s13, s29\n"
    "s_add_u32 s34, s15, s29\n"
    "s_add_u32 s35, s14, s29\n"
    "s_add_u32 s39, s10, s30\n"
    "s_add_u32 s40, s11, s30\n"
    "s_add_u32 s41, s16, s41\n"
    "s_add_u32 s36, s35, 4096\n"
    "s_add_u32 s37, s35, 16384\n"
    "s_add_u32 s38, s35, 20480\n"
    "s_add_u32 s43, s26, 300\n"
    "s_lshl_b32 s45, s28, 19\n"
    "s_lshl_b32 s25, s28, 11\n"
    "s_xor_b32 s25, s25, s8\n"
    "s_and_b32 s25, s25, 0x2000\n"
    "s_add_u32 s45, s45, s25\n"
    "s_add_u32 s45, s45, s9\n"
    "s_mul_i32 s46, s28, 40960\n"
    "s_cmp_eq_u32 s8, 0\n"
    "s_cselect_b32 s46, s46, s45\n"
    "s_cmp_eq_u32 s28, 63\n"
    "s_cselect_b32 s39, 2130706432, s39\n"
    "s_cselect_b32 s40, 2130706432, s40\n"
    "v_mov_b64 v[76:77], 0\n"
    "v_mov_b64 v[78:79], 0\n"
    "v_mov_b64 v[80:81], 0\n"
    "v_mov_b64 v[82:83], 0\n"
    "s_mov_b32 s39, s10\n"
    "s_mov_b32 s40, s11\n"
    "buffer_load_dwordx4 a[0:3], v192, s[0:3], s39 offen offset:0 sc1\n"
    "buffer_load_dwordx4 a[4:7], v192, s[0:3], s39 offen offset:1024 sc1\n"
    "buffer_load_dwordx4 a[8:11], v192, s[0:3], s39 offen offset:2048 sc1\n"
    "buffer_load_dwordx4 a[12:15], v192, s[0:3], s39 offen offset:3072 sc1\n"
    "buffer_load_dwordx4 a[16:19], v193, s[0:3], s39 offen offset:0 sc1\n"
    "buffer_load_dwordx4 a[20:23], v193, s[0:3], s39 offen offset:1024 sc1\n"
    "buffer_load_dwordx4 a[24:27], v193, s[0:3], s39 offen offset:2048 sc1\n"
    "buffer_load_dwordx4 a[28:31], v193, s[0:3], s39 offen offset:3072 sc1\n"
    "buffer_load_dwordx4 v[0:3], v196, s[0:3], s40 offen offset:0 sc1\n"
    "buffer_load_dwordx4 v[4:7], v196, s[0:3], s40 offen offset:1024 sc1\n"
    "s_add_u32 s39, s10, s30\n"
    "s_add_u32 s40, s11, s30\n"
    "buffer_load_dwordx2 v[190:191], off, s[0:3], s41 sc1\n"
    "s_waitcnt vmcnt(0)\n"
    ".Lct_top_%=:\n"
    "s_waitcnt vmcnt(2)\n"
    "v_mov_b64 v[8:9], v[0:1]\n"
    "v_mov_b64 v[10:11], v[2:3]\n"
    "v_mov_b64 v[12:13], v[4:5]\n"
    "v_mov_b64 v[14:15], v[6:7]\n"
    "v_mov_b32_dpp v16, v0 row_shl:4 row_mask:0xf bank_mask:0xf bound_ctrl:1\n"
    "v_mov_b32_dpp v17, v1 row_shl:4 row_mask:0xf bank_mask:0xf bound_ctrl:1\n"
    "v_mov_b32_dpp v18, v2 row_shl:4 row_mask:0xf bank_mask:0xf bound_ctrl:1\n"
    "v_mov_b32_dpp v19, v3 row_shl:4 row_mask:0xf bank_mask:0xf bound_ctrl:1\n"
    "v_mov_b32_dpp v20, v4 row_shl:4 row_mask:0xf bank_mask:0xf bound_ctrl:1\n"
    "v_mov_b32_dpp v21, v5 row_shl:4 row_mask:0xf bank_mask:0xf bound_ctrl:1\n"
    "v_mov_b32_dpp v22, v6 row_shl:4 row_mask:0xf bank_mask:0xf bound_ctrl:1\n"
    "v_mov_b32_dpp v23, v7 row_shl:4 row_mask:0xf bank_mask:0xf bound_ctrl:1\n"
    "v_mov_b32_dpp v24, v0 row_shl:8 row_mask:0xf bank_mask:0xf bound_ctrl:1\n"
    "v_mov_b32_dpp v25, v1 row_shl:8 row_mask:0xf bank_mask:0xf bound_ctrl:1\n"
    "v_mov_b32_dpp v26, v2 row_shl:8 row_mask:0xf bank_mask:0xf bound_ctrl:1\n"
    "v_mov_b32_dpp v27, v3 row_shl:8 row_mask:0xf bank_mask:0xf bound_ctrl:1\n"
    "v_mov_b32_dpp v28, v4 row_shl:8 row_mask:0xf bank_mask:0xf bound_ctrl:1\n"
    "v_mov_b32_dpp v29, v5 row_shl:8 row_mask:0xf bank_mask:0xf bound_ctrl:1\n"
    "v_mov_b32_dpp v30, v6 row_shl:8 row_mask:0xf bank_mask:0xf bound_ctrl:1\n"
    "v_mov_b32_dpp v31, v7 row_shl:8 row_mask:0xf bank_mask:0xf bound_ctrl:1\n"
    "v_mov_b32_dpp v32, v0 row_shl:12 row_mask:0xf bank_mask:0xf bound_ctrl:1\n"
    "v_mov_b32_dpp v33, v1 row_shl:12 row_mask:0xf bank_mask:0xf bound_ctrl:1\n"
    "v_mov_b32_dpp v34, v2 row_shl:12 row_mask:0xf bank_mask:0xf bound_ctrl:1\n"
    "v_mov_b32_dpp v35, v3 row_shl:12 row_mask:0xf bank_mask:0xf bound_ctrl:1\n"
    "v_mov_b32_dpp v36, v4 row_shl:12 row_mask:0xf bank_mask:0xf bound_ctrl:1\n"
    "v_mov_b32_dpp v37, v5 row_shl:12 row_mask:0xf bank_mask:0xf bound_ctrl:1\n"
    "v_mov_b32_dpp v38, v6 row_shl:12 row_mask:0xf bank_mask:0xf bound_ctrl:1\n"
    "v_mov_b32_dpp v39, v7 row_shl:12 row_mask:0xf bank_mask:0xf bound_ctrl:1\n"
    "s_waitcnt vmcnt(1)\n"
    ".Lct_x0_%=:\n"
    "v_readfirstlane_b32 s25, v191\n"
    "v_readfirstlane_b32 s24, v190\n"
    "s_cmp_eq_u32 s25, s27\n"
    "s_cselect_b32 s26, s24, s26\n"
    "s_cbranch_scc0 .Lct_xr0_%=\n"
    ".Lct_xg0_%=:\n"
    "s_add_u32 s43, s26, 85\n"
    "s_add_u32 s44, s26, 93\n"
    ".Lct_mk0x3_%=:\n"
    ".Lct_rt5_%=:\n"
    "s_memrealtime s[22:23]\n"
    "s_waitcnt lgkmcnt(0)\n"
    "s_sub_u32 s24, s43, s22\n"
    "s_cmp_lt_u32 s24, 600\n"
    "s_cbranch_scc1 .Lct_rt5_%=\n"
    "buffer_load_dwordx4 v[40:43], v192, s[0:3], s32 offen offset:0 sc1\n"
    "buffer_load_dwordx4 v[44:47], v192, s[0:3], s32 offen offset:1024 sc1\n"
    "buffer_load_dwordx4 v[48:51], v197, s[0:3], s33 offen offset:0 sc1\n"
    "buffer_load_dwordx4 v[52:55], v197, s[0:3], s33 offen offset:256 sc1\n"
    ".Lct_rt6_%=:\n"
    "s_memrealtime s[22:23]\n"
    "s_waitcnt lgkmcnt(0)\n"
    "s_sub_u32 s24, s44, s22\n"
    "s_cmp_lt_u32 s24, 600\n"
    "s_cbranch_scc1 .Lct_rt6_%=\n"
    "buffer_load_dwordx4 v[72:75], v193, s[0:3], s34 offen sc1\n"
    "buffer_load_dwordx4 v[56:59], v192, s[0:3], s34 offen offset:0 sc1\n"
    "buffer_load_dwordx4 v[60:63], v192, s[0:3], s34 offen offset:1024 sc1\n"
    "buffer_load_dwordx4 v[64:67], v192, s[0:3], s34 offen offset:2048 sc1\n"
    "buffer_load_dwordx4 v[68:71], v192, s[0:3], s34 offen offset:3072 sc1\n"
    "v_mov_b32 v148, s28\n"
    "v_mov_b32 v149, s28\n"
    "v_mov_b32 v150, s28\n"
    "v_mov_b32 v151, s28\n"
    "s_waitcnt vmcnt(9)\n"
    "s_barrier\n"
    "buffer_store_dwordx4 v[148:151], v211, s[0:3], 0 offen sc1\n"
    ".Lct_mk1x6_%=:\n"
    ".Lct_take0_%=:\n"
    "s_waitcnt vmcnt(6)\n"
    "v_max_u32 v184, v40, v44\n"
    "v_max3_u32 v184, v48, v50, v184\n"
    "v_max3_u32 v184, v52, v54, v184\n"
    "s_waitcnt vmcnt(5)\n"
    "v_smfmac_f32_16x16x64_bf16 v[76:79], v[72:75], v[40:47], v194\n"
    "v_max3_u32 v184, v72, v74, v184\n"
    "v_smfmac_f32_16x16x64_bf16 v[80:83], v[72:75], v[48:55], v194\n"
    "s_waitcnt vmcnt(4)\n"
    "v_smfmac_f32_16x16x64_bf16 v[80:83], v[56:59], v[8:15], v194\n"
    "v_max3_u32 v184, v56, v58, v184\n"
    "v_smfmac_f32_16x16x64_bf16 v[76:79], v[56:59], a[0:7], v194\n"
    "s_waitcnt vmcnt(3)\n"
    "v_smfmac_f32_16x16x64_bf16 v[80:83], v[60:63], v[16:23], v194\n"
    "v_max3_u32 v184, v60, v62, v184\n"
    "v_smfmac_f32_16x16x64_bf16 v[76:79], v[60:63], a[8:15], v194\n"
    "s_waitcnt vmcnt(2)\n"
    "v_smfmac_f32_16x16x64_bf16 v[80:83], v[64:67], v[24:31], v194\n"
    "v_max3_u32 v184, v64, v66, v184\n"
    "v_smfmac_f32_16x16x64_bf16 v[76:79], v[64:67], a[16:23], v194\n"
    "s_waitcnt vmcnt(1)\n"
    "v_smfmac_f32_16x16x64_bf16 v[80:83], v[68:71], v[32:39], v194\n"
    "v_max3_u32 v184, v68, v70, v184\n"
    "v_smfmac_f32_16x16x64_bf16 v[76:79], v[68:71], a[24:31], v194\n"
    ".Lct_mk2x7_%=:\n"
    "s_nop 1\n"
    "v_cmp_eq_u32 vcc, -1, v184\n"
    "s_nop 1\n"
    "v_add_f32 v80, v80, v82\n"
    "v_add_f32 v81, v81, v83\n"
    "s_nop 0\n"
    "v_pk_add_f32 v[76:77], v[76:77], v[78:79]\n"
    "ds_write2_b32 v201, v80, v81 offset1:20\n"
    "ds_write2_b32 v200, v76, v77 offset1:20\n"
    "s_cbranch_vccnz .Lct_retry0_%=\n"
    ".Lct_go0_%=:\n"
    "s_waitcnt lgkmcnt(0)\n"
    "s_barrier\n"
    "ds_read_b128 v[164:167], v202 offset:0\n"
    "ds_read_b128 v[168:171], v202 offset:640\n"
    "ds_read_b128 v[172:175], v202 offset:1280\n"
    "ds_read_b128 v[176:179], v202 offset:1920\n"
    "s_waitcnt lgkmcnt(2)\n"
    "v_pk_add_f32 v[164:165], v[164:165], v[168:169]\n"
    "v_pk_add_f32 v[166:167], v[166:167], v[170:171]\n"
    "s_waitcnt lgkmcnt(1)\n"
    "v_pk_add_f32 v[164:165], v[164:165], v[172:173]\n"
    "v_pk_add_f32 v[166:167], v[166:167], v[174:175]\n"
    "s_waitcnt lgkmcnt(0)\n"
    "v_pk_add_f32 v[164:165], v[164:165], v[176:177]\n"
    "v_pk_add_f32 v[166:167], v[166:167], v[178:179]\n"
    "v_cvt_pk_bf16_f32 v164, v164, v165\n"
    "v_cvt_pk_bf16_f32 v165, v166, v167\n"
    "v_min_u32 v164, -2, v164\n"
    "buffer_store_dwordx2 v[164:165], v203, s[0:3], s30 offen sc1\n"
    ".Lct_mk3x8_%=:\n"
    "s_sleep 2\n"
    "v_mov_b64 v[166:167], -1\n"
    "buffer_load_dwordx4 v[132:135], v199, s[0:3], s35 offen offset:0 sc1\n"
    "buffer_load_dwordx4 v[136:139], v199, s[0:3], s37 offen offset:0 sc1\n"
    "buffer_load_dwordx4 v[140:143], v199, s[0:3], s35 offen offset:256 sc1\n"
    "buffer_load_dwordx4 v[144:147], v199, s[0:3], s37 offen offset:256 sc1\n"
    "buffer_load_dwordx4 v[116:119], v198, s[0:3], s35 offen sc1\n"
    "buffer_load_dwordx4 v[84:87], v192, s[0:3], s35 offen offset:0 sc1\n"
    "buffer_load_dwordx4 v[88:91], v192, s[0:3], s35 offen offset:2048 sc1\n"
    "buffer_load_dwordx4 v[120:123], v198, s[0:3], s36 offen sc1\n"
    "buffer_load_dwordx4 v[92:95], v192, s[0:3], s36 offen offset:0 sc1\n"
    "buffer_load_dwordx4 v[96:99], v192, s[0:3], s36 offen offset:2048 sc1\n"
    ".Lct_mk7x9_%=:\n"
    "buffer_load_dwordx4 v[124:127], v198, s[0:3], s37 offen sc1\n"
    "s_waitcnt vmcnt(10)\n"
    "v_max3_u32 v184, v132, v134, v132\n"
    "v_swap_b32 v133, v134\n"
    "s_nop 1\n"
    "v_mfma_f32_16x16x32_bf16 v[148:151], v[132:135], v[40:43], 0\n"
    "v_mfma_f32_16x16x32_bf16 v[152:155], v[132:135], v[48:51], 0\n"
    "buffer_load_dwordx4 v[100:103], v192, s[0:3], s37 offen offset:0 sc1\n"
    "s_waitcnt vmcnt(10)\n"
    "v_max3_u32 v184, v136, v138, v184\n"
    "v_swap_b32 v137, v138\n"
    "s_nop 1\n"
    "v_mfma_f32_16x16x32_bf16 v[148:151], v[136:139], v[44:47], v[148:151]\n"
    "v_mfma_f32_16x16x32_bf16 v[152:155], v[136:139], v[52:55], v[152:155]\n"
    "buffer_load_dwordx4 v[104:107], v192, s[0:3], s37 offen offset:2048 sc1\n"
    "s_waitcnt vmcnt(10)\n"
    "v_max3_u32 v184, v140, v142, v184\n"
    "v_swap_b32 v141, v142\n"
    "s_nop 1\n"
    "v_mfma_f32_16x16x32_bf16 v[156:159], v[140:143], v[40:43], 0\n"
    "v_mfma_f32_16x16x32_bf16 v[160:163], v[140:143], v[48:51], 0\n"
    "buffer_load_dwordx4 v[128:131], v198, s[0:3], s38 offen sc1\n"
    "s_waitcnt vmcnt(10)\n"
    "v_max3_u32 v184, v144, v146, v184\n"
    "v_swap_b32 v145, v146\n"
    "s_nop 1\n"
    "v_mfma_f32_16x16x32_bf16 v[156:159], v[144:147], v[44:47], v[156:159]\n"
    "v_mfma_f32_16x16x32_bf16 v[160:163], v[144:147], v[52:55], v[160:163]\n"
    "buffer_load_dwordx4 v[108:111], v192, s[0:3], s38 offen offset:0 sc1\n"
    "s_waitcnt vmcnt(10)\n"
    "v_max3_u32 v184, v116, v118, v184\n"
    "v_smfmac_f32_16x16x64_bf16 v[148:151], v[116:119], a[0:7], v195\n"
    "v_smfmac_f32_16x16x64_bf16 v[152:155], v[116:119], v[8:15], v195\n"
    "buffer_load_dwordx4 v[112:115], v192, s[0:3], s38 offen offset:2048 sc1\n"
    "s_waitcnt vmcnt(10)\n"
    "v_max3_u32 v184, v84, v86, v184\n"
    "v_swap_b32 v85, v86\n"
    "s_nop 1\n"
    "v_mfma_f32_16x16x32_bf16 v[156:159], v[84:87], a[0:3], v[156:159]\n"
    "v_mfma_f32_16x16x32_bf16 v[160:163], v[84:87], v[8:11], v[160:163]\n"
    "buffer_load_dwordx2 v[186:187], v210, s[4:7], s46 offen sc1\n"
    "s_waitcnt vmcnt(10)\n"
    "v_max3_u32 v184, v88, v90, v184\n"
    "v_swap_b32 v89, v90\n"
    "s_nop 1\n"
    "v_mfma_f32_16x16x32_bf16 v[156:159], v[88:91], a[4:7], v[156:159]\n"
    "v_mfma_f32_16x16x32_bf16 v[160:163], v[88:91], v[12:15], v[160:163]\n"
    "buffer_store_dwordx2 v[166:167], v203, s[0:3], s31 offen sc1\n"
    "s_waitcnt vmcnt(10)\n"
    "v_max3_u32 v184, v120, v122, v184\n"
    "v_smfmac_f32_16x16x64_bf16 v[148:151], v[120:123], a[8:15], v195\n"
    "v_smfmac_f32_16x16x64_bf16 v[152:155], v[120:123], v[16:23], v195\n"
    "buffer_load_dwordx4 a[32:35], v192, s[0:3], s39 offen offset:0 sc1\n"
    "s_waitcnt vmcnt(10)\n"
    "v_max3_u32 v184, v92, v94, v184\n"
    "v_swap_b32 v93, v94\n"
    "s_nop 1\n"
    "v_mfma_f32_16x16x32_bf16 v[156:159], v[92:95], a[8:11], v[156:159]\n"
    "v_mfma_f32_16x16x32_bf16 v[160:163], v[92:95], v[16:19], v[160:163]\n"
    "buffer_load_dwordx4 a[36:39], v192, s[0:3], s39 offen offset:1024 sc1\n"
    "s_waitcnt vmcnt(10)\n"
    "v_max3_u32 v184, v96, v98, v184\n"
    "v_swap_b32 v97, v98\n"
    "s_nop 1\n"
    "v_mfma_f32_16x16x32_bf16 v[156:159], v[96:99], a[12:15], v[156:159]\n"
    "v_mfma_f32_16x16x32_bf16 v[160:163], v[96:99], v[20:23], v[160:163]\n"
    "buffer_load_dwordx4 a[40:43], v192, s[0:3], s39 offen offset:2048 sc1\n"
    "s_waitcnt vmcnt(10)\n"
    "v_max3_u32 v184, v124, v126, v184\n"
    "v_smfmac_f32_16x16x64_bf16 v[148:151], v[124:127], a[16:23], v195\n"
    "v_smfmac_f32_16x16x64_bf16 v[152:155], v[124:127], v[24:31], v195\n"
    "buffer_load_dwordx4 a[44:47], v192, s[0:3], s39 offen offset:3072 sc1\n"
    "s_waitcnt vmcnt(10)\n"
    "v_max3_u32 v184, v100, v102, v184\n"
    "v_swap_b32 v101, v102\n"
    "s_nop 1\n"
    "v_mfma_f32_16x16x32_bf16 v[156:159], v[100:103], a[16:19], v[156:159]\n"
    "v_mfma_f32_16x16x32_bf16 v[160:163], v[100:103], v[24:27], v[160:163]\n"
    "buffer_load_dwordx4 a[48:51], v193, s[0:3], s39 offen offset:0 sc1\n"
    "s_waitcnt vmcnt(10)\n"
    "v_max3_u32 v184, v104, v106, v184\n"
    "v_swap_b32 v105, v106\n"
    "s_nop 1\n"
    "v_mfma_f32_16x16x32_bf16 v[156:159], v[104:107], a[20:23], v[156:159]\n"
    "v_mfma_f32_16x16x32_bf16 v[160:163], v[104:107], v[28:31], v[160:163]\n"
    "buffer_load_dwordx4 a[52:55], v193, s[0:3], s39 offen offset:1024 sc1\n"
    "s_waitcnt vmcnt(10)\n"
    "v_max3_u32 v184, v128, v130, v184\n"
    "v_smfmac_f32_16x16x64_bf16 v[148:151], v[128:131], a[24:31], v195\n"
    "v_smfmac_f32_16x16x64_bf16 v[152:155], v[128:131], v[32:39], v195\n"
    "buffer_load_dwordx4 a[56:59], v193, s[0:3], s39 offen offset:2048 sc1\n"
    "s_waitcnt vmcnt(10)\n"
    "v_max3_u32 v184, v108, v110, v184\n"
    "v_swap_b32 v109, v110\n"
    "s_nop 1\n"
    "v_mfma_f32_16x16x32_bf16 v[156:159], v[108:111], a[24:27], v[156:159]\n"
    "v_mfma_f32_16x16x32_bf16 v[160:163], v[108:111], v[32:35], v[160:163]\n"
    "buffer_load_dwordx4 a[60:63], v193, s[0:3], s39 offen offset:3072 sc1\n"
    "s_waitcnt vmcnt(10)\n"
    "v_max3_u32 v184, v112, v114, v184\n"
    "v_swap_b32 v113, v114\n"
    "s_nop 1\n"
    "v_mfma_f32_16x16x32_bf16 v[156:159], v[112:115], a[28:31], v[156:159]\n"
    "v_mfma_f32_16x16x32_bf16 v[160:163], v[112:115], v[36:39], v[160:163]\n"
    "v_cmp_eq_u32 vcc, -1, v184\n"
    "s_nop 0\n"
    "s_cbranch_vccnz .Lct_tr0_%=\n"
    ".Lct_tg0_%=:\n"
    ".Lct_mk4x10_%=:\n"
    "ds_write2_b32 v204, v148, v149 offset0:0 offset1:20\n"
    "ds_write2_b32 v204, v150, v151 offset0:40 offset1:60\n"
    "ds_write2_b32 v205, v152, v153 offset0:0 offset1:20\n"
    "ds_write2_b32 v205, v154, v155 offset0:40 offset1:60\n"
    "ds_write2_b32 v206, v156, v157 offset0:0 offset1:20\n"
    "ds_write2_b32 v206, v158, v159 offset0:40 offset1:60\n"
    "ds_write2_b32 v207, v160, v161 offset0:0 offset1:20\n"
    "ds_write2_b32 v207, v162, v163 offset0:40 offset1:60\n"
    "s_mov_b32 s42, s45\n"
    "buffer_load_dwordx4 v[0:3], v196, s[0:3], s40 offen offset:0 sc1\n"
    "buffer_load_dwordx4 v[4:7], v196, s[0:3], s40 offen offset:1024 sc1\n"
    "s_add_u32 s28, s28, 1\n"
    "s_add_u32 s27, s28, 1\n"
    "s_add_u32 s29, s28, 0\n"
    "s_and_b32 s29, s29, 3\n"
    "s_lshl_b32 s29, s29, 22\n"
    "s_add_u32 s30, s28, 1\n"
    "s_and_b32 s30, s30, 3\n"
    "s_lshl_b32 s30, s30, 22\n"
    "s_add_u32 s31, s28, 3\n"
    "s_and_b32 s31, s31, 3\n"
    "s_lshl_b32 s31, s31, 22\n"
    "s_and_b32 s41, s28, 63\n"
    "s_lshl_b32 s41, s41, 7\n"
    "s_add_u32 s32, s12, s29\n"
    "s_add_u32 s33, s13, s29\n"
    "s_add_u32 s34, s15, s29\n"
    "s_add_u32 s35, s14, s29\n"
    "s_add_u32 s39, s10, s30\n"
    "s_add_u32 s40, s11, s30\n"
    "s_add_u32 s41, s16, s41\n"
    "s_add_u32 s36, s35, 4096\n"
    "s_add_u32 s37, s35, 16384\n"
    "s_add_u32 s38, s35, 20480\n"
    "s_add_u32 s43, s26, 300\n"
    "s_lshl_b32 s45, s28, 19\n"
    "s_lshl_b32 s25, s28, 11\n"
    "s_xor_b32 s25, s25, s8\n"
    "s_and_b32 s25, s25, 0x2000\n"
    "s_add_u32 s45, s45, s25\n"
    "s_add_u32 s45, s45, s9\n"
    "s_mul_i32 s46, s28, 40960\n"
    "s_cmp_eq_u32 s8, 0\n"
    "s_cselect_b32 s46, s46, s45\n"
    "s_cmp_eq_u32 s28, 63\n"
    "s_cselect_b32 s39, 2130706432, s39\n"
    "s_cselect_b32 s40, 2130706432, s40\n"
    "v_mov_b64 v[76:77], 0\n"
    "v_mov_b64 v[78:79], 0\n"
    "v_mov_b64 v[80:81], 0\n"
    "v_mov_b64 v[82:83], 0\n"
    "s_waitcnt vmcnt(11)\n"
    "v_lshlrev_b32 v148, 16, v186\n"
    "v_and_b32 v149, 0xffff0000, v186\n"
    "v_lshlrev_b32 v150, 16, v187\n"
    "v_and_b32 v151, 0xffff0000, v187\n"
    "s_memrealtime s[22:23]\n"
    "s_waitcnt lgkmcnt(0)\n"
    "s_barrier\n"
    "s_sub_u32 s24, s43, s22\n"
    "s_cmp_gt_i32 s24, 14\n"
    "s_cselect_b32 s24, 2130706432, s41\n"
    "buffer_load_dwordx2 v[190:191], off, s[0:3], s24 sc1\n"
    ".Lct_mk6x11_%=:\n"
    "ds_read_b128 v[164:167], v208 offset:0\n"
    "ds_read_b128 v[168:171], v208 offset:2560\n"
    "ds_read_b128 v[172:175], v208 offset:5120\n"
    "ds_read_b128 v[176:179], v208 offset:7680\n"
    "s_waitcnt lgkmcnt(2)\n"
    "v_pk_add_f32 v[164:165], v[164:165], v[168:169]\n"
    "v_pk_add_f32 v[166:167], v[166:167], v[170:171]\n"
    "s_waitcnt lgkmcnt(1)\n"
    "v_pk_add_f32 v[164:165], v[164:165], v[172:173]\n"
    "v_pk_add_f32 v[166:167], v[166:167], v[174:175]\n"
    "s_waitcnt lgkmcnt(0)\n"
    "v_pk_add_f32 v[164:165], v[164:165], v[176:177]\n"
    "v_pk_add_f32 v[166:167], v[166:167], v[178:179]\n"
    "v_cvt_pk_bf16_f32 v164, v164, v165\n"
    "v_cvt_pk_bf16_f32 v165, v166, v167\n"
    "v_lshlrev_b32 v168, 16, v164\n"
    "v_and_b32 v169, 0xffff0000, v164\n"
    "v_lshlrev_b32 v170, 16, v165\n"
    "v_and_b32 v171, 0xffff0000, v165\n"
    "v_pk_add_f32 v[168:169], v[168:169], v[148:149]\n"
    "v_pk_add_f32 v[170:171], v[170:171], v[150:151]\n"
    "v_cvt_pk_bf16_f32 v164, v168, v169\n"
    "v_cvt_pk_bf16_f32 v165, v170, v171\n"
    "buffer_store_dwordx2 v[164:165], v209, s[0:3], s42 offen sc1\n"
    ".Lct_mk5x12_%=:\n"
    "s_waitcnt vmcnt(2)\n"
    "v_mov_b64 v[8:9], v[0:1]\n"
    "v_mov_b64 v[10:11], v[2:3]\n"
    "v_mov_b64 v[12:13], v[4:5]\n"
    "v_mov_b64 v[14:15], v[6:7]\n"
    "v_mov_b32_dpp v16, v0 row_shl:4 row_mask:0xf bank_mask:0xf bound_ctrl:1\n"
    "v_mov_b32_dpp v17, v1 row_shl:4 row_mask:0xf bank_mask:0xf bound_ctrl:1\n"
    "v_mov_b32_dpp v18, v2 row_shl:4 row_mask:0xf bank_mask:0xf bound_ctrl:1\n"
    "v_mov_b32_dpp v19, v3 row_shl:4 row_mask:0xf bank_mask:0xf bound_ctrl:1\n"
    "v_mov_b32_dpp v20, v4 row_shl:4 row_mask:0xf bank_mask:0xf bound_ctrl:1\n"
    "v_mov_b32_dpp v21, v5 row_shl:4 row_mask:0xf bank_mask:0xf bound_ctrl:1\n"
    "v_mov_b32_dpp v22, v6 row_shl:4 row_mask:0xf bank_mask:0xf bound_ctrl:1\n"
    "v_mov_b32_dpp v23, v7 row_shl:4 row_mask:0xf bank_mask:0xf bound_ctrl:1\n"
    "v_mov_b32_dpp v24, v0 row_shl:8 row_mask:0xf bank_mask:0xf bound_ctrl:1\n"
    "v_mov_b32_dpp v25, v1 row_shl:8 row_mask:0xf bank_mask:0xf bound_ctrl:1\n"
    "v_mov_b32_dpp v26, v2 row_shl:8 row_mask:0xf bank_mask:0xf bound_ctrl:1\n"
    "v_mov_b32_dpp v27, v3 row_shl:8 row_mask:0xf bank_mask:0xf bound_ctrl:1\n"
    "v_mov_b32_dpp v28, v4 row_shl:8 row_mask:0xf bank_mask:0xf bound_ctrl:1\n"
    "v_mov_b32_dpp v29, v5 row_shl:8 row_mask:0xf bank_mask:0xf bound_ctrl:1\n"
    "v_mov_b32_dpp v30, v6 row_shl:8 row_mask:0xf bank_mask:0xf bound_ctrl:1\n"
    "v_mov_b32_dpp v31, v7 row_shl:8 row_mask:0xf bank_mask:0xf bound_ctrl:1\n"
    "v_mov_b32_dpp v32, v0 row_shl:12 row_mask:0xf bank_mask:0xf bound_ctrl:1\n"
    "v_mov_b32_dpp v33, v1 row_shl:12 row_mask:0xf bank_mask:0xf bound_ctrl:1\n"
    "v_mov_b32_dpp v34, v2 row_shl:12 row_mask:0xf bank_mask:0xf bound_ctrl:1\n"
    "v_mov_b32_dpp v35, v3 row_shl:12 row_mask:0xf bank_mask:0xf bound_ctrl:1\n"
    "v_mov_b32_dpp v36, v4 row_shl:12 row_mask:0xf bank_mask:0xf bound_ctrl:1\n"
    "v_mov_b32_dpp v37, v5 row_shl:12 row_mask:0xf bank_mask:0xf bound_ctrl:1\n"
    "v_mov_b32_dpp v38, v6 row_shl:12 row_mask:0xf bank_mask:0xf bound_ctrl:1\n"
    "v_mov_b32_dpp v39, v7 row_shl:12 row_mask:0xf bank_mask:0xf bound_ctrl:1\n"
    "s_waitcnt vmcnt(1)\n"
    ".Lct_x1_%=:\n"
    "v_readfirstlane_b32 s25, v191\n"
    "v_readfirstlane_b32 s24, v190\n"
    "s_cmp_eq_u32 s25, s27\n"
    "s_cselect_b32 s26, s24, s26\n"
    "s_cbranch_scc0 .Lct_xr1_%=\n"
    ".Lct_xg1_%=:\n"
    "s_add_u32 s43, s26, 85\n"
    "s_add_u32 s44, s26, 93\n"
    ".Lct_mk0x17_%=:\n"
    ".Lct_rt19_%=:\n"
    "s_memrealtime s[22:23]\n"
    "s_waitcnt lgkmcnt(0)\n"
    "s_sub_u32 s24, s43, s22\n"
    "s_cmp_lt_u32 s24, 600\n"
    "s_cbranch_scc1 .Lct_rt19_%=\n"
    "buffer_load_dwordx4 v[40:43], v192, s[0:3], s32 offen offset:0 sc1\n"
    "buffer_load_dwordx4 v[44:47], v192, s[0:3], s32 offen offset:1024 sc1\n"
    "buffer_load_dwordx4 v[48:51], v197, s[0:3], s33 offen offset:0 sc1\n"
    "buffer_load_dwordx4 v[52:55], v197, s[0:3], s33 offen offset:256 sc1\n"
    ".Lct_rt20_%=:\n"
    "s_memrealtime s[22:23]\n"
    "s_waitcnt lgkmcnt(0)\n"
    "s_sub_u32 s24, s44, s22\n"
    "s_cmp_lt_u32 s24, 600\n"
    "s_cbranch_scc1 .Lct_rt20_%=\n"
    "buffer_load_dwordx4 v[72:75], v193, s[0:3], s34 offen sc1\n"
    "buffer_load_dwordx4 v[56:59], v192, s[0:3], s34 offen offset:0 sc1\n"
    "buffer_load_dwordx4 v[60:63], v192, s[0:3], s34 offen offset:1024 sc1\n"
    "buffer_load_dwordx4 v[64:67], v192, s[0:3], s34 offen offset:2048 sc1\n"
    "buffer_load_dwordx4 v[68:71], v192, s[0:3], s34 offen offset:3072 sc1\n"
    "v_mov_b32 v148, s28\n"
    "v_mov_b32 v149, s28\n"
    "v_mov_b32 v150, s28\n"
    "v_mov_b32 v151, s28\n"
    "s_waitcnt vmcnt(9)\n"
    "s_barrier\n"
    "buffer_store_dwordx4 v[148:151], v211, s[0:3], 0 offen sc1\n"
    ".Lct_mk1x20_%=:\n"
    ".Lct_take1_%=:\n"
    "s_waitcnt vmcnt(6)\n"
    "v_max_u32 v184, v40, v44\n"
    "v_max3_u32 v184, v48, v50, v184\n"
    "v_max3_u32 v184, v52, v54, v184\n"
    "s_waitcnt vmcnt(5)\n"
    "v_smfmac_f32_16x16x64_bf16 v[76:79], v[72:75], v[40:47], v194\n"
    "v_max3_u32 v184, v72, v74, v184\n"
    "v_smfmac_f32_16x16x64_bf16 v[80:83], v[72:75], v[48:55], v194\n"
    "s_waitcnt vmcnt(4)\n"
    "v_smfmac_f32_16x16x64_bf16 v[80:83], v[56:59], v[8:15], v194\n"
    "v_max3_u32 v184, v56, v58, v184\n"
    "v_smfmac_f32_16x16x64_bf16 v[76:79], v[56:59], a[32:39], v194\n"
    "s_waitcnt vmcnt(3)\n"
    "v_smfmac_f32_16x16x64_bf16 v[80:83], v[60:63], v[16:23], v194\n"
    "v_max3_u32 v184, v60, v62, v184\n"
    "v_smfmac_f32_16x16x64_bf16 v[76:79], v[60:63], a[40:47], v194\n"
    "s_waitcnt vmcnt(2)\n"
    "v_smfmac_f32_16x16x64_bf16 v[80:83], v[64:67], v[24:31], v194\n"
    "v_max3_u32 v184, v64, v66, v184\n"
    "v_smfmac_f32_16x16x64_bf16 v[76:79], v[64:67], a[48:55], v194\n"
    "s_waitcnt vmcnt(1)\n"
    "v_smfmac_f32_16x16x64_bf16 v[80:83], v[68:71], v[32:39], v194\n"
    "v_max3_u32 v184, v68, v70, v184\n"
    "v_smfmac_f32_16x16x64_bf16 v[76:79], v[68:71], a[56:63], v194\n"
    ".Lct_mk2x21_%=:\n"
    "s_nop 1\n"
    "v_cmp_eq_u32 vcc, -1, v184\n"
    "s_nop 1\n"
    "v_add_f32 v80, v80, v82\n"
    "v_add_f32 v81, v81, v83\n"
    "s_nop 0\n"
    "v_pk_add_f32 v[76:77], v[76:77], v[78:79]\n"
    "ds_write2_b32 v201, v80, v81 offset1:20\n"
    "ds_write2_b32 v200, v76, v77 offset1:20\n"
    "s_cbranch_vccnz .Lct_retry1_%=\n"
    ".Lct_go1_%=:\n"
    "s_waitcnt lgkmcnt(0)\n"
    "s_barrier\n"
    "ds_read_b128 v[164:167], v202 offset:0\n"
    "ds_read_b128 v[168:171], v202 offset:640\n"
    "ds_read_b128 v[172:175], v202 offset:1280\n"
    "ds_read_b128 v[176:179], v202 offset:1920\n"
    "s_waitcnt lgkmcnt(2)\n"
    "v_pk_add_f32 v[164:165], v[164:165], v[168:169]\n"
    "v_pk_add_f32 v[166:167], v[166:167], v[170:171]\n"
    "s_waitcnt lgkmcnt(1)\n"
    "v_pk_add_f32 v[164:165], v[164:165], v[172:173]\n"
    "v_pk_add_f32 v[166:167], v[166:167], v[174:175]\n"
    "s_waitcnt lgkmcnt(0)\n"
    "v_pk_add_f32 v[164:165], v[164:165], v[176:177]\n"
    "v_pk_add_f32 v[166:167], v[166:167], v[178:179]\n"
    "v_cvt_pk_bf16_f32 v164, v164, v165\n"
    "v_cvt_pk_bf16_f32 v165, v166, v167\n"
    "v_min_u32 v164, -2, v164\n"
    "buffer_store_dwordx2 v[164:165], v203, s[0:3], s30 offen sc1\n"
    ".Lct_mk3x22_%=:\n"
    "s_sleep 2\n"
    "v_mov_b64 v[166:167], -1\n"
    "buffer_load_dwordx4 v[132:135], v199, s[0:3], s35 offen offset:0 sc1\n"
    "buffer_load_dwordx4 v[136:139], v199, s[0:3], s37 offen offset:0 sc1\n"
    "buffer_load_dwordx4 v[140:143], v199, s[0:3], s35 offen offset:256 sc1\n"
    "buffer_load_dwordx4 v[144:147], v199, s[0:3], s37 offen offset:256 sc1\n"
    "buffer_load_dwordx4 v[116:119], v198, s[0:3], s35 offen sc1\n"
    "buffer_load_dwordx4 v[84:87], v192, s[0:3], s35 offen offset:0 sc1\n"
    "buffer_load_dwordx4 v[88:91], v192, s[0:3], s35 offen offset:2048 sc1\n"
    "buffer_load_dwordx4 v[120:123], v198, s[0:3], s36 offen sc1\n"
    "buffer_load_dwordx4 v[92:95], v192, s[0:3], s36 offen offset:0 sc1\n"
    "buffer_load_dwordx4 v[96:99], v192, s[0:3], s36 offen offset:2048 sc1\n"
    ".Lct_mk7x23_%=:\n"
    "buffer_load_dwordx4 v[124:127], v198, s[0:3], s37 offen sc1\n"
    "s_waitcnt vmcnt(10)\n"
    "v_max3_u32 v184, v132, v134, v132\n"
    "v_swap_b32 v133, v134\n"
    "s_nop 1\n"
    "v_mfma_f32_16x16x32_bf16 v[148:151], v[132:135], v[40:43], 0\n"
    "v_mfma_f32_16x16x32_bf16 v[152:155], v[132:135], v[48:51], 0\n"
    "buffer_load_dwordx4 v[100:103], v192, s[0:3], s37 offen offset:0 sc1\n"
    "s_waitcnt vmcnt(10)\n"
    "v_max3_u32 v184, v136, v138, v184\n"
    "v_swap_b32 v137, v138\n"
    "s_nop 1\n"
    "v_mfma_f32_16x16x32_bf16 v[148:151], v[136:139], v[44:47], v[148:151]\n"
    "v_mfma_f32_16x16x32_bf16 v[152:155], v[136:139], v[52:55], v[152:155]\n"
    "buffer_load_dwordx4 v[104:107], v192, s[0:3], s37 offen offset:2048 sc1\n"
    "s_waitcnt vmcnt(10)\n"
    "v_max3_u32 v184, v140, v142, v184\n"
    "v_swap_b32 v141, v142\n"
    "s_nop 1\n"
    "v_mfma_f32_16x16x32_bf16 v[156:159], v[140:143], v[40:43], 0\n"
    "v_mfma_f32_16x16x32_bf16 v[160:163], v[140:143], v[48:51], 0\n"
    "buffer_load_dwordx4 v[128:131], v198, s[0:3], s38 offen sc1\n"
    "s_waitcnt vmcnt(10)\n"
    "v_max3_u32 v184, v144, v146, v184\n"
    "v_swap_b32 v145, v146\n"
    "s_nop 1\n"
    "v_mfma_f32_16x16x32_bf16 v[156:159], v[144:147], v[44:47], v[156:159]\n"
    "v_mfma_f32_16x16x32_bf16 v[160:163], v[144:147], v[52:55], v[160:163]\n"
    "buffer_load_dwordx4 v[108:111], v192, s[0:3], s38 offen offset:0 sc1\n"
    "s_waitcnt vmcnt(10)\n"
    "v_max3_u32 v184, v116, v118, v184\n"
    "v_smfmac_f32_16x16x64_bf16 v[148:151], v[116:119], a[32:39], v195\n"
    "v_smfmac_f32_16x16x64_bf16 v[152:155], v[116:119], v[8:15], v195\n"
    "buffer_load_dwordx4 v[112:115], v192, s[0:3], s38 offen offset:2048 sc1\n"
    "s_waitcnt vmcnt(10)\n"
    "v_max3_u32 v184, v84, v86, v184\n"
    "v_swap_b32 v85, v86\n"
    "s_nop 1\n"
    "v_mfma_f32_16x16x32_bf16 v[156:159], v[84:87], a[32:35], v[156:159]\n"
    "v_mfma_f32_16x16x32_bf16 v[160:163], v[84:87], v[8:11], v[160:163]\n"
    "buffer_load_dwordx2 v[186:187], v210, s[4:7], s46 offen sc1\n"
    "s_waitcnt vmcnt(10)\n"
    "v_max3_u32 v184, v88, v90, v184\n"
    "v_swap_b32 v89, v90\n"
    "s_nop 1\n"
    "v_mfma_f32_16x16x32_bf16 v[156:159], v[88:91], a[36:39], v[156:159]\n"
    "v_mfma_f32_16x16x32_bf16 v[160:163], v[88:91], v[12:15], v[160:163]\n"
    "buffer_store_dwordx2 v[166:167], v203, s[0:3], s31 offen sc1\n"
    "s_waitcnt vmcnt(10)\n"
    "v_max3_u32 v184, v120, v122, v184\n"
    "v_smfmac_f32_16x16x64_bf16 v[148:151], v[120:123], a[40:47], v195\n"
    "v_smfmac_f32_16x16x64_bf16 v[152:155], v[120:123], v[16:23], v195\n"
    "buffer_load_dwordx4 a[0:3], v192, s[0:3], s39 offen offset:0 sc1\n"
    "s_waitcnt vmcnt(10)\n"
    "v_max3_u32 v184, v92, v94, v184\n"
    "v_swap_b32 v93, v94\n"
    "s_nop 1\n"
    "v_mfma_f32_16x16x32_bf16 v[156:159], v[92:95], a[40:43], v[156:159]\n"
    "v_mfma_f32_16x16x32_bf16 v[160:163], v[92:95], v[16:19], v[160:163]\n"
    "buffer_load_dwordx4 a[4:7], v192, s[0:3], s39 offen offset:1024 sc1\n"
    "s_waitcnt vmcnt(10)\n"
    "v_max3_u32 v184, v96, v98, v184\n"
    "v_swap_b32 v97, v98\n"
    "s_nop 1\n"
    "v_mfma_f32_16x16x32_bf16 v[156:159], v[96:99], a[44:47], v[156:159]\n"
    "v_mfma_f32_16x16x32_bf16 v[160:163], v[96:99], v[20:23], v[160:163]\n"
    "buffer_load_dwordx4 a[8:11], v192, s[0:3], s39 offen offset:2048 sc1\n"
    "s_waitcnt vmcnt(10)\n"
    "v_max3_u32 v184, v124, v126, v184\n"
    "v_smfmac_f32_16x16x64_bf16 v[148:151], v[124:127], a[48:55], v195\n"
    "v_smfmac_f32_16x16x64_bf16 v[152:155], v[124:127], v[24:31], v195\n"
    "buffer_load_dwordx4 a[12:15], v192, s[0:3], s39 offen offset:3072 sc1\n"
    "s_waitcnt vmcnt(10)\n"
    "v_max3_u32 v184, v100, v102, v184\n"
    "v_swap_b32 v101, v102\n"
    "s_nop 1\n"
    "v_mfma_f32_16x16x32_bf16 v[156:159], v[100:103], a[48:51], v[156:159]\n"
    "v_mfma_f32_16x16x32_bf16 v[160:163], v[100:103], v[24:27], v[160:163]\n"
    "buffer_load_dwordx4 a[16:19], v193, s[0:3], s39 offen offset:0 sc1\n"
    "s_waitcnt vmcnt(10)\n"
    "v_max3_u32 v184, v104, v106, v184\n"
    "v_swap_b32 v105, v106\n"
    "s_nop 1\n"
    "v_mfma_f32_16x16x32_bf16 v[156:159], v[104:107], a[52:55], v[156:159]\n"
    "v_mfma_f32_16x16x32_bf16 v[160:163], v[104:107], v[28:31], v[160:163]\n"
    "buffer_load_dwordx4 a[20:23], v193, s[0:3], s39 offen offset:1024 sc1\n"
    "s_waitcnt vmcnt(10)\n"
    "v_max3_u32 v184, v128, v130, v184\n"
    "v_smfmac_f32_16x16x64_bf16 v[148:151], v[128:131], a[56:63], v195\n"
    "v_smfmac_f32_16x16x64_bf16 v[152:155], v[128:131], v[32:39], v195\n"
    "buffer_load_dwordx4 a[24:27], v193, s[0:3], s39 offen offset:2048 sc1\n"
    "s_waitcnt vmcnt(10)\n"
    "v_max3_u32 v184, v108, v110, v184\n"
    "v_swap_b32 v109, v110\n"
    "s_nop 1\n"
    "v_mfma_f32_16x16x32_bf16 v[156:159], v[108:111], a[56:59], v[156:159]\n"
    "v_mfma_f32_16x16x32_bf16 v[160:163], v[108:111], v[32:35], v[160:163]\n"
    "buffer_load_dwordx4 a[28:31], v193, s[0:3], s39 offen offset:3072 sc1\n"
    "s_waitcnt vmcnt(10)\n"
    "v_max3_u32 v184, v112, v114, v184\n"
    "v_swap_b32 v113, v114\n"
    "s_nop 1\n"
    "v_mfma_f32_16x16x32_bf16 v[156:159], v[112:115], a[60:63], v[156:159]\n"
    "v_mfma_f32_16x16x32_bf16 v[160:163], v[112:115], v[36:39], v[160:163]\n"
    "v_cmp_eq_u32 vcc, -1, v184\n"
    "s_nop 0\n"
    "s_cbranch_vccnz .Lct_tr1_%=\n"
    ".Lct_tg1_%=:\n"
    ".Lct_mk4x24_%=:\n"
    "ds_write2_b32 v204, v148, v149 offset0:0 offset1:20\n"
    "ds_write2_b32 v204, v150, v151 offset0:40 offset1:60\n"
    "ds_write2_b32 v205, v152, v153 offset0:0 offset1:20\n"
    "ds_write2_b32 v205, v154, v155 offset0:40 offset1:60\n"
    "ds_write2_b32 v206, v156, v157 offset0:0 offset1:20\n"
    "ds_write2_b32 v206, v158, v159 offset0:40 offset1:60\n"
    "ds_write2_b32 v207, v160, v161 offset0:0 offset1:20\n"
    "ds_write2_b32 v207, v162, v163 offset0:40 offset1:60\n"
    "s_mov_b32 s42, s45\n"
    "buffer_load_dwordx4 v[0:3], v196, s[0:3], s40 offen offset:0 sc1\n"
    "buffer_load_dwordx4 v[4:7], v196, s[0:3], s40 offen offset:1024 sc1\n"
    "s_add_u32 s28, s28, 1\n"
    "s_add_u32 s27, s28, 1\n"
    "s_add_u32 s29, s28, 0\n"
    "s_and_b32 s29, s29, 3\n"
    "s_lshl_b32 s29, s29, 22\n"
    "s_add_u32 s30, s28, 1\n"
    "s_and_b32 s30, s30, 3\n"
    "s_lshl_b32 s30, s30, 22\n"
    "s_add_u32 s31, s28, 3\n"
    "s_and_b32 s31, s31, 3\n"
    "s_lshl_b32 s31, s31, 22\n"
    "s_and_b32 s41, s28, 63\n"
    "s_lshl_b32 s41, s41, 7\n"
    "s_add_u32 s32, s12, s29\n"
    "s_add_u32 s33, s13, s29\n"
    "s_add_u32 s34, s15, s29\n"
    "s_add_u32 s35, s14, s29\n"
    "s_add_u32 s39, s10, s30\n"
    "s_add_u32 s40, s11, s30\n"
    "s_add_u32 s41, s16, s41\n"
    "s_add_u32 s36, s35, 4096\n"
    "s_add_u32 s37, s35, 16384\n"
    "s_add_u32 s38, s35, 20480\n"
    "s_add_u32 s43, s26, 300\n"
    "s_lshl_b32 s45, s28, 19\n"
    "s_lshl_b32 s25, s28, 11\n"
    "s_xor_b32 s25, s25, s8\n"
    "s_and_b32 s25, s25, 0x2000\n"
    "s_add_u32 s45, s45, s25\n"
    "s_add_u32 s45, s45, s9\n"
    "s_mul_i32 s46, s28, 40960\n"
    "s_cmp_eq_u32 s8, 0\n"
    "s_cselect_b32 s46, s46, s45\n"
    "s_cmp_eq_u32 s28, 63\n"
    "s_cselect_b32 s39, 2130706432, s39\n"
    "s_cselect_b32 s40, 2130706432, s40\n"
    "v_mov_b64 v[76:77], 0\n"
    "v_mov_b64 v[78:79], 0\n"
    "v_mov_b64 v[80:81], 0\n"
    "v_mov_b64 v[82:83], 0\n"
    "s_waitcnt vmcnt(11)\n"
    "v_lshlrev_b32 v148, 16, v186\n"
    "v_and_b32 v149, 0xffff0000, v186\n"
    "v_lshlrev_b32 v150, 16, v187\n"
    "v_and_b32 v151, 0xffff0000, v187\n"
    "s_memrealtime s[22:23]\n"
    "s_waitcnt lgkmcnt(0)\n"
    "s_barrier\n"
    "s_sub_u32 s24, s43, s22\n"
    "s_cmp_gt_i32 s24, 14\n"
    "s_cselect_b32 s24, 2130706432, s41\n"
    "buffer_load_dwordx2 v[190:191], off, s[0:3], s24 sc1\n"
    ".Lct_mk6x25_%=:\n"
    "ds_read_b128 v[164:167], v208 offset:0\n"
    "ds_read_b128 v[168:171], v208 offset:2560\n"
    "ds_read_b128 v[172:175], v208 offset:5120\n"
    "ds_read_b128 v[176:179], v208 offset:7680\n"
    "s_waitcnt lgkmcnt(2)\n"
    "v_pk_add_f32 v[164:165], v[164:165], v[168:169]\n"
    "v_pk_add_f32 v[166:167], v[166:167], v[170:171]\n"
    "s_waitcnt lgkmcnt(1)\n"
    "v_pk_add_f32 v[164:165], v[164:165], v[172:173]\n"
    "v_pk_add_f32 v[166:167], v[166:167], v[174:175]\n"
    "s_waitcnt lgkmcnt(0)\n"
    "v_pk_add_f32 v[164:165], v[164:165], v[176:177]\n"
    "v_pk_add_f32 v[166:167], v[166:167], v[178:179]\n"
    "v_cvt_pk_bf16_f32 v164, v164, v165\n"
    "v_cvt_pk_bf16_f32 v165, v166, v167\n"
    "v_lshlrev_b32 v168, 16, v164\n"
    "v_and_b32 v169, 0xffff0000, v164\n"
    "v_lshlrev_b32 v170, 16, v165\n"
    "v_and_b32 v171, 0xffff0000, v165\n"
    "v_pk_add_f32 v[168:169], v[168:169], v[148:149]\n"
    "v_pk_add_f32 v[170:171], v[170:171], v[150:151]\n"
    "v_cvt_pk_bf16_f32 v164, v168, v169\n"
    "v_cvt_pk_bf16_f32 v165, v170, v171\n"
    "buffer_store_dwordx2 v[164:165], v209, s[0:3], s42 offen sc1\n"
    ".Lct_mk5x26_%=:\n"
    "s_cmp_lt_u32 s28, 64\n"
    "s_cbranch_scc1 .Lct_top_%=\n"
    "v_mov_b32 v148, s28\n"
    "v_mov_b32 v149, s28\n"
    "v_mov_b32 v150, s28\n"
    "v_mov_b32 v151, s28\n"
    "s_waitcnt vmcnt(0)\n"
    "s_barrier\n"
    "buffer_store_dwordx4 v[148:151], v211, s[0:3], 0 offen sc1\n"
    "s_waitcnt vmcnt(0) lgkmcnt(0)\n"
    "s_endpgm\n"
    ".Lct_xr0_%=:\n"
    "s_add_u32 s20, s20, 1\n"
    "s_cmp_lt_u32 s20, 0x40000\n"
    "s_cbranch_scc1 .Lct_sp14_%=\n"
    "v_mov_b32 v184, 42\n"
    "s_mov_b32 s24, 33728\n"
    "buffer_store_dword v184, off, s[0:3], s24 sc1\n"
    "s_waitcnt vmcnt(0)\n"
    "s_branch .Lct_xg0_%=\n"
    ".Lct_sp14_%=:\n"
    ".Lct_rt15_%=:\n"
    "s_memrealtime s[22:23]\n"
    "s_waitcnt lgkmcnt(0)\n"
    "s_sub_u32 s24, s43, s22\n"
    "s_cmp_lt_u32 s24, 600\n"
    "s_cbranch_scc1 .Lct_rt15_%=\n"
    "s_add_u32 s43, s22, 44\n"
    "buffer_load_dwordx2 v[190:191], off, s[0:3], s41 sc1\n"
    "s_waitcnt vmcnt(0)\n"
    "s_branch .Lct_x0_%=\n"
    ".Lct_retry0_%=:\n"
    "s_add_u32 s20, s20, 1\n"
    "s_cmp_lt_u32 s20, 0x40000\n"
    "s_cbranch_scc1 .Lct_sp16_%=\n"
    "v_mov_b32 v184, 3\n"
    "s_mov_b32 s24, 33728\n"
    "buffer_store_dword v184, off, s[0:3], s24 sc1\n"
    "s_waitcnt vmcnt(0)\n"
    "s_branch .Lct_go0_%=\n"
    ".Lct_sp16_%=:\n"
    "s_waitcnt vmcnt(0) lgkmcnt(0)\n"
    "v_mov_b64 v[76:77], 0\n"
    "v_mov_b64 v[78:79], 0\n"
    "v_mov_b64 v[80:81], 0\n"
    "v_mov_b64 v[82:83], 0\n"
    "buffer_load_dwordx4 v[40:43], v192, s[0:3], s32 offen offset:0 sc1\n"
    "buffer_load_dwordx4 v[44:47], v192, s[0:3], s32 offen offset:1024 sc1\n"
    "buffer_load_dwordx4 v[48:51], v197, s[0:3], s33 offen offset:0 sc1\n"
    "buffer_load_dwordx4 v[52:55], v197, s[0:3], s33 offen offset:256 sc1\n"
    "buffer_load_dwordx4 v[72:75], v193, s[0:3], s34 offen sc1\n"
    "buffer_load_dwordx4 v[56:59], v192, s[0:3], s34 offen offset:0 sc1\n"
    "buffer_load_dwordx4 v[60:63], v192, s[0:3], s34 offen offset:1024 sc1\n"
    "buffer_load_dwordx4 v[64:67], v192, s[0:3], s34 offen offset:2048 sc1\n"
    "buffer_load_dwordx4 v[68:71], v192, s[0:3], s34 offen offset:3072 sc1\n"
    "buffer_store_dwordx4 v[148:151], v211, s[0:3], 0 offen sc1\n"
    "s_waitcnt vmcnt(0)\n"
    "s_branch .Lct_take0_%=\n"
    ".Lct_tr0_%=:\n"
    "s_add_u32 s20, s20, 1\n"
    "s_cmp_lt_u32 s20, 0x40000\n"
    "s_cbranch_scc1 .Lct_sp17_%=\n"
    "v_mov_b32 v184, 4\n"
    "s_mov_b32 s24, 33728\n"
    "buffer_store_dword v184, off, s[0:3], s24 sc1\n"
    "s_waitcnt vmcnt(0)\n"
    "s_branch .Lct_tg0_%=\n"
    ".Lct_sp17_%=:\n"
    "buffer_load_dwordx4 v[132:135], v199, s[0:3], s35 offen offset:0 sc1\n"
    "buffer_load_dwordx4 v[136:139], v199, s[0:3], s37 offen offset:0 sc1\n"
    "buffer_load_dwordx4 v[140:143], v199, s[0:3], s35 offen offset:256 sc1\n"
    "buffer_load_dwordx4 v[144:147], v199, s[0:3], s37 offen offset:256 sc1\n"
    "buffer_load_dwordx4 v[116:119], v198, s[0:3], s35 offen sc1\n"
    "buffer_load_dwordx4 v[84:87], v192, s[0:3], s35 offen offset:0 sc1\n"
    "buffer_load_dwordx4 v[88:91], v192, s[0:3], s35 offen offset:2048 sc1\n"
    "buffer_load_dwordx4 v[120:123], v198, s[0:3], s36 offen sc1\n"
    "buffer_load_dwordx4 v[92:95], v192, s[0:3], s36 offen offset:0 sc1\n"
    "buffer_load_dwordx4 v[96:99], v192, s[0:3], s36 offen offset:2048 sc1\n"
    "buffer_load_dwordx4 v[124:127], v198, s[0:3], s37 offen sc1\n"
    "buffer_load_dwordx4 v[100:103], v192, s[0:3], s37 offen offset:0 sc1\n"
    "buffer_load_dwordx4 v[104:107], v192, s[0:3], s37 offen offset:2048 sc1\n"
    "buffer_load_dwordx4 v[128:131], v198, s[0:3], s38 offen sc1\n"
    "buffer_load_dwordx4 v[108:111], v192, s[0:3], s38 offen offset:0 sc1\n"
    "buffer_load_dwordx4 v[112:115], v192, s[0:3], s38 offen offset:2048 sc1\n"
    "s_waitcnt vmcnt(0)\n"
    "v_max3_u32 v184, v132, v134, v132\n"
    "v_swap_b32 v133, v134\n"
    "s_nop 1\n"
    "v_mfma_f32_16x16x32_bf16 v[148:151], v[132:135], v[40:43], 0\n"
    "v_mfma_f32_16x16x32_bf16 v[152:155], v[132:135], v[48:51], 0\n"
    "v_max3_u32 v184, v136, v138, v184\n"
    "v_swap_b32 v137, v138\n"
    "s_nop 1\n"
    "v_mfma_f32_16x16x32_bf16 v[148:151], v[136:139], v[44:47], v[148:151]\n"
    "v_mfma_f32_16x16x32_bf16 v[152:155], v[136:139], v[52:55], v[152:155]\n"
    "v_max3_u32 v184, v140, v142, v184\n"
    "v_swap_b32 v141, v142\n"
    "s_nop 1\n"
    "v_mfma_f32_16x16x32_bf16 v[156:159], v[140:143], v[40:43], 0\n"
    "v_mfma_f32_16x16x32_bf16 v[160:163], v[140:143], v[48:51], 0\n"
    "v_max3_u32 v184, v144, v146, v184\n"
    "v_swap_b32 v145, v146\n"
    "s_nop 1\n"
    "v_mfma_f32_16x16x32_bf16 v[156:159], v[144:147], v[44:47], v[156:159]\n"
    "v_mfma_f32_16x16x32_bf16 v[160:163], v[144:147], v[52:55], v[160:163]\n"
    "v_max3_u32 v184, v116, v118, v184\n"
    "v_smfmac_f32_16x16x64_bf16 v[148:151], v[116:119], a[0:7], v195\n"
    "v_smfmac_f32_16x16x64_bf16 v[152:155], v[116:119], v[8:15], v195\n"
    "v_max3_u32 v184, v84, v86, v184\n"
    "v_swap_b32 v85, v86\n"
    "s_nop 1\n"
    "v_mfma_f32_16x16x32_bf16 v[156:159], v[84:87], a[0:3], v[156:159]\n"
    "v_mfma_f32_16x16x32_bf16 v[160:163], v[84:87], v[8:11], v[160:163]\n"
    "v_max3_u32 v184, v88, v90, v184\n"
    "v_swap_b32 v89, v90\n"
    "s_nop 1\n"
    "v_mfma_f32_16x16x32_bf16 v[156:159], v[88:91], a[4:7], v[156:159]\n"
    "v_mfma_f32_16x16x32_bf16 v[160:163], v[88:91], v[12:15], v[160:163]\n"
    "v_max3_u32 v184, v120, v122, v184\n"
    "v_smfmac_f32_16x16x64_bf16 v[148:151], v[120:123], a[8:15], v195\n"
    "v_smfmac_f32_16x16x64_bf16 v[152:155], v[120:123], v[16:23], v195\n"
    "v_max3_u32 v184, v92, v94, v184\n"
    "v_swap_b32 v93, v94\n"
    "s_nop 1\n"
    "v_mfma_f32_16x16x32_bf16 v[156:159], v[92:95], a[8:11], v[156:159]\n"
    "v_mfma_f32_16x16x32_bf16 v[160:163], v[92:95], v[16:19], v[160:163]\n"
    "v_max3_u32 v184, v96, v98, v184\n"
    "v_swap_b32 v97, v98\n"
    "s_nop 1\n"
    "v_mfma_f32_16x16x32_bf16 v[156:159], v[96:99], a[12:15], v[156:159]\n"
    "v_mfma_f32_16x16x32_bf16 v[160:163], v[96:99], v[20:23], v[160:163]\n"
    "v_max3_u32 v184, v124, v126, v184\n"
    "v_smfmac_f32_16x16x64_bf16 v[148:151], v[124:127], a[16:23], v195\n"
    "v_smfmac_f32_16x16x64_bf16 v[152:155], v[124:127], v[24:31], v195\n"
    "v_max3_u32 v184, v100, v102, v184\n"
    "v_swap_b32 v101, v102\n"
    "s_nop 1\n"
    "v_mfma_f32_16x16x32_bf16 v[156:159], v[100:103], a[16:19], v[156:159]\n"
    "v_mfma_f32_16x16x32_bf16 v[160:163], v[100:103], v[24:27], v[160:163]\n"
    "v_max3_u32 v184, v104, v106, v184\n"
    "v_swap_b32 v105, v106\n"
    "s_nop 1\n"
    "v_mfma_f32_16x16x32_bf16 v[156:159], v[104:107], a[20:23], v[156:159]\n"
    "v_mfma_f32_16x16x32_bf16 v[160:163], v[104:107], v[28:31], v[160:163]\n"
    "v_max3_u32 v184, v128, v130, v184\n"
    "v_smfmac_f32_16x16x64_bf16 v[148:151], v[128:131], a[24:31], v195\n"
    "v_smfmac_f32_16x16x64_bf16 v[152:155], v[128:131], v[32:39], v195\n"
    "v_max3_u32 v184, v108, v110, v184\n"
    "v_swap_b32 v109, v110\n"
    "s_nop 1\n"
    "v_mfma_f32_16x16x32_bf16 v[156:159], v[108:111], a[24:27], v[156:159]\n"
    "v_mfma_f32_16x16x32_bf16 v[160:163], v[108:111], v[32:35], v[160:163]\n"
    "v_max3_u32 v184, v112, v114, v184\n"
    "v_swap_b32 v113, v114\n"
    "s_nop 1\n"
    "v_mfma_f32_16x16x32_bf16 v[156:159], v[112:115], a[28:31], v[156:159]\n"
    "v_mfma_f32_16x16x32_bf16 v[160:163], v[112:115], v[36:39], v[160:163]\n"
    "v_cmp_eq_u32 vcc, -1, v184\n"
    "s_nop 0\n"
    "s_cbranch_vccnz .Lct_tr0_%=\n"
    "s_nop 7\n"
    "s_branch .Lct_tg0_%=\n"
    ".Lct_xr1_%=:\n"
    "s_add_u32 s20, s20, 1\n"
    "s_cmp_lt_u32 s20, 0x40000\n"
    "s_cbranch_scc1 .Lct_sp28_%=\n"
    "v_mov_b32 v184, 42\n"
    "s_mov_b32 s24, 33728\n"
    "buffer_store_dword v184, off, s[0:3], s24 sc1\n"
    "s_waitcnt vmcnt(0)\n"
    "s_branch .Lct_xg1_%=\n"
    ".Lct_sp28_%=:\n"
    ".Lct_rt29_%=:\n"
    "s_memrealtime s[22:23]\n"
    "s_waitcnt lgkmcnt(0)\n"
    "s_sub_u32 s24, s43, s22\n"
    "s_cmp_lt_u32 s24, 600\n"
    "s_cbranch_scc1 .Lct_rt29_%=\n"
    "s_add_u32 s43, s22, 44\n"
    "buffer_load_dwordx2 v[190:191], off, s[0:3], s41 sc1\n"
    "s_waitcnt vmcnt(0)\n"
    "s_branch .Lct_x1_%=\n"
    ".Lct_retry1_%=:\n"
    "s_add_u32 s20, s20, 1\n"
    "s_cmp_lt_u32 s20, 0x40000\n"
    "s_cbranch_scc1 .Lct_sp30_%=\n"
    "v_mov_b32 v184, 3\n"
    "s_mov_b32 s24, 33728\n"
    "buffer_store_dword v184, off, s[0:3], s24 sc1\n"
    "s_waitcnt vmcnt(0)\n"
    "s_branch .Lct_go1_%=\n"
    ".Lct_sp30_%=:\n"
    "s_waitcnt vmcnt(0) lgkmcnt(0)\n"
    "v_mov_b64 v[76:77], 0\n"
    "v_mov_b64 v[78:79], 0\n"
    "v_mov_b64 v[80:81], 0\n"
    "v_mov_b64 v[82:83], 0\n"
    "buffer_load_dwordx4 v[40:43], v192, s[0:3], s32 offen offset:0 sc1\n"
    "buffer_load_dwordx4 v[44:47], v192, s[0:3], s32 offen offset:1024 sc1\n"
    "buffer_load_dwordx4 v[48:51], v197, s[0:3], s33 offen offset:0 sc1\n"
    "buffer_load_dwordx4 v[52:55], v197, s[0:3], s33 offen offset:256 sc1\n"
    "buffer_load_dwordx4 v[72:75], v193, s[0:3], s34 offen sc1\n"
    "buffer_load_dwordx4 v[56:59], v192, s[0:3], s34 offen offset:0 sc1\n"
    "buffer_load_dwordx4 v[60:63], v192, s[0:3], s34 offen offset:1024 sc1\n"
    "buffer_load_dwordx4 v[64:67], v192, s[0:3], s34 offen offset:2048 sc1\n"
    "buffer_load_dwordx4 v[68:71], v192, s[0:3], s34 offen offset:3072 sc1\n"
    "buffer_store_dwordx4 v[148:151], v211, s[0:3], 0 offen sc1\n"
    "s_waitcnt vmcnt(0)\n"
    "s_branch .Lct_take1_%=\n"
    ".Lct_tr1_%=:\n"
    "s_add_u32 s20, s20, 1\n"
    "s_cmp_lt_u32 s20, 0x40000\n"
    "s_cbranch_scc1 .Lct_sp31_%=\n"
    "v_mov_b32 v184, 4\n"
    "s_mov_b32 s24, 33728\n"
    "buffer_store_dword v184, off, s[0:3], s24 sc1\n"
    "s_waitcnt vmcnt(0)\n"
    "s_branch .Lct_tg1_%=\n"
    ".Lct_sp31_%=:\n"
    "buffer_load_dwordx4 v[132:135], v199, s[0:3], s35 offen offset:0 sc1\n"
    "buffer_load_dwordx4 v[136:139], v199, s[0:3], s37 offen offset:0 sc1\n"
    "buffer_load_dwordx4 v[140:143], v199, s[0:3], s35 offen offset:256 sc1\n"
    "buffer_load_dwordx4 v[144:147], v199, s[0:3], s37 offen offset:256 sc1\n"
    "buffer_load_dwordx4 v[116:119], v198, s[0:3], s35 offen sc1\n"
    "buffer_load_dwordx4 v[84:87], v192, s[0:3], s35 offen offset:0 sc1\n"
    "buffer_load_dwordx4 v[88:91], v192, s[0:3], s35 offen offset:2048 sc1\n"
    "buffer_load_dwordx4 v[120:123], v198, s[0:3], s36 offen sc1\n"
    "buffer_load_dwordx4 v[92:95], v192, s[0:3], s36 offen offset:0 sc1\n"
    "buffer_load_dwordx4 v[96:99], v192, s[0:3], s36 offen offset:2048 sc1\n"
    "buffer_load_dwordx4 v[124:127], v198, s[0:3], s37 offen sc1\n"
    "buffer_load_dwordx4 v[100:103], v192, s[0:3], s37 offen offset:0 sc1\n"
    "buffer_load_dwordx4 v[104:107], v192, s[0:3], s37 offen offset:2048 sc1\n"
    "buffer_load_dwordx4 v[128:131], v198, s[0:3], s38 offen sc1\n"
    "buffer_load_dwordx4 v[108:111], v192, s[0:3], s38 offen offset:0 sc1\n"
    "buffer_load_dwordx4 v[112:115], v192, s[0:3], s38 offen offset:2048 sc1\n"
    "s_waitcnt vmcnt(0)\n"
    "v_max3_u32 v184, v132, v134, v132\n"
    "v_swap_b32 v133, v134\n"
    "s_nop 1\n"
    "v_mfma_f32_16x16x32_bf16 v[148:151], v[132:135], v[40:43], 0\n"
    "v_mfma_f32_16x16x32_bf16 v[152:155], v[132:135], v[48:51], 0\n"
    "v_max3_u32 v184, v136, v138, v184\n"
    "v_swap_b32 v137, v138\n"
    "s_nop 1\n"
    "v_mfma_f32_16x16x32_bf16 v[148:151], v[136:139], v[44:47], v[148:151]\n"
    "v_mfma_f32_16x16x32_bf16 v[152:155], v[136:139], v[52:55], v[152:155]\n"
    "v_max3_u32 v184, v140, v142, v184\n"
    "v_swap_b32 v141, v142\n"
    "s_nop 1\n"
    "v_mfma_f32_16x16x32_bf16 v[156:159], v[140:143], v[40:43], 0\n"
    "v_mfma_f32_16x16x32_bf16 v[160:163], v[140:143], v[48:51], 0\n"
    "v_max3_u32 v184, v144, v146, v184\n"
    "v_swap_b32 v145, v146\n"
    "s_nop 1\n"
    "v_mfma_f32_16x16x32_bf16 v[156:159], v[144:147], v[44:47], v[156:159]\n"
    "v_mfma_f32_16x16x32_bf16 v[160:163], v[144:147], v[52:55], v[160:163]\n"
    "v_max3_u32 v184, v116, v118, v184\n"
    "v_smfmac_f32_16x16x64_bf16 v[148:151], v[116:119], a[32:39], v195\n"
    "v_smfmac_f32_16x16x64_bf16 v[152:155], v[116:119], v[8:15], v195\n"
    "v_max3_u32 v184, v84, v86, v184\n"
    "v_swap_b32 v85, v86\n"
    "s_nop 1\n"
    "v_mfma_f32_16x16x32_bf16 v[156:159], v[84:87], a[32:35], v[156:159]\n"
    "v_mfma_f32_16x16x32_bf16 v[160:163], v[84:87], v[8:11], v[160:163]\n"
    "v_max3_u32 v184, v88, v90, v184\n"
    "v_swap_b32 v89, v90\n"
    "s_nop 1\n"
    "v_mfma_f32_16x16x32_bf16 v[156:159], v[88:91], a[36:39], v[156:159]\n"
    "v_mfma_f32_16x16x32_bf16 v[160:163], v[88:91], v[12:15], v[160:163]\n"
    "v_max3_u32 v184, v120, v122, v184\n"
    "v_smfmac_f32_16x16x64_bf16 v[148:151], v[120:123], a[40:47], v195\n"
    "v_smfmac_f32_16x16x64_bf16 v[152:155], v[120:123], v[16:23], v195\n"
    "v_max3_u32 v184, v92, v94, v184\n"
    "v_swap_b32 v93, v94\n"
    "s_nop 1\n"
    "v_mfma_f32_16x16x32_bf16 v[156:159], v[92:95], a[40:43], v[156:159]\n"
    "v_mfma_f32_16x16x32_bf16 v[160:163], v[92:95], v[16:19], v[160:163]\n"
    "v_max3_u32 v184, v96, v98, v184\n"
    "v_swap_b32 v97, v98\n"
    "s_nop 1\n"
    "v_mfma_f32_16x16x32_bf16 v[156:159], v[96:99], a[44:47], v[156:159]\n"
    "v_mfma_f32_16x16x32_bf16 v[160:163], v[96:99], v[20:23], v[160:163]\n"
    "v_max3_u32 v184, v124, v126, v184\n"
    "v_smfmac_f32_16x16x64_bf16 v[148:151], v[124:127], a[48:55], v195\n"
    "v_smfmac_f32_16x16x64_bf16 v[152:155], v[124:127], v[24:31], v195\n"
    "v_max3_u32 v184, v100, v102, v184\n"
    "v_swap_b32 v101, v102\n"
    "s_nop 1\n"
    "v_mfma_f32_16x16x32_bf16 v[156:159], v[100:103], a[48:51], v[156:159]\n"
    "v_mfma_f32_16x16x32_bf16 v[160:163], v[100:103], v[24:27], v[160:163]\n"
    "v_max3_u32 v184, v104, v106, v184\n"
    "v_swap_b32 v105, v106\n"
    "s_nop 1\n"
    "v_mfma_f32_16x16x32_bf16 v[156:159], v[104:107], a[52:55], v[156:159]\n"
    "v_mfma_f32_16x16x32_bf16 v[160:163], v[104:107], v[28:31], v[160:163]\n"
    "v_max3_u32 v184, v128, v130, v184\n"
    "v_smfmac_f32_16x16x64_bf16 v[148:151], v[128:131], a[56:63], v195\n"
    "v_smfmac_f32_16x16x64_bf16 v[152:155], v[128:131], v[32:39], v195\n"
    "v_max3_u32 v184, v108, v110, v184\n"
    "v_swap_b32 v109, v110\n"
    "s_nop 1\n"
    "v_mfma_f32_16x16x32_bf16 v[156:159], v[108:111], a[56:59], v[156:159]\n"
    "v_mfma_f32_16x16x32_bf16 v[160:163], v[108:111], v[32:35], v[160:163]\n"
    "v_max3_u32 v184, v112, v114, v184\n"
    "v_swap_b32 v113, v114\n"
    "s_nop 1\n"
    "v_mfma_f32_16x16x32_bf16 v[156:159], v[112:115], a[60:63], v[156:159]\n"
    "v_mfma_f32_16x16x32_bf16 v[160:163], v[112:115], v[36:39], v[160:163]\n"
    "v_cmp_eq_u32 vcc, -1, v184\n"
    "s_nop 0\n"
    "s_cbranch_vccnz .Lct_tr1_%=\n"
    "s_nop 7\n"
    "s_branch .Lct_tg1_%=\n"
    :: "{s[0:3]}"(s0_), "{s[4:7]}"(s4_), "{s[8:11]}"(s8_), "{s[12:15]}"(s12_), "{s[16:19]}"(s16_), "{v[192:195]}"(v192_), "{v[196:199]}"(v196_), "{v[200:203]}"(v200_), "{v[204:207]}"(v204_), "{v[208:211]}"(v208_), "{v[212:215]}"(v212_) : "memory", "v255", "a255", "s95");
    __builtin_unreachable();
}
DEV void qw_role(const MegaArgs& A, const int L, const int h, const int rep) {
    ROLE_HEAD;
    const u32x4 s0_ = {RFL(rx[0]), RFL(rx[1]), RFL(rx[2]), RFL(rx[3])};
    const u32x4 s4_ = {RFL(lo(A.Wq + h * 192 + 64)), RFL(hi(A.Wq + h * 192 + 64)), RFL(WBYTES - h * 384 - 128), RFL(0x20000u)};
    const u32x4 s8_ = {RFL(lo(A.Wv + h * 192 + 128)), RFL(hi(A.Wv + h * 192 + 128)), RFL(WBYTES - h * 384 - 256), RFL(0x20000u)};
    const u32x4 s12_ = {RFL(w), RFL(REC0 + w * 2048), RFL(QT0 + h * 32768), RFL((A.layers & 2) ? 64 : 0)};
    const u32x4 s16_ = {RFL(xl(Z_CT + (h & 7) * 256, L)), RFL(DBG0 + ((L * 128 + 112 + (h >> 3) * 4 + (h & 3)) * 4 + w) * 2048), RFL(0u), RFL(0u)};
    const u32x4 v0_ = {(unsigned)((8 * w + (l >> 3)) * 12288 + ((l & 7) ^ (((8 * w + (l >> 3)) >> 1) & 6)) * 16), (unsigned)((8 * w + (l >> 3)) * 12288 + ((l & 7) ^ (((8 * w + (l >> 3)) >> 1) & 6)) * 16 + 128), (unsigned)((8 * lq + ((l >> 2) & 1) + ((l >> 1) & 4)) * 128 + (w ^ (((8 * lq + ((l >> 2) & 1) + ((l >> 1) & 4)) >> 2) & 3)) * 32 + (l & 3) * 8), (unsigned)(0u)};
    const u32x4 v64_ = {(unsigned)(l * 16), (unsigned)(l * 16 + 40960), (unsigned)(l * 16 + w * 2048), (unsigned)(l * 16 + w * 1024 + 16384)};
    const u32x4 v68_ = {(unsigned)(l == 99 ? xl(Z_X1 + rep * 4096 + h * 128, 1) : xl(Z_XN + rep * 2048, 0) + (l < 20 ? (l >> 2) * 128 + (l & 3) * 4 : 0)), (unsigned)(l == 0 ? xl(Z_QW + h * 128 + w * 4, 0) : OOB), (unsigned)(l == 20 ? 64 : 0), (unsigned)(0u)};
    asm volatile(
    "s_mov_b32 s20, 0\n"
    "s_mov_b64 s[22:23], 0\n"
    "s_mov_b32 s25, 0\n"
    "s_lshl_b32 s32, s12, 10\n"
    "s_mov_b32 s38, 0\n"
    "s_add_u32 m0, s32, 0\n"
    "s_nop 0\n"
    "buffer_load_dwordx4 v0, s[4:7], s38 offen nt lds\n"
    "s_add_u32 m0, s32, 4096\n"
    "s_nop 0\n"
    "buffer_load_dwordx4 v1, s[4:7], s38 offen nt lds\n"
    "s_add_u32 m0, s32, 8192\n"
    "s_nop 0\n"
    "buffer_load_dwordx4 v0, s[8:11], s38 offen nt lds\n"
    "s_mov_b32 s38, 1572864\n"
    "s_add_u32 m0, s32, 12288\n"
    "s_nop 0\n"
    "buffer_load_dwordx4 v0, s[4:7], s38 offen nt lds\n"
    "s_add_u32 m0, s32, 16384\n"
    "s_nop 0\n"
    "buffer_load_dwordx4 v1, s[4:7], s38 offen nt lds\n"
    "s_add_u32 m0, s32, 20480\n"
    "s_nop 0\n"
    "buffer_load_dwordx4 v0, s[8:11], s38 offen nt lds\n"
    "s_mov_b32 s38, 3145728\n"
    "s_add_u32 m0, s32, 24576\n"
    "s_nop 0\n"
    "buffer_load_dwordx4 v0, s[4:7], s38 offen nt lds\n"
    "s_add_u32 m0, s32, 28672\n"
    "s_nop 0\n"
    "buffer_load_dwordx4 v1, s[4:7], s38 offen nt lds\n"
    "s_add_u32 m0, s32, 32768\n"
    "s_nop 0\n"
    "buffer_load_dwordx4 v0, s[8:11], s38 offen nt lds\n"
    "s_mov_b32 s38, 4718592\n"
    "s_add_u32 m0, s32, 36864\n"
    "s_nop 0\n"
    "buffer_load_dwordx4 v0, s[4:7], s38 offen nt lds\n"
    "s_add_u32 m0, s32, 40960\n"
    "s_nop 0\n"
    "buffer_load_dwordx4 v1, s[4:7], s38 offen nt lds\n"
    "s_add_u32 m0, s32, 45056\n"
    "s_nop 0\n"
    "buffer_load_dwordx4 v0, s[8:11], s38 offen nt lds\n"
    "s_mov_b32 s38, 393216\n"
    "s_add_u32 m0, s32, 49152\n"
    "s_nop 0\n"
    "buffer_load_dwordx4 v0, s[4:7], s38 offen nt lds\n"
    "s_add_u32 m0, s32, 53248\n"
    "s_nop 0\n"
    "buffer_load_dwordx4 v1, s[4:7], s38 offen nt lds\n"
    "s_add_u32 m0, s32, 57344\n"
    "s_nop 0\n"
    "buffer_load_dwordx4 v0, s[8:11], s38 offen nt lds\n"
    "s_mov_b32 s38, 1966080\n"
    "s_add_u32 m0, s32, 61440\n"
    "s_nop 0\n"
    "buffer_load_dwordx4 v0, s[4:7], s38 offen nt lds\n"
    "s_add_u32 m0, s32, 65536\n"
    "s_nop 0\n"
    "buffer_load_dwordx4 v1, s[4:7], s38 offen nt lds\n"
    "s_add_u32 m0, s32, 69632\n"
    "s_nop 0\n"
    "buffer_load_dwordx4 v0, s[8:11], s38 offen nt lds\n"
    "s_mov_b32 s38, 3538944\n"
    "s_add_u32 m0, s32, 73728\n"
    "s_nop 0\n"
    "buffer_load_dwordx4 v0, s[4:7], s38 offen nt lds\n"
    "s_add_u32 m0, s32, 77824\n"
    "s_nop 0\n"
    "buffer_load_dwordx4 v1, s[4:7], s38 offen nt lds\n"
    "s_add_u32 m0, s32, 81920\n"
    "s_nop 0\n"
    "buffer_load_dwordx4 v0, s[8:11], s38 offen nt lds\n"
    "s_mov_b32 s38, 5111808\n"
    "s_add_u32 m0, s32, 86016\n"
    "s_nop 0\n"
    "buffer_load_dwordx4 v0, s[4:7], s38 offen nt lds\n"
    "s_add_u32 m0, s32, 90112\n"
    "s_nop 0\n"
    "buffer_load_dwordx4 v1, s[4:7], s38 offen nt lds\n"
    "s_add_u32 m0, s32, 94208\n"
    "s_nop 0\n"
    "buffer_load_dwordx4 v0, s[8:11], s38 offen nt lds\n"
    "s_mov_b32 s38, 786432\n"
    "s_add_u32 m0, s32, 98304\n"
    "s_nop 0\n"
    "buffer_load_dwordx4 v0, s[4:7], s38 offen nt lds\n"
    "s_add_u32 m0, s32, 102400\n"
    "s_nop 0\n"
    "buffer_load_dwordx4 v1, s[4:7], s38 offen nt lds\n"
    "s_add_u32 m0, s32, 106496\n"
    "s_nop 0\n"
    "buffer_load_dwordx4 v0, s[8:11], s38 offen nt lds\n"
    "s_mov_b32 s38, 2359296\n"
    "s_add_u32 m0, s32, 110592\n"
    "s_nop 0\n"
    "buffer_load_dwordx4 v0, s[4:7], s38 offen nt lds\n"
    "s_add_u32 m0, s32, 114688\n"
    "s_nop 0\n"
    "buffer_load_dwordx4 v1, s[4:7], s38 offen nt lds\n"
    "s_add_u32 m0, s32, 118784\n"
    "s_nop 0\n"
    "buffer_load_dwordx4 v0, s[8:11], s38 offen nt lds\n"
    "s_mov_b32 s38, 3932160\n"
    "s_add_u32 m0, s32, 122880\n"
    "s_nop 0\n"
    "buffer_load_dwordx4 v0, s[4:7], s38 offen nt lds\n"
    "s_add_u32 m0, s32, 126976\n"
    "s_nop 0\n"
    "buffer_load_dwordx4 v1, s[4:7], s38 offen nt lds\n"
    "s_add_u32 m0, s32, 131072\n"
    "s_nop 0\n"
    "buffer_load_dwordx4 v0, s[8:11], s38 offen nt lds\n"
    "s_mov_b32 s38, 5505024\n"
    "s_add_u32 m0, s32, 135168\n"
    "s_nop 0\n"
    "buffer_load_dwordx4 v0, s[4:7], s38 offen nt lds\n"
    "s_add_u32 m0, s32, 139264\n"
    "s_nop 0\n"
    "buffer_load_dwordx4 v1, s[4:7], s38 offen nt lds\n"
    "s_add_u32 m0, s32, 143360\n"
    "s_nop 0\n"
    "buffer_load_dwordx4 v0, s[8:11], s38 offen nt lds\n"
    "s_waitcnt vmcnt(24)\n"
    "s_barrier\n"
    "v_add_u32 v8, 0, v2\n"
    "ds_read_b64_tr_b16 a[0:1], v8 offset:0\n"
    "ds_read_b64_tr_b16 a[2:3], v8 offset:256\n"
    "ds_read_b64_tr_b16 a[80:81], v8 offset:4096\n"
    "ds_read_b64_tr_b16 a[82:83], v8 offset:4352\n"
    "ds_read_b64_tr_b16 a[160:161], v8 offset:8192\n"
    "ds_read_b64_tr_b16 a[162:163], v8 offset:8448\n"
    "ds_read_b64_tr_b16 a[16:17], v8 offset:12288\n"
    "ds_read_b64_tr_b16 a[18:19], v8 offset:12544\n"
    "ds_read_b64_tr_b16 a[96:97], v8 offset:16384\n"
    "ds_read_b64_tr_b16 a[98:99], v8 offset:16640\n"
    "ds_read_b64_tr_b16 a[176:177], v8 offset:20480\n"
    "ds_read_b64_tr_b16 a[178:179], v8 offset:20736\n"
    "ds_read_b64_tr_b16 a[32:33], v8 offset:24576\n"
    "ds_read_b64_tr_b16 a[34:35], v8 offset:24832\n"
    "ds_read_b64_tr_b16 a[112:113], v8 offset:28672\n"
    "ds_read_b64_tr_b16 a[114:115], v8 offset:28928\n"
    "ds_read_b64_tr_b16 a[192:193], v8 offset:32768\n"
    "ds_read_b64_tr_b16 a[194:195], v8 offset:33024\n"
    "ds_read_b64_tr_b16 a[48:49], v8 offset:36864\n"
    "ds_read_b64_tr_b16 a[50:51], v8 offset:37120\n"
    "ds_read_b64_tr_b16 a[128:129], v8 offset:40960\n"
    "ds_read_b64_tr_b16 a[130:131], v8 offset:41216\n"
    "ds_read_b64_tr_b16 a[208:209], v8 offset:45056\n"
    "ds_read_b64_tr_b16 a[210:211], v8 offset:45312\n"
    "s_waitcnt lgkmcnt(0)\n"
    "s_barrier\n"
    "s_mov_b32 s38, 1179648\n"
    "s_add_u32 m0, s32, 0\n"
    "s_nop 0\n"
    "buffer_load_dwordx4 v0, s[4:7], s38 offen nt lds\n"
    "s_add_u32 m0, s32, 4096\n"
    "s_nop 0\n"
    "buffer_load_dwordx4 v1, s[4:7], s38 offen nt lds\n"
    "s_add_u32 m0, s32, 8192\n"
    "s_nop 0\n"
    "buffer_load_dwordx4 v0, s[8:11], s38 offen nt lds\n"
    "s_mov_b32 s38, 2752512\n"
    "s_add_u32 m0, s32, 12288\n"
    "s_nop 0\n"
    "buffer_load_dwordx4 v0, s[4:7], s38 offen nt lds\n"
    "s_add_u32 m0, s32, 16384\n"
    "s_nop 0\n"
    "buffer_load_dwordx4 v1, s[4:7], s38 offen nt lds\n"
    "s_add_u32 m0, s32, 20480\n"
    "s_nop 0\n"
    "buffer_load_dwordx4 v0, s[8:11], s38 offen nt lds\n"
    "s_mov_b32 s38, 4325376\n"
    "s_add_u32 m0, s32, 24576\n"
    "s_nop 0\n"
    "buffer_load_dwordx4 v0, s[4:7], s38 offen nt lds\n"
    "s_add_u32 m0, s32, 28672\n"
    "s_nop 0\n"
    "buffer_load_dwordx4 v1, s[4:7], s38 offen nt lds\n"
    "s_add_u32 m0, s32, 32768\n"
    "s_nop 0\n"
    "buffer_load_dwordx4 v0, s[8:11], s38 offen nt lds\n"
    "s_mov_b32 s38, 5898240\n"
    "s_add_u32 m0, s32, 36864\n"
    "s_nop 0\n"
    "buffer_load_dwordx4 v0, s[4:7], s38 offen nt lds\n"
    "s_add_u32 m0, s32, 40960\n"
    "s_nop 0\n"
    "buffer_load_dwordx4 v1, s[4:7], s38 offen nt lds\n"
    "s_add_u32 m0, s32, 45056\n"
    "s_nop 0\n"
    "buffer_load_dwordx4 v0, s[8:11], s38 offen nt lds\n"
    "v_and_b32 v4, 0x70, v64\n"
    "v_lshlrev_b32 v4, 2, v4\n"
    "v_or_b32 v4, 0x8000, v4\n"
    "buffer_load_dword v5, v4, s[0:3], 0 offen sc1\n"
    "s_waitcnt vmcnt(25)\n"
    "s_barrier\n"
    "v_add_u32 v8, 49152, v2\n"
    "ds_read_b64_tr_b16 a[4:5], v8 offset:0\n"
    "ds_read_b64_tr_b16 a[6:7], v8 offset:256\n"
    "ds_read_b64_tr_b16 a[84:85], v8 offset:4096\n"
    "ds_read_b64_tr_b16 a[86:87], v8 offset:4352\n"
    "ds_read_b64_tr_b16 a[164:165], v8 offset:8192\n"
    "ds_read_b64_tr_b16 a[166:167], v8 offset:8448\n"
    "ds_read_b64_tr_b16 a[20:21], v8 offset:12288\n"
    "ds_read_b64_tr_b16 a[22:23], v8 offset:12544\n"
    "ds_read_b64_tr_b16 a[100:101], v8 offset:16384\n"
    "ds_read_b64_tr_b16 a[102:103], v8 offset:16640\n"
    "ds_read_b64_tr_b16 a[180:181], v8 offset:20480\n"
    "ds_read_b64_tr_b16 a[182:183], v8 offset:20736\n"
    "ds_read_b64_tr_b16 a[36:37], v8 offset:24576\n"
    "ds_read_b64_tr_b16 a[38:39], v8 offset:24832\n"
    "ds_read_b64_tr_b16 a[116:117], v8 offset:28672\n"
    "ds_read_b64_tr_b16 a[118:119], v8 offset:28928\n"
    "ds_read_b64_tr_b16 a[196:197], v8 offset:32768\n"
    "ds_read_b64_tr_b16 a[198:199], v8 offset:33024\n"
    "ds_read_b64_tr_b16 a[52:53], v8 offset:36864\n"
    "ds_read_b64_tr_b16 a[54:55], v8 offset:37120\n"
    "ds_read_b64_tr_b16 a[132:133], v8 offset:40960\n"
    "ds_read_b64_tr_b16 a[134:135], v8 offset:41216\n"
    "ds_read_b64_tr_b16 a[212:213], v8 offset:45056\n"
    "ds_read_b64_tr_b16 a[214:215], v8 offset:45312\n"
    "s_waitcnt lgkmcnt(0)\n"
    "s_barrier\n"
    "s_mov_b32 s38, 6291456\n"
    "s_add_u32 m0, s32, 49152\n"
    "s_nop 0\n"
    "buffer_load_dwordx4 v0, s[4:7], s38 offen nt lds\n"
    "s_add_u32 m0, s32, 53248\n"
    "s_nop 0\n"
    "buffer_load_dwordx4 v1, s[4:7], s38 offen nt lds\n"
    "s_add_u32 m0, s32, 57344\n"
    "s_nop 0\n"
    "buffer_load_dwordx4 v0, s[8:11], s38 offen nt lds\n"
    "s_mov_b32 s38, 6684672\n"
    "s_add_u32 m0, s32, 61440\n"
    "s_nop 0\n"
    "buffer_load_dwordx4 v0, s[4:7], s38 offen nt lds\n"
    "s_add_u32 m0, s32, 65536\n"
    "s_nop 0\n"
    "buffer_load_dwordx4 v1, s[4:7], s38 offen nt lds\n"
    "s_add_u32 m0, s32, 69632\n"
    "s_nop 0\n"
    "buffer_load_dwordx4 v0, s[8:11], s38 offen nt lds\n"
    "s_mov_b32 s38, 7077888\n"
    "s_add_u32 m0, s32, 73728\n"
    "s_nop 0\n"
    "buffer_load_dwordx4 v0, s[4:7], s38 offen nt lds\n"
    "s_add_u32 m0, s32, 77824\n"
    "s_nop 0\n"
    "buffer_load_dwordx4 v1, s[4:7], s38 offen nt lds\n"
    "s_add_u32 m0, s32, 81920\n"
    "s_nop 0\n"
    "buffer_load_dwordx4 v0, s[8:11], s38 offen nt lds\n"
    "s_mov_b32 s38, 7471104\n"
    "s_add_u32 m0, s32, 86016\n"
    "s_nop 0\n"
    "buffer_load_dwordx4 v0, s[4:7], s38 offen nt lds\n"
    "s_add_u32 m0, s32, 90112\n"
    "s_nop 0\n"
    "buffer_load_dwordx4 v1, s[4:7], s38 offen nt lds\n"
    "s_add_u32 m0, s32, 94208\n"
    "s_nop 0\n"
    "buffer_load_dwordx4 v0, s[8:11], s38 offen nt lds\n"
    "s_waitcnt vmcnt(25)\n"
    "s_barrier\n"
    "v_add_u32 v8, 98304, v2\n"
    "ds_read_b64_tr_b16 a[8:9], v8 offset:0\n"
    "ds_read_b64_tr_b16 a[10:11], v8 offset:256\n"
    "ds_read_b64_tr_b16 a[88:89], v8 offset:4096\n"
    "ds_read_b64_tr_b16 a[90:91], v8 offset:4352\n"
    "ds_read_b64_tr_b16 a[168:169], v8 offset:8192\n"
    "ds_read_b64_tr_b16 a[170:171], v8 offset:8448\n"
    "ds_read_b64_tr_b16 a[24:25], v8 offset:12288\n"
    "ds_read_b64_tr_b16 a[26:27], v8 offset:12544\n"
    "ds_read_b64_tr_b16 a[104:105], v8 offset:16384\n"
    "ds_read_b64_tr_b16 a[106:107], v8 offset:16640\n"
    "ds_read_b64_tr_b16 a[184:185], v8 offset:20480\n"
    "ds_read_b64_tr_b16 a[186:187], v8 offset:20736\n"
    "ds_read_b64_tr_b16 a[40:41], v8 offset:24576\n"
    "ds_read_b64_tr_b16 a[42:43], v8 offset:24832\n"
    "ds_read_b64_tr_b16 a[120:121], v8 offset:28672\n"
    "ds_read_b64_tr_b16 a[122:123], v8 offset:28928\n"
    "ds_read_b64_tr_b16 a[200:201], v8 offset:32768\n"
    "ds_read_b64_tr_b16 a[202:203], v8 offset:33024\n"
    "ds_read_b64_tr_b16 a[56:57], v8 offset:36864\n"
    "ds_read_b64_tr_b16 a[58:59], v8 offset:37120\n"
    "ds_read_b64_tr_b16 a[136:137], v8 offset:40960\n"
    "ds_read_b64_tr_b16 a[138:139], v8 offset:41216\n"
    "ds_read_b64_tr_b16 a[216:217], v8 offset:45056\n"
    "ds_read_b64_tr_b16 a[218:219], v8 offset:45312\n"
    "s_waitcnt lgkmcnt(0)\n"
    "s_barrier\n"
    "s_waitcnt vmcnt(13)\n"
    "s_barrier\n"
    "v_add_u32 v8, 0, v2\n"
    "ds_read_b64_tr_b16 a[12:13], v8 offset:0\n"
    "ds_read_b64_tr_b16 a[14:15], v8 offset:256\n"
    "ds_read_b64_tr_b16 a[92:93], v8 offset:4096\n"
    "ds_read_b64_tr_b16 a[94:95], v8 offset:4352\n"
    "ds_read_b64_tr_b16 a[172:173], v8 offset:8192\n"
    "ds_read_b64_tr_b16 a[174:175], v8 offset:8448\n"
    "ds_read_b64_tr_b16 a[28:29], v8 offset:12288\n"
    "ds_read_b64_tr_b16 a[30:31], v8 offset:12544\n"
    "ds_read_b64_tr_b16 a[108:109], v8 offset:16384\n"
    "ds_read_b64_tr_b16 a[110:111], v8 offset:16640\n"
    "ds_read_b64_tr_b16 a[188:189], v8 offset:20480\n"
    "ds_read_b64_tr_b16 a[190:191], v8 offset:20736\n"
    "ds_read_b64_tr_b16 a[44:45], v8 offset:24576\n"
    "ds_read_b64_tr_b16 a[46:47], v8 offset:24832\n"
    "ds_read_b64_tr_b16 a[124:125], v8 offset:28672\n"
    "ds_read_b64_tr_b16 a[126:127], v8 offset:28928\n"
    "ds_read_b64_tr_b16 a[204:205], v8 offset:32768\n"
    "ds_read_b64_tr_b16 a[206:207], v8 offset:33024\n"
    "ds_read_b64_tr_b16 a[60:61], v8 offset:36864\n"
    "ds_read_b64_tr_b16 a[62:63], v8 offset:37120\n"
    "ds_read_b64_tr_b16 a[140:141], v8 offset:40960\n"
    "ds_read_b64_tr_b16 a[142:143], v8 offset:41216\n"
    "ds_read_b64_tr_b16 a[220:221], v8 offset:45056\n"
    "ds_read_b64_tr_b16 a[222:223], v8 offset:45312\n"
    "s_waitcnt lgkmcnt(0)\n"
    "s_barrier\n"
    "s_waitcnt vmcnt(0)\n"
    "s_barrier\n"
    "v_add_u32 v8, 49152, v2\n"
    "ds_read_b64_tr_b16 a[64:65], v8 offset:0\n"
    "ds_read_b64_tr_b16 a[66:67], v8 offset:256\n"
    "ds_read_b64_tr_b16 a[144:145], v8 offset:4096\n"
    "ds_read_b64_tr_b16 a[146:147], v8 offset:4352\n"
    "ds_read_b64_tr_b16 a[224:225], v8 offset:8192\n"
    "ds_read_b64_tr_b16 a[226:227], v8 offset:8448\n"
    "ds_read_b64_tr_b16 a[68:69], v8 offset:12288\n"
    "ds_read_b64_tr_b16 a[70:71], v8 offset:12544\n"
    "ds_read_b64_tr_b16 a[148:149], v8 offset:16384\n"
    "ds_read_b64_tr_b16 a[150:151], v8 offset:16640\n"
    "ds_read_b64_tr_b16 a[228:229], v8 offset:20480\n"
    "ds_read_b64_tr_b16 a[230:231], v8 offset:20736\n"
    "ds_read_b64_tr_b16 a[72:73], v8 offset:24576\n"
    "ds_read_b64_tr_b16 a[74:75], v8 offset:24832\n"
    "ds_read_b64_tr_b16 a[152:153], v8 offset:28672\n"
    "ds_read_b64_tr_b16 a[154:155], v8 offset:28928\n"
    "ds_read_b64_tr_b16 a[232:233], v8 offset:32768\n"
    "ds_read_b64_tr_b16 a[234:235], v8 offset:33024\n"
    "ds_read_b64_tr_b16 a[76:77], v8 offset:36864\n"
    "ds_read_b64_tr_b16 a[78:79], v8 offset:37120\n"
    "ds_read_b64_tr_b16 a[156:157], v8 offset:40960\n"
    "ds_read_b64_tr_b16 a[158:159], v8 offset:41216\n"
    "ds_read_b64_tr_b16 a[236:237], v8 offset:45056\n"
    "ds_read_b64_tr_b16 a[238:239], v8 offset:45312\n"
    "s_waitcnt lgkmcnt(0)\n"
    "s_barrier\n"
    "s_waitcnt vmcnt(12)\n"
    "v_cmp_gt_i32 vcc, 32, v5\n"
    "s_nop 0\n"
    "s_cbranch_vccnz .Lqw_tk0_%=\n"
    ".Lqw_tk1_%=:\n"
    "s_mov_b32 s26, -2\n"
    "v_lshrrev_b32 v4, 2, v64\n"
    ".Lqw_po4_%=:\n"
    "buffer_load_dword v8, v4, s[0:3], s16 offen sc1\n"
    "s_waitcnt vmcnt(0)\n"
    "v_cmp_gt_i32 vcc, 1, v8\n"
    "s_nop 0\n"
    "s_cbranch_vccz .Lqw_pd5_%=\n"
    "s_add_u32 s20, s20, 1\n"
    "s_cmp_lt_u32 s20, 0x40000\n"
    "s_cbranch_scc1 .Lqw_sp6_%=\n"
    "v_mov_b32 v8, 50\n"
    "s_mov_b32 s24, 33728\n"
    "buffer_store_dword v8, off, s[0:3], s24 sc1\n"
    "s_waitcnt vmcnt(0)\n"
    "s_branch .Lqw_pd5_%=\n"
    ".Lqw_sp6_%=:\n"
    "s_sleep 4\n"
    "s_branch .Lqw_po4_%=\n"
    ".Lqw_pd5_%=:\n"
    "s_mov_b32 s26, -1\n"
    "s_mov_b32 s35, 0\n"
    "s_lshl_b32 s37, s12, 11\n"
    "s_mov_b32 s45, 0\n"
    "v_mov_b32 v60, 0\n"
    "s_mov_b32 s43, 2130706432\n"
    "s_mov_b32 s41, 2130706432\n"
    "s_mov_b32 s46, 2130706432\n"
    "s_mov_b32 s34, 2130706432\n"
    "s_add_u32 s26, s26, 1\n"
    "s_sub_u32 s27, s26, 14\n"
    "s_ashr_i32 s28, s27, 1\n"
    "s_and_b32 s29, s27, 1\n"
    "s_add_u32 s30, s28, 14\n"
    "s_cmp_lt_i32 s27, 0\n"
    "s_cselect_b32 s29, 0, s29\n"
    "s_cselect_b32 s30, s26, s30\n"
    "s_cmp_eq_u32 s29, 1\n"
    "s_cselect_b32 s30, s28, s30\n"
    "s_cselect_b32 s27, s15, 64\n"
    "s_lshl_b32 s31, s29, 13\n"
    "s_add_u32 s40, s30, 1\n"
    "s_lshl_b32 s32, s30, 19\n"
    "s_lshl_b32 s25, s30, 11\n"
    "s_xor_b32 s25, s25, s31\n"
    "s_and_b32 s25, s25, 0x2000\n"
    "s_add_u32 s32, s32, s25\n"
    "s_add_u32 s32, s32, s13\n"
    "s_lshl_b32 s33, s30, 20\n"
    "s_lshl_b32 s25, s30, 12\n"
    "s_xor_b32 s25, s25, s31\n"
    "s_and_b32 s25, s25, 0x2000\n"
    "s_add_u32 s33, s33, s25\n"
    "s_add_u32 s33, s33, s14\n"
    "s_mul_i32 s47, s40, s29\n"
    "s_cmp_lt_u32 s30, s27\n"
    "s_cselect_b32 s36, 1, 0\n"
    "s_cselect_b32 s32, s32, 2130706432\n"
    "s_cmp_lt_u32 s30, s27\n"
    "s_cselect_b32 s39, s31, 2130706432\n"
    "s_cselect_b32 s47, s47, 0\n"
    "s_cmp_gt_u32 s47, 0\n"
    "s_cselect_b32 s38, 0, 2130706432\n"
    ".Lqw_top_%=:\n"
    "ds_read_b128 v[24:27], v65 offset:0\n"
    "ds_read_b128 v[28:31], v65 offset:1024\n"
    "ds_read_b128 v[32:35], v65 offset:2048\n"
    "ds_read_b128 v[36:39], v65 offset:3072\n"
    "ds_read_b128 v[40:43], v65 offset:4096\n"
    "ds_read_b128 v[44:47], v65 offset:5120\n"
    "v_add_u32 v60, v60, v70\n"
    "v_cmp_gt_i32 vcc, s47, v60\n"
    "s_nop 0\n"
    "s_cbranch_vccnz .Lqw_wx_%=\n"
    ".Lqw_wxb_%=:\n"
    "s_cmp_eq_u32 s35, 0\n"
    "s_cbranch_scc1 .Lqw_skip_%=\n"
    ".Lqw_mk0x6_%=:\n"
    "s_waitcnt lgkmcnt(4)\n"
    "v_mfma_f32_16x16x32_bf16 v[0:3], a[0:3], v[24:27], 0\n"
    "buffer_store_dwordx4 v[48:51], v66, s[0:3], s46 offen offset:0 sc1\n"
    "v_mfma_f32_16x16x32_bf16 v[4:7], a[0:3], v[28:31], 0\n"
    "v_mfma_f32_16x16x32_bf16 v[8:11], a[80:83], v[24:27], 0\n"
    "v_mfma_f32_16x16x32_bf16 v[12:15], a[80:83], v[28:31], 0\n"
    "v_mfma_f32_16x16x32_bf16 v[16:19], a[160:163], v[24:27], 0\n"
    "ds_read_b128 v[24:27], v65 offset:6144\n"
    "v_mfma_f32_16x16x32_bf16 v[20:23], a[160:163], v[28:31], 0\n"
    "ds_read_b128 v[28:31], v65 offset:7168\n"
    "s_waitcnt lgkmcnt(4)\n"
    "v_mfma_f32_16x16x32_bf16 v[0:3], a[4:7], v[32:35], v[0:3]\n"
    "v_mfma_f32_16x16x32_bf16 v[4:7], a[4:7], v[36:39], v[4:7]\n"
    "buffer_store_dwordx4 v[52:55], v66, s[0:3], s46 offen offset:1024 sc1\n"
    "v_mfma_f32_16x16x32_bf16 v[8:11], a[84:87], v[32:35], v[8:11]\n"
    "v_mfma_f32_16x16x32_bf16 v[12:15], a[84:87], v[36:39], v[12:15]\n"
    "v_mfma_f32_16x16x32_bf16 v[16:19], a[164:167], v[32:35], v[16:19]\n"
    "ds_read_b128 v[32:35], v65 offset:8192\n"
    "v_mfma_f32_16x16x32_bf16 v[20:23], a[164:167], v[36:39], v[20:23]\n"
    "ds_read_b128 v[36:39], v65 offset:9216\n"
    "s_waitcnt lgkmcnt(4)\n"
    "v_mfma_f32_16x16x32_bf16 v[0:3], a[8:11], v[40:43], v[0:3]\n"
    "v_mfma_f32_16x16x32_bf16 v[4:7], a[8:11], v[44:47], v[4:7]\n"
    "v_mfma_f32_16x16x32_bf16 v[8:11], a[88:91], v[40:43], v[8:11]\n"
    "buffer_store_dwordx4 v[56:59], v67, s[0:3], s46 offen sc1\n"
    "s_add_u32 m0, s37, 0\n"
    "v_mfma_f32_16x16x32_bf16 v[12:15], a[88:91], v[44:47], v[12:15]\n"
    "v_mfma_f32_16x16x32_bf16 v[16:19], a[168:171], v[40:43], v[16:19]\n"
    "ds_read_b128 v[40:43], v65 offset:10240\n"
    "v_mfma_f32_16x16x32_bf16 v[20:23], a[168:171], v[44:47], v[20:23]\n"
    "ds_read_b128 v[44:47], v65 offset:11264\n"
    "s_waitcnt lgkmcnt(4)\n"
    "v_mfma_f32_16x16x32_bf16 v[0:3], a[12:15], v[24:27], v[0:3]\n"
    "v_mfma_f32_16x16x32_bf16 v[4:7], a[12:15], v[28:31], v[4:7]\n"
    "v_mfma_f32_16x16x32_bf16 v[8:11], a[92:95], v[24:27], v[8:11]\n"
    "v_mfma_f32_16x16x32_bf16 v[12:15], a[92:95], v[28:31], v[12:15]\n"
    "buffer_load_dwordx4 v64, s[0:3], s32 offen sc1 lds\n"
    "v_mfma_f32_16x16x32_bf16 v[16:19], a[172:175], v[24:27], v[16:19]\n"
    "ds_read_b128 v[24:27], v65 offset:12288\n"
    "v_mfma_f32_16x16x32_bf16 v[20:23], a[172:175], v[28:31], v[20:23]\n"
    "ds_read_b128 v[28:31], v65 offset:13312\n"
    "s_waitcnt lgkmcnt(4)\n"
    "v_mfma_f32_16x16x32_bf16 v[0:3], a[16:19], v[32:35], v[0:3]\n"
    "v_mfma_f32_16x16x32_bf16 v[4:7], a[16:19], v[36:39], v[4:7]\n"
    "buffer_load_dwordx4 v64, s[0:3], s32 offen offset:1024 sc1 lds\n"
    "v_mfma_f32_16x16x32_bf16 v[8:11], a[96:99], v[32:35], v[8:11]\n"
    "s_add_u32 m0, s37, 8192\n"
    "s_add_u32 s32, s32, 0x4000\n"
    "v_mfma_f32_16x16x32_bf16 v[12:15], a[96:99], v[36:39], v[12:15]\n"
    "v_mfma_f32_16x16x32_bf16 v[16:19], a[176:179], v[32:35], v[16:19]\n"
    "ds_read_b128 v[32:35], v65 offset:14336\n"
    "v_mfma_f32_16x16x32_bf16 v[20:23], a[176:179], v[36:39], v[20:23]\n"
    "ds_read_b128 v[36:39], v65 offset:15360\n"
    "s_waitcnt lgkmcnt(4)\n"
    "v_mfma_f32_16x16x32_bf16 v[0:3], a[20:23], v[40:43], v[0:3]\n"
    "buffer_load_dwordx4 v64, s[0:3], s32 offen sc1 lds\n"
    "v_mfma_f32_16x16x32_bf16 v[4:7], a[20:23], v[44:47], v[4:7]\n"
    "v_mfma_f32_16x16x32_bf16 v[8:11], a[100:103], v[40:43], v[8:11]\n"
    "v_mfma_f32_16x16x32_bf16 v[12:15], a[100:103], v[44:47], v[12:15]\n"
    "v_mfma_f32_16x16x32_bf16 v[16:19], a[180:183], v[40:43], v[16:19]\n"
    "ds_read_b128 v[40:43], v65 offset:16384\n"
    "v_mfma_f32_16x16x32_bf16 v[20:23], a[180:183], v[44:47], v[20:23]\n"
    "ds_read_b128 v[44:47], v65 offset:17408\n"
    "s_waitcnt lgkmcnt(4)\n"
    "v_mfma_f32_16x16x32_bf16 v[0:3], a[24:27], v[24:27], v[0:3]\n"
    "buffer_load_dwordx4 v64, s[0:3], s32 offen offset:1024 sc1 lds\n"
    "v_mfma_f32_16x16x32_bf16 v[4:7], a[24:27], v[28:31], v[4:7]\n"
    "s_add_u32 m0, s37, 16384\n"
    "s_add_u32 s32, s32, 0x4000\n"
    "v_mfma_f32_16x16x32_bf16 v[8:11], a[104:107], v[24:27], v[8:11]\n"
    "v_mfma_f32_16x16x32_bf16 v[12:15], a[104:107], v[28:31], v[12:15]\n"
    "v_mfma_f32_16x16x32_bf16 v[16:19], a[184:187], v[24:27], v[16:19]\n"
    "ds_read_b128 v[24:27], v65 offset:18432\n"
    "v_mfma_f32_16x16x32_bf16 v[20:23], a[184:187], v[28:31], v[20:23]\n"
    "ds_read_b128 v[28:31], v65 offset:19456\n"
    "s_waitcnt lgkmcnt(4)\n"
    "v_mfma_f32_16x16x32_bf16 v[0:3], a[28:31], v[32:35], v[0:3]\n"
    "buffer_load_dwordx4 v64, s[0:3], s32 offen sc1 lds\n"
    "v_mfma_f32_16x16x32_bf16 v[4:7], a[28:31], v[36:39], v[4:7]\n"
    "v_mfma_f32_16x16x32_bf16 v[8:11], a[108:111], v[32:35], v[8:11]\n"
    "v_mfma_f32_16x16x32_bf16 v[12:15], a[108:111], v[36:39], v[12:15]\n"
    "v_mfma_f32_16x16x32_bf16 v[16:19], a[188:191], v[32:35], v[16:19]\n"
    "ds_read_b128 v[32:35], v65 offset:20480\n"
    "v_mfma_f32_16x16x32_bf16 v[20:23], a[188:191], v[36:39], v[20:23]\n"
    "ds_read_b128 v[36:39], v65 offset:21504\n"
    "s_waitcnt lgkmcnt(4)\n"
    "v_mfma_f32_16x16x32_bf16 v[0:3], a[32:35], v[40:43], v[0:3]\n"
    "buffer_load_dwordx4 v64, s[0:3], s32 offen offset:1024 sc1 lds\n"
    "v_mfma_f32_16x16x32_bf16 v[4:7], a[32:35], v[44:47], v[4:7]\n"
    "s_add_u32 m0, s37, 24576\n"
    "s_add_u32 s32, s32, 0x4000\n"
    "v_mfma_f32_16x16x32_bf16 v[8:11], a[112:115], v[40:43], v[8:11]\n"
    "v_mfma_f32_16x16x32_bf16 v[12:15], a[112:115], v[44:47], v[12:15]\n"
    "v_mfma_f32_16x16x32_bf16 v[16:19], a[192:195], v[40:43], v[16:19]\n"
    "ds_read_b128 v[40:43], v65 offset:22528\n"
    "v_mfma_f32_16x16x32_bf16 v[20:23], a[192:195], v[44:47], v[20:23]\n"
    "ds_read_b128 v[44:47], v65 offset:23552\n"
    "s_waitcnt lgkmcnt(4)\n"
    "v_mfma_f32_16x16x32_bf16 v[0:3], a[36:39], v[24:27], v[0:3]\n"
    "buffer_load_dwordx4 v64, s[0:3], s32 offen sc1 lds\n"
    "v_mfma_f32_16x16x32_bf16 v[4:7], a[36:39], v[28:31], v[4:7]\n"
    "v_mfma_f32_16x16x32_bf16 v[8:11], a[116:119], v[24:27], v[8:11]\n"
    "v_mfma_f32_16x16x32_bf16 v[12:15], a[116:119], v[28:31], v[12:15]\n"
    "v_mfma_f32_16x16x32_bf16 v[16:19], a[196:199], v[24:27], v[16:19]\n"
    "ds_read_b128 v[24:27], v65 offset:24576\n"
    "v_mfma_f32_16x16x32_bf16 v[20:23], a[196:199], v[28:31], v[20:23]\n"
    "ds_read_b128 v[28:31], v65 offset:25600\n"
    "s_waitcnt lgkmcnt(4)\n"
    "v_mfma_f32_16x16x32_bf16 v[0:3], a[40:43], v[32:35], v[0:3]\n"
    "buffer_load_dwordx4 v64, s[0:3], s32 offen offset:1024 sc1 lds\n"
    "v_mfma_f32_16x16x32_bf16 v[4:7], a[40:43], v[36:39], v[4:7]\n"
    "s_add_u32 m0, s37, 32768\n"
    "s_add_u32 s32, s32, 0x4000\n"
    "v_mfma_f32_16x16x32_bf16 v[8:11], a[120:123], v[32:35], v[8:11]\n"
    "v_mfma_f32_16x16x32_bf16 v[12:15], a[120:123], v[36:39], v[12:15]\n"
    "v_mfma_f32_16x16x32_bf16 v[16:19], a[200:203], v[32:35], v[16:19]\n"
    "ds_read_b128 v[32:35], v65 offset:26624\n"
    "v_mfma_f32_16x16x32_bf16 v[20:23], a[200:203], v[36:39], v[20:23]\n"
    "ds_read_b128 v[36:39], v65 offset:27648\n"
    "s_waitcnt lgkmcnt(4)\n"
    "v_mfma_f32_16x16x32_bf16 v[0:3], a[44:47], v[40:43], v[0:3]\n"
    "buffer_load_dwordx4 v64, s[0:3], s32 offen sc1 lds\n"
    "v_mfma_f32_16x16x32_bf16 v[4:7], a[44:47], v[44:47], v[4:7]\n"
    "v_mfma_f32_16x16x32_bf16 v[8:11], a[124:127], v[40:43], v[8:11]\n"
    "v_mfma_f32_16x16x32_bf16 v[12:15], a[124:127], v[44:47], v[12:15]\n"
    "v_mfma_f32_16x16x32_bf16 v[16:19], a[204:207], v[40:43], v[16:19]\n"
    "ds_read_b128 v[40:43], v65 offset:28672\n"
    "v_mfma_f32_16x16x32_bf16 v[20:23], a[204:207], v[44:47], v[20:23]\n"
    "ds_read_b128 v[44:47], v65 offset:29696\n"
    "s_waitcnt lgkmcnt(4)\n"
    "v_mfma_f32_16x16x32_bf16 v[0:3], a[48:51], v[24:27], v[0:3]\n"
    "buffer_load_dwordx4 v64, s[0:3], s32 offen offset:1024 sc1 lds\n"
    "v_mfma_f32_16x16x32_bf16 v[4:7], a[48:51], v[28:31], v[4:7]\n"
    "s_waitcnt vmcnt(10)\n"
    "v_mov_b32 v61, s44\n"
    "v_mfma_f32_16x16x32_bf16 v[8:11], a[128:131], v[24:27], v[8:11]\n"
    "v_mfma_f32_16x16x32_bf16 v[12:15], a[128:131], v[28:31], v[12:15]\n"
    "v_mfma_f32_16x16x32_bf16 v[16:19], a[208:211], v[24:27], v[16:19]\n"
    "buffer_store_dword v61, v69, s[0:3], s43 offen sc1\n"
    "ds_read_b128 v[24:27], v65 offset:30720\n"
    "v_mfma_f32_16x16x32_bf16 v[20:23], a[208:211], v[28:31], v[20:23]\n"
    "s_mov_b32 s46, s34\n"
    "ds_read_b128 v[28:31], v65 offset:31744\n"
    "s_waitcnt lgkmcnt(4)\n"
    "v_mfma_f32_16x16x32_bf16 v[0:3], a[52:55], v[32:35], v[0:3]\n"
    "s_mov_b32 s43, s41\n"
    "s_mov_b32 s44, s42\n"
    "s_mov_b32 s41, s39\n"
    "v_mfma_f32_16x16x32_bf16 v[4:7], a[52:55], v[36:39], v[4:7]\n"
    "s_mov_b32 s42, s40\n"
    "s_mov_b32 s34, s33\n"
    "s_mov_b32 s35, s36\n"
    "v_mfma_f32_16x16x32_bf16 v[8:11], a[132:135], v[32:35], v[8:11]\n"
    "s_xor_b32 s37, s37, 40960\n"
    "s_add_u32 s26, s26, 1\n"
    "s_sub_u32 s27, s26, 14\n"
    "v_mfma_f32_16x16x32_bf16 v[12:15], a[132:135], v[36:39], v[12:15]\n"
    "s_ashr_i32 s28, s27, 1\n"
    "s_and_b32 s29, s27, 1\n"
    "s_add_u32 s30, s28, 14\n"
    "v_mfma_f32_16x16x32_bf16 v[16:19], a[212:215], v[32:35], v[16:19]\n"
    "ds_read_b128 v[32:35], v65 offset:32768\n"
    "v_mfma_f32_16x16x32_bf16 v[20:23], a[212:215], v[36:39], v[20:23]\n"
    "ds_read_b128 v[36:39], v65 offset:33792\n"
    "s_waitcnt lgkmcnt(4)\n"
    "v_mfma_f32_16x16x32_bf16 v[0:3], a[56:59], v[40:43], v[0:3]\n"
    "s_cmp_lt_i32 s27, 0\n"
    "s_cselect_b32 s29, 0, s29\n"
    "s_cselect_b32 s30, s26, s30\n"
    "v_mfma_f32_16x16x32_bf16 v[4:7], a[56:59], v[44:47], v[4:7]\n"
    "s_cmp_eq_u32 s29, 1\n"
    "s_cselect_b32 s30, s28, s30\n"
    "s_cselect_b32 s27, s15, 64\n"
    "v_mfma_f32_16x16x32_bf16 v[8:11], a[136:139], v[40:43], v[8:11]\n"
    "s_lshl_b32 s31, s29, 13\n"
    "s_add_u32 s40, s30, 1\n"
    "s_lshl_b32 s32, s30, 19\n"
    "v_mfma_f32_16x16x32_bf16 v[12:15], a[136:139], v[44:47], v[12:15]\n"
    "s_lshl_b32 s25, s30, 11\n"
    "s_xor_b32 s25, s25, s31\n"
    "s_and_b32 s25, s25, 0x2000\n"
    "v_mfma_f32_16x16x32_bf16 v[16:19], a[216:219], v[40:43], v[16:19]\n"
    "ds_read_b128 v[40:43], v65 offset:34816\n"
    "s_add_u32 s32, s32, s25\n"
    "v_mfma_f32_16x16x32_bf16 v[20:23], a[216:219], v[44:47], v[20:23]\n"
    "s_add_u32 s32, s32, s13\n"
    "ds_read_b128 v[44:47], v65 offset:35840\n"
    "s_waitcnt lgkmcnt(4)\n"
    "v_mfma_f32_16x16x32_bf16 v[0:3], a[60:63], v[24:27], v[0:3]\n"
    "s_lshl_b32 s33, s30, 20\n"
    "s_lshl_b32 s25, s30, 12\n"
    "s_xor_b32 s25, s25, s31\n"
    "v_mfma_f32_16x16x32_bf16 v[4:7], a[60:63], v[28:31], v[4:7]\n"
    "s_and_b32 s25, s25, 0x2000\n"
    "s_add_u32 s33, s33, s25\n"
    "s_add_u32 s33, s33, s14\n"
    "v_mfma_f32_16x16x32_bf16 v[8:11], a[140:143], v[24:27], v[8:11]\n"
    "s_mul_i32 s47, s40, s29\n"
    "v_mfma_f32_16x16x32_bf16 v[12:15], a[140:143], v[28:31], v[12:15]\n"
    "s_cmp_lt_u32 s30, s27\n"
    "s_cselect_b32 s36, 1, 0\n"
    "s_cselect_b32 s32, s32, 2130706432\n"
    "v_mfma_f32_16x16x32_bf16 v[16:19], a[220:223], v[24:27], v[16:19]\n"
    "ds_read_b128 v[24:27], v65 offset:36864\n"
    "v_mfma_f32_16x16x32_bf16 v[20:23], a[220:223], v[28:31], v[20:23]\n"
    "ds_read_b128 v[28:31], v65 offset:37888\n"
    "s_waitcnt lgkmcnt(4)\n"
    "v_mfma_f32_16x16x32_bf16 v[0:3], a[64:67], v[32:35], v[0:3]\n"
    "s_cmp_lt_u32 s30, s27\n"
    "s_cselect_b32 s39, s31, 2130706432\n"
    "s_cselect_b32 s47, s47, 0\n"
    "v_mfma_f32_16x16x32_bf16 v[4:7], a[64:67], v[36:39], v[4:7]\n"
    "s_cmp_gt_u32 s47, 0\n"
    "s_cselect_b32 s38, 0, 2130706432\n"
    "v_mfma_f32_16x16x32_bf16 v[8:11], a[144:147], v[32:35], v[8:11]\n"
    "buffer_load_dword v60, v68, s[0:3], s38 offen sc1\n"
    "v_mfma_f32_16x16x32_bf16 v[12:15], a[144:147], v[36:39], v[12:15]\n"
    "v_mfma_f32_16x16x32_bf16 v[16:19], a[224:227], v[32:35], v[16:19]\n"
    "ds_read_b128 v[32:35], v65 offset:38912\n"
    "v_mfma_f32_16x16x32_bf16 v[20:23], a[224:227], v[36:39], v[20:23]\n"
    "ds_read_b128 v[36:39], v65 offset:39936\n"
    "s_waitcnt lgkmcnt(4)\n"
    "v_mfma_f32_16x16x32_bf16 v[0:3], a[68:71], v[40:43], v[0:3]\n"
    "v_mfma_f32_16x16x32_bf16 v[4:7], a[68:71], v[44:47], v[4:7]\n"
    "v_mfma_f32_16x16x32_bf16 v[8:11], a[148:151], v[40:43], v[8:11]\n"
    "v_mfma_f32_16x16x32_bf16 v[12:15], a[148:151], v[44:47], v[12:15]\n"
    "v_mfma_f32_16x16x32_bf16 v[16:19], a[228:231], v[40:43], v[16:19]\n"
    "v_mfma_f32_16x16x32_bf16 v[20:23], a[228:231], v[44:47], v[20:23]\n"
    "s_waitcnt lgkmcnt(2)\n"
    "v_mfma_f32_16x16x32_bf16 v[0:3], a[72:75], v[24:27], v[0:3]\n"
    "v_mfma_f32_16x16x32_bf16 v[4:7], a[72:75], v[28:31], v[4:7]\n"
    "v_mfma_f32_16x16x32_bf16 v[8:11], a[152:155], v[24:27], v[8:11]\n"
    "v_mfma_f32_16x16x32_bf16 v[12:15], a[152:155], v[28:31], v[12:15]\n"
    "v_mfma_f32_16x16x32_bf16 v[16:19], a[232:235], v[24:27], v[16:19]\n"
    "v_mfma_f32_16x16x32_bf16 v[20:23], a[232:235], v[28:31], v[20:23]\n"
    "s_waitcnt lgkmcnt(0)\n"
    "v_mfma_f32_16x16x32_bf16 v[0:3], a[76:79], v[32:35], v[0:3]\n"
    "v_mfma_f32_16x16x32_bf16 v[4:7], a[76:79], v[36:39], v[4:7]\n"
    "v_mfma_f32_16x16x32_bf16 v[8:11], a[156:159], v[32:35], v[8:11]\n"
    "v_mfma_f32_16x16x32_bf16 v[12:15], a[156:159], v[36:39], v[12:15]\n"
    "v_mfma_f32_16x16x32_bf16 v[16:19], a[236:239], v[32:35], v[16:19]\n"
    "v_mfma_f32_16x16x32_bf16 v[20:23], a[236:239], v[36:39], v[20:23]\n"
    "v_cvt_pk_bf16_f32 v48, v0, v1\n"
    "v_cvt_pk_bf16_f32 v49, v2, v3\n"
    "v_cvt_pk_bf16_f32 v50, v8, v9\n"
    "v_cvt_pk_bf16_f32 v51, v10, v11\n"
    "v_cvt_pk_bf16_f32 v52, v4, v5\n"
    "v_cvt_pk_bf16_f32 v53, v6, v7\n"
    "v_cvt_pk_bf16_f32 v54, v12, v13\n"
    "v_cvt_pk_bf16_f32 v55, v14, v15\n"
    "v_cvt_pk_bf16_f32 v56, v16, v17\n"
    "v_cvt_pk_bf16_f32 v57, v18, v19\n"
    "v_cvt_pk_bf16_f32 v58, v20, v21\n"
    "v_cvt_pk_bf16_f32 v59, v22, v23\n"
    ".Lqw_mk1x7_%=:\n"
    ".Lqw_join_%=:\n"
    "v_xor_b32 v65, 40960, v65\n"
    "s_waitcnt vmcnt(0) lgkmcnt(0)\n"
    "s_barrier\n"
    ".Lqw_mk2x8_%=:\n"
    "s_cmp_le_u32 s26, 142\n"
    "s_cbranch_scc1 .Lqw_top_%=\n"
    "buffer_store_dwordx4 v[48:51], v66, s[0:3], s46 offen offset:0 sc1\n"
    "buffer_store_dwordx4 v[52:55], v66, s[0:3], s46 offen offset:1024 sc1\n"
    "buffer_store_dwordx4 v[56:59], v67, s[0:3], s46 offen sc1\n"
    "s_waitcnt vmcnt(0)\n"
    "v_mov_b32 v61, s44\n"
    "buffer_store_dword v61, v69, s[0:3], s43 offen sc1\n"
    "s_waitcnt vmcnt(0) lgkmcnt(0)\n"
    "s_endpgm\n"
    ".Lqw_tk0_%=:\n"
    ".Lqw_po1_%=:\n"
    "buffer_load_dword v5, v4, s[0:3], 0 offen sc1\n"
    "s_waitcnt vmcnt(0)\n"
    "v_cmp_gt_i32 vcc, 32, v5\n"
    "s_nop 0\n"
    "s_cbranch_vccz .Lqw_pd2_%=\n"
    "s_add_u32 s20, s20, 1\n"
    "s_cmp_lt_u32 s20, 0x40000\n"
    "s_cbranch_scc1 .Lqw_sp3_%=\n"
    "v_mov_b32 v5, 90\n"
    "s_mov_b32 s24, 33728\n"
    "buffer_store_dword v5, off, s[0:3], s24 sc1\n"
    "s_waitcnt vmcnt(0)\n"
    "s_branch .Lqw_pd2_%=\n"
    ".Lqw_sp3_%=:\n"
    "s_sleep 8\n"
    "s_branch .Lqw_po1_%=\n"
    ".Lqw_pd2_%=:\n"
    "s_branch .Lqw_tk1_%=\n"
    ".Lqw_wx_%=:\n"
    "buffer_store_dwordx4 v[48:51], v66, s[0:3], s46 offen offset:0 sc1\n"
    "buffer_store_dwordx4 v[52:55], v66, s[0:3], s46 offen offset:1024 sc1\n"
    "buffer_store_dwordx4 v[56:59], v67, s[0:3], s46 offen sc1\n"
    "s_waitcnt vmcnt(0)\n"
    "v_mov_b32 v61, s44\n"
    "buffer_store_dword v61, v69, s[0:3], s43 offen sc1\n"
    "s_mov_b32 s43, 2130706432\n"
    "s_mov_b32 s46, 2130706432\n"
    ".Lqw_rt10_%=:\n"
    "s_memrealtime s[22:23]\n"
    "s_waitcnt lgkmcnt(0)\n"
    "s_sub_u32 s24, s45, s22\n"
    "s_cmp_lt_u32 s24, 600\n"
    "s_cbranch_scc1 .Lqw_rt10_%=\n"
    ".Lqw_po11_%=:\n"
    "buffer_load_dword v60, v68, s[0:3], 0 offen sc1\n"
    "s_waitcnt vmcnt(0)\n"
    "v_add_u32 v60, v60, v70\n"
    "s_memrealtime s[22:23]\n"
    "s_waitcnt lgkmcnt(0)\n"
    "s_add_u32 s45, s22, 224\n"
    "v_cmp_gt_i32 vcc, s47, v60\n"
    "s_nop 0\n"
    "s_cbranch_vccz .Lqw_pd12_%=\n"
    "s_add_u32 s20, s20, 1\n"
    "s_cmp_lt_u32 s20, 0x40000\n"
    "s_cbranch_scc1 .Lqw_sp13_%=\n"
    "v_mov_b32 v60, 51\n"
    "s_mov_b32 s24, 33728\n"
    "buffer_store_dword v60, off, s[0:3], s24 sc1\n"
    "s_waitcnt vmcnt(0)\n"
    "s_branch .Lqw_pd12_%=\n"
    ".Lqw_sp13_%=:\n"
    "s_sleep 10\n"
    "s_branch .Lqw_po11_%=\n"
    ".Lqw_pd12_%=:\n"
    "s_branch .Lqw_wxb_%=\n"
    ".Lqw_skip_%=:\n"
    "buffer_store_dwordx4 v[48:51], v66, s[0:3], s46 offen offset:0 sc1\n"
    "buffer_store_dwordx4 v[52:55], v66, s[0:3], s46 offen offset:1024 sc1\n"
    "buffer_store_dwordx4 v[56:59], v67, s[0:3], s46 offen sc1\n"
    "s_add_u32 m0, s37, 0\n"
    "s_nop 0\n"
    "buffer_load_dwordx4 v64, s[0:3], s32 offen sc1 lds\n"
    "buffer_load_dwordx4 v64, s[0:3], s32 offen offset:1024 sc1 lds\n"
    "s_add_u32 m0, s37, 8192\n"
    "s_add_u32 s32, s32, 0x4000\n"
    "buffer_load_dwordx4 v64, s[0:3], s32 offen sc1 lds\n"
    "buffer_load_dwordx4 v64, s[0:3], s32 offen offset:1024 sc1 lds\n"
    "s_add_u32 m0, s37, 16384\n"
    "s_add_u32 s32, s32, 0x4000\n"
    "buffer_load_dwordx4 v64, s[0:3], s32 offen sc1 lds\n"
    "buffer_load_dwordx4 v64, s[0:3], s32 offen offset:1024 sc1 lds\n"
    "s_add_u32 m0, s37, 24576\n"
    "s_add_u32 s32, s32, 0x4000\n"
    "buffer_load_dwordx4 v64, s[0:3], s32 offen sc1 lds\n"
    "buffer_load_dwordx4 v64, s[0:3], s32 offen offset:1024 sc1 lds\n"
    "s_add_u32 m0, s37, 32768\n"
    "s_add_u32 s32, s32, 0x4000\n"
    "buffer_load_dwordx4 v64, s[0:3], s32 offen sc1 lds\n"
    "buffer_load_dwordx4 v64, s[0:3], s32 offen offset:1024 sc1 lds\n"
    "s_waitcnt vmcnt(0)\n"
    "v_mov_b32 v61, s44\n"
    "buffer_store_dword v61, v69, s[0:3], s43 offen sc1\n"
    "s_mov_b32 s34, 2130706432\n"
    "s_mov_b32 s46, s34\n"
    "s_mov_b32 s43, s41\n"
    "s_mov_b32 s44, s42\n"
    "s_mov_b32 s41, s39\n"
    "s_mov_b32 s42, s40\n"
    "s_mov_b32 s34, s33\n"
    "s_mov_b32 s35, s36\n"
    "s_xor_b32 s37, s37, 40960\n"
    "s_add_u32 s26, s26, 1\n"
    "s_sub_u32 s27, s26, 14\n"
    "s_ashr_i32 s28, s27, 1\n"
    "s_and_b32 s29, s27, 1\n"
    "s_add_u32 s30, s28, 14\n"
    "s_cmp_lt_i32 s27, 0\n"
    "s_cselect_b32 s29, 0, s29\n"
    "s_cselect_b32 s30, s26, s30\n"
    "s_cmp_eq_u32 s29, 1\n"
    "s_cselect_b32 s30, s28, s30\n"
    "s_cselect_b32 s27, s15, 64\n"
    "s_lshl_b32 s31, s29, 13\n"
    "s_add_u32 s40, s30, 1\n"
    "s_lshl_b32 s32, s30, 19\n"
    "s_lshl_b32 s25, s30, 11\n"
    "s_xor_b32 s25, s25, s31\n"
    "s_and_b32 s25, s25, 0x2000\n"
    "s_add_u32 s32, s32, s25\n"
    "s_add_u32 s32, s32, s13\n"
    "s_lshl_b32 s33, s30, 20\n"
    "s_lshl_b32 s25, s30, 12\n"
    "s_xor_b32 s25, s25, s31\n"
    "s_and_b32 s25, s25, 0x2000\n"
    "s_add_u32 s33, s33, s25\n"
    "s_add_u32 s33, s33, s14\n"
    "s_mul_i32 s47, s40, s29\n"
    "s_cmp_lt_u32 s30, s27\n"
    "s_cselect_b32 s36, 1, 0\n"
    "s_cselect_b32 s32, s32, 2130706432\n"
    "s_cmp_lt_u32 s30, s27\n"
    "s_cselect_b32 s39, s31, 2130706432\n"
    "s_cselect_b32 s47, s47, 0\n"
    "s_cmp_gt_u32 s47, 0\n"
    "s_cselect_b32 s38, 0, 2130706432\n"
    "buffer_load_dword v60, v68, s[0:3], s38 offen sc1\n"
    "s_branch .Lqw_join_%=\n"
    :: "{s[0:3]}"(s0_), "{s[4:7]}"(s4_), "{s[8:11]}"(s8_), "{s[12:15]}"(s12_), "{s[16:19]}"(s16_), "{v[0:3]}"(v0_), "{v[64:67]}"(v64_), "{v[68:71]}"(v68_) : "memory", "v255", "a255", "s95");
    __builtin_unreachable();
}
DEV void b1_role(const MegaArgs& A, const int L, const int i, const int rep) {
    ROLE_HEAD;
    const int qd = 4 * w + lq, sw = (l16 & 6) << 3, hw = w < 3 ? w : 2, hd = 3 * i + w;
    const bool live = w < 3 && hd < 32;
    const unsigned om = live ? RC_O - RC_X1 - w * 2048 + (hd >> 3) * 16384 + ((hd >> 1) & 3) * 2048 + (lq & 1) * 1024 + (2 * (hd & 1) + (lq >> 1)) * 256 + l16 * 16 : OOB;
    const unsigned ox = live && l < 16 ? RC_O - RC_X1 - w * 2048 + 65536 + hd * 256 + l16 * 16 : OOB;
    u64 wm[4];
#pragma unroll
    for (int e = 0; e < 4; ++e) { const int d = l16 + 16 - 16 * (e >> 1) - 4 * lq - 2 * (e & 1) - (l16 & 1); wm[e] = __ballot(d >= 0 && d <= 14); }
    const u32x4 s0_ = {RFL(rx[0]), RFL(rx[1]), RFL(rx[2]), RFL(rx[3])};
    const u32x4 s4_ = {RFL(lo(A.wqkv + 60 * i + 16 * w)), RFL(hi(A.wqkv + 60 * i + 16 * w)), RFL((640 * 1920 - 60 * i - 16 * w) * 2), RFL(0x20000u)};
    const u32x4 s8_ = {RFL((unsigned)wm[0]), RFL((unsigned)(wm[0] >> 32)), RFL((unsigned)wm[1]), RFL((unsigned)(wm[1] >> 32))};
    const u32x4 s12_ = {RFL((unsigned)wm[2]), RFL((unsigned)(wm[2] >> 32)), RFL((unsigned)wm[3]), RFL((unsigned)(wm[3] >> 32))};
    const u32x4 s16_ = {RFL(w == 3 ? 0xf000f000u : 0u), RFL(w == 3 ? 0xf000f000u : 0u), RFL(w), RFL(L << 13)};
    const u32x4 s20_ = {RFL(REC0 + RC_X1 + w * 2048), RFL(xl(Z_X1 + rep * 4096, L)), RFL(DBG0 + ((L * 128 + 96 + i) * 4 + w) * 2048), RFL(blockIdx.x == 0 ? 6 : 0)};
    const u32x4 v0_ = {(unsigned)((l >> 1) * 3840 + (l & 1) * 16 + 0), (unsigned)((l >> 1) * 3840 + (l & 1) * 16 + 256), (unsigned)((l >> 1) * 3840 + (l & 1) * 16 + 512), (unsigned)(w * 40960 + (8 * lq + (l16 >> 2)) * 32 + (l & 3) * 8)};
    const u32x4 v224_ = {(unsigned)(l * 16 + ((w + 0) & 3) * 2048), (unsigned)(l * 16 + ((w + 1) & 3) * 2048), (unsigned)(l * 16 + ((w + 2) & 3) * 2048), (unsigned)(l * 16 + ((w + 3) & 3) * 2048)};
    const u32x4 v228_ = {(unsigned)(l * 16), (unsigned)((l & 31) * 128), (unsigned)(l < 4 && w == 3 ? xl(Z_O + l * 2048 + i * 128, L) : OOB), (unsigned)(qd == 15 ? 86016 : 40960 + (qd / 5) * 2048 + l16 * 64 + (((qd % 5) * 8) ^ sw))};
    const u32x4 v232_ = {(unsigned)(qd == 15 ? 86016 : 49152 + (qd / 5) * 4096 + l16 * 64 + (((qd % 5) * 8) ^ sw)), (unsigned)(qd == 15 ? 86016 : 61440 + (qd / 5) * 4096 + l16 * 64 + (((qd % 5) * 8) ^ sw)), (unsigned)(40960 + hw * 2048 + l16 * 64 + ((lq * 16) ^ sw)), (unsigned)(49152 + hw * 4096 + l16 * 64 + ((lq * 16) ^ sw))};
    const u32x4 v236_ = {(unsigned)(51200 + hw * 4096 + l16 * 64 + ((lq * 16) ^ sw)), (unsigned)(61440 + hw * 4096 + (l >> 2) * 64 + ((0 + (l & 3) * 8) ^ (((l >> 2) & 6) << 3))), (unsigned)(63488 + hw * 4096 + (l >> 2) * 64 + ((0 + (l & 3) * 8) ^ (((l >> 2) & 6) << 3))), (unsigned)(61440 + hw * 4096 + (l >> 2) * 64 + ((32 + (l & 3) * 8) ^ (((l >> 2) & 6) << 3)))};
    const u32x4 v240_ = {(unsigned)(63488 + hw * 4096 + (l >> 2) * 64 + ((32 + (l & 3) * 8) ^ (((l >> 2) & 6) << 3))), (unsigned)(73728 + w * 2048 + l * 16), (unsigned)(73728 + (l >> 5) * 1024 + ((l16 >> 2) * 16 + l16) * 16 + (l & 3) * 4), (unsigned)(81920 + w * 512 + l * 4)};
    const u32x4 v244_ = {(unsigned)(81920 + 3 * 512 + (l >> 5) * 256 + (48 + l16) * 4), (unsigned)(om), (unsigned)(ox), (unsigned)((l & 1) ? 0x01000c0cu : 0x0c0c0100u)};
    const u32x4 v248_ = {(unsigned)((l & 1) ? 0x03020c0cu : 0x0c0c0302u), (unsigned)(0xff800000u), (unsigned)(0u), (unsigned)(0u)};
    asm volatile(
    "s_mov_b32 s24, 0\n"
    "s_mov_b64 s[26:27], 0\n"
    "s_mov_b32 s29, 0\n"
    "s_cmp_eq_u32 s19, 0\n"
    "s_cselect_b32 s28, 12, 50\n"
    "s_sub_u32 s28, s28, s23\n"
    ".Lb1_ho1_%=:\n"
    "s_sub_u32 s28, s28, 1\n"
    "s_cbranch_scc1 .Lb1_hd2_%=\n"
    "s_sleep 17\n"
    "s_branch .Lb1_ho1_%=\n"
    ".Lb1_hd2_%=:\n"
    "v_and_b32 v30, 0x70, v228\n"
    "v_lshlrev_b32 v30, 2, v30\n"
    "v_or_b32 v30, 0x8000, v30\n"
    "buffer_load_dword v31, v30, s[0:3], 0 offen sc1\n"
    "s_waitcnt vmcnt(0)\n"
    "v_cmp_gt_i32 vcc, 32, v31\n"
    "s_nop 0\n"
    "s_cbranch_vccnz .Lb1_tk0_%=\n"
    ".Lb1_tk1_%=:\n"
    "s_mul_i32 s32, s18, 40960\n"
    "s_add_u32 s35, s18, 0\n"
    "s_and_b32 s35, s35, 3\n"
    "s_add_u32 s35, s35, 0\n"
    "s_mul_i32 s35, s35, 122880\n"
    "s_add_u32 m0, s32, 0\n"
    "s_nop 0\n"
    "buffer_load_dwordx4 v0, s[4:7], s35 offen offset:0 lds\n"
    "buffer_load_dwordx4 v1, s[4:7], s35 offen offset:1024 lds\n"
    "buffer_load_dwordx4 v2, s[4:7], s35 offen offset:2048 lds\n"
    "s_add_u32 s35, s18, 1\n"
    "s_and_b32 s35, s35, 3\n"
    "s_add_u32 s35, s35, 0\n"
    "s_mul_i32 s35, s35, 122880\n"
    "s_add_u32 m0, s32, 3072\n"
    "s_nop 0\n"
    "buffer_load_dwordx4 v0, s[4:7], s35 offen offset:0 lds\n"
    "buffer_load_dwordx4 v1, s[4:7], s35 offen offset:1024 lds\n"
    "buffer_load_dwordx4 v2, s[4:7], s35 offen offset:2048 lds\n"
    "s_add_u32 s35, s18, 2\n"
    "s_and_b32 s35, s35, 3\n"
    "s_add_u32 s35, s35, 0\n"
    "s_mul_i32 s35, s35, 122880\n"
    "s_add_u32 m0, s32, 6144\n"
    "s_nop 0\n"
    "buffer_load_dwordx4 v0, s[4:7], s35 offen offset:0 lds\n"
    "buffer_load_dwordx4 v1, s[4:7], s35 offen offset:1024 lds\n"
    "buffer_load_dwordx4 v2, s[4:7], s35 offen offset:2048 lds\n"
    "s_add_u32 s35, s18, 3\n"
    "s_and_b32 s35, s35, 3\n"
    "s_add_u32 s35, s35, 0\n"
    "s_mul_i32 s35, s35, 122880\n"
    "s_add_u32 m0, s32, 9216\n"
    "s_nop 0\n"
    "buffer_load_dwordx4 v0, s[4:7], s35 offen offset:0 lds\n"
    "buffer_load_dwordx4 v1, s[4:7], s35 offen offset:1024 lds\n"
    "buffer_load_dwordx4 v2, s[4:7], s35 offen offset:2048 lds\n"
    "s_add_u32 s35, s18, 0\n"
    "s_and_b32 s35, s35, 3\n"
    "s_add_u32 s35, s35, 4\n"
    "s_mul_i32 s35, s35, 122880\n"
    "s_add_u32 m0, s32, 12288\n"
    "s_nop 0\n"
    "buffer_load_dwordx4 v0, s[4:7], s35 offen offset:0 lds\n"
    "buffer_load_dwordx4 v1, s[4:7], s35 offen offset:1024 lds\n"
    "buffer_load_dwordx4 v2, s[4:7], s35 offen offset:2048 lds\n"
    "s_add_u32 s35, s18, 1\n"
    "s_and_b32 s35, s35, 3\n"
    "s_add_u32 s35, s35, 4\n"
    "s_mul_i32 s35, s35, 122880\n"
    "s_add_u32 m0, s32, 15360\n"
    "s_nop 0\n"
    "buffer_load_dwordx4 v0, s[4:7], s35 offen offset:0 lds\n"
    "buffer_load_dwordx4 v1, s[4:7], s35 offen offset:1024 lds\n"
    "buffer_load_dwordx4 v2, s[4:7], s35 offen offset:2048 lds\n"
    "s_add_u32 s35, s18, 2\n"
    "s_and_b32 s35, s35, 3\n"
    "s_add_u32 s35, s35, 4\n"
    "s_mul_i32 s35, s35, 122880\n"
    "s_add_u32 m0, s32, 18432\n"
    "s_nop 0\n"
    "buffer_load_dwordx4 v0, s[4:7], s35 offen offset:0 lds\n"
    "buffer_load_dwordx4 v1, s[4:7], s35 offen offset:1024 lds\n"
    "buffer_load_dwordx4 v2, s[4:7], s35 offen offset:2048 lds\n"
    "s_add_u32 s35, s18, 3\n"
    "s_and_b32 s35, s35, 3\n"
    "s_add_u32 s35, s35, 4\n"
    "s_mul_i32 s35, s35, 122880\n"
    "s_add_u32 m0, s32, 21504\n"
    "s_nop 0\n"
    "buffer_load_dwordx4 v0, s[4:7], s35 offen offset:0 lds\n"
    "buffer_load_dwordx4 v1, s[4:7], s35 offen offset:1024 lds\n"
    "buffer_load_dwordx4 v2, s[4:7], s35 offen offset:2048 lds\n"
    "s_add_u32 s35, s18, 0\n"
    "s_and_b32 s35, s35, 3\n"
    "s_add_u32 s35, s35, 8\n"
    "s_mul_i32 s35, s35, 122880\n"
    "s_add_u32 m0, s32, 24576\n"
    "s_nop 0\n"
    "buffer_load_dwordx4 v0, s[4:7], s35 offen offset:0 lds\n"
    "buffer_load_dwordx4 v1, s[4:7], s35 offen offset:1024 lds\n"
    "buffer_load_dwordx4 v2, s[4:7], s35 offen offset:2048 lds\n"
    "s_add_u32 s35, s18, 1\n"
    "s_and_b32 s35, s35, 3\n"
    "s_add_u32 s35, s35, 8\n"
    "s_mul_i32 s35, s35, 122880\n"
    "s_add_u32 m0, s32, 27648\n"
    "s_nop 0\n"
    "buffer_load_dwordx4 v0, s[4:7], s35 offen offset:0 lds\n"
    "buffer_load_dwordx4 v1, s[4:7], s35 offen offset:1024 lds\n"
    "buffer_load_dwordx4 v2, s[4:7], s35 offen offset:2048 lds\n"
    "s_add_u32 s35, s18, 2\n"
    "s_and_b32 s35, s35, 3\n"
    "s_add_u32 s35, s35, 8\n"
    "s_mul_i32 s35, s35, 122880\n"
    "s_add_u32 m0, s32, 30720\n"
    "s_nop 0\n"
    "buffer_load_dwordx4 v0, s[4:7], s35 offen offset:0 lds\n"
    "buffer_load_dwordx4 v1, s[4:7], s35 offen offset:1024 lds\n"
    "buffer_load_dwordx4 v2, s[4:7], s35 offen offset:2048 lds\n"
    "s_add_u32 s35, s18, 3\n"
    "s_and_b32 s35, s35, 3\n"
    "s_add_u32 s35, s35, 8\n"
    "s_mul_i32 s35, s35, 122880\n"
    "s_add_u32 m0, s32, 33792\n"
    "s_nop 0\n"
    "buffer_load_dwordx4 v0, s[4:7], s35 offen offset:0 lds\n"
    "buffer_load_dwordx4 v1, s[4:7], s35 offen offset:1024 lds\n"
    "buffer_load_dwordx4 v2, s[4:7], s35 offen offset:2048 lds\n"
    "s_add_u32 s35, s18, 0\n"
    "s_and_b32 s35, s35, 3\n"
    "s_add_u32 s35, s35, 12\n"
    "s_mul_i32 s35, s35, 122880\n"
    "s_add_u32 m0, s32, 36864\n"
    "s_nop 0\n"
    "buffer_load_dwordx4 v0, s[4:7], s35 offen offset:0 lds\n"
    "buffer_load_dwordx4 v1, s[4:7], s35 offen offset:1024 lds\n"
    "buffer_load_dwordx4 v2, s[4:7], s35 offen offset:2048 lds\n"
    "s_waitcnt vmcnt(36)\n"
    "ds_read_b64_tr_b16 a[0:1], v3 offset:0\n"
    "ds_read_b64_tr_b16 a[2:3], v3 offset:128\n"
    "ds_read_b64_tr_b16 a[80:81], v3 offset:1024\n"
    "ds_read_b64_tr_b16 a[82:83], v3 offset:1152\n"
    "ds_read_b64_tr_b16 a[160:161], v3 offset:2048\n"
    "ds_read_b64_tr_b16 a[162:163], v3 offset:2176\n"
    "s_waitcnt lgkmcnt(0)\n"
    "s_add_u32 s35, s18, 1\n"
    "s_and_b32 s35, s35, 3\n"
    "s_add_u32 s35, s35, 12\n"
    "s_mul_i32 s35, s35, 122880\n"
    "s_add_u32 m0, s32, 0\n"
    "s_nop 0\n"
    "buffer_load_dwordx4 v0, s[4:7], s35 offen offset:0 lds\n"
    "buffer_load_dwordx4 v1, s[4:7], s35 offen offset:1024 lds\n"
    "buffer_load_dwordx4 v2, s[4:7], s35 offen offset:2048 lds\n"
    "s_waitcnt vmcnt(36)\n"
    "ds_read_b64_tr_b16 a[4:5], v3 offset:3072\n"
    "ds_read_b64_tr_b16 a[6:7], v3 offset:3200\n"
    "ds_read_b64_tr_b16 a[84:85], v3 offset:4096\n"
    "ds_read_b64_tr_b16 a[86:87], v3 offset:4224\n"
    "ds_read_b64_tr_b16 a[164:165], v3 offset:5120\n"
    "ds_read_b64_tr_b16 a[166:167], v3 offset:5248\n"
    "s_waitcnt lgkmcnt(0)\n"
    "s_add_u32 s35, s18, 2\n"
    "s_and_b32 s35, s35, 3\n"
    "s_add_u32 s35, s35, 12\n"
    "s_mul_i32 s35, s35, 122880\n"
    "s_add_u32 m0, s32, 3072\n"
    "s_nop 0\n"
    "buffer_load_dwordx4 v0, s[4:7], s35 offen offset:0 lds\n"
    "buffer_load_dwordx4 v1, s[4:7], s35 offen offset:1024 lds\n"
    "buffer_load_dwordx4 v2, s[4:7], s35 offen offset:2048 lds\n"
    "s_waitcnt vmcnt(36)\n"
    "ds_read_b64_tr_b16 a[8:9], v3 offset:6144\n"
    "ds_read_b64_tr_b16 a[10:11], v3 offset:6272\n"
    "ds_read_b64_tr_b16 a[88:89], v3 offset:7168\n"
    "ds_read_b64_tr_b16 a[90:91], v3 offset:7296\n"
    "ds_read_b64_tr_b16 a[168:169], v3 offset:8192\n"
    "ds_read_b64_tr_b16 a[170:171], v3 offset:8320\n"
    "s_waitcnt lgkmcnt(0)\n"
    "s_add_u32 s35, s18, 3\n"
    "s_and_b32 s35, s35, 3\n"
    "s_add_u32 s35, s35, 12\n"
    "s_mul_i32 s35, s35, 122880\n"
    "s_add_u32 m0, s32, 6144\n"
    "s_nop 0\n"
    "buffer_load_dwordx4 v0, s[4:7], s35 offen offset:0 lds\n"
    "buffer_load_dwordx4 v1, s[4:7], s35 offen offset:1024 lds\n"
    "buffer_load_dwordx4 v2, s[4:7], s35 offen offset:2048 lds\n"
    "s_waitcnt vmcnt(36)\n"
    "ds_read_b64_tr_b16 a[12:13], v3 offset:9216\n"
    "ds_read_b64_tr_b16 a[14:15], v3 offset:9344\n"
    "ds_read_b64_tr_b16 a[92:93], v3 offset:10240\n"
    "ds_read_b64_tr_b16 a[94:95], v3 offset:10368\n"
    "ds_read_b64_tr_b16 a[172:173], v3 offset:11264\n"
    "ds_read_b64_tr_b16 a[174:175], v3 offset:11392\n"
    "s_waitcnt lgkmcnt(0)\n"
    "s_add_u32 s35, s18, 0\n"
    "s_and_b32 s35, s35, 3\n"
    "s_add_u32 s35, s35, 16\n"
    "s_mul_i32 s35, s35, 122880\n"
    "s_add_u32 m0, s32, 9216\n"
    "s_nop 0\n"
    "buffer_load_dwordx4 v0, s[4:7], s35 offen offset:0 lds\n"
    "buffer_load_dwordx4 v1, s[4:7], s35 offen offset:1024 lds\n"
    "buffer_load_dwordx4 v2, s[4:7], s35 offen offset:2048 lds\n"
    "s_waitcnt vmcnt(36)\n"
    "ds_read_b64_tr_b16 a[16:17], v3 offset:12288\n"
    "ds_read_b64_tr_b16 a[18:19], v3 offset:12416\n"
    "ds_read_b64_tr_b16 a[96:97], v3 offset:13312\n"
    "ds_read_b64_tr_b16 a[98:99], v3 offset:13440\n"
    "ds_read_b64_tr_b16 a[176:177], v3 offset:14336\n"
    "ds_read_b64_tr_b16 a[178:179], v3 offset:14464\n"
    "s_waitcnt lgkmcnt(0)\n"
    "s_add_u32 s35, s18, 1\n"
    "s_and_b32 s35, s35, 3\n"
    "s_add_u32 s35, s35, 16\n"
    "s_mul_i32 s35, s35, 122880\n"
    "s_add_u32 m0, s32, 12288\n"
    "s_nop 0\n"
    "buffer_load_dwordx4 v0, s[4:7], s35 offen offset:0 lds\n"
    "buffer_load_dwordx4 v1, s[4:7], s35 offen offset:1024 lds\n"
    "buffer_load_dwordx4 v2, s[4:7], s35 offen offset:2048 lds\n"
    "s_waitcnt vmcnt(36)\n"
    "ds_read_b64_tr_b16 a[20:21], v3 offset:15360\n"
    "ds_read_b64_tr_b16 a[22:23], v3 offset:15488\n"
    "ds_read_b64_tr_b16 a[100:101], v3 offset:16384\n"
    "ds_read_b64_tr_b16 a[102:103], v3 offset:16512\n"
    "ds_read_b64_tr_b16 a[180:181], v3 offset:17408\n"
    "ds_read_b64_tr_b16 a[182:183], v3 offset:17536\n"
    "s_waitcnt lgkmcnt(0)\n"
    "s_add_u32 s35, s18, 2\n"
    "s_and_b32 s35, s35, 3\n"
    "s_add_u32 s35, s35, 16\n"
    "s_mul_i32 s35, s35, 122880\n"
    "s_add_u32 m0, s32, 15360\n"
    "s_nop 0\n"
    "buffer_load_dwordx4 v0, s[4:7], s35 offen offset:0 lds\n"
    "buffer_load_dwordx4 v1, s[4:7], s35 offen offset:1024 lds\n"
    "buffer_load_dwordx4 v2, s[4:7], s35 offen offset:2048 lds\n"
    "s_waitcnt vmcnt(36)\n"
    "ds_read_b64_tr_b16 a[24:25], v3 offset:18432\n"
    "ds_read_b64_tr_b16 a[26:27], v3 offset:18560\n"
    "ds_read_b64_tr_b16 a[104:105], v3 offset:19456\n"
    "ds_read_b64_tr_b16 a[106:107], v3 offset:19584\n"
    "ds_read_b64_tr_b16 a[184:185], v3 offset:20480\n"
    "ds_read_b64_tr_b16 a[186:187], v3 offset:20608\n"
    "s_waitcnt lgkmcnt(0)\n"
    "s_add_u32 s35, s18, 3\n"
    "s_and_b32 s35, s35, 3\n"
    "s_add_u32 s35, s35, 16\n"
    "s_mul_i32 s35, s35, 122880\n"
    "s_add_u32 m0, s32, 18432\n"
    "s_nop 0\n"
    "buffer_load_dwordx4 v0, s[4:7], s35 offen offset:0 lds\n"
    "buffer_load_dwordx4 v1, s[4:7], s35 offen offset:1024 lds\n"
    "buffer_load_dwordx4 v2, s[4:7], s35 offen offset:2048 lds\n"
    "s_waitcnt vmcnt(36)\n"
    "ds_read_b64_tr_b16 a[28:29], v3 offset:21504\n"
    "ds_read_b64_tr_b16 a[30:31], v3 offset:21632\n"
    "ds_read_b64_tr_b16 a[108:109], v3 offset:22528\n"
    "ds_read_b64_tr_b16 a[110:111], v3 offset:22656\n"
    "ds_read_b64_tr_b16 a[188:189], v3 offset:23552\n"
    "ds_read_b64_tr_b16 a[190:191], v3 offset:23680\n"
    "s_waitcnt lgkmcnt(0)\n"
    "s_waitcnt vmcnt(33)\n"
    "ds_read_b64_tr_b16 a[32:33], v3 offset:24576\n"
    "ds_read_b64_tr_b16 a[34:35], v3 offset:24704\n"
    "ds_read_b64_tr_b16 a[112:113], v3 offset:25600\n"
    "ds_read_b64_tr_b16 a[114:115], v3 offset:25728\n"
    "ds_read_b64_tr_b16 a[192:193], v3 offset:26624\n"
    "ds_read_b64_tr_b16 a[194:195], v3 offset:26752\n"
    "s_waitcnt lgkmcnt(0)\n"
    "s_waitcnt vmcnt(30)\n"
    "ds_read_b64_tr_b16 a[36:37], v3 offset:27648\n"
    "ds_read_b64_tr_b16 a[38:39], v3 offset:27776\n"
    "ds_read_b64_tr_b16 a[116:117], v3 offset:28672\n"
    "ds_read_b64_tr_b16 a[118:119], v3 offset:28800\n"
    "ds_read_b64_tr_b16 a[196:197], v3 offset:29696\n"
    "ds_read_b64_tr_b16 a[198:199], v3 offset:29824\n"
    "s_waitcnt lgkmcnt(0)\n"
    "s_waitcnt vmcnt(27)\n"
    "ds_read_b64_tr_b16 a[40:41], v3 offset:30720\n"
    "ds_read_b64_tr_b16 a[42:43], v3 offset:30848\n"
    "ds_read_b64_tr_b16 a[120:121], v3 offset:31744\n"
    "ds_read_b64_tr_b16 a[122:123], v3 offset:31872\n"
    "ds_read_b64_tr_b16 a[200:201], v3 offset:32768\n"
    "ds_read_b64_tr_b16 a[202:203], v3 offset:32896\n"
    "s_waitcnt lgkmcnt(0)\n"
    "s_waitcnt vmcnt(24)\n"
    "ds_read_b64_tr_b16 a[44:45], v3 offset:33792\n"
    "ds_read_b64_tr_b16 a[46:47], v3 offset:33920\n"
    "ds_read_b64_tr_b16 a[124:125], v3 offset:34816\n"
    "ds_read_b64_tr_b16 a[126:127], v3 offset:34944\n"
    "ds_read_b64_tr_b16 a[204:205], v3 offset:35840\n"
    "ds_read_b64_tr_b16 a[206:207], v3 offset:35968\n"
    "s_waitcnt lgkmcnt(0)\n"
    "s_waitcnt vmcnt(21)\n"
    "ds_read_b64_tr_b16 a[48:49], v3 offset:36864\n"
    "ds_read_b64_tr_b16 a[50:51], v3 offset:36992\n"
    "ds_read_b64_tr_b16 a[128:129], v3 offset:37888\n"
    "ds_read_b64_tr_b16 a[130:131], v3 offset:38016\n"
    "ds_read_b64_tr_b16 a[208:209], v3 offset:38912\n"
    "ds_read_b64_tr_b16 a[210:211], v3 offset:39040\n"
    "s_waitcnt lgkmcnt(0)\n"
    "s_waitcnt vmcnt(18)\n"
    "ds_read_b64_tr_b16 a[52:53], v3 offset:0\n"
    "ds_read_b64_tr_b16 a[54:55], v3 offset:128\n"
    "ds_read_b64_tr_b16 a[132:133], v3 offset:1024\n"
    "ds_read_b64_tr_b16 a[134:135], v3 offset:1152\n"
    "ds_read_b64_tr_b16 a[212:213], v3 offset:2048\n"
    "ds_read_b64_tr_b16 a[214:215], v3 offset:2176\n"
    "s_waitcnt lgkmcnt(0)\n"
    "s_waitcnt vmcnt(15)\n"
    "ds_read_b64_tr_b16 a[56:57], v3 offset:3072\n"
    "ds_read_b64_tr_b16 a[58:59], v3 offset:3200\n"
    "ds_read_b64_tr_b16 a[136:137], v3 offset:4096\n"
    "ds_read_b64_tr_b16 a[138:139], v3 offset:4224\n"
    "ds_read_b64_tr_b16 a[216:217], v3 offset:5120\n"
    "ds_read_b64_tr_b16 a[218:219], v3 offset:5248\n"
    "s_waitcnt lgkmcnt(0)\n"
    "s_waitcnt vmcnt(12)\n"
    "ds_read_b64_tr_b16 a[60:61], v3 offset:6144\n"
    "ds_read_b64_tr_b16 a[62:63], v3 offset:6272\n"
    "ds_read_b64_tr_b16 a[140:141], v3 offset:7168\n"
    "ds_read_b64_tr_b16 a[142:143], v3 offset:7296\n"
    "ds_read_b64_tr_b16 a[220:221], v3 offset:8192\n"
    "ds_read_b64_tr_b16 a[222:223], v3 offset:8320\n"
    "s_waitcnt lgkmcnt(0)\n"
    "s_waitcnt vmcnt(9)\n"
    "ds_read_b64_tr_b16 a[64:65], v3 offset:9216\n"
    "ds_read_b64_tr_b16 a[66:67], v3 offset:9344\n"
    "ds_read_b64_tr_b16 a[144:145], v3 offset:10240\n"
    "ds_read_b64_tr_b16 a[146:147], v3 offset:10368\n"
    "ds_read_b64_tr_b16 a[224:225], v3 offset:11264\n"
    "ds_read_b64_tr_b16 a[226:227], v3 offset:11392\n"
    "s_waitcnt lgkmcnt(0)\n"
    "s_waitcnt vmcnt(6)\n"
    "ds_read_b64_tr_b16 a[68:69], v3 offset:12288\n"
    "ds_read_b64_tr_b16 a[70:71], v3 offset:12416\n"
    "ds_read_b64_tr_b16 a[148:149], v3 offset:13312\n"
    "ds_read_b64_tr_b16 a[150:151], v3 offset:13440\n"
    "ds_read_b64_tr_b16 a[228:229], v3 offset:14336\n"
    "ds_read_b64_tr_b16 a[230:231], v3 offset:14464\n"
    "s_waitcnt lgkmcnt(0)\n"
    "s_waitcnt vmcnt(3)\n"
    "ds_read_b64_tr_b16 a[72:73], v3 offset:15360\n"
    "ds_read_b64_tr_b16 a[74:75], v3 offset:15488\n"
    "ds_read_b64_tr_b16 a[152:153], v3 offset:16384\n"
    "ds_read_b64_tr_b16 a[154:155], v3 offset:16512\n"
    "ds_read_b64_tr_b16 a[232:233], v3 offset:17408\n"
    "ds_read_b64_tr_b16 a[234:235], v3 offset:17536\n"
    "s_waitcnt lgkmcnt(0)\n"
    "s_waitcnt vmcnt(0)\n"
    "ds_read_b64_tr_b16 a[76:77], v3 offset:18432\n"
    "ds_read_b64_tr_b16 a[78:79], v3 offset:18560\n"
    "ds_read_b64_tr_b16 a[156:157], v3 offset:19456\n"
    "ds_read_b64_tr_b16 a[158:159], v3 offset:19584\n"
    "ds_read_b64_tr_b16 a[236:237], v3 offset:20480\n"
    "ds_read_b64_tr_b16 a[238:239], v3 offset:20608\n"
    "s_waitcnt lgkmcnt(0)\n"
    "s_waitcnt lgkmcnt(0)\n"
    "s_barrier\n"
    "v_mov_b32 v32, 0x3f803f80\n"
    "v_mov_b32 v36, 0\n"
    "v_mov_b32 v33, 0x3f803f80\n"
    "v_mov_b32 v37, 0\n"
    "v_mov_b32 v34, 0x3f803f80\n"
    "v_mov_b32 v38, 0\n"
    "v_mov_b32 v35, 0x3f803f80\n"
    "v_mov_b32 v39, 0\n"
    "s_nop 0\n"
    "v_mfma_f32_16x16x32_bf16 v[96:99], a[0:3], v[32:35], 0\n"
    "v_mfma_f32_16x16x32_bf16 v[100:103], a[80:83], v[32:35], 0\n"
    "v_mfma_f32_16x16x32_bf16 v[104:107], a[160:163], v[32:35], 0\n"
    "v_mfma_f32_16x16x32_bf16 v[96:99], a[4:7], v[32:35], v[96:99]\n"
    "v_mfma_f32_16x16x32_bf16 v[100:103], a[84:87], v[32:35], v[100:103]\n"
    "v_mfma_f32_16x16x32_bf16 v[104:107], a[164:167], v[32:35], v[104:107]\n"
    "v_mfma_f32_16x16x32_bf16 v[96:99], a[8:11], v[32:35], v[96:99]\n"
    "v_mfma_f32_16x16x32_bf16 v[100:103], a[88:91], v[32:35], v[100:103]\n"
    "v_mfma_f32_16x16x32_bf16 v[104:107], a[168:171], v[32:35], v[104:107]\n"
    "v_mfma_f32_16x16x32_bf16 v[96:99], a[12:15], v[32:35], v[96:99]\n"
    "v_mfma_f32_16x16x32_bf16 v[100:103], a[92:95], v[32:35], v[100:103]\n"
    "v_mfma_f32_16x16x32_bf16 v[104:107], a[172:175], v[32:35], v[104:107]\n"
    "v_mfma_f32_16x16x32_bf16 v[96:99], a[16:19], v[32:35], v[96:99]\n"
    "v_mfma_f32_16x16x32_bf16 v[100:103], a[96:99], v[32:35], v[100:103]\n"
    "v_mfma_f32_16x16x32_bf16 v[104:107], a[176:179], v[32:35], v[104:107]\n"
    "v_mfma_f32_16x16x32_bf16 v[96:99], a[20:23], v[32:35], v[96:99]\n"
    "v_mfma_f32_16x16x32_bf16 v[100:103], a[100:103], v[32:35], v[100:103]\n"
    "v_mfma_f32_16x16x32_bf16 v[104:107], a[180:183], v[32:35], v[104:107]\n"
    "v_mfma_f32_16x16x32_bf16 v[96:99], a[24:27], v[32:35], v[96:99]\n"
    "v_mfma_f32_16x16x32_bf16 v[100:103], a[104:107], v[32:35], v[100:103]\n"
    "v_mfma_f32_16x16x32_bf16 v[104:107], a[184:187], v[32:35], v[104:107]\n"
    "v_mfma_f32_16x16x32_bf16 v[96:99], a[28:31], v[32:35], v[96:99]\n"
    "v_mfma_f32_16x16x32_bf16 v[100:103], a[108:111], v[32:35], v[100:103]\n"
    "v_mfma_f32_16x16x32_bf16 v[104:107], a[188:191], v[32:35], v[104:107]\n"
    "v_mfma_f32_16x16x32_bf16 v[96:99], a[32:35], v[32:35], v[96:99]\n"
    "v_mfma_f32_16x16x32_bf16 v[100:103], a[112:115], v[32:35], v[100:103]\n"
    "v_mfma_f32_16x16x32_bf16 v[104:107], a[192:195], v[32:35], v[104:107]\n"
    "v_mfma_f32_16x16x32_bf16 v[96:99], a[36:39], v[32:35], v[96:99]\n"
    "v_mfma_f32_16x16x32_bf16 v[100:103], a[116:119], v[32:35], v[100:103]\n"
    "v_mfma_f32_16x16x32_bf16 v[104:107], a[196:199], v[32:35], v[104:107]\n"
    "v_mfma_f32_16x16x32_bf16 v[96:99], a[40:43], v[32:35], v[96:99]\n"
    "v_mfma_f32_16x16x32_bf16 v[100:103], a[120:123], v[32:35], v[100:103]\n"
    "v_mfma_f32_16x16x32_bf16 v[104:107], a[200:203], v[32:35], v[104:107]\n"
    "v_mfma_f32_16x16x32_bf16 v[96:99], a[44:47], v[32:35], v[96:99]\n"
    "v_mfma_f32_16x16x32_bf16 v[100:103], a[124:127], v[32:35], v[100:103]\n"
    "v_mfma_f32_16x16x32_bf16 v[104:107], a[204:207], v[32:35], v[104:107]\n"
    "v_mfma_f32_16x16x32_bf16 v[96:99], a[48:51], v[32:35], v[96:99]\n"
    "v_mfma_f32_16x16x32_bf16 v[100:103], a[128:131], v[32:35], v[100:103]\n"
    "v_mfma_f32_16x16x32_bf16 v[104:107], a[208:211], v[32:35], v[104:107]\n"
    "v_mfma_f32_16x16x32_bf16 v[96:99], a[52:55], v[32:35], v[96:99]\n"
    "v_mfma_f32_16x16x32_bf16 v[100:103], a[132:135], v[32:35], v[100:103]\n"
    "v_mfma_f32_16x16x32_bf16 v[104:107], a[212:215], v[32:35], v[104:107]\n"
    "v_mfma_f32_16x16x32_bf16 v[96:99], a[56:59], v[32:35], v[96:99]\n"
    "v_mfma_f32_16x16x32_bf16 v[100:103], a[136:139], v[32:35], v[100:103]\n"
    "v_mfma_f32_16x16x32_bf16 v[104:107], a[216:219], v[32:35], v[104:107]\n"
    "v_mfma_f32_16x16x32_bf16 v[96:99], a[60:63], v[32:35], v[96:99]\n"
    "v_mfma_f32_16x16x32_bf16 v[100:103], a[140:143], v[32:35], v[100:103]\n"
    "v_mfma_f32_16x16x32_bf16 v[104:107], a[220:223], v[32:35], v[104:107]\n"
    "v_mfma_f32_16x16x32_bf16 v[96:99], a[64:67], v[32:35], v[96:99]\n"
    "v_mfma_f32_16x16x32_bf16 v[100:103], a[144:147], v[32:35], v[100:103]\n"
    "v_mfma_f32_16x16x32_bf16 v[104:107], a[224:227], v[32:35], v[104:107]\n"
    "v_mfma_f32_16x16x32_bf16 v[96:99], a[68:71], v[32:35], v[96:99]\n"
    "v_mfma_f32_16x16x32_bf16 v[100:103], a[148:151], v[32:35], v[100:103]\n"
    "v_mfma_f32_16x16x32_bf16 v[104:107], a[228:231], v[32:35], v[104:107]\n"
    "v_mfma_f32_16x16x32_bf16 v[96:99], a[72:75], v[32:35], v[96:99]\n"
    "v_mfma_f32_16x16x32_bf16 v[100:103], a[152:155], v[32:35], v[100:103]\n"
    "v_mfma_f32_16x16x32_bf16 v[104:107], a[232:235], v[32:35], v[104:107]\n"
    "v_mfma_f32_16x16x32_bf16 v[96:99], a[76:79], v[32:35], v[96:99]\n"
    "v_mfma_f32_16x16x32_bf16 v[100:103], a[156:159], v[32:35], v[100:103]\n"
    "v_mfma_f32_16x16x32_bf16 v[104:107], a[236:239], v[32:35], v[104:107]\n"
    "s_lshl_b32 s35, s18, 13\n"
    "v_add_u32 v8, s35, v228\n"
    "ds_write_b128 v8, v[36:39] offset:40960\n"
    "ds_write_b128 v8, v[36:39] offset:41984\n"
    "ds_write_b128 v8, v[36:39] offset:43008\n"
    "ds_write_b128 v8, v[36:39] offset:44032\n"
    "ds_write_b128 v8, v[36:39] offset:45056\n"
    "ds_write_b128 v8, v[36:39] offset:46080\n"
    "ds_write_b128 v8, v[36:39] offset:47104\n"
    "ds_write_b128 v8, v[36:39] offset:48128\n"
    "s_nop 7\n"
    "s_nop 7\n"
    "s_mov_b64 exec, s[16:17]\n"
    "v_accvgpr_write_b32 a0, v32\n"
    "v_accvgpr_write_b32 a1, v32\n"
    "v_accvgpr_write_b32 a2, v32\n"
    "v_accvgpr_write_b32 a3, v32\n"
    "v_accvgpr_write_b32 a4, v32\n"
    "v_accvgpr_write_b32 a5, v32\n"
    "v_accvgpr_write_b32 a6, v32\n"
    "v_accvgpr_write_b32 a7, v32\n"
    "v_accvgpr_write_b32 a8, v32\n"
    "v_accvgpr_write_b32 a9, v32\n"
    "v_accvgpr_write_b32 a10, v32\n"
    "v_accvgpr_write_b32 a11, v32\n"
    "v_accvgpr_write_b32 a12, v32\n"
    "v_accvgpr_write_b32 a13, v32\n"
    "v_accvgpr_write_b32 a14, v32\n"
    "v_accvgpr_write_b32 a15, v32\n"
    "v_accvgpr_write_b32 a16, v32\n"
    "v_accvgpr_write_b32 a17, v32\n"
    "v_accvgpr_write_b32 a18, v32\n"
    "v_accvgpr_write_b32 a19, v32\n"
    "v_accvgpr_write_b32 a20, v32\n"
    "v_accvgpr_write_b32 a21, v32\n"
    "v_accvgpr_write_b32 a22, v32\n"
    "v_accvgpr_write_b32 a23, v32\n"
    "v_accvgpr_write_b32 a24, v32\n"
    "v_accvgpr_write_b32 a25, v32\n"
    "v_accvgpr_write_b32 a26, v32\n"
    "v_accvgpr_write_b32 a27, v32\n"
    "v_accvgpr_write_b32 a28, v32\n"
    "v_accvgpr_write_b32 a29, v32\n"
    "v_accvgpr_write_b32 a30, v32\n"
    "v_accvgpr_write_b32 a31, v32\n"
    "v_accvgpr_write_b32 a32, v32\n"
    "v_accvgpr_write_b32 a33, v32\n"
    "v_accvgpr_write_b32 a34, v32\n"
    "v_accvgpr_write_b32 a35, v32\n"
    "v_accvgpr_write_b32 a36, v32\n"
    "v_accvgpr_write_b32 a37, v32\n"
    "v_accvgpr_write_b32 a38, v32\n"
    "v_accvgpr_write_b32 a39, v32\n"
    "v_accvgpr_write_b32 a40, v32\n"
    "v_accvgpr_write_b32 a41, v32\n"
    "v_accvgpr_write_b32 a42, v32\n"
    "v_accvgpr_write_b32 a43, v32\n"
    "v_accvgpr_write_b32 a44, v32\n"
    "v_accvgpr_write_b32 a45, v32\n"
    "v_accvgpr_write_b32 a46, v32\n"
    "v_accvgpr_write_b32 a47, v32\n"
    "v_accvgpr_write_b32 a48, v32\n"
    "v_accvgpr_write_b32 a49, v32\n"
    "v_accvgpr_write_b32 a50, v32\n"
    "v_accvgpr_write_b32 a51, v32\n"
    "v_accvgpr_write_b32 a52, v32\n"
    "v_accvgpr_write_b32 a53, v32\n"
    "v_accvgpr_write_b32 a54, v32\n"
    "v_accvgpr_write_b32 a55, v32\n"
    "v_accvgpr_write_b32 a56, v32\n"
    "v_accvgpr_write_b32 a57, v32\n"
    "v_accvgpr_write_b32 a58, v32\n"
    "v_accvgpr_write_b32 a59, v32\n"
    "v_accvgpr_write_b32 a60, v32\n"
    "v_accvgpr_write_b32 a61, v32\n"
    "v_accvgpr_write_b32 a62, v32\n"
    "v_accvgpr_write_b32 a63, v32\n"
    "v_accvgpr_write_b32 a64, v32\n"
    "v_accvgpr_write_b32 a65, v32\n"
    "v_accvgpr_write_b32 a66, v32\n"
    "v_accvgpr_write_b32 a67, v32\n"
    "v_accvgpr_write_b32 a68, v32\n"
    "v_accvgpr_write_b32 a69, v32\n"
    "v_accvgpr_write_b32 a70, v32\n"
    "v_accvgpr_write_b32 a71, v32\n"
    "v_accvgpr_write_b32 a72, v32\n"
    "v_accvgpr_write_b32 a73, v32\n"
    "v_accvgpr_write_b32 a74, v32\n"
    "v_accvgpr_write_b32 a75, v32\n"
    "v_accvgpr_write_b32 a76, v32\n"
    "v_accvgpr_write_b32 a77, v32\n"
    "v_accvgpr_write_b32 a78, v32\n"
    "v_accvgpr_write_b32 a79, v32\n"
    "s_mov_b64 exec, -1\n"
    "s_mov_b32 s30, -2\n"
    "s_mov_b32 s30, 0\n"
    "s_mov_b32 s31, 1\n"
    "s_lshl_b32 s34, s18, 11\n"
    "s_add_u32 s32, s20, s19\n"
    "s_mov_b32 s37, 0x3ea52b5c\n"
    "s_mov_b64 s[38:39], 0\n"
    "s_mov_b64 s[40:41], 0\n"
    "s_mov_b32 s36, 0\n"
    "v_mov_b32 v108, 0\n"
    "s_mov_b32 s42, 0xaaaaaaaa\n"
    "s_mov_b32 s43, 0xaaaaaaaa\n"
    "s_waitcnt lgkmcnt(0)\n"
    ".Lb1_top_%=:\n"
    "s_cmp_lg_u32 s18, 0\n"
    "s_cbranch_scc1 .Lb1_pd7_%=\n"
    "s_add_u32 s29, s26, 240\n"
    "v_min3_i32 v108, v108, v109, v110\n"
    "v_min_i32 v108, v108, v111\n"
    "v_cmp_gt_i32 vcc, s31, v108\n"
    "s_nop 0\n"
    "s_cbranch_vccz .Lb1_pd7_%=\n"
    ".Lb1_rt8_%=:\n"
    "s_memrealtime s[26:27]\n"
    "s_waitcnt lgkmcnt(0)\n"
    "s_sub_u32 s28, s36, s26\n"
    "s_cmp_lt_u32 s28, 600\n"
    "s_cbranch_scc1 .Lb1_rt8_%=\n"
    ".Lb1_po6_%=:\n"
    "buffer_load_dwordx4 v[108:111], v229, s[0:3], s21 offen sc1\n"
    "s_memrealtime s[26:27]\n"
    "s_waitcnt vmcnt(0) lgkmcnt(0)\n"
    "s_add_u32 s29, s26, 240\n"
    "v_min3_i32 v108, v108, v109, v110\n"
    "v_min_i32 v108, v108, v111\n"
    "v_cmp_gt_i32 vcc, s31, v108\n"
    "s_nop 0\n"
    "s_cbranch_vccz .Lb1_pd7_%=\n"
    "s_add_u32 s24, s24, 1\n"
    "s_cmp_lt_u32 s24, 0x40000\n"
    "s_cbranch_scc1 .Lb1_sp9_%=\n"
    "v_mov_b32 v108, 60\n"
    "s_mov_b32 s28, 33728\n"
    "buffer_store_dword v108, off, s[0:3], s28 sc1\n"
    "s_waitcnt vmcnt(0)\n"
    "s_branch .Lb1_pd7_%=\n"
    ".Lb1_sp9_%=:\n"
    "s_sleep 6\n"
    "s_branch .Lb1_po6_%=\n"
    ".Lb1_pd7_%=:\n"
    "s_barrier\n"
    "s_cmp_eq_u32 s31, 63\n"
    "s_cselect_b32 s36, s26, s29\n"
    ".Lb1_mk0x9_%=:\n"
    "s_mov_b32 s33, s32\n"
    "s_add_u32 m0, s34, 0\n"
    "s_nop 0\n"
    "buffer_load_dwordx4 v228, s[0:3], s32 offen sc1 lds\n"
    "buffer_load_dwordx4 v228, s[0:3], s32 offen offset:1024 sc1 lds\n"
    "s_add_u32 m0, s34, 8192\n"
    "s_add_u32 s32, s32, 0x4000\n"
    "buffer_load_dwordx4 v228, s[0:3], s32 offen sc1 lds\n"
    "buffer_load_dwordx4 v228, s[0:3], s32 offen offset:1024 sc1 lds\n"
    "s_add_u32 m0, s34, 16384\n"
    "s_add_u32 s32, s32, 0x4000\n"
    "buffer_load_dwordx4 v228, s[0:3], s32 offen sc1 lds\n"
    "buffer_load_dwordx4 v228, s[0:3], s32 offen offset:1024 sc1 lds\n"
    "s_add_u32 m0, s34, 24576\n"
    "s_add_u32 s32, s32, 0x4000\n"
    "buffer_load_dwordx4 v228, s[0:3], s32 offen sc1 lds\n"
    "buffer_load_dwordx4 v228, s[0:3], s32 offen offset:1024 sc1 lds\n"
    "s_add_u32 m0, s34, 32768\n"
    "s_add_u32 s32, s32, 0x4000\n"
    "buffer_load_dwordx4 v228, s[0:3], s32 offen sc1 lds\n"
    "buffer_load_dwordx4 v228, s[0:3], s32 offen offset:1024 sc1 lds\n"
    "s_waitcnt vmcnt(8)\n"
    "s_barrier\n"
    ".Lb1_mk1x10_%=:\n"
    "ds_read_b128 v[32:35], v224 offset:0\n"
    "ds_read_b128 v[36:39], v224 offset:1024\n"
    "ds_read_b128 v[40:43], v225 offset:0\n"
    "ds_read_b128 v[44:47], v225 offset:1024\n"
    "s_waitcnt lgkmcnt(2)\n"
    "v_mfma_f32_16x16x32_bf16 v[24:27], v[32:35], v[32:35], 0\n"
    "ds_read_b128 v[48:51], v226 offset:0\n"
    "ds_read_b128 v[52:55], v226 offset:1024\n"
    "v_mfma_f32_16x16x32_bf16 v[28:31], v[36:39], v[36:39], 0\n"
    "v_mfma_f32_16x16x32_bf16 v[0:3], a[0:3], v[32:35], 0\n"
    "v_mfma_f32_16x16x32_bf16 v[4:7], a[0:3], v[36:39], 0\n"
    "v_mfma_f32_16x16x32_bf16 v[8:11], a[80:83], v[32:35], 0\n"
    "s_add_u32 s35, s30, 1\n"
    "s_lshl_b32 s32, s35, 19\n"
    "s_lshl_b32 s29, s35, 11\n"
    "v_mfma_f32_16x16x32_bf16 v[12:15], a[80:83], v[36:39], 0\n"
    "s_xor_b32 s29, s29, s19\n"
    "s_and_b32 s29, s29, 0x2000\n"
    "s_add_u32 s32, s32, s29\n"
    "v_mfma_f32_16x16x32_bf16 v[16:19], a[160:163], v[32:35], 0\n"
    "s_add_u32 s32, s32, s20\n"
    "v_xor_b32 v232, 0x800, v232\n"
    "v_xor_b32 v233, 0x800, v233\n"
    "v_mfma_f32_16x16x32_bf16 v[20:23], a[160:163], v[36:39], 0\n"
    "s_waitcnt lgkmcnt(2)\n"
    "v_xor_b32 v235, 0x800, v235\n"
    "v_xor_b32 v236, 0x800, v236\n"
    "v_mfma_f32_16x16x32_bf16 v[0:3], a[4:7], v[40:43], v[0:3]\n"
    "ds_read_b128 v[56:59], v227 offset:0\n"
    "ds_read_b128 v[60:63], v227 offset:1024\n"
    "v_mfma_f32_16x16x32_bf16 v[4:7], a[4:7], v[44:47], v[4:7]\n"
    "v_xor_b32 v237, 0x800, v237\n"
    "v_xor_b32 v238, 0x800, v238\n"
    "v_mfma_f32_16x16x32_bf16 v[8:11], a[84:87], v[40:43], v[8:11]\n"
    "v_xor_b32 v239, 0x800, v239\n"
    "v_xor_b32 v240, 0x800, v240\n"
    "v_mfma_f32_16x16x32_bf16 v[12:15], a[84:87], v[44:47], v[12:15]\n"
    "v_mov_b32 v116, s31\n"
    "v_mov_b32 v117, s31\n"
    "v_mfma_f32_16x16x32_bf16 v[16:19], a[164:167], v[40:43], v[16:19]\n"
    "v_mov_b32 v118, s31\n"
    "v_mov_b32 v119, s31\n"
    "v_mfma_f32_16x16x32_bf16 v[20:23], a[164:167], v[44:47], v[20:23]\n"
    "s_waitcnt lgkmcnt(2)\n"
    "v_mfma_f32_16x16x32_bf16 v[0:3], a[8:11], v[48:51], v[0:3]\n"
    "s_waitcnt vmcnt(6)\n"
    "s_barrier\n"
    "ds_read_b128 v[64:67], v224 offset:8192\n"
    "v_mfma_f32_16x16x32_bf16 v[4:7], a[8:11], v[52:55], v[4:7]\n"
    "ds_read_b128 v[68:71], v224 offset:9216\n"
    "v_mfma_f32_16x16x32_bf16 v[8:11], a[88:91], v[48:51], v[8:11]\n"
    "v_mfma_f32_16x16x32_bf16 v[12:15], a[88:91], v[52:55], v[12:15]\n"
    "v_mfma_f32_16x16x32_bf16 v[16:19], a[168:171], v[48:51], v[16:19]\n"
    "v_mfma_f32_16x16x32_bf16 v[20:23], a[168:171], v[52:55], v[20:23]\n"
    "s_waitcnt lgkmcnt(2)\n"
    "v_mfma_f32_16x16x32_bf16 v[0:3], a[12:15], v[56:59], v[0:3]\n"
    "ds_read_b128 v[72:75], v225 offset:8192\n"
    "ds_read_b128 v[76:79], v225 offset:9216\n"
    "v_mfma_f32_16x16x32_bf16 v[4:7], a[12:15], v[60:63], v[4:7]\n"
    "v_mfma_f32_16x16x32_bf16 v[8:11], a[92:95], v[56:59], v[8:11]\n"
    "v_mfma_f32_16x16x32_bf16 v[12:15], a[92:95], v[60:63], v[12:15]\n"
    "v_mfma_f32_16x16x32_bf16 v[16:19], a[172:175], v[56:59], v[16:19]\n"
    "v_mfma_f32_16x16x32_bf16 v[20:23], a[172:175], v[60:63], v[20:23]\n"
    "s_waitcnt lgkmcnt(2)\n"
    "v_mfma_f32_16x16x32_bf16 v[24:27], v[64:67], v[64:67], v[24:27]\n"
    "ds_read_b128 v[80:83], v226 offset:8192\n"
    "ds_read_b128 v[84:87], v226 offset:9216\n"
    "v_mfma_f32_16x16x32_bf16 v[28:31], v[68:71], v[68:71], v[28:31]\n"
    "v_mfma_f32_16x16x32_bf16 v[0:3], a[16:19], v[64:67], v[0:3]\n"
    "v_mfma_f32_16x16x32_bf16 v[4:7], a[16:19], v[68:71], v[4:7]\n"
    "v_mfma_f32_16x16x32_bf16 v[8:11], a[96:99], v[64:67], v[8:11]\n"
    "v_mfma_f32_16x16x32_bf16 v[12:15], a[96:99], v[68:71], v[12:15]\n"
    "v_mfma_f32_16x16x32_bf16 v[16:19], a[176:179], v[64:67], v[16:19]\n"
    "v_mfma_f32_16x16x32_bf16 v[20:23], a[176:179], v[68:71], v[20:23]\n"
    "s_waitcnt lgkmcnt(2)\n"
    "v_mfma_f32_16x16x32_bf16 v[0:3], a[20:23], v[72:75], v[0:3]\n"
    "ds_read_b128 v[88:91], v227 offset:8192\n"
    "ds_read_b128 v[92:95], v227 offset:9216\n"
    "v_mfma_f32_16x16x32_bf16 v[4:7], a[20:23], v[76:79], v[4:7]\n"
    "v_mfma_f32_16x16x32_bf16 v[8:11], a[100:103], v[72:75], v[8:11]\n"
    "v_mfma_f32_16x16x32_bf16 v[12:15], a[100:103], v[76:79], v[12:15]\n"
    "v_mfma_f32_16x16x32_bf16 v[16:19], a[180:183], v[72:75], v[16:19]\n"
    "v_mfma_f32_16x16x32_bf16 v[20:23], a[180:183], v[76:79], v[20:23]\n"
    "s_waitcnt lgkmcnt(2)\n"
    "v_mfma_f32_16x16x32_bf16 v[0:3], a[24:27], v[80:83], v[0:3]\n"
    "s_waitcnt vmcnt(4)\n"
    "s_barrier\n"
    "ds_read_b128 v[32:35], v224 offset:16384\n"
    "v_mfma_f32_16x16x32_bf16 v[4:7], a[24:27], v[84:87], v[4:7]\n"
    "ds_read_b128 v[36:39], v224 offset:17408\n"
    "v_mfma_f32_16x16x32_bf16 v[8:11], a[104:107], v[80:83], v[8:11]\n"
    "v_mfma_f32_16x16x32_bf16 v[12:15], a[104:107], v[84:87], v[12:15]\n"
    "v_mfma_f32_16x16x32_bf16 v[16:19], a[184:187], v[80:83], v[16:19]\n"
    "v_mfma_f32_16x16x32_bf16 v[20:23], a[184:187], v[84:87], v[20:23]\n"
    "s_waitcnt lgkmcnt(2)\n"
    "v_mfma_f32_16x16x32_bf16 v[0:3], a[28:31], v[88:91], v[0:3]\n"
    "ds_read_b128 v[40:43], v225 offset:16384\n"
    "ds_read_b128 v[44:47], v225 offset:17408\n"
    "v_mfma_f32_16x16x32_bf16 v[4:7], a[28:31], v[92:95], v[4:7]\n"
    "v_mfma_f32_16x16x32_bf16 v[8:11], a[108:111], v[88:91], v[8:11]\n"
    "v_mfma_f32_16x16x32_bf16 v[12:15], a[108:111], v[92:95], v[12:15]\n"
    "v_mfma_f32_16x16x32_bf16 v[16:19], a[188:191], v[88:91], v[16:19]\n"
    "v_mfma_f32_16x16x32_bf16 v[20:23], a[188:191], v[92:95], v[20:23]\n"
    "s_waitcnt lgkmcnt(2)\n"
    "v_mfma_f32_16x16x32_bf16 v[24:27], v[32:35], v[32:35], v[24:27]\n"
    "ds_read_b128 v[48:51], v226 offset:16384\n"
    "ds_read_b128 v[52:55], v226 offset:17408\n"
    "v_mfma_f32_16x16x32_bf16 v[28:31], v[36:39], v[36:39], v[28:31]\n"
    "v_mfma_f32_16x16x32_bf16 v[0:3], a[32:35], v[32:35], v[0:3]\n"
    "v_mfma_f32_16x16x32_bf16 v[4:7], a[32:35], v[36:39], v[4:7]\n"
    "v_mfma_f32_16x16x32_bf16 v[8:11], a[112:115], v[32:35], v[8:11]\n"
    "v_mfma_f32_16x16x32_bf16 v[12:15], a[112:115], v[36:39], v[12:15]\n"
    "v_mfma_f32_16x16x32_bf16 v[16:19], a[192:195], v[32:35], v[16:19]\n"
    "v_mfma_f32_16x16x32_bf16 v[20:23], a[192:195], v[36:39], v[20:23]\n"
    "s_waitcnt lgkmcnt(2)\n"
    "v_mfma_f32_16x16x32_bf16 v[0:3], a[36:39], v[40:43], v[0:3]\n"
    "ds_read_b128 v[56:59], v227 offset:16384\n"
    "ds_read_b128 v[60:63], v227 offset:17408\n"
    "v_mfma_f32_16x16x32_bf16 v[4:7], a[36:39], v[44:47], v[4:7]\n"
    "v_mfma_f32_16x16x32_bf16 v[8:11], a[116:119], v[40:43], v[8:11]\n"
    "v_mfma_f32_16x16x32_bf16 v[12:15], a[116:119], v[44:47], v[12:15]\n"
    "v_mfma_f32_16x16x32_bf16 v[16:19], a[196:199], v[40:43], v[16:19]\n"
    "v_mfma_f32_16x16x32_bf16 v[20:23], a[196:199], v[44:47], v[20:23]\n"
    "s_waitcnt lgkmcnt(2)\n"
    "v_mfma_f32_16x16x32_bf16 v[0:3], a[40:43], v[48:51], v[0:3]\n"
    "s_waitcnt vmcnt(2)\n"
    "s_barrier\n"
    "ds_read_b128 v[64:67], v224 offset:24576\n"
    "v_mfma_f32_16x16x32_bf16 v[4:7], a[40:43], v[52:55], v[4:7]\n"
    "ds_read_b128 v[68:71], v224 offset:25600\n"
    "ds_read_b128 v[72:75], v225 offset:24576\n"
    "v_mfma_f32_16x16x32_bf16 v[8:11], a[120:123], v[48:51], v[8:11]\n"
    "v_mfma_f32_16x16x32_bf16 v[12:15], a[120:123], v[52:55], v[12:15]\n"
    "ds_read_b128 v[76:79], v225 offset:25600\n"
    "v_mfma_f32_16x16x32_bf16 v[16:19], a[200:203], v[48:51], v[16:19]\n"
    "ds_read_b128 v[80:83], v226 offset:24576\n"
    "ds_read_b128 v[84:87], v226 offset:25600\n"
    "v_mfma_f32_16x16x32_bf16 v[20:23], a[200:203], v[52:55], v[20:23]\n"
    "s_waitcnt lgkmcnt(6)\n"
    "v_mfma_f32_16x16x32_bf16 v[0:3], a[44:47], v[56:59], v[0:3]\n"
    "ds_read_b128 v[88:91], v227 offset:24576\n"
    "v_mfma_f32_16x16x32_bf16 v[4:7], a[44:47], v[60:63], v[4:7]\n"
    "ds_read_b128 v[92:95], v227 offset:25600\n"
    "s_waitcnt vmcnt(0)\n"
    "v_mfma_f32_16x16x32_bf16 v[8:11], a[124:127], v[56:59], v[8:11]\n"
    "s_barrier\n"
    "ds_read_b128 v[32:35], v224 offset:32768\n"
    "v_mfma_f32_16x16x32_bf16 v[12:15], a[124:127], v[60:63], v[12:15]\n"
    "ds_read_b128 v[36:39], v224 offset:33792\n"
    "v_mfma_f32_16x16x32_bf16 v[16:19], a[204:207], v[56:59], v[16:19]\n"
    "ds_read_b128 v[40:43], v225 offset:32768\n"
    "v_mfma_f32_16x16x32_bf16 v[20:23], a[204:207], v[60:63], v[20:23]\n"
    "s_waitcnt lgkmcnt(9)\n"
    "ds_read_b128 v[44:47], v225 offset:33792\n"
    "v_mfma_f32_16x16x32_bf16 v[24:27], v[64:67], v[64:67], v[24:27]\n"
    "ds_read_b128 v[48:51], v226 offset:32768\n"
    "v_mfma_f32_16x16x32_bf16 v[28:31], v[68:71], v[68:71], v[28:31]\n"
    "ds_read_b128 v[52:55], v226 offset:33792\n"
    "v_mfma_f32_16x16x32_bf16 v[0:3], a[48:51], v[64:67], v[0:3]\n"
    "ds_read_b128 v[56:59], v227 offset:32768\n"
    "v_mfma_f32_16x16x32_bf16 v[4:7], a[48:51], v[68:71], v[4:7]\n"
    "s_waitcnt lgkmcnt(11)\n"
    "ds_read_b128 v[60:63], v227 offset:33792\n"
    "v_mfma_f32_16x16x32_bf16 v[0:3], a[52:55], v[72:75], v[0:3]\n"
    "v_mfma_f32_16x16x32_bf16 v[4:7], a[52:55], v[76:79], v[4:7]\n"
    "s_waitcnt lgkmcnt(10)\n"
    "v_mfma_f32_16x16x32_bf16 v[0:3], a[56:59], v[80:83], v[0:3]\n"
    "v_mfma_f32_16x16x32_bf16 v[4:7], a[56:59], v[84:87], v[4:7]\n"
    "s_waitcnt lgkmcnt(8)\n"
    "v_mfma_f32_16x16x32_bf16 v[0:3], a[60:63], v[88:91], v[0:3]\n"
    "v_mfma_f32_16x16x32_bf16 v[4:7], a[60:63], v[92:95], v[4:7]\n"
    "s_waitcnt lgkmcnt(6)\n"
    "v_mfma_f32_16x16x32_bf16 v[24:27], v[32:35], v[32:35], v[24:27]\n"
    "v_mfma_f32_16x16x32_bf16 v[28:31], v[36:39], v[36:39], v[28:31]\n"
    "v_mfma_f32_16x16x32_bf16 v[0:3], a[64:67], v[32:35], v[0:3]\n"
    "v_mfma_f32_16x16x32_bf16 v[4:7], a[64:67], v[36:39], v[4:7]\n"
    "s_waitcnt lgkmcnt(4)\n"
    "v_mfma_f32_16x16x32_bf16 v[0:3], a[68:71], v[40:43], v[0:3]\n"
    "v_mfma_f32_16x16x32_bf16 v[4:7], a[68:71], v[44:47], v[4:7]\n"
    "s_waitcnt lgkmcnt(2)\n"
    "v_mfma_f32_16x16x32_bf16 v[0:3], a[72:75], v[48:51], v[0:3]\n"
    "v_mfma_f32_16x16x32_bf16 v[4:7], a[72:75], v[52:55], v[4:7]\n"
    "s_waitcnt lgkmcnt(0)\n"
    "v_mfma_f32_16x16x32_bf16 v[0:3], a[76:79], v[56:59], v[0:3]\n"
    "v_mfma_f32_16x16x32_bf16 v[4:7], a[76:79], v[60:63], v[4:7]\n"
    "ds_write_b128 v241, v[24:27]\n"
    "v_mfma_f32_16x16x32_bf16 v[8:11], a[128:131], v[64:67], v[8:11]\n"
    "ds_write_b128 v241, v[28:31] offset:1024\n"
    "v_mfma_f32_16x16x32_bf16 v[12:15], a[128:131], v[68:71], v[12:15]\n"
    "ds_write2st64_b32 v243, v0, v4 offset1:1\n"
    "v_mfma_f32_16x16x32_bf16 v[8:11], a[132:135], v[72:75], v[8:11]\n"
    "s_waitcnt lgkmcnt(0)\n"
    "s_barrier\n"
    "ds_read2st64_b32 v[24:25], v242 offset1:8\n"
    "v_mfma_f32_16x16x32_bf16 v[12:15], a[132:135], v[76:79], v[12:15]\n"
    "ds_read2st64_b32 v[26:27], v242 offset0:16 offset1:24\n"
    "ds_read_b32 v108, v244\n"
    "v_mfma_f32_16x16x32_bf16 v[8:11], a[136:139], v[80:83], v[8:11]\n"
    "v_mfma_f32_16x16x32_bf16 v[12:15], a[136:139], v[84:87], v[12:15]\n"
    "v_mfma_f32_16x16x32_bf16 v[8:11], a[140:143], v[88:91], v[8:11]\n"
    "v_mfma_f32_16x16x32_bf16 v[12:15], a[140:143], v[92:95], v[12:15]\n"
    "v_mfma_f32_16x16x32_bf16 v[8:11], a[144:147], v[32:35], v[8:11]\n"
    "s_waitcnt lgkmcnt(0)\n"
    "v_add_f32 v24, v24, v26\n"
    "v_add_f32 v25, v25, v27\n"
    "v_mfma_f32_16x16x32_bf16 v[12:15], a[144:147], v[36:39], v[12:15]\n"
    "v_add_f32 v112, v24, v25\n"
    "v_mul_f32 v108, 0x3acccccd, v108\n"
    "v_mfma_f32_16x16x32_bf16 v[8:11], a[148:151], v[40:43], v[8:11]\n"
    "v_mul_f32 v112, 0x3acccccd, v112\n"
    "v_fma_f32 v112, -v108, v108, v112\n"
    "v_mfma_f32_16x16x32_bf16 v[12:15], a[148:151], v[44:47], v[12:15]\n"
    "v_max_f32 v112, 0, v112\n"
    "v_cvt_pk_bf16_f32 v108, v108, v112\n"
    "v_mfma_f32_16x16x32_bf16 v[8:11], a[152:155], v[48:51], v[8:11]\n"
    "v_and_b32 v112, 0xffff0000, v108\n"
    "v_lshlrev_b32 v108, 16, v108\n"
    "v_mfma_f32_16x16x32_bf16 v[12:15], a[152:155], v[52:55], v[12:15]\n"
    "v_add_f32 v112, 0x3727c5ac, v112\n"
    "v_mov_b32 v110, v108\n"
    "v_mfma_f32_16x16x32_bf16 v[8:11], a[156:159], v[56:59], v[8:11]\n"
    "v_cvt_pk_bf16_f32 v112, v112, v112\n"
    "v_lshlrev_b32 v112, 16, v112\n"
    "v_mfma_f32_16x16x32_bf16 v[12:15], a[156:159], v[60:63], v[12:15]\n"
    "v_sqrt_f32 v112, v112\n"
    "v_mfma_f32_16x16x32_bf16 v[16:19], a[208:211], v[64:67], v[16:19]\n"
    "v_permlane32_swap_b32 v108, v110\n"
    "v_mfma_f32_16x16x32_bf16 v[20:23], a[208:211], v[68:71], v[20:23]\n"
    "v_cvt_pk_bf16_f32 v112, v112, v112\n"
    "v_lshlrev_b32 v112, 16, v112\n"
    "v_mfma_f32_16x16x32_bf16 v[16:19], a[212:215], v[72:75], v[16:19]\n"
    "v_rcp_f32 v112, v112\n"
    "v_mfma_f32_16x16x32_bf16 v[20:23], a[212:215], v[76:79], v[20:23]\n"
    "v_mov_b32 v114, v112\n"
    "v_mfma_f32_16x16x32_bf16 v[16:19], a[216:219], v[80:83], v[16:19]\n"
    "s_nop 0\n"
    "v_permlane32_swap_b32 v112, v114\n"
    "v_mfma_f32_16x16x32_bf16 v[20:23], a[216:219], v[84:87], v[20:23]\n"
    "v_fma_f32 v0, v96, -v108, v0\n"
    "v_fma_f32 v1, v97, -v108, v1\n"
    "v_mfma_f32_16x16x32_bf16 v[16:19], a[220:223], v[88:91], v[16:19]\n"
    "v_fma_f32 v2, v98, -v108, v2\n"
    "v_fma_f32 v3, v99, -v108, v3\n"
    "v_mfma_f32_16x16x32_bf16 v[20:23], a[220:223], v[92:95], v[20:23]\n"
    "v_fma_f32 v4, v96, -v110, v4\n"
    "v_fma_f32 v5, v97, -v110, v5\n"
    "v_mfma_f32_16x16x32_bf16 v[16:19], a[224:227], v[32:35], v[16:19]\n"
    "v_fma_f32 v6, v98, -v110, v6\n"
    "v_fma_f32 v7, v99, -v110, v7\n"
    "v_mfma_f32_16x16x32_bf16 v[20:23], a[224:227], v[36:39], v[20:23]\n"
    "v_mul_f32 v0, v0, v112\n"
    "v_mul_f32 v1, v1, v112\n"
    "v_mfma_f32_16x16x32_bf16 v[16:19], a[228:231], v[40:43], v[16:19]\n"
    "v_mul_f32 v2, v2, v112\n"
    "v_mul_f32 v3, v3, v112\n"
    "v_mfma_f32_16x16x32_bf16 v[20:23], a[228:231], v[44:47], v[20:23]\n"
    "v_mul_f32 v4, v4, v114\n"
    "v_mul_f32 v5, v5, v114\n"
    "v_mfma_f32_16x16x32_bf16 v[16:19], a[232:235], v[48:51], v[16:19]\n"
    "v_mul_f32 v6, v6, v114\n"
    "v_mul_f32 v7, v7, v114\n"
    "v_mfma_f32_16x16x32_bf16 v[20:23], a[232:235], v[52:55], v[20:23]\n"
    "v_cvt_pk_bf16_f32 v0, v0, v1\n"
    "v_cvt_pk_bf16_f32 v1, v2, v3\n"
    "v_mfma_f32_16x16x32_bf16 v[16:19], a[236:239], v[56:59], v[16:19]\n"
    "ds_write_b64 v231, v[0:1] offset:0\n"
    "v_mfma_f32_16x16x32_bf16 v[20:23], a[236:239], v[60:63], v[20:23]\n"
    "v_cvt_pk_bf16_f32 v4, v4, v5\n"
    "v_cvt_pk_bf16_f32 v5, v6, v7\n"
    ".Lb1_mk2x11_%=:\n"
    "ds_write_b64 v231, v[4:5] offset:1024\n"
    "v_pk_fma_f32 v[8:9], v[100:101], v[108:109], v[8:9] op_sel_hi:[1,0,1] neg_lo:[0,1,0] neg_hi:[0,1,0]\n"
    "v_pk_fma_f32 v[10:11], v[102:103], v[108:109], v[10:11] op_sel_hi:[1,0,1] neg_lo:[0,1,0] neg_hi:[0,1,0]\n"
    "v_pk_fma_f32 v[12:13], v[100:101], v[110:111], v[12:13] op_sel_hi:[1,0,1] neg_lo:[0,1,0] neg_hi:[0,1,0]\n"
    "v_pk_fma_f32 v[14:15], v[102:103], v[110:111], v[14:15] op_sel_hi:[1,0,1] neg_lo:[0,1,0] neg_hi:[0,1,0]\n"
    "v_pk_mul_f32 v[8:9], v[8:9], v[112:113] op_sel_hi:[1,0]\n"
    "v_pk_mul_f32 v[10:11], v[10:11], v[112:113] op_sel_hi:[1,0]\n"
    "v_pk_mul_f32 v[12:13], v[12:13], v[114:115] op_sel_hi:[1,0]\n"
    "v_pk_mul_f32 v[14:15], v[14:15], v[114:115] op_sel_hi:[1,0]\n"
    "v_cvt_pk_bf16_f32 v8, v8, v9\n"
    "v_cvt_pk_bf16_f32 v9, v10, v11\n"
    "ds_write_b64 v232, v[8:9] offset:0\n"
    "v_cvt_pk_bf16_f32 v12, v12, v13\n"
    "v_cvt_pk_bf16_f32 v13, v14, v15\n"
    "ds_write_b64 v232, v[12:13] offset:1024\n"
    "v_pk_fma_f32 v[16:17], v[104:105], v[108:109], v[16:17] op_sel_hi:[1,0,1] neg_lo:[0,1,0] neg_hi:[0,1,0]\n"
    "v_pk_fma_f32 v[18:19], v[106:107], v[108:109], v[18:19] op_sel_hi:[1,0,1] neg_lo:[0,1,0] neg_hi:[0,1,0]\n"
    "v_pk_fma_f32 v[20:21], v[104:105], v[110:111], v[20:21] op_sel_hi:[1,0,1] neg_lo:[0,1,0] neg_hi:[0,1,0]\n"
    "v_pk_fma_f32 v[22:23], v[106:107], v[110:111], v[22:23] op_sel_hi:[1,0,1] neg_lo:[0,1,0] neg_hi:[0,1,0]\n"
    "v_pk_mul_f32 v[16:17], v[16:17], v[112:113] op_sel_hi:[1,0]\n"
    "v_pk_mul_f32 v[18:19], v[18:19], v[112:113] op_sel_hi:[1,0]\n"
    "v_pk_mul_f32 v[20:21], v[20:21], v[114:115] op_sel_hi:[1,0]\n"
    "v_pk_mul_f32 v[22:23], v[22:23], v[114:115] op_sel_hi:[1,0]\n"
    "v_cvt_pk_bf16_f32 v16, v16, v17\n"
    "v_cvt_pk_bf16_f32 v17, v18, v19\n"
    "ds_write_b64 v233, v[16:17] offset:0\n"
    "v_cvt_pk_bf16_f32 v20, v20, v21\n"
    "v_cvt_pk_bf16_f32 v21, v22, v23\n"
    "ds_write_b64 v233, v[20:21] offset:1024\n"
    "s_waitcnt lgkmcnt(0)\n"
    "s_barrier\n"
    ".Lb1_mk3x12_%=:\n"
    "ds_read_b128 v[32:35], v236 offset:1024\n"
    "ds_read_b128 v[44:47], v234 offset:0\n"
    "ds_read_b128 v[36:39], v235 offset:0\n"
    "ds_read_b128 v[48:51], v234 offset:1024\n"
    "ds_read_b128 v[40:43], v235 offset:1024\n"
    "ds_read_b64_tr_b16 v[52:53], v238 offset:1024\n"
    "ds_read_b64_tr_b16 v[54:55], v237 offset:0\n"
    "ds_read_b64_tr_b16 v[56:57], v237 offset:1024\n"
    "ds_read_b64_tr_b16 v[58:59], v240 offset:1024\n"
    "ds_read_b64_tr_b16 v[60:61], v239 offset:0\n"
    "ds_read_b64_tr_b16 v[62:63], v239 offset:1024\n"
    "s_waitcnt lgkmcnt(9)\n"
    "v_mfma_f32_16x16x32_bf16 v[64:67], v[32:35], v[44:47], 0\n"
    "s_waitcnt lgkmcnt(8)\n"
    "v_mfma_f32_16x16x32_bf16 v[68:71], v[36:39], v[44:47], 0\n"
    "s_waitcnt lgkmcnt(7)\n"
    "v_mfma_f32_16x16x32_bf16 v[72:75], v[36:39], v[48:51], 0\n"
    "s_waitcnt lgkmcnt(6)\n"
    "v_mfma_f32_16x16x32_bf16 v[76:79], v[40:43], v[48:51], 0\n"
    "v_cndmask_b32 v64, v64, v65, s[42:43]\n"
    "v_cndmask_b32 v65, v66, v67, s[42:43]\n"
    "v_cndmask_b32 v66, v68, v69, s[42:43]\n"
    "v_cndmask_b32 v67, v70, v71, s[42:43]\n"
    "s_nop 0\n"
    "v_cndmask_b32 v68, v72, v73, s[42:43]\n"
    "v_cndmask_b32 v69, v74, v75, s[42:43]\n"
    "s_nop 0\n"
    "v_cndmask_b32 v70, v76, v77, s[42:43]\n"
    "v_cndmask_b32 v71, v78, v79, s[42:43]\n"
    "v_cndmask_b32 v64, v249, v64, s[38:39]\n"
    "v_cndmask_b32 v65, v249, v65, s[40:41]\n"
    "v_cndmask_b32 v66, v249, v66, s[12:13]\n"
    "v_cndmask_b32 v67, v249, v67, s[14:15]\n"
    "v_cndmask_b32 v68, v249, v68, s[8:9]\n"
    "v_cndmask_b32 v69, v249, v69, s[10:11]\n"
    "v_cndmask_b32 v70, v249, v70, s[12:13]\n"
    "v_cndmask_b32 v71, v249, v71, s[14:15]\n"
    "v_max3_f32 v72, v64, v65, v66\n"
    "v_max3_f32 v74, v68, v69, v70\n"
    "v_max_f32 v72, v72, v67\n"
    "v_max_f32 v74, v74, v71\n"
    "s_nop 1\n"
    "v_permlane16_swap_b32 v72, v74\n"
    "v_max_f32 v72, v72, v74\n"
    "v_mov_b32 v74, v72\n"
    "s_nop 1\n"
    "v_permlane32_swap_b32 v72, v74\n"
    "v_max_f32 v72, v72, v74\n"
    "v_mul_f32 v72, s37, v72\n"
    "v_mov_b32 v74, v72\n"
    "s_nop 1\n"
    "v_permlane16_swap_b32 v72, v74\n"
    "v_fma_f32 v64, v64, s37, -v72\n"
    "v_fma_f32 v65, v65, s37, -v72\n"
    "v_fma_f32 v66, v66, s37, -v72\n"
    "v_fma_f32 v67, v67, s37, -v72\n"
    "v_fma_f32 v68, v68, s37, -v74\n"
    "v_fma_f32 v69, v69, s37, -v74\n"
    "v_fma_f32 v70, v70, s37, -v74\n"
    "v_fma_f32 v71, v71, s37, -v74\n"
    "v_exp_f32 v64, v64\n"
    "v_exp_f32 v65, v65\n"
    "v_exp_f32 v66, v66\n"
    "v_exp_f32 v67, v67\n"
    "v_exp_f32 v68, v68\n"
    "v_exp_f32 v69, v69\n"
    "v_exp_f32 v70, v70\n"
    "v_exp_f32 v71, v71\n"
    "v_pk_add_f32 v[72:73], v[64:65], v[66:67]\n"
    "v_pk_add_f32 v[74:75], v[68:69], v[70:71]\n"
    "v_add_f32 v72, v72, v73\n"
    "v_add_f32 v74, v74, v75\n"
    "s_nop 1\n"
    "v_permlane16_swap_b32 v72, v74\n"
    "v_add_f32 v72, v72, v74\n"
    "v_mov_b32 v74, v72\n"
    "s_nop 1\n"
    "v_permlane32_swap_b32 v72, v74\n"
    "v_add_f32 v72, v72, v74\n"
    "v_rcp_f32 v72, v72\n"
    "s_nop 0\n"
    "v_mov_b32 v74, v72\n"
    "s_nop 1\n"
    "v_permlane16_swap_b32 v72, v74\n"
    "v_pk_mul_f32 v[64:65], v[64:65], v[72:73] op_sel_hi:[1,0]\n"
    "v_pk_mul_f32 v[66:67], v[66:67], v[72:73] op_sel_hi:[1,0]\n"
    "v_pk_mul_f32 v[68:69], v[68:69], v[74:75] op_sel_hi:[1,0]\n"
    "v_pk_mul_f32 v[70:71], v[70:71], v[74:75] op_sel_hi:[1,0]\n"
    "v_cvt_pk_bf16_f32 v64, v64, v65\n"
    "v_cvt_pk_bf16_f32 v66, v66, v67\n"
    "v_cvt_pk_bf16_f32 v68, v68, v69\n"
    "v_cvt_pk_bf16_f32 v70, v70, v71\n"
    "v_perm_b32 v80, v64, v64, v247\n"
    "v_perm_b32 v81, v64, v64, v248\n"
    "v_perm_b32 v82, v66, v66, v247\n"
    "v_perm_b32 v83, v66, v66, v248\n"
    "v_perm_b32 v84, v68, v68, v247\n"
    "v_perm_b32 v85, v68, v68, v248\n"
    "v_perm_b32 v86, v70, v70, v247\n"
    "v_perm_b32 v87, v70, v70, v248\n"
    "s_waitcnt lgkmcnt(0)\n"
    "v_mfma_f32_16x16x32_bf16 v[0:3], v[52:55], v[80:83], 0\n"
    "v_mfma_f32_16x16x32_bf16 v[4:7], v[54:57], v[84:87], 0\n"
    "v_mfma_f32_16x16x32_bf16 v[8:11], v[58:61], v[80:83], 0\n"
    "v_mfma_f32_16x16x32_bf16 v[12:15], v[60:63], v[84:87], 0\n"
    "s_mov_b64 s[38:39], s[8:9]\n"
    "s_mov_b64 s[40:41], s[10:11]\n"
    "v_cvt_pk_bf16_f32 v16, v0, v1\n"
    "v_cvt_pk_bf16_f32 v17, v2, v3\n"
    "v_cvt_pk_bf16_f32 v18, v4, v5\n"
    "v_cvt_pk_bf16_f32 v19, v6, v7\n"
    "s_nop 0\n"
    "v_permlane16_swap_b32 v16, v18\n"
    "v_permlane16_swap_b32 v17, v19\n"
    "buffer_store_dwordx4 v[16:19], v245, s[0:3], s33 offen sc1\n"
    "v_cvt_pk_bf16_f32 v20, v8, v9\n"
    "v_cvt_pk_bf16_f32 v21, v10, v11\n"
    "v_cvt_pk_bf16_f32 v22, v12, v13\n"
    "v_cvt_pk_bf16_f32 v23, v14, v15\n"
    "buffer_store_dwordx4 v[20:23], v246, s[0:3], s33 offen sc1\n"
    ".Lb1_mk4x13_%=:\n"
    "v_mov_b32 v108, 0\n"
    "s_memrealtime s[26:27]\n"
    "s_cmp_lg_u32 s18, 0\n"
    "s_waitcnt lgkmcnt(0)\n"
    "s_cbranch_scc1 .Lb1_pl15_%=\n"
    "s_sub_u32 s28, s36, s26\n"
    "s_cmp_gt_i32 s28, 40\n"
    "s_cbranch_scc1 .Lb1_pl15_%=\n"
    "buffer_load_dwordx4 v[108:111], v229, s[0:3], s21 offen sc1\n"
    ".Lb1_pl15_%=:\n"
    "s_waitcnt vmcnt(0)\n"
    "s_barrier\n"
    "buffer_store_dwordx4 v[116:119], v230, s[0:3], 0 offen sc1\n"
    ".Lb1_mk5x15_%=:\n"
    "s_mov_b32 s30, s31\n"
    "s_add_u32 s31, s31, 1\n"
    "s_cmp_lt_u32 s30, 64\n"
    "s_cbranch_scc1 .Lb1_top_%=\n"
    "s_waitcnt vmcnt(0) lgkmcnt(0)\n"
    "s_endpgm\n"
    ".Lb1_tk0_%=:\n"
    ".Lb1_po3_%=:\n"
    "buffer_load_dword v31, v30, s[0:3], 0 offen sc1\n"
    "s_waitcnt vmcnt(0)\n"
    "v_cmp_gt_i32 vcc, 32, v31\n"
    "s_nop 0\n"
    "s_cbranch_vccz .Lb1_pd4_%=\n"
    "s_add_u32 s24, s24, 1\n"
    "s_cmp_lt_u32 s24, 0x40000\n"
    "s_cbranch_scc1 .Lb1_sp5_%=\n"
    "v_mov_b32 v31, 90\n"
    "s_mov_b32 s28, 33728\n"
    "buffer_store_dword v31, off, s[0:3], s28 sc1\n"
    "s_waitcnt vmcnt(0)\n"
    "s_branch .Lb1_pd4_%=\n"
    ".Lb1_sp5_%=:\n"
    "s_sleep 8\n"
    "s_branch .Lb1_po3_%=\n"
    ".Lb1_pd4_%=:\n"
    "s_branch .Lb1_tk1_%=\n"
    :: "{s[0:3]}"(s0_), "{s[4:7]}"(s4_), "{s[8:11]}"(s8_), "{s[12:15]}"(s12_), "{s[16:19]}"(s16_), "{s[20:23]}"(s20_), "{v[0:3]}"(v0_), "{v[224:227]}"(v224_), "{v[228:231]}"(v228_), "{v[232:235]}"(v232_), "{v[236:239]}"(v236_), "{v[240:243]}"(v240_), "{v[244:247]}"(v244_), "{v[248:251]}"(v248_) : "memory", "v255", "a255", "s95");
    __builtin_unreachable();
}
DEV void b2_role(const MegaArgs& A, const int L, const int c, const int rep) {
    ROLE_HEAD;
    const int b = 4 * c + w;
    const u32x4 ro = L ? u32x4{lo(A.out), hi(A.out), 2048u * 1280u, 0x20000u} : rx;
    if (L == 0) {
#pragma unroll
        for (int k = 0; k < 16; ++k) bst16(rr, ST0 + b * 32768 + (k >> 3) * 16384 + (k & 7) * 1024 + l * 16, ones);
        DRAIN();
        wait_drawn(A.ar);
        DRAIN_BARRIER();
        bst4(rr, threadIdx.x == 0 ? xl(Z_ST + c * 128, 0) : OOB, 1u);
    }
    const u32x4 s0_ = {RFL(rx[0]), RFL(rx[1]), RFL(rx[2]), RFL(rx[3])};
    const u32x4 s4_ = {RFL(lo(A.wo + 32 * b)), RFL(hi(A.wo + 32 * b)), RFL((640 * 640 - 32 * b) * 2), RFL(0x20000u)};
    const u32x4 s8_ = {RFL(ro[0]), RFL(ro[1]), RFL(ro[2]), RFL(ro[3])};
    const u32x4 s12_ = {RFL(w), RFL(L << 13), RFL(REC0 + RC_O + w * 2048), RFL(xl(Z_O + rep * 2048, L))};
    const u32x4 s16_ = {RFL(xl(Z_ST, 0)), RFL(ST0), RFL(DBG0 + ((L * 128 + 107 + c) * 4 + w) * 2048), RFL(blockIdx.x == 0 ? 6 : 0)};
    const u32x4 v0_ = {(unsigned)((0 + (l >> 2)) * 1280 + (l & 3) * 16 - 0), (unsigned)((16 + (l >> 2)) * 1280 + (l & 3) * 16 - 1024), (unsigned)((32 + (l >> 2)) * 1280 + (l & 3) * 16 - 2048), (unsigned)((48 + (l >> 2)) * 1280 + (l & 3) * 16 - 3072)};
    const u32x4 v4_ = {(unsigned)(w * 40960 + (8 * (lq & 1) + 20 * (lq >> 1) + (l16 >> 2)) * 64 + (l & 3) * 16), (unsigned)(w * 40960 + (20 * lq + 16 + (l16 >> 2)) * 64 + (l & 3) * 16), (unsigned)((l < 5 ? l : 0) * 128), (unsigned)(0u)};
    const u32x4 v224_ = {(unsigned)(l * 16), (unsigned)((l < 11 ? l : 0) * 128), (unsigned)(l < 4 && w == 1 ? xl(Z_XN + l * 2048 + c * 128, 0) : OOB), (unsigned)(c * 16384 + l * 16)};
    const u32x4 v228_ = {(unsigned)(L ? (l16 + 0) * 1280 + b * 64 + lq * 16 : RC_X2 + c * 16384 + 0 + l * 16), (unsigned)(L ? (l16 + 16) * 1280 + b * 64 + lq * 16 : RC_X2 + c * 16384 + 1024 + l * 16), (unsigned)(l < 16 ? b * 32768 + l * 16 : OOB), (unsigned)((0 + lq) * 32768 + l16 * 16)};
    const u32x4 v232_ = {(unsigned)((4 + lq) * 32768 + l16 * 16), (unsigned)((8 + lq) * 32768 + l16 * 16), (unsigned)((12 + lq) * 32768 + l16 * 16), (unsigned)((16 + lq) * 32768 + l16 * 16)};
    asm volatile(
    "s_mov_b32 s20, 0\n"
    "s_mov_b64 s[22:23], 0\n"
    "s_mov_b32 s25, 0\n"
    "s_cmp_eq_u32 s13, 0\n"
    "s_cselect_b32 s24, 24, 60\n"
    "s_sub_u32 s24, s24, s19\n"
    ".Lb2_ho1_%=:\n"
    "s_sub_u32 s24, s24, 1\n"
    "s_cbranch_scc1 .Lb2_hd2_%=\n"
    "s_sleep 17\n"
    "s_branch .Lb2_ho1_%=\n"
    ".Lb2_hd2_%=:\n"
    "v_and_b32 v30, 0x70, v224\n"
    "v_lshlrev_b32 v30, 2, v30\n"
    "v_or_b32 v30, 0x8000, v30\n"
    "buffer_load_dword v31, v30, s[0:3], 0 offen sc1\n"
    "s_waitcnt vmcnt(0)\n"
    "v_cmp_gt_i32 vcc, 32, v31\n"
    "s_nop 0\n"
    "s_cbranch_vccnz .Lb2_tk0_%=\n"
    ".Lb2_tk1_%=:\n"
    "s_mul_i32 s28, s12, 40960\n"
    "s_mov_b32 s34, 0\n"
    "s_add_u32 m0, s28, 0\n"
    "s_nop 0\n"
    "buffer_load_dwordx4 v0, s[4:7], s34 offen offset:0 lds\n"
    "buffer_load_dwordx4 v1, s[4:7], s34 offen offset:1024 lds\n"
    "buffer_load_dwordx4 v2, s[4:7], s34 offen offset:2048 lds\n"
    "buffer_load_dwordx4 v3, s[4:7], s34 offen offset:3072 lds\n"
    "s_mov_b32 s34, 81920\n"
    "s_add_u32 m0, s28, 4096\n"
    "s_nop 0\n"
    "buffer_load_dwordx4 v0, s[4:7], s34 offen offset:0 lds\n"
    "buffer_load_dwordx4 v1, s[4:7], s34 offen offset:1024 lds\n"
    "buffer_load_dwordx4 v2, s[4:7], s34 offen offset:2048 lds\n"
    "buffer_load_dwordx4 v3, s[4:7], s34 offen offset:3072 lds\n"
    "s_mov_b32 s34, 163840\n"
    "s_add_u32 m0, s28, 8192\n"
    "s_nop 0\n"
    "buffer_load_dwordx4 v0, s[4:7], s34 offen offset:0 lds\n"
    "buffer_load_dwordx4 v1, s[4:7], s34 offen offset:1024 lds\n"
    "buffer_load_dwordx4 v2, s[4:7], s34 offen offset:2048 lds\n"
    "buffer_load_dwordx4 v3, s[4:7], s34 offen offset:3072 lds\n"
    "s_mov_b32 s34, 245760\n"
    "s_add_u32 m0, s28, 12288\n"
    "s_nop 0\n"
    "buffer_load_dwordx4 v0, s[4:7], s34 offen offset:0 lds\n"
    "buffer_load_dwordx4 v1, s[4:7], s34 offen offset:1024 lds\n"
    "buffer_load_dwordx4 v2, s[4:7], s34 offen offset:2048 lds\n"
    "buffer_load_dwordx4 v3, s[4:7], s34 offen offset:3072 lds\n"
    "s_mov_b32 s34, 327680\n"
    "s_add_u32 m0, s28, 16384\n"
    "s_nop 0\n"
    "buffer_load_dwordx4 v0, s[4:7], s34 offen offset:0 lds\n"
    "buffer_load_dwordx4 v1, s[4:7], s34 offen offset:1024 lds\n"
    "buffer_load_dwordx4 v2, s[4:7], s34 offen offset:2048 lds\n"
    "buffer_load_dwordx4 v3, s[4:7], s34 offen offset:3072 lds\n"
    "s_mov_b32 s34, 409600\n"
    "s_add_u32 m0, s28, 20480\n"
    "s_nop 0\n"
    "buffer_load_dwordx4 v0, s[4:7], s34 offen offset:0 lds\n"
    "buffer_load_dwordx4 v1, s[4:7], s34 offen offset:1024 lds\n"
    "buffer_load_dwordx4 v2, s[4:7], s34 offen offset:2048 lds\n"
    "buffer_load_dwordx4 v3, s[4:7], s34 offen offset:3072 lds\n"
    "s_mov_b32 s34, 491520\n"
    "s_add_u32 m0, s28, 24576\n"
    "s_nop 0\n"
    "buffer_load_dwordx4 v0, s[4:7], s34 offen offset:0 lds\n"
    "buffer_load_dwordx4 v1, s[4:7], s34 offen offset:1024 lds\n"
    "buffer_load_dwordx4 v2, s[4:7], s34 offen offset:2048 lds\n"
    "buffer_load_dwordx4 v3, s[4:7], s34 offen offset:3072 lds\n"
    "s_mov_b32 s34, 573440\n"
    "s_add_u32 m0, s28, 28672\n"
    "s_nop 0\n"
    "buffer_load_dwordx4 v0, s[4:7], s34 offen offset:0 lds\n"
    "buffer_load_dwordx4 v1, s[4:7], s34 offen offset:1024 lds\n"
    "buffer_load_dwordx4 v2, s[4:7], s34 offen offset:2048 lds\n"
    "buffer_load_dwordx4 v3, s[4:7], s34 offen offset:3072 lds\n"
    "s_mov_b32 s34, 655360\n"
    "s_add_u32 m0, s28, 32768\n"
    "s_nop 0\n"
    "buffer_load_dwordx4 v0, s[4:7], s34 offen offset:0 lds\n"
    "buffer_load_dwordx4 v1, s[4:7], s34 offen offset:1024 lds\n"
    "buffer_load_dwordx4 v2, s[4:7], s34 offen offset:2048 lds\n"
    "buffer_load_dwordx4 v3, s[4:7], s34 offen offset:3072 lds\n"
    "s_mov_b32 s34, 737280\n"
    "s_add_u32 m0, s28, 36864\n"
    "s_nop 0\n"
    "buffer_load_dwordx4 v0, s[4:7], s34 offen offset:0 lds\n"
    "buffer_load_dwordx4 v1, s[4:7], s34 offen offset:1024 lds\n"
    "buffer_load_dwordx4 v2, s[4:7], s34 offen offset:2048 lds\n"
    "buffer_load_dwordx4 v3, s[4:7], s34 offen offset:3072 lds\n"
    "s_waitcnt vmcnt(0)\n"
    "ds_read_b64_tr_b16 a[0:1], v4 offset:0\n"
    "ds_read_b64_tr_b16 a[2:3], v4 offset:256\n"
    "ds_read_b64_tr_b16 a[80:81], v4 offset:8\n"
    "ds_read_b64_tr_b16 a[82:83], v4 offset:264\n"
    "s_waitcnt lgkmcnt(0)\n"
    "ds_read_b64_tr_b16 a[4:5], v4 offset:2560\n"
    "ds_read_b64_tr_b16 a[6:7], v4 offset:2816\n"
    "ds_read_b64_tr_b16 a[84:85], v4 offset:2568\n"
    "ds_read_b64_tr_b16 a[86:87], v4 offset:2824\n"
    "s_waitcnt lgkmcnt(0)\n"
    "ds_read_b64_tr_b16 a[8:9], v4 offset:5120\n"
    "ds_read_b64_tr_b16 a[10:11], v4 offset:5376\n"
    "ds_read_b64_tr_b16 a[88:89], v4 offset:5128\n"
    "ds_read_b64_tr_b16 a[90:91], v4 offset:5384\n"
    "s_waitcnt lgkmcnt(0)\n"
    "ds_read_b64_tr_b16 a[12:13], v4 offset:7680\n"
    "ds_read_b64_tr_b16 a[14:15], v4 offset:7936\n"
    "ds_read_b64_tr_b16 a[92:93], v4 offset:7688\n"
    "ds_read_b64_tr_b16 a[94:95], v4 offset:7944\n"
    "s_waitcnt lgkmcnt(0)\n"
    "ds_read_b64_tr_b16 a[16:17], v4 offset:10240\n"
    "ds_read_b64_tr_b16 a[18:19], v4 offset:10496\n"
    "ds_read_b64_tr_b16 a[96:97], v4 offset:10248\n"
    "ds_read_b64_tr_b16 a[98:99], v4 offset:10504\n"
    "s_waitcnt lgkmcnt(0)\n"
    "ds_read_b64_tr_b16 a[20:21], v4 offset:12800\n"
    "ds_read_b64_tr_b16 a[22:23], v4 offset:13056\n"
    "ds_read_b64_tr_b16 a[100:101], v4 offset:12808\n"
    "ds_read_b64_tr_b16 a[102:103], v4 offset:13064\n"
    "s_waitcnt lgkmcnt(0)\n"
    "ds_read_b64_tr_b16 a[24:25], v4 offset:15360\n"
    "ds_read_b64_tr_b16 a[26:27], v4 offset:15616\n"
    "ds_read_b64_tr_b16 a[104:105], v4 offset:15368\n"
    "ds_read_b64_tr_b16 a[106:107], v4 offset:15624\n"
    "s_waitcnt lgkmcnt(0)\n"
    "ds_read_b64_tr_b16 a[28:29], v4 offset:17920\n"
    "ds_read_b64_tr_b16 a[30:31], v4 offset:18176\n"
    "ds_read_b64_tr_b16 a[108:109], v4 offset:17928\n"
    "ds_read_b64_tr_b16 a[110:111], v4 offset:18184\n"
    "s_waitcnt lgkmcnt(0)\n"
    "ds_read_b64_tr_b16 a[32:33], v4 offset:20480\n"
    "ds_read_b64_tr_b16 a[34:35], v4 offset:20736\n"
    "ds_read_b64_tr_b16 a[112:113], v4 offset:20488\n"
    "ds_read_b64_tr_b16 a[114:115], v4 offset:20744\n"
    "s_waitcnt lgkmcnt(0)\n"
    "ds_read_b64_tr_b16 a[36:37], v4 offset:23040\n"
    "ds_read_b64_tr_b16 a[38:39], v4 offset:23296\n"
    "ds_read_b64_tr_b16 a[116:117], v4 offset:23048\n"
    "ds_read_b64_tr_b16 a[118:119], v4 offset:23304\n"
    "s_waitcnt lgkmcnt(0)\n"
    "ds_read_b64_tr_b16 a[40:41], v4 offset:25600\n"
    "ds_read_b64_tr_b16 a[42:43], v4 offset:25856\n"
    "ds_read_b64_tr_b16 a[120:121], v4 offset:25608\n"
    "ds_read_b64_tr_b16 a[122:123], v4 offset:25864\n"
    "s_waitcnt lgkmcnt(0)\n"
    "ds_read_b64_tr_b16 a[44:45], v4 offset:28160\n"
    "ds_read_b64_tr_b16 a[46:47], v4 offset:28416\n"
    "ds_read_b64_tr_b16 a[124:125], v4 offset:28168\n"
    "ds_read_b64_tr_b16 a[126:127], v4 offset:28424\n"
    "s_waitcnt lgkmcnt(0)\n"
    "ds_read_b64_tr_b16 a[48:49], v4 offset:30720\n"
    "ds_read_b64_tr_b16 a[50:51], v4 offset:30976\n"
    "ds_read_b64_tr_b16 a[128:129], v4 offset:30728\n"
    "ds_read_b64_tr_b16 a[130:131], v4 offset:30984\n"
    "s_waitcnt lgkmcnt(0)\n"
    "ds_read_b64_tr_b16 a[52:53], v4 offset:33280\n"
    "ds_read_b64_tr_b16 a[54:55], v4 offset:33536\n"
    "ds_read_b64_tr_b16 a[132:133], v4 offset:33288\n"
    "ds_read_b64_tr_b16 a[134:135], v4 offset:33544\n"
    "s_waitcnt lgkmcnt(0)\n"
    "ds_read_b64_tr_b16 a[56:57], v4 offset:35840\n"
    "ds_read_b64_tr_b16 a[58:59], v4 offset:36096\n"
    "ds_read_b64_tr_b16 a[136:137], v4 offset:35848\n"
    "ds_read_b64_tr_b16 a[138:139], v4 offset:36104\n"
    "s_waitcnt lgkmcnt(0)\n"
    "ds_read_b64_tr_b16 a[60:61], v4 offset:38400\n"
    "ds_read_b64_tr_b16 a[62:63], v4 offset:38656\n"
    "ds_read_b64_tr_b16 a[140:141], v4 offset:38408\n"
    "ds_read_b64_tr_b16 a[142:143], v4 offset:38664\n"
    "s_waitcnt lgkmcnt(0)\n"
    "ds_read_b64_tr_b16 a[64:65], v5 offset:0\n"
    "ds_read_b64_tr_b16 a[66:67], v5 offset:5120\n"
    "ds_read_b64_tr_b16 a[144:145], v5 offset:8\n"
    "ds_read_b64_tr_b16 a[146:147], v5 offset:5128\n"
    "s_waitcnt lgkmcnt(0)\n"
    "ds_read_b64_tr_b16 a[68:69], v5 offset:10240\n"
    "ds_read_b64_tr_b16 a[70:71], v5 offset:15360\n"
    "ds_read_b64_tr_b16 a[148:149], v5 offset:10248\n"
    "ds_read_b64_tr_b16 a[150:151], v5 offset:15368\n"
    "s_waitcnt lgkmcnt(0)\n"
    "ds_read_b64_tr_b16 a[72:73], v5 offset:20480\n"
    "ds_read_b64_tr_b16 a[74:75], v5 offset:25600\n"
    "ds_read_b64_tr_b16 a[152:153], v5 offset:20488\n"
    "ds_read_b64_tr_b16 a[154:155], v5 offset:25608\n"
    "s_waitcnt lgkmcnt(0)\n"
    "ds_read_b64_tr_b16 a[76:77], v5 offset:30720\n"
    "ds_read_b64_tr_b16 a[78:79], v5 offset:35840\n"
    "ds_read_b64_tr_b16 a[156:157], v5 offset:30728\n"
    "ds_read_b64_tr_b16 a[158:159], v5 offset:35848\n"
    "s_waitcnt lgkmcnt(0)\n"
    "s_mov_b32 s26, -2\n"
    ".Lb2_po6_%=:\n"
    "buffer_load_dword v8, v6, s[0:3], s16 offen sc1\n"
    "s_waitcnt vmcnt(0)\n"
    "v_cmp_gt_i32 vcc, 1, v8\n"
    "s_nop 0\n"
    "s_cbranch_vccz .Lb2_pd7_%=\n"
    "s_add_u32 s20, s20, 1\n"
    "s_cmp_lt_u32 s20, 0x40000\n"
    "s_cbranch_scc1 .Lb2_sp8_%=\n"
    "v_mov_b32 v8, 70\n"
    "s_mov_b32 s24, 33728\n"
    "buffer_store_dword v8, off, s[0:3], s24 sc1\n"
    "s_waitcnt vmcnt(0)\n"
    "s_branch .Lb2_pd7_%=\n"
    ".Lb2_sp8_%=:\n"
    "s_sleep 4\n"
    "s_branch .Lb2_po6_%=\n"
    ".Lb2_pd7_%=:\n"
    "s_mov_b32 s26, 0\n"
    "s_lshl_b32 s33, s12, 11\n"
    "s_mov_b32 s35, 0\n"
    "v_mov_b32 v84, 0\n"
    ".Lb2_top_%=:\n"
    "s_lshl_b32 s28, s26, 19\n"
    "s_lshl_b32 s25, s26, 11\n"
    "s_xor_b32 s25, s25, s13\n"
    "s_and_b32 s25, s25, 0x2000\n"
    "s_add_u32 s28, s28, s25\n"
    "s_add_u32 s28, s28, s14\n"
    "s_add_u32 s27, s26, 1\n"
    "s_sub_u32 s29, s28, 81920\n"
    "s_sub_u32 s31, s28, 163840\n"
    "s_xor_b32 s31, s31, 0x2000\n"
    "s_mul_i32 s30, s26, 40960\n"
    "s_cmp_eq_u32 s13, 0\n"
    "s_cselect_b32 s30, s31, s30\n"
    "s_and_b32 s32, s26, 32\n"
    "s_add_u32 s32, s32, s26\n"
    "s_lshl_b32 s32, s32, 8\n"
    "s_add_u32 s32, s32, s17\n"
    "s_cmp_lg_u32 s12, 0\n"
    "s_cbranch_scc1 .Lb2_pd10_%=\n"
    "s_add_u32 s25, s22, 240\n"
    "v_min3_i32 v84, v84, v85, v86\n"
    "v_min_i32 v84, v84, v87\n"
    "v_cmp_gt_i32 vcc, s27, v84\n"
    "s_nop 0\n"
    "s_cbranch_vccz .Lb2_pd10_%=\n"
    ".Lb2_rt11_%=:\n"
    "s_memrealtime s[22:23]\n"
    "s_waitcnt lgkmcnt(0)\n"
    "s_sub_u32 s24, s35, s22\n"
    "s_cmp_lt_u32 s24, 600\n"
    "s_cbranch_scc1 .Lb2_rt11_%=\n"
    ".Lb2_po9_%=:\n"
    "buffer_load_dwordx4 v[84:87], v225, s[0:3], s15 offen sc1\n"
    "s_memrealtime s[22:23]\n"
    "s_waitcnt vmcnt(0) lgkmcnt(0)\n"
    "s_add_u32 s25, s22, 240\n"
    "v_min3_i32 v84, v84, v85, v86\n"
    "v_min_i32 v84, v84, v87\n"
    "v_cmp_gt_i32 vcc, s27, v84\n"
    "s_nop 0\n"
    "s_cbranch_vccz .Lb2_pd10_%=\n"
    "s_add_u32 s20, s20, 1\n"
    "s_cmp_lt_u32 s20, 0x40000\n"
    "s_cbranch_scc1 .Lb2_sp12_%=\n"
    "v_mov_b32 v84, 71\n"
    "s_mov_b32 s24, 33728\n"
    "buffer_store_dword v84, off, s[0:3], s24 sc1\n"
    "s_waitcnt vmcnt(0)\n"
    "s_branch .Lb2_pd10_%=\n"
    ".Lb2_sp12_%=:\n"
    "s_sleep 6\n"
    "s_branch .Lb2_po9_%=\n"
    ".Lb2_pd10_%=:\n"
    "s_barrier\n"
    "s_cmp_eq_u32 s27, 63\n"
    "s_cselect_b32 s35, s22, s25\n"
    ".Lb2_mk0x12_%=:\n"
    "s_add_u32 m0, s33, 0\n"
    "s_nop 0\n"
    "buffer_load_dwordx4 v224, s[0:3], s28 offen sc1 lds\n"
    "buffer_load_dwordx4 v224, s[0:3], s28 offen offset:1024 sc1 lds\n"
    "s_add_u32 m0, s33, 8192\n"
    "s_add_u32 s28, s28, 0x4000\n"
    "buffer_load_dwordx4 v224, s[0:3], s28 offen sc1 lds\n"
    "buffer_load_dwordx4 v224, s[0:3], s28 offen offset:1024 sc1 lds\n"
    "s_add_u32 m0, s33, 16384\n"
    "s_add_u32 s28, s28, 0x4000\n"
    "buffer_load_dwordx4 v224, s[0:3], s28 offen sc1 lds\n"
    "buffer_load_dwordx4 v224, s[0:3], s28 offen offset:1024 sc1 lds\n"
    "s_add_u32 m0, s33, 24576\n"
    "s_add_u32 s28, s28, 0x4000\n"
    "buffer_load_dwordx4 v224, s[0:3], s28 offen sc1 lds\n"
    "buffer_load_dwordx4 v224, s[0:3], s28 offen offset:1024 sc1 lds\n"
    "s_add_u32 m0, s33, 32768\n"
    "s_add_u32 s28, s28, 0x4000\n"
    "buffer_load_dwordx4 v224, s[0:3], s28 offen sc1 lds\n"
    "buffer_load_dwordx4 v224, s[0:3], s28 offen offset:1024 sc1 lds\n"
    "buffer_load_dwordx4 v[48:51], v227, s[0:3], s29 offen offset:0 sc1\n"
    "buffer_load_dwordx4 v[52:55], v227, s[0:3], s29 offen offset:1024 sc1\n"
    "s_waitcnt vmcnt(10)\n"
    "s_barrier\n"
    ".Lb2_mk1x13_%=:\n"
    "ds_read_b128 v[16:19], v224 offset:0\n"
    "ds_read_b128 v[20:23], v224 offset:1024\n"
    "ds_read_b128 v[24:27], v224 offset:2048\n"
    "ds_read_b128 v[28:31], v224 offset:3072\n"
    "ds_read_b128 v[32:35], v224 offset:4096\n"
    "ds_read_b128 v[36:39], v224 offset:5120\n"
    "s_waitcnt lgkmcnt(4)\n"
    "v_mfma_f32_16x16x32_bf16 v[0:3], a[0:3], v[16:19], 0\n"
    "ds_read_b128 v[40:43], v224 offset:6144\n"
    "ds_read_b128 v[44:47], v224 offset:7168\n"
    "v_mfma_f32_16x16x32_bf16 v[8:11], a[0:3], v[20:23], 0\n"
    "v_mfma_f32_16x16x32_bf16 v[4:7], a[80:83], v[16:19], 0\n"
    "v_mfma_f32_16x16x32_bf16 v[12:15], a[80:83], v[20:23], 0\n"
    "s_waitcnt lgkmcnt(4)\n"
    "v_mfma_f32_16x16x32_bf16 v[0:3], a[4:7], v[24:27], v[0:3]\n"
    "s_waitcnt vmcnt(8)\n"
    "s_barrier\n"
    "ds_read_b128 v[16:19], v224 offset:8192\n"
    "v_mfma_f32_16x16x32_bf16 v[8:11], a[4:7], v[28:31], v[8:11]\n"
    "ds_read_b128 v[20:23], v224 offset:9216\n"
    "v_mfma_f32_16x16x32_bf16 v[4:7], a[84:87], v[24:27], v[4:7]\n"
    "v_mfma_f32_16x16x32_bf16 v[12:15], a[84:87], v[28:31], v[12:15]\n"
    "s_waitcnt lgkmcnt(4)\n"
    "v_mfma_f32_16x16x32_bf16 v[0:3], a[8:11], v[32:35], v[0:3]\n"
    "ds_read_b128 v[24:27], v224 offset:10240\n"
    "ds_read_b128 v[28:31], v224 offset:11264\n"
    "v_mfma_f32_16x16x32_bf16 v[8:11], a[8:11], v[36:39], v[8:11]\n"
    "v_mfma_f32_16x16x32_bf16 v[4:7], a[88:91], v[32:35], v[4:7]\n"
    "v_mfma_f32_16x16x32_bf16 v[12:15], a[88:91], v[36:39], v[12:15]\n"
    "s_waitcnt lgkmcnt(4)\n"
    "v_mfma_f32_16x16x32_bf16 v[0:3], a[12:15], v[40:43], v[0:3]\n"
    "ds_read_b128 v[32:35], v224 offset:12288\n"
    "ds_read_b128 v[36:39], v224 offset:13312\n"
    "v_mfma_f32_16x16x32_bf16 v[8:11], a[12:15], v[44:47], v[8:11]\n"
    "v_mfma_f32_16x16x32_bf16 v[4:7], a[92:95], v[40:43], v[4:7]\n"
    "v_mfma_f32_16x16x32_bf16 v[12:15], a[92:95], v[44:47], v[12:15]\n"
    "s_waitcnt lgkmcnt(4)\n"
    "v_mfma_f32_16x16x32_bf16 v[0:3], a[16:19], v[16:19], v[0:3]\n"
    "ds_read_b128 v[40:43], v224 offset:14336\n"
    "ds_read_b128 v[44:47], v224 offset:15360\n"
    "v_mfma_f32_16x16x32_bf16 v[8:11], a[16:19], v[20:23], v[8:11]\n"
    "v_mfma_f32_16x16x32_bf16 v[4:7], a[96:99], v[16:19], v[4:7]\n"
    "v_mfma_f32_16x16x32_bf16 v[12:15], a[96:99], v[20:23], v[12:15]\n"
    "s_waitcnt lgkmcnt(4)\n"
    "v_mfma_f32_16x16x32_bf16 v[0:3], a[20:23], v[24:27], v[0:3]\n"
    "s_waitcnt vmcnt(6)\n"
    "s_barrier\n"
    "ds_read_b128 v[16:19], v224 offset:16384\n"
    "v_mfma_f32_16x16x32_bf16 v[8:11], a[20:23], v[28:31], v[8:11]\n"
    "ds_read_b128 v[20:23], v224 offset:17408\n"
    "v_mfma_f32_16x16x32_bf16 v[4:7], a[100:103], v[24:27], v[4:7]\n"
    "v_mfma_f32_16x16x32_bf16 v[12:15], a[100:103], v[28:31], v[12:15]\n"
    "s_waitcnt lgkmcnt(4)\n"
    "v_mfma_f32_16x16x32_bf16 v[0:3], a[24:27], v[32:35], v[0:3]\n"
    "ds_read_b128 v[24:27], v224 offset:18432\n"
    "ds_read_b128 v[28:31], v224 offset:19456\n"
    "v_mfma_f32_16x16x32_bf16 v[8:11], a[24:27], v[36:39], v[8:11]\n"
    "v_mfma_f32_16x16x32_bf16 v[4:7], a[104:107], v[32:35], v[4:7]\n"
    "v_mfma_f32_16x16x32_bf16 v[12:15], a[104:107], v[36:39], v[12:15]\n"
    "s_waitcnt lgkmcnt(4)\n"
    "v_mfma_f32_16x16x32_bf16 v[0:3], a[28:31], v[40:43], v[0:3]\n"
    "ds_read_b128 v[32:35], v224 offset:20480\n"
    "ds_read_b128 v[36:39], v224 offset:21504\n"
    "v_mfma_f32_16x16x32_bf16 v[8:11], a[28:31], v[44:47], v[8:11]\n"
    "v_mfma_f32_16x16x32_bf16 v[4:7], a[108:111], v[40:43], v[4:7]\n"
    "v_mfma_f32_16x16x32_bf16 v[12:15], a[108:111], v[44:47], v[12:15]\n"
    "s_waitcnt lgkmcnt(4)\n"
    "v_mfma_f32_16x16x32_bf16 v[0:3], a[32:35], v[16:19], v[0:3]\n"
    "ds_read_b128 v[40:43], v224 offset:22528\n"
    "ds_read_b128 v[44:47], v224 offset:23552\n"
    "v_mfma_f32_16x16x32_bf16 v[8:11], a[32:35], v[20:23], v[8:11]\n"
    "v_mfma_f32_16x16x32_bf16 v[4:7], a[112:115], v[16:19], v[4:7]\n"
    "v_mfma_f32_16x16x32_bf16 v[12:15], a[112:115], v[20:23], v[12:15]\n"
    "s_waitcnt lgkmcnt(4)\n"
    "v_mfma_f32_16x16x32_bf16 v[0:3], a[36:39], v[24:27], v[0:3]\n"
    "s_waitcnt vmcnt(4)\n"
    "s_barrier\n"
    "ds_read_b128 v[16:19], v224 offset:24576\n"
    "v_mfma_f32_16x16x32_bf16 v[8:11], a[36:39], v[28:31], v[8:11]\n"
    "ds_read_b128 v[20:23], v224 offset:25600\n"
    "v_mfma_f32_16x16x32_bf16 v[4:7], a[116:119], v[24:27], v[4:7]\n"
    "v_mfma_f32_16x16x32_bf16 v[12:15], a[116:119], v[28:31], v[12:15]\n"
    "s_waitcnt lgkmcnt(4)\n"
    "v_mfma_f32_16x16x32_bf16 v[0:3], a[40:43], v[32:35], v[0:3]\n"
    "ds_read_b128 v[24:27], v224 offset:26624\n"
    "ds_read_b128 v[28:31], v224 offset:27648\n"
    "v_mfma_f32_16x16x32_bf16 v[8:11], a[40:43], v[36:39], v[8:11]\n"
    "v_mfma_f32_16x16x32_bf16 v[4:7], a[120:123], v[32:35], v[4:7]\n"
    "v_mfma_f32_16x16x32_bf16 v[12:15], a[120:123], v[36:39], v[12:15]\n"
    "s_waitcnt lgkmcnt(4)\n"
    "v_mfma_f32_16x16x32_bf16 v[0:3], a[44:47], v[40:43], v[0:3]\n"
    "ds_read_b128 v[32:35], v224 offset:28672\n"
    "ds_read_b128 v[36:39], v224 offset:29696\n"
    "v_mfma_f32_16x16x32_bf16 v[8:11], a[44:47], v[44:47], v[8:11]\n"
    "v_mfma_f32_16x16x32_bf16 v[4:7], a[124:127], v[40:43], v[4:7]\n"
    "v_mfma_f32_16x16x32_bf16 v[12:15], a[124:127], v[44:47], v[12:15]\n"
    "s_waitcnt lgkmcnt(4)\n"
    "v_mfma_f32_16x16x32_bf16 v[0:3], a[48:51], v[16:19], v[0:3]\n"
    "ds_read_b128 v[40:43], v224 offset:30720\n"
    "ds_read_b128 v[44:47], v224 offset:31744\n"
    "v_mfma_f32_16x16x32_bf16 v[8:11], a[48:51], v[20:23], v[8:11]\n"
    "v_mfma_f32_16x16x32_bf16 v[4:7], a[128:131], v[16:19], v[4:7]\n"
    "v_mfma_f32_16x16x32_bf16 v[12:15], a[128:131], v[20:23], v[12:15]\n"
    "s_waitcnt lgkmcnt(4)\n"
    "v_mfma_f32_16x16x32_bf16 v[0:3], a[52:55], v[24:27], v[0:3]\n"
    "s_waitcnt vmcnt(2)\n"
    "s_barrier\n"
    "ds_read_b128 v[16:19], v224 offset:32768\n"
    "v_mfma_f32_16x16x32_bf16 v[8:11], a[52:55], v[28:31], v[8:11]\n"
    "ds_read_b128 v[20:23], v224 offset:33792\n"
    "v_mfma_f32_16x16x32_bf16 v[4:7], a[132:135], v[24:27], v[4:7]\n"
    "v_mfma_f32_16x16x32_bf16 v[12:15], a[132:135], v[28:31], v[12:15]\n"
    "s_waitcnt lgkmcnt(4)\n"
    "v_mfma_f32_16x16x32_bf16 v[0:3], a[56:59], v[32:35], v[0:3]\n"
    "ds_read_b128 v[24:27], v224 offset:34816\n"
    "ds_read_b128 v[28:31], v224 offset:35840\n"
    "v_mfma_f32_16x16x32_bf16 v[8:11], a[56:59], v[36:39], v[8:11]\n"
    "v_mfma_f32_16x16x32_bf16 v[4:7], a[136:139], v[32:35], v[4:7]\n"
    "s_waitcnt vmcnt(0)\n"
    "v_lshlrev_b32 v96, 16, v48\n"
    "v_and_b32 v97, 0xffff0000, v48\n"
    "v_mfma_f32_16x16x32_bf16 v[12:15], a[136:139], v[36:39], v[12:15]\n"
    "s_waitcnt lgkmcnt(4)\n"
    "v_lshlrev_b32 v98, 16, v49\n"
    "v_and_b32 v99, 0xffff0000, v49\n"
    "v_mfma_f32_16x16x32_bf16 v[0:3], a[60:63], v[40:43], v[0:3]\n"
    "s_waitcnt lgkmcnt(2)\n"
    "ds_read_b128 v[32:35], v224 offset:36864\n"
    "ds_read_b128 v[36:39], v224 offset:37888\n"
    "v_mfma_f32_16x16x32_bf16 v[8:11], a[60:63], v[44:47], v[8:11]\n"
    "v_swap_b32 v18, v20\n"
    "v_mfma_f32_16x16x32_bf16 v[4:7], a[140:143], v[40:43], v[4:7]\n"
    "v_swap_b32 v19, v21\n"
    "v_mfma_f32_16x16x32_bf16 v[12:15], a[140:143], v[44:47], v[12:15]\n"
    "v_lshlrev_b32 v100, 16, v50\n"
    "v_and_b32 v101, 0xffff0000, v50\n"
    "v_mfma_f32_16x16x32_bf16 v[0:3], a[64:67], v[16:19], v[0:3]\n"
    "s_waitcnt lgkmcnt(2)\n"
    "ds_read_b128 v[40:43], v224 offset:38912\n"
    "ds_read_b128 v[44:47], v224 offset:39936\n"
    "v_mfma_f32_16x16x32_bf16 v[8:11], a[64:67], v[20:23], v[8:11]\n"
    "v_swap_b32 v26, v28\n"
    "v_mfma_f32_16x16x32_bf16 v[4:7], a[144:147], v[16:19], v[4:7]\n"
    "v_swap_b32 v27, v29\n"
    "v_mfma_f32_16x16x32_bf16 v[12:15], a[144:147], v[20:23], v[12:15]\n"
    "v_lshlrev_b32 v102, 16, v51\n"
    "v_and_b32 v103, 0xffff0000, v51\n"
    "v_mfma_f32_16x16x32_bf16 v[0:3], a[68:71], v[24:27], v[0:3]\n"
    "s_waitcnt lgkmcnt(2)\n"
    "v_lshlrev_b32 v104, 16, v52\n"
    "v_and_b32 v105, 0xffff0000, v52\n"
    "v_mfma_f32_16x16x32_bf16 v[8:11], a[68:71], v[28:31], v[8:11]\n"
    "v_swap_b32 v34, v36\n"
    "v_mfma_f32_16x16x32_bf16 v[4:7], a[148:151], v[24:27], v[4:7]\n"
    "v_swap_b32 v35, v37\n"
    "v_mfma_f32_16x16x32_bf16 v[12:15], a[148:151], v[28:31], v[12:15]\n"
    "v_lshlrev_b32 v106, 16, v53\n"
    "v_and_b32 v107, 0xffff0000, v53\n"
    "v_mfma_f32_16x16x32_bf16 v[0:3], a[72:75], v[32:35], v[0:3]\n"
    "s_waitcnt lgkmcnt(0)\n"
    "v_lshlrev_b32 v108, 16, v54\n"
    "v_and_b32 v109, 0xffff0000, v54\n"
    "v_mfma_f32_16x16x32_bf16 v[8:11], a[72:75], v[36:39], v[8:11]\n"
    "v_swap_b32 v42, v44\n"
    "v_mfma_f32_16x16x32_bf16 v[4:7], a[152:155], v[32:35], v[4:7]\n"
    "v_swap_b32 v43, v45\n"
    "v_mfma_f32_16x16x32_bf16 v[12:15], a[152:155], v[36:39], v[12:15]\n"
    "v_lshlrev_b32 v110, 16, v55\n"
    "v_and_b32 v111, 0xffff0000, v55\n"
    "v_mfma_f32_16x16x32_bf16 v[0:3], a[76:79], v[40:43], v[0:3]\n"
    "v_mfma_f32_16x16x32_bf16 v[8:11], a[76:79], v[44:47], v[8:11]\n"
    "v_mfma_f32_16x16x32_bf16 v[4:7], a[156:159], v[40:43], v[4:7]\n"
    "v_mfma_f32_16x16x32_bf16 v[12:15], a[156:159], v[44:47], v[12:15]\n"
    ".Lb2_mk2x14_%=:\n"
    "v_pk_add_f32 v[0:1], v[0:1], v[96:97]\n"
    "v_pk_add_f32 v[2:3], v[2:3], v[98:99]\n"
    "v_pk_add_f32 v[8:9], v[8:9], v[104:105]\n"
    "v_pk_add_f32 v[10:11], v[10:11], v[106:107]\n"
    "s_nop 0\n"
    "v_pk_add_f32 v[4:5], v[4:5], v[100:101]\n"
    "v_pk_add_f32 v[6:7], v[6:7], v[102:103]\n"
    "s_nop 0\n"
    "v_pk_add_f32 v[12:13], v[12:13], v[108:109]\n"
    "v_pk_add_f32 v[14:15], v[14:15], v[110:111]\n"
    "s_cmp_lg_u32 s13, 0\n"
    "s_cbranch_scc1 .Lb2_out_%=\n"
    "v_pk_add_f32 v[64:65], v[0:1], v[2:3]\n"
    "v_pk_add_f32 v[68:69], v[8:9], v[10:11]\n"
    "v_pk_mul_f32 v[66:67], v[0:1], v[0:1]\n"
    "v_pk_mul_f32 v[70:71], v[8:9], v[8:9]\n"
    "v_pk_add_f32 v[96:97], v[4:5], v[6:7]\n"
    "v_pk_add_f32 v[98:99], v[12:13], v[14:15]\n"
    "v_pk_fma_f32 v[66:67], v[2:3], v[2:3], v[66:67]\n"
    "v_pk_fma_f32 v[70:71], v[10:11], v[10:11], v[70:71]\n"
    "v_pk_add_f32 v[64:65], v[64:65], v[96:97]\n"
    "v_pk_add_f32 v[68:69], v[68:69], v[98:99]\n"
    "v_pk_fma_f32 v[66:67], v[4:5], v[4:5], v[66:67]\n"
    "v_pk_fma_f32 v[70:71], v[12:13], v[12:13], v[70:71]\n"
    "v_add_f32 v64, v64, v65\n"
    "v_add_f32 v68, v68, v69\n"
    "v_pk_fma_f32 v[66:67], v[6:7], v[6:7], v[66:67]\n"
    "v_pk_fma_f32 v[70:71], v[14:15], v[14:15], v[70:71]\n"
    "v_add_f32 v66, v66, v67\n"
    "v_add_f32 v70, v70, v71\n"
    "s_nop 0\n"
    "v_permlane16_swap_b32 v64, v66\n"
    "v_permlane16_swap_b32 v68, v70\n"
    "v_add_f32 v64, v64, v66\n"
    "v_add_f32 v68, v68, v70\n"
    "s_nop 1\n"
    "v_permlane32_swap_b32 v64, v68\n"
    "v_add_f32 v64, v64, v68\n"
    "v_min_u32 v64, -2, v64\n"
    "v_mov_b32 v65, v64\n"
    "v_cvt_pk_bf16_f32 v56, v0, v1\n"
    "v_cvt_pk_bf16_f32 v57, v2, v3\n"
    "v_permlane16_swap_b32 v64, v65\n"
    "v_mov_b32 v66, v64\n"
    "v_mov_b32 v67, v65\n"
    "v_cvt_pk_bf16_f32 v58, v4, v5\n"
    "v_cvt_pk_bf16_f32 v59, v6, v7\n"
    "v_permlane32_swap_b32 v64, v66\n"
    "v_permlane32_swap_b32 v65, v67\n"
    "buffer_store_dwordx4 v[64:67], v230, s[0:3], s32 offen sc1\n"
    "v_cvt_pk_bf16_f32 v60, v8, v9\n"
    "v_cvt_pk_bf16_f32 v61, v10, v11\n"
    "v_cvt_pk_bf16_f32 v62, v12, v13\n"
    "v_cvt_pk_bf16_f32 v63, v14, v15\n"
    ".Lb2_mk3x15_%=:\n"
    "v_lshlrev_b32 v0, 16, v56\n"
    "v_and_b32 v1, 0xffff0000, v56\n"
    "v_lshlrev_b32 v2, 16, v57\n"
    "v_and_b32 v3, 0xffff0000, v57\n"
    "v_lshlrev_b32 v4, 16, v58\n"
    "v_and_b32 v5, 0xffff0000, v58\n"
    "v_lshlrev_b32 v6, 16, v59\n"
    "v_and_b32 v7, 0xffff0000, v59\n"
    "v_lshlrev_b32 v8, 16, v60\n"
    "v_and_b32 v9, 0xffff0000, v60\n"
    "v_lshlrev_b32 v10, 16, v61\n"
    "v_and_b32 v11, 0xffff0000, v61\n"
    "v_lshlrev_b32 v12, 16, v62\n"
    "v_and_b32 v13, 0xffff0000, v62\n"
    "v_lshlrev_b32 v14, 16, v63\n"
    "v_and_b32 v15, 0xffff0000, v63\n"
    "s_sleep 6\n"
    "buffer_load_dwordx4 v[64:67], v231, s[0:3], s32 offen sc1\n"
    "buffer_load_dwordx4 v[68:71], v232, s[0:3], s32 offen sc1\n"
    "buffer_load_dwordx4 v[72:75], v233, s[0:3], s32 offen sc1\n"
    "buffer_load_dwordx4 v[76:79], v234, s[0:3], s32 offen sc1\n"
    "buffer_load_dwordx4 v[80:83], v235, s[0:3], s32 offen sc1\n"
    "buffer_store_dwordx4 v[56:59], v228, s[8:11], s30 offen sc1\n"
    "buffer_store_dwordx4 v[60:63], v229, s[8:11], s30 offen sc1\n"
    ".Lb2_sl_%=:\n"
    "s_waitcnt vmcnt(6)\n"
    "v_max_u32 v84, v64, v64\n"
    "s_waitcnt vmcnt(5)\n"
    "v_max_u32 v84, v68, v84\n"
    "v_pk_add_f32 v[64:65], v[64:65], v[68:69]\n"
    "v_pk_add_f32 v[66:67], v[66:67], v[70:71]\n"
    "s_waitcnt vmcnt(4)\n"
    "v_max_u32 v84, v72, v84\n"
    "v_pk_add_f32 v[64:65], v[64:65], v[72:73]\n"
    "v_pk_add_f32 v[66:67], v[66:67], v[74:75]\n"
    "s_waitcnt vmcnt(3)\n"
    "v_max_u32 v84, v76, v84\n"
    "v_pk_add_f32 v[64:65], v[64:65], v[76:77]\n"
    "v_pk_add_f32 v[66:67], v[66:67], v[78:79]\n"
    "s_waitcnt vmcnt(2)\n"
    "v_max_u32 v84, v80, v84\n"
    "v_pk_add_f32 v[64:65], v[64:65], v[80:81]\n"
    "v_pk_add_f32 v[66:67], v[66:67], v[82:83]\n"
    "v_cmp_eq_u32 vcc, -1, v84\n"
    "s_nop 0\n"
    "s_cbranch_vccnz .Lb2_sr_%=\n"
    ".Lb2_sg_%=:\n"
    ".Lb2_mk4x16_%=:\n"
    "v_permlane16_swap_b32 v64, v65\n"
    "v_permlane16_swap_b32 v66, v67\n"
    "v_add_f32 v64, v64, v65\n"
    "v_add_f32 v66, v66, v67\n"
    "s_nop 1\n"
    "v_permlane32_swap_b32 v64, v66\n"
    "v_add_f32 v64, v64, v66\n"
    "v_mov_b32 v66, v64\n"
    "s_nop 1\n"
    "v_permlane16_swap_b32 v64, v66\n"
    "v_mul_f32 v64, 0x3acccccd, v64\n"
    "v_mul_f32 v66, 0x3acccccd, v66\n"
    "v_fma_f32 v66, -v64, v64, v66\n"
    "v_max_f32 v66, 0, v66\n"
    "v_cvt_pk_bf16_f32 v64, v64, v66\n"
    "v_and_b32 v66, 0xffff0000, v64\n"
    "v_lshlrev_b32 v64, 16, v64\n"
    "v_add_f32 v66, 0x3727c5ac, v66\n"
    "v_mov_b32 v68, v64\n"
    "v_cvt_pk_bf16_f32 v66, v66, v66\n"
    "v_lshlrev_b32 v66, 16, v66\n"
    "v_sqrt_f32 v66, v66\n"
    "v_permlane32_swap_b32 v64, v68\n"
    "v_cvt_pk_bf16_f32 v66, v66, v66\n"
    "v_lshlrev_b32 v66, 16, v66\n"
    "v_rcp_f32 v66, v66\n"
    "s_nop 0\n"
    "v_mov_b32 v70, v66\n"
    "s_nop 1\n"
    "v_permlane32_swap_b32 v66, v70\n"
    "v_mul_f32 v64, v64, v66\n"
    "v_mul_f32 v68, v68, v70\n"
    "v_pk_fma_f32 v[0:1], v[0:1], v[66:67], v[64:65] op_sel_hi:[1,0,0] neg_lo:[0,0,1] neg_hi:[0,0,1]\n"
    "v_pk_fma_f32 v[2:3], v[2:3], v[66:67], v[64:65] op_sel_hi:[1,0,0] neg_lo:[0,0,1] neg_hi:[0,0,1]\n"
    "v_pk_fma_f32 v[4:5], v[4:5], v[66:67], v[64:65] op_sel_hi:[1,0,0] neg_lo:[0,0,1] neg_hi:[0,0,1]\n"
    "v_pk_fma_f32 v[6:7], v[6:7], v[66:67], v[64:65] op_sel_hi:[1,0,0] neg_lo:[0,0,1] neg_hi:[0,0,1]\n"
    "v_cvt_pk_bf16_f32 v96, v0, v1\n"
    "v_cvt_pk_bf16_f32 v98, v2, v3\n"
    "v_cvt_pk_bf16_f32 v97, v4, v5\n"
    "v_cvt_pk_bf16_f32 v99, v6, v7\n"
    "buffer_store_dwordx4 v[96:99], v227, s[0:3], s31 offen offset:0 sc1\n"
    "v_pk_fma_f32 v[8:9], v[8:9], v[70:71], v[68:69] op_sel_hi:[1,0,0] neg_lo:[0,0,1] neg_hi:[0,0,1]\n"
    "v_pk_fma_f32 v[10:11], v[10:11], v[70:71], v[68:69] op_sel_hi:[1,0,0] neg_lo:[0,0,1] neg_hi:[0,0,1]\n"
    "v_pk_fma_f32 v[12:13], v[12:13], v[70:71], v[68:69] op_sel_hi:[1,0,0] neg_lo:[0,0,1] neg_hi:[0,0,1]\n"
    "v_pk_fma_f32 v[14:15], v[14:15], v[70:71], v[68:69] op_sel_hi:[1,0,0] neg_lo:[0,0,1] neg_hi:[0,0,1]\n"
    "v_cvt_pk_bf16_f32 v100, v8, v9\n"
    "v_cvt_pk_bf16_f32 v102, v10, v11\n"
    "v_cvt_pk_bf16_f32 v101, v12, v13\n"
    "v_cvt_pk_bf16_f32 v103, v14, v15\n"
    "buffer_store_dwordx4 v[100:103], v227, s[0:3], s31 offen offset:1024 sc1\n"
    "v_mov_b32 v88, s27\n"
    "v_mov_b32 v89, s27\n"
    "v_mov_b32 v90, s27\n"
    "v_mov_b32 v91, s27\n"
    ".Lb2_mk5x17_%=:\n"
    "v_mov_b32 v84, 0\n"
    "s_memrealtime s[22:23]\n"
    "s_cmp_lg_u32 s12, 0\n"
    "s_waitcnt lgkmcnt(0)\n"
    "s_cbranch_scc1 .Lb2_pl19_%=\n"
    "s_sub_u32 s24, s35, s22\n"
    "s_cmp_gt_i32 s24, 40\n"
    "s_cbranch_scc1 .Lb2_pl19_%=\n"
    "buffer_load_dwordx4 v[84:87], v225, s[0:3], s15 offen sc1\n"
    ".Lb2_pl19_%=:\n"
    "s_waitcnt vmcnt(0)\n"
    "s_barrier\n"
    "buffer_store_dwordx4 v[88:91], v226, s[0:3], 0 offen sc1\n"
    ".Lb2_mk6x19_%=:\n"
    ".Lb2_nx_%=:\n"
    "s_mov_b32 s26, s27\n"
    "s_cmp_lt_u32 s26, 64\n"
    "s_cbranch_scc1 .Lb2_top_%=\n"
    "s_waitcnt vmcnt(0) lgkmcnt(0)\n"
    "s_endpgm\n"
    ".Lb2_tk0_%=:\n"
    ".Lb2_po3_%=:\n"
    "buffer_load_dword v31, v30, s[0:3], 0 offen sc1\n"
    "s_waitcnt vmcnt(0)\n"
    "v_cmp_gt_i32 vcc, 32, v31\n"
    "s_nop 0\n"
    "s_cbranch_vccz .Lb2_pd4_%=\n"
    "s_add_u32 s20, s20, 1\n"
    "s_cmp_lt_u32 s20, 0x40000\n"
    "s_cbranch_scc1 .Lb2_sp5_%=\n"
    "v_mov_b32 v31, 90\n"
    "s_mov_b32 s24, 33728\n"
    "buffer_store_dword v31, off, s[0:3], s24 sc1\n"
    "s_waitcnt vmcnt(0)\n"
    "s_branch .Lb2_pd4_%=\n"
    ".Lb2_sp5_%=:\n"
    "s_sleep 8\n"
    "s_branch .Lb2_po3_%=\n"
    ".Lb2_pd4_%=:\n"
    "s_branch .Lb2_tk1_%=\n"
    ".Lb2_out_%=:\n"
    "v_cvt_pk_bf16_f32 v56, v0, v1\n"
    "v_cvt_pk_bf16_f32 v57, v2, v3\n"
    "v_cvt_pk_bf16_f32 v58, v4, v5\n"
    "v_cvt_pk_bf16_f32 v59, v6, v7\n"
    "v_cvt_pk_bf16_f32 v60, v8, v9\n"
    "v_cvt_pk_bf16_f32 v61, v10, v11\n"
    "v_cvt_pk_bf16_f32 v62, v12, v13\n"
    "v_cvt_pk_bf16_f32 v63, v14, v15\n"
    "buffer_store_dwordx4 v[56:59], v228, s[8:11], s30 offen\n"
    "buffer_store_dwordx4 v[60:63], v229, s[8:11], s30 offen\n"
    "v_mov_b32 v84, 0\n"
    "s_branch .Lb2_nx_%=\n"
    ".Lb2_sr_%=:\n"
    "s_add_u32 s20, s20, 1\n"
    "s_cmp_lt_u32 s20, 0x40000\n"
    "s_cbranch_scc1 .Lb2_sp21_%=\n"
    "v_mov_b32 v84, 72\n"
    "s_mov_b32 s24, 33728\n"
    "buffer_store_dword v84, off, s[0:3], s24 sc1\n"
    "s_waitcnt vmcnt(0)\n"
    "s_branch .Lb2_sg_%=\n"
    ".Lb2_sp21_%=:\n"
    "s_sleep 2\n"
    "buffer_load_dwordx4 v[64:67], v231, s[0:3], s32 offen sc1\n"
    "buffer_load_dwordx4 v[68:71], v232, s[0:3], s32 offen sc1\n"
    "buffer_load_dwordx4 v[72:75], v233, s[0:3], s32 offen sc1\n"
    "buffer_load_dwordx4 v[76:79], v234, s[0:3], s32 offen sc1\n"
    "buffer_load_dwordx4 v[80:83], v235, s[0:3], s32 offen sc1\n"
    "s_waitcnt vmcnt(0)\n"
    "s_branch .Lb2_sl_%=\n"
    :: "{s[0:3]}"(s0_), "{s[4:7]}"(s4_), "{s[8:11]}"(s8_), "{s[12:15]}"(s12_), "{s[16:19]}"(s16_), "{v[0:3]}"(v0_), "{v[4:7]}"(v4_), "{v[224:227]}"(v224_), "{v[228:231]}"(v228_), "{v[232:235]}"(v232_) : "memory", "v255", "a255", "s95");
    __builtin_unreachable();
}
#define SWP 112 - ((k - 24) >> 2) * 16
#define BOX A.ar - 262144
#define NP 4
constexpr int MEGA_LDS = 163840;
__global__ void __launch_bounds__(256, 1) mega_kernel(MegaArgs A) {
    extern __shared__ __attribute__((aligned(16))) char smem[];
    int* sRole = (int*)smem;
    asm volatile("" :: "s"(A.x0), "s"(A.mem0), "s"(A.Wq), "s"(A.Wk), "s"(A.Wv), "s"(A.Wo), "s"(A.wqkv), "s"(A.wo));
    const int x4 = (int)(__builtin_amdgcn_s_getreg((3 << 11) | 20) & 3u);
    const rsrc_t rf = mk_rsrc(arena_base(A.ar), ARENA);
    unsigned* const cnt = (unsigned*)(arena_base(A.ar) + xl(Z_TK, 0));
    u64* const box = (u64*)(BOX);
    const int ln = threadIdx.x & 63;
    static_assert(2 * (Z_END - Z_TK) == 256 * 640, "");
    if (threadIdx.x < 64) {
        bst16(rf, ln < 40 ? 2 * Z_TK + blockIdx.x * 640 + ln * 16 : OOB, u32x4{0u, 0u, 0u, 0u});
        int want;
        if (blockIdx.x == 0) {
            DRAIN();
            u64 sent[4];
            for (int n = 0;; ++n) {
                u64 v[4];
#pragma unroll
                for (int k = 0; k < 4; ++k) v[k] = __hip_atomic_load(box + 8 * (((64 * k + ln) & 31) * 8 + ((64 * k + ln) >> 5)), RLX_AGENT);
                const unsigned c = n < 2 ? 0u : __hip_atomic_load(cnt + 16 * (ln & 7), RLX_AGENT);
                const unsigned open = (unsigned)__ballot(c < 32u) & 255u;
                want = __ffs(open) - 1;
                if ((open & (open - 1)) == 0 && __all(c >= 31u)) break;
#pragma unroll
                for (int k = 0; k < 4; ++k) { if (n == 0 || v[k] != sent[k]) __hip_atomic_store(box + 2048 + 64 * k + ln, v[k], RLX_AGENT); sent[k] = v[k]; }
                if (n > 0) __builtin_amdgcn_s_sleep(16);
            }
        } else {
            const u64 mine = __builtin_amdgcn_s_memrealtime();
            if (threadIdx.x == 0) __hip_atomic_store(box + 8 * blockIdx.x, mine, RLX_AGENT);
            unsigned tm[2] = {~0u, ~0u};
            u64* const in = box + 2048 + (blockIdx.x & 7) * 32 + (blockIdx.x >> 3);
            u64 back;
            const unsigned pr = (blockIdx.x >> 7) * 16384 + (blockIdx.x & 127) * 64;
            if (threadIdx.x < 2) bst4(rf, pr + threadIdx.x * 8192, 0u);
            DRAIN();
#pragma unroll
            for (int k = 0; k < NP; ++k) {
                const unsigned long long t0 = __builtin_amdgcn_s_memtime();
                if (threadIdx.x == 0) bst4(rf, pr + (k & 1) * 8192, 0u);
                if (k < NP - 1) DRAIN();
                else asm volatile("global_load_dwordx2 %0, %1, %2 sc1\n\ts_waitcnt vmcnt(1)" : "=&v"(back) : "v"(0u), "s"(in) : "memory");
                tm[k & 1] = min(tm[k & 1], (unsigned)(__builtin_amdgcn_s_memtime() - t0));
            }
            asm volatile("s_waitcnt vmcnt(0)" : "+v"(back) :: "memory");
            while (back != mine) { __builtin_amdgcn_s_sleep(2); back = __hip_atomic_load(in, RLX_AGENT); }
            want = (tm[1] < tm[0] ? 4 : 0) + x4;
        }
        asm volatile("" ::: "memory");
        if (threadIdx.x == 0) {
            int role = -1;
            for (int p = -1; role < 0; ++p) {
                const int pool = p < 0 ? want : p, x = pool & 3;
                const int k = (int)__hip_atomic_fetch_add(cnt + 16 * pool, 1u, RLX_AGENT);
                if (k < 32) role = (pool >> 2) * 128 + (k < 24 ? (k >> 3) * 32 + 8 * x + (k & 7) : SWP + 4 * x + (k & 3));
            }
            *sRole = role;
        }
    }
    __syncthreads();
    const int role = RFL(*sRole), L = role >> 7, r = role & 127;
    __syncthreads();
    const bool on = (A.layers >> L) & 1;
    if (r < 32) { if (on) qk_role(A, L, r, x4); }
    else if (r < 64) { if (on) vo_role(A, L, r - 32, x4); }
    else if (r < 96) ct_role(A, L, r - 64, smem);
    else if (r < 112) { if (on) { if (r < 107) b1_role(A, L, r - 96, x4); else b2_role(A, L, r - 107, x4); } }
    else qw_role(A, L, 8 * ((r - 112) >> 2) + 4 * L + (r & 3), x4);
}

#define LAYERS 3
extern "C" void kernel_launch(void* const* d_in, const int* in_sizes, int n_in, void* d_out, int out_size, void* d_ws, size_t ws_size, hipStream_t stream) {
    MegaArgs A;
    A.x0 = (const bf16_t*)d_in[0]; A.mem0 = (const bf16_t*)d_in[1]; A.Wq = (const bf16_t*)d_in[2]; A.Wk = (const bf16_t*)d_in[3]; A.Wv = (const bf16_t*)d_in[4];
    A.Wo = (const bf16_t*)d_in[5]; A.wqkv = (const bf16_t*)d_in[8]; A.wo = (const bf16_t*)d_in[9];
    A.ar = (char*)d_ws + 266240;
    A.out = (bf16_t*)d_out; A.layers = LAYERS; A.pad = 0;
    (void)hipFuncSetAttribute((const void*)mega_kernel, hipFuncAttributeMaxDynamicSharedMemorySize, MEGA_LDS);
    mega_kernel<<<256, 256, MEGA_LDS, stream>>>(A);
}
```
